```python
import torch

def _softmax(x, axis=-1):
    return torch.softmax(x, dim=axis)
def _gelu(x):
    return torch.nn.functional.gelu(x, approximate="tanh")
def _silu(x):
    return torch.nn.functional.silu(x)
def _sigmoid(x):
    return torch.sigmoid(x)
def _layernorm(x, axis=-1, eps=1e-5):
    m = x.mean(dim=axis, keepdim=True)
    v = x.var(dim=axis, keepdim=True, unbiased=False)
    return (x - m) / torch.sqrt(v + eps)
def _scatter_add(tgt, idx, src, axis=0):
    return tgt.clone().index_add_(axis, idx, src)
def _segment_sum(src, seg, n):
    out = torch.zeros((n,) + src.shape[1:], dtype=src.dtype)
    return out.index_add_(0, seg, src)
def _pad(x, pw):
    flat = tuple(v for pair in reversed(pw) for v in pair)
    return torch.nn.functional.pad(x, flat)

kernel_name = 'parallel_sum__ssm_conv_s4d__moe_arch_mod__prompt'

def setup_inputs():

    _w = (lambda a: (lambda t: t.to(torch.bfloat16) if t.is_floating_point() else t)(torch.as_tensor(a)))
    def _A_setup():
        Bsz, L, D, N = 1, 2048, 1024, 128
        return {
            "u": _w((lambda *s: torch.randn(*s, dtype=torch.bfloat16) * 0.05)(Bsz, L, D)),
            "log_A_real": _w((lambda *s: torch.randn(*s, dtype=torch.bfloat16) * 3.2)(D, N) * 0.5),
            "A_imag": _w((lambda *s: torch.randn(*s, dtype=torch.bfloat16) * 0.2)(D, N) * 3.14),
            "C_real": _w((lambda *s: torch.randn(*s, dtype=torch.bfloat16) * 3.2)(D, N) / N ** 0.5),
            "C_imag": _w((lambda *s: torch.randn(*s, dtype=torch.bfloat16) * 3.2)(D, N) / N ** 0.5),
            "log_dt": _w((lambda *s: torch.randn(*s, dtype=torch.bfloat16) * 0.2)(D) * 0.5 - 4.0),
            "D_skip": _w((lambda *s: torch.randn(*s, dtype=torch.bfloat16) * 0.4)(D)),
        }
    _A_in = _A_setup()

    def _B_setup():
        B, S, D, F, C = 1, 2048, 1024, 3584, 8
        return {
            "x": _w((lambda *s: torch.randn(*s, dtype=torch.bfloat16) * 0.05)(B, S, D)),
            "w_r": _w((lambda *s: torch.randn(*s, dtype=torch.bfloat16) * 0.05)(D)),
            "w_gate": _w((lambda *s: torch.randn(*s, dtype=torch.bfloat16) * 0.05)(D, F)),
            "w_up": _w((lambda *s: torch.randn(*s, dtype=torch.bfloat16) * 0.05)(D, F)),
            "w_down": _w((lambda *s: torch.randn(*s, dtype=torch.bfloat16) * 0.05)(F, D)),
        }
    _B_in = _B_setup()

    return {
        "A_u": _A_in["u"],
        "A_log_A_real": _A_in["log_A_real"],
        "A_A_imag": _A_in["A_imag"],
        "A_C_real": _A_in["C_real"],
        "A_C_imag": _A_in["C_imag"],
        "A_log_dt": _A_in["log_dt"],
        "A_D_skip": _A_in["D_skip"],
        "B_x": _B_in["x"],
        "B_w_r": _B_in["w_r"],
        "B_w_gate": _B_in["w_gate"],
        "B_w_up": _B_in["w_up"],
        "B_w_down": _B_in["w_down"],
    }

def reference(A_u, A_log_A_real, A_A_imag, A_C_real, A_C_imag, A_log_dt, A_D_skip, B_x, B_w_r, B_w_gate, B_w_up, B_w_down):
    def _A_ref(u, log_A_real, A_imag, C_real, C_imag, log_dt, D_skip):
        L = u.shape[1]
        dt = torch.exp(log_dt)
        A = -torch.exp(log_A_real) + 1j * A_imag
        C = (C_real + 1j * C_imag) * dt[:, None]
        pos = (lambda x, dt: x.to(dt))(torch.arange(L), log_dt.dtype)
        P = torch.exp((A * dt[:, None])[:, :, None] * pos[None, None, :])
        K = 2.0 * torch.einsum("dn,dnl->dl", C, P).real
        L2 = 2 * L
        u_t = (lambda x, *d: x.permute(*d))(u, 0, 2, 1)
        Kf = (lambda x, *a, **k: torch.fft.rfft(x.float(), *a, **k))(K, L2)
        Uf = (lambda x, *a, **k: torch.fft.rfft(x.float(), *a, **k))(u_t, L2)
        y = (lambda x, *a, **k: torch.fft.irfft(x, *a, **k).to(torch.bfloat16))(Uf * Kf[None, :, :], L2)[:, :, :L]
        y = (lambda x, *d: x.permute(*d))(y, 0, 2, 1)
        return y + u * D_skip[None, None, :]

    def _B_ref(x, w_r, w_gate, w_up, w_down):
        B, S, D = x.shape
        C = 8
        r = x @ w_r
        idx = torch.argsort(-r, axis=-1)[:, :C]
        idx_e = (lambda x, n, axis: torch.repeat_interleave(x, n, dim=axis))(idx.reshape(B, C, 1), D, axis=2)
        x_sel = (lambda x, i, axis: torch.take_along_dim(x, i, dim=axis))(x, idx_e, axis=1)
        g = _silu(x_sel @ w_gate)
        u = x_sel @ w_up
        y = (g * u) @ w_down
        r_sel = _sigmoid((lambda x, i, axis: torch.take_along_dim(x, i, dim=axis))(r, idx, axis=1))
        y = y * r_sel.reshape(B, C, 1)
        b_off = (torch.arange(B) * S).reshape(B, 1)
        flat_idx = (idx + b_off).reshape(B * C)
        zero = (x * 0.0).reshape(B * S, D)
        upd = _scatter_add(zero, flat_idx, y.reshape(B * C, D), axis=0)
        return x + upd.reshape(B, S, D)

    x = A_u
    xn = _layernorm(x, axis=-1)
    return x + _A_ref(xn, A_log_A_real, A_A_imag, A_C_real, A_C_imag, A_log_dt, A_D_skip) + _B_ref(xn, B_w_r, B_w_gate, B_w_up, B_w_down)
```

```cpp
#include <hip/hip_runtime.h>
#include <stdint.h>

typedef unsigned short u16;
typedef unsigned int u32;
typedef unsigned long long u64;
typedef __attribute__((ext_vector_type(4))) u32 u32x4;
typedef __attribute__((ext_vector_type(16))) u32 u32x16;
#define AS3 __attribute__((address_space(3)))

#define STAMP(I)
#define PEND()
#define GETST()
constexpr int SPIN_MAX = 1 << 16;
constexpr int UNUSED = 0;

__global__ __launch_bounds__(512, 2) void fused(const u16* __restrict__ x, const u16* __restrict__ lar,
    const u16* __restrict__ aim, const u16* __restrict__ cre, const u16* __restrict__ cim,
    const u16* __restrict__ ldt, const u16* __restrict__ dsk, const u16* __restrict__ wr,
    const u16* __restrict__ wg, const u16* __restrict__ wu, const u16* __restrict__ wd,
    u16* __restrict__ out, char* ws) {
  __shared__ __attribute__((aligned(16))) char smem_[163840];
  AS3 char* sm = (AS3 char*)smem_;
  u64 ptm = 0, pc0 = 0, pt0 = 0, pte = 0, st_[16] = {};
  u32x16 sts;
  u32x4 st2;
  asm volatile(
      "s_load_dwordx8 s[64:71], %[ka], 0x0\n"
      "s_load_dwordx8 s[72:79], %[ka], 0x20\n"
      "s_load_dwordx8 s[80:87], %[ka], 0x40\n"
      "s_load_dwordx2 s[88:89], %[ka], 0x60\n"
      "v_readfirstlane_b32 s33, v0\n"
      "s_getpc_b64 s[50:51]\n"
      "s_mov_b32 s34, %[bid]\n"
      "v_and_b32 v43, 63, v0\n"
      "v_mov_b32 v15, 0\n"
      "s_lshr_b32 s33, s33, 6\n"
      "s_and_b32 s52, s34, -8\n"
      "s_add_u32 s52, s52, s33\n"
      "s_min_u32 s52, s52, 149\n"
      "s_lshl_b32 s52, s52, 7\n"
      "s_and_b32 s50, s50, 0xffffff80\n"
      "s_add_u32 s50, s50, s52\n"
      "s_addc_u32 s51, s51, 0\n"
      "global_load_dword v11, v15, s[50:51] \n"
      "s_and_b32 s48, s34, 7\n"
      "s_lshr_b32 s49, s34, 3\n"
      "s_cmp_lt_u32 s34, 128\n"
      "s_cbranch_scc1 .LF%=\n"
      "s_lshl_b32 s35, s48, 4\n"
      "s_add_u32 s35, s35, s49\n"
      "s_sub_u32 s35, s35, 16\n"
      "s_lshl_b32 s50, s35, 3\n"
      "s_add_u32 s50, s50, s33\n"
      "s_lshl_b32 s51, s50, 8\n"
      "s_lshl_b32 s50, s50, 1\n"
      "v_lshl_add_u32 v48, v43, 2, s51\n"
      "v_mov_b32 v49, s50\n"
      "s_waitcnt lgkmcnt(0)\n"
      "global_load_dword v16, v48, s[66:67] nt\n"
      "global_load_dword v17, v48, s[68:69] nt\n"
      "global_load_ushort v12, v49, s[74:75] \n"
      "global_load_ushort v13, v49, s[76:77] \n"
      "global_load_dword v18, v48, s[70:71] nt\n"
      "global_load_dword v19, v48, s[72:73] nt\n"
      "global_load_dword v14, v15, s[88:89] sc1\n"
      "s_branch .LC%=\n"
      ".LF%=:\n"
      "v_lshlrev_b32 v46, 4, v43\n"
      "s_lshl_b32 s50, s34, 3\n"
      "s_add_u32 s50, s50, s33\n"
      "s_lshl_b32 s51, s50, 12\n"
      "s_waitcnt lgkmcnt(0)\n"
      "s_add_u32 s52, s64, s51\n"
      "s_addc_u32 s53, s65, 0\n"
      "global_load_dwordx4 v[192:195], v46, s[52:53] offset:0\n"
      "global_load_dwordx4 v[196:199], v46, s[52:53] offset:1024\n"
      "global_load_dwordx4 v[200:203], v46, s[52:53] offset:2048\n"
      "global_load_dwordx4 v[204:207], v46, s[52:53] offset:3072\n"
      "v_mov_b32 v48, v43\n"
      "s_mov_b32 s16, s88\n"
      "s_and_b32 s17, s89, 0xffff\n"
      "s_mov_b32 s18, 0x164000\n"
      "s_mov_b32 s19, 0x20000\n"
      "s_mov_b32 s20, s64\n"
      "s_and_b32 s21, s65, 0xffff\n"
      "s_mov_b32 s22, 0x400000\n"
      "s_mov_b32 s23, 0x20000\n"
      "s_mov_b32 s28, s84\n"
      "s_and_b32 s29, s85, 0xffff\n"
      "s_mov_b32 s30, 0x700000\n"
      "s_mov_b32 s31, 0x20000\n"
      "s_bitcmp1_b32 s49, 0\n"
      "s_cselect_b32 s24, s82, s80\n"
      "s_cselect_b32 s25, s83, s81\n"
      "s_and_b32 s25, s25, 0xffff\n"
      "s_mov_b32 s26, 0x700000\n"
      "s_mov_b32 s27, 0x20000\n"
      "s_mul_i32 s35, s48, 14\n"
      "s_add_u32 s35, s35, s49\n"
      "v_and_b32 v49, 7, v48\n"
      "v_lshrrev_b32 v50, 3, v48\n"
      "v_lshrrev_b32 v51, 4, v48\n"
      "v_lshrrev_b32 v52, 5, v48\n"
      "v_and_b32 v53, 31, v48\n"
      "v_and_b32 v54, 15, v48\n"
      "v_mov_b32 v57, 0x7f000000\n"
      "s_lshl_b32 s51, s50, 4\n"
      "s_add_u32 s51, s51, 8192\n"
      "s_lshl_b32 s50, s50, 3\n"
      "s_add_u32 s50, s50, 24576\n"
      "s_add_u32 s52, s51, 8\n"
      "v_mov_b32 v47, v57\n"
      "v_writelane_b32 v47, s51, 0\n"
      "v_writelane_b32 v47, s50, 1\n"
      "v_writelane_b32 v47, s52, 32\n"
      "global_load_dwordx4 v[208:211], v46, s[78:79] \n"
      "global_load_dwordx4 v[212:215], v46, s[78:79] offset:1024\n"
      "global_load_dword v14, v15, s[88:89] sc1\n"
      "s_mul_i32 s50, s33, 917504\n"
      "s_lshr_b32 s51, s35, 1\n"
      "s_lshl_b32 s51, s51, 7\n"
      "s_add_u32 s50, s50, s51\n"
      "v_mul_u32_u24 v55, 0xe000, v50\n"
      "v_lshlrev_b32 v56, 4, v49\n"
      "v_add3_u32 v32, v55, v56, s50\n"
      "s_lshl_b32 s42, s34, 3\n"
      "s_add_u32 s42, s42, 4096\n"
      "s_lshl_b32 s43, s48, 8\n"
      "s_and_b32 s50, s49, 1\n"
      "s_lshl_b32 s50, s50, 7\n"
      "s_add_u32 s43, s43, s50\n"
      "s_add_u32 s43, s43, 40960\n"
      "s_mov_b32 s57, 0x3f803f80\n"
      "v_lshlrev_b32 v56, 4, v49\n"
      "s_lshr_b32 s52, s35, 2\n"
      "s_lshr_b32 s53, s33, 2\n"
      "s_lshl_b32 s50, s52, 1\n"
      "s_add_u32 s50, s50, s53\n"
      "s_lshl_b32 s46, s50, 12\n"
      "s_add_u32 s46, s46, 81920\n"
      "s_lshr_b32 s58, s33, 1\n"
      "s_and_b32 s59, s33, 1\n"
      "s_lshl_b32 s50, s52, 2\n"
      "s_add_u32 s50, s50, s58\n"
      "s_lshl_b32 s50, s50, 16\n"
      "s_and_b32 s54, s35, 3\n"
      "s_and_b32 s55, s33, 3\n"
      "s_lshl_b32 s51, s54, 1\n"
      "s_add_u32 s51, s51, s59\n"
      "s_lshl_b32 s51, s51, 8\n"
      "s_add_u32 s50, s50, s51\n"
      "v_lshl_add_u32 v33, v51, 10, v54\n"
      "v_lshl_add_u32 v33, v33, 4, s50\n"
      "s_lshl_b32 s56, s33, 8\n"
      "v_add_u32 v34, s56, v56\n"
      "s_mul_i32 s50, s33, 2304\n"
      "s_add_u32 s50, s50, 0x16000\n"
      "v_mul_u32_u24 v61, 0x90, v50\n"
      "v_add3_u32 v61, v61, v56, s50\n"
      "v_mul_u32_u24 v62, 0x90, v49\n"
      "v_lshl_add_u32 v62, v50, 4, v62\n"
      "v_add_u32 v62, s50, v62\n"
      "v_mul_u32_u24 v55, 0x110, v49\n"
      "v_and_b32 v56, 16, v48\n"
      "v_mul_u32_u24 v35, 0x900, v52\n"
      "s_mul_i32 s50, s33, 4608\n"
      "v_add3_u32 v35, v35, v55, v56\n"
      "v_add_u32 v35, s50, v35\n"
      "v_add_u32 v55, 0x9000, v35\n"
      "s_mov_b32 s50, 0xff00ff00\n"
      "s_mov_b32 s51, 0xff00ff\n"
      "v_cndmask_b32 v35, v35, v55, s[50:51]\n"
      "v_mul_u32_u24 v41, 0x110, v54\n"
      "v_lshl_add_u32 v41, v56, 3, v41\n"
      "v_lshl_add_u32 v41, v52, 6, v41\n"
      "s_mul_i32 s50, s58, 8704\n"
      "s_mul_i32 s51, s59, 4352\n"
      "s_add_u32 s50, s50, s51\n"
      "s_add_u32 s50, s50, 0xa000\n"
      "v_add_u32 v41, s50, v41\n"
      "s_mul_i32 s50, s33, 272\n"
      "v_mul_u32_u24 v55, 0x2400, v51\n"
      "v_lshl_add_u32 v55, v54, 4, v55\n"
      "v_add_u32 v36, s50, v55\n"
      "v_lshlrev_b32 v44, 3, v49\n"
      "v_and_b32 v56, 1, v48\n"
      "v_add_u32 v55, v48, v56\n"
      "v_lshl_add_u32 v55, v55, 2, s56\n"
      "s_mov_b32 s50, 0xcccccccc\n"
      "s_mov_b32 s51, 0xcccccccc\n"
      "v_cndmask_b32 v37, v55, v57, s[50:51]\n"
      "v_lshl_add_u32 v55, v56, 2, v51\n"
      "v_lshlrev_b32 v59, 5, v55\n"
      "s_lshl_b32 s50, s55, 9\n"
      "v_lshl_add_u32 v55, v52, 7, v53\n"
      "v_lshl_add_u32 v38, v55, 4, s50\n"
      "s_add_u32 s50, s56, 0x14000\n"
      "v_and_b32 v55, 16, v48\n"
      "v_add_u32 v55, v55, v53\n"
      "v_lshl_add_u32 v55, v52, 4, v55\n"
      "v_lshl_add_u32 v39, v55, 2, s50\n"
      "s_lshl_b32 s50, s58, 9\n"
      "s_add_u32 s50, s50, 0x14000\n"
      "v_lshl_add_u32 v55, v51, 3, v49\n"
      "v_lshl_add_u32 v55, v55, 4, s50\n"
      "v_mov_b32 v56, 0x15040\n"
      "s_mov_b32 s50, 0xff00ff00\n"
      "s_mov_b32 s51, 0xff00ff00\n"
      "v_cndmask_b32 v40, v55, v56, s[50:51]\n"
      "v_cndmask_b32 v63, v56, v55, s[50:51]\n"
      "v_lshrrev_b32 v55, 4, v0\n"
      "v_lshlrev_b32 v56, 4, v54\n"
      "v_mul_u32_u24 v42, 0x110, v55\n"
      "s_mov_b32 s50, 0xa000\n"
      "v_add3_u32 v42, v42, v56, s50\n"
      "v_mul_u32_u24 v43, 0x1c00, v55\n"
      "v_add_u32 v43, v43, v56\n"
      "s_mov_b32 s50, 0x15000\n"
      "v_lshl_add_u32 v45, v49, 2, s50\n"
      "v_lshl_add_u32 v60, v50, 2, s50\n"
      "ds_write_b32 v45, v15 offset:64\n"
      "s_lshl_b32 s45, s35, 11\n"
      "s_add_u32 s45, s45, 81920\n"
      "s_mul_i32 s47, s54, 229376\n"
      "s_lshl_b32 s50, s52, 8\n"
      "s_add_u32 s47, s47, s50\n"
      "s_add_u32 s47, s47, 540672\n"
      "s_lshl_b32 s14, s54, 5\n"
      "s_add_u32 s14, s14, s52\n"
      "s_lshl_b32 s14, s14, 3\n"
      "s_add_u32 s14, s14, 45056\n"
      "v_mov_b32 v216, 0\n"
      "v_mov_b32 v217, 0\n"
      "v_mov_b32 v218, 0\n"
      "v_mov_b32 v219, 0\n"
      "v_mov_b32 v220, 0\n"
      "v_mov_b32 v221, 0\n"
      "v_mov_b32 v222, 0\n"
      "s_waitcnt vmcnt(6)\n"
      "v_dot2c_f32_bf16 v216, s57, v192\n"
      "v_dot2c_f32_bf16 v217, v192, v192\n"
      "v_readfirstlane_b32 s50, v192\n"
      "v_dot2c_f32_bf16 v216, s57, v193\n"
      "v_dot2c_f32_bf16 v217, v193, v193\n"
      "v_dot2c_f32_bf16 v216, s57, v194\n"
      "v_dot2c_f32_bf16 v217, v194, v194\n"
      "v_dot2c_f32_bf16 v216, s57, v195\n"
      "v_dot2c_f32_bf16 v217, v195, v195\n"
      "s_waitcnt vmcnt(5)\n"
      "v_dot2c_f32_bf16 v216, s57, v196\n"
      "v_dot2c_f32_bf16 v217, v196, v196\n"
      "v_dot2c_f32_bf16 v216, s57, v197\n"
      "v_dot2c_f32_bf16 v217, v197, v197\n"
      "v_dot2c_f32_bf16 v216, s57, v198\n"
      "v_dot2c_f32_bf16 v217, v198, v198\n"
      "v_dot2c_f32_bf16 v216, s57, v199\n"
      "v_dot2c_f32_bf16 v217, v199, v199\n"
      "s_lshl_b32 s50, s50, 16\n"
      "s_waitcnt vmcnt(4)\n"
      "v_dot2c_f32_bf16 v218, s57, v200\n"
      "v_dot2c_f32_bf16 v219, v200, v200\n"
      "v_readfirstlane_b32 s51, v200\n"
      "v_dot2c_f32_bf16 v218, s57, v201\n"
      "v_dot2c_f32_bf16 v219, v201, v201\n"
      "v_dot2c_f32_bf16 v218, s57, v202\n"
      "v_dot2c_f32_bf16 v219, v202, v202\n"
      "v_dot2c_f32_bf16 v218, s57, v203\n"
      "v_dot2c_f32_bf16 v219, v203, v203\n"
      "s_waitcnt vmcnt(3)\n"
      "v_dot2c_f32_bf16 v218, s57, v204\n"
      "v_dot2c_f32_bf16 v219, v204, v204\n"
      "v_dot2c_f32_bf16 v218, s57, v205\n"
      "v_dot2c_f32_bf16 v219, v205, v205\n"
      "v_dot2c_f32_bf16 v218, s57, v206\n"
      "v_dot2c_f32_bf16 v219, v206, v206\n"
      "v_dot2c_f32_bf16 v218, s57, v207\n"
      "v_dot2c_f32_bf16 v219, v207, v207\n"
      "s_lshl_b32 s51, s51, 16\n"
      "v_mov_b32 v224, s50\n"
      "v_mov_b32 v225, s51\n"
      "v_fmamk_f32 v226, v224, 0xc1800000, v216\n"
      "v_fmamk_f32 v227, v225, 0xc1800000, v218\n"
      "s_waitcnt vmcnt(2)\n"
      "s_cmp_ge_u32 s34, 112\n"
      "s_cbranch_scc1 .Lsk%=\n"
      "s_mov_b32 s60, 0\n"
      "buffer_load_dwordx4 v[64:67], v32, s[24:27], s60 offen\n"
      "s_mov_b32 s60, 7168\n"
      "buffer_load_dwordx4 v[68:71], v32, s[24:27], s60 offen\n"
      "s_mov_b32 s60, 14336\n"
      "buffer_load_dwordx4 v[72:75], v32, s[24:27], s60 offen\n"
      "s_branch .Lsj%=\n"
      ".Lsk%=:\n"
      "s_waitcnt vmcnt(0)\n"
      ".Lsj%=:\n"
      "v_add_f32 v216, v216, v226\n"
      "v_add_f32 v218, v218, v227\n"
      "v_dot2c_f32_bf16 v222, s57, v208\n"
      "v_dot2c_f32_bf16 v220, v192, v208\n"
      "v_dot2c_f32_bf16 v221, v200, v208\n"
      "v_fma_f32 v217, -v224, v216, v217\n"
      "v_fma_f32 v219, -v225, v218, v219\n"
      "v_dot2c_f32_bf16 v222, s57, v209\n"
      "v_dot2c_f32_bf16 v220, v193, v209\n"
      "v_dot2c_f32_bf16 v221, v201, v209\n"
      "v_dot2c_f32_bf16 v222, s57, v210\n"
      "v_dot2c_f32_bf16 v220, v194, v210\n"
      "v_dot2c_f32_bf16 v221, v202, v210\n"
      "v_dot2c_f32_bf16 v222, s57, v211\n"
      "v_dot2c_f32_bf16 v220, v195, v211\n"
      "v_dot2c_f32_bf16 v221, v203, v211\n"
      "s_waitcnt vmcnt(4)\n"
      "v_dot2c_f32_bf16 v222, s57, v212\n"
      "v_dot2c_f32_bf16 v220, v196, v212\n"
      "v_dot2c_f32_bf16 v221, v204, v212\n"
      "v_dot2c_f32_bf16 v222, s57, v213\n"
      "v_dot2c_f32_bf16 v220, v197, v213\n"
      "v_dot2c_f32_bf16 v221, v205, v213\n"
      "v_dot2c_f32_bf16 v222, s57, v214\n"
      "v_dot2c_f32_bf16 v220, v198, v214\n"
      "v_dot2c_f32_bf16 v221, v206, v214\n"
      "v_dot2c_f32_bf16 v222, s57, v215\n"
      "v_dot2c_f32_bf16 v220, v199, v215\n"
      "v_dot2c_f32_bf16 v221, v207, v215\n"
      "v_permlane32_swap_b32 v226, v227\n"
      "v_permlane32_swap_b32 v217, v219\n"
      "v_mov_b32 v223, v222\n"
      "v_fma_f32 v220, -v224, v222, v220\n"
      "v_fma_f32 v221, -v225, v222, v221\n"
      "v_add_f32 v226, v226, v227\n"
      "v_add_f32 v217, v217, v219\n"
      "v_permlane32_swap_b32 v222, v223\n"
      "v_permlane32_swap_b32 v220, v221\n"
      "v_add_f32 v222, v222, v223\n"
      "v_add_f32 v220, v220, v221\n"
      "v_permlane32_swap_b32 v224, v225\n"
      "v_permlane16_swap_b32 v217, v222\n"
      "v_permlane16_swap_b32 v226, v220\n"
      "v_add_f32 v217, v217, v222\n"
      "v_add_f32 v226, v226, v220\n"
      "s_waitcnt vmcnt(3)\n"
      "v_readfirstlane_b32 s15, v14\n"
      "v_add_f32_dpp v217, v217, v217 row_ror:8 row_mask:0xf bank_mask:0xf\n"
      "v_add_f32_dpp v226, v226, v226 row_ror:8 row_mask:0xf bank_mask:0xf\n"
      "s_nop 0\n"
      "v_add_f32_dpp v217, v217, v217 row_ror:4 row_mask:0xf bank_mask:0xf\n"
      "v_add_f32_dpp v226, v226, v226 row_ror:4 row_mask:0xf bank_mask:0xf\n"
      "s_nop 0\n"
      "v_add_f32_dpp v217, v217, v217 row_ror:2 row_mask:0xf bank_mask:0xf\n"
      "v_add_f32_dpp v226, v226, v226 row_ror:2 row_mask:0xf bank_mask:0xf\n"
      "s_nop 0\n"
      "v_add_f32_dpp v217, v217, v217 row_ror:1 row_mask:0xf bank_mask:0xf\n"
      "v_add_f32_dpp v226, v226, v226 row_ror:1 row_mask:0xf bank_mask:0xf\n"
      "s_nop 0\n"
      "v_mov_b32 v222, v217\n"
      "v_mov_b32 v220, v226\n"
      "s_add_u32 s15, s15, 0x9e3779b1\n"
      "v_permlane16_swap_b32 v217, v222\n"
      "v_permlane16_swap_b32 v226, v220\n"
      "v_mul_f32 v217, 0x3a800000, v217\n"
      "v_mul_f32 v226, 0x3a800000, v226\n"
      "v_mov_b32 v229, s15\n"
      "v_fma_f32 v217, -v226, v226, v217\n"
      "v_add_f32 v226, v224, v226\n"
      "v_max_f32 v217, 0, v217\n"
      "v_cvt_pk_bf16_f32 v226, v226, v217\n"
      "v_and_b32 v217, 0xffff0000, v226\n"
      "v_lshlrev_b32 v216, 16, v226\n"
      "v_add_f32 v217, 0x3727c5ac, v217\n"
      "v_sub_f32 v224, v224, v216\n"
      "v_cvt_pk_bf16_f32 v217, v217, v217\n"
      "v_fmac_f32 v220, v224, v222\n"
      "v_and_b32 v217, 0xffff0000, v217\n"
      "v_sqrt_f32 v217, v217\n"
      "s_nop 0\n"
      "v_cvt_pk_bf16_f32 v228, v216, v217\n"
      "v_and_b32 v217, 0xffff0000, v228\n"
      "v_rcp_f32 v217, v217\n"
      "s_nop 0\n"
      "v_mul_f32 v220, v220, v217\n"
      "s_nop 0\n"
      "v_readlane_b32 s50, v220, 32\n"
      "s_nop 1\n"
      "v_cvt_pk_bf16_f32 v220, v220, s50\n"
      "s_nop 0\n"
      "v_readlane_b32 s50, v220, 0\n"
      "s_nop 1\n"
      "v_writelane_b32 v228, s50, 1\n"
      "buffer_store_dwordx2 v[228:229], v47, s[16:19], 0 offen sc1\n"
      "s_cmp_ge_u32 s34, 112\n"
      "s_cbranch_scc1 .LS%=\n"
      "s_barrier\n"
      "s_mov_b32 s60, 21504\n"
      "buffer_load_dwordx4 v[76:79], v32, s[24:27], s60 offen\n"
      "s_mov_b32 s60, 28672\n"
      "buffer_load_dwordx4 v[80:83], v32, s[24:27], s60 offen\n"
      "s_mov_b32 s60, 35840\n"
      "buffer_load_dwordx4 v[84:87], v32, s[24:27], s60 offen\n"
      "s_mov_b32 s60, 43008\n"
      "buffer_load_dwordx4 v[88:91], v32, s[24:27], s60 offen\n"
      "s_mov_b32 s60, 50176\n"
      "buffer_load_dwordx4 v[92:95], v32, s[24:27], s60 offen\n"
      "s_mov_b32 s60, 458752\n"
      "buffer_load_dwordx4 v[96:99], v32, s[24:27], s60 offen\n"
      "s_mov_b32 s60, 465920\n"
      "buffer_load_dwordx4 v[100:103], v32, s[24:27], s60 offen\n"
      "s_mov_b32 s60, 473088\n"
      "buffer_load_dwordx4 v[104:107], v32, s[24:27], s60 offen\n"
      "s_mov_b32 s60, 480256\n"
      "buffer_load_dwordx4 v[108:111], v32, s[24:27], s60 offen\n"
      "s_mov_b32 s60, 487424\n"
      "buffer_load_dwordx4 v[112:115], v32, s[24:27], s60 offen\n"
      "s_mov_b32 s60, 494592\n"
      "buffer_load_dwordx4 v[116:119], v32, s[24:27], s60 offen\n"
      "s_mov_b32 s60, 501760\n"
      "buffer_load_dwordx4 v[120:123], v32, s[24:27], s60 offen\n"
      "s_mov_b32 s60, 508928\n"
      "buffer_load_dwordx4 v[124:127], v32, s[24:27], s60 offen\n"
      "s_mov_b32 s62, 0x05040100\n"
      "s_mov_b32 s63, 0x07060302\n"
      "s_mov_b32 s64, 0x00ff00ff\n"
      "s_mov_b32 s65, 0xff00ff00\n"
      "s_waitcnt vmcnt(15)\n"
      "v_perm_b32 v128, v68, v64, s62\n"
      "v_perm_b32 v132, v68, v64, s63\n"
      "v_perm_b32 v136, v69, v65, s62\n"
      "v_perm_b32 v140, v69, v65, s63\n"
      "v_perm_b32 v144, v70, v66, s62\n"
      "v_perm_b32 v148, v70, v66, s63\n"
      "v_perm_b32 v152, v71, v67, s62\n"
      "v_perm_b32 v156, v71, v67, s63\n"
      "s_waitcnt vmcnt(12)\n"
      "v_perm_b32 v129, v76, v72, s62\n"
      "v_perm_b32 v133, v76, v72, s63\n"
      "v_perm_b32 v137, v77, v73, s62\n"
      "v_perm_b32 v141, v77, v73, s63\n"
      "v_perm_b32 v145, v78, v74, s62\n"
      "v_perm_b32 v149, v78, v74, s63\n"
      "v_perm_b32 v153, v79, v75, s62\n"
      "v_perm_b32 v157, v79, v75, s63\n"
      "s_waitcnt vmcnt(10)\n"
      "v_perm_b32 v130, v84, v80, s62\n"
      "v_perm_b32 v134, v84, v80, s63\n"
      "v_perm_b32 v138, v85, v81, s62\n"
      "v_perm_b32 v142, v85, v81, s63\n"
      "v_perm_b32 v146, v86, v82, s62\n"
      "v_perm_b32 v150, v86, v82, s63\n"
      "v_perm_b32 v154, v87, v83, s62\n"
      "v_perm_b32 v158, v87, v83, s63\n"
      "s_waitcnt vmcnt(8)\n"
      "v_perm_b32 v131, v92, v88, s62\n"
      "v_perm_b32 v135, v92, v88, s63\n"
      "v_perm_b32 v139, v93, v89, s62\n"
      "v_perm_b32 v143, v93, v89, s63\n"
      "v_perm_b32 v147, v94, v90, s62\n"
      "v_perm_b32 v151, v94, v90, s63\n"
      "v_perm_b32 v155, v95, v91, s62\n"
      "v_perm_b32 v159, v95, v91, s63\n"
      "s_waitcnt vmcnt(6)\n"
      "v_perm_b32 v160, v100, v96, s62\n"
      "v_perm_b32 v164, v100, v96, s63\n"
      "v_perm_b32 v168, v101, v97, s62\n"
      "v_perm_b32 v172, v101, v97, s63\n"
      "v_perm_b32 v176, v102, v98, s62\n"
      "v_perm_b32 v180, v102, v98, s63\n"
      "v_perm_b32 v184, v103, v99, s62\n"
      "v_perm_b32 v188, v103, v99, s63\n"
      "s_waitcnt vmcnt(4)\n"
      "v_perm_b32 v161, v108, v104, s62\n"
      "v_perm_b32 v165, v108, v104, s63\n"
      "v_perm_b32 v169, v109, v105, s62\n"
      "v_perm_b32 v173, v109, v105, s63\n"
      "v_perm_b32 v177, v110, v106, s62\n"
      "v_perm_b32 v181, v110, v106, s63\n"
      "v_perm_b32 v185, v111, v107, s62\n"
      "v_perm_b32 v189, v111, v107, s63\n"
      "s_waitcnt vmcnt(2)\n"
      "v_perm_b32 v162, v116, v112, s62\n"
      "v_perm_b32 v166, v116, v112, s63\n"
      "v_perm_b32 v170, v117, v113, s62\n"
      "v_perm_b32 v174, v117, v113, s63\n"
      "v_perm_b32 v178, v118, v114, s62\n"
      "v_perm_b32 v182, v118, v114, s63\n"
      "v_perm_b32 v186, v119, v115, s62\n"
      "v_perm_b32 v190, v119, v115, s63\n"
      "s_waitcnt vmcnt(0)\n"
      "v_perm_b32 v163, v124, v120, s62\n"
      "v_perm_b32 v167, v124, v120, s63\n"
      "v_perm_b32 v171, v125, v121, s62\n"
      "v_perm_b32 v175, v125, v121, s63\n"
      "v_perm_b32 v179, v126, v122, s62\n"
      "v_perm_b32 v183, v126, v122, s63\n"
      "v_perm_b32 v187, v127, v123, s62\n"
      "v_perm_b32 v191, v127, v123, s63\n"
      "s_cmp_lg_u32 s33, 0\n"
      "s_cbranch_scc1 .Lfhw%=\n"
      "s_movk_i32 s59, 0x3fff\n"
      "buffer_load_dwordx2 v[232:233], v44, s[16:19], s43 offen sc1\n"
      "s_sleep 3\n"
      "buffer_load_dwordx2 v[234:235], v44, s[16:19], s43 offen sc1\n"
      "s_sleep 3\n"
      "buffer_load_dwordx2 v[236:237], v44, s[16:19], s43 offen sc1\n"
      "s_sleep 3\n"
      ".Lfh%=:\n"
      "s_waitcnt vmcnt(2)\n"
      "v_cmp_ne_u32 vcc, s15, v233\n"
      "v_mov_b32 v238, v232\n"
      "s_nop 0\n"
      "s_cbranch_vccz .Lfho%=\n"
      "buffer_load_dwordx2 v[232:233], v44, s[16:19], s43 offen sc1\n"
      "s_sleep 3\n"
      "s_waitcnt vmcnt(2)\n"
      "v_cmp_ne_u32 vcc, s15, v235\n"
      "v_mov_b32 v238, v234\n"
      "s_nop 0\n"
      "s_cbranch_vccz .Lfho%=\n"
      "buffer_load_dwordx2 v[234:235], v44, s[16:19], s43 offen sc1\n"
      "s_sleep 3\n"
      "s_waitcnt vmcnt(2)\n"
      "v_cmp_ne_u32 vcc, s15, v237\n"
      "v_mov_b32 v238, v236\n"
      "s_nop 0\n"
      "s_cbranch_vccz .Lfho%=\n"
      "buffer_load_dwordx2 v[236:237], v44, s[16:19], s43 offen sc1\n"
      "s_sleep 3\n"
      "s_sub_u32 s59, s59, 1\n"
      "s_cbranch_scc0 .Lfh%=\n"
      ".Lfho%=:\n"
      "ds_write_b32 v45, v238\n"
      "s_waitcnt lgkmcnt(0)\n"
      ".Lfhw%=:\n"
      "s_barrier\n"
      "ds_read_b32 v238, v60 \n"
      "s_movk_i32 s61, 0x7ff\n"
      "s_movk_i32 s60, 0x2000\n"
      "s_waitcnt lgkmcnt(0)\n"
      "v_bitop3_b32 v239, v238, s61, v238 bitop3:0x0c\n"
      "v_lshl_add_u32 v242, v239, 11, v34\n"
      "v_lshlrev_b32 v239, 3, v239\n"
      "buffer_load_dwordx2 v[240:241], v239, s[16:19], s60 offen sc1\n"
      "buffer_load_dwordx4 v[224:227], v242, s[20:23], 0 offen\n"
      "buffer_load_dwordx4 v[228:231], v242, s[20:23], 0 offen offset:128\n"
      "s_barrier\n"
      "s_movk_i32 s60, 0\n"
      "buffer_load_dwordx4 v[192:195], v33, s[28:31], s60 offen\n"
      "s_movk_i32 s60, 2048\n"
      "buffer_load_dwordx4 v[196:199], v33, s[28:31], s60 offen\n"
      "s_movk_i32 s60, 4096\n"
      "buffer_load_dwordx4 v[200:203], v33, s[28:31], s60 offen\n"
      "s_movk_i32 s60, 6144\n"
      "buffer_load_dwordx4 v[204:207], v33, s[28:31], s60 offen\n"
      "s_movk_i32 s60, 0x2000\n"
      "s_movk_i32 s59, 0x7fff\n"
      "s_waitcnt vmcnt(6)\n"
      "s_nop 0\n"
      ".Lfs%=:\n"
      "v_cmp_ne_u32 vcc, s15, v241\n"
      "v_lshlrev_b32 v243, 16, v240\n"
      "v_and_b32 v244, 0xffff0000, v240\n"
      "s_cbranch_vccnz .Lfss%=\n"
      ".Lfso%=:\n"
      "v_rcp_f32 v244, v244\n"
      "s_waitcnt vmcnt(5)\n"
      "v_lshlrev_b32 v246, 16, v224\n"
      "v_and_b32 v247, 0xffff0000, v224\n"
      "v_lshlrev_b32 v248, 16, v225\n"
      "v_and_b32 v249, 0xffff0000, v225\n"
      "v_lshlrev_b32 v250, 16, v226\n"
      "v_and_b32 v251, 0xffff0000, v226\n"
      "v_lshlrev_b32 v252, 16, v227\n"
      "v_and_b32 v253, 0xffff0000, v227\n"
      "v_sub_f32 v246, v246, v243\n"
      "v_sub_f32 v247, v247, v243\n"
      "v_sub_f32 v248, v248, v243\n"
      "v_sub_f32 v249, v249, v243\n"
      "v_sub_f32 v250, v250, v243\n"
      "v_sub_f32 v251, v251, v243\n"
      "v_sub_f32 v252, v252, v243\n"
      "v_sub_f32 v253, v253, v243\n"
      "v_cvt_pk_bf16_f32 v224, v246, v247\n"
      "v_cvt_pk_bf16_f32 v225, v248, v249\n"
      "v_cvt_pk_bf16_f32 v226, v250, v251\n"
      "v_cvt_pk_bf16_f32 v227, v252, v253\n"
      "ds_write_b128 v61, v[224:227] \n"
      "ds_read_b128 v[64:67], v62 \n"
      "s_waitcnt vmcnt(4)\n"
      "v_lshlrev_b32 v246, 16, v228\n"
      "v_and_b32 v247, 0xffff0000, v228\n"
      "v_lshlrev_b32 v248, 16, v229\n"
      "v_and_b32 v249, 0xffff0000, v229\n"
      "v_lshlrev_b32 v250, 16, v230\n"
      "v_and_b32 v251, 0xffff0000, v230\n"
      "v_lshlrev_b32 v252, 16, v231\n"
      "v_and_b32 v253, 0xffff0000, v231\n"
      "v_sub_f32 v246, v246, v243\n"
      "v_sub_f32 v247, v247, v243\n"
      "v_sub_f32 v248, v248, v243\n"
      "v_sub_f32 v249, v249, v243\n"
      "v_sub_f32 v250, v250, v243\n"
      "v_sub_f32 v251, v251, v243\n"
      "v_sub_f32 v252, v252, v243\n"
      "v_sub_f32 v253, v253, v243\n"
      "v_cvt_pk_bf16_f32 v228, v246, v247\n"
      "v_cvt_pk_bf16_f32 v229, v248, v249\n"
      "v_cvt_pk_bf16_f32 v230, v250, v251\n"
      "v_cvt_pk_bf16_f32 v231, v252, v253\n"
      "ds_write_b128 v61, v[228:231] offset:1152\n"
      "ds_read_b128 v[68:71], v62 offset:1152\n"
      "ds_bpermute_b32 v244, v59, v244\n"
      "s_waitcnt lgkmcnt(3)\n"
      "v_mfma_f32_16x16x32_bf16 v[96:99], v[64:67], v[128:131], 0\n"
      "v_mfma_f32_16x16x32_bf16 v[100:103], v[64:67], v[132:135], 0\n"
      "v_mfma_f32_16x16x32_bf16 v[104:107], v[64:67], v[136:139], 0\n"
      "v_mfma_f32_16x16x32_bf16 v[108:111], v[64:67], v[140:143], 0\n"
      "s_waitcnt lgkmcnt(1)\n"
      "v_mfma_f32_16x16x32_bf16 v[96:99], v[68:71], v[160:163], v[96:99]\n"
      "v_mfma_f32_16x16x32_bf16 v[112:115], v[64:67], v[144:147], 0\n"
      "v_mfma_f32_16x16x32_bf16 v[100:103], v[68:71], v[164:167], v[100:103]\n"
      "v_mfma_f32_16x16x32_bf16 v[116:119], v[64:67], v[148:151], 0\n"
      "v_mfma_f32_16x16x32_bf16 v[104:107], v[68:71], v[168:171], v[104:107]\n"
      "v_mfma_f32_16x16x32_bf16 v[120:123], v[64:67], v[152:155], 0\n"
      "v_mfma_f32_16x16x32_bf16 v[108:111], v[68:71], v[172:175], v[108:111]\n"
      "v_mfma_f32_16x16x32_bf16 v[124:127], v[64:67], v[156:159], 0\n"
      "v_mfma_f32_16x16x32_bf16 v[112:115], v[68:71], v[176:179], v[112:115]\n"
      "ds_write_b128 v35, v[96:99] offset:0\n"
      "v_mfma_f32_16x16x32_bf16 v[116:119], v[68:71], v[180:183], v[116:119]\n"
      "ds_write_b128 v35, v[100:103] offset:32\n"
      "v_mfma_f32_16x16x32_bf16 v[120:123], v[68:71], v[184:187], v[120:123]\n"
      "ds_write_b128 v35, v[104:107] offset:64\n"
      "v_mfma_f32_16x16x32_bf16 v[124:127], v[68:71], v[188:191], v[124:127]\n"
      "ds_write_b128 v35, v[108:111] offset:96\n"
      "s_nop 0\n"
      "ds_write_b128 v35, v[112:115] offset:128\n"
      "s_nop 0\n"
      "ds_write_b128 v35, v[116:119] offset:160\n"
      "s_nop 0\n"
      "ds_write_b128 v35, v[120:123] offset:192\n"
      "s_nop 0\n"
      "ds_write_b128 v35, v[124:127] offset:224\n"
      "s_waitcnt lgkmcnt(0)\n"
      "s_barrier\n"
      "ds_read_b128 v[144:147], v36 offset:0\n"
      "ds_read_b128 v[148:151], v36 offset:2304\n"
      "ds_read_b128 v[152:155], v36 offset:4608\n"
      "ds_read_b128 v[156:159], v36 offset:6912\n"
      "s_waitcnt lgkmcnt(2)\n"
      "v_add_f32 v144, v144, v148\n"
      "v_add_f32 v145, v145, v149\n"
      "v_add_f32 v146, v146, v150\n"
      "v_add_f32 v147, v147, v151\n"
      "s_waitcnt lgkmcnt(0)\n"
      "v_add_f32 v152, v152, v156\n"
      "v_add_f32 v153, v153, v157\n"
      "v_add_f32 v154, v154, v158\n"
      "v_add_f32 v155, v155, v159\n"
      "v_add_f32 v144, v144, v152\n"
      "v_add_f32 v146, v146, v154\n"
      "v_add_f32 v145, v145, v153\n"
      "v_add_f32 v147, v147, v155\n"
      "v_permlane32_swap_b32 v144, v146\n"
      "s_nop 0\n"
      "v_permlane32_swap_b32 v145, v147\n"
      "v_add_f32 v144, v144, v146\n"
      "v_add_f32 v145, v145, v147\n"
      "s_nop 1\n"
      "v_permlane16_swap_b32 v144, v145\n"
      "v_add_f32 v144, v144, v145\n"
      "v_mul_f32 v144, v144, v244\n"
      "v_mov_b32 v131, s15\n"
      "v_cvt_pk_bf16_f32 v144, v144, v144\n"
      "v_and_b32 v144, 0xffff0000, v144\n"
      "s_bitcmp1_b32 s43, 7\n"
      "s_cbranch_scc1 .Lfu%=\n"
      "v_mul_f32 v145, 0xbfb8aa3b, v144\n"
      "v_exp_f32 v145, v145\n"
      "s_nop 0\n"
      "v_add_f32 v145, 1.0, v145\n"
      "v_rcp_f32 v145, v145\n"
      "s_nop 0\n"
      "v_mul_f32 v144, v144, v145\n"
      ".Lfu%=:\n"
      "s_nop 1\n"
      "s_nop 0\n"
      "v_mov_b32_dpp v145, v144 quad_perm:[2,3,0,1] row_mask:0xf bank_mask:0xf\n"
      "v_cvt_pk_bf16_f32 v130, v144, v145\n"
      "buffer_store_dwordx2 v[130:131], v37, s[16:19], s45 offen sc1\n"
      "s_cmp_lg_u32 s33, 0\n"
      "s_cbranch_scc1 .Lfa%=\n"
      "v_mov_b32 v132, s15\n"
      "v_mov_b32 v133, s15\n"
      "buffer_store_dwordx2 v[132:133], off, s[16:19], s42 sc1\n"
      ".Lfa%=:\n"
      "s_movk_i32 s60, 8192\n"
      "buffer_load_dwordx4 v[208:211], v33, s[28:31], s60 offen\n"
      "s_movk_i32 s60, 10240\n"
      "buffer_load_dwordx4 v[212:215], v33, s[28:31], s60 offen\n"
      "s_movk_i32 s60, 12288\n"
      "buffer_load_dwordx4 v[216:219], v33, s[28:31], s60 offen\n"
      "s_movk_i32 s60, 14336\n"
      "buffer_load_dwordx4 v[220:223], v33, s[28:31], s60 offen\n"
      "s_sleep 6\n"
      "buffer_load_dwordx4 v[136:139], v38, s[16:19], s46 offen sc1\n"
      "s_waitcnt vmcnt(8)\n"
      "v_perm_b32 v64, v196, v192, s62\n"
      "v_perm_b32 v68, v196, v192, s63\n"
      "v_perm_b32 v72, v197, v193, s62\n"
      "v_perm_b32 v76, v197, v193, s63\n"
      "v_perm_b32 v80, v198, v194, s62\n"
      "v_perm_b32 v84, v198, v194, s63\n"
      "v_perm_b32 v88, v199, v195, s62\n"
      "v_perm_b32 v92, v199, v195, s63\n"
      "s_waitcnt vmcnt(6)\n"
      "v_perm_b32 v65, v204, v200, s62\n"
      "v_perm_b32 v69, v204, v200, s63\n"
      "v_perm_b32 v73, v205, v201, s62\n"
      "v_perm_b32 v77, v205, v201, s63\n"
      "v_perm_b32 v81, v206, v202, s62\n"
      "v_perm_b32 v85, v206, v202, s63\n"
      "v_perm_b32 v89, v207, v203, s62\n"
      "v_perm_b32 v93, v207, v203, s63\n"
      "s_waitcnt vmcnt(3)\n"
      "v_perm_b32 v66, v212, v208, s62\n"
      "v_perm_b32 v70, v212, v208, s63\n"
      "v_perm_b32 v74, v213, v209, s62\n"
      "v_perm_b32 v78, v213, v209, s63\n"
      "v_perm_b32 v82, v214, v210, s62\n"
      "v_perm_b32 v86, v214, v210, s63\n"
      "v_perm_b32 v90, v215, v211, s62\n"
      "v_perm_b32 v94, v215, v211, s63\n"
      "s_waitcnt vmcnt(1)\n"
      "v_perm_b32 v67, v220, v216, s62\n"
      "v_perm_b32 v71, v220, v216, s63\n"
      "v_perm_b32 v75, v221, v217, s62\n"
      "v_perm_b32 v79, v221, v217, s63\n"
      "v_perm_b32 v83, v222, v218, s62\n"
      "v_perm_b32 v87, v222, v218, s63\n"
      "v_perm_b32 v91, v223, v219, s62\n"
      "v_perm_b32 v95, v223, v219, s63\n"
      "s_movk_i32 s59, 0x7fff\n"
      "s_waitcnt vmcnt(0)\n"
      "s_nop 0\n"
      ".Lfg%=:\n"
      "v_cmp_ne_u32 vcc, s15, v137\n"
      "v_cmp_ne_u32 s[66:67], s15, v139\n"
      "s_nop 0\n"
      "s_or_b64 vcc, vcc, s[66:67]\n"
      "s_cbranch_scc1 .Lfgs%=\n"
      ".Lfgo%=:\n"
      "v_permlane32_swap_b32 v136, v138\n"
      "v_lshlrev_b32 v137, 16, v136\n"
      "v_lshlrev_b32 v139, 16, v138\n"
      "v_and_b32 v136, 0xffff0000, v136\n"
      "v_and_b32 v138, 0xffff0000, v138\n"
      "v_mul_f32 v137, v137, v139\n"
      "v_mul_f32 v136, v136, v138\n"
      "v_cvt_pk_bf16_f32 v136, v137, v136\n"
      "ds_write_b32 v39, v136 \n"
      "s_waitcnt lgkmcnt(0)\n"
      "s_barrier\n"
      "ds_read_b128 v[140:143], v40 \n"
      "ds_read_b128 v[136:139], v63 \n"
      "s_waitcnt lgkmcnt(1)\n"
      "v_mfma_f32_16x16x32_bf16 v[96:99], v[140:143], v[64:67], 0\n"
      "v_mfma_f32_16x16x32_bf16 v[100:103], v[140:143], v[68:71], 0\n"
      "v_mfma_f32_16x16x32_bf16 v[104:107], v[140:143], v[72:75], 0\n"
      "v_mfma_f32_16x16x32_bf16 v[108:111], v[140:143], v[76:79], 0\n"
      "s_waitcnt lgkmcnt(0)\n"
      "v_mfma_f32_16x16x32_bf16 v[96:99], v[136:139], v[80:83], v[96:99]\n"
      "v_mfma_f32_16x16x32_bf16 v[100:103], v[136:139], v[84:87], v[100:103]\n"
      "v_mfma_f32_16x16x32_bf16 v[104:107], v[136:139], v[88:91], v[104:107]\n"
      "v_mfma_f32_16x16x32_bf16 v[108:111], v[136:139], v[92:95], v[108:111]\n"
      "s_nop 4\n"
      "ds_write_b128 v41, v[96:99] offset:0\n"
      "ds_write_b128 v41, v[100:103] offset:16\n"
      "ds_write_b128 v41, v[104:107] offset:32\n"
      "ds_write_b128 v41, v[108:111] offset:48\n"
      "s_waitcnt lgkmcnt(0)\n"
      "s_barrier\n"
      "ds_read_b128 v[144:147], v42 offset:0\n"
      "ds_read_b128 v[148:151], v42 offset:8704\n"
      "ds_read_b128 v[152:155], v42 offset:17408\n"
      "ds_read_b128 v[156:159], v42 offset:26112\n"
      "v_mov_b32 v161, s15\n"
      "v_mov_b32 v163, s15\n"
      "s_waitcnt lgkmcnt(2)\n"
      "v_add_f32 v144, v144, v148\n"
      "v_add_f32 v145, v145, v149\n"
      "v_add_f32 v146, v146, v150\n"
      "v_add_f32 v147, v147, v151\n"
      "s_waitcnt lgkmcnt(0)\n"
      "v_add_f32 v152, v152, v156\n"
      "v_add_f32 v153, v153, v157\n"
      "v_add_f32 v154, v154, v158\n"
      "v_add_f32 v155, v155, v159\n"
      "v_add_f32 v144, v144, v152\n"
      "v_add_f32 v145, v145, v153\n"
      "v_add_f32 v146, v146, v154\n"
      "v_add_f32 v147, v147, v155\n"
      "v_cvt_pk_bf16_f32 v160, v144, v145\n"
      "v_cvt_pk_bf16_f32 v162, v146, v147\n"
      "buffer_store_dwordx4 v[160:163], v43, s[16:19], s47 offen sc1\n"
      "s_waitcnt lgkmcnt(0)\n"
      "s_endpgm\n"
      ".Lfss%=:\n"
      "s_sleep 1\n"
      "s_sub_u32 s59, s59, 1\n"
      "s_cbranch_scc1 .Lfso%=\n"
      "buffer_load_dwordx2 v[240:241], v239, s[16:19], s60 offen sc1\n"
      "s_waitcnt vmcnt(0)\n"
      "s_branch .Lfs%=\n"
      ".Lfgs%=:\n"
      "s_sleep 1\n"
      "s_sub_u32 s59, s59, 1\n"
      "s_cbranch_scc1 .Lfgo%=\n"
      "buffer_load_dwordx4 v[136:139], v38, s[16:19], s46 offen sc1\n"
      "s_waitcnt vmcnt(0)\n"
      "s_branch .Lfg%=\n"
      "s_endpgm\n"
      ".LS%=:\n"
      "v_lshlrev_b32 v112, 4, v0\n"
      "v_lshlrev_b32 v113, 2, v0\n"
      "v_sub_u32 v113, 0xfff, v113\n"
      "v_lshlrev_b32 v115, 2, v48\n"
      "v_mov_b32 v116, 0\n"
      "ds_write_b32 v115, v116 \n"
      "ds_write_b32 v116, v116 offset:288\n"
      "v_add_u32 v115, 0x140, v115\n"
      "v_mov_b32 v114, v115\n"
      "s_lshl_b32 s50, s33, 2\n"
      "s_add_u32 s50, s50, 256\n"
      "v_writelane_b32 v114, s50, 63\n"
      "s_lshl_b32 s50, s33, 5\n"
      "v_lshl_add_u32 v117, v49, 2, s50\n"
      "v_lshlrev_b32 v118, 5, v50\n"
      "s_and_b32 s50, s49, 1\n"
      "s_lshl_b32 s50, s50, 10\n"
      "v_lshl_add_u32 v119, v50, 7, s50\n"
      "s_movk_i32 s51, 24576\n"
      "s_mov_b32 s52, 0x80008000\n"
      "s_mov_b32 s53, 0xffff0000\n"
      "s_movk_i32 s59, 0x7fff\n"
      "s_sleep 24\n"
      ".Lt_p%=:\n"
      "buffer_load_dwordx4 v[72:75], v112, s[16:19], s51 offen sc1\n"
      "s_waitcnt vmcnt(0)\n"
      "v_cmp_ne_u32 vcc, s15, v73\n"
      "v_cmp_ne_u32 s[54:55], s15, v75\n"
      "v_pk_ashrrev_i16 v81, 15, v72 op_sel_hi:[0,1]\n"
      "v_pk_ashrrev_i16 v82, 15, v74 op_sel_hi:[0,1]\n"
      "s_or_b64 vcc, vcc, s[54:55]\n"
      "s_cbranch_scc1 .Lt_ps%=\n"
      ".Lt_po%=:\n"
      "v_bitop3_b32 v81, v72, v81, s52 bitop3:0x1e\n"
      "v_bitop3_b32 v82, v74, v82, s52 bitop3:0x1e\n"
      "v_lshl_or_b32 v76, v81, 16, v113\n"
      "v_and_or_b32 v77, v81, s53, v113\n"
      "v_lshl_or_b32 v78, v82, 16, v113\n"
      "v_and_or_b32 v79, v82, s53, v113\n"
      "v_add_u32 v77, -1, v77\n"
      "v_add_u32 v78, -2, v78\n"
      "v_add_u32 v79, -3, v79\n"
      "v_max3_u32 v80, v76, v77, v78\n"
      "v_max_u32 v80, v80, v79\n"
      "s_nop 1\n"
      "v_max_u32_dpp v80, v80, v80 row_shr:1 row_mask:0xf bank_mask:0xf bound_ctrl:1\n"
      "s_nop 1\n"
      "v_max_u32_dpp v80, v80, v80 row_shr:2 row_mask:0xf bank_mask:0xf bound_ctrl:1\n"
      "s_nop 1\n"
      "v_max_u32_dpp v80, v80, v80 row_shr:4 row_mask:0xf bank_mask:0xf bound_ctrl:1\n"
      "s_nop 1\n"
      "v_max_u32_dpp v80, v80, v80 row_shr:8 row_mask:0xf bank_mask:0xf bound_ctrl:1\n"
      "s_nop 1\n"
      "v_max_u32_dpp v80, v80, v80 row_bcast:15 row_mask:0xa bank_mask:0xf\n"
      "s_nop 1\n"
      "v_max_u32_dpp v80, v80, v80 row_bcast:31 row_mask:0xc bank_mask:0xf\n"
      "ds_write_b32 v114, v80 \n"
      "s_waitcnt lgkmcnt(0)\n"
      "s_barrier\n"
      "ds_read_b128 v[92:95], v116 offset:256\n"
      "ds_read_b128 v[96:99], v116 offset:272\n"
      "s_waitcnt lgkmcnt(1)\n"
      "v_min3_u32 v81, v92, v93, v94\n"
      "s_waitcnt lgkmcnt(0)\n"
      "v_min3_u32 v82, v96, v97, v98\n"
      "v_min3_u32 v81, v81, v95, v99\n"
      "v_min_u32 v81, v81, v82\n"
      "v_cmp_ge_u32 s[60:61], v76, v81\n"
      "v_cmp_ge_u32 s[62:63], v77, v81\n"
      "v_cmp_ge_u32 s[64:65], v78, v81\n"
      "v_cmp_ge_u32 s[66:67], v79, v81\n"
      "s_bcnt1_i32_b64 s68, s[60:61]\n"
      "s_bcnt1_i32_b64 s69, s[62:63]\n"
      "s_bcnt1_i32_b64 s70, s[64:65]\n"
      "s_bcnt1_i32_b64 s71, s[66:67]\n"
      "s_add_u32 s69, s69, s68\n"
      "s_add_u32 s70, s70, s69\n"
      "s_add_u32 s71, s71, s70\n"
      "v_mov_b32 v82, s71\n"
      "s_mov_b64 exec, 1\n"
      "ds_add_rtn_u32 v83, v116, v82 offset:288\n"
      "s_mov_b64 exec, -1\n"
      "v_mbcnt_lo_u32_b32 v84, s60, 0\n"
      "v_mbcnt_hi_u32_b32 v84, s61, v84\n"
      "v_mbcnt_lo_u32_b32 v85, s62, 0\n"
      "v_mbcnt_hi_u32_b32 v85, s63, v85\n"
      "v_add_u32 v85, s68, v85\n"
      "v_mbcnt_lo_u32_b32 v86, s64, 0\n"
      "v_mbcnt_hi_u32_b32 v86, s65, v86\n"
      "v_add_u32 v86, s69, v86\n"
      "v_mbcnt_lo_u32_b32 v87, s66, 0\n"
      "v_mbcnt_hi_u32_b32 v87, s67, v87\n"
      "v_add_u32 v87, s70, v87\n"
      "s_waitcnt lgkmcnt(0)\n"
      "v_readfirstlane_b32 s72, v83\n"
      "s_nop 1\n"
      "v_add_lshl_u32 v84, v84, s72, 2\n"
      "v_add_lshl_u32 v85, v85, s72, 2\n"
      "v_add_lshl_u32 v86, v86, s72, 2\n"
      "v_add_lshl_u32 v87, v87, s72, 2\n"
      "v_and_b32 v84, 0xff, v84\n"
      "v_and_b32 v85, 0xff, v85\n"
      "v_and_b32 v86, 0xff, v86\n"
      "v_and_b32 v87, 0xff, v87\n"
      "v_cndmask_b32 v84, v115, v84, s[60:61]\n"
      "ds_write_b32 v84, v76 \n"
      "v_cndmask_b32 v85, v115, v85, s[62:63]\n"
      "ds_write_b32 v85, v77 \n"
      "v_cndmask_b32 v86, v115, v86, s[64:65]\n"
      "ds_write_b32 v86, v78 \n"
      "v_cndmask_b32 v87, v115, v87, s[66:67]\n"
      "ds_write_b32 v87, v79 \n"
      "s_waitcnt lgkmcnt(0)\n"
      "s_barrier\n"
      "ds_read_b32 v81, v116 offset:288\n"
      ".Lt_r%=:\n"
      "ds_read_b32 v100, v117 \n"
      "ds_read_b128 v[92:95], v118 \n"
      "ds_read_b128 v[96:99], v118 offset:16\n"
      "v_mov_b32 v101, s15\n"
      "v_mov_b32 v89, 0\n"
      "s_waitcnt lgkmcnt(3)\n"
      "v_cmp_lt_u32 vcc, 64, v81\n"
      "s_movk_i32 s50, 0x7ff\n"
      "s_cbranch_vccnz .Lt_m%=\n"
      "s_waitcnt lgkmcnt(1)\n"
      "v_cmp_gt_u32 s[60:61], v92, v100\n"
      "v_cmp_gt_u32 s[62:63], v93, v100\n"
      "v_cmp_gt_u32 s[64:65], v94, v100\n"
      "v_cmp_gt_u32 s[66:67], v95, v100\n"
      "s_waitcnt lgkmcnt(0)\n"
      "v_cmp_gt_u32 s[68:69], v96, v100\n"
      "v_cmp_gt_u32 s[70:71], v97, v100\n"
      "v_cmp_gt_u32 s[72:73], v98, v100\n"
      "v_cmp_gt_u32 s[74:75], v99, v100\n"
      "v_bitop3_b32 v81, v100, s50, v100 bitop3:0x0c\n"
      "v_addc_co_u32 v89, vcc, 0, v89, s[60:61]\n"
      "v_addc_co_u32 v89, vcc, 0, v89, s[62:63]\n"
      "v_addc_co_u32 v89, vcc, 0, v89, s[64:65]\n"
      "v_addc_co_u32 v89, vcc, 0, v89, s[66:67]\n"
      "v_addc_co_u32 v89, vcc, 0, v89, s[68:69]\n"
      "v_addc_co_u32 v89, vcc, 0, v89, s[70:71]\n"
      "v_addc_co_u32 v89, vcc, 0, v89, s[72:73]\n"
      "v_addc_co_u32 v89, vcc, 0, v89, s[74:75]\n"
      "v_lshl_add_u32 v82, v81, 11, v119\n"
      "v_lshlrev_b32 v81, 3, v81\n"
      "v_add_u32_dpp v89, v89, v89 row_ror:8 row_mask:0xf bank_mask:0xf\n"
      "v_mov_b32 v83, v89\n"
      "s_nop 1\n"
      "v_permlane16_swap_b32 v89, v83\n"
      "v_add_u32 v89, v89, v83\n"
      "v_mov_b32 v83, v89\n"
      "s_nop 1\n"
      "v_permlane32_swap_b32 v89, v83\n"
      "v_add_u32 v89, v89, v83\n"
      "v_cmp_gt_u32 vcc, 8, v89\n"
      "v_lshlrev_b32 v89, 3, v89\n"
      "s_movk_i32 s51, 8192\n"
      "s_cbranch_vccz .Lt_n%=\n"
      "s_and_b64 exec, vcc, 0xff\n"
      "ds_write_b32 v89, v100 offset:640\n"
      "buffer_load_dword v84, v81, s[16:19], s51 offen sc1\n"
      "s_mov_b64 exec, vcc\n"
      "buffer_load_dword v85, v82, s[20:23], 0 offen\n"
      "s_mov_b64 exec, -1\n"
      ".Lt_n%=:\n"
      "v_lshlrev_b32 v89, 3, v49\n"
      "s_waitcnt lgkmcnt(0)\n"
      "s_barrier\n"
      "s_cmp_lg_u32 s33, 0\n"
      "s_cbranch_scc1 .Lt_a%=\n"
      "ds_read_b32 v100, v89 offset:640\n"
      "s_mov_b64 exec, 0xff\n"
      "s_waitcnt lgkmcnt(0)\n"
      "buffer_store_dwordx2 v[100:101], v89, s[16:19], s43 offen sc1\n"
      "s_mov_b64 exec, -1\n"
      "s_nop 1\n"
      "v_mov_b32 v100, s15\n"
      "buffer_store_dwordx2 v[100:101], off, s[16:19], s42 sc1\n"
      ".Lt_a%=:\n"
      "s_cmp_lg_u32 s34, 127\n"
      "s_cbranch_scc1 .Lt_e%=\n"
      "s_cmp_ge_u32 s33, 4\n"
      "s_cbranch_scc1 .Lt_kb%=\n"
      "v_lshlrev_b32 v81, 3, v0\n"
      "s_movk_i32 s51, 4096\n"
      "s_movk_i32 s59, 0x7fff\n"
      ".Lt_k%=:\n"
      "buffer_load_dword v86, v81, s[16:19], s51 offen offset:4 sc1\n"
      "s_waitcnt vmcnt(0)\n"
      "v_cmp_ne_u32 vcc, s15, v86\n"
      "s_nop 1\n"
      "s_cbranch_vccz .Lt_kb%=\n"
      "s_sleep 16\n"
      "s_sub_u32 s59, s59, 1\n"
      "s_cbranch_scc0 .Lt_k%=\n"
      ".Lt_kb%=:\n"
      "s_barrier\n"
      "s_cmp_lg_u32 s33, 0\n"
      "s_cbranch_scc1 .Lt_e%=\n"
      "buffer_store_dword v101, off, s[16:19], 0 sc1\n"
      ".Lt_e%=:\n"
      "s_waitcnt vmcnt(0)\n"
      "s_endpgm\n"
      ".Lt_ps%=:\n"
      "s_sleep 1\n"
      "s_sub_u32 s59, s59, 1\n"
      "s_cbranch_scc0 .Lt_p%=\n"
      "s_branch .Lt_po%=\n"
      ".Lt_m%=:\n"
      "s_waitcnt lgkmcnt(0)\n"
      "s_barrier\n"
      "s_mov_b32 s59, 0\n"
      "v_mov_b32 v90, 0\n"
      ".Lt_ml%=:\n"
      "s_nop 1\n"
      "v_max3_u32 v80, v76, v77, v78\n"
      "v_max_u32 v80, v80, v79\n"
      "s_nop 1\n"
      "v_max_u32_dpp v80, v80, v80 row_shr:1 row_mask:0xf bank_mask:0xf bound_ctrl:1\n"
      "s_nop 1\n"
      "v_max_u32_dpp v80, v80, v80 row_shr:2 row_mask:0xf bank_mask:0xf bound_ctrl:1\n"
      "s_nop 1\n"
      "v_max_u32_dpp v80, v80, v80 row_shr:4 row_mask:0xf bank_mask:0xf bound_ctrl:1\n"
      "s_nop 1\n"
      "v_max_u32_dpp v80, v80, v80 row_shr:8 row_mask:0xf bank_mask:0xf bound_ctrl:1\n"
      "s_nop 1\n"
      "v_max_u32_dpp v80, v80, v80 row_bcast:15 row_mask:0xa bank_mask:0xf\n"
      "s_nop 1\n"
      "v_max_u32_dpp v80, v80, v80 row_bcast:31 row_mask:0xc bank_mask:0xf\n"
      "s_nop 0\n"
      "v_readlane_b32 s50, v80, 63\n"
      "s_mov_b32 m0, s59\n"
      "s_nop 0\n"
      "v_cmp_ne_u32 vcc, s50, v76\n"
      "s_nop 1\n"
      "v_cndmask_b32 v76, 0, v76, vcc\n"
      "v_cmp_ne_u32 vcc, s50, v77\n"
      "s_nop 1\n"
      "v_cndmask_b32 v77, 0, v77, vcc\n"
      "v_cmp_ne_u32 vcc, s50, v78\n"
      "s_nop 1\n"
      "v_cndmask_b32 v78, 0, v78, vcc\n"
      "v_cmp_ne_u32 vcc, s50, v79\n"
      "s_nop 1\n"
      "v_cndmask_b32 v79, 0, v79, vcc\n"
      "v_writelane_b32 v90, s50, m0\n"
      "s_add_u32 s59, s59, 1\n"
      "s_cmp_lt_u32 s59, 8\n"
      "s_cbranch_scc1 .Lt_ml%=\n"
      "s_mov_b64 exec, 0xff\n"
      "ds_write_b32 v117, v90 \n"
      "s_mov_b64 exec, -1\n"
      "v_mov_b32 v81, 0\n"
      "s_waitcnt lgkmcnt(0)\n"
      "s_barrier\n"
      "s_branch .Lt_r%=\n"
      ".LC%=:\n"
      "s_setprio 3\n"
      "s_mov_b32 s16, s88\n"
      "s_and_b32 s17, s89, 0xffff\n"
      "s_mov_b32 s18, 0x164000\n"
      "s_mov_b32 s19, 0x20000\n"
      "s_mov_b32 s20, s64\n"
      "s_and_b32 s21, s65, 0xffff\n"
      "s_mov_b32 s22, 0x400000\n"
      "s_mov_b32 s23, 0x20000\n"
      "s_mov_b32 s24, s86\n"
      "s_and_b32 s25, s87, 0xffff\n"
      "s_mov_b32 s26, 0x400000\n"
      "s_mov_b32 s27, 0x20000\n"
      "s_mul_i32 s39, s33, 6528\n"
      "s_mul_i32 s40, s33, 8768\n"
      "s_add_u32 s52, s40, 52224\n"
      "s_add_u32 s53, s52, 4160\n"
      "s_mul_i32 s54, s33, 4864\n"
      "s_add_u32 s54, s54, 122368\n"
      "s_lshl_b32 s41, s35, 4\n"
      "s_lshr_b32 s14, s35, 5\n"
      "s_lshl_b32 s14, s14, 8\n"
      "s_add_u32 s14, s14, 45056\n"
      "s_mul_i32 s44, s35, 7168\n"
      "s_add_u32 s44, s44, 540672\n"
      "s_lshl_b32 s42, s34, 3\n"
      "s_add_u32 s42, s42, 4096\n"
      "s_lshl_b32 s43, s48, 8\n"
      "s_and_b32 s50, s49, 1\n"
      "s_lshl_b32 s50, s50, 7\n"
      "s_add_u32 s43, s43, s50\n"
      "s_add_u32 s43, s43, 40960\n"
      "v_and_b32 v48, 31, v43\n"
      "v_lshrrev_b32 v49, 5, v43\n"
      "v_and_b32 v50, 15, v43\n"
      "v_lshrrev_b32 v51, 4, v43\n"
      "s_sub_u32 s55, s49, 16\n"
      "s_lshl_b32 s50, s33, 4\n"
      "s_add_u32 s50, s50, s55\n"
      "s_lshl_b32 s50, s50, 15\n"
      "s_lshl_b32 s51, s48, 8\n"
      "s_add_u32 s50, s50, s51\n"
      "v_lshrrev_b32 v52, 1, v48\n"
      "v_and_b32 v53, 1, v43\n"
      "v_lshl_add_u32 v52, v52, 4, v53\n"
      "v_lshl_add_u32 v56, v52, 7, s50\n"
      "s_bitcmp1_b32 s33, 0\n"
      "s_cselect_b32 s56, s82, s80\n"
      "s_cselect_b32 s57, s83, s81\n"
      "s_lshr_b32 s50, s56, 7\n"
      "s_sub_u32 s50, s48, s50\n"
      "s_add_u32 s50, s50, 3\n"
      "s_and_b32 s50, s50, 7\n"
      "s_min_u32 s50, s50, 6\n"
      "s_mul_i32 s51, s48, 7\n"
      "s_add_u32 s50, s50, s51\n"
      "s_lshl_b32 s50, s50, 7\n"
      "s_lshl_b32 s51, s55, 2\n"
      "s_lshr_b32 s58, s33, 1\n"
      "s_add_u32 s51, s51, s58\n"
      "s_mul_i32 s51, s51, 114688\n"
      "s_add_u32 s50, s50, s51\n"
      "v_mul_u32_u24 v57, 0x1c00, v50\n"
      "v_add_u32 v57, s50, v57\n"
      "v_lshlrev_b32 v55, 4, v49\n"
      "v_mul_u32_u24 v52, 0x110, v48\n"
      "v_add3_u32 v20, v52, v55, s52\n"
      "v_and_b32 v52, 12, v43\n"
      "s_mov_b32 s50, 0x6600\n"
      "s_mov_b32 s51, 0x6600\n"
      "v_cndmask_b32 v52, v48, v52, s[50:51]\n"
      "s_mov_b32 s50, 0xff000000\n"
      "s_mov_b32 s51, 0xff000000\n"
      "v_cndmask_b32 v52, v52, 9, s[50:51]\n"
      "v_mul_u32_u24 v52, 0x110, v52\n"
      "v_add3_u32 v21, v52, v55, s39\n"
      "v_lshl_add_u32 v52, v48, 1, v49\n"
      "v_lshlrev_b32 v54, 3, v52\n"
      "s_add_u32 s50, s53, 570\n"
      "v_sub_u32 v22, s50, v54\n"
      "s_lshl_b32 s50, s33, 17\n"
      "v_lshl_add_u32 v8, v52, 11, s50\n"
      "v_mov_b32 v52, 0x63e\n"
      "v_mov_b32 v54, 0x6de\n"
      "s_mov_b32 s50, 0x55555555\n"
      "s_mov_b32 s51, 0x55555555\n"
      "v_cndmask_b32 v24, v52, v54, s[50:51]\n"
      "v_mov_b32 v52, 0x4de\n"
      "v_mov_b32 v54, 0x59e\n"
      "v_cndmask_b32 v52, v52, v54, s[50:51]\n"
      "s_mov_b32 s58, 0\n"
      "s_mov_b32 s59, -1\n"
      "v_cndmask_b32 v24, v24, v52, s[58:59]\n"
      "v_add_u32 v52, -1, v48\n"
      "v_and_b32 v52, -2, v52\n"
      "v_sub_u32 v24, v24, v52\n"
      "v_add_u32 v24, s53, v24\n"
      "v_add_u32 v23, 32, v24\n"
      "s_add_u32 s60, s53, 2048\n"
      "v_writelane_b32 v23, s60, 0\n"
      "v_writelane_b32 v23, s60, 32\n"
      "v_mul_u32_u24 v52, 0xc1, v53\n"
      "v_sub_u32 v52, v52, v48\n"
      "s_add_u32 s60, s53, 1150\n"
      "v_add_u32 v52, s60, v52\n"
      "v_add_u32 v25, 0xffffffc0, v24\n"
      "v_cndmask_b32 v25, v25, v52, s[58:59]\n"
      "v_and_b32 v52, -2, v43\n"
      "v_add_u32 v52, 58, v52\n"
      "v_mov_b32 v54, 0x810\n"
      "s_mov_b32 s60, 0xfffff000\n"
      "s_mov_b32 s61, 0xffffffff\n"
      "v_cndmask_b32 v52, v52, v54, s[60:61]\n"
      "s_movk_i32 s60, 1408\n"
      "s_movk_i32 s61, 1664\n"
      "v_writelane_b32 v52, s60, 8\n"
      "v_writelane_b32 v52, s60, 9\n"
      "v_writelane_b32 v52, s61, 10\n"
      "v_writelane_b32 v52, s61, 11\n"
      "v_mul_u32_u24 v54, 0x83e, v53\n"
      "v_add3_u32 v26, v52, v54, s53\n"
      "v_lshlrev_b32 v52, 1, v50\n"
      "v_add_u32 v52, v52, v54\n"
      "s_add_u32 s60, s53, 2140\n"
      "v_sub_u32 v52, s60, v52\n"
      "v_lshl_add_u32 v29, v51, 4, v52\n"
      "v_lshl_add_u32 v30, v51, 3, v52\n"
      "v_add_u32 v30, 0x440, v30\n"
      "v_bfe_u32 v52, v43, 2, 3\n"
      "v_max_u32 v54, 5, v52\n"
      "v_mul_u32_u24 v52, 0x60, v52\n"
      "v_lshl_add_u32 v52, v54, 6, v52\n"
      "v_add_u32 v52, 0x2a0, v52\n"
      "s_mov_b32 s60, 0xf\n"
      "s_mov_b32 s61, 0xf\n"
      "v_cndmask_b32 v52, v52, v55, s[60:61]\n"
      "v_mul_u32_u24 v54, 0x830, v49\n"
      "v_add3_u32 v27, v52, v54, s53\n"
      "v_and_b32 v55, 3, v43\n"
      "v_lshl_add_u32 v27, v55, 4, v27\n"
      "s_add_u32 s60, s39, 3104\n"
      "s_add_u32 s61, s52, 2080\n"
      "v_mov_b32 v52, s39\n"
      "v_mov_b32 v54, s60\n"
      "s_mov_b32 s50, 0xff00ff00\n"
      "s_mov_b32 s51, 0xff00ff00\n"
      "v_cndmask_b32 v28, v52, v54, s[50:51]\n"
      "v_mov_b32 v52, s52\n"
      "v_mov_b32 v54, s61\n"
      "v_cndmask_b32 v52, v52, v54, s[50:51]\n"
      "s_mov_b32 s50, 0xffff0000\n"
      "s_mov_b32 s51, 0xffff0000\n"
      "v_cndmask_b32 v28, v28, v52, s[50:51]\n"
      "v_and_b32 v52, 7, v43\n"
      "v_lshl_add_u32 v28, v52, 2, v28\n"
      "v_lshlrev_b32 v10, 1, v52\n"
      "v_and_b32 v52, 8, v43\n"
      "v_lshl_add_u32 v52, v51, 1, v52\n"
      "v_lshlrev_b32 v9, 2, v52\n"
      "v_lshl_add_u32 v58, v49, 8, v48\n"
      "s_lshl_b32 s50, s33, 5\n"
      "v_add_u32 v58, s50, v58\n"
      "s_mov_b32 s50, 122912\n"
      "v_lshl_add_u32 v32, v58, 2, s50\n"
      "v_lshlrev_b32 v41, 12, v58\n"
      "v_lshlrev_b32 v42, 4, v58\n"
      "v_lshl_add_u32 v52, v50, 1, v51\n"
      "v_lshl_add_u32 v33, v52, 4, s54\n"
      "s_add_u32 s50, s54, 528\n"
      "v_lshl_add_u32 v34, v43, 4, s50\n"
      "v_lshl_add_u32 v35, v43, 3, s39\n"
      "v_lshl_add_u32 v36, v43, 3, s52\n"
      "s_add_u32 s50, s39, 32\n"
      "v_lshl_add_u32 v37, v43, 4, s50\n"
      "s_add_u32 s50, s52, 32\n"
      "v_lshl_add_u32 v38, v43, 4, s50\n"
      "v_lshl_add_u32 v52, v50, 2, v51\n"
      "v_lshl_add_u32 v39, v52, 2, s53\n"
      "v_bfe_u32 v52, v43, 2, 2\n"
      "v_mul_u32_u24 v52, 0x2240, v52\n"
      "v_lshl_add_u32 v52, v49, 11, v52\n"
      "v_lshl_add_u32 v52, v55, 3, v52\n"
      "v_and_b32 v54, 16, v43\n"
      "v_lshl_add_u32 v52, v54, 1, v52\n"
      "s_lshl_b32 s50, s33, 6\n"
      "s_add_u32 s50, s50, 56384\n"
      "v_add_u32 v40, s50, v52\n"
      "v_mov_b32 v54, 0\n"
      "v_mov_b32 v55, 0\n"
      "v_lshl_add_u32 v52, v43, 3, s54\n"
      "ds_write_b64 v52, v[54:55] offset:32\n"
      "s_mov_b32 s50, 161536\n"
      "v_lshl_add_u32 v52, v0, 2, s50\n"
      "ds_write_b32 v52, v54 \n"
      "v_lshl_add_u32 v52, v43, 2, s39\n"
      "v_mov_b32 v53, 0x3c00\n"
      "ds_write_b32 v52, v53 \n"
      "v_mov_b32 v52, 0xffff\n"
      "v_mov_b32 v53, 0xffff0000\n"
      "s_mov_b32 s50, 0x0\n"
      "s_mov_b32 s51, 0x18000018\n"
      "v_cndmask_b32 v44, 0, v52, s[50:51]\n"
      "s_mov_b32 s60, 0x0\n"
      "s_mov_b32 s61, 0x30000030\n"
      "v_cndmask_b32 v54, 0, v53, s[60:61]\n"
      "v_or_b32 v44, v44, v54\n"
      "s_mov_b32 s50, 0x0\n"
      "s_mov_b32 s51, 0x60000060\n"
      "v_cndmask_b32 v45, 0, v52, s[50:51]\n"
      "s_mov_b32 s60, 0x0\n"
      "s_mov_b32 s61, 0xc00000c0\n"
      "v_cndmask_b32 v54, 0, v53, s[60:61]\n"
      "v_or_b32 v45, v45, v54\n"
      "s_mov_b32 s50, 0x10000\n"
      "s_mov_b32 s51, 0x80000180\n"
      "v_cndmask_b32 v46, 0, v52, s[50:51]\n"
      "s_mov_b32 s60, 0x30000\n"
      "s_mov_b32 s61, 0x300\n"
      "v_cndmask_b32 v54, 0, v53, s[60:61]\n"
      "v_or_b32 v46, v46, v54\n"
      "s_mov_b32 s50, 0x60000\n"
      "s_mov_b32 s51, 0x600\n"
      "v_cndmask_b32 v47, 0, v52, s[50:51]\n"
      "s_mov_b32 s60, 0xc0000\n"
      "s_mov_b32 s61, 0xc00\n"
      "v_cndmask_b32 v54, 0, v53, s[60:61]\n"
      "v_or_b32 v47, v47, v54\n"
      "s_waitcnt vmcnt(0)\n"
      "s_mov_b32 s50, 0x3fb8aa3b\n"
      "global_load_dword v11, v56, s[64:65] \n"
      "global_load_dword v11, v57, s[56:57] \n"
      "v_lshlrev_b32 v48, 16, v12\n"
      "v_and_b32 v52, 0x7fff7fff, v18\n"
      "v_and_b32 v53, 0x7fff7fff, v19\n"
      "v_mul_f32 v49, s50, v48\n"
      "v_pk_max_u16 v52, v52, v53\n"
      "v_fma_f32 v50, v48, s50, -v49\n"
      "v_rndne_f32 v51, v49\n"
      "v_lshlrev_b32 v53, 16, v52\n"
      "v_fmamk_f32 v50, v48, 0x32a5705f, v50\n"
      "v_sub_f32 v49, v49, v51\n"
      "v_max_u32 v52, v52, v53\n"
      "v_add_f32 v49, v49, v50\n"
      "v_cvt_i32_f32 v51, v51\n"
      "v_max_u32_dpp v52, v52, v52 row_shr:1 row_mask:0xf bank_mask:0xf bound_ctrl:1\n"
      "v_exp_f32 v49, v49\n"
      "v_lshlrev_b32 v54, 16, v13\n"
      "v_max_u32_dpp v52, v52, v52 row_shr:2 row_mask:0xf bank_mask:0xf bound_ctrl:1\n"
      "v_ldexp_f32 v49, v49, v51\n"
      "v_add_f32 v54, 1.0, v54\n"
      "v_max_u32_dpp v52, v52, v52 row_shr:4 row_mask:0xf bank_mask:0xf bound_ctrl:1\n"
      "v_cvt_pk_bf16_f32 v49, v49, v49\n"
      "v_readfirstlane_b32 s15, v14\n"
      "v_max_u32_dpp v52, v52, v52 row_shr:8 row_mask:0xf bank_mask:0xf bound_ctrl:1\n"
      "v_and_b32 v49, 0xffff0000, v49\n"
      "v_and_b32 v55, 0x7fffffff, v54\n"
      "v_max_u32_dpp v52, v52, v52 row_bcast:15 row_mask:0xa bank_mask:0xf\n"
      "v_mul_f32 v50, 0xbfb8aa3b, v49\n"
      "v_mul_f32 v51, 0xbe22f983, v49\n"
      "v_max_u32_dpp v52, v52, v52 row_bcast:31 row_mask:0xc bank_mask:0xf\n"
      "v_readfirstlane_b32 s36, v50\n"
      "v_readlane_b32 s50, v52, 63\n"
      "v_readfirstlane_b32 s37, v51\n"
      "v_mul_f32 v55, 0x3b800000, v55\n"
      "v_mul_f32 v52, s50, v49\n"
      "s_add_u32 s15, s15, 0x9e3779b1\n"
      "v_max_f32 v52, v52, v55\n"
      "v_frexp_exp_i32_f32 v52, v52\n"
      "s_nop 0\n"
      "v_readfirstlane_b32 s50, v52\n"
      "s_nop 0\n"
      "s_sub_i32 s50, 7, s50\n"
      "s_max_i32 s50, s50, -107\n"
      "s_min_i32 s50, s50, 93\n"
      "v_ldexp_f32 v49, v49, s50\n"
      "s_sub_i32 s51, s50, 1\n"
      "v_ldexp_f32 v54, v54, s51\n"
      "s_sub_i32 s51, 128, s50\n"
      "s_lshl_b32 s51, s51, 23\n"
      "v_readfirstlane_b32 s38, v49\n"
      "s_mov_b64 s[60:61], 1\n"
      "v_cndmask_b32 v31, 0, v54, s[60:61]\n"
      "s_lshl_b32 s50, s33, 2\n"
      "s_add_u32 s50, s50, 161280\n"
      "v_mov_b32 v50, s50\n"
      "v_mov_b32 v51, s51\n"
      "ds_write_b32 v50, v51 \n"
      "s_mov_b32 s60, s41\n"
      "s_add_u32 s61, s41, 0x200000\n"
      "s_movk_i32 s62, 8192\n"
      "s_movk_i32 s63, 16384\n"
      "s_mov_b32 m0, s40\n"
      "v_lshlrev_b32 v72, 16, v16\n"
      "v_and_b32 v73, 0xffff0000, v16\n"
      "v_lshlrev_b32 v74, 16, v17\n"
      "v_and_b32 v75, 0xffff0000, v17\n"
      "s_mov_b32 s64, 0x3fb8aa3b\n"
      "s_mov_b32 s66, 0x43800000\n"
      "s_mov_b32 s68, 0x42000000\n"
      "s_mov_b32 s70, s37\n"
      "v_pk_mul_f32 v[72:73], s[64:65], v[72:73] op_sel_hi:[0,1]\n"
      "v_pk_mul_f32 v[74:75], s[70:71], v[74:75] op_sel_hi:[0,1]\n"
      "v_exp_f32 v72, v72\n"
      "v_exp_f32 v73, v73\n"
      "v_pk_mul_f32 v[78:79], s[66:67], v[74:75] op_sel_hi:[0,1]\n"
      "v_pk_mul_f32 v[80:81], s[68:69], v[74:75] op_sel_hi:[0,1]\n"
      "v_fract_f32 v76, v74\n"
      "v_fract_f32 v77, v75\n"
      "v_cvt_pk_bf16_f32 v72, v72, v73\n"
      "v_cos_f32 v90, v76\n"
      "v_cos_f32 v91, v77\n"
      "v_fract_f32 v78, v78\n"
      "v_fract_f32 v79, v79\n"
      "v_and_b32 v73, 0xffff0000, v72\n"
      "v_lshlrev_b32 v72, 16, v72\n"
      "v_sin_f32 v92, v76\n"
      "v_sin_f32 v93, v77\n"
      "v_pk_mul_f32 v[82:83], s[36:37], v[72:73] op_sel_hi:[0,1]\n"
      "v_fract_f32 v80, v80\n"
      "v_fract_f32 v81, v81\n"
      "v_exp_f32 v84, v82\n"
      "v_exp_f32 v85, v83\n"
      "v_pk_mul_f32 v[86:87], s[66:67], v[82:83] op_sel_hi:[0,1]\n"
      "v_cos_f32 v94, v78\n"
      "v_cos_f32 v95, v79\n"
      "v_pk_mul_f32 v[88:89], s[68:69], v[82:83] op_sel_hi:[0,1]\n"
      "v_sin_f32 v96, v78\n"
      "v_sin_f32 v97, v79\n"
      "v_pk_mul_f32 v[90:91], v[90:91], v[84:85]\n"
      "v_exp_f32 v86, v86\n"
      "v_exp_f32 v87, v87\n"
      "v_pk_mul_f32 v[92:93], v[92:93], v[84:85]\n"
      "v_pk_add_f32 v[82:83], v[90:91], v[92:93]\n"
      "v_cos_f32 v202, v80\n"
      "v_pk_add_f32 v[84:85], v[90:91], v[92:93] neg_lo:[0,1] neg_hi:[0,1]\n"
      "v_cos_f32 v150, v81\n"
      "v_pk_mul_f32 v[100:101], v[90:91], v[92:93]\n"
      "v_pk_mul_f32 v[94:95], v[94:95], v[86:87]\n"
      "v_pk_mul_f32 v[98:99], v[82:83], v[84:85]\n"
      "v_pk_mul_f32 v[96:97], v[96:97], v[86:87]\n"
      "v_pk_add_f32 v[100:101], v[100:101], v[100:101]\n"
      "v_sin_f32 v203, v80\n"
      "v_cvt_pk_f16_f32 v208, v90, v92\n"
      "v_sin_f32 v151, v81\n"
      "v_cvt_pk_f16_f32 v156, v91, v93\n"
      "v_exp_f32 v88, v88\n"
      "v_pk_add_f32 v[82:83], v[98:99], v[100:101]\n"
      "v_exp_f32 v89, v89\n"
      "v_pk_add_f32 v[84:85], v[98:99], v[100:101] neg_lo:[0,1] neg_hi:[0,1]\n"
      "v_pk_add_f32 v[114:115], v[94:95], v[96:97]\n"
      "v_pk_mul_f32 v[104:105], v[98:99], v[100:101]\n"
      "v_pk_add_f32 v[116:117], v[94:95], v[96:97] neg_lo:[0,1] neg_hi:[0,1]\n"
      "v_pk_mul_f32 v[102:103], v[82:83], v[84:85]\n"
      "v_pk_mul_f32 v[108:109], v[94:95], v[96:97]\n"
      "v_pk_add_f32 v[104:105], v[104:105], v[104:105]\n"
      "v_pk_mul_f32 v[106:107], v[114:115], v[116:117]\n"
      "v_cvt_pk_f16_f32 v209, v98, v100\n"
      "v_pk_add_f32 v[108:109], v[108:109], v[108:109]\n"
      "v_cvt_pk_f16_f32 v157, v99, v101\n"
      "v_cvt_pk_f16_f32 v212, v94, v96\n"
      "v_pk_add_f32 v[82:83], v[102:103], v[104:105]\n"
      "v_cvt_pk_f16_f32 v160, v95, v97\n"
      "v_pk_add_f32 v[84:85], v[102:103], v[104:105] neg_lo:[0,1] neg_hi:[0,1]\n"
      "v_pk_mul_f32 v[202:203], v[202:203], v[88:89] op_sel:[0,0] op_sel_hi:[1,0]\n"
      "v_pk_mul_f32 v[92:93], v[102:103], v[104:105]\n"
      "v_pk_mul_f32 v[150:151], v[150:151], v[88:89] op_sel:[0,1] op_sel_hi:[1,1]\n"
      "v_pk_mul_f32 v[90:91], v[82:83], v[84:85]\n"
      "v_pk_mul_f32 v[110:111], v[108:109], v[96:97]\n"
      "v_pk_add_f32 v[92:93], v[92:93], v[92:93]\n"
      "v_pk_mul_f32 v[112:113], v[108:109], v[94:95]\n"
      "v_cvt_pk_f16_f32 v210, v102, v104\n"
      "v_pk_fma_f32 v[110:111], v[106:107], v[94:95], v[110:111] neg_lo:[0,0,1] neg_hi:[0,0,1]\n"
      "v_cvt_pk_f16_f32 v158, v103, v105\n"
      "v_pk_fma_f32 v[112:113], v[106:107], v[96:97], v[112:113]\n"
      "v_pk_add_f32 v[82:83], v[90:91], v[92:93]\n"
      "v_cvt_pk_f16_f32 v213, v106, v108\n"
      "v_pk_add_f32 v[84:85], v[90:91], v[92:93] neg_lo:[0,1] neg_hi:[0,1]\n"
      "v_cvt_pk_f16_f32 v161, v107, v109\n"
      "v_pk_mul_f32 v[74:75], v[90:91], v[92:93]\n"
      "v_pk_add_f32 v[114:115], v[106:107], v[108:109]\n"
      "v_pk_mul_f32 v[72:73], v[82:83], v[84:85]\n"
      "v_pk_add_f32 v[116:117], v[106:107], v[108:109] neg_lo:[0,1] neg_hi:[0,1]\n"
      "v_pk_add_f32 v[74:75], v[74:75], v[74:75]\n"
      "v_pk_mul_f32 v[96:97], v[106:107], v[108:109]\n"
      "v_cvt_pk_f16_f32 v205, v90, v92\n"
      "v_pk_mul_f32 v[94:95], v[114:115], v[116:117]\n"
      "v_cvt_pk_f16_f32 v153, v91, v93\n"
      "v_pk_add_f32 v[96:97], v[96:97], v[96:97]\n"
      "v_pk_mul_f32 v[82:83], v[74:75], v[92:93]\n"
      "v_cvt_pk_f16_f32 v214, v110, v112\n"
      "v_pk_mul_f32 v[84:85], v[74:75], v[90:91]\n"
      "v_cvt_pk_f16_f32 v162, v111, v113\n"
      "v_pk_fma_f32 v[82:83], v[72:73], v[90:91], v[82:83] neg_lo:[0,0,1] neg_hi:[0,0,1]\n"
      "v_pk_mul_f32 v[76:77], v[100:101], v[104:105]\n"
      "v_pk_fma_f32 v[84:85], v[72:73], v[92:93], v[84:85]\n"
      "v_pk_mul_f32 v[78:79], v[100:101], v[102:103]\n"
      "v_cvt_pk_f16_f32 v206, v72, v74\n"
      "v_pk_fma_f32 v[76:77], v[98:99], v[102:103], v[76:77] neg_lo:[0,0,1] neg_hi:[0,0,1]\n"
      "v_cvt_pk_f16_f32 v154, v73, v75\n"
      "v_pk_fma_f32 v[78:79], v[98:99], v[104:105], v[78:79]\n"
      "v_cvt_pk_f16_f32 v207, v82, v84\n"
      "v_cvt_pk_f16_f32 v215, v94, v96\n"
      "v_cvt_pk_f16_f32 v155, v83, v85\n"
      "v_cvt_pk_f16_f32 v163, v95, v97\n"
      "v_cvt_pk_f16_f32 v211, v76, v78\n"
      "v_cvt_pk_f16_f32 v159, v77, v79\n"
      "v_lshlrev_b32 v198, 16, v18\n"
      "v_lshlrev_b32 v199, 16, v19\n"
      "v_pk_mul_f32 v[198:199], s[38:39], v[198:199] op_sel_hi:[0,1]\n"
      "v_and_b32 v146, 0xffff0000, v18\n"
      "v_and_b32 v147, 0xffff0000, v19\n"
      "v_pk_mul_f32 v[146:147], s[38:39], v[146:147] op_sel_hi:[0,1]\n"
      "v_cvt_pk_f16_f32 v240, v198, v199\n"
      "v_pk_mul_f32 v[200:201], v[198:199], v[202:203] op_sel_hi:[1,0]\n"
      "v_pk_mul_f16 v241, v240, v205 op_sel_hi:[0,1] neg_hi:[0,1]\n"
      "v_pk_mul_f16 v242, v240, v206 op_sel_hi:[0,1] neg_hi:[0,1]\n"
      "v_pk_mul_f16 v243, v240, v207 op_sel_hi:[0,1] neg_hi:[0,1]\n"
      "ds_write_addtid_b32 v240 offset:52224\n"
      "v_pk_fma_f32 v[200:201], v[198:199], v[202:203], v[200:201] op_sel:[1,1,0] op_sel_hi:[0,1,1] neg_hi:[0,1,0]\n"
      "v_pk_fma_f16 v241, v240, v205, v241 op_sel:[1,1,0] op_sel_hi:[1,0,1]\n"
      "v_pk_fma_f16 v242, v240, v206, v242 op_sel:[1,1,0] op_sel_hi:[1,0,1]\n"
      "ds_write_addtid_b32 v241 offset:52496\n"
      "v_pk_fma_f16 v243, v240, v207, v243 op_sel:[1,1,0] op_sel_hi:[1,0,1]\n"
      "ds_write_addtid_b32 v242 offset:52768\n"
      "ds_write_addtid_b32 v243 offset:53040\n"
      "v_cvt_pk_f16_f32 v244, v200, v201\n"
      "v_pk_mul_f32 v[198:199], v[200:201], v[202:203] op_sel_hi:[1,0]\n"
      "v_pk_mul_f16 v245, v244, v205 op_sel_hi:[0,1] neg_hi:[0,1]\n"
      "v_pk_mul_f16 v246, v244, v206 op_sel_hi:[0,1] neg_hi:[0,1]\n"
      "v_pk_mul_f16 v247, v244, v207 op_sel_hi:[0,1] neg_hi:[0,1]\n"
      "ds_write_addtid_b32 v244 offset:53312\n"
      "v_pk_fma_f32 v[198:199], v[200:201], v[202:203], v[198:199] op_sel:[1,1,0] op_sel_hi:[0,1,1] neg_hi:[0,1,0]\n"
      "v_pk_fma_f16 v245, v244, v205, v245 op_sel:[1,1,0] op_sel_hi:[1,0,1]\n"
      "v_pk_fma_f16 v246, v244, v206, v246 op_sel:[1,1,0] op_sel_hi:[1,0,1]\n"
      "ds_write_addtid_b32 v245 offset:53584\n"
      "v_pk_fma_f16 v247, v244, v207, v247 op_sel:[1,1,0] op_sel_hi:[1,0,1]\n"
      "ds_write_addtid_b32 v246 offset:53856\n"
      "ds_write_addtid_b32 v247 offset:54128\n"
      "v_cvt_pk_f16_f32 v240, v198, v199\n"
      "v_pk_mul_f32 v[200:201], v[198:199], v[202:203] op_sel_hi:[1,0]\n"
      "v_pk_mul_f16 v241, v240, v205 op_sel_hi:[0,1] neg_hi:[0,1]\n"
      "v_pk_mul_f16 v242, v240, v206 op_sel_hi:[0,1] neg_hi:[0,1]\n"
      "v_pk_mul_f16 v243, v240, v207 op_sel_hi:[0,1] neg_hi:[0,1]\n"
      "ds_write_addtid_b32 v240 offset:54400\n"
      "v_pk_fma_f32 v[200:201], v[198:199], v[202:203], v[200:201] op_sel:[1,1,0] op_sel_hi:[0,1,1] neg_hi:[0,1,0]\n"
      "v_pk_fma_f16 v241, v240, v205, v241 op_sel:[1,1,0] op_sel_hi:[1,0,1]\n"
      "v_pk_fma_f16 v242, v240, v206, v242 op_sel:[1,1,0] op_sel_hi:[1,0,1]\n"
      "ds_write_addtid_b32 v241 offset:54672\n"
      "v_pk_fma_f16 v243, v240, v207, v243 op_sel:[1,1,0] op_sel_hi:[1,0,1]\n"
      "ds_write_addtid_b32 v242 offset:54944\n"
      "ds_write_addtid_b32 v243 offset:55216\n"
      "v_cvt_pk_f16_f32 v244, v200, v201\n"
      "v_pk_mul_f32 v[198:199], v[200:201], v[202:203] op_sel_hi:[1,0]\n"
      "v_pk_mul_f16 v245, v244, v205 op_sel_hi:[0,1] neg_hi:[0,1]\n"
      "v_pk_mul_f16 v246, v244, v206 op_sel_hi:[0,1] neg_hi:[0,1]\n"
      "v_pk_mul_f16 v247, v244, v207 op_sel_hi:[0,1] neg_hi:[0,1]\n"
      "ds_write_addtid_b32 v244 offset:55488\n"
      "v_pk_fma_f32 v[198:199], v[200:201], v[202:203], v[198:199] op_sel:[1,1,0] op_sel_hi:[0,1,1] neg_hi:[0,1,0]\n"
      "v_pk_fma_f16 v245, v244, v205, v245 op_sel:[1,1,0] op_sel_hi:[1,0,1]\n"
      "v_pk_fma_f16 v246, v244, v206, v246 op_sel:[1,1,0] op_sel_hi:[1,0,1]\n"
      "ds_write_addtid_b32 v245 offset:55760\n"
      "v_pk_fma_f16 v247, v244, v207, v247 op_sel:[1,1,0] op_sel_hi:[1,0,1]\n"
      "ds_write_addtid_b32 v246 offset:56032\n"
      "ds_write_addtid_b32 v247 offset:56304\n"
      "v_cvt_pk_f16_f32 v240, v198, v199\n"
      "v_pk_mul_f32 v[200:201], v[198:199], v[202:203] op_sel_hi:[1,0]\n"
      "v_pk_mul_f16 v241, v240, v205 op_sel_hi:[0,1] neg_hi:[0,1]\n"
      "v_pk_mul_f16 v242, v240, v206 op_sel_hi:[0,1] neg_hi:[0,1]\n"
      "v_pk_mul_f16 v243, v240, v207 op_sel_hi:[0,1] neg_hi:[0,1]\n"
      "ds_write_addtid_b32 v240 offset:56576\n"
      "v_pk_fma_f32 v[200:201], v[198:199], v[202:203], v[200:201] op_sel:[1,1,0] op_sel_hi:[0,1,1] neg_hi:[0,1,0]\n"
      "v_pk_fma_f16 v241, v240, v205, v241 op_sel:[1,1,0] op_sel_hi:[1,0,1]\n"
      "v_pk_fma_f16 v242, v240, v206, v242 op_sel:[1,1,0] op_sel_hi:[1,0,1]\n"
      "ds_write_addtid_b32 v241 offset:56848\n"
      "v_pk_fma_f16 v243, v240, v207, v243 op_sel:[1,1,0] op_sel_hi:[1,0,1]\n"
      "ds_write_addtid_b32 v242 offset:57120\n"
      "ds_write_addtid_b32 v243 offset:57392\n"
      "v_cvt_pk_f16_f32 v244, v200, v201\n"
      "v_pk_mul_f32 v[198:199], v[200:201], v[202:203] op_sel_hi:[1,0]\n"
      "v_pk_mul_f16 v245, v244, v205 op_sel_hi:[0,1] neg_hi:[0,1]\n"
      "v_pk_mul_f16 v246, v244, v206 op_sel_hi:[0,1] neg_hi:[0,1]\n"
      "v_pk_mul_f16 v247, v244, v207 op_sel_hi:[0,1] neg_hi:[0,1]\n"
      "ds_write_addtid_b32 v244 offset:57664\n"
      "v_pk_fma_f32 v[198:199], v[200:201], v[202:203], v[198:199] op_sel:[1,1,0] op_sel_hi:[0,1,1] neg_hi:[0,1,0]\n"
      "v_pk_fma_f16 v245, v244, v205, v245 op_sel:[1,1,0] op_sel_hi:[1,0,1]\n"
      "v_pk_fma_f16 v246, v244, v206, v246 op_sel:[1,1,0] op_sel_hi:[1,0,1]\n"
      "ds_write_addtid_b32 v245 offset:57936\n"
      "v_pk_fma_f16 v247, v244, v207, v247 op_sel:[1,1,0] op_sel_hi:[1,0,1]\n"
      "ds_write_addtid_b32 v246 offset:58208\n"
      "ds_write_addtid_b32 v247 offset:58480\n"
      "v_cvt_pk_f16_f32 v240, v198, v199\n"
      "v_pk_mul_f32 v[200:201], v[198:199], v[202:203] op_sel_hi:[1,0]\n"
      "v_pk_mul_f16 v241, v240, v205 op_sel_hi:[0,1] neg_hi:[0,1]\n"
      "v_pk_mul_f16 v242, v240, v206 op_sel_hi:[0,1] neg_hi:[0,1]\n"
      "v_pk_mul_f16 v243, v240, v207 op_sel_hi:[0,1] neg_hi:[0,1]\n"
      "ds_write_addtid_b32 v240 offset:58752\n"
      "v_pk_fma_f32 v[200:201], v[198:199], v[202:203], v[200:201] op_sel:[1,1,0] op_sel_hi:[0,1,1] neg_hi:[0,1,0]\n"
      "v_pk_fma_f16 v241, v240, v205, v241 op_sel:[1,1,0] op_sel_hi:[1,0,1]\n"
      "v_pk_fma_f16 v242, v240, v206, v242 op_sel:[1,1,0] op_sel_hi:[1,0,1]\n"
      "ds_write_addtid_b32 v241 offset:59024\n"
      "v_pk_fma_f16 v243, v240, v207, v243 op_sel:[1,1,0] op_sel_hi:[1,0,1]\n"
      "ds_write_addtid_b32 v242 offset:59296\n"
      "ds_write_addtid_b32 v243 offset:59568\n"
      "v_cvt_pk_f16_f32 v244, v200, v201\n"
      "v_pk_mul_f16 v245, v244, v205 op_sel_hi:[0,1] neg_hi:[0,1]\n"
      "v_pk_mul_f16 v246, v244, v206 op_sel_hi:[0,1] neg_hi:[0,1]\n"
      "v_pk_mul_f16 v247, v244, v207 op_sel_hi:[0,1] neg_hi:[0,1]\n"
      "ds_write_addtid_b32 v244 offset:59840\n"
      "v_pk_fma_f16 v245, v244, v205, v245 op_sel:[1,1,0] op_sel_hi:[1,0,1]\n"
      "v_pk_fma_f16 v246, v244, v206, v246 op_sel:[1,1,0] op_sel_hi:[1,0,1]\n"
      "ds_write_addtid_b32 v245 offset:60112\n"
      "v_pk_fma_f16 v247, v244, v207, v247 op_sel:[1,1,0] op_sel_hi:[1,0,1]\n"
      "ds_write_addtid_b32 v246 offset:60384\n"
      "ds_write_addtid_b32 v247 offset:60656\n"
      "s_mov_b32 m0, s39\n"
      "s_setprio 2\n"
      "v_pk_mul_f16 v240, v209, v208 op_sel_hi:[0,1]\n"
      "v_pk_mul_f16 v241, v210, v208 op_sel_hi:[0,1]\n"
      "v_pk_mul_f16 v242, v211, v208 op_sel_hi:[0,1]\n"
      "ds_write_addtid_b32 v208 offset:272\n"
      "v_pk_fma_f16 v240, v209, v208, v240 op_sel:[1,1,0] op_sel_hi:[1,0,1] neg_lo:[0,1,0]\n"
      "ds_write_addtid_b32 v209 offset:544\n"
      "v_pk_fma_f16 v241, v210, v208, v241 op_sel:[1,1,0] op_sel_hi:[1,0,1] neg_lo:[0,1,0]\n"
      "ds_write_addtid_b32 v240 offset:816\n"
      "v_pk_fma_f16 v242, v211, v208, v242 op_sel:[1,1,0] op_sel_hi:[1,0,1] neg_lo:[0,1,0]\n"
      "ds_write_addtid_b32 v241 offset:1360\n"
      "ds_write_addtid_b32 v242 offset:1904\n"
      "v_pk_mul_f16 v243, v212, v209 op_sel_hi:[0,1]\n"
      "v_pk_mul_f16 v244, v212, v210 op_sel_hi:[0,1]\n"
      "v_pk_mul_f16 v245, v212, v211 op_sel_hi:[0,1]\n"
      "ds_write_addtid_b32 v210 offset:1088\n"
      "v_pk_fma_f16 v243, v212, v209, v243 op_sel:[1,1,0] op_sel_hi:[1,0,1] neg_lo:[0,1,0]\n"
      "ds_write_addtid_b32 v211 offset:1632\n"
      "v_pk_fma_f16 v244, v212, v210, v244 op_sel:[1,1,0] op_sel_hi:[1,0,1] neg_lo:[0,1,0]\n"
      "ds_write_addtid_b32 v243 offset:2992\n"
      "v_pk_fma_f16 v245, v212, v211, v245 op_sel:[1,1,0] op_sel_hi:[1,0,1] neg_lo:[0,1,0]\n"
      "ds_write_addtid_b32 v244 offset:3264\n"
      "ds_write_addtid_b32 v245 offset:4080\n"
      "v_pk_mul_f16 v246, v213, v210 op_sel_hi:[0,1]\n"
      "v_pk_mul_f16 v247, v214, v210 op_sel_hi:[0,1]\n"
      "v_pk_mul_f16 v248, v215, v215 op_sel_hi:[0,1]\n"
      "ds_write_addtid_b32 v212 offset:2176\n"
      "v_pk_fma_f16 v246, v213, v210, v246 op_sel:[1,1,0] op_sel_hi:[1,0,1] neg_lo:[0,1,0]\n"
      "ds_write_addtid_b32 v213 offset:5440\n"
      "v_pk_fma_f16 v247, v214, v210, v247 op_sel:[1,1,0] op_sel_hi:[1,0,1] neg_lo:[0,1,0]\n"
      "ds_write_addtid_b32 v246 offset:5984\n"
      "v_pk_fma_f16 v248, v215, v215, v248 op_sel:[1,1,0] op_sel_hi:[1,0,1] neg_lo:[0,1,0]\n"
      "ds_write_addtid_b32 v247 offset:6256\n"
      "ds_write_addtid_b32 v248 offset:2448\n"
      "v_pk_mul_f16 v249, v215, v212 op_sel_hi:[0,1]\n"
      "v_pk_mul_f16 v250, v215, v213 op_sel_hi:[0,1]\n"
      "v_pk_mul_f16 v251, v215, v214 op_sel_hi:[0,1]\n"
      "ds_write_addtid_b32 v214 offset:5712\n"
      "v_pk_fma_f16 v249, v215, v212, v249 op_sel:[1,1,0] op_sel_hi:[1,0,1] neg_lo:[0,1,0]\n"
      "ds_write_addtid_b32 v215 offset:4352\n"
      "v_pk_fma_f16 v250, v215, v213, v250 op_sel:[1,1,0] op_sel_hi:[1,0,1] neg_lo:[0,1,0]\n"
      "ds_write_addtid_b32 v249 offset:4624\n"
      "v_pk_fma_f16 v251, v215, v214, v251 op_sel:[1,1,0] op_sel_hi:[1,0,1] neg_lo:[0,1,0]\n"
      "ds_write_addtid_b32 v250 offset:4896\n"
      "ds_write_addtid_b32 v251 offset:5168\n"
      "ds_read_b128 v[72:75], v21 offset:0\n"
      "ds_read_b128 v[104:107], v20 offset:0\n"
      "ds_read_b128 v[76:79], v21 offset:32\n"
      "ds_read_b128 v[108:111], v20 offset:32\n"
      "ds_read_b128 v[80:83], v21 offset:64\n"
      "ds_read_b128 v[112:115], v20 offset:64\n"
      "ds_read_b128 v[84:87], v21 offset:96\n"
      "ds_read_b128 v[116:119], v20 offset:96\n"
      "ds_read_b128 v[88:91], v21 offset:128\n"
      "ds_read_b128 v[120:123], v20 offset:128\n"
      "ds_read_b128 v[92:95], v21 offset:160\n"
      "ds_read_b128 v[124:127], v20 offset:160\n"
      "ds_read_b128 v[96:99], v21 offset:192\n"
      "ds_read_b128 v[128:131], v20 offset:192\n"
      "ds_read_b128 v[100:103], v21 offset:224\n"
      "ds_read_b128 v[132:135], v20 offset:224\n"
      "buffer_load_dwordx4 v[48:51], v41, s[20:23], s60 offen nt\n"
      "s_mov_b32 m0, s40\n"
      "s_waitcnt lgkmcnt(14)\n"
      "v_mfma_f32_32x32x16_f16 v[180:195], v[72:75], v[104:107], 0\n"
      "v_cvt_pk_f16_f32 v240, v146, v147\n"
      "v_pk_mul_f32 v[148:149], v[146:147], v[150:151] op_sel_hi:[1,0]\n"
      "v_pk_mul_f16 v241, v240, v153 op_sel_hi:[0,1] neg_hi:[0,1]\n"
      "v_pk_mul_f16 v242, v240, v154 op_sel_hi:[0,1] neg_hi:[0,1]\n"
      "v_pk_mul_f16 v243, v240, v155 op_sel_hi:[0,1] neg_hi:[0,1]\n"
      "ds_write_addtid_b32 v240 offset:52224\n"
      "v_pk_fma_f32 v[148:149], v[146:147], v[150:151], v[148:149] op_sel:[1,1,0] op_sel_hi:[0,1,1] neg_hi:[0,1,0]\n"
      "v_pk_fma_f16 v241, v240, v153, v241 op_sel:[1,1,0] op_sel_hi:[1,0,1]\n"
      "v_pk_fma_f16 v242, v240, v154, v242 op_sel:[1,1,0] op_sel_hi:[1,0,1]\n"
      "ds_write_addtid_b32 v241 offset:52496\n"
      "v_pk_fma_f16 v243, v240, v155, v243 op_sel:[1,1,0] op_sel_hi:[1,0,1]\n"
      "ds_write_addtid_b32 v242 offset:52768\n"
      "ds_write_addtid_b32 v243 offset:53040\n"
      "s_waitcnt lgkmcnt(15)\n"
      "v_mfma_f32_32x32x16_f16 v[180:195], v[76:79], v[108:111], v[180:195]\n"
      "v_cvt_pk_f16_f32 v244, v148, v149\n"
      "v_pk_mul_f32 v[146:147], v[148:149], v[150:151] op_sel_hi:[1,0]\n"
      "v_pk_mul_f16 v245, v244, v153 op_sel_hi:[0,1] neg_hi:[0,1]\n"
      "v_pk_mul_f16 v246, v244, v154 op_sel_hi:[0,1] neg_hi:[0,1]\n"
      "v_pk_mul_f16 v247, v244, v155 op_sel_hi:[0,1] neg_hi:[0,1]\n"
      "ds_write_addtid_b32 v244 offset:53312\n"
      "v_pk_fma_f32 v[146:147], v[148:149], v[150:151], v[146:147] op_sel:[1,1,0] op_sel_hi:[0,1,1] neg_hi:[0,1,0]\n"
      "v_pk_fma_f16 v245, v244, v153, v245 op_sel:[1,1,0] op_sel_hi:[1,0,1]\n"
      "v_pk_fma_f16 v246, v244, v154, v246 op_sel:[1,1,0] op_sel_hi:[1,0,1]\n"
      "ds_write_addtid_b32 v245 offset:53584\n"
      "v_pk_fma_f16 v247, v244, v155, v247 op_sel:[1,1,0] op_sel_hi:[1,0,1]\n"
      "ds_write_addtid_b32 v246 offset:53856\n"
      "ds_write_addtid_b32 v247 offset:54128\n"
      "s_waitcnt lgkmcnt(15)\n"
      "v_mfma_f32_32x32x16_f16 v[180:195], v[80:83], v[112:115], v[180:195]\n"
      "v_cvt_pk_f16_f32 v240, v146, v147\n"
      "v_pk_mul_f32 v[148:149], v[146:147], v[150:151] op_sel_hi:[1,0]\n"
      "v_pk_mul_f16 v241, v240, v153 op_sel_hi:[0,1] neg_hi:[0,1]\n"
      "v_pk_mul_f16 v242, v240, v154 op_sel_hi:[0,1] neg_hi:[0,1]\n"
      "v_pk_mul_f16 v243, v240, v155 op_sel_hi:[0,1] neg_hi:[0,1]\n"
      "ds_write_addtid_b32 v240 offset:54400\n"
      "v_pk_fma_f32 v[148:149], v[146:147], v[150:151], v[148:149] op_sel:[1,1,0] op_sel_hi:[0,1,1] neg_hi:[0,1,0]\n"
      "v_pk_fma_f16 v241, v240, v153, v241 op_sel:[1,1,0] op_sel_hi:[1,0,1]\n"
      "v_pk_fma_f16 v242, v240, v154, v242 op_sel:[1,1,0] op_sel_hi:[1,0,1]\n"
      "ds_write_addtid_b32 v241 offset:54672\n"
      "v_pk_fma_f16 v243, v240, v155, v243 op_sel:[1,1,0] op_sel_hi:[1,0,1]\n"
      "ds_write_addtid_b32 v242 offset:54944\n"
      "ds_write_addtid_b32 v243 offset:55216\n"
      "buffer_load_dwordx4 v[52:55], v41, s[20:23], s60 offen offset:2048 nt\n"
      "s_waitcnt lgkmcnt(15)\n"
      "v_mfma_f32_32x32x16_f16 v[180:195], v[84:87], v[116:119], v[180:195]\n"
      "v_cvt_pk_f16_f32 v244, v148, v149\n"
      "v_pk_mul_f32 v[146:147], v[148:149], v[150:151] op_sel_hi:[1,0]\n"
      "v_pk_mul_f16 v245, v244, v153 op_sel_hi:[0,1] neg_hi:[0,1]\n"
      "v_pk_mul_f16 v246, v244, v154 op_sel_hi:[0,1] neg_hi:[0,1]\n"
      "v_pk_mul_f16 v247, v244, v155 op_sel_hi:[0,1] neg_hi:[0,1]\n"
      "ds_write_addtid_b32 v244 offset:55488\n"
      "v_pk_fma_f32 v[146:147], v[148:149], v[150:151], v[146:147] op_sel:[1,1,0] op_sel_hi:[0,1,1] neg_hi:[0,1,0]\n"
      "v_pk_fma_f16 v245, v244, v153, v245 op_sel:[1,1,0] op_sel_hi:[1,0,1]\n"
      "v_pk_fma_f16 v246, v244, v154, v246 op_sel:[1,1,0] op_sel_hi:[1,0,1]\n"
      "ds_write_addtid_b32 v245 offset:55760\n"
      "v_pk_fma_f16 v247, v244, v155, v247 op_sel:[1,1,0] op_sel_hi:[1,0,1]\n"
      "ds_write_addtid_b32 v246 offset:56032\n"
      "ds_write_addtid_b32 v247 offset:56304\n"
      "s_waitcnt lgkmcnt(15)\n"
      "v_mfma_f32_32x32x16_f16 v[180:195], v[88:91], v[120:123], v[180:195]\n"
      "v_cvt_pk_f16_f32 v240, v146, v147\n"
      "v_pk_mul_f32 v[148:149], v[146:147], v[150:151] op_sel_hi:[1,0]\n"
      "v_pk_mul_f16 v241, v240, v153 op_sel_hi:[0,1] neg_hi:[0,1]\n"
      "v_pk_mul_f16 v242, v240, v154 op_sel_hi:[0,1] neg_hi:[0,1]\n"
      "v_pk_mul_f16 v243, v240, v155 op_sel_hi:[0,1] neg_hi:[0,1]\n"
      "ds_write_addtid_b32 v240 offset:56576\n"
      "v_pk_fma_f32 v[148:149], v[146:147], v[150:151], v[148:149] op_sel:[1,1,0] op_sel_hi:[0,1,1] neg_hi:[0,1,0]\n"
      "v_pk_fma_f16 v241, v240, v153, v241 op_sel:[1,1,0] op_sel_hi:[1,0,1]\n"
      "v_pk_fma_f16 v242, v240, v154, v242 op_sel:[1,1,0] op_sel_hi:[1,0,1]\n"
      "ds_write_addtid_b32 v241 offset:56848\n"
      "v_pk_fma_f16 v243, v240, v155, v243 op_sel:[1,1,0] op_sel_hi:[1,0,1]\n"
      "ds_write_addtid_b32 v242 offset:57120\n"
      "ds_write_addtid_b32 v243 offset:57392\n"
      "s_waitcnt lgkmcnt(15)\n"
      "v_mfma_f32_32x32x16_f16 v[180:195], v[92:95], v[124:127], v[180:195]\n"
      "v_cvt_pk_f16_f32 v244, v148, v149\n"
      "v_pk_mul_f32 v[146:147], v[148:149], v[150:151] op_sel_hi:[1,0]\n"
      "v_pk_mul_f16 v245, v244, v153 op_sel_hi:[0,1] neg_hi:[0,1]\n"
      "v_pk_mul_f16 v246, v244, v154 op_sel_hi:[0,1] neg_hi:[0,1]\n"
      "v_pk_mul_f16 v247, v244, v155 op_sel_hi:[0,1] neg_hi:[0,1]\n"
      "ds_write_addtid_b32 v244 offset:57664\n"
      "v_pk_fma_f32 v[146:147], v[148:149], v[150:151], v[146:147] op_sel:[1,1,0] op_sel_hi:[0,1,1] neg_hi:[0,1,0]\n"
      "v_pk_fma_f16 v245, v244, v153, v245 op_sel:[1,1,0] op_sel_hi:[1,0,1]\n"
      "v_pk_fma_f16 v246, v244, v154, v246 op_sel:[1,1,0] op_sel_hi:[1,0,1]\n"
      "ds_write_addtid_b32 v245 offset:57936\n"
      "v_pk_fma_f16 v247, v244, v155, v247 op_sel:[1,1,0] op_sel_hi:[1,0,1]\n"
      "ds_write_addtid_b32 v246 offset:58208\n"
      "ds_write_addtid_b32 v247 offset:58480\n"
      "s_waitcnt lgkmcnt(15)\n"
      "v_mfma_f32_32x32x16_f16 v[180:195], v[96:99], v[128:131], v[180:195]\n"
      "v_cvt_pk_f16_f32 v240, v146, v147\n"
      "v_pk_mul_f32 v[148:149], v[146:147], v[150:151] op_sel_hi:[1,0]\n"
      "v_pk_mul_f16 v241, v240, v153 op_sel_hi:[0,1] neg_hi:[0,1]\n"
      "v_pk_mul_f16 v242, v240, v154 op_sel_hi:[0,1] neg_hi:[0,1]\n"
      "v_pk_mul_f16 v243, v240, v155 op_sel_hi:[0,1] neg_hi:[0,1]\n"
      "ds_write_addtid_b32 v240 offset:58752\n"
      "v_pk_fma_f32 v[148:149], v[146:147], v[150:151], v[148:149] op_sel:[1,1,0] op_sel_hi:[0,1,1] neg_hi:[0,1,0]\n"
      "v_pk_fma_f16 v241, v240, v153, v241 op_sel:[1,1,0] op_sel_hi:[1,0,1]\n"
      "v_pk_fma_f16 v242, v240, v154, v242 op_sel:[1,1,0] op_sel_hi:[1,0,1]\n"
      "ds_write_addtid_b32 v241 offset:59024\n"
      "v_pk_fma_f16 v243, v240, v155, v243 op_sel:[1,1,0] op_sel_hi:[1,0,1]\n"
      "ds_write_addtid_b32 v242 offset:59296\n"
      "ds_write_addtid_b32 v243 offset:59568\n"
      "buffer_load_dwordx4 v[56:59], v41, s[20:23], s61 offen nt\n"
      "s_waitcnt lgkmcnt(15)\n"
      "v_mfma_f32_32x32x16_f16 v[180:195], v[100:103], v[132:135], v[180:195]\n"
      "v_cvt_pk_f16_f32 v244, v148, v149\n"
      "v_pk_mul_f16 v245, v244, v153 op_sel_hi:[0,1] neg_hi:[0,1]\n"
      "v_pk_mul_f16 v246, v244, v154 op_sel_hi:[0,1] neg_hi:[0,1]\n"
      "v_pk_mul_f16 v247, v244, v155 op_sel_hi:[0,1] neg_hi:[0,1]\n"
      "ds_write_addtid_b32 v244 offset:59840\n"
      "v_pk_fma_f16 v245, v244, v153, v245 op_sel:[1,1,0] op_sel_hi:[1,0,1]\n"
      "v_pk_fma_f16 v246, v244, v154, v246 op_sel:[1,1,0] op_sel_hi:[1,0,1]\n"
      "ds_write_addtid_b32 v245 offset:60112\n"
      "v_pk_fma_f16 v247, v244, v155, v247 op_sel:[1,1,0] op_sel_hi:[1,0,1]\n"
      "ds_write_addtid_b32 v246 offset:60384\n"
      "ds_write_addtid_b32 v247 offset:60656\n"
      "s_mov_b32 m0, s39\n"
      "s_setprio 1\n"
      "v_pk_mul_f16 v240, v157, v156 op_sel_hi:[0,1]\n"
      "v_pk_mul_f16 v241, v158, v156 op_sel_hi:[0,1]\n"
      "v_pk_mul_f16 v242, v159, v156 op_sel_hi:[0,1]\n"
      "ds_write_addtid_b32 v156 offset:272\n"
      "v_pk_fma_f16 v240, v157, v156, v240 op_sel:[1,1,0] op_sel_hi:[1,0,1] neg_lo:[0,1,0]\n"
      "ds_write_addtid_b32 v157 offset:544\n"
      "v_pk_fma_f16 v241, v158, v156, v241 op_sel:[1,1,0] op_sel_hi:[1,0,1] neg_lo:[0,1,0]\n"
      "ds_write_addtid_b32 v240 offset:816\n"
      "v_pk_fma_f16 v242, v159, v156, v242 op_sel:[1,1,0] op_sel_hi:[1,0,1] neg_lo:[0,1,0]\n"
      "ds_write_addtid_b32 v241 offset:1360\n"
      "ds_write_addtid_b32 v242 offset:1904\n"
      "v_pk_mul_f16 v243, v160, v157 op_sel_hi:[0,1]\n"
      "v_pk_mul_f16 v244, v160, v158 op_sel_hi:[0,1]\n"
      "v_pk_mul_f16 v245, v160, v159 op_sel_hi:[0,1]\n"
      "ds_write_addtid_b32 v158 offset:1088\n"
      "v_pk_fma_f16 v243, v160, v157, v243 op_sel:[1,1,0] op_sel_hi:[1,0,1] neg_lo:[0,1,0]\n"
      "ds_write_addtid_b32 v159 offset:1632\n"
      "v_pk_fma_f16 v244, v160, v158, v244 op_sel:[1,1,0] op_sel_hi:[1,0,1] neg_lo:[0,1,0]\n"
      "ds_write_addtid_b32 v243 offset:2992\n"
      "v_pk_fma_f16 v245, v160, v159, v245 op_sel:[1,1,0] op_sel_hi:[1,0,1] neg_lo:[0,1,0]\n"
      "ds_write_addtid_b32 v244 offset:3264\n"
      "ds_write_addtid_b32 v245 offset:4080\n"
      "v_pk_mul_f16 v246, v161, v158 op_sel_hi:[0,1]\n"
      "v_pk_mul_f16 v247, v162, v158 op_sel_hi:[0,1]\n"
      "v_pk_mul_f16 v248, v163, v163 op_sel_hi:[0,1]\n"
      "ds_write_addtid_b32 v160 offset:2176\n"
      "v_pk_fma_f16 v246, v161, v158, v246 op_sel:[1,1,0] op_sel_hi:[1,0,1] neg_lo:[0,1,0]\n"
      "ds_write_addtid_b32 v161 offset:5440\n"
      "v_pk_fma_f16 v247, v162, v158, v247 op_sel:[1,1,0] op_sel_hi:[1,0,1] neg_lo:[0,1,0]\n"
      "ds_write_addtid_b32 v246 offset:5984\n"
      "v_pk_fma_f16 v248, v163, v163, v248 op_sel:[1,1,0] op_sel_hi:[1,0,1] neg_lo:[0,1,0]\n"
      "ds_write_addtid_b32 v247 offset:6256\n"
      "ds_write_addtid_b32 v248 offset:2448\n"
      "v_pk_mul_f16 v249, v163, v160 op_sel_hi:[0,1]\n"
      "v_pk_mul_f16 v250, v163, v161 op_sel_hi:[0,1]\n"
      "v_pk_mul_f16 v251, v163, v162 op_sel_hi:[0,1]\n"
      "ds_write_addtid_b32 v162 offset:5712\n"
      "v_pk_fma_f16 v249, v163, v160, v249 op_sel:[1,1,0] op_sel_hi:[1,0,1] neg_lo:[0,1,0]\n"
      "ds_write_addtid_b32 v163 offset:4352\n"
      "v_pk_fma_f16 v250, v163, v161, v250 op_sel:[1,1,0] op_sel_hi:[1,0,1] neg_lo:[0,1,0]\n"
      "ds_write_addtid_b32 v249 offset:4624\n"
      "v_pk_fma_f16 v251, v163, v162, v251 op_sel:[1,1,0] op_sel_hi:[1,0,1] neg_lo:[0,1,0]\n"
      "ds_write_addtid_b32 v250 offset:4896\n"
      "ds_write_addtid_b32 v251 offset:5168\n"
      "buffer_load_dwordx4 v[60:63], v41, s[20:23], s61 offen offset:2048 nt\n"
      "buffer_load_dwordx4 v[64:67], v42, s[16:19], s62 offen sc1\n"
      "buffer_load_dwordx4 v[68:71], v42, s[16:19], s63 offen sc1\n"
      "v_mov_b32 v140, 0\n"
      "v_mov_b32 v141, 0\n"
      "v_mov_b32 v142, 0\n"
      "v_mov_b32 v143, 0\n"
      "ds_read_b128 v[72:75], v21 offset:0\n"
      "ds_read_b128 v[104:107], v20 offset:0\n"
      "ds_read_b128 v[76:79], v21 offset:32\n"
      "ds_read_b128 v[108:111], v20 offset:32\n"
      "ds_read_b128 v[80:83], v21 offset:64\n"
      "ds_read_b128 v[112:115], v20 offset:64\n"
      "ds_read_b128 v[84:87], v21 offset:96\n"
      "ds_read_b128 v[116:119], v20 offset:96\n"
      "ds_read_b128 v[88:91], v21 offset:128\n"
      "ds_read_b128 v[120:123], v20 offset:128\n"
      "ds_read_b128 v[92:95], v21 offset:160\n"
      "ds_read_b128 v[124:127], v20 offset:160\n"
      "ds_read_b128 v[96:99], v21 offset:192\n"
      "ds_read_b128 v[128:131], v20 offset:192\n"
      "ds_read_b128 v[100:103], v21 offset:224\n"
      "ds_read_b128 v[132:135], v20 offset:224\n"
      "ds_write_b128 v27, v[140:143] \n"
      "ds_write_b32 v28, v140 \n"
      "s_waitcnt lgkmcnt(15)\n"
      "v_mfma_f32_32x32x16_f16 v[180:195], v[72:75], v[104:107], v[180:195]\n"
      "s_waitcnt lgkmcnt(14)\n"
      "v_mfma_f32_32x32x16_f16 v[180:195], v[76:79], v[108:111], v[180:195]\n"
      "s_waitcnt lgkmcnt(12)\n"
      "v_mfma_f32_32x32x16_f16 v[180:195], v[80:83], v[112:115], v[180:195]\n"
      "s_waitcnt lgkmcnt(10)\n"
      "v_mfma_f32_32x32x16_f16 v[180:195], v[84:87], v[116:119], v[180:195]\n"
      "s_waitcnt lgkmcnt(8)\n"
      "v_mfma_f32_32x32x16_f16 v[180:195], v[88:91], v[120:123], v[180:195]\n"
      "s_waitcnt lgkmcnt(6)\n"
      "v_mfma_f32_32x32x16_f16 v[180:195], v[92:95], v[124:127], v[180:195]\n"
      "s_waitcnt lgkmcnt(4)\n"
      "v_mfma_f32_32x32x16_f16 v[180:195], v[96:99], v[128:131], v[180:195]\n"
      "s_waitcnt lgkmcnt(2)\n"
      "v_mfma_f32_32x32x16_f16 v[180:195], v[100:103], v[132:135], v[180:195]\n"
      "s_nop 11\n"
      "v_add_f32 v180, v180, v31\n"
      "v_cvt_pk_f16_f32 v222, v187, v186\n"
      "v_cvt_pk_f16_f32 v223, v185, v184\n"
      "v_cvt_pk_f16_f32 v225, v186, v185\n"
      "v_cvt_pk_f16_f32 v226, v188, v189\n"
      "ds_write_b64 v22, v[222:223] offset:2110\n"
      "v_cvt_pk_f16_f32 v228, v192, v192\n"
      "ds_write_b16 v22, v222 \n"
      "v_cvt_pk_f16_f32 v220, v183, v182\n"
      "ds_write_b32 v22, v225 offset:2\n"
      "v_cvt_pk_f16_f32 v221, v181, v180\n"
      "ds_write_b16_d16_hi v22, v223 offset:6\n"
      "v_cvt_pk_f16_f32 v224, v182, v181\n"
      "v_readlane_b32 s64, v226, 0\n"
      "v_readlane_b32 s65, v226, 32\n"
      "v_readlane_b32 s66, v228, 0\n"
      "ds_write_b64 v22, v[220:221] offset:510\n"
      "v_cvt_pk_f16_f32 v227, v190, v191\n"
      "ds_write_b16 v22, v220 offset:2620\n"
      "v_mov_b32 v229, s65\n"
      "ds_write_b32 v22, v224 offset:2622\n"
      "v_writelane_b32 v229, s64, 8\n"
      "v_writelane_b32 v229, s64, 9\n"
      "ds_write_b16_d16_hi v22, v221 offset:2626\n"
      "v_writelane_b32 v229, s66, 10\n"
      "v_writelane_b32 v229, s66, 11\n"
      "s_pack_ll_b32_b16 s64, s64, s64\n"
      "ds_write_b16 v26, v229 \n"
      "v_add_u32 v230, 0x240, v29\n"
      "s_setprio 0\n"
      "v_and_b32 v44, s64, v44\n"
      "v_and_b32 v45, s64, v45\n"
      "v_and_b32 v46, s64, v46\n"
      "v_and_b32 v47, s64, v47\n"
      "ds_write_b16 v23, v226 \n"
      "ds_write_b16 v23, v226 offset:2110\n"
      "ds_write_b16_d16_hi v24, v226 \n"
      "ds_write_b16_d16_hi v24, v226 offset:2110\n"
      "ds_write_b16 v25, v227 offset:32\n"
      "ds_write_b16 v25, v227 offset:2142\n"
      "ds_write_b16_d16_hi v25, v227 \n"
      "ds_write_b16_d16_hi v25, v227 offset:2110\n"
      "ds_read2_b32 v[108:109], v230 offset0:0 offset1:1\n"
      "ds_read2_b32 v[110:111], v230 offset0:2 offset1:3\n"
      "ds_read2_b32 v[76:77], v29 offset0:16 offset1:17\n"
      "ds_read2_b32 v[78:79], v29 offset0:18 offset1:19\n"
      "ds_read2_b32 v[112:113], v230 offset0:16 offset1:17\n"
      "ds_read2_b32 v[114:115], v230 offset0:18 offset1:19\n"
      "ds_read2_b32 v[80:81], v29 offset0:32 offset1:33\n"
      "ds_read2_b32 v[82:83], v29 offset0:34 offset1:35\n"
      "ds_read2_b32 v[116:117], v230 offset0:32 offset1:33\n"
      "ds_read2_b32 v[118:119], v230 offset0:34 offset1:35\n"
      "ds_read2_b32 v[84:85], v29 offset0:48 offset1:49\n"
      "ds_read2_b32 v[86:87], v29 offset0:50 offset1:51\n"
      "s_movk_i32 s59, 0x7fff\n"
      ".Lcsg%=:\n"
      "s_waitcnt vmcnt(1)\n"
      "v_cmp_eq_u32 s[64:65], s15, v65\n"
      "v_cmp_eq_u32 s[66:67], s15, v67\n"
      "s_waitcnt vmcnt(0)\n"
      "v_cmp_eq_u32 s[68:69], s15, v69\n"
      "v_cmp_eq_u32 vcc, s15, v71\n"
      "s_and_b64 s[64:65], s[64:65], s[66:67]\n"
      "s_and_b64 s[68:69], s[68:69], vcc\n"
      "s_and_b64 s[64:65], s[64:65], s[68:69]\n"
      "s_cmp_eq_u64 s[64:65], exec\n"
      "s_cbranch_scc0 .Lcsgs%=\n"
      ".Lcsgo%=:\n"
      "v_and_b32 v196, 0xffff0000, v64\n"
      "v_lshlrev_b32 v200, 16, v64\n"
      "v_rcp_f32 v196, v196\n"
      "v_and_b32 v197, 0xffff0000, v66\n"
      "v_lshlrev_b32 v201, 16, v66\n"
      "v_rcp_f32 v197, v197\n"
      "v_and_b32 v198, 0xffff0000, v68\n"
      "v_lshlrev_b32 v202, 16, v68\n"
      "ds_read2_b32 v[120:121], v230 offset0:48 offset1:49\n"
      "ds_read2_b32 v[122:123], v230 offset0:50 offset1:51\n"
      "v_rcp_f32 v198, v198\n"
      "v_and_b32 v199, 0xffff0000, v70\n"
      "v_lshlrev_b32 v203, 16, v70\n"
      "v_rcp_f32 v199, v199\n"
      "v_mul_f32 v200, -v200, v196\n"
      "v_mul_f32 v201, -v201, v197\n"
      "v_mul_f32 v202, -v202, v198\n"
      "v_mul_f32 v203, -v203, v199\n"
      "ds_read2_b32 v[88:89], v29 offset0:64 offset1:65\n"
      "ds_read2_b32 v[90:91], v29 offset0:66 offset1:67\n"
      "v_lshlrev_b32 v204, 16, v48\n"
      "v_lshlrev_b32 v205, 16, v52\n"
      "v_lshlrev_b32 v206, 16, v56\n"
      "v_lshlrev_b32 v207, 16, v60\n"
      "v_pk_fma_f32 v[204:205], v[204:205], v[196:197], v[200:201]\n"
      "v_pk_fma_f32 v[206:207], v[206:207], v[198:199], v[202:203]\n"
      "v_cvt_pk_f16_f32 v204, v204, v205\n"
      "v_cvt_pk_f16_f32 v206, v206, v207\n"
      "ds_read2_b32 v[124:125], v230 offset0:64 offset1:65\n"
      "ds_read2_b32 v[126:127], v230 offset0:66 offset1:67\n"
      "ds_write2st64_b32 v32, v204, v206 offset0:0 offset1:8\n"
      "v_and_b32 v208, 0xffff0000, v48\n"
      "v_and_b32 v209, 0xffff0000, v52\n"
      "v_and_b32 v210, 0xffff0000, v56\n"
      "v_and_b32 v211, 0xffff0000, v60\n"
      "v_pk_fma_f32 v[208:209], v[208:209], v[196:197], v[200:201]\n"
      "v_pk_fma_f32 v[210:211], v[210:211], v[198:199], v[202:203]\n"
      "v_cvt_pk_f16_f32 v208, v208, v209\n"
      "ds_read2_b32 v[92:93], v29 offset0:80 offset1:81\n"
      "ds_read2_b32 v[94:95], v29 offset0:82 offset1:83\n"
      "v_cvt_pk_f16_f32 v210, v210, v211\n"
      "ds_write2st64_b32 v32, v208, v210 offset0:19 offset1:27\n"
      "v_lshlrev_b32 v212, 16, v49\n"
      "v_lshlrev_b32 v213, 16, v53\n"
      "v_lshlrev_b32 v214, 16, v57\n"
      "v_lshlrev_b32 v215, 16, v61\n"
      "v_pk_fma_f32 v[212:213], v[212:213], v[196:197], v[200:201]\n"
      "v_pk_fma_f32 v[214:215], v[214:215], v[198:199], v[202:203]\n"
      "ds_read2_b32 v[128:129], v230 offset0:80 offset1:81\n"
      "ds_read2_b32 v[130:131], v230 offset0:82 offset1:83\n"
      "v_cvt_pk_f16_f32 v212, v212, v213\n"
      "v_cvt_pk_f16_f32 v214, v214, v215\n"
      "ds_write2st64_b32 v32, v212, v214 offset0:38 offset1:46\n"
      "v_and_b32 v216, 0xffff0000, v49\n"
      "v_and_b32 v217, 0xffff0000, v53\n"
      "v_and_b32 v218, 0xffff0000, v57\n"
      "v_and_b32 v219, 0xffff0000, v61\n"
      "v_pk_fma_f32 v[216:217], v[216:217], v[196:197], v[200:201]\n"
      "ds_read2_b32 v[96:97], v29 offset0:96 offset1:97\n"
      "ds_read2_b32 v[98:99], v29 offset0:98 offset1:99\n"
      "v_pk_fma_f32 v[218:219], v[218:219], v[198:199], v[202:203]\n"
      "v_cvt_pk_f16_f32 v216, v216, v217\n"
      "v_cvt_pk_f16_f32 v218, v218, v219\n"
      "ds_write2st64_b32 v32, v216, v218 offset0:57 offset1:65\n"
      "v_lshlrev_b32 v204, 16, v50\n"
      "v_lshlrev_b32 v205, 16, v54\n"
      "v_lshlrev_b32 v206, 16, v58\n"
      "v_lshlrev_b32 v207, 16, v62\n"
      "ds_read2_b32 v[132:133], v230 offset0:96 offset1:97\n"
      "ds_read2_b32 v[134:135], v230 offset0:98 offset1:99\n"
      "v_pk_fma_f32 v[204:205], v[204:205], v[196:197], v[200:201]\n"
      "v_pk_fma_f32 v[206:207], v[206:207], v[198:199], v[202:203]\n"
      "v_cvt_pk_f16_f32 v204, v204, v205\n"
      "v_cvt_pk_f16_f32 v206, v206, v207\n"
      "ds_write2st64_b32 v32, v204, v206 offset0:76 offset1:84\n"
      "v_and_b32 v208, 0xffff0000, v50\n"
      "v_and_b32 v209, 0xffff0000, v54\n"
      "v_and_b32 v210, 0xffff0000, v58\n"
      "ds_read2_b32 v[100:101], v29 offset0:112 offset1:113\n"
      "ds_read2_b32 v[102:103], v29 offset0:114 offset1:115\n"
      "v_and_b32 v211, 0xffff0000, v62\n"
      "v_pk_fma_f32 v[208:209], v[208:209], v[196:197], v[200:201]\n"
      "v_pk_fma_f32 v[210:211], v[210:211], v[198:199], v[202:203]\n"
      "v_cvt_pk_f16_f32 v208, v208, v209\n"
      "v_cvt_pk_f16_f32 v210, v210, v211\n"
      "ds_write2st64_b32 v32, v208, v210 offset0:95 offset1:103\n"
      "v_lshlrev_b32 v212, 16, v51\n"
      "v_lshlrev_b32 v213, 16, v55\n"
      "ds_read2_b32 v[136:137], v230 offset0:112 offset1:113\n"
      "ds_read2_b32 v[138:139], v230 offset0:114 offset1:115\n"
      "v_lshlrev_b32 v214, 16, v59\n"
      "v_lshlrev_b32 v215, 16, v63\n"
      "v_pk_fma_f32 v[212:213], v[212:213], v[196:197], v[200:201]\n"
      "v_pk_fma_f32 v[214:215], v[214:215], v[198:199], v[202:203]\n"
      "v_cvt_pk_f16_f32 v212, v212, v213\n"
      "v_cvt_pk_f16_f32 v214, v214, v215\n"
      "ds_write2st64_b32 v32, v212, v214 offset0:114 offset1:122\n"
      "v_and_b32 v216, 0xffff0000, v51\n"
      "ds_read2_b32 v[104:105], v29 offset0:128 offset1:129\n"
      "ds_read2_b32 v[106:107], v29 offset0:130 offset1:131\n"
      "v_and_b32 v217, 0xffff0000, v55\n"
      "v_and_b32 v218, 0xffff0000, v59\n"
      "v_and_b32 v219, 0xffff0000, v63\n"
      "v_pk_fma_f32 v[216:217], v[216:217], v[196:197], v[200:201]\n"
      "v_pk_fma_f32 v[218:219], v[218:219], v[198:199], v[202:203]\n"
      "v_cvt_pk_f16_f32 v216, v216, v217\n"
      "v_cvt_pk_f16_f32 v218, v218, v219\n"
      "ds_write2st64_b32 v32, v216, v218 offset0:133 offset1:141\n"
      "ds_read2_b32 v[72:73], v29 offset0:0 offset1:1\n"
      "ds_read2_b32 v[74:75], v29 offset0:2 offset1:3\n"
      "s_mov_b32 s54, s41\n"
      "s_add_u32 s55, s41, 0x100000\n"
      "s_add_u32 s56, s41, 0x200000\n"
      "s_add_u32 s57, s41, 0x300000\n"
      "s_mov_b32 s58, 0x40000000\n"
      "s_lshr_b32 s13, s33, 1\n"
      "v_lshrrev_b32 v14, 9, v8\n"
      "v_add_u32 v14, 0x27600, v14\n"
      "v_mov_b32 v42, v8\n"
      "s_waitcnt lgkmcnt(2)\n"
      "s_barrier\n"
      "s_setprio 3\n"
      "ds_read_b128 v[216:219], v33 offset:64\n"
      "ds_read_b128 v[220:223], v33 offset:128\n"
      "ds_read_b128 v[224:227], v33 offset:192\n"
      "ds_read_b128 v[212:215], v33 offset:256\n"
      "v_permlane32_swap_b32 v48, v52\n"
      "v_permlane32_swap_b32 v49, v53\n"
      "v_permlane32_swap_b32 v50, v54\n"
      "v_permlane32_swap_b32 v51, v55\n"
      "v_permlane32_swap_b32 v56, v60\n"
      "v_permlane32_swap_b32 v57, v61\n"
      "v_permlane32_swap_b32 v58, v62\n"
      "v_permlane32_swap_b32 v59, v63\n"
      "s_cmp_lg_u32 s33, 0\n"
      "s_cbranch_scc1 .Lca%=\n"
      "v_mov_b32 v16, s15\n"
      "v_mov_b32 v17, s15\n"
      "buffer_store_dwordx2 v[16:17], off, s[16:19], s42 sc1\n"
      ".Lca%=:\n"
      "s_waitcnt lgkmcnt(3)\n"
      "v_mfma_f32_16x16x32_f16 v[180:183], v[108:111], v[216:219], 0\n"
      "ds_read_b128 v[236:239], v34 offset:0\n"
      "ds_read_b128 v[240:243], v34 offset:16\n"
      "v_lshlrev_b32 v29, 2, v43\n"
      "v_add_u32 v29, 0x27600, v29\n"
      "ds_read_b32 v255, v29 \n"
      "v_mfma_f32_16x16x32_f16 v[184:187], v[76:79], v[216:219], 0\n"
      "ds_read2_b32 v[140:141], v30 offset0:16 offset1:17\n"
      "ds_read_b128 v[216:219], v33 offset:320\n"
      "s_waitcnt lgkmcnt(7)\n"
      "v_mfma_f32_16x16x32_f16 v[180:183], v[112:115], v[220:223], v[180:183]\n"
      "v_mfma_f32_16x16x32_f16 v[184:187], v[80:83], v[220:223], v[184:187]\n"
      "ds_read2_b32 v[142:143], v30 offset0:64 offset1:65\n"
      "ds_read_b128 v[220:223], v33 offset:384\n"
      "s_waitcnt lgkmcnt(8)\n"
      "v_mfma_f32_16x16x32_f16 v[180:183], v[116:119], v[224:227], v[180:183]\n"
      "s_waitcnt lgkmcnt(6)\n"
      "v_alignbit_b32 v245, v238, v237, 16\n"
      "v_mfma_f32_16x16x32_f16 v[184:187], v[84:87], v[224:227], v[184:187]\n"
      "ds_read2_b32 v[144:145], v30 offset0:8 offset1:9\n"
      "ds_read_b128 v[224:227], v33 offset:448\n"
      "v_alignbit_b32 v246, v239, v238, 16\n"
      "v_mfma_f32_16x16x32_f16 v[180:183], v[120:123], v[212:215], v[180:183]\n"
      "s_waitcnt lgkmcnt(7)\n"
      "v_alignbit_b32 v247, v240, v239, 16\n"
      "s_waitcnt lgkmcnt(6)\n"
      "v_readlane_b32 s72, v255, 0\n"
      "v_mfma_f32_16x16x32_f16 v[184:187], v[88:91], v[212:215], v[184:187]\n"
      "ds_read2_b32 v[146:147], v30 offset0:56 offset1:57\n"
      "ds_read_b128 v[212:215], v33 offset:512\n"
      "v_alignbit_b32 v248, v241, v240, 16\n"
      "s_waitcnt lgkmcnt(6)\n"
      "v_mfma_f32_16x16x32_f16 v[180:183], v[124:127], v[216:219], v[180:183]\n"
      "v_alignbit_b32 v249, v242, v241, 16\n"
      "v_readlane_b32 s73, v255, 1\n"
      "v_mfma_f32_16x16x32_f16 v[184:187], v[92:95], v[216:219], v[184:187]\n"
      "ds_read2_b32 v[148:149], v30 offset0:0 offset1:1\n"
      "v_pk_add_f16 v16, v245, v246\n"
      "s_waitcnt lgkmcnt(5)\n"
      "v_mfma_f32_16x16x32_f16 v[180:183], v[128:131], v[220:223], v[180:183]\n"
      "v_pk_add_f16 v17, v246, v247\n"
      "v_readlane_b32 s74, v255, 2\n"
      "s_setprio 2\n"
      "v_mfma_f32_16x16x32_f16 v[184:187], v[96:99], v[220:223], v[184:187]\n"
      "ds_read2_b32 v[150:151], v30 offset0:48 offset1:49\n"
      "ds_read_b128 v[216:219], v33 offset:576\n"
      "v_pk_add_f16 v18, v247, v248\n"
      "s_waitcnt lgkmcnt(5)\n"
      "v_mfma_f32_16x16x32_f16 v[180:183], v[132:135], v[224:227], v[180:183]\n"
      "v_pk_add_f16 v19, v248, v249\n"
      "v_readlane_b32 s75, v255, 3\n"
      "v_mfma_f32_16x16x32_f16 v[184:187], v[100:103], v[224:227], v[184:187]\n"
      "ds_read2_b32 v[152:153], v30 offset0:40 offset1:41\n"
      "ds_read_b128 v[220:223], v33 offset:640\n"
      "v_pk_add_f16 v20, v16, v238\n"
      "s_waitcnt lgkmcnt(5)\n"
      "v_mfma_f32_16x16x32_f16 v[180:183], v[136:139], v[212:215], v[180:183]\n"
      "v_pk_add_f16 v21, v17, v239\n"
      "v_readlane_b32 s76, v255, 4\n"
      "s_cmp_lg_u32 s33, 0\n"
      "s_cbranch_scc1 .Lcg%=\n"
      "buffer_load_dwordx2 v[252:253], v9, s[16:19], s43 offen sc1\n"
      ".Lcg%=:\n"
      "v_mfma_f32_16x16x32_f16 v[184:187], v[104:107], v[212:215], v[184:187]\n"
      "ds_read2_b32 v[154:155], v30 offset0:88 offset1:89\n"
      "ds_read_b128 v[224:227], v33 offset:704\n"
      "v_pk_add_f16 v22, v18, v240\n"
      "v_mfma_f32_16x16x32_f16 v[188:191], v[72:75], v[212:215], 0\n"
      "v_pk_add_f16 v23, v19, v241\n"
      "v_readlane_b32 s77, v255, 5\n"
      "v_mfma_f32_16x16x32_f16 v[196:199], v[44:47], v[212:215], 0\n"
      "ds_read2_b32 v[156:157], v30 offset0:32 offset1:33\n"
      "ds_read_b128 v[212:215], v33 offset:768\n"
      "ds_write_b128 v37, v[20:23] offset:0\n"
      "v_pk_add_f16 v16, v237, v239\n"
      "s_waitcnt lgkmcnt(7)\n"
      "v_mfma_f32_16x16x32_f16 v[184:187], v[108:111], v[216:219], v[184:187]\n"
      "v_pk_add_f16 v17, v238, v240\n"
      "v_readlane_b32 s78, v255, 6\n"
      "v_cvt_pk_bf16_f32 v24, v180, v182\n"
      "v_cvt_pk_bf16_f32 v25, v181, v183\n"
      "ds_write2st64_b32 v39, v24, v25 offset0:0 offset1:8\n"
      "v_mfma_f32_16x16x32_f16 v[188:191], v[76:79], v[216:219], v[188:191]\n"
      "ds_read2_b32 v[158:159], v30 offset0:80 offset1:81\n"
      "ds_read_b128 v[216:219], v33 offset:832\n"
      "v_pk_add_f16 v18, v239, v241\n"
      "s_waitcnt lgkmcnt(8)\n"
      "v_mfma_f32_16x16x32_f16 v[184:187], v[112:115], v[220:223], v[184:187]\n"
      "v_pk_add_f16 v19, v240, v242\n"
      "v_readlane_b32 s79, v255, 7\n"
      "s_setprio 1\n"
      "v_mfma_f32_16x16x32_f16 v[188:191], v[80:83], v[220:223], v[188:191]\n"
      "ds_read2_b32 v[160:161], v30 offset0:24 offset1:25\n"
      "ds_read_b128 v[220:223], v33 offset:896\n"
      "v_pk_add_f16 v20, v20, v16\n"
      "s_waitcnt lgkmcnt(8)\n"
      "v_mfma_f32_16x16x32_f16 v[184:187], v[116:119], v[224:227], v[184:187]\n"
      "v_pk_add_f16 v21, v21, v17\n"
      "v_mfma_f32_16x16x32_f16 v[188:191], v[84:87], v[224:227], v[188:191]\n"
      "ds_read2_b32 v[162:163], v30 offset0:72 offset1:73\n"
      "ds_read_b128 v[224:227], v33 offset:960\n"
      "v_pk_add_f16 v22, v22, v18\n"
      "s_waitcnt lgkmcnt(8)\n"
      "v_mfma_f32_16x16x32_f16 v[184:187], v[120:123], v[212:215], v[184:187]\n"
      "v_pk_add_f16 v23, v23, v19\n"
      "v_mfma_f32_16x16x32_f16 v[188:191], v[88:91], v[212:215], v[188:191]\n"
      "ds_read2_b32 v[164:165], v30 offset0:128 offset1:129\n"
      "ds_read_b128 v[212:215], v33 offset:1024\n"
      "ds_write_b128 v37, v[20:23] offset:3104\n"
      "v_alignbit_b32 v244, v237, v236, 16\n"
      "s_waitcnt lgkmcnt(7)\n"
      "v_mfma_f32_16x16x32_f16 v[184:187], v[124:127], v[216:219], v[184:187]\n"
      "v_alignbit_b32 v250, v243, v242, 16\n"
      "v_mfma_f32_16x16x32_f16 v[188:191], v[92:95], v[216:219], v[188:191]\n"
      "v_pk_add_f16 v16, v244, v247\n"
      "s_waitcnt lgkmcnt(5)\n"
      "v_mfma_f32_16x16x32_f16 v[184:187], v[128:131], v[220:223], v[184:187]\n"
      "v_pk_add_f16 v17, v245, v248\n"
      "s_setprio 0\n"
      "v_mfma_f32_16x16x32_f16 v[188:191], v[96:99], v[220:223], v[188:191]\n"
      "ds_read2_b32 v[166:167], v30 offset0:168 offset1:169\n"
      "ds_read_b64_tr_b16 v[228:229], v35 offset:32\n"
      "ds_read_b64_tr_b16 v[230:231], v35 offset:16\n"
      "v_pk_add_f16 v18, v246, v249\n"
      "s_waitcnt lgkmcnt(6)\n"
      "v_mfma_f32_16x16x32_f16 v[184:187], v[132:135], v[224:227], v[184:187]\n"
      "v_pk_add_f16 v19, v247, v250\n"
      "v_mfma_f32_16x16x32_f16 v[188:191], v[100:103], v[224:227], v[188:191]\n"
      "v_pk_add_f16 v20, v20, v16\n"
      "s_waitcnt lgkmcnt(4)\n"
      "v_mfma_f32_16x16x32_f16 v[184:187], v[136:139], v[212:215], v[184:187]\n"
      "ds_read_b128 v[216:219], v33 offset:1088\n"
      "v_pk_add_f16 v21, v21, v17\n"
      "v_mfma_f32_16x16x32_f16 v[188:191], v[104:107], v[212:215], v[188:191]\n"
      "ds_read2_b32 v[168:169], v30 offset0:120 offset1:121\n"
      "v_pk_add_f16 v22, v22, v18\n"
      "v_mfma_f32_16x16x32_f16 v[192:195], v[72:75], v[212:215], 0\n"
      "ds_read_b128 v[220:223], v33 offset:1152\n"
      "v_pk_add_f16 v23, v23, v19\n"
      "v_mfma_f32_16x16x32_f16 v[200:203], v[44:47], v[212:215], 0\n"
      "ds_write_b128 v38, v[20:23] offset:0\n"
      "v_pk_add_f16 v16, v236, v240\n"
      "s_waitcnt lgkmcnt(4)\n"
      "v_mfma_f32_16x16x32_f16 v[188:191], v[228:231], v[140:143], v[188:191]\n"
      "ds_read_b128 v[224:227], v33 offset:1216\n"
      "v_pk_add_f16 v17, v237, v241\n"
      "v_cvt_pk_bf16_f32 v24, v184, v186\n"
      "v_cvt_pk_bf16_f32 v25, v185, v187\n"
      "ds_write2st64_b32 v39, v24, v25 offset0:1 offset1:9\n"
      "v_mfma_f32_16x16x32_f16 v[192:195], v[228:231], v[144:147], v[192:195]\n"
      "ds_read2_b32 v[170:171], v30 offset0:160 offset1:161\n"
      "v_pk_add_f16 v18, v238, v242\n"
      "v_mfma_f32_16x16x32_f16 v[196:199], v[228:231], v[148:151], v[196:199]\n"
      "ds_read_b64_tr_b16 v[232:233], v35 offset:3128\n"
      "ds_read_b64_tr_b16 v[234:235], v35 offset:3112\n"
      "v_pk_add_f16 v19, v239, v243\n"
      "s_waitcnt lgkmcnt(8)\n"
      "v_mfma_f32_16x16x32_f16 v[188:191], v[108:111], v[216:219], v[188:191]\n"
      "v_pk_add_f16 v20, v20, v16\n"
      "ds_read_b128 v[236:239], v34 offset:1024\n"
      "ds_read_b128 v[240:243], v34 offset:1040\n"
      "s_cmp_eq_u32 s33, 0\n"
      "s_cbranch_scc1 .Lch%=\n"
      ".Lcho%=:\n"
      "s_waitcnt lgkmcnt(5)\n"
      "s_barrier\n"
      "s_setprio 3\n"
      "v_mfma_f32_16x16x32_f16 v[192:195], v[76:79], v[216:219], v[192:195]\n"
      "ds_read_b64_tr_b16 v[64:65], v40 offset:0\n"
      "ds_read_b64_tr_b16 v[66:67], v40 offset:35072\n"
      "v_lshlrev_b32 v68, 16, v48\n"
      "v_and_b32 v69, 0xffff0000, v48\n"
      "v_mfma_f32_16x16x32_f16 v[188:191], v[112:115], v[220:223], v[188:191]\n"
      "ds_read2_b32 v[172:173], v30 offset0:112 offset1:113\n"
      "ds_read_b128 v[212:215], v33 offset:1280\n"
      "v_lshlrev_b32 v70, 16, v49\n"
      "v_and_b32 v71, 0xffff0000, v49\n"
      "ds_read_b32 v12, v14 offset:256\n"
      "v_mfma_f32_16x16x32_f16 v[192:195], v[80:83], v[220:223], v[192:195]\n"
      "v_lshlrev_b32 v26, 16, v50\n"
      "v_and_b32 v27, 0xffff0000, v50\n"
      "v_mfma_f32_16x16x32_f16 v[188:191], v[116:119], v[224:227], v[188:191]\n"
      "ds_read_b128 v[216:219], v33 offset:1344\n"
      "v_lshlrev_b32 v28, 16, v51\n"
      "v_and_b32 v29, 0xffff0000, v51\n"
      "v_mfma_f32_16x16x32_f16 v[192:195], v[84:87], v[224:227], v[192:195]\n"
      "s_waitcnt lgkmcnt(5)\n"
      "v_lshlrev_b32 v48, 16, v64\n"
      "v_and_b32 v64, 0xffff0000, v64\n"
      "v_mfma_f32_16x16x32_f16 v[188:191], v[232:235], v[152:155], v[188:191]\n"
      "ds_read2_b32 v[174:175], v30 offset0:152 offset1:153\n"
      "ds_read_b128 v[220:223], v33 offset:1408\n"
      "v_fmac_f32 v68, s72, v48\n"
      "v_fmac_f32 v69, s73, v64\n"
      "v_mfma_f32_16x16x32_f16 v[192:195], v[232:235], v[156:159], v[192:195]\n"
      "v_lshlrev_b32 v49, 16, v65\n"
      "v_and_b32 v65, 0xffff0000, v65\n"
      "v_mfma_f32_16x16x32_f16 v[196:199], v[232:235], v[160:163], v[196:199]\n"
      "ds_read_b128 v[224:227], v33 offset:1472\n"
      "v_fmac_f32 v70, s74, v49\n"
      "v_fmac_f32 v71, s75, v65\n"
      "s_waitcnt lgkmcnt(5)\n"
      "v_mfma_f32_16x16x32_f16 v[188:191], v[120:123], v[212:215], v[188:191]\n"
      "v_lshlrev_b32 v50, 16, v66\n"
      "v_and_b32 v66, 0xffff0000, v66\n"
      "v_mfma_f32_16x16x32_f16 v[192:195], v[88:91], v[212:215], v[192:195]\n"
      "ds_read2_b32 v[176:177], v30 offset0:104 offset1:105\n"
      "ds_read_b128 v[212:215], v33 offset:1536\n"
      "v_fmac_f32 v26, s76, v50\n"
      "v_fmac_f32 v27, s77, v66\n"
      "s_waitcnt lgkmcnt(5)\n"
      "v_mfma_f32_16x16x32_f16 v[188:191], v[124:127], v[216:219], v[188:191]\n"
      "v_lshlrev_b32 v51, 16, v67\n"
      "v_and_b32 v67, 0xffff0000, v67\n"
      "v_mfma_f32_16x16x32_f16 v[192:195], v[92:95], v[216:219], v[192:195]\n"
      "v_fmac_f32 v28, s78, v51\n"
      "v_fmac_f32 v29, s79, v67\n"
      "s_waitcnt lgkmcnt(3)\n"
      "v_mfma_f32_16x16x32_f16 v[188:191], v[128:131], v[220:223], v[188:191]\n"
      "v_cvt_pk_bf16_f32 v48, v68, v69\n"
      "s_setprio 2\n"
      "v_mfma_f32_16x16x32_f16 v[192:195], v[96:99], v[220:223], v[192:195]\n"
      "ds_read2_b32 v[178:179], v30 offset0:144 offset1:145\n"
      "ds_read_b64_tr_b16 v[228:229], v35 offset:544\n"
      "ds_read_b64_tr_b16 v[230:231], v35 offset:528\n"
      "v_cvt_pk_bf16_f32 v49, v70, v71\n"
      "s_waitcnt lgkmcnt(5)\n"
      "v_mfma_f32_16x16x32_f16 v[188:191], v[132:135], v[224:227], v[188:191]\n"
      "v_cvt_pk_bf16_f32 v50, v26, v27\n"
      "v_mfma_f32_16x16x32_f16 v[192:195], v[100:103], v[224:227], v[192:195]\n"
      "v_cvt_pk_bf16_f32 v51, v28, v29\n"
      "v_lshlrev_b32 v13, 24, v12\n"
      "v_and_or_b32 v13, v13, s58, v42\n"
      "s_waitcnt lgkmcnt(3)\n"
      "v_mfma_f32_16x16x32_f16 v[188:191], v[136:139], v[212:215], v[188:191]\n"
      "ds_read_b128 v[216:219], v33 offset:1600\n"
      "s_cmp_lt_u32 s33, 4\n"
      "s_cbranch_scc0 .Lcs0%=\n"
      "buffer_store_dwordx4 v[48:51], v13, s[24:27], s54 offen\n"
      ".Lcs0%=:\n"
      "v_pk_add_f16 v21, v21, v17\n"
      "v_mfma_f32_16x16x32_f16 v[192:195], v[104:107], v[212:215], v[192:195]\n"
      "v_pk_add_f16 v22, v22, v18\n"
      "v_mfma_f32_16x16x32_f16 v[196:199], v[72:75], v[212:215], v[196:199]\n"
      "ds_read_b128 v[220:223], v33 offset:1664\n"
      "v_pk_add_f16 v23, v23, v19\n"
      "v_mfma_f32_16x16x32_f16 v[204:207], v[44:47], v[212:215], 0\n"
      "ds_write_b128 v38, v[20:23] offset:2080\n"
      "s_waitcnt lgkmcnt(3)\n"
      "v_mfma_f32_16x16x32_f16 v[192:195], v[228:231], v[140:143], v[192:195]\n"
      "ds_read_b128 v[224:227], v33 offset:1728\n"
      "v_alignbit_b32 v245, v238, v237, 16\n"
      "v_cvt_pk_bf16_f32 v24, v188, v190\n"
      "v_cvt_pk_bf16_f32 v25, v189, v191\n"
      "ds_write2st64_b32 v39, v24, v25 offset0:2 offset1:10\n"
      "v_mfma_f32_16x16x32_f16 v[196:199], v[228:231], v[144:147], v[196:199]\n"
      "v_alignbit_b32 v246, v239, v238, 16\n"
      "v_mfma_f32_16x16x32_f16 v[200:203], v[228:231], v[148:151], v[200:203]\n"
      "ds_read_b64_tr_b16 v[232:233], v35 offset:3640\n"
      "ds_read_b64_tr_b16 v[234:235], v35 offset:3624\n"
      "v_alignbit_b32 v247, v240, v239, 16\n"
      "s_waitcnt lgkmcnt(6)\n"
      "v_mfma_f32_16x16x32_f16 v[192:195], v[108:111], v[216:219], v[192:195]\n"
      "v_alignbit_b32 v248, v241, v240, 16\n"
      "v_mfma_f32_16x16x32_f16 v[196:199], v[76:79], v[216:219], v[196:199]\n"
      "v_alignbit_b32 v249, v242, v241, 16\n"
      "s_setprio 1\n"
      "s_waitcnt lgkmcnt(5)\n"
      "v_mfma_f32_16x16x32_f16 v[192:195], v[112:115], v[220:223], v[192:195]\n"
      "ds_read_b128 v[212:215], v33 offset:1792\n"
      "v_pk_add_f16 v16, v245, v246\n"
      "v_mfma_f32_16x16x32_f16 v[196:199], v[80:83], v[220:223], v[196:199]\n"
      "s_cmp_lt_u32 s33, 4\n"
      "s_cbranch_scc1 .Lct0%=\n"
      "buffer_store_dwordx4 v[48:51], v13, s[24:27], s54 offen\n"
      ".Lct0%=:\n"
      "v_pk_add_f16 v17, v246, v247\n"
      "s_waitcnt lgkmcnt(4)\n"
      "v_mfma_f32_16x16x32_f16 v[192:195], v[116:119], v[224:227], v[192:195]\n"
      "ds_read_b128 v[216:219], v33 offset:1856\n"
      "v_pk_add_f16 v18, v247, v248\n"
      "v_mfma_f32_16x16x32_f16 v[196:199], v[84:87], v[224:227], v[196:199]\n"
      "v_pk_add_f16 v19, v248, v249\n"
      "s_waitcnt lgkmcnt(2)\n"
      "v_mfma_f32_16x16x32_f16 v[192:195], v[232:235], v[152:155], v[192:195]\n"
      "ds_read_b128 v[220:223], v33 offset:1920\n"
      "v_pk_add_f16 v20, v16, v238\n"
      "v_mfma_f32_16x16x32_f16 v[196:199], v[232:235], v[156:159], v[196:199]\n"
      "v_pk_add_f16 v21, v17, v239\n"
      "v_mfma_f32_16x16x32_f16 v[200:203], v[232:235], v[160:163], v[200:203]\n"
      "ds_read_b128 v[224:227], v33 offset:1984\n"
      "v_pk_add_f16 v22, v18, v240\n"
      "s_waitcnt lgkmcnt(3)\n"
      "v_mfma_f32_16x16x32_f16 v[192:195], v[120:123], v[212:215], v[192:195]\n"
      "v_pk_add_f16 v23, v19, v241\n"
      "v_mfma_f32_16x16x32_f16 v[196:199], v[88:91], v[212:215], v[196:199]\n"
      "ds_read_b128 v[212:215], v33 offset:2048\n"
      "ds_write_b128 v37, v[20:23] offset:1024\n"
      "v_pk_add_f16 v16, v237, v239\n"
      "s_waitcnt lgkmcnt(4)\n"
      "v_mfma_f32_16x16x32_f16 v[192:195], v[124:127], v[216:219], v[192:195]\n"
      "v_pk_add_f16 v17, v238, v240\n"
      "s_setprio 0\n"
      "v_mfma_f32_16x16x32_f16 v[196:199], v[92:95], v[216:219], v[196:199]\n"
      "v_pk_add_f16 v18, v239, v241\n"
      "s_waitcnt lgkmcnt(3)\n"
      "v_mfma_f32_16x16x32_f16 v[192:195], v[128:131], v[220:223], v[192:195]\n"
      "v_pk_add_f16 v19, v240, v242\n"
      "v_mfma_f32_16x16x32_f16 v[196:199], v[96:99], v[220:223], v[196:199]\n"
      "ds_read_b128 v[216:219], v33 offset:2112\n"
      "v_pk_add_f16 v20, v20, v16\n"
      "s_waitcnt lgkmcnt(3)\n"
      "v_mfma_f32_16x16x32_f16 v[192:195], v[132:135], v[224:227], v[192:195]\n"
      "v_pk_add_f16 v21, v21, v17\n"
      "v_mfma_f32_16x16x32_f16 v[196:199], v[100:103], v[224:227], v[196:199]\n"
      "ds_read_b64_tr_b16 v[228:229], v36 offset:24\n"
      "ds_read_b64_tr_b16 v[230:231], v36 offset:2088\n"
      "v_pk_add_f16 v22, v22, v18\n"
      "s_waitcnt lgkmcnt(4)\n"
      "v_mfma_f32_16x16x32_f16 v[192:195], v[136:139], v[212:215], v[192:195]\n"
      "v_pk_add_f16 v23, v23, v19\n"
      "v_mfma_f32_16x16x32_f16 v[196:199], v[104:107], v[212:215], v[196:199]\n"
      "ds_read_b128 v[220:223], v33 offset:2176\n"
      "ds_write_b128 v37, v[20:23] offset:4128\n"
      "v_alignbit_b32 v244, v237, v236, 16\n"
      "v_mfma_f32_16x16x32_f16 v[200:203], v[72:75], v[212:215], v[200:203]\n"
      "v_alignbit_b32 v250, v243, v242, 16\n"
      "v_mfma_f32_16x16x32_f16 v[208:211], v[44:47], v[212:215], 0\n"
      "ds_read_b128 v[224:227], v33 offset:2240\n"
      "v_pk_add_f16 v16, v244, v247\n"
      "s_waitcnt lgkmcnt(5)\n"
      "v_mfma_f32_16x16x32_f16 v[196:199], v[108:111], v[216:219], v[196:199]\n"
      "v_pk_add_f16 v17, v245, v248\n"
      "v_cvt_pk_bf16_f32 v24, v192, v194\n"
      "v_cvt_pk_bf16_f32 v25, v193, v195\n"
      "ds_write2st64_b32 v39, v24, v25 offset0:3 offset1:11\n"
      "v_mfma_f32_16x16x32_f16 v[200:203], v[76:79], v[216:219], v[200:203]\n"
      "ds_read_b128 v[212:215], v33 offset:2304\n"
      "v_pk_add_f16 v18, v246, v249\n"
      "s_waitcnt lgkmcnt(5)\n"
      "v_mfma_f32_16x16x32_f16 v[196:199], v[228:231], v[164:167], v[196:199]\n"
      "v_pk_add_f16 v19, v247, v250\n"
      "v_mfma_f32_16x16x32_f16 v[200:203], v[228:231], v[168:171], v[200:203]\n"
      "ds_read_b64_tr_b16 v[232:233], v35 offset:1056\n"
      "ds_read_b64_tr_b16 v[234:235], v35 offset:1040\n"
      "v_pk_add_f16 v20, v20, v16\n"
      "s_waitcnt lgkmcnt(3)\n"
      "s_barrier\n"
      "s_setprio 3\n"
      "v_mfma_f32_16x16x32_f16 v[196:199], v[112:115], v[220:223], v[196:199]\n"
      "ds_read_b64_tr_b16 v[64:65], v40 offset:512\n"
      "ds_read_b64_tr_b16 v[66:67], v40 offset:35584\n"
      "v_lshlrev_b32 v68, 16, v52\n"
      "v_and_b32 v69, 0xffff0000, v52\n"
      "v_mfma_f32_16x16x32_f16 v[200:203], v[80:83], v[220:223], v[200:203]\n"
      "ds_read_b128 v[216:219], v33 offset:2368\n"
      "v_lshlrev_b32 v70, 16, v53\n"
      "v_and_b32 v71, 0xffff0000, v53\n"
      "v_mfma_f32_16x16x32_f16 v[196:199], v[116:119], v[224:227], v[196:199]\n"
      "v_lshlrev_b32 v26, 16, v54\n"
      "v_and_b32 v27, 0xffff0000, v54\n"
      "v_mfma_f32_16x16x32_f16 v[200:203], v[84:87], v[224:227], v[200:203]\n"
      "ds_read_b128 v[220:223], v33 offset:2432\n"
      "v_lshlrev_b32 v28, 16, v55\n"
      "v_and_b32 v29, 0xffff0000, v55\n"
      "s_waitcnt lgkmcnt(6)\n"
      "v_mfma_f32_16x16x32_f16 v[196:199], v[120:123], v[212:215], v[196:199]\n"
      "s_waitcnt lgkmcnt(3)\n"
      "v_lshlrev_b32 v52, 16, v64\n"
      "v_and_b32 v64, 0xffff0000, v64\n"
      "v_mfma_f32_16x16x32_f16 v[200:203], v[88:91], v[212:215], v[200:203]\n"
      "ds_read_b64_tr_b16 v[228:229], v35 offset:4152\n"
      "ds_read_b64_tr_b16 v[230:231], v35 offset:4136\n"
      "v_fmac_f32 v68, s72, v52\n"
      "v_fmac_f32 v69, s73, v64\n"
      "v_mfma_f32_16x16x32_f16 v[196:199], v[232:235], v[140:143], v[196:199]\n"
      "v_lshlrev_b32 v53, 16, v65\n"
      "v_and_b32 v65, 0xffff0000, v65\n"
      "v_mfma_f32_16x16x32_f16 v[200:203], v[232:235], v[144:147], v[200:203]\n"
      "ds_read_b128 v[224:227], v33 offset:2496\n"
      "v_fmac_f32 v70, s74, v53\n"
      "v_fmac_f32 v71, s75, v65\n"
      "s_waitcnt lgkmcnt(4)\n"
      "v_mfma_f32_16x16x32_f16 v[196:199], v[124:127], v[216:219], v[196:199]\n"
      "v_lshlrev_b32 v54, 16, v66\n"
      "v_and_b32 v66, 0xffff0000, v66\n"
      "v_mfma_f32_16x16x32_f16 v[200:203], v[92:95], v[216:219], v[200:203]\n"
      "ds_read_b128 v[212:215], v33 offset:2560\n"
      "v_fmac_f32 v26, s76, v54\n"
      "v_fmac_f32 v27, s77, v66\n"
      "s_waitcnt lgkmcnt(4)\n"
      "v_mfma_f32_16x16x32_f16 v[196:199], v[128:131], v[220:223], v[196:199]\n"
      "v_lshlrev_b32 v55, 16, v67\n"
      "v_and_b32 v67, 0xffff0000, v67\n"
      "v_mfma_f32_16x16x32_f16 v[200:203], v[96:99], v[220:223], v[200:203]\n"
      "v_fmac_f32 v28, s78, v55\n"
      "v_fmac_f32 v29, s79, v67\n"
      "s_setprio 2\n"
      "s_waitcnt lgkmcnt(2)\n"
      "v_mfma_f32_16x16x32_f16 v[196:199], v[228:231], v[152:155], v[196:199]\n"
      "ds_read_b128 v[216:219], v33 offset:2624\n"
      "v_cvt_pk_bf16_f32 v52, v68, v69\n"
      "v_mfma_f32_16x16x32_f16 v[200:203], v[228:231], v[156:159], v[200:203]\n"
      "v_cvt_pk_bf16_f32 v53, v70, v71\n"
      "s_waitcnt lgkmcnt(2)\n"
      "v_mfma_f32_16x16x32_f16 v[196:199], v[132:135], v[224:227], v[196:199]\n"
      "ds_read_b64_tr_b16 v[232:233], v36 offset:536\n"
      "ds_read_b64_tr_b16 v[234:235], v36 offset:2600\n"
      "v_cvt_pk_bf16_f32 v54, v26, v27\n"
      "v_mfma_f32_16x16x32_f16 v[200:203], v[100:103], v[224:227], v[200:203]\n"
      "ds_read_b128 v[220:223], v33 offset:2688\n"
      "v_cvt_pk_bf16_f32 v55, v28, v29\n"
      "v_lshlrev_b32 v13, 16, v12\n"
      "v_and_or_b32 v13, v13, s58, v42\n"
      "s_waitcnt lgkmcnt(4)\n"
      "v_mfma_f32_16x16x32_f16 v[196:199], v[136:139], v[212:215], v[196:199]\n"
      "s_cmp_lt_u32 s33, 4\n"
      "s_cbranch_scc0 .Lcs1%=\n"
      "buffer_store_dwordx4 v[52:55], v13, s[24:27], s55 offen\n"
      ".Lcs1%=:\n"
      "v_pk_add_f16 v21, v21, v17\n"
      "v_mfma_f32_16x16x32_f16 v[200:203], v[104:107], v[212:215], v[200:203]\n"
      "ds_read_b128 v[224:227], v33 offset:2752\n"
      "v_pk_add_f16 v22, v22, v18\n"
      "v_mfma_f32_16x16x32_f16 v[204:207], v[72:75], v[212:215], v[204:207]\n"
      "v_pk_add_f16 v23, v23, v19\n"
      "s_waitcnt lgkmcnt(4)\n"
      "v_mfma_f32_16x16x32_f16 v[200:203], v[108:111], v[216:219], v[200:203]\n"
      "ds_read_b64_tr_b16 v[228:229], v35 offset:1568\n"
      "ds_read_b64_tr_b16 v[230:231], v35 offset:1552\n"
      "ds_write_b128 v38, v[20:23] offset:1024\n"
      "v_pk_add_f16 v16, v236, v240\n"
      "v_mfma_f32_16x16x32_f16 v[204:207], v[76:79], v[216:219], v[204:207]\n"
      "ds_read_b128 v[212:215], v33 offset:2816\n"
      "v_pk_add_f16 v17, v237, v241\n"
      "v_cvt_pk_bf16_f32 v24, v196, v198\n"
      "v_cvt_pk_bf16_f32 v25, v197, v199\n"
      "ds_write2st64_b32 v39, v24, v25 offset0:4 offset1:12\n"
      "s_waitcnt lgkmcnt(7)\n"
      "v_mfma_f32_16x16x32_f16 v[200:203], v[232:235], v[164:167], v[200:203]\n"
      "v_pk_add_f16 v18, v238, v242\n"
      "s_waitcnt lgkmcnt(6)\n"
      "v_mfma_f32_16x16x32_f16 v[200:203], v[112:115], v[220:223], v[200:203]\n"
      "ds_read_b128 v[216:219], v33 offset:2880\n"
      "v_pk_add_f16 v19, v239, v243\n"
      "s_setprio 1\n"
      "v_mfma_f32_16x16x32_f16 v[204:207], v[80:83], v[220:223], v[204:207]\n"
      "v_pk_add_f16 v20, v20, v16\n"
      "ds_read_b128 v[236:239], v34 offset:2048\n"
      "ds_read_b128 v[240:243], v34 offset:2064\n"
      "s_waitcnt lgkmcnt(8)\n"
      "v_mfma_f32_16x16x32_f16 v[200:203], v[116:119], v[224:227], v[200:203]\n"
      "ds_read_b64_tr_b16 v[232:233], v35 offset:4664\n"
      "ds_read_b64_tr_b16 v[234:235], v35 offset:4648\n"
      "v_pk_add_f16 v21, v21, v17\n"
      "v_mfma_f32_16x16x32_f16 v[204:207], v[84:87], v[224:227], v[204:207]\n"
      "ds_read_b128 v[220:223], v33 offset:2944\n"
      "v_pk_add_f16 v22, v22, v18\n"
      "s_waitcnt lgkmcnt(9)\n"
      "v_mfma_f32_16x16x32_f16 v[200:203], v[228:231], v[140:143], v[200:203]\n"
      "s_cmp_lt_u32 s33, 4\n"
      "s_cbranch_scc1 .Lct1%=\n"
      "buffer_store_dwordx4 v[52:55], v13, s[24:27], s55 offen\n"
      ".Lct1%=:\n"
      "v_pk_add_f16 v23, v23, v19\n"
      "s_waitcnt lgkmcnt(7)\n"
      "v_mfma_f32_16x16x32_f16 v[200:203], v[120:123], v[212:215], v[200:203]\n"
      "ds_read_b128 v[224:227], v33 offset:3008\n"
      "ds_write_b128 v38, v[20:23] offset:3104\n"
      "v_mfma_f32_16x16x32_f16 v[204:207], v[88:91], v[212:215], v[204:207]\n"
      "s_waitcnt lgkmcnt(6)\n"
      "v_alignbit_b32 v245, v238, v237, 16\n"
      "v_mfma_f32_16x16x32_f16 v[200:203], v[124:127], v[216:219], v[200:203]\n"
      "ds_read_b128 v[212:215], v33 offset:3072\n"
      "v_alignbit_b32 v246, v239, v238, 16\n"
      "v_mfma_f32_16x16x32_f16 v[204:207], v[92:95], v[216:219], v[204:207]\n"
      "s_waitcnt lgkmcnt(6)\n"
      "v_alignbit_b32 v247, v240, v239, 16\n"
      "s_waitcnt lgkmcnt(4)\n"
      "v_mfma_f32_16x16x32_f16 v[200:203], v[232:235], v[152:155], v[200:203]\n"
      "v_alignbit_b32 v248, v241, v240, 16\n"
      "s_setprio 0\n"
      "s_waitcnt lgkmcnt(3)\n"
      "v_mfma_f32_16x16x32_f16 v[200:203], v[128:131], v[220:223], v[200:203]\n"
      "ds_read_b128 v[216:219], v33 offset:3136\n"
      "v_alignbit_b32 v249, v242, v241, 16\n"
      "v_mfma_f32_16x16x32_f16 v[204:207], v[96:99], v[220:223], v[204:207]\n"
      "v_pk_add_f16 v16, v245, v246\n"
      "s_waitcnt lgkmcnt(3)\n"
      "v_mfma_f32_16x16x32_f16 v[200:203], v[132:135], v[224:227], v[200:203]\n"
      "ds_read_b64_tr_b16 v[228:229], v36 offset:24\n"
      "ds_read_b64_tr_b16 v[230:231], v36 offset:2088\n"
      "v_pk_add_f16 v17, v246, v247\n"
      "v_mfma_f32_16x16x32_f16 v[204:207], v[100:103], v[224:227], v[204:207]\n"
      "v_pk_add_f16 v18, v247, v248\n"
      "s_waitcnt lgkmcnt(3)\n"
      "v_mfma_f32_16x16x32_f16 v[200:203], v[136:139], v[212:215], v[200:203]\n"
      "ds_read_b128 v[220:223], v33 offset:3200\n"
      "v_pk_add_f16 v19, v248, v249\n"
      "v_mfma_f32_16x16x32_f16 v[204:207], v[104:107], v[212:215], v[204:207]\n"
      "v_pk_add_f16 v20, v16, v238\n"
      "v_mfma_f32_16x16x32_f16 v[208:211], v[72:75], v[212:215], v[208:211]\n"
      "ds_read_b64_tr_b16 v[232:233], v36 offset:536\n"
      "ds_read_b64_tr_b16 v[234:235], v36 offset:2600\n"
      "v_pk_add_f16 v21, v17, v239\n"
      "s_waitcnt lgkmcnt(5)\n"
      "v_mfma_f32_16x16x32_f16 v[204:207], v[108:111], v[216:219], v[204:207]\n"
      "v_pk_add_f16 v22, v18, v240\n"
      "v_mfma_f32_16x16x32_f16 v[208:211], v[76:79], v[216:219], v[208:211]\n"
      "ds_read_b128 v[224:227], v33 offset:3264\n"
      "v_pk_add_f16 v23, v19, v241\n"
      "v_cvt_pk_bf16_f32 v24, v200, v202\n"
      "v_cvt_pk_bf16_f32 v25, v201, v203\n"
      "ds_write2st64_b32 v39, v24, v25 offset0:5 offset1:13\n"
      "s_waitcnt lgkmcnt(5)\n"
      "v_mfma_f32_16x16x32_f16 v[204:207], v[228:231], v[172:175], v[204:207]\n"
      "ds_write_b128 v37, v[20:23] offset:2048\n"
      "v_pk_add_f16 v16, v237, v239\n"
      "v_mfma_f32_16x16x32_f16 v[208:211], v[228:231], v[176:179], v[208:211]\n"
      "ds_read_b64_tr_b16 v[228:229], v35 offset:1056\n"
      "ds_read_b64_tr_b16 v[230:231], v35 offset:1040\n"
      "v_pk_add_f16 v17, v238, v240\n"
      "s_waitcnt lgkmcnt(7)\n"
      "v_mfma_f32_16x16x32_f16 v[204:207], v[112:115], v[220:223], v[204:207]\n"
      "ds_read_b128 v[212:215], v33 offset:3328\n"
      "v_pk_add_f16 v18, v239, v241\n"
      "s_waitcnt lgkmcnt(4)\n"
      "s_barrier\n"
      "s_setprio 3\n"
      "v_mfma_f32_16x16x32_f16 v[208:211], v[80:83], v[220:223], v[208:211]\n"
      "ds_read_b64_tr_b16 v[64:65], v40 offset:1024\n"
      "ds_read_b64_tr_b16 v[66:67], v40 offset:36096\n"
      "v_lshlrev_b32 v68, 16, v56\n"
      "v_and_b32 v69, 0xffff0000, v56\n"
      "v_mfma_f32_16x16x32_f16 v[204:207], v[232:235], v[168:171], v[204:207]\n"
      "v_lshlrev_b32 v70, 16, v57\n"
      "v_and_b32 v71, 0xffff0000, v57\n"
      "v_mfma_f32_16x16x32_f16 v[208:211], v[232:235], v[172:175], v[208:211]\n"
      "ds_read_b128 v[216:219], v33 offset:3392\n"
      "ds_read_b64_tr_b16 v[232:233], v35 offset:4152\n"
      "ds_read_b64_tr_b16 v[234:235], v35 offset:4136\n"
      "v_lshlrev_b32 v26, 16, v58\n"
      "v_and_b32 v27, 0xffff0000, v58\n"
      "v_mfma_f32_16x16x32_f16 v[204:207], v[116:119], v[224:227], v[204:207]\n"
      "v_lshlrev_b32 v28, 16, v59\n"
      "v_and_b32 v29, 0xffff0000, v59\n"
      "v_mfma_f32_16x16x32_f16 v[208:211], v[84:87], v[224:227], v[208:211]\n"
      "s_waitcnt lgkmcnt(4)\n"
      "v_lshlrev_b32 v56, 16, v64\n"
      "v_and_b32 v64, 0xffff0000, v64\n"
      "v_mfma_f32_16x16x32_f16 v[204:207], v[228:231], v[148:151], v[204:207]\n"
      "ds_read_b64_tr_b16 v[228:229], v35 offset:1568\n"
      "ds_read_b64_tr_b16 v[230:231], v35 offset:1552\n"
      "v_fmac_f32 v68, s72, v56\n"
      "v_fmac_f32 v69, s73, v64\n"
      "v_mfma_f32_16x16x32_f16 v[204:207], v[120:123], v[212:215], v[204:207]\n"
      "ds_read_b128 v[220:223], v33 offset:3456\n"
      "v_lshlrev_b32 v57, 16, v65\n"
      "v_and_b32 v65, 0xffff0000, v65\n"
      "v_mfma_f32_16x16x32_f16 v[208:211], v[88:91], v[212:215], v[208:211]\n"
      "v_fmac_f32 v70, s74, v57\n"
      "v_fmac_f32 v71, s75, v65\n"
      "s_waitcnt lgkmcnt(3)\n"
      "v_mfma_f32_16x16x32_f16 v[204:207], v[232:235], v[160:163], v[204:207]\n"
      "ds_read_b64_tr_b16 v[232:233], v35 offset:4664\n"
      "ds_read_b64_tr_b16 v[234:235], v35 offset:4648\n"
      "v_lshlrev_b32 v58, 16, v66\n"
      "v_and_b32 v66, 0xffff0000, v66\n"
      "v_mfma_f32_16x16x32_f16 v[204:207], v[124:127], v[216:219], v[204:207]\n"
      "v_fmac_f32 v26, s76, v58\n"
      "v_fmac_f32 v27, s77, v66\n"
      "s_setprio 2\n"
      "v_mfma_f32_16x16x32_f16 v[208:211], v[92:95], v[216:219], v[208:211]\n"
      "ds_read_b128 v[224:227], v33 offset:3520\n"
      "v_lshlrev_b32 v59, 16, v67\n"
      "v_and_b32 v67, 0xffff0000, v67\n"
      "s_waitcnt lgkmcnt(4)\n"
      "v_mfma_f32_16x16x32_f16 v[204:207], v[228:231], v[144:147], v[204:207]\n"
      "v_fmac_f32 v28, s78, v59\n"
      "v_fmac_f32 v29, s79, v67\n"
      "v_mfma_f32_16x16x32_f16 v[208:211], v[228:231], v[148:151], v[208:211]\n"
      "ds_read_b64_tr_b16 v[228:229], v36 offset:1048\n"
      "ds_read_b64_tr_b16 v[230:231], v36 offset:3112\n"
      "v_cvt_pk_bf16_f32 v56, v68, v69\n"
      "s_waitcnt lgkmcnt(5)\n"
      "v_mfma_f32_16x16x32_f16 v[204:207], v[128:131], v[220:223], v[204:207]\n"
      "v_cvt_pk_bf16_f32 v57, v70, v71\n"
      "v_mfma_f32_16x16x32_f16 v[208:211], v[96:99], v[220:223], v[208:211]\n"
      "ds_read_b128 v[212:215], v33 offset:3584\n"
      "v_cvt_pk_bf16_f32 v58, v26, v27\n"
      "s_waitcnt lgkmcnt(4)\n"
      "v_mfma_f32_16x16x32_f16 v[204:207], v[232:235], v[156:159], v[204:207]\n"
      "v_cvt_pk_bf16_f32 v59, v28, v29\n"
      "v_lshlrev_b32 v13, 8, v12\n"
      "v_and_or_b32 v13, v13, s58, v42\n"
      "s_cmp_lg_u32 s33, 0\n"
      "s_cbranch_scc1 .Lcp%=\n"
      "v_lshlrev_b32 v46, 4, v43\n"
      "s_add_u32 s67, s44, 0x1000\n"
      "buffer_load_dwordx4 v[72:75], v46, s[16:19], s44 offen offset:0 sc1\n"
      "buffer_load_dwordx4 v[76:79], v46, s[16:19], s44 offen offset:1024 sc1\n"
      "buffer_load_dwordx4 v[80:83], v46, s[16:19], s44 offen offset:2048 sc1\n"
      "buffer_load_dwordx4 v[84:87], v46, s[16:19], s44 offen offset:3072 sc1\n"
      "buffer_load_dwordx4 v[88:91], v46, s[16:19], s67 offen offset:0 sc1\n"
      "buffer_load_dwordx4 v[92:95], v46, s[16:19], s67 offen offset:1024 sc1\n"
      "buffer_load_dwordx4 v[96:99], v46, s[16:19], s67 offen offset:2048 sc1\n"
      ".Lcp%=:\n"
      "v_mfma_f32_16x16x32_f16 v[208:211], v[232:235], v[160:163], v[208:211]\n"
      "ds_read_b128 v[216:219], v33 offset:3648\n"
      "s_cmp_lt_u32 s33, 4\n"
      "s_cbranch_scc0 .Lcs2%=\n"
      "buffer_store_dwordx4 v[56:59], v13, s[24:27], s56 offen\n"
      ".Lcs2%=:\n"
      "v_pk_add_f16 v19, v240, v242\n"
      "s_waitcnt lgkmcnt(4)\n"
      "v_mfma_f32_16x16x32_f16 v[204:207], v[132:135], v[224:227], v[204:207]\n"
      "ds_read_b64_tr_b16 v[232:233], v35 offset:2080\n"
      "ds_read_b64_tr_b16 v[234:235], v35 offset:2064\n"
      "v_pk_add_f16 v20, v20, v16\n"
      "v_mfma_f32_16x16x32_f16 v[208:211], v[100:103], v[224:227], v[208:211]\n"
      "v_pk_add_f16 v21, v21, v17\n"
      "s_waitcnt lgkmcnt(4)\n"
      "v_mfma_f32_16x16x32_f16 v[204:207], v[228:231], v[164:167], v[204:207]\n"
      "ds_read_b128 v[220:223], v33 offset:3712\n"
      "v_pk_add_f16 v22, v22, v18\n"
      "s_setprio 1\n"
      "v_mfma_f32_16x16x32_f16 v[208:211], v[228:231], v[168:171], v[208:211]\n"
      "ds_read_b128 v[224:227], v33 offset:3776\n"
      "v_pk_add_f16 v23, v23, v19\n"
      "s_waitcnt lgkmcnt(5)\n"
      "v_mfma_f32_16x16x32_f16 v[204:207], v[136:139], v[212:215], v[204:207]\n"
      "ds_write_b128 v37, v[20:23] offset:5152\n"
      "v_mfma_f32_16x16x32_f16 v[208:211], v[104:107], v[212:215], v[208:211]\n"
      "ds_read_b64_tr_b16 v[228:229], v35 offset:5176\n"
      "ds_read_b64_tr_b16 v[230:231], v35 offset:5160\n"
      "s_waitcnt lgkmcnt(7)\n"
      "v_mfma_f32_16x16x32_f16 v[208:211], v[108:111], v[216:219], v[208:211]\n"
      "ds_read_b128 v[212:215], v33 offset:3840\n"
      "s_waitcnt lgkmcnt(6)\n"
      "v_mfma_f32_16x16x32_f16 v[204:207], v[232:235], v[140:143], v[204:207]\n"
      "ds_read_b128 v[216:219], v33 offset:3904\n"
      "v_mfma_f32_16x16x32_f16 v[208:211], v[232:235], v[144:147], v[208:211]\n"
      "ds_read_b64_tr_b16 v[232:233], v36 offset:1560\n"
      "ds_read_b64_tr_b16 v[234:235], v36 offset:3624\n"
      "s_waitcnt lgkmcnt(8)\n"
      "v_mfma_f32_16x16x32_f16 v[208:211], v[112:115], v[220:223], v[208:211]\n"
      "ds_read_b128 v[220:223], v33 offset:3968\n"
      "s_cmp_lt_u32 s33, 4\n"
      "s_cbranch_scc1 .Lct2%=\n"
      "buffer_store_dwordx4 v[56:59], v13, s[24:27], s56 offen\n"
      ".Lct2%=:\n"
      "s_setprio 0\n"
      "s_waitcnt lgkmcnt(8)\n"
      "v_mfma_f32_16x16x32_f16 v[208:211], v[116:119], v[224:227], v[208:211]\n"
      "s_waitcnt lgkmcnt(5)\n"
      "v_mfma_f32_16x16x32_f16 v[204:207], v[228:231], v[152:155], v[204:207]\n"
      "ds_read_b128 v[224:227], v33 offset:4032\n"
      "v_mfma_f32_16x16x32_f16 v[208:211], v[228:231], v[156:159], v[208:211]\n"
      "ds_read_b64_tr_b16 v[228:229], v35 offset:2592\n"
      "ds_read_b64_tr_b16 v[230:231], v35 offset:2576\n"
      "s_waitcnt lgkmcnt(7)\n"
      "v_mfma_f32_16x16x32_f16 v[208:211], v[120:123], v[212:215], v[208:211]\n"
      "ds_read_b128 v[212:215], v33 offset:4096\n"
      "s_waitcnt lgkmcnt(7)\n"
      "v_mfma_f32_16x16x32_f16 v[208:211], v[124:127], v[216:219], v[208:211]\n"
      "s_waitcnt lgkmcnt(5)\n"
      "v_mfma_f32_16x16x32_f16 v[208:211], v[232:235], v[164:167], v[208:211]\n"
      "ds_read_b64_tr_b16 v[232:233], v35 offset:5688\n"
      "ds_read_b64_tr_b16 v[234:235], v35 offset:5672\n"
      "v_cvt_pk_bf16_f32 v24, v204, v206\n"
      "v_cvt_pk_bf16_f32 v25, v205, v207\n"
      "ds_write2st64_b32 v39, v24, v25 offset0:6 offset1:14\n"
      "s_waitcnt lgkmcnt(7)\n"
      "v_mfma_f32_16x16x32_f16 v[208:211], v[128:131], v[220:223], v[208:211]\n"
      "s_waitcnt lgkmcnt(4)\n"
      "v_mfma_f32_16x16x32_f16 v[208:211], v[228:231], v[140:143], v[208:211]\n"
      "v_mfma_f32_16x16x32_f16 v[208:211], v[132:135], v[224:227], v[208:211]\n"
      "s_waitcnt lgkmcnt(1)\n"
      "v_mfma_f32_16x16x32_f16 v[208:211], v[232:235], v[152:155], v[208:211]\n"
      "v_mfma_f32_16x16x32_f16 v[208:211], v[136:139], v[212:215], v[208:211]\n"
      "v_lshlrev_b32 v68, 16, v60\n"
      "v_and_b32 v69, 0xffff0000, v60\n"
      "v_lshlrev_b32 v70, 16, v61\n"
      "v_and_b32 v71, 0xffff0000, v61\n"
      "v_lshlrev_b32 v26, 16, v62\n"
      "v_and_b32 v27, 0xffff0000, v62\n"
      "v_lshlrev_b32 v28, 16, v63\n"
      "v_and_b32 v29, 0xffff0000, v63\n"
      "v_cvt_pk_bf16_f32 v24, v208, v210\n"
      "v_cvt_pk_bf16_f32 v25, v209, v211\n"
      "ds_write2st64_b32 v39, v24, v25 offset0:7 offset1:15\n"
      "s_waitcnt lgkmcnt(0)\n"
      "s_barrier\n"
      "ds_read_b64_tr_b16 v[64:65], v40 offset:1536\n"
      "ds_read_b64_tr_b16 v[66:67], v40 offset:36608\n"
      "s_waitcnt lgkmcnt(1)\n"
      "v_lshlrev_b32 v60, 16, v64\n"
      "v_and_b32 v64, 0xffff0000, v64\n"
      "v_fmac_f32 v68, s72, v60\n"
      "v_fmac_f32 v69, s73, v64\n"
      "v_lshlrev_b32 v61, 16, v65\n"
      "v_and_b32 v65, 0xffff0000, v65\n"
      "v_fmac_f32 v70, s74, v61\n"
      "v_fmac_f32 v71, s75, v65\n"
      "s_waitcnt lgkmcnt(0)\n"
      "v_lshlrev_b32 v62, 16, v66\n"
      "v_and_b32 v66, 0xffff0000, v66\n"
      "v_fmac_f32 v26, s76, v62\n"
      "v_fmac_f32 v27, s77, v66\n"
      "v_lshlrev_b32 v63, 16, v67\n"
      "v_and_b32 v67, 0xffff0000, v67\n"
      "v_fmac_f32 v28, s78, v63\n"
      "v_fmac_f32 v29, s79, v67\n"
      "v_cvt_pk_bf16_f32 v60, v68, v69\n"
      "v_cvt_pk_bf16_f32 v61, v70, v71\n"
      "v_cvt_pk_bf16_f32 v62, v26, v27\n"
      "v_cvt_pk_bf16_f32 v63, v28, v29\n"
      "v_and_or_b32 v13, v12, s58, v42\n"
      "buffer_store_dwordx4 v[60:63], v13, s[24:27], s57 offen\n"
      "s_cmp_lg_u32 s33, 0\n"
      "s_cbranch_scc1 .LEx%=\n"
      "v_and_b32 v31, 1, v255\n"
      "v_and_b32 v8, 0x7fe, v255\n"
      "v_lshl_add_u32 v8, v31, 11, v8\n"
      "v_mul_u32_u24 v31, 0x1120, v10\n"
      "v_add_u32 v8, v8, v31\n"
      "v_not_b32 v9, v252\n"
      "v_lshlrev_b32 v31, 1, v10\n"
      "v_ashrrev_i32 v9, 31, v9\n"
      "v_add_u32 v31, 0x27600, v31\n"
      "ds_read_u16 v45, v8 offset:56384\n"
      "ds_read_b32 v15, v31 \n"
      "v_or_b32 v9, 0x80000000, v9\n"
      "v_xor_b32 v9, v9, v252\n"
      "v_and_b32 v9, 0xffff0000, v9\n"
      "v_mul_f32 v9, 0xbfb8aa3b, v9\n"
      "v_exp_f32 v9, v9\n"
      "s_nop 0\n"
      "v_add_f32 v9, 1.0, v9\n"
      "v_rcp_f32 v9, v9\n"
      "s_nop 0\n"
      "v_cvt_pk_bf16_f32 v9, v9, v9\n"
      "v_and_b32 v9, 0xffff0000, v9\n"
      "s_movk_i32 s59, 0x7fff\n"
      "s_waitcnt vmcnt(2)\n"
      "s_nop 0\n"
      "v_lshlrev_b32 v254, 16, v254\n"
      ".Lcq%=:\n"
      "v_xor_b32 v16, s15, v73\n"
      "v_xor_b32 v17, s15, v75\n"
      "v_bitop3_b32 v16, s15, v77, v16 bitop3:0xbe\n"
      "v_bitop3_b32 v17, s15, v79, v17 bitop3:0xbe\n"
      "v_bitop3_b32 v16, s15, v81, v16 bitop3:0xbe\n"
      "v_bitop3_b32 v17, s15, v83, v17 bitop3:0xbe\n"
      "v_bitop3_b32 v16, s15, v85, v16 bitop3:0xbe\n"
      "v_bitop3_b32 v17, s15, v87, v17 bitop3:0xbe\n"
      "v_bitop3_b32 v16, s15, v89, v16 bitop3:0xbe\n"
      "v_bitop3_b32 v17, s15, v91, v17 bitop3:0xbe\n"
      "v_bitop3_b32 v16, s15, v93, v16 bitop3:0xbe\n"
      "v_bitop3_b32 v17, s15, v95, v17 bitop3:0xbe\n"
      "v_bitop3_b32 v16, s15, v97, v16 bitop3:0xbe\n"
      "v_bitop3_b32 v17, s15, v99, v17 bitop3:0xbe\n"
      "v_or_b32 v16, v16, v17\n"
      "v_cmp_ne_u32 vcc, 0, v16\n"
      "v_lshlrev_b32 v16, 16, v72\n"
      "v_and_b32 v17, 0xffff0000, v72\n"
      "v_lshlrev_b32 v18, 16, v74\n"
      "v_and_b32 v19, 0xffff0000, v74\n"
      "s_cbranch_vccnz .Lcqs%=\n"
      ".Lcqo%=:\n"
      "v_lshlrev_b32 v20, 16, v76\n"
      "v_and_b32 v21, 0xffff0000, v76\n"
      "v_lshlrev_b32 v22, 16, v78\n"
      "v_and_b32 v23, 0xffff0000, v78\n"
      "v_add_f32 v16, v16, v20\n"
      "v_add_f32 v17, v17, v21\n"
      "v_add_f32 v18, v18, v22\n"
      "v_add_f32 v19, v19, v23\n"
      "v_lshlrev_b32 v20, 16, v80\n"
      "v_and_b32 v21, 0xffff0000, v80\n"
      "v_lshlrev_b32 v22, 16, v82\n"
      "v_and_b32 v23, 0xffff0000, v82\n"
      "v_add_f32 v16, v16, v20\n"
      "v_add_f32 v17, v17, v21\n"
      "v_add_f32 v18, v18, v22\n"
      "v_add_f32 v19, v19, v23\n"
      "v_lshlrev_b32 v20, 16, v84\n"
      "v_and_b32 v21, 0xffff0000, v84\n"
      "v_lshlrev_b32 v22, 16, v86\n"
      "v_and_b32 v23, 0xffff0000, v86\n"
      "v_add_f32 v16, v16, v20\n"
      "v_add_f32 v17, v17, v21\n"
      "v_add_f32 v18, v18, v22\n"
      "v_add_f32 v19, v19, v23\n"
      "v_lshlrev_b32 v20, 16, v88\n"
      "v_and_b32 v21, 0xffff0000, v88\n"
      "v_lshlrev_b32 v22, 16, v90\n"
      "v_and_b32 v23, 0xffff0000, v90\n"
      "v_add_f32 v16, v16, v20\n"
      "v_add_f32 v17, v17, v21\n"
      "v_add_f32 v18, v18, v22\n"
      "v_add_f32 v19, v19, v23\n"
      "v_lshlrev_b32 v20, 16, v92\n"
      "v_and_b32 v21, 0xffff0000, v92\n"
      "v_lshlrev_b32 v22, 16, v94\n"
      "v_and_b32 v23, 0xffff0000, v94\n"
      "v_add_f32 v16, v16, v20\n"
      "v_add_f32 v17, v17, v21\n"
      "v_add_f32 v18, v18, v22\n"
      "v_add_f32 v19, v19, v23\n"
      "v_lshlrev_b32 v20, 16, v96\n"
      "v_and_b32 v21, 0xffff0000, v96\n"
      "v_lshlrev_b32 v22, 16, v98\n"
      "v_and_b32 v23, 0xffff0000, v98\n"
      "v_add_f32 v16, v16, v20\n"
      "v_add_f32 v17, v17, v21\n"
      "v_add_f32 v18, v18, v22\n"
      "v_add_f32 v19, v19, v23\n"
      "s_waitcnt lgkmcnt(1)\n"
      "v_lshlrev_b32 v45, 16, v45\n"
      "v_permlane32_swap_b32 v16, v18\n"
      "v_permlane32_swap_b32 v17, v19\n"
      "v_add_f32 v16, v16, v18\n"
      "v_add_f32 v17, v17, v19\n"
      "s_waitcnt lgkmcnt(0)\n"
      "v_fmac_f32 v254, v45, v15\n"
      "v_permlane16_swap_b32 v16, v17\n"
      "v_add_f32 v16, v16, v17\n"
      "v_cvt_pk_bf16_f32 v16, v16, v16\n"
      "v_and_b32 v16, 0xffff0000, v16\n"
      "v_mul_f32 v16, v16, v9\n"
      "v_cvt_pk_bf16_f32 v16, v16, v16\n"
      "v_and_b32 v16, 0xffff0000, v16\n"
      "v_add_f32 v254, v254, v16\n"
      "v_cvt_pk_bf16_f32 v254, v254, v254\n"
      "buffer_store_short v254, v251, s[24:27], s41 offen\n"
      ".LEx%=:\ns_endpgm\n"
      ".Lcsgs%=:\n"
      "s_sleep 2\n"
      "s_sub_u32 s59, s59, 1\n"
      "s_cbranch_scc1 .Lcsgo%=\n"
      "buffer_load_dwordx4 v[64:67], v42, s[16:19], s62 offen sc1\n"
      "buffer_load_dwordx4 v[68:71], v42, s[16:19], s63 offen sc1\n"
      "s_waitcnt vmcnt(0)\n"
      "s_branch .Lcsg%=\n"
      ".Lch%=:\n"
      "s_movk_i32 s59, 0x7fff\n"
      ".Lchl%=:\n"
      "s_waitcnt vmcnt(0)\n"
      "v_cmp_ne_u32 vcc, s15, v253\n"
      "v_not_b32 v255, v252\n"
      "s_sub_u32 s59, s59, 1\n"
      "s_cbranch_scc1 .Lchk%=\n"
      "s_cbranch_vccz .Lchk%=\n"
      "s_sleep 1\n"
      "buffer_load_dwordx2 v[252:253], v9, s[16:19], s43 offen sc1\n"
      "s_branch .Lchl%=\n"
      ".Lchk%=:\n"
      "v_and_b32 v31, 0x1ff, v255\n"
      "v_bfe_u32 v8, v255, 9, 2\n"
      "v_lshl_add_u32 v31, v31, 2, v8\n"
      "v_mov_b32 v8, 64\n"
      "v_add_u32 v31, 0x27700, v31\n"
      "ds_write_b8 v31, v8 \n"
      "v_and_b32 v255, 0x7ff, v255\n"
      "v_lshl_add_u32 v251, v255, 11, v10\n"
      "buffer_load_ushort v254, v251, s[20:23], s41 offen\n"
      "s_waitcnt lgkmcnt(0)\n"
      "s_branch .Lcho%=\n"
      ".Lcqs%=:\n"
      "s_sleep 4\n"
      "s_sub_u32 s59, s59, 1\n"
      "s_cbranch_scc1 .Lcqo%=\n"
      "v_cmp_ne_u32 vcc, s15, v73\n"
      "v_cmp_ne_u32 s[64:65], s15, v75\n"
      "s_nop 0\n"
      "s_or_b64 exec, vcc, s[64:65]\n"
      "s_cbranch_scc0 .Lcqk0%=\n"
      "buffer_load_dwordx4 v[72:75], v46, s[16:19], s44 offen offset:0 sc1\n"
      ".Lcqk0%=:\n"
      "s_mov_b64 exec, -1\n"
      "v_cmp_ne_u32 vcc, s15, v77\n"
      "v_cmp_ne_u32 s[64:65], s15, v79\n"
      "s_nop 0\n"
      "s_or_b64 exec, vcc, s[64:65]\n"
      "s_cbranch_scc0 .Lcqk1%=\n"
      "buffer_load_dwordx4 v[76:79], v46, s[16:19], s44 offen offset:1024 sc1\n"
      ".Lcqk1%=:\n"
      "s_mov_b64 exec, -1\n"
      "v_cmp_ne_u32 vcc, s15, v81\n"
      "v_cmp_ne_u32 s[64:65], s15, v83\n"
      "s_nop 0\n"
      "s_or_b64 exec, vcc, s[64:65]\n"
      "s_cbranch_scc0 .Lcqk2%=\n"
      "buffer_load_dwordx4 v[80:83], v46, s[16:19], s44 offen offset:2048 sc1\n"
      ".Lcqk2%=:\n"
      "s_mov_b64 exec, -1\n"
      "v_cmp_ne_u32 vcc, s15, v85\n"
      "v_cmp_ne_u32 s[64:65], s15, v87\n"
      "s_nop 0\n"
      "s_or_b64 exec, vcc, s[64:65]\n"
      "s_cbranch_scc0 .Lcqk3%=\n"
      "buffer_load_dwordx4 v[84:87], v46, s[16:19], s44 offen offset:3072 sc1\n"
      ".Lcqk3%=:\n"
      "s_mov_b64 exec, -1\n"
      "v_cmp_ne_u32 vcc, s15, v89\n"
      "v_cmp_ne_u32 s[64:65], s15, v91\n"
      "s_nop 0\n"
      "s_or_b64 exec, vcc, s[64:65]\n"
      "s_cbranch_scc0 .Lcqk4%=\n"
      "buffer_load_dwordx4 v[88:91], v46, s[16:19], s67 offen offset:0 sc1\n"
      ".Lcqk4%=:\n"
      "s_mov_b64 exec, -1\n"
      "v_cmp_ne_u32 vcc, s15, v93\n"
      "v_cmp_ne_u32 s[64:65], s15, v95\n"
      "s_nop 0\n"
      "s_or_b64 exec, vcc, s[64:65]\n"
      "s_cbranch_scc0 .Lcqk5%=\n"
      "buffer_load_dwordx4 v[92:95], v46, s[16:19], s67 offen offset:1024 sc1\n"
      ".Lcqk5%=:\n"
      "s_mov_b64 exec, -1\n"
      "v_cmp_ne_u32 vcc, s15, v97\n"
      "v_cmp_ne_u32 s[64:65], s15, v99\n"
      "s_nop 0\n"
      "s_or_b64 exec, vcc, s[64:65]\n"
      "s_cbranch_scc0 .Lcqk6%=\n"
      "buffer_load_dwordx4 v[96:99], v46, s[16:19], s67 offen offset:2048 sc1\n"
      ".Lcqk6%=:\n"
      "s_mov_b64 exec, -1\n"
      "s_mov_b64 exec, -1\n"
      "s_waitcnt vmcnt(0)\n"
      "s_branch .Lcq%=\n"
      ".LEnd%=:\n"
      : "={s[80:95]}"(sts), "={s[96:99]}"(st2)
      : [ka] "s"(__builtin_amdgcn_kernarg_segment_ptr()), [bid] "s"(blockIdx.x), "{v0}"(threadIdx.x), "s"((u32)(uintptr_t)sm)
      : "v8", "v9", "v10", "v11", "v12", "v13", "v14", "v15", "v16", "v17", "v18", "v19", "v20", "v21", "v22", "v23", "v24", "v25", "v26", "v27",
        "v28", "v29", "v30", "v31", "v32", "v33", "v34", "v35", "v36", "v37", "v38", "v39", "v40", "v41", "v42", "v43", "v44", "v45", "v46", "v47",
        "v48", "v49", "v50", "v51", "v52", "v53", "v54", "v55", "v56", "v57", "v58", "v59", "v60", "v61", "v62", "v63", "v64", "v65", "v66", "v67",
        "v68", "v69", "v70", "v71", "v72", "v73", "v74", "v75", "v76", "v77", "v78", "v79", "v80", "v81", "v82", "v83", "v84", "v85", "v86", "v87",
        "v88", "v89", "v90", "v91", "v92", "v93", "v94", "v95", "v96", "v97", "v98", "v99", "v100", "v101", "v102", "v103", "v104", "v105", "v106", "v107",
        "v108", "v109", "v110", "v111", "v112", "v113", "v114", "v115", "v116", "v117", "v118", "v119", "v120", "v121", "v122", "v123", "v124", "v125", "v126", "v127",
        "v128", "v129", "v130", "v131", "v132", "v133", "v134", "v135", "v136", "v137", "v138", "v139", "v140", "v141", "v142", "v143", "v144", "v145", "v146", "v147",
        "v148", "v149", "v150", "v151", "v152", "v153", "v154", "v155", "v156", "v157", "v158", "v159", "v160", "v161", "v162", "v163", "v164", "v165", "v166", "v167",
        "v168", "v169", "v170", "v171", "v172", "v173", "v174", "v175", "v176", "v177", "v178", "v179", "v180", "v181", "v182", "v183", "v184", "v185", "v186", "v187",
        "v188", "v189", "v190", "v191", "v192", "v193", "v194", "v195", "v196", "v197", "v198", "v199", "v200", "v201", "v202", "v203", "v204", "v205", "v206", "v207",
        "v208", "v209", "v210", "v211", "v212", "v213", "v214", "v215", "v216", "v217", "v218", "v219", "v220", "v221", "v222", "v223", "v224", "v225", "v226", "v227",
        "v228", "v229", "v230", "v231", "v232", "v233", "v234", "v235", "v236", "v237", "v238", "v239", "v240", "v241", "v242", "v243", "v244", "v245", "v246", "v247",
        "v248", "v249", "v250", "v251", "v252", "v253", "v254", "v255", "s12", "s13", "s14", "s15", "s16", "s17", "s18", "s19", "s20", "s21", "s22", "s23",
        "s24", "s25", "s26", "s27", "s28", "s29", "s30", "s31", "s33", "s34", "s35", "s36", "s37", "s38", "s39", "s40", "s41", "s42", "s43", "s44",
        "s45", "s46", "s47", "s48", "s49", "s50", "s51", "s52", "s53", "s54", "s55", "s56", "s57", "s58", "s59", "s60", "s61", "s62", "s63", "s64",
        "s65", "s66", "s67", "s68", "s69", "s70", "s71", "s72", "s73", "s74", "s75", "s76", "s77", "s78", "s79",
        "vcc", "scc", "memory");
  const int b = blockIdx.x;
  GETST();
  STAMP(11);
  PEND();
}

extern "C" void kernel_launch(void* const* d_in, const int* in_sizes, int n_in, void* d_out, int out_size,
                              void* d_ws, size_t ws_size, hipStream_t stream) {
  const u16* const* in = (const u16* const*)d_in;
  fused<<<256, 512, 0, stream>>>(in[0], in[1], in[2], in[3], in[4], in[5], in[6], in[8], in[9], in[10], in[11],
                                 (u16*)d_out, (char*)d_ws);
}
```

```cpp
#include <hip/hip_runtime.h>
#include <stdint.h>

typedef unsigned short u16;
typedef unsigned int u32;
typedef unsigned long long u64;
typedef __attribute__((ext_vector_type(4))) u32 u32x4;
typedef __attribute__((ext_vector_type(16))) u32 u32x16;
#define AS3 __attribute__((address_space(3)))

#define STAMP(I)
#define PEND()
#define GETST()
constexpr int SPIN_MAX = 1 << 16;
constexpr int UNUSED = 0;

__global__ __launch_bounds__(512, 2) void fused(const u16* __restrict__ x, const u16* __restrict__ lar,
    const u16* __restrict__ aim, const u16* __restrict__ cre, const u16* __restrict__ cim,
    const u16* __restrict__ ldt, const u16* __restrict__ dsk, const u16* __restrict__ wr,
    const u16* __restrict__ wg, const u16* __restrict__ wu, const u16* __restrict__ wd,
    u16* __restrict__ out, char* ws) {
  __shared__ __attribute__((aligned(16))) char smem_[163840];
  AS3 char* sm = (AS3 char*)smem_;
  u64 ptm = 0, pc0 = 0, pt0 = 0, pte = 0, st_[16] = {};
  u32x16 sts;
  u32x4 st2;
  asm volatile(
      "s_load_dwordx16 s[64:79], %[ka], 0x0\n"
      "s_load_dwordx8 s[80:87], %[ka], 0x40\n"
      "s_load_dwordx2 s[88:89], %[ka], 0x60\n"
      "v_readfirstlane_b32 s33, v0\n"
      "s_getpc_b64 s[50:51]\n"
      ".Lpc%=:\n"
      "s_mov_b32 s34, %[bid]\n"
      "v_and_b32 v43, 63, v0\n"
      "v_mov_b32 v15, 0\n"
      "s_lshr_b32 s33, s33, 6\n"
      "s_and_b32 s52, s34, -8\n"
      "s_add_u32 s52, s52, s33\n"
      "s_min_u32 s52, s52, (.LEnd%=-.Lpc%=)>>7\n"
      "s_lshl_b32 s52, s52, 7\n"
      "s_and_b32 s50, s50, 0xffffff80\n"
      "s_add_u32 s50, s50, s52\n"
      "s_addc_u32 s51, s51, 0\n"
      "global_load_dword v11, v15, s[50:51] \n"
      "s_and_b32 s48, s34, 7\n"
      "s_lshr_b32 s49, s34, 3\n"
      "s_and_b32 s55, s49, 15\n"
      "s_lshl4_add_u32 s35, s48, s55\n"
      "s_cmp_lt_u32 s34, 128\n"
      "s_cbranch_scc1 .LH%=\n"
      "s_lshl3_add_u32 s50, s35, s33\n"
      "s_lshr_b32 s51, s34, 5\n"
      "s_andn2_b32 s51, 4, s51\n"
      "s_add_u32 s50, s50, s51\n"
      "s_lshl_b32 s51, s50, 8\n"
      "s_lshl_b32 s50, s50, 1\n"
      "v_lshl_add_u32 v48, v43, 2, s51\n"
      "v_mov_b32 v49, s50\n"
      "s_waitcnt lgkmcnt(0)\n"
      "global_load_dword v16, v48, s[66:67] nt\n"
      "global_load_dword v17, v48, s[68:69] nt\n"
      "global_load_ushort v12, v49, s[74:75] \n"
      "global_load_ushort v13, v49, s[76:77] \n"
      "global_load_dword v18, v48, s[70:71] nt\n"
      "global_load_dword v19, v48, s[72:73] nt\n"
      "s_load_dword s15, s[88:89], 0x0\n"
      "s_setprio 3\n"
      "s_branch .LC%=\n"
      ".LH%=:\n"
      "s_cmp_ge_u32 s33, 4\n"
      "s_cbranch_scc1 .LG%=\n"
      "s_branch .LD%=\n"
      ".LC%=:\n"
      "s_mov_b32 s16, s88\n"
      "s_and_b32 s17, s89, 0xffff\n"
      "s_mov_b32 s18, 0x33c000\n"
      "s_mov_b32 s19, 0x20000\n"
      "s_mov_b32 s20, s64\n"
      "s_and_b32 s21, s65, 0xffff\n"
      "s_mov_b32 s22, 0x400000\n"
      "s_mov_b32 s23, 0x20000\n"
      "s_mov_b32 s28, s84\n"
      "s_and_b32 s29, s85, 0xffff\n"
      "s_mov_b32 s30, 0x700000\n"
      "s_mov_b32 s31, 0x20000\n"
      "s_mul_i32 s39, s33, 6528\n"
      "s_mul_i32 s40, s33, 8768\n"
      "s_add_u32 s52, s40, 52224\n"
      "s_add_u32 s53, s52, 4160\n"
      "s_lshl_b32 s42, s34, 3\n"
      "s_add_u32 s42, s42, 4096\n"
      "s_lshl_b32 s43, s48, 8\n"
      "s_and_b32 s50, s49, 1\n"
      "s_lshl_b32 s50, s50, 7\n"
      "s_add_u32 s43, s43, s50\n"
      "s_add_u32 s43, s43, 40960\n"
      "s_and_b32 s50, s33, 3\n"
      "s_lshl2_add_u32 s45, s35, s50\n"
      "s_lshl_b32 s45, s45, 12\n"
      "s_add_u32 s45, s45, 1228800\n"
      "v_and_b32 v48, 31, v43\n"
      "v_lshrrev_b32 v49, 5, v43\n"
      "v_lshlrev_b32 v55, 4, v49\n"
      "v_mul_u32_u24 v52, 0x110, v48\n"
      "v_add3_u32 v20, v52, v55, s52\n"
      "v_and_b32 v52, 12, v43\n"
      "s_mov_b32 s50, 0x6600\n"
      "s_mov_b32 s51, 0x6600\n"
      "v_cndmask_b32 v52, v48, v52, s[50:51]\n"
      "s_mov_b32 s50, 0xff000000\n"
      "s_mov_b32 s51, 0xff000000\n"
      "v_cndmask_b32 v52, v52, 9, s[50:51]\n"
      "v_mul_u32_u24 v52, 0x110, v52\n"
      "v_add3_u32 v21, v52, v55, s39\n"
      "v_lshl_add_u32 v52, v43, 2, s39\n"
      "v_mov_b32 v53, 0x3c00\n"
      "ds_write_b32 v52, v53 \n"
      "s_cmp_lt_u32 s34, 128\n"
      "s_cselect_b64 vcc, -1, 0\n"
      "s_cbranch_scc1 .Lsc%=\n"
      "s_mov_b32 s24, s86\n"
      "s_and_b32 s25, s87, 0xffff\n"
      "s_mov_b32 s26, 0x400000\n"
      "s_mov_b32 s27, 0x20000\n"
      "s_mul_i32 s54, s33, 4864\n"
      "s_add_u32 s54, s54, 122368\n"
      "s_lshl_b32 s41, s35, 4\n"
      "s_lshl_b32 s44, s35, 9\n"
      "s_add_u32 s44, s44, 3325952\n"
      "s_mul_i32 s50, s48, 14\n"
      "s_add_u32 s50, s50, s55\n"
      "s_lshr_b32 s51, s50, 2\n"
      "s_lshl4_add_u32 s51, s51, s33\n"
      "s_lshl_b32 s46, s51, 14\n"
      "s_and_b32 s50, s50, 3\n"
      "s_lshl_b32 s50, s50, 9\n"
      "s_add_u32 s46, s46, s50\n"
      "s_cmp_lt_u32 s55, 14\n"
      "s_cselect_b32 s30, s30, 0\n"
      "s_cselect_b32 s47, s46, 0\n"
      "s_add_u32 s46, s47, 0x20000\n"
      "v_bfe_u32 v14, v43, 2, 3\n"
      "v_and_b32 v53, 3, v43\n"
      "v_lshl_add_u32 v14, v14, 4, v53\n"
      "v_lshlrev_b32 v14, 7, v14\n"
      "v_and_b32 v50, 15, v43\n"
      "v_lshrrev_b32 v51, 4, v43\n"
      "v_and_b32 v53, 1, v43\n"
      "s_and_b32 s58, s33, 3\n"
      "s_lshl4_add_u32 s50, s33, s55\n"
      "s_lshl_b32 s50, s50, 14\n"
      "s_and_b32 s51, s55, 8\n"
      "s_lshl_b32 s51, s51, 4\n"
      "s_sub_u32 s50, s50, s51\n"
      "v_bfe_u32 v52, v43, 1, 3\n"
      "v_lshl_add_u32 v52, v52, 4, v53\n"
      "v_lshlrev_b32 v52, 7, v52\n"
      "v_add_u32 v15, s50, v52\n"
      "v_max_i32 v15, 0, v15\n"
      "s_bitcmp1_b32 s55, 0\n"
      "s_cselect_b32 s56, s82, s80\n"
      "s_cselect_b32 s57, s83, s81\n"
      "s_lshr_b32 s50, s56, 7\n"
      "s_sub_u32 s50, s48, s50\n"
      "s_add_u32 s50, s50, 3\n"
      "s_and_b32 s50, s50, 7\n"
      "s_min_u32 s50, s50, 6\n"
      "s_mul_i32 s51, s48, 7\n"
      "s_add_u32 s50, s50, s51\n"
      "s_lshl_b32 s50, s50, 7\n"
      "s_lshr_b32 s51, s55, 1\n"
      "s_mul_i32 s51, s51, 24\n"
      "s_lshl_b32 s58, s58, 8\n"
      "s_add_u32 s51, s51, s58\n"
      "s_add_u32 s51, s51, 64\n"
      "s_mul_i32 s51, s51, 7168\n"
      "s_add_u32 s50, s50, s51\n"
      "v_min_u32 v57, 23, v43\n"
      "v_mul_u32_u24 v57, 0x1c00, v57\n"
      "v_add_u32 v57, s50, v57\n"
      "v_lshl_add_u32 v52, v48, 1, v49\n"
      "v_lshlrev_b32 v54, 3, v52\n"
      "s_add_u32 s50, s53, 570\n"
      "v_sub_u32 v22, s50, v54\n"
      "s_lshl_b32 s50, s33, 17\n"
      "v_lshl_add_u32 v8, v52, 11, s50\n"
      "v_mov_b32 v52, 0x63e\n"
      "v_mov_b32 v54, 0x6de\n"
      "s_mov_b32 s50, 0x55555555\n"
      "s_mov_b32 s51, 0x55555555\n"
      "v_cndmask_b32 v24, v52, v54, s[50:51]\n"
      "v_mov_b32 v52, 0x4de\n"
      "v_mov_b32 v54, 0x59e\n"
      "v_cndmask_b32 v52, v52, v54, s[50:51]\n"
      "s_mov_b32 s58, 0\n"
      "s_mov_b32 s59, -1\n"
      "v_cndmask_b32 v24, v24, v52, s[58:59]\n"
      "v_add_u32 v52, -1, v48\n"
      "v_and_b32 v52, -2, v52\n"
      "v_sub_u32 v24, v24, v52\n"
      "v_add_u32 v24, s53, v24\n"
      "v_add_u32 v23, 32, v24\n"
      "s_add_u32 s60, s53, 2048\n"
      "v_writelane_b32 v23, s60, 0\n"
      "v_writelane_b32 v23, s60, 32\n"
      "v_mul_u32_u24 v52, 0xc1, v53\n"
      "v_sub_u32 v52, v52, v48\n"
      "s_add_u32 s60, s53, 1150\n"
      "v_add_u32 v52, s60, v52\n"
      "v_add_u32 v25, 0xffffffc0, v24\n"
      "v_cndmask_b32 v25, v25, v52, s[58:59]\n"
      "v_and_b32 v52, -2, v43\n"
      "v_add_u32 v52, 58, v52\n"
      "v_mov_b32 v54, 0x810\n"
      "s_mov_b32 s60, 0xfffff000\n"
      "s_mov_b32 s61, 0xffffffff\n"
      "v_cndmask_b32 v52, v52, v54, s[60:61]\n"
      "s_movk_i32 s60, 1408\n"
      "s_movk_i32 s61, 1664\n"
      "v_writelane_b32 v52, s60, 8\n"
      "v_writelane_b32 v52, s60, 9\n"
      "v_writelane_b32 v52, s61, 10\n"
      "v_writelane_b32 v52, s61, 11\n"
      "v_mul_u32_u24 v54, 0x83e, v53\n"
      "v_add3_u32 v26, v52, v54, s53\n"
      "v_lshlrev_b32 v52, 1, v50\n"
      "v_add_u32 v52, v52, v54\n"
      "s_add_u32 s60, s53, 2140\n"
      "v_sub_u32 v52, s60, v52\n"
      "v_lshl_add_u32 v29, v51, 4, v52\n"
      "v_lshl_add_u32 v30, v51, 3, v52\n"
      "v_add_u32 v30, 0x440, v30\n"
      "v_bfe_u32 v52, v43, 2, 3\n"
      "v_max_u32 v54, 5, v52\n"
      "v_mul_u32_u24 v52, 0x60, v52\n"
      "v_lshl_add_u32 v52, v54, 6, v52\n"
      "v_add_u32 v52, 0x2a0, v52\n"
      "s_mov_b32 s60, 0xf\n"
      "s_mov_b32 s61, 0xf\n"
      "v_cndmask_b32 v52, v52, v55, s[60:61]\n"
      "v_mul_u32_u24 v54, 0x830, v49\n"
      "v_add3_u32 v27, v52, v54, s53\n"
      "v_and_b32 v55, 3, v43\n"
      "v_lshl_add_u32 v27, v55, 4, v27\n"
      "s_add_u32 s60, s39, 3104\n"
      "s_add_u32 s61, s52, 2080\n"
      "v_mov_b32 v52, s39\n"
      "v_mov_b32 v54, s60\n"
      "s_mov_b32 s50, 0xff00ff00\n"
      "s_mov_b32 s51, 0xff00ff00\n"
      "v_cndmask_b32 v28, v52, v54, s[50:51]\n"
      "v_mov_b32 v52, s52\n"
      "v_mov_b32 v54, s61\n"
      "v_cndmask_b32 v52, v52, v54, s[50:51]\n"
      "s_mov_b32 s50, 0xffff0000\n"
      "s_mov_b32 s51, 0xffff0000\n"
      "v_cndmask_b32 v28, v28, v52, s[50:51]\n"
      "v_and_b32 v52, 7, v43\n"
      "v_lshl_add_u32 v28, v52, 2, v28\n"
      "v_lshlrev_b32 v9, 3, v52\n"
      "v_and_b32 v52, 8, v43\n"
      "v_lshl_add_u32 v10, v51, 1, v52\n"
      "v_lshl_add_u32 v58, v49, 8, v48\n"
      "s_lshl_b32 s50, s33, 5\n"
      "v_add_u32 v58, s50, v58\n"
      "s_mov_b32 s50, 122912\n"
      "v_lshl_add_u32 v32, v58, 2, s50\n"
      "v_lshlrev_b32 v41, 12, v58\n"
      "v_lshlrev_b32 v42, 4, v58\n"
      "v_lshl_add_u32 v52, v50, 1, v51\n"
      "v_lshl_add_u32 v33, v52, 4, s54\n"
      "s_add_u32 s50, s54, 528\n"
      "v_lshl_add_u32 v34, v43, 4, s50\n"
      "v_lshl_add_u32 v35, v43, 3, s39\n"
      "v_lshl_add_u32 v36, v43, 3, s52\n"
      "s_add_u32 s50, s39, 32\n"
      "v_lshl_add_u32 v37, v43, 4, s50\n"
      "s_add_u32 s50, s52, 32\n"
      "v_lshl_add_u32 v38, v43, 4, s50\n"
      "v_lshl_add_u32 v52, v50, 2, v51\n"
      "v_lshl_add_u32 v39, v52, 2, s53\n"
      "v_bfe_u32 v52, v43, 2, 2\n"
      "v_mul_u32_u24 v52, 0x2240, v52\n"
      "v_lshl_add_u32 v52, v49, 11, v52\n"
      "v_lshl_add_u32 v52, v55, 3, v52\n"
      "v_and_b32 v54, 16, v43\n"
      "v_lshl_add_u32 v52, v54, 1, v52\n"
      "s_lshl_b32 s50, s33, 6\n"
      "s_add_u32 s50, s50, 56384\n"
      "v_add_u32 v40, s50, v52\n"
      "v_mov_b32 v54, 0\n"
      "v_mov_b32 v55, 0\n"
      "v_lshl_add_u32 v52, v43, 3, s54\n"
      "ds_write_b64 v52, v[54:55] offset:32\n"
      "s_mov_b32 s50, 161536\n"
      "v_lshl_add_u32 v52, v0, 2, s50\n"
      "ds_write_b32 v52, v54 \n"
      "v_mov_b32 v52, 0xffff\n"
      "v_mov_b32 v53, 0xffff0000\n"
      "s_mov_b32 s50, 0x0\n"
      "s_mov_b32 s51, 0x18000018\n"
      "v_cndmask_b32 v44, 0, v52, s[50:51]\n"
      "s_mov_b32 s60, 0x0\n"
      "s_mov_b32 s61, 0x30000030\n"
      "v_cndmask_b32 v54, 0, v53, s[60:61]\n"
      "v_or_b32 v44, v44, v54\n"
      "s_mov_b32 s50, 0x0\n"
      "s_mov_b32 s51, 0x60000060\n"
      "v_cndmask_b32 v45, 0, v52, s[50:51]\n"
      "s_mov_b32 s60, 0x0\n"
      "s_mov_b32 s61, 0xc00000c0\n"
      "v_cndmask_b32 v54, 0, v53, s[60:61]\n"
      "v_or_b32 v45, v45, v54\n"
      "s_mov_b32 s50, 0x10000\n"
      "s_mov_b32 s51, 0x80000180\n"
      "v_cndmask_b32 v46, 0, v52, s[50:51]\n"
      "s_mov_b32 s60, 0x30000\n"
      "s_mov_b32 s61, 0x300\n"
      "v_cndmask_b32 v54, 0, v53, s[60:61]\n"
      "v_or_b32 v46, v46, v54\n"
      "s_mov_b32 s50, 0x60000\n"
      "s_mov_b32 s51, 0x600\n"
      "v_cndmask_b32 v47, 0, v52, s[50:51]\n"
      "s_mov_b32 s60, 0xc0000\n"
      "s_mov_b32 s61, 0xc00\n"
      "v_cndmask_b32 v54, 0, v53, s[60:61]\n"
      "v_or_b32 v47, v47, v54\n"
      ".Lsc%=:\n"
      "s_waitcnt vmcnt(0)\n"
      "s_mov_b32 s50, 0x3fb8aa3b\n"
      "buffer_load_dword v11, v15, s[20:23], s41 offen\n"
      "s_cmp_ge_u32 s33, 4\n"
      "s_cbranch_scc1 .LR%=\n"
      "global_load_dword v11, v57, s[56:57]\n"
      ".LR%=:\n"
      "v_lshlrev_b32 v48, 16, v12\n"
      "v_and_b32 v52, 0x7fff7fff, v18\n"
      "v_and_b32 v53, 0x7fff7fff, v19\n"
      "v_mul_f32 v49, s50, v48\n"
      "v_pk_max_u16 v52, v52, v53\n"
      "v_fma_f32 v50, v48, s50, -v49\n"
      "v_rndne_f32 v51, v49\n"
      "v_lshlrev_b32 v53, 16, v52\n"
      "v_fmamk_f32 v50, v48, 0x32a5705f, v50\n"
      "v_sub_f32 v49, v49, v51\n"
      "v_max_u32 v52, v52, v53\n"
      "v_add_f32 v49, v49, v50\n"
      "v_cvt_i32_f32 v51, v51\n"
      "v_max_u32_dpp v52, v52, v52 row_shr:1 row_mask:0xf bank_mask:0xf bound_ctrl:1\n"
      "v_exp_f32 v49, v49\n"
      "v_lshlrev_b32 v54, 16, v13\n"
      "v_max_u32_dpp v52, v52, v52 row_shr:2 row_mask:0xf bank_mask:0xf bound_ctrl:1\n"
      "v_ldexp_f32 v49, v49, v51\n"
      "v_add_f32 v54, 1.0, v54\n"
      "v_max_u32_dpp v52, v52, v52 row_shr:4 row_mask:0xf bank_mask:0xf bound_ctrl:1\n"
      "v_cvt_pk_bf16_f32 v49, v49, v49\n"
      "s_waitcnt lgkmcnt(0)\n"
      "v_max_u32_dpp v52, v52, v52 row_shr:8 row_mask:0xf bank_mask:0xf bound_ctrl:1\n"
      "v_and_b32 v49, 0xffff0000, v49\n"
      "v_and_b32 v55, 0x7fffffff, v54\n"
      "v_max_u32_dpp v52, v52, v52 row_bcast:15 row_mask:0xa bank_mask:0xf\n"
      "v_mul_f32 v50, 0xbfb8aa3b, v49\n"
      "v_mul_f32 v51, 0xbe22f983, v49\n"
      "v_max_u32_dpp v52, v52, v52 row_bcast:31 row_mask:0xc bank_mask:0xf\n"
      "v_readfirstlane_b32 s36, v50\n"
      "v_readlane_b32 s50, v52, 63\n"
      "v_readfirstlane_b32 s37, v51\n"
      "v_mul_f32 v55, 0x3b800000, v55\n"
      "v_mul_f32 v52, s50, v49\n"
      "s_add_u32 s15, s15, 0x9e3779b1\n"
      "v_max_f32 v52, v52, v55\n"
      "v_frexp_exp_i32_f32 v52, v52\n"
      "s_nop 0\n"
      "v_readfirstlane_b32 s50, v52\n"
      "s_nop 0\n"
      "s_sub_i32 s50, 7, s50\n"
      "s_max_i32 s50, s50, -107\n"
      "s_min_i32 s50, s50, 93\n"
      "v_ldexp_f32 v49, v49, s50\n"
      "s_sub_i32 s51, s50, 1\n"
      "v_ldexp_f32 v54, v54, s51\n"
      "s_sub_i32 s14, 128, s50\n"
      "s_lshl_b32 s14, s14, 23\n"
      "v_readfirstlane_b32 s38, v49\n"
      "s_mov_b64 s[60:61], 1\n"
      "v_cndmask_b32 v31, 0, v54, s[60:61]\n"
      "s_lshl_b32 s50, s33, 2\n"
      "s_add_u32 s50, s50, 161280\n"
      "v_mov_b32 v50, s50\n"
      "v_mov_b32 v51, s14\n"
      "ds_write_b32 v50, v51 \n"
      "s_mov_b32 s60, s41\n"
      "s_add_u32 s61, s41, 0x200000\n"
      "s_movk_i32 s62, 8192\n"
      "s_movk_i32 s63, 16384\n"
      "s_mov_b32 m0, s40\n"
      "s_add_u32 s13, s40, 52224\n"
      "s_add_u32 s12, s39, 272\n"
      "v_lshlrev_b32 v72, 16, v16\n"
      "v_and_b32 v73, 0xffff0000, v16\n"
      "v_lshlrev_b32 v74, 16, v17\n"
      "v_and_b32 v75, 0xffff0000, v17\n"
      "s_mov_b32 s64, 0x3fb8aa3b\n"
      "s_mov_b32 s66, 0x43800000\n"
      "s_mov_b32 s68, 0x42000000\n"
      "s_mov_b32 s70, s37\n"
      "v_pk_mul_f32 v[72:73], s[64:65], v[72:73] op_sel_hi:[0,1]\n"
      "v_pk_mul_f32 v[74:75], s[70:71], v[74:75] op_sel_hi:[0,1]\n"
      "v_exp_f32 v72, v72\n"
      "v_exp_f32 v73, v73\n"
      "v_pk_mul_f32 v[78:79], s[66:67], v[74:75] op_sel_hi:[0,1]\n"
      "v_pk_mul_f32 v[80:81], s[68:69], v[74:75] op_sel_hi:[0,1]\n"
      "v_fract_f32 v76, v74\n"
      "v_fract_f32 v77, v75\n"
      "v_cvt_pk_bf16_f32 v72, v72, v73\n"
      "v_cos_f32 v90, v76\n"
      "v_cos_f32 v91, v77\n"
      "v_fract_f32 v78, v78\n"
      "v_fract_f32 v79, v79\n"
      "v_and_b32 v73, 0xffff0000, v72\n"
      "v_lshlrev_b32 v72, 16, v72\n"
      "v_sin_f32 v92, v76\n"
      "v_sin_f32 v93, v77\n"
      "v_pk_mul_f32 v[82:83], s[36:37], v[72:73] op_sel_hi:[0,1]\n"
      "v_fract_f32 v80, v80\n"
      "v_fract_f32 v81, v81\n"
      "v_exp_f32 v84, v82\n"
      "v_exp_f32 v85, v83\n"
      "v_pk_mul_f32 v[86:87], s[66:67], v[82:83] op_sel_hi:[0,1]\n"
      "v_cos_f32 v94, v78\n"
      "v_cos_f32 v95, v79\n"
      "v_pk_mul_f32 v[88:89], s[68:69], v[82:83] op_sel_hi:[0,1]\n"
      "v_sin_f32 v96, v78\n"
      "v_sin_f32 v97, v79\n"
      "v_pk_mul_f32 v[90:91], v[90:91], v[84:85]\n"
      "v_exp_f32 v86, v86\n"
      "v_exp_f32 v87, v87\n"
      "v_pk_mul_f32 v[92:93], v[92:93], v[84:85]\n"
      "v_pk_add_f32 v[82:83], v[90:91], v[92:93]\n"
      "v_cos_f32 v202, v80\n"
      "v_pk_add_f32 v[84:85], v[90:91], v[92:93] neg_lo:[0,1] neg_hi:[0,1]\n"
      "v_cos_f32 v150, v81\n"
      "v_pk_mul_f32 v[100:101], v[90:91], v[92:93]\n"
      "v_pk_mul_f32 v[94:95], v[94:95], v[86:87]\n"
      "v_pk_mul_f32 v[98:99], v[82:83], v[84:85]\n"
      "v_pk_mul_f32 v[96:97], v[96:97], v[86:87]\n"
      "v_pk_add_f32 v[100:101], v[100:101], v[100:101]\n"
      "v_sin_f32 v203, v80\n"
      "v_cvt_pk_f16_f32 v208, v90, v92\n"
      "v_sin_f32 v151, v81\n"
      "v_cvt_pk_f16_f32 v156, v91, v93\n"
      "v_exp_f32 v88, v88\n"
      "v_pk_add_f32 v[82:83], v[98:99], v[100:101]\n"
      "v_exp_f32 v89, v89\n"
      "v_pk_add_f32 v[84:85], v[98:99], v[100:101] neg_lo:[0,1] neg_hi:[0,1]\n"
      "v_pk_add_f32 v[114:115], v[94:95], v[96:97]\n"
      "v_pk_mul_f32 v[104:105], v[98:99], v[100:101]\n"
      "v_pk_add_f32 v[116:117], v[94:95], v[96:97] neg_lo:[0,1] neg_hi:[0,1]\n"
      "v_pk_mul_f32 v[102:103], v[82:83], v[84:85]\n"
      "v_pk_mul_f32 v[108:109], v[94:95], v[96:97]\n"
      "v_pk_add_f32 v[104:105], v[104:105], v[104:105]\n"
      "v_pk_mul_f32 v[106:107], v[114:115], v[116:117]\n"
      "v_cvt_pk_f16_f32 v209, v98, v100\n"
      "v_pk_add_f32 v[108:109], v[108:109], v[108:109]\n"
      "v_cvt_pk_f16_f32 v157, v99, v101\n"
      "v_cvt_pk_f16_f32 v212, v94, v96\n"
      "v_pk_add_f32 v[82:83], v[102:103], v[104:105]\n"
      "v_cvt_pk_f16_f32 v160, v95, v97\n"
      "v_pk_add_f32 v[84:85], v[102:103], v[104:105] neg_lo:[0,1] neg_hi:[0,1]\n"
      "v_pk_mul_f32 v[202:203], v[202:203], v[88:89] op_sel:[0,0] op_sel_hi:[1,0]\n"
      "v_pk_mul_f32 v[92:93], v[102:103], v[104:105]\n"
      "v_pk_mul_f32 v[150:151], v[150:151], v[88:89] op_sel:[0,1] op_sel_hi:[1,1]\n"
      "v_pk_mul_f32 v[90:91], v[82:83], v[84:85]\n"
      "v_pk_mul_f32 v[110:111], v[108:109], v[96:97]\n"
      "v_pk_add_f32 v[92:93], v[92:93], v[92:93]\n"
      "v_pk_mul_f32 v[112:113], v[108:109], v[94:95]\n"
      "v_cvt_pk_f16_f32 v210, v102, v104\n"
      "v_pk_fma_f32 v[110:111], v[106:107], v[94:95], v[110:111] neg_lo:[0,0,1] neg_hi:[0,0,1]\n"
      "v_cvt_pk_f16_f32 v158, v103, v105\n"
      "v_pk_fma_f32 v[112:113], v[106:107], v[96:97], v[112:113]\n"
      "v_pk_add_f32 v[82:83], v[90:91], v[92:93]\n"
      "v_cvt_pk_f16_f32 v213, v106, v108\n"
      "v_pk_add_f32 v[84:85], v[90:91], v[92:93] neg_lo:[0,1] neg_hi:[0,1]\n"
      "v_cvt_pk_f16_f32 v161, v107, v109\n"
      "v_pk_mul_f32 v[74:75], v[90:91], v[92:93]\n"
      "v_pk_add_f32 v[114:115], v[106:107], v[108:109]\n"
      "v_pk_mul_f32 v[72:73], v[82:83], v[84:85]\n"
      "v_pk_add_f32 v[116:117], v[106:107], v[108:109] neg_lo:[0,1] neg_hi:[0,1]\n"
      "v_pk_add_f32 v[74:75], v[74:75], v[74:75]\n"
      "v_pk_mul_f32 v[96:97], v[106:107], v[108:109]\n"
      "v_cvt_pk_f16_f32 v205, v90, v92\n"
      "v_pk_mul_f32 v[94:95], v[114:115], v[116:117]\n"
      "v_cvt_pk_f16_f32 v153, v91, v93\n"
      "v_pk_add_f32 v[96:97], v[96:97], v[96:97]\n"
      "v_pk_mul_f32 v[82:83], v[74:75], v[92:93]\n"
      "v_cvt_pk_f16_f32 v214, v110, v112\n"
      "v_pk_mul_f32 v[84:85], v[74:75], v[90:91]\n"
      "v_cvt_pk_f16_f32 v162, v111, v113\n"
      "v_pk_fma_f32 v[82:83], v[72:73], v[90:91], v[82:83] neg_lo:[0,0,1] neg_hi:[0,0,1]\n"
      "v_pk_mul_f32 v[76:77], v[100:101], v[104:105]\n"
      "v_pk_fma_f32 v[84:85], v[72:73], v[92:93], v[84:85]\n"
      "v_pk_mul_f32 v[78:79], v[100:101], v[102:103]\n"
      "v_cvt_pk_f16_f32 v206, v72, v74\n"
      "v_pk_fma_f32 v[76:77], v[98:99], v[102:103], v[76:77] neg_lo:[0,0,1] neg_hi:[0,0,1]\n"
      "v_cvt_pk_f16_f32 v154, v73, v75\n"
      "v_pk_fma_f32 v[78:79], v[98:99], v[104:105], v[78:79]\n"
      "v_cvt_pk_f16_f32 v207, v82, v84\n"
      "v_cvt_pk_f16_f32 v215, v94, v96\n"
      "v_cvt_pk_f16_f32 v155, v83, v85\n"
      "v_cvt_pk_f16_f32 v163, v95, v97\n"
      "v_cvt_pk_f16_f32 v211, v76, v78\n"
      "v_cvt_pk_f16_f32 v159, v77, v79\n"
      "v_lshlrev_b32 v198, 16, v18\n"
      "v_lshlrev_b32 v199, 16, v19\n"
      "v_pk_mul_f32 v[198:199], s[38:39], v[198:199] op_sel_hi:[0,1]\n"
      "v_and_b32 v146, 0xffff0000, v18\n"
      "v_and_b32 v147, 0xffff0000, v19\n"
      "v_pk_mul_f32 v[146:147], s[38:39], v[146:147] op_sel_hi:[0,1]\n"
      "v_cvt_pk_f16_f32 v240, v198, v199\n"
      "v_pk_mul_f32 v[200:201], v[198:199], v[202:203] op_sel_hi:[1,0]\n"
      "v_pk_mul_f16 v241, v240, v205 op_sel_hi:[0,1] neg_hi:[0,1]\n"
      "v_pk_mul_f16 v242, v240, v206 op_sel_hi:[0,1] neg_hi:[0,1]\n"
      "v_pk_mul_f16 v243, v240, v207 op_sel_hi:[0,1] neg_hi:[0,1]\n"
      "ds_write_addtid_b32 v240 offset:52224\n"
      "v_pk_fma_f32 v[200:201], v[198:199], v[202:203], v[200:201] op_sel:[1,1,0] op_sel_hi:[0,1,1] neg_hi:[0,1,0]\n"
      "v_pk_fma_f16 v241, v240, v205, v241 op_sel:[1,1,0] op_sel_hi:[1,0,1]\n"
      "v_pk_fma_f16 v242, v240, v206, v242 op_sel:[1,1,0] op_sel_hi:[1,0,1]\n"
      "ds_write_addtid_b32 v241 offset:52496\n"
      "v_pk_fma_f16 v243, v240, v207, v243 op_sel:[1,1,0] op_sel_hi:[1,0,1]\n"
      "ds_write_addtid_b32 v242 offset:52768\n"
      "ds_write_addtid_b32 v243 offset:53040\n"
      "v_cvt_pk_f16_f32 v244, v200, v201\n"
      "v_pk_mul_f32 v[198:199], v[200:201], v[202:203] op_sel_hi:[1,0]\n"
      "v_pk_mul_f16 v245, v244, v205 op_sel_hi:[0,1] neg_hi:[0,1]\n"
      "v_pk_mul_f16 v246, v244, v206 op_sel_hi:[0,1] neg_hi:[0,1]\n"
      "v_pk_mul_f16 v247, v244, v207 op_sel_hi:[0,1] neg_hi:[0,1]\n"
      "ds_write_addtid_b32 v244 offset:53312\n"
      "v_pk_fma_f32 v[198:199], v[200:201], v[202:203], v[198:199] op_sel:[1,1,0] op_sel_hi:[0,1,1] neg_hi:[0,1,0]\n"
      "v_pk_fma_f16 v245, v244, v205, v245 op_sel:[1,1,0] op_sel_hi:[1,0,1]\n"
      "v_pk_fma_f16 v246, v244, v206, v246 op_sel:[1,1,0] op_sel_hi:[1,0,1]\n"
      "ds_write_addtid_b32 v245 offset:53584\n"
      "v_pk_fma_f16 v247, v244, v207, v247 op_sel:[1,1,0] op_sel_hi:[1,0,1]\n"
      "ds_write_addtid_b32 v246 offset:53856\n"
      "ds_write_addtid_b32 v247 offset:54128\n"
      "v_cvt_pk_f16_f32 v240, v198, v199\n"
      "v_pk_mul_f32 v[200:201], v[198:199], v[202:203] op_sel_hi:[1,0]\n"
      "v_pk_mul_f16 v241, v240, v205 op_sel_hi:[0,1] neg_hi:[0,1]\n"
      "v_pk_mul_f16 v242, v240, v206 op_sel_hi:[0,1] neg_hi:[0,1]\n"
      "v_pk_mul_f16 v243, v240, v207 op_sel_hi:[0,1] neg_hi:[0,1]\n"
      "ds_write_addtid_b32 v240 offset:54400\n"
      "v_pk_fma_f32 v[200:201], v[198:199], v[202:203], v[200:201] op_sel:[1,1,0] op_sel_hi:[0,1,1] neg_hi:[0,1,0]\n"
      "v_pk_fma_f16 v241, v240, v205, v241 op_sel:[1,1,0] op_sel_hi:[1,0,1]\n"
      "v_pk_fma_f16 v242, v240, v206, v242 op_sel:[1,1,0] op_sel_hi:[1,0,1]\n"
      "ds_write_addtid_b32 v241 offset:54672\n"
      "v_pk_fma_f16 v243, v240, v207, v243 op_sel:[1,1,0] op_sel_hi:[1,0,1]\n"
      "ds_write_addtid_b32 v242 offset:54944\n"
      "ds_write_addtid_b32 v243 offset:55216\n"
      "v_cvt_pk_f16_f32 v244, v200, v201\n"
      "v_pk_mul_f32 v[198:199], v[200:201], v[202:203] op_sel_hi:[1,0]\n"
      "v_pk_mul_f16 v245, v244, v205 op_sel_hi:[0,1] neg_hi:[0,1]\n"
      "v_pk_mul_f16 v246, v244, v206 op_sel_hi:[0,1] neg_hi:[0,1]\n"
      "v_pk_mul_f16 v247, v244, v207 op_sel_hi:[0,1] neg_hi:[0,1]\n"
      "ds_write_addtid_b32 v244 offset:55488\n"
      "v_pk_fma_f32 v[198:199], v[200:201], v[202:203], v[198:199] op_sel:[1,1,0] op_sel_hi:[0,1,1] neg_hi:[0,1,0]\n"
      "v_pk_fma_f16 v245, v244, v205, v245 op_sel:[1,1,0] op_sel_hi:[1,0,1]\n"
      "v_pk_fma_f16 v246, v244, v206, v246 op_sel:[1,1,0] op_sel_hi:[1,0,1]\n"
      "ds_write_addtid_b32 v245 offset:55760\n"
      "v_pk_fma_f16 v247, v244, v207, v247 op_sel:[1,1,0] op_sel_hi:[1,0,1]\n"
      "ds_write_addtid_b32 v246 offset:56032\n"
      "ds_write_addtid_b32 v247 offset:56304\n"
      "v_cvt_pk_f16_f32 v240, v198, v199\n"
      "v_pk_mul_f32 v[200:201], v[198:199], v[202:203] op_sel_hi:[1,0]\n"
      "v_pk_mul_f16 v241, v240, v205 op_sel_hi:[0,1] neg_hi:[0,1]\n"
      "v_pk_mul_f16 v242, v240, v206 op_sel_hi:[0,1] neg_hi:[0,1]\n"
      "v_pk_mul_f16 v243, v240, v207 op_sel_hi:[0,1] neg_hi:[0,1]\n"
      "ds_write_addtid_b32 v240 offset:56576\n"
      "v_pk_fma_f32 v[200:201], v[198:199], v[202:203], v[200:201] op_sel:[1,1,0] op_sel_hi:[0,1,1] neg_hi:[0,1,0]\n"
      "v_pk_fma_f16 v241, v240, v205, v241 op_sel:[1,1,0] op_sel_hi:[1,0,1]\n"
      "v_pk_fma_f16 v242, v240, v206, v242 op_sel:[1,1,0] op_sel_hi:[1,0,1]\n"
      "ds_write_addtid_b32 v241 offset:56848\n"
      "v_pk_fma_f16 v243, v240, v207, v243 op_sel:[1,1,0] op_sel_hi:[1,0,1]\n"
      "ds_write_addtid_b32 v242 offset:57120\n"
      "ds_write_addtid_b32 v243 offset:57392\n"
      "v_cvt_pk_f16_f32 v244, v200, v201\n"
      "v_pk_mul_f32 v[198:199], v[200:201], v[202:203] op_sel_hi:[1,0]\n"
      "v_pk_mul_f16 v245, v244, v205 op_sel_hi:[0,1] neg_hi:[0,1]\n"
      "v_pk_mul_f16 v246, v244, v206 op_sel_hi:[0,1] neg_hi:[0,1]\n"
      "v_pk_mul_f16 v247, v244, v207 op_sel_hi:[0,1] neg_hi:[0,1]\n"
      "ds_write_addtid_b32 v244 offset:57664\n"
      "v_pk_fma_f32 v[198:199], v[200:201], v[202:203], v[198:199] op_sel:[1,1,0] op_sel_hi:[0,1,1] neg_hi:[0,1,0]\n"
      "v_pk_fma_f16 v245, v244, v205, v245 op_sel:[1,1,0] op_sel_hi:[1,0,1]\n"
      "v_pk_fma_f16 v246, v244, v206, v246 op_sel:[1,1,0] op_sel_hi:[1,0,1]\n"
      "ds_write_addtid_b32 v245 offset:57936\n"
      "v_pk_fma_f16 v247, v244, v207, v247 op_sel:[1,1,0] op_sel_hi:[1,0,1]\n"
      "ds_write_addtid_b32 v246 offset:58208\n"
      "ds_write_addtid_b32 v247 offset:58480\n"
      "v_cvt_pk_f16_f32 v240, v198, v199\n"
      "v_pk_mul_f32 v[200:201], v[198:199], v[202:203] op_sel_hi:[1,0]\n"
      "v_pk_mul_f16 v241, v240, v205 op_sel_hi:[0,1] neg_hi:[0,1]\n"
      "v_pk_mul_f16 v242, v240, v206 op_sel_hi:[0,1] neg_hi:[0,1]\n"
      "v_pk_mul_f16 v243, v240, v207 op_sel_hi:[0,1] neg_hi:[0,1]\n"
      "ds_write_addtid_b32 v240 offset:58752\n"
      "v_pk_fma_f32 v[200:201], v[198:199], v[202:203], v[200:201] op_sel:[1,1,0] op_sel_hi:[0,1,1] neg_hi:[0,1,0]\n"
      "v_pk_fma_f16 v241, v240, v205, v241 op_sel:[1,1,0] op_sel_hi:[1,0,1]\n"
      "v_pk_fma_f16 v242, v240, v206, v242 op_sel:[1,1,0] op_sel_hi:[1,0,1]\n"
      "ds_write_addtid_b32 v241 offset:59024\n"
      "v_pk_fma_f16 v243, v240, v207, v243 op_sel:[1,1,0] op_sel_hi:[1,0,1]\n"
      "ds_write_addtid_b32 v242 offset:59296\n"
      "ds_write_addtid_b32 v243 offset:59568\n"
      "v_cvt_pk_f16_f32 v244, v200, v201\n"
      "v_pk_mul_f16 v245, v244, v205 op_sel_hi:[0,1] neg_hi:[0,1]\n"
      "v_pk_mul_f16 v246, v244, v206 op_sel_hi:[0,1] neg_hi:[0,1]\n"
      "v_pk_mul_f16 v247, v244, v207 op_sel_hi:[0,1] neg_hi:[0,1]\n"
      "ds_write_addtid_b32 v244 offset:59840\n"
      "v_pk_fma_f16 v245, v244, v205, v245 op_sel:[1,1,0] op_sel_hi:[1,0,1]\n"
      "v_pk_fma_f16 v246, v244, v206, v246 op_sel:[1,1,0] op_sel_hi:[1,0,1]\n"
      "ds_write_addtid_b32 v245 offset:60112\n"
      "v_pk_fma_f16 v247, v244, v207, v247 op_sel:[1,1,0] op_sel_hi:[1,0,1]\n"
      "ds_write_addtid_b32 v246 offset:60384\n"
      "ds_write_addtid_b32 v247 offset:60656\n"
      "s_mov_b32 m0, s39\n"
      "s_setprio 2\n"
      "v_pk_mul_f16 v240, v209, v208 op_sel_hi:[0,1]\n"
      "v_pk_mul_f16 v241, v210, v208 op_sel_hi:[0,1]\n"
      "v_pk_mul_f16 v242, v211, v208 op_sel_hi:[0,1]\n"
      "ds_write_addtid_b32 v208 offset:272\n"
      "v_pk_fma_f16 v240, v209, v208, v240 op_sel:[1,1,0] op_sel_hi:[1,0,1] neg_lo:[0,1,0]\n"
      "ds_write_addtid_b32 v209 offset:544\n"
      "v_pk_fma_f16 v241, v210, v208, v241 op_sel:[1,1,0] op_sel_hi:[1,0,1] neg_lo:[0,1,0]\n"
      "ds_write_addtid_b32 v240 offset:816\n"
      "v_pk_fma_f16 v242, v211, v208, v242 op_sel:[1,1,0] op_sel_hi:[1,0,1] neg_lo:[0,1,0]\n"
      "ds_write_addtid_b32 v241 offset:1360\n"
      "ds_write_addtid_b32 v242 offset:1904\n"
      "v_pk_mul_f16 v243, v212, v209 op_sel_hi:[0,1]\n"
      "v_pk_mul_f16 v244, v212, v210 op_sel_hi:[0,1]\n"
      "v_pk_mul_f16 v245, v212, v211 op_sel_hi:[0,1]\n"
      "ds_write_addtid_b32 v210 offset:1088\n"
      "v_pk_fma_f16 v243, v212, v209, v243 op_sel:[1,1,0] op_sel_hi:[1,0,1] neg_lo:[0,1,0]\n"
      "ds_write_addtid_b32 v211 offset:1632\n"
      "v_pk_fma_f16 v244, v212, v210, v244 op_sel:[1,1,0] op_sel_hi:[1,0,1] neg_lo:[0,1,0]\n"
      "ds_write_addtid_b32 v243 offset:2992\n"
      "v_pk_fma_f16 v245, v212, v211, v245 op_sel:[1,1,0] op_sel_hi:[1,0,1] neg_lo:[0,1,0]\n"
      "ds_write_addtid_b32 v244 offset:3264\n"
      "ds_write_addtid_b32 v245 offset:4080\n"
      "v_pk_mul_f16 v246, v213, v210 op_sel_hi:[0,1]\n"
      "v_pk_mul_f16 v247, v214, v210 op_sel_hi:[0,1]\n"
      "v_pk_mul_f16 v248, v215, v215 op_sel_hi:[0,1]\n"
      "ds_write_addtid_b32 v212 offset:2176\n"
      "v_pk_fma_f16 v246, v213, v210, v246 op_sel:[1,1,0] op_sel_hi:[1,0,1] neg_lo:[0,1,0]\n"
      "ds_write_addtid_b32 v213 offset:5440\n"
      "v_pk_fma_f16 v247, v214, v210, v247 op_sel:[1,1,0] op_sel_hi:[1,0,1] neg_lo:[0,1,0]\n"
      "ds_write_addtid_b32 v246 offset:5984\n"
      "v_pk_fma_f16 v248, v215, v215, v248 op_sel:[1,1,0] op_sel_hi:[1,0,1] neg_lo:[0,1,0]\n"
      "ds_write_addtid_b32 v247 offset:6256\n"
      "ds_write_addtid_b32 v248 offset:2448\n"
      "v_pk_mul_f16 v249, v215, v212 op_sel_hi:[0,1]\n"
      "v_pk_mul_f16 v250, v215, v213 op_sel_hi:[0,1]\n"
      "v_pk_mul_f16 v251, v215, v214 op_sel_hi:[0,1]\n"
      "ds_write_addtid_b32 v214 offset:5712\n"
      "v_pk_fma_f16 v249, v215, v212, v249 op_sel:[1,1,0] op_sel_hi:[1,0,1] neg_lo:[0,1,0]\n"
      "ds_write_addtid_b32 v215 offset:4352\n"
      "v_pk_fma_f16 v250, v215, v213, v250 op_sel:[1,1,0] op_sel_hi:[1,0,1] neg_lo:[0,1,0]\n"
      "ds_write_addtid_b32 v249 offset:4624\n"
      "v_pk_fma_f16 v251, v215, v214, v251 op_sel:[1,1,0] op_sel_hi:[1,0,1] neg_lo:[0,1,0]\n"
      "ds_write_addtid_b32 v250 offset:4896\n"
      "ds_write_addtid_b32 v251 offset:5168\n"
      "ds_read_b128 v[72:75], v21 offset:0\n"
      "ds_read_b128 v[104:107], v20 offset:0\n"
      "ds_read_b128 v[76:79], v21 offset:32\n"
      "ds_read_b128 v[108:111], v20 offset:32\n"
      "ds_read_b128 v[80:83], v21 offset:64\n"
      "ds_read_b128 v[112:115], v20 offset:64\n"
      "ds_read_b128 v[84:87], v21 offset:96\n"
      "ds_read_b128 v[116:119], v20 offset:96\n"
      "ds_read_b128 v[88:91], v21 offset:128\n"
      "ds_read_b128 v[120:123], v20 offset:128\n"
      "ds_read_b128 v[92:95], v21 offset:160\n"
      "ds_read_b128 v[124:127], v20 offset:160\n"
      "ds_read_b128 v[96:99], v21 offset:192\n"
      "ds_read_b128 v[128:131], v20 offset:192\n"
      "ds_read_b128 v[100:103], v21 offset:224\n"
      "ds_read_b128 v[132:135], v20 offset:224\n"
      "s_mov_b32 m0, s40\n"
      "buffer_load_dwordx4 v[48:51], v41, s[20:23], s60 offen nt\n"
      "s_waitcnt lgkmcnt(14)\n"
      "v_mfma_f32_32x32x16_f16 v[180:195], v[72:75], v[104:107], 0\n"
      "v_cvt_pk_f16_f32 v240, v146, v147\n"
      "v_pk_mul_f32 v[148:149], v[146:147], v[150:151] op_sel_hi:[1,0]\n"
      "v_pk_mul_f16 v241, v240, v153 op_sel_hi:[0,1] neg_hi:[0,1]\n"
      "v_pk_mul_f16 v242, v240, v154 op_sel_hi:[0,1] neg_hi:[0,1]\n"
      "v_pk_mul_f16 v243, v240, v155 op_sel_hi:[0,1] neg_hi:[0,1]\n"
      "ds_write_addtid_b32 v240 offset:52224\n"
      "v_pk_fma_f32 v[148:149], v[146:147], v[150:151], v[148:149] op_sel:[1,1,0] op_sel_hi:[0,1,1] neg_hi:[0,1,0]\n"
      "v_pk_fma_f16 v241, v240, v153, v241 op_sel:[1,1,0] op_sel_hi:[1,0,1]\n"
      "v_pk_fma_f16 v242, v240, v154, v242 op_sel:[1,1,0] op_sel_hi:[1,0,1]\n"
      "ds_write_addtid_b32 v241 offset:52496\n"
      "v_pk_fma_f16 v243, v240, v155, v243 op_sel:[1,1,0] op_sel_hi:[1,0,1]\n"
      "ds_write_addtid_b32 v242 offset:52768\n"
      "ds_write_addtid_b32 v243 offset:53040\n"
      "s_waitcnt lgkmcnt(15)\n"
      "v_mfma_f32_32x32x16_f16 v[180:195], v[76:79], v[108:111], v[180:195]\n"
      "v_cvt_pk_f16_f32 v244, v148, v149\n"
      "v_pk_mul_f32 v[146:147], v[148:149], v[150:151] op_sel_hi:[1,0]\n"
      "v_pk_mul_f16 v245, v244, v153 op_sel_hi:[0,1] neg_hi:[0,1]\n"
      "v_pk_mul_f16 v246, v244, v154 op_sel_hi:[0,1] neg_hi:[0,1]\n"
      "v_pk_mul_f16 v247, v244, v155 op_sel_hi:[0,1] neg_hi:[0,1]\n"
      "ds_write_addtid_b32 v244 offset:53312\n"
      "v_pk_fma_f32 v[146:147], v[148:149], v[150:151], v[146:147] op_sel:[1,1,0] op_sel_hi:[0,1,1] neg_hi:[0,1,0]\n"
      "v_pk_fma_f16 v245, v244, v153, v245 op_sel:[1,1,0] op_sel_hi:[1,0,1]\n"
      "v_pk_fma_f16 v246, v244, v154, v246 op_sel:[1,1,0] op_sel_hi:[1,0,1]\n"
      "ds_write_addtid_b32 v245 offset:53584\n"
      "v_pk_fma_f16 v247, v244, v155, v247 op_sel:[1,1,0] op_sel_hi:[1,0,1]\n"
      "ds_write_addtid_b32 v246 offset:53856\n"
      "ds_write_addtid_b32 v247 offset:54128\n"
      "s_waitcnt lgkmcnt(15)\n"
      "v_mfma_f32_32x32x16_f16 v[180:195], v[80:83], v[112:115], v[180:195]\n"
      "v_cvt_pk_f16_f32 v240, v146, v147\n"
      "v_pk_mul_f32 v[148:149], v[146:147], v[150:151] op_sel_hi:[1,0]\n"
      "v_pk_mul_f16 v241, v240, v153 op_sel_hi:[0,1] neg_hi:[0,1]\n"
      "v_pk_mul_f16 v242, v240, v154 op_sel_hi:[0,1] neg_hi:[0,1]\n"
      "v_pk_mul_f16 v243, v240, v155 op_sel_hi:[0,1] neg_hi:[0,1]\n"
      "ds_write_addtid_b32 v240 offset:54400\n"
      "v_pk_fma_f32 v[148:149], v[146:147], v[150:151], v[148:149] op_sel:[1,1,0] op_sel_hi:[0,1,1] neg_hi:[0,1,0]\n"
      "v_pk_fma_f16 v241, v240, v153, v241 op_sel:[1,1,0] op_sel_hi:[1,0,1]\n"
      "v_pk_fma_f16 v242, v240, v154, v242 op_sel:[1,1,0] op_sel_hi:[1,0,1]\n"
      "ds_write_addtid_b32 v241 offset:54672\n"
      "v_pk_fma_f16 v243, v240, v155, v243 op_sel:[1,1,0] op_sel_hi:[1,0,1]\n"
      "ds_write_addtid_b32 v242 offset:54944\n"
      "ds_write_addtid_b32 v243 offset:55216\n"
      "buffer_load_dword v11, v15, s[20:23], s61 offen\n"
      "s_waitcnt lgkmcnt(15)\n"
      "v_mfma_f32_32x32x16_f16 v[180:195], v[84:87], v[116:119], v[180:195]\n"
      "v_cvt_pk_f16_f32 v244, v148, v149\n"
      "v_pk_mul_f32 v[146:147], v[148:149], v[150:151] op_sel_hi:[1,0]\n"
      "v_pk_mul_f16 v245, v244, v153 op_sel_hi:[0,1] neg_hi:[0,1]\n"
      "v_pk_mul_f16 v246, v244, v154 op_sel_hi:[0,1] neg_hi:[0,1]\n"
      "v_pk_mul_f16 v247, v244, v155 op_sel_hi:[0,1] neg_hi:[0,1]\n"
      "ds_write_addtid_b32 v244 offset:55488\n"
      "v_pk_fma_f32 v[146:147], v[148:149], v[150:151], v[146:147] op_sel:[1,1,0] op_sel_hi:[0,1,1] neg_hi:[0,1,0]\n"
      "v_pk_fma_f16 v245, v244, v153, v245 op_sel:[1,1,0] op_sel_hi:[1,0,1]\n"
      "v_pk_fma_f16 v246, v244, v154, v246 op_sel:[1,1,0] op_sel_hi:[1,0,1]\n"
      "ds_write_addtid_b32 v245 offset:55760\n"
      "v_pk_fma_f16 v247, v244, v155, v247 op_sel:[1,1,0] op_sel_hi:[1,0,1]\n"
      "ds_write_addtid_b32 v246 offset:56032\n"
      "ds_write_addtid_b32 v247 offset:56304\n"
      "s_waitcnt lgkmcnt(15)\n"
      "v_mfma_f32_32x32x16_f16 v[180:195], v[88:91], v[120:123], v[180:195]\n"
      "v_cvt_pk_f16_f32 v240, v146, v147\n"
      "v_pk_mul_f32 v[148:149], v[146:147], v[150:151] op_sel_hi:[1,0]\n"
      "v_pk_mul_f16 v241, v240, v153 op_sel_hi:[0,1] neg_hi:[0,1]\n"
      "v_pk_mul_f16 v242, v240, v154 op_sel_hi:[0,1] neg_hi:[0,1]\n"
      "v_pk_mul_f16 v243, v240, v155 op_sel_hi:[0,1] neg_hi:[0,1]\n"
      "ds_write_addtid_b32 v240 offset:56576\n"
      "v_pk_fma_f32 v[148:149], v[146:147], v[150:151], v[148:149] op_sel:[1,1,0] op_sel_hi:[0,1,1] neg_hi:[0,1,0]\n"
      "v_pk_fma_f16 v241, v240, v153, v241 op_sel:[1,1,0] op_sel_hi:[1,0,1]\n"
      "v_pk_fma_f16 v242, v240, v154, v242 op_sel:[1,1,0] op_sel_hi:[1,0,1]\n"
      "ds_write_addtid_b32 v241 offset:56848\n"
      "v_pk_fma_f16 v243, v240, v155, v243 op_sel:[1,1,0] op_sel_hi:[1,0,1]\n"
      "ds_write_addtid_b32 v242 offset:57120\n"
      "ds_write_addtid_b32 v243 offset:57392\n"
      "buffer_load_dwordx4 v[52:55], v41, s[20:23], s60 offen offset:2048 nt\n"
      "s_waitcnt lgkmcnt(15)\n"
      "v_mfma_f32_32x32x16_f16 v[180:195], v[92:95], v[124:127], v[180:195]\n"
      "v_cvt_pk_f16_f32 v244, v148, v149\n"
      "v_pk_mul_f32 v[146:147], v[148:149], v[150:151] op_sel_hi:[1,0]\n"
      "v_pk_mul_f16 v245, v244, v153 op_sel_hi:[0,1] neg_hi:[0,1]\n"
      "v_pk_mul_f16 v246, v244, v154 op_sel_hi:[0,1] neg_hi:[0,1]\n"
      "v_pk_mul_f16 v247, v244, v155 op_sel_hi:[0,1] neg_hi:[0,1]\n"
      "ds_write_addtid_b32 v244 offset:57664\n"
      "v_pk_fma_f32 v[146:147], v[148:149], v[150:151], v[146:147] op_sel:[1,1,0] op_sel_hi:[0,1,1] neg_hi:[0,1,0]\n"
      "v_pk_fma_f16 v245, v244, v153, v245 op_sel:[1,1,0] op_sel_hi:[1,0,1]\n"
      "v_pk_fma_f16 v246, v244, v154, v246 op_sel:[1,1,0] op_sel_hi:[1,0,1]\n"
      "ds_write_addtid_b32 v245 offset:57936\n"
      "v_pk_fma_f16 v247, v244, v155, v247 op_sel:[1,1,0] op_sel_hi:[1,0,1]\n"
      "ds_write_addtid_b32 v246 offset:58208\n"
      "ds_write_addtid_b32 v247 offset:58480\n"
      "s_waitcnt lgkmcnt(15)\n"
      "v_mfma_f32_32x32x16_f16 v[180:195], v[96:99], v[128:131], v[180:195]\n"
      "v_cvt_pk_f16_f32 v240, v146, v147\n"
      "v_pk_mul_f32 v[148:149], v[146:147], v[150:151] op_sel_hi:[1,0]\n"
      "v_pk_mul_f16 v241, v240, v153 op_sel_hi:[0,1] neg_hi:[0,1]\n"
      "v_pk_mul_f16 v242, v240, v154 op_sel_hi:[0,1] neg_hi:[0,1]\n"
      "v_pk_mul_f16 v243, v240, v155 op_sel_hi:[0,1] neg_hi:[0,1]\n"
      "ds_write_addtid_b32 v240 offset:58752\n"
      "v_pk_fma_f32 v[148:149], v[146:147], v[150:151], v[148:149] op_sel:[1,1,0] op_sel_hi:[0,1,1] neg_hi:[0,1,0]\n"
      "v_pk_fma_f16 v241, v240, v153, v241 op_sel:[1,1,0] op_sel_hi:[1,0,1]\n"
      "v_pk_fma_f16 v242, v240, v154, v242 op_sel:[1,1,0] op_sel_hi:[1,0,1]\n"
      "ds_write_addtid_b32 v241 offset:59024\n"
      "v_pk_fma_f16 v243, v240, v155, v243 op_sel:[1,1,0] op_sel_hi:[1,0,1]\n"
      "ds_write_addtid_b32 v242 offset:59296\n"
      "ds_write_addtid_b32 v243 offset:59568\n"
      "s_waitcnt lgkmcnt(15)\n"
      "v_mfma_f32_32x32x16_f16 v[180:195], v[100:103], v[132:135], v[180:195]\n"
      "v_cvt_pk_f16_f32 v244, v148, v149\n"
      "v_pk_mul_f16 v245, v244, v153 op_sel_hi:[0,1] neg_hi:[0,1]\n"
      "v_pk_mul_f16 v246, v244, v154 op_sel_hi:[0,1] neg_hi:[0,1]\n"
      "v_pk_mul_f16 v247, v244, v155 op_sel_hi:[0,1] neg_hi:[0,1]\n"
      "ds_write_addtid_b32 v244 offset:59840\n"
      "v_pk_fma_f16 v245, v244, v153, v245 op_sel:[1,1,0] op_sel_hi:[1,0,1]\n"
      "v_pk_fma_f16 v246, v244, v154, v246 op_sel:[1,1,0] op_sel_hi:[1,0,1]\n"
      "ds_write_addtid_b32 v245 offset:60112\n"
      "v_pk_fma_f16 v247, v244, v155, v247 op_sel:[1,1,0] op_sel_hi:[1,0,1]\n"
      "ds_write_addtid_b32 v246 offset:60384\n"
      "ds_write_addtid_b32 v247 offset:60656\n"
      "s_mov_b32 m0, s39\n"
      "s_setprio 1\n"
      "v_pk_mul_f16 v240, v157, v156 op_sel_hi:[0,1]\n"
      "v_pk_mul_f16 v241, v158, v156 op_sel_hi:[0,1]\n"
      "v_pk_mul_f16 v242, v159, v156 op_sel_hi:[0,1]\n"
      "ds_write_addtid_b32 v156 offset:272\n"
      "v_pk_fma_f16 v240, v157, v156, v240 op_sel:[1,1,0] op_sel_hi:[1,0,1] neg_lo:[0,1,0]\n"
      "ds_write_addtid_b32 v157 offset:544\n"
      "v_pk_fma_f16 v241, v158, v156, v241 op_sel:[1,1,0] op_sel_hi:[1,0,1] neg_lo:[0,1,0]\n"
      "ds_write_addtid_b32 v240 offset:816\n"
      "v_pk_fma_f16 v242, v159, v156, v242 op_sel:[1,1,0] op_sel_hi:[1,0,1] neg_lo:[0,1,0]\n"
      "ds_write_addtid_b32 v241 offset:1360\n"
      "ds_write_addtid_b32 v242 offset:1904\n"
      "v_pk_mul_f16 v243, v160, v157 op_sel_hi:[0,1]\n"
      "v_pk_mul_f16 v244, v160, v158 op_sel_hi:[0,1]\n"
      "v_pk_mul_f16 v245, v160, v159 op_sel_hi:[0,1]\n"
      "ds_write_addtid_b32 v158 offset:1088\n"
      "v_pk_fma_f16 v243, v160, v157, v243 op_sel:[1,1,0] op_sel_hi:[1,0,1] neg_lo:[0,1,0]\n"
      "ds_write_addtid_b32 v159 offset:1632\n"
      "v_pk_fma_f16 v244, v160, v158, v244 op_sel:[1,1,0] op_sel_hi:[1,0,1] neg_lo:[0,1,0]\n"
      "ds_write_addtid_b32 v243 offset:2992\n"
      "v_pk_fma_f16 v245, v160, v159, v245 op_sel:[1,1,0] op_sel_hi:[1,0,1] neg_lo:[0,1,0]\n"
      "ds_write_addtid_b32 v244 offset:3264\n"
      "ds_write_addtid_b32 v245 offset:4080\n"
      "v_pk_mul_f16 v246, v161, v158 op_sel_hi:[0,1]\n"
      "v_pk_mul_f16 v247, v162, v158 op_sel_hi:[0,1]\n"
      "v_pk_mul_f16 v248, v163, v163 op_sel_hi:[0,1]\n"
      "ds_write_addtid_b32 v160 offset:2176\n"
      "v_pk_fma_f16 v246, v161, v158, v246 op_sel:[1,1,0] op_sel_hi:[1,0,1] neg_lo:[0,1,0]\n"
      "ds_write_addtid_b32 v161 offset:5440\n"
      "v_pk_fma_f16 v247, v162, v158, v247 op_sel:[1,1,0] op_sel_hi:[1,0,1] neg_lo:[0,1,0]\n"
      "ds_write_addtid_b32 v246 offset:5984\n"
      "v_pk_fma_f16 v248, v163, v163, v248 op_sel:[1,1,0] op_sel_hi:[1,0,1] neg_lo:[0,1,0]\n"
      "ds_write_addtid_b32 v247 offset:6256\n"
      "ds_write_addtid_b32 v248 offset:2448\n"
      "v_pk_mul_f16 v249, v163, v160 op_sel_hi:[0,1]\n"
      "v_pk_mul_f16 v250, v163, v161 op_sel_hi:[0,1]\n"
      "v_pk_mul_f16 v251, v163, v162 op_sel_hi:[0,1]\n"
      "ds_write_addtid_b32 v162 offset:5712\n"
      "v_pk_fma_f16 v249, v163, v160, v249 op_sel:[1,1,0] op_sel_hi:[1,0,1] neg_lo:[0,1,0]\n"
      "ds_write_addtid_b32 v163 offset:4352\n"
      "v_pk_fma_f16 v250, v163, v161, v250 op_sel:[1,1,0] op_sel_hi:[1,0,1] neg_lo:[0,1,0]\n"
      "ds_write_addtid_b32 v249 offset:4624\n"
      "v_pk_fma_f16 v251, v163, v162, v251 op_sel:[1,1,0] op_sel_hi:[1,0,1] neg_lo:[0,1,0]\n"
      "ds_write_addtid_b32 v250 offset:4896\n"
      "ds_write_addtid_b32 v251 offset:5168\n"
      "buffer_load_dwordx4 v[64:67], v42, s[16:19], s62 offen sc1\n"
      "buffer_load_dwordx4 v[68:71], v42, s[16:19], s63 offen sc1\n"
      "v_mov_b32 v140, 0\n"
      "v_mov_b32 v141, 0\n"
      "v_mov_b32 v142, 0\n"
      "v_mov_b32 v143, 0\n"
      "ds_read_b128 v[72:75], v21 offset:0\n"
      "ds_read_b128 v[104:107], v20 offset:0\n"
      "ds_read_b128 v[76:79], v21 offset:32\n"
      "ds_read_b128 v[108:111], v20 offset:32\n"
      "ds_read_b128 v[80:83], v21 offset:64\n"
      "ds_read_b128 v[112:115], v20 offset:64\n"
      "ds_read_b128 v[84:87], v21 offset:96\n"
      "ds_read_b128 v[116:119], v20 offset:96\n"
      "ds_read_b128 v[88:91], v21 offset:128\n"
      "ds_read_b128 v[120:123], v20 offset:128\n"
      "ds_read_b128 v[92:95], v21 offset:160\n"
      "ds_read_b128 v[124:127], v20 offset:160\n"
      "ds_read_b128 v[96:99], v21 offset:192\n"
      "ds_read_b128 v[128:131], v20 offset:192\n"
      "ds_read_b128 v[100:103], v21 offset:224\n"
      "ds_read_b128 v[132:135], v20 offset:224\n"
      "ds_write_b128 v27, v[140:143] \n"
      "ds_write_b32 v28, v140 \n"
      "s_waitcnt lgkmcnt(14)\n"
      "v_mfma_f32_32x32x16_f16 v[180:195], v[72:75], v[104:107], v[180:195]\n"
      "s_waitcnt lgkmcnt(12)\n"
      "v_mfma_f32_32x32x16_f16 v[180:195], v[76:79], v[108:111], v[180:195]\n"
      "s_waitcnt lgkmcnt(10)\n"
      "v_mfma_f32_32x32x16_f16 v[180:195], v[80:83], v[112:115], v[180:195]\n"
      "s_waitcnt lgkmcnt(8)\n"
      "v_mfma_f32_32x32x16_f16 v[180:195], v[84:87], v[116:119], v[180:195]\n"
      "s_waitcnt lgkmcnt(6)\n"
      "v_mfma_f32_32x32x16_f16 v[180:195], v[88:91], v[120:123], v[180:195]\n"
      "s_waitcnt lgkmcnt(4)\n"
      "v_mfma_f32_32x32x16_f16 v[180:195], v[92:95], v[124:127], v[180:195]\n"
      "s_waitcnt lgkmcnt(2)\n"
      "v_mfma_f32_32x32x16_f16 v[180:195], v[96:99], v[128:131], v[180:195]\n"
      "s_waitcnt lgkmcnt(0)\n"
      "v_mfma_f32_32x32x16_f16 v[180:195], v[100:103], v[132:135], v[180:195]\n"
      "s_nop 11\n"
      "v_add_f32 v180, v180, v31\n"
      "v_cvt_pk_f16_f32 v222, v187, v186\n"
      "v_cvt_pk_f16_f32 v223, v185, v184\n"
      "v_cvt_pk_f16_f32 v225, v186, v185\n"
      "v_cvt_pk_f16_f32 v226, v188, v189\n"
      "ds_write_b64 v22, v[222:223] offset:2110\n"
      "v_cvt_pk_f16_f32 v228, v192, v192\n"
      "ds_write_b16 v22, v222 \n"
      "v_cvt_pk_f16_f32 v220, v183, v182\n"
      "ds_write_b32 v22, v225 offset:2\n"
      "v_cvt_pk_f16_f32 v221, v181, v180\n"
      "ds_write_b16_d16_hi v22, v223 offset:6\n"
      "v_cvt_pk_f16_f32 v224, v182, v181\n"
      "v_readlane_b32 s64, v226, 0\n"
      "v_readlane_b32 s65, v226, 32\n"
      "v_readlane_b32 s66, v228, 0\n"
      "ds_write_b64 v22, v[220:221] offset:510\n"
      "v_cvt_pk_f16_f32 v227, v190, v191\n"
      "ds_write_b16 v22, v220 offset:2620\n"
      "v_mov_b32 v229, s65\n"
      "ds_write_b32 v22, v224 offset:2622\n"
      "v_writelane_b32 v229, s64, 8\n"
      "v_writelane_b32 v229, s64, 9\n"
      "ds_write_b16_d16_hi v22, v221 offset:2626\n"
      "v_writelane_b32 v229, s66, 10\n"
      "v_writelane_b32 v229, s66, 11\n"
      "s_pack_ll_b32_b16 s64, s64, s64\n"
      "ds_write_b16 v26, v229 \n"
      "v_add_u32 v230, 0x240, v29\n"
      "s_setprio 0\n"
      "v_and_b32 v44, s64, v44\n"
      "v_and_b32 v45, s64, v45\n"
      "v_and_b32 v46, s64, v46\n"
      "v_and_b32 v47, s64, v47\n"
      "ds_write_b16 v23, v226 \n"
      "ds_write_b16 v23, v226 offset:2110\n"
      "ds_write_b16_d16_hi v24, v226 \n"
      "ds_write_b16_d16_hi v24, v226 offset:2110\n"
      "ds_write_b16 v25, v227 offset:32\n"
      "ds_write_b16 v25, v227 offset:2142\n"
      "ds_write_b16_d16_hi v25, v227 \n"
      "ds_write_b16_d16_hi v25, v227 offset:2110\n"
      "ds_read2_b32 v[108:109], v230 offset0:0 offset1:1\n"
      "ds_read2_b32 v[110:111], v230 offset0:2 offset1:3\n"
      "ds_read2_b32 v[76:77], v29 offset0:16 offset1:17\n"
      "ds_read2_b32 v[78:79], v29 offset0:18 offset1:19\n"
      "ds_read2_b32 v[112:113], v230 offset0:16 offset1:17\n"
      "ds_read2_b32 v[114:115], v230 offset0:18 offset1:19\n"
      "ds_read2_b32 v[80:81], v29 offset0:32 offset1:33\n"
      "ds_read2_b32 v[82:83], v29 offset0:34 offset1:35\n"
      "ds_read2_b32 v[116:117], v230 offset0:32 offset1:33\n"
      "ds_read2_b32 v[118:119], v230 offset0:34 offset1:35\n"
      "ds_read2_b32 v[84:85], v29 offset0:48 offset1:49\n"
      "ds_read2_b32 v[86:87], v29 offset0:50 offset1:51\n"
      "s_movk_i32 s59, 0x7fff\n"
      "s_waitcnt vmcnt(1)\n"
      "s_nop 0\n"
      ".Lcsgk%=:\n"
      "v_cmp_ne_u32 s[64:65], s15, v65\n"
      "v_cmp_ne_u32 vcc, s15, v67\n"
      "s_nop 0\n"
      "s_or_b64 vcc, vcc, s[64:65]\n"
      "s_cbranch_scc1 .Lcsgsk%=\n"
      ".Lcsgok%=:\n"
      "v_and_b32 v196, 0xffff0000, v64\n"
      "v_lshlrev_b32 v198, 16, v64\n"
      "v_rcp_f32 v196, v196\n"
      "v_and_b32 v197, 0xffff0000, v66\n"
      "v_lshlrev_b32 v199, 16, v66\n"
      "v_rcp_f32 v197, v197\n"
      "v_mul_f32 v198, -v198, v196\n"
      "v_mul_f32 v199, -v199, v197\n"
      "ds_read2_b32 v[120:121], v230 offset0:48 offset1:49\n"
      "ds_read2_b32 v[122:123], v230 offset0:50 offset1:51\n"
      "v_lshlrev_b32 v204, 16, v48\n"
      "v_lshlrev_b32 v205, 16, v52\n"
      "v_and_b32 v206, 0xffff0000, v48\n"
      "v_and_b32 v207, 0xffff0000, v52\n"
      "v_pk_fma_f32 v[204:205], v[204:205], v[196:197], v[198:199]\n"
      "v_pk_fma_f32 v[206:207], v[206:207], v[196:197], v[198:199]\n"
      "v_cvt_pk_f16_f32 v204, v204, v205\n"
      "v_cvt_pk_f16_f32 v206, v206, v207\n"
      "ds_read2_b32 v[88:89], v29 offset0:64 offset1:65\n"
      "ds_read2_b32 v[90:91], v29 offset0:66 offset1:67\n"
      "ds_write2st64_b32 v32, v204, v206 offset0:0 offset1:19\n"
      "v_lshlrev_b32 v208, 16, v49\n"
      "v_lshlrev_b32 v209, 16, v53\n"
      "v_and_b32 v210, 0xffff0000, v49\n"
      "v_and_b32 v211, 0xffff0000, v53\n"
      "v_pk_fma_f32 v[208:209], v[208:209], v[196:197], v[198:199]\n"
      "v_pk_fma_f32 v[210:211], v[210:211], v[196:197], v[198:199]\n"
      "v_cvt_pk_f16_f32 v208, v208, v209\n"
      "ds_read2_b32 v[124:125], v230 offset0:64 offset1:65\n"
      "ds_read2_b32 v[126:127], v230 offset0:66 offset1:67\n"
      "v_cvt_pk_f16_f32 v210, v210, v211\n"
      "ds_write2st64_b32 v32, v208, v210 offset0:38 offset1:57\n"
      "v_lshlrev_b32 v204, 16, v50\n"
      "v_lshlrev_b32 v205, 16, v54\n"
      "v_and_b32 v206, 0xffff0000, v50\n"
      "v_and_b32 v207, 0xffff0000, v54\n"
      "v_pk_fma_f32 v[204:205], v[204:205], v[196:197], v[198:199]\n"
      "v_pk_fma_f32 v[206:207], v[206:207], v[196:197], v[198:199]\n"
      "ds_read2_b32 v[92:93], v29 offset0:80 offset1:81\n"
      "ds_read2_b32 v[94:95], v29 offset0:82 offset1:83\n"
      "v_cvt_pk_f16_f32 v204, v204, v205\n"
      "v_cvt_pk_f16_f32 v206, v206, v207\n"
      "ds_write2st64_b32 v32, v204, v206 offset0:76 offset1:95\n"
      "v_lshlrev_b32 v208, 16, v51\n"
      "v_lshlrev_b32 v209, 16, v55\n"
      "v_and_b32 v210, 0xffff0000, v51\n"
      "v_and_b32 v211, 0xffff0000, v55\n"
      "v_pk_fma_f32 v[208:209], v[208:209], v[196:197], v[198:199]\n"
      "ds_read2_b32 v[128:129], v230 offset0:80 offset1:81\n"
      "ds_read2_b32 v[130:131], v230 offset0:82 offset1:83\n"
      "v_pk_fma_f32 v[210:211], v[210:211], v[196:197], v[198:199]\n"
      "v_cvt_pk_f16_f32 v208, v208, v209\n"
      "v_cvt_pk_f16_f32 v210, v210, v211\n"
      "ds_write2st64_b32 v32, v208, v210 offset0:114 offset1:133\n"
      "s_mov_b32 s54, s41\n"
      "s_add_u32 s55, s41, 0x100000\n"
      "s_add_u32 s56, s41, 0x200000\n"
      "s_add_u32 s57, s41, 0x300000\n"
      "ds_read2_b32 v[96:97], v29 offset0:96 offset1:97\n"
      "ds_read2_b32 v[98:99], v29 offset0:98 offset1:99\n"
      "s_mov_b32 s58, 0x40000000\n"
      "v_lshrrev_b32 v14, 9, v8\n"
      "v_add_u32 v14, 0x27600, v14\n"
      "ds_read2_b32 v[132:133], v230 offset0:96 offset1:97\n"
      "ds_read2_b32 v[134:135], v230 offset0:98 offset1:99\n"
      "ds_read2_b32 v[100:101], v29 offset0:112 offset1:113\n"
      "ds_read2_b32 v[102:103], v29 offset0:114 offset1:115\n"
      "ds_read2_b32 v[136:137], v230 offset0:112 offset1:113\n"
      "ds_read2_b32 v[138:139], v230 offset0:114 offset1:115\n"
      "ds_read2_b32 v[104:105], v29 offset0:128 offset1:129\n"
      "ds_read2_b32 v[106:107], v29 offset0:130 offset1:131\n"
      "ds_read2_b32 v[72:73], v29 offset0:0 offset1:1\n"
      "ds_read2_b32 v[74:75], v29 offset0:2 offset1:3\n"
      "s_waitcnt lgkmcnt(12)\n"
      ".Lccv%=:\n"
      "s_barrier\n"
      "s_setprio 3\n"
      "ds_read_b128 v[216:219], v33 offset:64\n"
      "ds_read_b128 v[220:223], v33 offset:128\n"
      "ds_read_b128 v[224:227], v33 offset:192\n"
      "ds_read_b128 v[212:215], v33 offset:256\n"
      "v_permlane32_swap_b32 v48, v52\n"
      "v_permlane32_swap_b32 v49, v53\n"
      "v_permlane32_swap_b32 v50, v54\n"
      "v_permlane32_swap_b32 v51, v55\n"
      "s_cmp_lg_u32 s33, 0\n"
      "s_cbranch_scc1 .Lca%=\n"
      "v_mov_b32 v16, s15\n"
      "v_mov_b32 v17, s15\n"
      "buffer_store_dwordx2 v[16:17], off, s[16:19], s42 sc1\n"
      ".Lca%=:\n"
      "s_waitcnt lgkmcnt(3)\n"
      "v_mfma_f32_16x16x32_f16 v[180:183], v[108:111], v[216:219], 0\n"
      "buffer_load_dwordx4 v[56:59], v41, s[20:23], s61 offen nt\n"
      "ds_read_b128 v[236:239], v34 offset:0\n"
      "ds_read_b128 v[240:243], v34 offset:16\n"
      "v_lshlrev_b32 v29, 2, v43\n"
      "v_add_u32 v29, 0x27600, v29\n"
      "ds_read_b32 v255, v29 \n"
      "v_mfma_f32_16x16x32_f16 v[184:187], v[76:79], v[216:219], 0\n"
      "ds_read2_b32 v[140:141], v30 offset0:16 offset1:17\n"
      "ds_read_b128 v[216:219], v33 offset:320\n"
      "s_waitcnt lgkmcnt(7)\n"
      "v_mfma_f32_16x16x32_f16 v[180:183], v[112:115], v[220:223], v[180:183]\n"
      "v_mfma_f32_16x16x32_f16 v[184:187], v[80:83], v[220:223], v[184:187]\n"
      "ds_read2_b32 v[142:143], v30 offset0:64 offset1:65\n"
      "ds_read_b128 v[220:223], v33 offset:384\n"
      "s_waitcnt lgkmcnt(8)\n"
      "v_mfma_f32_16x16x32_f16 v[180:183], v[116:119], v[224:227], v[180:183]\n"
      "s_waitcnt lgkmcnt(6)\n"
      "v_alignbit_b32 v245, v238, v237, 16\n"
      "v_mfma_f32_16x16x32_f16 v[184:187], v[84:87], v[224:227], v[184:187]\n"
      "ds_read2_b32 v[144:145], v30 offset0:8 offset1:9\n"
      "ds_read_b128 v[224:227], v33 offset:448\n"
      "v_alignbit_b32 v246, v239, v238, 16\n"
      "v_mfma_f32_16x16x32_f16 v[180:183], v[120:123], v[212:215], v[180:183]\n"
      "s_waitcnt lgkmcnt(7)\n"
      "v_alignbit_b32 v247, v240, v239, 16\n"
      "s_waitcnt lgkmcnt(6)\n"
      "v_readlane_b32 s72, v255, 0\n"
      "v_mfma_f32_16x16x32_f16 v[184:187], v[88:91], v[212:215], v[184:187]\n"
      "ds_read2_b32 v[146:147], v30 offset0:56 offset1:57\n"
      "ds_read_b128 v[212:215], v33 offset:512\n"
      "v_alignbit_b32 v248, v241, v240, 16\n"
      "s_waitcnt lgkmcnt(6)\n"
      "v_mfma_f32_16x16x32_f16 v[180:183], v[124:127], v[216:219], v[180:183]\n"
      "v_alignbit_b32 v249, v242, v241, 16\n"
      "v_readlane_b32 s73, v255, 1\n"
      "v_mfma_f32_16x16x32_f16 v[184:187], v[92:95], v[216:219], v[184:187]\n"
      "ds_read2_b32 v[148:149], v30 offset0:0 offset1:1\n"
      "v_pk_add_f16 v16, v245, v246\n"
      "s_waitcnt lgkmcnt(5)\n"
      "v_mfma_f32_16x16x32_f16 v[180:183], v[128:131], v[220:223], v[180:183]\n"
      "v_pk_add_f16 v17, v246, v247\n"
      "v_readlane_b32 s74, v255, 2\n"
      "v_mfma_f32_16x16x32_f16 v[184:187], v[96:99], v[220:223], v[184:187]\n"
      "ds_read2_b32 v[150:151], v30 offset0:48 offset1:49\n"
      "ds_read_b128 v[216:219], v33 offset:576\n"
      "v_pk_add_f16 v18, v247, v248\n"
      "s_waitcnt lgkmcnt(5)\n"
      "v_mfma_f32_16x16x32_f16 v[180:183], v[132:135], v[224:227], v[180:183]\n"
      "buffer_load_dwordx4 v[60:63], v41, s[20:23], s61 offen offset:2048 nt\n"
      "v_pk_add_f16 v19, v248, v249\n"
      "v_readlane_b32 s75, v255, 3\n"
      "v_mfma_f32_16x16x32_f16 v[184:187], v[100:103], v[224:227], v[184:187]\n"
      "ds_read2_b32 v[152:153], v30 offset0:40 offset1:41\n"
      "ds_read_b128 v[220:223], v33 offset:640\n"
      "v_pk_add_f16 v20, v16, v238\n"
      "s_waitcnt lgkmcnt(5)\n"
      "v_mfma_f32_16x16x32_f16 v[180:183], v[136:139], v[212:215], v[180:183]\n"
      "v_pk_add_f16 v21, v17, v239\n"
      "v_readlane_b32 s76, v255, 4\n"
      "v_mfma_f32_16x16x32_f16 v[184:187], v[104:107], v[212:215], v[184:187]\n"
      "ds_read2_b32 v[154:155], v30 offset0:88 offset1:89\n"
      "ds_read_b128 v[224:227], v33 offset:704\n"
      "v_pk_add_f16 v22, v18, v240\n"
      "v_mfma_f32_16x16x32_f16 v[188:191], v[72:75], v[212:215], 0\n"
      "v_pk_add_f16 v23, v19, v241\n"
      "v_readlane_b32 s77, v255, 5\n"
      "v_mfma_f32_16x16x32_f16 v[196:199], v[44:47], v[212:215], 0\n"
      "ds_read2_b32 v[156:157], v30 offset0:32 offset1:33\n"
      "ds_read_b128 v[212:215], v33 offset:768\n"
      "ds_write_b128 v37, v[20:23] offset:0\n"
      "v_pk_add_f16 v16, v237, v239\n"
      "s_waitcnt lgkmcnt(7)\n"
      "v_mfma_f32_16x16x32_f16 v[184:187], v[108:111], v[216:219], v[184:187]\n"
      "v_pk_add_f16 v17, v238, v240\n"
      "v_readlane_b32 s78, v255, 6\n"
      "v_cvt_pk_bf16_f32 v24, v180, v182\n"
      "v_cvt_pk_bf16_f32 v25, v181, v183\n"
      "ds_write2st64_b32 v39, v24, v25 offset0:0 offset1:8\n"
      "v_mfma_f32_16x16x32_f16 v[188:191], v[76:79], v[216:219], v[188:191]\n"
      "ds_read2_b32 v[158:159], v30 offset0:80 offset1:81\n"
      "ds_read_b128 v[216:219], v33 offset:832\n"
      "v_pk_add_f16 v18, v239, v241\n"
      "s_waitcnt lgkmcnt(8)\n"
      "v_mfma_f32_16x16x32_f16 v[184:187], v[112:115], v[220:223], v[184:187]\n"
      "v_pk_add_f16 v19, v240, v242\n"
      "v_readlane_b32 s79, v255, 7\n"
      "v_mfma_f32_16x16x32_f16 v[188:191], v[80:83], v[220:223], v[188:191]\n"
      "ds_read2_b32 v[160:161], v30 offset0:24 offset1:25\n"
      "ds_read_b128 v[220:223], v33 offset:896\n"
      "v_pk_add_f16 v20, v20, v16\n"
      "s_waitcnt lgkmcnt(8)\n"
      "v_mfma_f32_16x16x32_f16 v[184:187], v[116:119], v[224:227], v[184:187]\n"
      "v_pk_add_f16 v21, v21, v17\n"
      "s_setprio 2\n"
      "v_mfma_f32_16x16x32_f16 v[188:191], v[84:87], v[224:227], v[188:191]\n"
      "ds_read2_b32 v[162:163], v30 offset0:72 offset1:73\n"
      "ds_read_b128 v[224:227], v33 offset:960\n"
      "v_pk_add_f16 v22, v22, v18\n"
      "s_waitcnt lgkmcnt(8)\n"
      "v_mfma_f32_16x16x32_f16 v[184:187], v[120:123], v[212:215], v[184:187]\n"
      "v_pk_add_f16 v23, v23, v19\n"
      "v_mfma_f32_16x16x32_f16 v[188:191], v[88:91], v[212:215], v[188:191]\n"
      "ds_read2_b32 v[164:165], v30 offset0:128 offset1:129\n"
      "ds_read_b128 v[212:215], v33 offset:1024\n"
      "ds_write_b128 v37, v[20:23] offset:3104\n"
      "v_alignbit_b32 v244, v237, v236, 16\n"
      "s_waitcnt lgkmcnt(7)\n"
      "v_mfma_f32_16x16x32_f16 v[184:187], v[124:127], v[216:219], v[184:187]\n"
      "v_alignbit_b32 v250, v243, v242, 16\n"
      "v_mfma_f32_16x16x32_f16 v[188:191], v[92:95], v[216:219], v[188:191]\n"
      "v_pk_add_f16 v16, v244, v247\n"
      "s_waitcnt lgkmcnt(5)\n"
      "v_mfma_f32_16x16x32_f16 v[184:187], v[128:131], v[220:223], v[184:187]\n"
      "v_pk_add_f16 v17, v245, v248\n"
      "v_mfma_f32_16x16x32_f16 v[188:191], v[96:99], v[220:223], v[188:191]\n"
      "ds_read2_b32 v[166:167], v30 offset0:168 offset1:169\n"
      "ds_read_b64_tr_b16 v[228:229], v35 offset:32\n"
      "ds_read_b64_tr_b16 v[230:231], v35 offset:16\n"
      "v_pk_add_f16 v18, v246, v249\n"
      "s_waitcnt lgkmcnt(6)\n"
      "v_mfma_f32_16x16x32_f16 v[184:187], v[132:135], v[224:227], v[184:187]\n"
      "v_pk_add_f16 v19, v247, v250\n"
      "v_mfma_f32_16x16x32_f16 v[188:191], v[100:103], v[224:227], v[188:191]\n"
      "v_pk_add_f16 v20, v20, v16\n"
      "s_waitcnt lgkmcnt(4)\n"
      "v_mfma_f32_16x16x32_f16 v[184:187], v[136:139], v[212:215], v[184:187]\n"
      "ds_read_b128 v[216:219], v33 offset:1088\n"
      "v_pk_add_f16 v21, v21, v17\n"
      "v_mfma_f32_16x16x32_f16 v[188:191], v[104:107], v[212:215], v[188:191]\n"
      "ds_read2_b32 v[168:169], v30 offset0:120 offset1:121\n"
      "v_pk_add_f16 v22, v22, v18\n"
      "v_mfma_f32_16x16x32_f16 v[192:195], v[72:75], v[212:215], 0\n"
      "ds_read_b128 v[220:223], v33 offset:1152\n"
      "v_pk_add_f16 v23, v23, v19\n"
      "v_mfma_f32_16x16x32_f16 v[200:203], v[44:47], v[212:215], 0\n"
      "ds_write_b128 v38, v[20:23] offset:0\n"
      "v_pk_add_f16 v16, v236, v240\n"
      "s_waitcnt lgkmcnt(4)\n"
      "v_mfma_f32_16x16x32_f16 v[188:191], v[228:231], v[140:143], v[188:191]\n"
      "ds_read_b128 v[224:227], v33 offset:1216\n"
      "v_pk_add_f16 v17, v237, v241\n"
      "v_cvt_pk_bf16_f32 v24, v184, v186\n"
      "v_cvt_pk_bf16_f32 v25, v185, v187\n"
      "ds_write2st64_b32 v39, v24, v25 offset0:1 offset1:9\n"
      "v_mfma_f32_16x16x32_f16 v[192:195], v[228:231], v[144:147], v[192:195]\n"
      "ds_read2_b32 v[170:171], v30 offset0:160 offset1:161\n"
      "v_pk_add_f16 v18, v238, v242\n"
      "v_mfma_f32_16x16x32_f16 v[196:199], v[228:231], v[148:151], v[196:199]\n"
      "ds_read_b64_tr_b16 v[232:233], v35 offset:3128\n"
      "ds_read_b64_tr_b16 v[234:235], v35 offset:3112\n"
      "v_pk_add_f16 v19, v239, v243\n"
      "s_waitcnt lgkmcnt(8)\n"
      "v_mfma_f32_16x16x32_f16 v[188:191], v[108:111], v[216:219], v[188:191]\n"
      "v_pk_add_f16 v20, v20, v16\n"
      "ds_read_b128 v[236:239], v34 offset:1024\n"
      "ds_read_b128 v[240:243], v34 offset:1040\n"
      "v_mfma_f32_16x16x32_f16 v[192:195], v[76:79], v[216:219], v[192:195]\n"
      "v_pk_add_f16 v21, v21, v17\n"
      "s_waitcnt lgkmcnt(8)\n"
      "v_mfma_f32_16x16x32_f16 v[188:191], v[112:115], v[220:223], v[188:191]\n"
      "ds_read2_b32 v[172:173], v30 offset0:112 offset1:113\n"
      "ds_read_b128 v[212:215], v33 offset:1280\n"
      "v_pk_add_f16 v22, v22, v18\n"
      "v_mfma_f32_16x16x32_f16 v[192:195], v[80:83], v[220:223], v[192:195]\n"
      "v_pk_add_f16 v23, v23, v19\n"
      "s_waitcnt lgkmcnt(8)\n"
      "v_mfma_f32_16x16x32_f16 v[188:191], v[116:119], v[224:227], v[188:191]\n"
      "ds_read_b128 v[216:219], v33 offset:1344\n"
      "ds_write_b128 v38, v[20:23] offset:2080\n"
      "v_mfma_f32_16x16x32_f16 v[192:195], v[84:87], v[224:227], v[192:195]\n"
      "s_waitcnt lgkmcnt(5)\n"
      "v_alignbit_b32 v245, v238, v237, 16\n"
      "s_setprio 1\n"
      "v_mfma_f32_16x16x32_f16 v[188:191], v[232:235], v[152:155], v[188:191]\n"
      "ds_read2_b32 v[174:175], v30 offset0:152 offset1:153\n"
      "ds_read_b128 v[220:223], v33 offset:1408\n"
      "v_alignbit_b32 v246, v239, v238, 16\n"
      "v_mfma_f32_16x16x32_f16 v[192:195], v[232:235], v[156:159], v[192:195]\n"
      "s_waitcnt lgkmcnt(6)\n"
      "v_alignbit_b32 v247, v240, v239, 16\n"
      "v_mfma_f32_16x16x32_f16 v[196:199], v[232:235], v[160:163], v[196:199]\n"
      "ds_read_b128 v[224:227], v33 offset:1472\n"
      "v_alignbit_b32 v248, v241, v240, 16\n"
      "s_waitcnt lgkmcnt(5)\n"
      "v_mfma_f32_16x16x32_f16 v[188:191], v[120:123], v[212:215], v[188:191]\n"
      "v_alignbit_b32 v249, v242, v241, 16\n"
      "v_mfma_f32_16x16x32_f16 v[192:195], v[88:91], v[212:215], v[192:195]\n"
      "ds_read2_b32 v[176:177], v30 offset0:104 offset1:105\n"
      "ds_read_b128 v[212:215], v33 offset:1536\n"
      "v_pk_add_f16 v16, v245, v246\n"
      "s_waitcnt lgkmcnt(6)\n"
      "v_mfma_f32_16x16x32_f16 v[188:191], v[124:127], v[216:219], v[188:191]\n"
      "v_pk_add_f16 v17, v246, v247\n"
      "v_mfma_f32_16x16x32_f16 v[192:195], v[92:95], v[216:219], v[192:195]\n"
      "v_pk_add_f16 v18, v247, v248\n"
      "s_waitcnt lgkmcnt(3)\n"
      "v_mfma_f32_16x16x32_f16 v[188:191], v[128:131], v[220:223], v[188:191]\n"
      "v_pk_add_f16 v19, v248, v249\n"
      "v_mfma_f32_16x16x32_f16 v[192:195], v[96:99], v[220:223], v[192:195]\n"
      "ds_read2_b32 v[178:179], v30 offset0:144 offset1:145\n"
      "ds_read_b64_tr_b16 v[228:229], v35 offset:544\n"
      "ds_read_b64_tr_b16 v[230:231], v35 offset:528\n"
      "v_pk_add_f16 v20, v16, v238\n"
      "s_waitcnt lgkmcnt(5)\n"
      "v_mfma_f32_16x16x32_f16 v[188:191], v[132:135], v[224:227], v[188:191]\n"
      "v_pk_add_f16 v21, v17, v239\n"
      "v_mfma_f32_16x16x32_f16 v[192:195], v[100:103], v[224:227], v[192:195]\n"
      "v_pk_add_f16 v22, v18, v240\n"
      "s_waitcnt lgkmcnt(3)\n"
      "v_mfma_f32_16x16x32_f16 v[188:191], v[136:139], v[212:215], v[188:191]\n"
      "ds_read_b128 v[216:219], v33 offset:1600\n"
      "v_pk_add_f16 v23, v19, v241\n"
      "v_mfma_f32_16x16x32_f16 v[192:195], v[104:107], v[212:215], v[192:195]\n"
      "ds_write_b128 v37, v[20:23] offset:1024\n"
      "v_pk_add_f16 v16, v237, v239\n"
      "v_mfma_f32_16x16x32_f16 v[196:199], v[72:75], v[212:215], v[196:199]\n"
      "ds_read_b128 v[220:223], v33 offset:1664\n"
      "v_pk_add_f16 v17, v238, v240\n"
      "v_mfma_f32_16x16x32_f16 v[204:207], v[44:47], v[212:215], 0\n"
      "v_pk_add_f16 v18, v239, v241\n"
      "s_waitcnt lgkmcnt(3)\n"
      "v_mfma_f32_16x16x32_f16 v[192:195], v[228:231], v[140:143], v[192:195]\n"
      "ds_read_b128 v[224:227], v33 offset:1728\n"
      "s_movk_i32 s59, 0x7fff\n"
      "s_waitcnt vmcnt(2)\n"
      "s_nop 0\n"
      ".Lcsgh%=:\n"
      "v_cmp_ne_u32 s[64:65], s15, v69\n"
      "v_cmp_ne_u32 vcc, s15, v71\n"
      "s_nop 0\n"
      "s_or_b64 vcc, vcc, s[64:65]\n"
      "s_cbranch_scc1 .Lcsgsh%=\n"
      ".Lcsgoh%=:\n"
      "v_mov_b32 v42, v8\n"
      "v_and_b32 v26, 0xffff0000, v68\n"
      "v_lshlrev_b32 v28, 16, v68\n"
      "v_pk_add_f16 v19, v240, v242\n"
      "v_cvt_pk_bf16_f32 v24, v188, v190\n"
      "v_cvt_pk_bf16_f32 v25, v189, v191\n"
      "ds_write2st64_b32 v39, v24, v25 offset0:2 offset1:10\n"
      "v_mfma_f32_16x16x32_f16 v[196:199], v[228:231], v[144:147], v[196:199]\n"
      "v_rcp_f32 v26, v26\n"
      "v_and_b32 v27, 0xffff0000, v70\n"
      "v_pk_add_f16 v20, v20, v16\n"
      "v_mfma_f32_16x16x32_f16 v[200:203], v[228:231], v[148:151], v[200:203]\n"
      "ds_read_b64_tr_b16 v[232:233], v35 offset:3640\n"
      "ds_read_b64_tr_b16 v[234:235], v35 offset:3624\n"
      "v_lshlrev_b32 v29, 16, v70\n"
      "v_rcp_f32 v27, v27\n"
      "v_pk_add_f16 v21, v21, v17\n"
      "s_waitcnt lgkmcnt(6)\n"
      "v_mfma_f32_16x16x32_f16 v[192:195], v[108:111], v[216:219], v[192:195]\n"
      "v_mul_f32 v28, -v28, v26\n"
      "v_mul_f32 v29, -v29, v27\n"
      "v_pk_add_f16 v22, v22, v18\n"
      "v_mfma_f32_16x16x32_f16 v[196:199], v[76:79], v[216:219], v[196:199]\n"
      "s_waitcnt vmcnt(1)\n"
      "v_lshlrev_b32 v180, 16, v56\n"
      "s_waitcnt vmcnt(0)\n"
      "v_lshlrev_b32 v181, 16, v60\n"
      "v_pk_add_f16 v23, v23, v19\n"
      "s_setprio 0\n"
      "s_waitcnt lgkmcnt(4)\n"
      "v_mfma_f32_16x16x32_f16 v[192:195], v[112:115], v[220:223], v[192:195]\n"
      "ds_read_b128 v[212:215], v33 offset:1792\n"
      "v_and_b32 v182, 0xffff0000, v56\n"
      "v_and_b32 v183, 0xffff0000, v60\n"
      "ds_write_b128 v37, v[20:23] offset:4128\n"
      "v_alignbit_b32 v244, v237, v236, 16\n"
      "v_mfma_f32_16x16x32_f16 v[196:199], v[80:83], v[220:223], v[196:199]\n"
      "v_fma_f32 v180, v180, v26, v28\n"
      "v_fma_f32 v181, v181, v27, v29\n"
      "v_alignbit_b32 v250, v243, v242, 16\n"
      "s_waitcnt lgkmcnt(5)\n"
      "v_mfma_f32_16x16x32_f16 v[192:195], v[116:119], v[224:227], v[192:195]\n"
      "ds_read_b128 v[216:219], v33 offset:1856\n"
      "v_fma_f32 v182, v182, v26, v28\n"
      "v_fma_f32 v183, v183, v27, v29\n"
      "v_pk_add_f16 v16, v244, v247\n"
      "v_mfma_f32_16x16x32_f16 v[196:199], v[84:87], v[224:227], v[196:199]\n"
      "v_cvt_pk_f16_f32 v180, v180, v181\n"
      "v_cvt_pk_f16_f32 v182, v182, v183\n"
      "v_pk_add_f16 v17, v245, v248\n"
      "s_waitcnt lgkmcnt(3)\n"
      "v_mfma_f32_16x16x32_f16 v[192:195], v[232:235], v[152:155], v[192:195]\n"
      "ds_read_b128 v[220:223], v33 offset:1920\n"
      "ds_write2st64_b32 v32, v180, v182 offset0:8 offset1:27\n"
      "v_lshlrev_b32 v184, 16, v57\n"
      "v_lshlrev_b32 v185, 16, v61\n"
      "v_pk_add_f16 v18, v246, v249\n"
      "v_mfma_f32_16x16x32_f16 v[196:199], v[232:235], v[156:159], v[196:199]\n"
      "v_and_b32 v186, 0xffff0000, v57\n"
      "v_and_b32 v187, 0xffff0000, v61\n"
      "v_pk_add_f16 v19, v247, v250\n"
      "s_cmp_lg_u32 s33, 0\n"
      "s_cbranch_scc1 .Lcg%=\n"
      "buffer_load_dwordx2 v[252:253], v9, s[16:19], s43 offen sc1\n"
      ".Lcg%=:\n"
      "v_mfma_f32_16x16x32_f16 v[200:203], v[232:235], v[160:163], v[200:203]\n"
      "ds_read_b128 v[224:227], v33 offset:1984\n"
      "v_fma_f32 v184, v184, v26, v28\n"
      "v_fma_f32 v185, v185, v27, v29\n"
      "v_pk_add_f16 v20, v20, v16\n"
      "s_waitcnt lgkmcnt(5)\n"
      "v_mfma_f32_16x16x32_f16 v[192:195], v[120:123], v[212:215], v[192:195]\n"
      "v_fma_f32 v186, v186, v26, v28\n"
      "v_fma_f32 v187, v187, v27, v29\n"
      "v_pk_add_f16 v21, v21, v17\n"
      "v_mfma_f32_16x16x32_f16 v[196:199], v[88:91], v[212:215], v[196:199]\n"
      "ds_read_b128 v[212:215], v33 offset:2048\n"
      "v_cvt_pk_f16_f32 v184, v184, v185\n"
      "v_cvt_pk_f16_f32 v186, v186, v187\n"
      "v_pk_add_f16 v22, v22, v18\n"
      "s_waitcnt lgkmcnt(4)\n"
      "v_mfma_f32_16x16x32_f16 v[192:195], v[124:127], v[216:219], v[192:195]\n"
      "ds_write2st64_b32 v32, v184, v186 offset0:46 offset1:65\n"
      "v_lshlrev_b32 v180, 16, v58\n"
      "v_lshlrev_b32 v181, 16, v62\n"
      "v_pk_add_f16 v23, v23, v19\n"
      "v_mfma_f32_16x16x32_f16 v[196:199], v[92:95], v[216:219], v[196:199]\n"
      "v_and_b32 v182, 0xffff0000, v58\n"
      "v_and_b32 v183, 0xffff0000, v62\n"
      "ds_write_b128 v38, v[20:23] offset:1024\n"
      "v_pk_add_f16 v16, v236, v240\n"
      "s_waitcnt lgkmcnt(5)\n"
      "v_mfma_f32_16x16x32_f16 v[192:195], v[128:131], v[220:223], v[192:195]\n"
      "v_fma_f32 v180, v180, v26, v28\n"
      "v_fma_f32 v181, v181, v27, v29\n"
      "v_pk_add_f16 v17, v237, v241\n"
      "v_mfma_f32_16x16x32_f16 v[196:199], v[96:99], v[220:223], v[196:199]\n"
      "ds_read_b64_tr_b16 v[228:229], v36 offset:24\n"
      "ds_read_b64_tr_b16 v[230:231], v36 offset:2088\n"
      "v_fma_f32 v182, v182, v26, v28\n"
      "v_fma_f32 v183, v183, v27, v29\n"
      "v_pk_add_f16 v18, v238, v242\n"
      "s_waitcnt lgkmcnt(5)\n"
      "v_mfma_f32_16x16x32_f16 v[192:195], v[132:135], v[224:227], v[192:195]\n"
      "v_cvt_pk_f16_f32 v180, v180, v181\n"
      "v_cvt_pk_f16_f32 v182, v182, v183\n"
      "v_pk_add_f16 v19, v239, v243\n"
      "v_mfma_f32_16x16x32_f16 v[196:199], v[100:103], v[224:227], v[196:199]\n"
      "ds_write2st64_b32 v32, v180, v182 offset0:84 offset1:103\n"
      "v_lshlrev_b32 v184, 16, v59\n"
      "v_lshlrev_b32 v185, 16, v63\n"
      "v_pk_add_f16 v20, v20, v16\n"
      "s_waitcnt lgkmcnt(5)\n"
      "v_mfma_f32_16x16x32_f16 v[192:195], v[136:139], v[212:215], v[192:195]\n"
      "v_and_b32 v186, 0xffff0000, v59\n"
      "v_and_b32 v187, 0xffff0000, v63\n"
      "v_pk_add_f16 v21, v21, v17\n"
      "v_mfma_f32_16x16x32_f16 v[196:199], v[104:107], v[212:215], v[196:199]\n"
      "ds_read_b64_tr_b16 v[232:233], v36 offset:536\n"
      "ds_read_b64_tr_b16 v[234:235], v36 offset:2600\n"
      "v_fma_f32 v184, v184, v26, v28\n"
      "v_fma_f32 v185, v185, v27, v29\n"
      "v_pk_add_f16 v22, v22, v18\n"
      "v_mfma_f32_16x16x32_f16 v[200:203], v[72:75], v[212:215], v[200:203]\n"
      "v_fma_f32 v186, v186, v26, v28\n"
      "v_fma_f32 v187, v187, v27, v29\n"
      "v_pk_add_f16 v23, v23, v19\n"
      "v_mfma_f32_16x16x32_f16 v[208:211], v[44:47], v[212:215], 0\n"
      "v_cvt_pk_f16_f32 v184, v184, v185\n"
      "v_cvt_pk_f16_f32 v186, v186, v187\n"
      "ds_write_b128 v38, v[20:23] offset:3104\n"
      "s_waitcnt lgkmcnt(4)\n"
      "v_mfma_f32_16x16x32_f16 v[196:199], v[228:231], v[164:167], v[196:199]\n"
      "ds_write2st64_b32 v32, v184, v186 offset0:122 offset1:141\n"
      "v_permlane32_swap_b32 v56, v60\n"
      "v_permlane32_swap_b32 v57, v61\n"
      "v_cvt_pk_bf16_f32 v24, v192, v194\n"
      "v_cvt_pk_bf16_f32 v25, v193, v195\n"
      "ds_write2st64_b32 v39, v24, v25 offset0:3 offset1:11\n"
      "v_mfma_f32_16x16x32_f16 v[200:203], v[228:231], v[168:171], v[200:203]\n"
      "v_permlane32_swap_b32 v58, v62\n"
      "v_permlane32_swap_b32 v59, v63\n"
      "v_mfma_f32_16x16x32_f16 v[204:207], v[228:231], v[172:175], v[204:207]\n"
      "v_mfma_f32_16x16x32_f16 v[208:211], v[228:231], v[176:179], v[208:211]\n"
      "ds_read_b64_tr_b16 v[228:229], v35 offset:1056\n"
      "ds_read_b64_tr_b16 v[230:231], v35 offset:1040\n"
      "s_cmp_eq_u32 s33, 0\n"
      "s_cbranch_scc1 .Lch%=\n"
      ".Lcho%=:\n"
      "s_waitcnt lgkmcnt(2)\n"
      "s_barrier\n"
      "s_setprio 3\n"
      "v_mfma_f32_16x16x32_f16 v[200:203], v[232:235], v[164:167], v[200:203]\n"
      "ds_read_b64_tr_b16 v[64:65], v40 offset:0\n"
      "ds_read_b64_tr_b16 v[66:67], v40 offset:35072\n"
      "v_lshlrev_b32 v68, 16, v48\n"
      "v_and_b32 v69, 0xffff0000, v48\n"
      "v_mfma_f32_16x16x32_f16 v[204:207], v[232:235], v[168:171], v[204:207]\n"
      "v_lshlrev_b32 v70, 16, v49\n"
      "v_and_b32 v71, 0xffff0000, v49\n"
      "ds_read_b128 v[236:239], v34 offset:2048\n"
      "ds_read_b128 v[240:243], v34 offset:2064\n"
      "ds_read_b32 v12, v14 offset:256\n"
      "v_mfma_f32_16x16x32_f16 v[208:211], v[232:235], v[172:175], v[208:211]\n"
      "ds_read_b128 v[216:219], v33 offset:2112\n"
      "ds_read_b64_tr_b16 v[232:233], v35 offset:4152\n"
      "ds_read_b64_tr_b16 v[234:235], v35 offset:4136\n"
      "v_lshlrev_b32 v26, 16, v50\n"
      "v_and_b32 v27, 0xffff0000, v50\n"
      "s_waitcnt lgkmcnt(8)\n"
      "v_mfma_f32_16x16x32_f16 v[196:199], v[228:231], v[140:143], v[196:199]\n"
      "v_lshlrev_b32 v28, 16, v51\n"
      "v_and_b32 v29, 0xffff0000, v51\n"
      "v_mfma_f32_16x16x32_f16 v[200:203], v[228:231], v[144:147], v[200:203]\n"
      "ds_read_b128 v[220:223], v33 offset:2176\n"
      "s_waitcnt lgkmcnt(8)\n"
      "v_lshlrev_b32 v48, 16, v64\n"
      "v_and_b32 v64, 0xffff0000, v64\n"
      "v_mfma_f32_16x16x32_f16 v[204:207], v[228:231], v[148:151], v[204:207]\n"
      "v_fmac_f32 v68, s72, v48\n"
      "v_fmac_f32 v69, s73, v64\n"
      "s_waitcnt lgkmcnt(1)\n"
      "v_mfma_f32_16x16x32_f16 v[196:199], v[232:235], v[152:155], v[196:199]\n"
      "ds_read_b128 v[224:227], v33 offset:2240\n"
      "v_lshlrev_b32 v49, 16, v65\n"
      "v_and_b32 v65, 0xffff0000, v65\n"
      "v_mfma_f32_16x16x32_f16 v[200:203], v[232:235], v[156:159], v[200:203]\n"
      "v_fmac_f32 v70, s74, v49\n"
      "v_fmac_f32 v71, s75, v65\n"
      "v_mfma_f32_16x16x32_f16 v[204:207], v[232:235], v[160:163], v[204:207]\n"
      "ds_read_b128 v[212:215], v33 offset:2304\n"
      "v_lshlrev_b32 v50, 16, v66\n"
      "v_and_b32 v66, 0xffff0000, v66\n"
      "v_mfma_f32_16x16x32_f16 v[196:199], v[108:111], v[216:219], v[196:199]\n"
      "v_fmac_f32 v26, s76, v50\n"
      "v_fmac_f32 v27, s77, v66\n"
      "v_mfma_f32_16x16x32_f16 v[200:203], v[76:79], v[216:219], v[200:203]\n"
      "ds_read_b64_tr_b16 v[228:229], v35 offset:1568\n"
      "ds_read_b64_tr_b16 v[230:231], v35 offset:1552\n"
      "v_lshlrev_b32 v51, 16, v67\n"
      "v_and_b32 v67, 0xffff0000, v67\n"
      "s_waitcnt lgkmcnt(4)\n"
      "v_mfma_f32_16x16x32_f16 v[196:199], v[112:115], v[220:223], v[196:199]\n"
      "v_fmac_f32 v28, s78, v51\n"
      "v_fmac_f32 v29, s79, v67\n"
      "v_mfma_f32_16x16x32_f16 v[200:203], v[80:83], v[220:223], v[200:203]\n"
      "v_cvt_pk_bf16_f32 v48, v68, v69\n"
      "s_waitcnt lgkmcnt(3)\n"
      "v_mfma_f32_16x16x32_f16 v[196:199], v[116:119], v[224:227], v[196:199]\n"
      "ds_read_b128 v[216:219], v33 offset:2368\n"
      "v_cvt_pk_bf16_f32 v49, v70, v71\n"
      "s_setprio 2\n"
      "v_mfma_f32_16x16x32_f16 v[200:203], v[84:87], v[224:227], v[200:203]\n"
      "v_cvt_pk_bf16_f32 v50, v26, v27\n"
      "s_waitcnt lgkmcnt(3)\n"
      "v_mfma_f32_16x16x32_f16 v[196:199], v[120:123], v[212:215], v[196:199]\n"
      "ds_read_b128 v[220:223], v33 offset:2432\n"
      "v_cvt_pk_bf16_f32 v51, v28, v29\n"
      "v_lshlrev_b32 v13, 24, v12\n"
      "v_and_or_b32 v13, v13, s58, v42\n"
      "v_mfma_f32_16x16x32_f16 v[200:203], v[88:91], v[212:215], v[200:203]\n"
      "s_cmp_lt_u32 s33, 4\n"
      "s_cbranch_scc0 .Lcs0%=\n"
      "buffer_store_dwordx4 v[48:51], v13, s[24:27], s54 offen\n"
      ".Lcs0%=:\n"
      "v_alignbit_b32 v245, v238, v237, 16\n"
      "s_waitcnt lgkmcnt(2)\n"
      "v_mfma_f32_16x16x32_f16 v[200:203], v[228:231], v[140:143], v[200:203]\n"
      "ds_read_b128 v[224:227], v33 offset:2496\n"
      "v_alignbit_b32 v246, v239, v238, 16\n"
      "v_mfma_f32_16x16x32_f16 v[204:207], v[228:231], v[144:147], v[204:207]\n"
      "v_alignbit_b32 v247, v240, v239, 16\n"
      "v_mfma_f32_16x16x32_f16 v[208:211], v[228:231], v[148:151], v[208:211]\n"
      "ds_read_b128 v[212:215], v33 offset:2560\n"
      "v_alignbit_b32 v248, v241, v240, 16\n"
      "s_waitcnt lgkmcnt(3)\n"
      "v_mfma_f32_16x16x32_f16 v[196:199], v[124:127], v[216:219], v[196:199]\n"
      "v_alignbit_b32 v249, v242, v241, 16\n"
      "v_mfma_f32_16x16x32_f16 v[200:203], v[92:95], v[216:219], v[200:203]\n"
      "v_pk_add_f16 v16, v245, v246\n"
      "s_waitcnt lgkmcnt(2)\n"
      "v_mfma_f32_16x16x32_f16 v[196:199], v[128:131], v[220:223], v[196:199]\n"
      "ds_read_b64_tr_b16 v[232:233], v35 offset:4664\n"
      "ds_read_b64_tr_b16 v[234:235], v35 offset:4648\n"
      "ds_read_b64_tr_b16 v[64:65], v40 offset:512\n"
      "ds_read_b64_tr_b16 v[66:67], v40 offset:35584\n"
      "v_lshlrev_b32 v68, 16, v52\n"
      "v_and_b32 v69, 0xffff0000, v52\n"
      "v_mfma_f32_16x16x32_f16 v[200:203], v[96:99], v[220:223], v[200:203]\n"
      "v_lshlrev_b32 v70, 16, v53\n"
      "v_and_b32 v71, 0xffff0000, v53\n"
      "s_waitcnt lgkmcnt(5)\n"
      "v_mfma_f32_16x16x32_f16 v[196:199], v[132:135], v[224:227], v[196:199]\n"
      "v_lshlrev_b32 v26, 16, v54\n"
      "v_and_b32 v27, 0xffff0000, v54\n"
      "v_mfma_f32_16x16x32_f16 v[200:203], v[100:103], v[224:227], v[200:203]\n"
      "ds_read_b128 v[216:219], v33 offset:2624\n"
      "v_lshlrev_b32 v28, 16, v55\n"
      "v_and_b32 v29, 0xffff0000, v55\n"
      "s_waitcnt lgkmcnt(5)\n"
      "v_mfma_f32_16x16x32_f16 v[196:199], v[136:139], v[212:215], v[196:199]\n"
      "s_cmp_lt_u32 s33, 4\n"
      "s_cbranch_scc1 .Lct0%=\n"
      "buffer_store_dwordx4 v[48:51], v13, s[24:27], s54 offen\n"
      ".Lct0%=:\n"
      "s_waitcnt lgkmcnt(2)\n"
      "v_lshlrev_b32 v52, 16, v64\n"
      "v_and_b32 v64, 0xffff0000, v64\n"
      "v_mfma_f32_16x16x32_f16 v[200:203], v[104:107], v[212:215], v[200:203]\n"
      "ds_read_b128 v[220:223], v33 offset:2688\n"
      "v_fmac_f32 v68, s72, v52\n"
      "v_fmac_f32 v69, s73, v64\n"
      "s_setprio 1\n"
      "v_mfma_f32_16x16x32_f16 v[204:207], v[72:75], v[212:215], v[204:207]\n"
      "v_lshlrev_b32 v53, 16, v65\n"
      "v_and_b32 v65, 0xffff0000, v65\n"
      "v_mfma_f32_16x16x32_f16 v[200:203], v[232:235], v[152:155], v[200:203]\n"
      "ds_read_b128 v[224:227], v33 offset:2752\n"
      "v_fmac_f32 v70, s74, v53\n"
      "v_fmac_f32 v71, s75, v65\n"
      "v_mfma_f32_16x16x32_f16 v[204:207], v[232:235], v[156:159], v[204:207]\n"
      "s_waitcnt lgkmcnt(3)\n"
      "v_lshlrev_b32 v54, 16, v66\n"
      "v_and_b32 v66, 0xffff0000, v66\n"
      "v_cvt_pk_bf16_f32 v24, v196, v198\n"
      "v_cvt_pk_bf16_f32 v25, v197, v199\n"
      "ds_write2st64_b32 v39, v24, v25 offset0:4 offset1:12\n"
      "v_mfma_f32_16x16x32_f16 v[208:211], v[232:235], v[160:163], v[208:211]\n"
      "ds_read_b128 v[212:215], v33 offset:2816\n"
      "v_fmac_f32 v26, s76, v54\n"
      "v_fmac_f32 v27, s77, v66\n"
      "s_waitcnt lgkmcnt(4)\n"
      "v_mfma_f32_16x16x32_f16 v[200:203], v[108:111], v[216:219], v[200:203]\n"
      "v_lshlrev_b32 v55, 16, v67\n"
      "v_and_b32 v67, 0xffff0000, v67\n"
      "v_mfma_f32_16x16x32_f16 v[204:207], v[76:79], v[216:219], v[204:207]\n"
      "ds_read_b128 v[216:219], v33 offset:2880\n"
      "v_fmac_f32 v28, s78, v55\n"
      "v_fmac_f32 v29, s79, v67\n"
      "s_waitcnt lgkmcnt(4)\n"
      "v_mfma_f32_16x16x32_f16 v[200:203], v[112:115], v[220:223], v[200:203]\n"
      "v_cvt_pk_bf16_f32 v52, v68, v69\n"
      "v_mfma_f32_16x16x32_f16 v[204:207], v[80:83], v[220:223], v[204:207]\n"
      "ds_read_b128 v[220:223], v33 offset:2944\n"
      "v_cvt_pk_bf16_f32 v53, v70, v71\n"
      "s_waitcnt lgkmcnt(4)\n"
      "v_mfma_f32_16x16x32_f16 v[200:203], v[116:119], v[224:227], v[200:203]\n"
      "v_cvt_pk_bf16_f32 v54, v26, v27\n"
      "v_mfma_f32_16x16x32_f16 v[204:207], v[84:87], v[224:227], v[204:207]\n"
      "ds_read_b128 v[224:227], v33 offset:3008\n"
      "v_cvt_pk_bf16_f32 v55, v28, v29\n"
      "v_lshlrev_b32 v13, 16, v12\n"
      "v_and_or_b32 v13, v13, s58, v42\n"
      "s_waitcnt lgkmcnt(3)\n"
      "v_mfma_f32_16x16x32_f16 v[200:203], v[120:123], v[212:215], v[200:203]\n"
      "s_cmp_lt_u32 s33, 4\n"
      "s_cbranch_scc0 .Lcs1%=\n"
      "buffer_store_dwordx4 v[52:55], v13, s[24:27], s55 offen\n"
      ".Lcs1%=:\n"
      "v_pk_add_f16 v17, v246, v247\n"
      "s_setprio 0\n"
      "v_mfma_f32_16x16x32_f16 v[204:207], v[88:91], v[212:215], v[204:207]\n"
      "ds_read_b128 v[212:215], v33 offset:3072\n"
      "v_pk_add_f16 v18, v247, v248\n"
      "s_waitcnt lgkmcnt(3)\n"
      "v_mfma_f32_16x16x32_f16 v[200:203], v[124:127], v[216:219], v[200:203]\n"
      "v_pk_add_f16 v19, v248, v249\n"
      "v_mfma_f32_16x16x32_f16 v[204:207], v[92:95], v[216:219], v[204:207]\n"
      "v_pk_add_f16 v20, v16, v238\n"
      "s_waitcnt lgkmcnt(2)\n"
      "v_mfma_f32_16x16x32_f16 v[200:203], v[128:131], v[220:223], v[200:203]\n"
      "ds_read_b64_tr_b16 v[228:229], v36 offset:1048\n"
      "ds_read_b64_tr_b16 v[230:231], v36 offset:3112\n"
      "v_pk_add_f16 v21, v17, v239\n"
      "v_mfma_f32_16x16x32_f16 v[204:207], v[96:99], v[220:223], v[204:207]\n"
      "v_pk_add_f16 v22, v18, v240\n"
      "s_waitcnt lgkmcnt(3)\n"
      "v_mfma_f32_16x16x32_f16 v[200:203], v[132:135], v[224:227], v[200:203]\n"
      "ds_read_b128 v[216:219], v33 offset:3136\n"
      "v_pk_add_f16 v23, v19, v241\n"
      "v_mfma_f32_16x16x32_f16 v[204:207], v[100:103], v[224:227], v[204:207]\n"
      "ds_write_b128 v37, v[20:23] offset:2048\n"
      "v_pk_add_f16 v16, v237, v239\n"
      "s_waitcnt lgkmcnt(4)\n"
      "v_mfma_f32_16x16x32_f16 v[200:203], v[136:139], v[212:215], v[200:203]\n"
      "ds_read_b128 v[220:223], v33 offset:3200\n"
      "v_pk_add_f16 v17, v238, v240\n"
      "v_mfma_f32_16x16x32_f16 v[204:207], v[104:107], v[212:215], v[204:207]\n"
      "v_pk_add_f16 v18, v239, v241\n"
      "v_mfma_f32_16x16x32_f16 v[208:211], v[72:75], v[212:215], v[208:211]\n"
      "ds_read_b128 v[224:227], v33 offset:3264\n"
      "s_cmp_lt_u32 s33, 4\n"
      "s_cbranch_scc1 .Lct1%=\n"
      "buffer_store_dwordx4 v[52:55], v13, s[24:27], s55 offen\n"
      ".Lct1%=:\n"
      "v_pk_add_f16 v19, v240, v242\n"
      "s_waitcnt lgkmcnt(4)\n"
      "v_mfma_f32_16x16x32_f16 v[204:207], v[228:231], v[164:167], v[204:207]\n"
      "v_pk_add_f16 v20, v20, v16\n"
      "v_mfma_f32_16x16x32_f16 v[208:211], v[228:231], v[168:171], v[208:211]\n"
      "ds_read_b64_tr_b16 v[232:233], v36 offset:1560\n"
      "ds_read_b64_tr_b16 v[234:235], v36 offset:3624\n"
      "v_pk_add_f16 v21, v21, v17\n"
      "v_cvt_pk_bf16_f32 v24, v200, v202\n"
      "v_cvt_pk_bf16_f32 v25, v201, v203\n"
      "ds_write2st64_b32 v39, v24, v25 offset0:5 offset1:13\n"
      "s_waitcnt lgkmcnt(6)\n"
      "v_mfma_f32_16x16x32_f16 v[204:207], v[108:111], v[216:219], v[204:207]\n"
      "ds_read_b128 v[212:215], v33 offset:3328\n"
      "v_pk_add_f16 v22, v22, v18\n"
      "v_mfma_f32_16x16x32_f16 v[208:211], v[76:79], v[216:219], v[208:211]\n"
      "v_pk_add_f16 v23, v23, v19\n"
      "s_waitcnt lgkmcnt(5)\n"
      "v_mfma_f32_16x16x32_f16 v[204:207], v[112:115], v[220:223], v[204:207]\n"
      "ds_read_b128 v[216:219], v33 offset:3392\n"
      "ds_write_b128 v37, v[20:23] offset:5152\n"
      "s_waitcnt lgkmcnt(3)\n"
      "s_barrier\n"
      "s_setprio 3\n"
      "v_mfma_f32_16x16x32_f16 v[208:211], v[80:83], v[220:223], v[208:211]\n"
      "ds_read_b64_tr_b16 v[64:65], v40 offset:1024\n"
      "ds_read_b64_tr_b16 v[66:67], v40 offset:36096\n"
      "v_lshlrev_b32 v68, 16, v56\n"
      "v_and_b32 v69, 0xffff0000, v56\n"
      "v_mfma_f32_16x16x32_f16 v[204:207], v[116:119], v[224:227], v[204:207]\n"
      "ds_read_b128 v[220:223], v33 offset:3456\n"
      "v_lshlrev_b32 v70, 16, v57\n"
      "v_and_b32 v71, 0xffff0000, v57\n"
      "v_mfma_f32_16x16x32_f16 v[208:211], v[84:87], v[224:227], v[208:211]\n"
      "v_lshlrev_b32 v26, 16, v58\n"
      "v_and_b32 v27, 0xffff0000, v58\n"
      "v_mfma_f32_16x16x32_f16 v[208:211], v[232:235], v[164:167], v[208:211]\n"
      "ds_read_b64_tr_b16 v[228:229], v35 offset:2080\n"
      "ds_read_b64_tr_b16 v[230:231], v35 offset:2064\n"
      "v_lshlrev_b32 v28, 16, v59\n"
      "v_and_b32 v29, 0xffff0000, v59\n"
      "s_waitcnt lgkmcnt(7)\n"
      "v_mfma_f32_16x16x32_f16 v[204:207], v[120:123], v[212:215], v[204:207]\n"
      "s_waitcnt lgkmcnt(4)\n"
      "v_lshlrev_b32 v56, 16, v64\n"
      "v_and_b32 v64, 0xffff0000, v64\n"
      "v_mfma_f32_16x16x32_f16 v[208:211], v[88:91], v[212:215], v[208:211]\n"
      "ds_read_b128 v[224:227], v33 offset:3520\n"
      "v_fmac_f32 v68, s72, v56\n"
      "v_fmac_f32 v69, s73, v64\n"
      "v_mfma_f32_16x16x32_f16 v[204:207], v[124:127], v[216:219], v[204:207]\n"
      "v_lshlrev_b32 v57, 16, v65\n"
      "v_and_b32 v65, 0xffff0000, v65\n"
      "v_mfma_f32_16x16x32_f16 v[208:211], v[92:95], v[216:219], v[208:211]\n"
      "ds_read_b128 v[212:215], v33 offset:3584\n"
      "v_fmac_f32 v70, s74, v57\n"
      "v_fmac_f32 v71, s75, v65\n"
      "s_setprio 2\n"
      "s_waitcnt lgkmcnt(4)\n"
      "v_mfma_f32_16x16x32_f16 v[204:207], v[128:131], v[220:223], v[204:207]\n"
      "v_lshlrev_b32 v58, 16, v66\n"
      "v_and_b32 v66, 0xffff0000, v66\n"
      "v_mfma_f32_16x16x32_f16 v[208:211], v[96:99], v[220:223], v[208:211]\n"
      "ds_read_b128 v[216:219], v33 offset:3648\n"
      "v_fmac_f32 v26, s76, v58\n"
      "v_fmac_f32 v27, s77, v66\n"
      "s_waitcnt lgkmcnt(3)\n"
      "v_mfma_f32_16x16x32_f16 v[204:207], v[228:231], v[140:143], v[204:207]\n"
      "ds_read_b64_tr_b16 v[232:233], v35 offset:5176\n"
      "ds_read_b64_tr_b16 v[234:235], v35 offset:5160\n"
      "v_lshlrev_b32 v59, 16, v67\n"
      "v_and_b32 v67, 0xffff0000, v67\n"
      "v_mfma_f32_16x16x32_f16 v[208:211], v[228:231], v[144:147], v[208:211]\n"
      "v_fmac_f32 v28, s78, v59\n"
      "v_fmac_f32 v29, s79, v67\n"
      "s_waitcnt lgkmcnt(4)\n"
      "v_mfma_f32_16x16x32_f16 v[204:207], v[132:135], v[224:227], v[204:207]\n"
      "ds_read_b128 v[220:223], v33 offset:3712\n"
      "v_cvt_pk_bf16_f32 v56, v68, v69\n"
      "v_mfma_f32_16x16x32_f16 v[208:211], v[100:103], v[224:227], v[208:211]\n"
      "ds_read_b128 v[224:227], v33 offset:3776\n"
      "v_cvt_pk_bf16_f32 v57, v70, v71\n"
      "s_waitcnt lgkmcnt(5)\n"
      "v_mfma_f32_16x16x32_f16 v[204:207], v[136:139], v[212:215], v[204:207]\n"
      "v_cvt_pk_bf16_f32 v58, v26, v27\n"
      "s_setprio 1\n"
      "v_mfma_f32_16x16x32_f16 v[208:211], v[104:107], v[212:215], v[208:211]\n"
      "ds_read_b64_tr_b16 v[228:229], v35 offset:2592\n"
      "ds_read_b64_tr_b16 v[230:231], v35 offset:2576\n"
      "ds_read_b128 v[212:215], v33 offset:3840\n"
      "v_cvt_pk_bf16_f32 v59, v28, v29\n"
      "v_lshlrev_b32 v13, 8, v12\n"
      "v_and_or_b32 v13, v13, s58, v42\n"
      "s_waitcnt lgkmcnt(7)\n"
      "v_mfma_f32_16x16x32_f16 v[208:211], v[108:111], v[216:219], v[208:211]\n"
      "ds_read_b128 v[216:219], v33 offset:3904\n"
      "s_cmp_lt_u32 s33, 4\n"
      "s_cbranch_scc0 .Lcs2%=\n"
      "buffer_store_dwordx4 v[56:59], v13, s[24:27], s56 offen\n"
      ".Lcs2%=:\n"
      "s_waitcnt lgkmcnt(6)\n"
      "v_mfma_f32_16x16x32_f16 v[204:207], v[232:235], v[152:155], v[204:207]\n"
      "v_mfma_f32_16x16x32_f16 v[208:211], v[232:235], v[156:159], v[208:211]\n"
      "ds_read_b64_tr_b16 v[232:233], v35 offset:5688\n"
      "ds_read_b64_tr_b16 v[234:235], v35 offset:5672\n"
      "s_waitcnt lgkmcnt(7)\n"
      "v_mfma_f32_16x16x32_f16 v[208:211], v[112:115], v[220:223], v[208:211]\n"
      "ds_read_b128 v[220:223], v33 offset:3968\n"
      "s_setprio 0\n"
      "s_waitcnt lgkmcnt(7)\n"
      "v_mfma_f32_16x16x32_f16 v[208:211], v[116:119], v[224:227], v[208:211]\n"
      "ds_read_b128 v[224:227], v33 offset:4032\n"
      "s_waitcnt lgkmcnt(5)\n"
      "v_mfma_f32_16x16x32_f16 v[208:211], v[120:123], v[212:215], v[208:211]\n"
      "ds_read_b128 v[212:215], v33 offset:4096\n"
      "v_cvt_pk_bf16_f32 v24, v204, v206\n"
      "v_cvt_pk_bf16_f32 v25, v205, v207\n"
      "ds_write2st64_b32 v39, v24, v25 offset0:6 offset1:14\n"
      "v_mfma_f32_16x16x32_f16 v[208:211], v[228:231], v[140:143], v[208:211]\n"
      "s_waitcnt lgkmcnt(6)\n"
      "v_mfma_f32_16x16x32_f16 v[208:211], v[124:127], v[216:219], v[208:211]\n"
      "s_waitcnt lgkmcnt(3)\n"
      "v_mfma_f32_16x16x32_f16 v[208:211], v[128:131], v[220:223], v[208:211]\n"
      "v_mfma_f32_16x16x32_f16 v[208:211], v[232:235], v[152:155], v[208:211]\n"
      "s_waitcnt lgkmcnt(2)\n"
      "v_mfma_f32_16x16x32_f16 v[208:211], v[132:135], v[224:227], v[208:211]\n"
      "s_cmp_lt_u32 s33, 4\n"
      "s_cbranch_scc1 .Lct2%=\n"
      "buffer_store_dwordx4 v[56:59], v13, s[24:27], s56 offen\n"
      ".Lct2%=:\n"
      "s_waitcnt lgkmcnt(1)\n"
      "v_mfma_f32_16x16x32_f16 v[208:211], v[136:139], v[212:215], v[208:211]\n"
      "v_lshlrev_b32 v68, 16, v60\n"
      "v_and_b32 v69, 0xffff0000, v60\n"
      "v_lshlrev_b32 v70, 16, v61\n"
      "v_and_b32 v71, 0xffff0000, v61\n"
      "v_lshlrev_b32 v26, 16, v62\n"
      "v_and_b32 v27, 0xffff0000, v62\n"
      "v_lshlrev_b32 v28, 16, v63\n"
      "v_and_b32 v29, 0xffff0000, v63\n"
      "v_cvt_pk_bf16_f32 v24, v208, v210\n"
      "v_cvt_pk_bf16_f32 v25, v209, v211\n"
      "ds_write2st64_b32 v39, v24, v25 offset0:7 offset1:15\n"
      "s_waitcnt lgkmcnt(0)\n"
      "s_barrier\n"
      "ds_read_b64_tr_b16 v[64:65], v40 offset:1536\n"
      "ds_read_b64_tr_b16 v[66:67], v40 offset:36608\n"
      "s_cmp_lg_u32 s33, 0\n"
      "s_cbranch_scc1 .Lcb%=\n"
      "ds_read_u16 v252, v8 offset:56384\n"
      "s_waitcnt lgkmcnt(0)\n"
      "v_lshlrev_b32 v252, 16, v252\n"
      "v_mul_f32 v252, v252, v15\n"
      "buffer_store_dwordx2 v[252:253], v251, s[16:19], s44 offen sc1\n"
      ".Lcb%=:\n"
      "s_waitcnt lgkmcnt(1)\n"
      "v_lshlrev_b32 v60, 16, v64\n"
      "v_and_b32 v64, 0xffff0000, v64\n"
      "v_fmac_f32 v68, s72, v60\n"
      "v_fmac_f32 v69, s73, v64\n"
      "v_lshlrev_b32 v61, 16, v65\n"
      "v_and_b32 v65, 0xffff0000, v65\n"
      "v_fmac_f32 v70, s74, v61\n"
      "v_fmac_f32 v71, s75, v65\n"
      "s_waitcnt lgkmcnt(0)\n"
      "v_lshlrev_b32 v62, 16, v66\n"
      "v_and_b32 v66, 0xffff0000, v66\n"
      "v_fmac_f32 v26, s76, v62\n"
      "v_fmac_f32 v27, s77, v66\n"
      "v_lshlrev_b32 v63, 16, v67\n"
      "v_and_b32 v67, 0xffff0000, v67\n"
      "v_fmac_f32 v28, s78, v63\n"
      "v_fmac_f32 v29, s79, v67\n"
      "v_cvt_pk_bf16_f32 v60, v68, v69\n"
      "v_cvt_pk_bf16_f32 v61, v70, v71\n"
      "v_cvt_pk_bf16_f32 v62, v26, v27\n"
      "v_cvt_pk_bf16_f32 v63, v28, v29\n"
      "v_and_or_b32 v13, v12, s58, v42\n"
      "buffer_store_dwordx4 v[60:63], v13, s[24:27], s57 offen\n"
      ".LEx%=:\ns_endpgm\n"
      ".Lcsgsk%=:\n"
      "s_sleep 4\n"
      "s_sub_u32 s59, s59, 1\n"
      "s_cbranch_scc1 .Lcsgok%=\n"
      "buffer_load_dwordx4 v[64:67], v42, s[16:19], s62 offen sc1\n"
      "s_waitcnt vmcnt(0)\n"
      "s_branch .Lcsgk%=\n"
      ".Lcsgsh%=:\n"
      "s_sleep 4\n"
      "s_sub_u32 s59, s59, 1\n"
      "s_cbranch_scc1 .Lcsgoh%=\n"
      "buffer_load_dwordx4 v[68:71], v42, s[16:19], s63 offen sc1\n"
      "s_waitcnt vmcnt(0)\n"
      "s_branch .Lcsgh%=\n"
      ".Lch%=:\n"
      "s_movk_i32 s59, 0x7fff\n"
      ".Lchl%=:\n"
      "s_waitcnt vmcnt(0)\n"
      "v_cmp_ne_u32 vcc, s15, v253\n"
      "v_not_b32 v255, v252\n"
      "s_sub_u32 s59, s59, 1\n"
      "s_cbranch_scc1 .Lchk%=\n"
      "s_cbranch_vccz .Lchk%=\n"
      "s_sleep 1\n"
      "buffer_load_dwordx2 v[252:253], v9, s[16:19], s43 offen sc1\n"
      "s_branch .Lchl%=\n"
      ".Lchk%=:\n"
      "v_and_b32 v31, 0x1ff, v255\n"
      "v_bfe_u32 v8, v255, 9, 2\n"
      "v_lshl_add_u32 v31, v31, 2, v8\n"
      "v_mov_b32 v8, 64\n"
      "v_add_u32 v31, 0x27700, v31\n"
      "ds_write_b8 v31, v8 \n"
      "v_and_b32 v31, 1, v255\n"
      "v_and_b32 v8, 0x7fe, v255\n"
      "v_lshl_add_u32 v8, v31, 11, v8\n"
      "v_mul_u32_u24 v31, 0x1120, v10\n"
      "v_add_u32 v8, v8, v31\n"
      "v_lshlrev_b32 v31, 1, v10\n"
      "v_add_u32 v31, 0x27600, v31\n"
      "ds_read_b32 v15, v31 \n"
      "v_lshlrev_b32 v251, 3, v43\n"
      "v_mov_b32 v253, s15\n"
      "s_waitcnt lgkmcnt(0)\n"
      "s_branch .Lcho%=\n"
      ".LG%=:\n"
      "v_and_b32 v52, 48, v43\n"
      "v_and_b32 v53, 3, v43\n"
      "v_lshl_add_u32 v53, v53, 2, v52\n"
      "v_and_b32 v54, 12, v43\n"
      "v_lshlrev_b32 v53, 2, v53\n"
      "v_lshl_add_u32 v52, v54, 9, v53\n"
      "s_and_b32 s53, s34, 127\n"
      "s_and_b32 s52, s33, 3\n"
      "s_lshl2_add_u32 s14, s53, s52\n"
      "s_lshl_b32 s51, s14, 13\n"
      "v_lshl_add_u32 v53, v54, 6, v53\n"
      "s_waitcnt lgkmcnt(0)\n"
      "s_add_u32 s52, s64, s51\n"
      "s_addc_u32 s53, s65, 0\n"
      "global_load_dwordx4 v[192:195], v52, s[52:53] offset:0\n"
      "global_load_dwordx4 v[196:199], v52, s[52:53] offset:256\n"
      "global_load_dwordx4 v[224:227], v53, s[78:79] offset:0\n"
      "global_load_dwordx4 v[228:231], v53, s[78:79] offset:1024\n"
      "global_load_dwordx4 v[200:203], v52, s[52:53] offset:512\n"
      "global_load_dwordx4 v[204:207], v52, s[52:53] offset:768\n"
      "global_load_dwordx4 v[208:211], v52, s[52:53] offset:1024\n"
      "global_load_dwordx4 v[212:215], v52, s[52:53] offset:1280\n"
      "global_load_dwordx4 v[216:219], v52, s[52:53] offset:1536\n"
      "global_load_dwordx4 v[220:223], v52, s[52:53] offset:1792\n"
      "s_load_dword s15, s[88:89], 0x0\n"
      "s_setprio 3\n"
      "s_mul_i32 s35, s48, 14\n"
      "s_add_u32 s35, s35, s49\n"
      "s_and_b32 s55, s33, 3\n"
      "s_mul_i32 s50, s55, 1835008\n"
      "s_lshr_b32 s51, s35, 1\n"
      "s_lshl_b32 s51, s51, 7\n"
      "s_add_u32 s50, s50, s51\n"
      "v_lshrrev_b32 v37, 3, v43\n"
      "v_and_b32 v38, 7, v43\n"
      "v_mul_u32_u24 v37, 0xe000, v37\n"
      "v_lshlrev_b32 v38, 4, v38\n"
      "v_add3_u32 v32, v37, v38, s50\n"
      "s_cmp_lt_u32 s34, 112\n"
      "s_cselect_b32 s26, 0x700000, 0\n"
      "s_mov_b32 s27, 0x20000\n"
      "s_bitcmp1_b32 s49, 0\n"
      "s_cselect_b32 s24, s82, s80\n"
      "s_cselect_b32 s25, s83, s81\n"
      "s_and_b32 s25, s25, 0xffff\n"
      "s_mov_b32 s16, s88\n"
      "s_and_b32 s17, s89, 0xffff\n"
      "s_mov_b32 s18, 0x33c000\n"
      "s_mov_b32 s19, 0x20000\n"
      "v_mov_b32 v20, 0x3f803f80\n"
      "v_mov_b32 v21, 0x3f803f80\n"
      "v_mov_b32 v22, 0x3f803f80\n"
      "v_mov_b32 v23, 0x3f803f80\n"
      "v_mov_b32 v44, 0x7f000000\n"
      "v_mov_b32 v25, 0\n"
      "s_lshl_b32 s68, s14, 5\n"
      "s_add_u32 s68, s68, 8192\n"
      "s_lshl_b32 s69, s14, 4\n"
      "s_add_u32 s69, s69, 24576\n"
      "v_writelane_b32 v44, s68, 0\n"
      "s_add_u32 s68, s68, 8\n"
      "v_writelane_b32 v44, s68, 20\n"
      "s_add_u32 s68, s68, 8\n"
      "v_writelane_b32 v44, s68, 40\n"
      "s_add_u32 s68, s68, 8\n"
      "v_writelane_b32 v44, s68, 60\n"
      "s_add_u32 s68, s68, 8\n"
      "v_writelane_b32 v44, s69, 1\n"
      "s_add_u32 s69, s69, 8\n"
      "v_writelane_b32 v44, s69, 2\n"
      "s_waitcnt lgkmcnt(0)\n"
      "s_add_u32 s15, s15, 0x9e3779b1\n"
      "v_mov_b32 v47, s15\n"
      "s_waitcnt vmcnt(9)\n"
      "v_mfma_f32_16x16x32_bf16 v[248:251], v[192:195], v[20:23], 0\n"
      "v_mfma_f32_16x16x32_bf16 v[252:255], v[192:195], v[192:195], 0\n"
      "s_waitcnt vmcnt(8)\n"
      "v_mfma_f32_16x16x32_bf16 v[248:251], v[196:199], v[20:23], v[248:251]\n"
      "v_mfma_f32_16x16x32_bf16 v[252:255], v[196:199], v[196:199], v[252:255]\n"
      "s_waitcnt vmcnt(7)\n"
      "v_mfma_f32_16x16x32_bf16 v[16:19], v[20:23], v[224:227], 0\n"
      "s_waitcnt vmcnt(6)\n"
      "v_mfma_f32_16x16x32_bf16 v[16:19], v[20:23], v[228:231], v[16:19]\n"
      "v_mfma_f32_16x16x32_bf16 v[232:235], v[192:195], v[224:227], 0\n"
      "v_mfma_f32_16x16x32_bf16 v[236:239], v[196:199], v[224:227], 0\n"
      "s_waitcnt vmcnt(5)\n"
      "v_mfma_f32_16x16x32_bf16 v[240:243], v[200:203], v[224:227], 0\n"
      "v_mfma_f32_16x16x32_bf16 v[252:255], v[200:203], v[200:203], v[252:255]\n"
      "v_mfma_f32_16x16x32_bf16 v[248:251], v[200:203], v[20:23], v[248:251]\n"
      "s_waitcnt vmcnt(4)\n"
      "v_mfma_f32_16x16x32_bf16 v[244:247], v[204:207], v[224:227], 0\n"
      "v_mfma_f32_16x16x32_bf16 v[252:255], v[204:207], v[204:207], v[252:255]\n"
      "v_mfma_f32_16x16x32_bf16 v[248:251], v[204:207], v[20:23], v[248:251]\n"
      "s_waitcnt vmcnt(3)\n"
      "v_mfma_f32_16x16x32_bf16 v[232:235], v[208:211], v[228:231], v[232:235]\n"
      "v_mfma_f32_16x16x32_bf16 v[252:255], v[208:211], v[208:211], v[252:255]\n"
      "v_mfma_f32_16x16x32_bf16 v[248:251], v[208:211], v[20:23], v[248:251]\n"
      "v_add_f32_dpp v16, v16, v16 quad_perm:[1,0,3,2] row_mask:0xf bank_mask:0xf\n"
      "s_waitcnt vmcnt(2)\n"
      "v_mfma_f32_16x16x32_bf16 v[236:239], v[212:215], v[228:231], v[236:239]\n"
      "v_mfma_f32_16x16x32_bf16 v[252:255], v[212:215], v[212:215], v[252:255]\n"
      "v_mfma_f32_16x16x32_bf16 v[248:251], v[212:215], v[20:23], v[248:251]\n"
      "s_mov_b32 s60, 0\n"
      "buffer_load_dwordx4 v[64:67], v32, s[24:27], s60 offen\n"
      "v_add_f32_dpp v16, v16, v16 quad_perm:[2,3,0,1] row_mask:0xf bank_mask:0xf\n"
      "v_add_f32_dpp v25, v232, v25 quad_perm:[0,0,0,0] row_mask:0xf bank_mask:0x1\n"
      "s_nop 1\n"
      "v_add_f32_dpp v25, v233, v25 quad_perm:[1,1,1,1] row_mask:0xf bank_mask:0x1\n"
      "s_nop 1\n"
      "v_add_f32_dpp v25, v234, v25 quad_perm:[2,2,2,2] row_mask:0xf bank_mask:0x1\n"
      "s_nop 1\n"
      "v_add_f32_dpp v25, v235, v25 quad_perm:[3,3,3,3] row_mask:0xf bank_mask:0x1\n"
      "s_waitcnt vmcnt(2)\n"
      "v_mfma_f32_16x16x32_bf16 v[240:243], v[216:219], v[228:231], v[240:243]\n"
      "v_mfma_f32_16x16x32_bf16 v[252:255], v[216:219], v[216:219], v[252:255]\n"
      "v_mfma_f32_16x16x32_bf16 v[248:251], v[216:219], v[20:23], v[248:251]\n"
      "s_mov_b32 s60, 7168\n"
      "buffer_load_dwordx4 v[68:71], v32, s[24:27], s60 offen\n"
      "v_add_f32_dpp v16, v16, v16 row_ror:4 row_mask:0xf bank_mask:0xf\n"
      "v_add_f32_dpp v25, v236, v25 quad_perm:[0,0,0,0] row_mask:0xf bank_mask:0x2\n"
      "s_nop 1\n"
      "v_add_f32_dpp v25, v237, v25 quad_perm:[1,1,1,1] row_mask:0xf bank_mask:0x2\n"
      "s_nop 1\n"
      "v_add_f32_dpp v25, v238, v25 quad_perm:[2,2,2,2] row_mask:0xf bank_mask:0x2\n"
      "s_nop 1\n"
      "v_add_f32_dpp v25, v239, v25 quad_perm:[3,3,3,3] row_mask:0xf bank_mask:0x2\n"
      "s_waitcnt vmcnt(2)\n"
      "v_mfma_f32_16x16x32_bf16 v[244:247], v[220:223], v[228:231], v[244:247]\n"
      "v_mfma_f32_16x16x32_bf16 v[252:255], v[220:223], v[220:223], v[252:255]\n"
      "v_mfma_f32_16x16x32_bf16 v[248:251], v[220:223], v[20:23], v[248:251]\n"
      "s_mov_b32 s60, 14336\n"
      "buffer_load_dwordx4 v[72:75], v32, s[24:27], s60 offen\n"
      "v_add_f32_dpp v16, v16, v16 row_ror:8 row_mask:0xf bank_mask:0xf\n"
      "v_add_f32_dpp v25, v240, v25 quad_perm:[0,0,0,0] row_mask:0xf bank_mask:0x4\n"
      "s_nop 1\n"
      "v_add_f32_dpp v25, v241, v25 quad_perm:[1,1,1,1] row_mask:0xf bank_mask:0x4\n"
      "s_nop 1\n"
      "v_add_f32_dpp v25, v242, v25 quad_perm:[2,2,2,2] row_mask:0xf bank_mask:0x4\n"
      "s_nop 1\n"
      "v_add_f32_dpp v25, v243, v25 quad_perm:[3,3,3,3] row_mask:0xf bank_mask:0x4\n"
      "s_nop 1\n"
      "v_add_f32_dpp v25, v244, v25 quad_perm:[0,0,0,0] row_mask:0xf bank_mask:0x8\n"
      "v_mov_b32_dpp v24, v252 quad_perm:[0,0,0,0] row_mask:0xf bank_mask:0xf\n"
      "v_mov_b32_dpp v27, v254 quad_perm:[2,2,2,2] row_mask:0xf bank_mask:0xf\n"
      "v_add_f32_dpp v25, v245, v25 quad_perm:[1,1,1,1] row_mask:0xf bank_mask:0x8\n"
      "v_add_f32 v248, v248, v249\n"
      "v_add_f32 v250, v250, v251\n"
      "v_add_f32_dpp v24, v253, v24 quad_perm:[1,1,1,1] row_mask:0xf bank_mask:0xf\n"
      "v_add_f32_dpp v27, v255, v27 quad_perm:[3,3,3,3] row_mask:0xf bank_mask:0xf\n"
      "v_add_f32_dpp v25, v246, v25 quad_perm:[2,2,2,2] row_mask:0xf bank_mask:0x8\n"
      "v_add_f32 v248, v248, v250\n"
      "v_add_f32 v24, v24, v27\n"
      "v_add_f32_dpp v25, v247, v25 quad_perm:[3,3,3,3] row_mask:0xf bank_mask:0x8\n"
      "v_mul_f32 v248, 0x3a800000, v248\n"
      "v_mul_f32 v24, 0x3a800000, v24\n"
      "v_add_f32_dpp v25, v25, v25 row_ror:4 row_mask:0xf bank_mask:0xf\n"
      "v_fma_f32 v24, -v248, v248, v24\n"
      "v_max_f32 v24, 0, v24\n"
      "v_add_f32_dpp v25, v25, v25 row_ror:8 row_mask:0xf bank_mask:0xf\n"
      "v_cvt_pk_bf16_f32 v248, v248, v24\n"
      "v_and_b32 v24, 0xffff0000, v248\n"
      "v_add_f32 v24, 0x3727c5ac, v24\n"
      "v_lshlrev_b32 v26, 16, v248\n"
      "v_cvt_pk_bf16_f32 v24, v24, v24\n"
      "v_fma_f32 v25, -v26, v16, v25\n"
      "v_and_b32 v24, 0xffff0000, v24\n"
      "v_sqrt_f32 v24, v24\n"
      "s_nop 0\n"
      "v_cvt_pk_bf16_f32 v46, v26, v24\n"
      "v_and_b32 v24, 0xffff0000, v46\n"
      "v_rcp_f32 v24, v24\n"
      "s_nop 0\n"
      "v_mul_f32 v25, v25, v24\n"
      "v_cvt_pk_bf16_f32 v25, v25, v25\n"
      "s_nop 0\n"
      "v_readlane_b32 s68, v25, 0\n"
      "v_readlane_b32 s69, v25, 20\n"
      "v_readlane_b32 s70, v25, 40\n"
      "v_readlane_b32 s71, v25, 60\n"
      "s_pack_ll_b32_b16 s68, s68, s69\n"
      "s_pack_ll_b32_b16 s70, s70, s71\n"
      "v_writelane_b32 v46, s68, 1\n"
      "v_writelane_b32 v46, s70, 2\n"
      "buffer_store_dwordx2 v[46:47], v44, s[16:19], 0 offen sc1\n"
      "s_cmp_ge_u32 s34, 112\n"
      "s_cbranch_scc1 .LT%=\n"
      "s_mov_b32 s60, 21504\n"
      "buffer_load_dwordx4 v[76:79], v32, s[24:27], s60 offen\n"
      "s_mov_b32 s60, 28672\n"
      "buffer_load_dwordx4 v[80:83], v32, s[24:27], s60 offen\n"
      "s_mov_b32 s60, 35840\n"
      "buffer_load_dwordx4 v[84:87], v32, s[24:27], s60 offen\n"
      "s_mov_b32 s60, 43008\n"
      "buffer_load_dwordx4 v[88:91], v32, s[24:27], s60 offen\n"
      "s_mov_b32 s60, 50176\n"
      "buffer_load_dwordx4 v[92:95], v32, s[24:27], s60 offen\n"
      "s_mov_b32 s62, 0x05040100\n"
      "s_mov_b32 s63, 0x07060302\n"
      "s_lshl_b32 s42, s34, 3\n"
      "s_add_u32 s42, s42, 4096\n"
      "s_lshl_b32 s43, s48, 8\n"
      "s_and_b32 s50, s49, 1\n"
      "s_lshl_b32 s50, s50, 7\n"
      "s_add_u32 s43, s43, s50\n"
      "s_add_u32 s43, s43, 40960\n"
      "s_mov_b32 s20, s64\n"
      "s_and_b32 s21, s65, 0xffff\n"
      "s_mov_b32 s22, 0x400000\n"
      "s_mov_b32 s23, 0x20000\n"
      "v_lshrrev_b32 v39, 4, v43\n"
      "v_and_b32 v38, 15, v43\n"
      "v_lshlrev_b32 v38, 4, v38\n"
      "v_bfe_u32 v33, v43, 2, 2\n"
      "v_and_b32 v37, 1, v43\n"
      "v_lshl_add_u32 v33, v33, 1, v37\n"
      "v_lshlrev_b32 v33, 3, v33\n"
      "v_and_b32 v37, 2, v43\n"
      "v_lshl_add_u32 v37, v39, 2, v37\n"
      "s_lshl_b32 s50, s55, 9\n"
      "v_lshl_add_u32 v36, v37, 3, s50\n"
      "s_lshl_b32 s51, s55, 4\n"
      "v_lshrrev_b32 v40, 5, v43\n"
      "v_lshl_add_u32 v40, v40, 2, s51\n"
      "s_lshl_b32 s51, s55, 2\n"
      "s_add_u32 s51, s51, 140800\n"
      "v_mov_b32 v41, s51\n"
      "v_mov_b32 v42, 0x22600\n"
      "v_lshl_add_u32 v37, v43, 3, s50\n"
      "v_add_u32 v35, 0x22700, v37\n"
      "s_lshl_b32 s50, s55, 11\n"
      "s_add_u32 s50, s50, 141056\n"
      "v_lshl_add_u32 v34, v39, 9, v38\n"
      "v_add_u32 v34, s50, v34\n"
      "s_lshl_b32 s45, s35, 11\n"
      "s_add_u32 s45, s45, 81920\n"
      "s_mov_b32 s56, 0x3f80\n"
      "s_mov_b32 s57, 0x3f800000\n"
      "s_movk_i32 s61, 0x7ff\n"
      "s_movk_i32 s58, 8192\n"
      "s_mov_b32 s54, 0\n"
      "s_waitcnt vmcnt(8)\n"
      "s_nop 0\n"
      "s_mov_b32 s60, 458752\n"
      "buffer_load_dwordx4 v[96:99], v32, s[24:27], s60 offen\n"
      "s_waitcnt vmcnt(8)\n"
      "s_nop 0\n"
      "s_mov_b32 s60, 465920\n"
      "buffer_load_dwordx4 v[100:103], v32, s[24:27], s60 offen\n"
      "v_perm_b32 v192, v68, v64, s62\n"
      "v_perm_b32 v196, v68, v64, s63\n"
      "v_perm_b32 v200, v69, v65, s62\n"
      "v_perm_b32 v204, v69, v65, s63\n"
      "v_perm_b32 v208, v70, v66, s62\n"
      "v_perm_b32 v212, v70, v66, s63\n"
      "v_perm_b32 v216, v71, v67, s62\n"
      "v_perm_b32 v220, v71, v67, s63\n"
      "s_waitcnt vmcnt(8)\n"
      "s_nop 0\n"
      "s_mov_b32 s60, 473088\n"
      "buffer_load_dwordx4 v[104:107], v32, s[24:27], s60 offen\n"
      "s_waitcnt vmcnt(7)\n"
      "s_nop 0\n"
      "s_mov_b32 s60, 480256\n"
      "buffer_load_dwordx4 v[108:111], v32, s[24:27], s60 offen\n"
      "v_perm_b32 v193, v76, v72, s62\n"
      "v_perm_b32 v197, v76, v72, s63\n"
      "v_perm_b32 v201, v77, v73, s62\n"
      "v_perm_b32 v205, v77, v73, s63\n"
      "v_perm_b32 v209, v78, v74, s62\n"
      "v_perm_b32 v213, v78, v74, s63\n"
      "v_perm_b32 v217, v79, v75, s62\n"
      "v_perm_b32 v221, v79, v75, s63\n"
      "s_waitcnt vmcnt(7)\n"
      "s_nop 0\n"
      "s_mov_b32 s60, 487424\n"
      "buffer_load_dwordx4 v[112:115], v32, s[24:27], s60 offen\n"
      "s_waitcnt vmcnt(7)\n"
      "s_nop 0\n"
      "s_mov_b32 s60, 494592\n"
      "buffer_load_dwordx4 v[116:119], v32, s[24:27], s60 offen\n"
      "v_perm_b32 v194, v84, v80, s62\n"
      "v_perm_b32 v198, v84, v80, s63\n"
      "v_perm_b32 v202, v85, v81, s62\n"
      "v_perm_b32 v206, v85, v81, s63\n"
      "v_perm_b32 v210, v86, v82, s62\n"
      "v_perm_b32 v214, v86, v82, s63\n"
      "v_perm_b32 v218, v87, v83, s62\n"
      "v_perm_b32 v222, v87, v83, s63\n"
      "s_waitcnt vmcnt(7)\n"
      "s_nop 0\n"
      "s_mov_b32 s60, 501760\n"
      "buffer_load_dwordx4 v[120:123], v32, s[24:27], s60 offen\n"
      "s_waitcnt vmcnt(7)\n"
      "s_nop 0\n"
      "s_mov_b32 s60, 508928\n"
      "buffer_load_dwordx4 v[124:127], v32, s[24:27], s60 offen\n"
      "v_perm_b32 v195, v92, v88, s62\n"
      "v_perm_b32 v199, v92, v88, s63\n"
      "v_perm_b32 v203, v93, v89, s62\n"
      "v_perm_b32 v207, v93, v89, s63\n"
      "v_perm_b32 v211, v94, v90, s62\n"
      "v_perm_b32 v215, v94, v90, s63\n"
      "v_perm_b32 v219, v95, v91, s62\n"
      "v_perm_b32 v223, v95, v91, s63\n"
      "s_waitcnt vmcnt(7)\n"
      "s_nop 0\n"
      "s_mov_b32 s60, 917504\n"
      "buffer_load_dwordx4 v[128:131], v32, s[24:27], s60 offen\n"
      "s_waitcnt vmcnt(7)\n"
      "s_nop 0\n"
      "s_mov_b32 s60, 924672\n"
      "buffer_load_dwordx4 v[132:135], v32, s[24:27], s60 offen\n"
      "v_perm_b32 v64, v100, v96, s62\n"
      "v_perm_b32 v68, v100, v96, s63\n"
      "v_perm_b32 v72, v101, v97, s62\n"
      "v_perm_b32 v76, v101, v97, s63\n"
      "v_perm_b32 v80, v102, v98, s62\n"
      "v_perm_b32 v84, v102, v98, s63\n"
      "v_perm_b32 v88, v103, v99, s62\n"
      "v_perm_b32 v92, v103, v99, s63\n"
      "s_waitcnt vmcnt(7)\n"
      "s_nop 0\n"
      "s_mov_b32 s60, 931840\n"
      "buffer_load_dwordx4 v[136:139], v32, s[24:27], s60 offen\n"
      "s_waitcnt vmcnt(7)\n"
      "s_nop 0\n"
      "s_mov_b32 s60, 939008\n"
      "buffer_load_dwordx4 v[140:143], v32, s[24:27], s60 offen\n"
      "v_perm_b32 v65, v108, v104, s62\n"
      "v_perm_b32 v69, v108, v104, s63\n"
      "v_perm_b32 v73, v109, v105, s62\n"
      "v_perm_b32 v77, v109, v105, s63\n"
      "v_perm_b32 v81, v110, v106, s62\n"
      "v_perm_b32 v85, v110, v106, s63\n"
      "v_perm_b32 v89, v111, v107, s62\n"
      "v_perm_b32 v93, v111, v107, s63\n"
      "s_waitcnt vmcnt(7)\n"
      "s_nop 0\n"
      "s_mov_b32 s60, 946176\n"
      "buffer_load_dwordx4 v[144:147], v32, s[24:27], s60 offen\n"
      "s_waitcnt vmcnt(7)\n"
      "s_nop 0\n"
      "s_mov_b32 s60, 953344\n"
      "buffer_load_dwordx4 v[148:151], v32, s[24:27], s60 offen\n"
      "v_perm_b32 v66, v116, v112, s62\n"
      "v_perm_b32 v70, v116, v112, s63\n"
      "v_perm_b32 v74, v117, v113, s62\n"
      "v_perm_b32 v78, v117, v113, s63\n"
      "v_perm_b32 v82, v118, v114, s62\n"
      "v_perm_b32 v86, v118, v114, s63\n"
      "v_perm_b32 v90, v119, v115, s62\n"
      "v_perm_b32 v94, v119, v115, s63\n"
      "s_waitcnt vmcnt(7)\n"
      "s_nop 0\n"
      "s_mov_b32 s60, 960512\n"
      "buffer_load_dwordx4 v[152:155], v32, s[24:27], s60 offen\n"
      "s_waitcnt vmcnt(7)\n"
      "s_nop 0\n"
      "s_mov_b32 s60, 967680\n"
      "buffer_load_dwordx4 v[156:159], v32, s[24:27], s60 offen\n"
      "v_perm_b32 v67, v124, v120, s62\n"
      "v_perm_b32 v71, v124, v120, s63\n"
      "v_perm_b32 v75, v125, v121, s62\n"
      "v_perm_b32 v79, v125, v121, s63\n"
      "v_perm_b32 v83, v126, v122, s62\n"
      "v_perm_b32 v87, v126, v122, s63\n"
      "v_perm_b32 v91, v127, v123, s62\n"
      "v_perm_b32 v95, v127, v123, s63\n"
      "s_waitcnt vmcnt(7)\n"
      "s_nop 0\n"
      "s_mov_b32 s60, 1376256\n"
      "buffer_load_dwordx4 v[160:163], v32, s[24:27], s60 offen\n"
      "s_waitcnt vmcnt(7)\n"
      "s_nop 0\n"
      "s_mov_b32 s60, 1383424\n"
      "buffer_load_dwordx4 v[164:167], v32, s[24:27], s60 offen\n"
      "v_perm_b32 v96, v132, v128, s62\n"
      "v_perm_b32 v100, v132, v128, s63\n"
      "v_perm_b32 v104, v133, v129, s62\n"
      "v_perm_b32 v108, v133, v129, s63\n"
      "v_perm_b32 v112, v134, v130, s62\n"
      "v_perm_b32 v116, v134, v130, s63\n"
      "v_perm_b32 v120, v135, v131, s62\n"
      "v_perm_b32 v124, v135, v131, s63\n"
      "s_waitcnt vmcnt(7)\n"
      "s_nop 0\n"
      "s_mov_b32 s60, 1390592\n"
      "buffer_load_dwordx4 v[168:171], v32, s[24:27], s60 offen\n"
      "s_waitcnt vmcnt(7)\n"
      "s_nop 0\n"
      "s_mov_b32 s60, 1397760\n"
      "buffer_load_dwordx4 v[172:175], v32, s[24:27], s60 offen\n"
      "v_perm_b32 v97, v140, v136, s62\n"
      "v_perm_b32 v101, v140, v136, s63\n"
      "v_perm_b32 v105, v141, v137, s62\n"
      "v_perm_b32 v109, v141, v137, s63\n"
      "v_perm_b32 v113, v142, v138, s62\n"
      "v_perm_b32 v117, v142, v138, s63\n"
      "v_perm_b32 v121, v143, v139, s62\n"
      "v_perm_b32 v125, v143, v139, s63\n"
      "s_waitcnt vmcnt(7)\n"
      "s_nop 0\n"
      "s_mov_b32 s60, 1404928\n"
      "buffer_load_dwordx4 v[176:179], v32, s[24:27], s60 offen\n"
      "s_waitcnt vmcnt(7)\n"
      "s_nop 0\n"
      "s_mov_b32 s60, 1412096\n"
      "buffer_load_dwordx4 v[180:183], v32, s[24:27], s60 offen\n"
      "v_perm_b32 v98, v148, v144, s62\n"
      "v_perm_b32 v102, v148, v144, s63\n"
      "v_perm_b32 v106, v149, v145, s62\n"
      "v_perm_b32 v110, v149, v145, s63\n"
      "v_perm_b32 v114, v150, v146, s62\n"
      "v_perm_b32 v118, v150, v146, s63\n"
      "v_perm_b32 v122, v151, v147, s62\n"
      "v_perm_b32 v126, v151, v147, s63\n"
      "s_waitcnt vmcnt(7)\n"
      "s_nop 0\n"
      "s_mov_b32 s60, 1419264\n"
      "buffer_load_dwordx4 v[184:187], v32, s[24:27], s60 offen\n"
      "s_waitcnt vmcnt(7)\n"
      "s_nop 0\n"
      "s_mov_b32 s60, 1426432\n"
      "buffer_load_dwordx4 v[188:191], v32, s[24:27], s60 offen\n"
      "v_perm_b32 v99, v156, v152, s62\n"
      "v_perm_b32 v103, v156, v152, s63\n"
      "v_perm_b32 v107, v157, v153, s62\n"
      "v_perm_b32 v111, v157, v153, s63\n"
      "v_perm_b32 v115, v158, v154, s62\n"
      "v_perm_b32 v119, v158, v154, s63\n"
      "v_perm_b32 v123, v159, v155, s62\n"
      "v_perm_b32 v127, v159, v155, s63\n"
      "s_waitcnt vmcnt(7)\n"
      "s_nop 0\n"
      "s_waitcnt vmcnt(6)\n"
      "s_nop 0\n"
      "v_perm_b32 v128, v164, v160, s62\n"
      "v_perm_b32 v132, v164, v160, s63\n"
      "v_perm_b32 v136, v165, v161, s62\n"
      "v_perm_b32 v140, v165, v161, s63\n"
      "v_perm_b32 v144, v166, v162, s62\n"
      "v_perm_b32 v148, v166, v162, s63\n"
      "v_perm_b32 v152, v167, v163, s62\n"
      "v_perm_b32 v156, v167, v163, s63\n"
      "s_waitcnt vmcnt(5)\n"
      "s_nop 0\n"
      "s_waitcnt vmcnt(4)\n"
      "s_nop 0\n"
      "buffer_load_dwordx2 v[8:9], v33, s[16:19], s43 offen sc1\n"
      "v_perm_b32 v129, v172, v168, s62\n"
      "v_perm_b32 v133, v172, v168, s63\n"
      "v_perm_b32 v137, v173, v169, s62\n"
      "v_perm_b32 v141, v173, v169, s63\n"
      "v_perm_b32 v145, v174, v170, s62\n"
      "v_perm_b32 v149, v174, v170, s63\n"
      "v_perm_b32 v153, v175, v171, s62\n"
      "v_perm_b32 v157, v175, v171, s63\n"
      "s_waitcnt vmcnt(3)\n"
      "s_nop 0\n"
      "s_waitcnt vmcnt(2)\n"
      "s_nop 0\n"
      "v_perm_b32 v130, v180, v176, s62\n"
      "v_perm_b32 v134, v180, v176, s63\n"
      "v_perm_b32 v138, v181, v177, s62\n"
      "v_perm_b32 v142, v181, v177, s63\n"
      "v_perm_b32 v146, v182, v178, s62\n"
      "v_perm_b32 v150, v182, v178, s63\n"
      "v_perm_b32 v154, v183, v179, s62\n"
      "v_perm_b32 v158, v183, v179, s63\n"
      "s_waitcnt vmcnt(1)\n"
      "s_nop 0\n"
      "s_waitcnt vmcnt(0)\n"
      "s_nop 0\n"
      "v_perm_b32 v131, v188, v184, s62\n"
      "v_perm_b32 v135, v188, v184, s63\n"
      "v_perm_b32 v139, v189, v185, s62\n"
      "v_perm_b32 v143, v189, v185, s63\n"
      "v_perm_b32 v147, v190, v186, s62\n"
      "v_perm_b32 v151, v190, v186, s63\n"
      "v_perm_b32 v155, v191, v187, s62\n"
      "v_perm_b32 v159, v191, v187, s63\n"
      "s_cmp_lg_u32 s54, 0\n"
      "s_cbranch_scc1 .Lghx%=\n"
      "s_movk_i32 s59, 0x3fff\n"
      "buffer_load_dwordx2 v[12:13], v33, s[16:19], s43 offen sc1\n"
      ".Lgh%=:\n"
      "s_waitcnt vmcnt(1)\n"
      "v_cmp_ne_u32 vcc, s15, v9\n"
      "v_bitop3_b32 v10, v8, s61, v8 bitop3:0x0c\n"
      "s_nop 0\n"
      "s_cbranch_vccz .Lgho%=\n"
      "s_sleep 2\n"
      "buffer_load_dwordx2 v[8:9], v33, s[16:19], s43 offen sc1\n"
      "s_waitcnt vmcnt(1)\n"
      "v_cmp_ne_u32 vcc, s15, v13\n"
      "v_bitop3_b32 v10, v12, s61, v12 bitop3:0x0c\n"
      "s_nop 0\n"
      "s_cbranch_vccz .Lgho%=\n"
      "s_sleep 2\n"
      "buffer_load_dwordx2 v[12:13], v33, s[16:19], s43 offen sc1\n"
      "s_sub_u32 s59, s59, 1\n"
      "s_cbranch_scc0 .Lgh%=\n"
      ".Lgho%=:\n"
      "v_lshl_add_u32 v11, v10, 11, v36\n"
      "v_lshlrev_b32 v45, 3, v10\n"
      "buffer_load_dwordx2 v[62:63], v45, s[16:19], s58 offen sc1\n"
      "buffer_load_dwordx4 v[16:19], v11, s[20:23], 0 offen offset:0\n"
      "buffer_load_dwordx4 v[20:23], v11, s[20:23], 0 offen offset:128\n"
      "buffer_load_dwordx4 v[24:27], v11, s[20:23], 0 offen offset:256\n"
      "buffer_load_dwordx4 v[28:31], v11, s[20:23], 0 offen offset:384\n"
      "ds_write_b32 v41, v47 offset:64\n"
      ".Lghx%=:\n"
      "s_movk_i32 s59, 0x7fff\n"
      "s_waitcnt vmcnt(4)\n"
      "s_nop 0\n"
      ".Lgs%=:\n"
      "v_cmp_ne_u32 vcc, s15, v63\n"
      "v_lshlrev_b32 v10, 16, v62\n"
      "v_and_b32 v44, 0xffff0000, v62\n"
      "s_cbranch_vccnz .Lgss%=\n"
      ".Lgso%=:\n"
      "v_rcp_f32 v44, v44\n"
      "v_xor_b32 v10, 0x80000000, v10\n"
      "s_waitcnt vmcnt(3)\n"
      "v_dot2_f32_bf16 v168, v16, s56, v10\n"
      "v_dot2_f32_bf16 v169, v16, s57, v10\n"
      "v_dot2_f32_bf16 v170, v17, s56, v10\n"
      "v_dot2_f32_bf16 v171, v17, s57, v10\n"
      "v_dot2_f32_bf16 v172, v18, s56, v10\n"
      "v_dot2_f32_bf16 v173, v18, s57, v10\n"
      "v_dot2_f32_bf16 v174, v19, s56, v10\n"
      "v_dot2_f32_bf16 v175, v19, s57, v10\n"
      "v_cvt_pk_bf16_f32 v16, v168, v169\n"
      "v_cvt_pk_bf16_f32 v17, v170, v171\n"
      "v_cvt_pk_bf16_f32 v18, v172, v173\n"
      "v_cvt_pk_bf16_f32 v19, v174, v175\n"
      "ds_bpermute_b32 v44, v40, v44\n"
      "s_nop 0\n"
      "v_mfma_f32_16x16x32_bf16 v[224:227], v[16:19], v[192:195], 0\n"
      "s_waitcnt vmcnt(2)\n"
      "v_dot2_f32_bf16 v176, v20, s56, v10\n"
      "v_dot2_f32_bf16 v177, v20, s57, v10\n"
      "v_mfma_f32_16x16x32_bf16 v[228:231], v[16:19], v[196:199], 0\n"
      "v_dot2_f32_bf16 v178, v21, s56, v10\n"
      "v_dot2_f32_bf16 v179, v21, s57, v10\n"
      "v_mfma_f32_16x16x32_bf16 v[232:235], v[16:19], v[200:203], 0\n"
      "v_dot2_f32_bf16 v180, v22, s56, v10\n"
      "v_dot2_f32_bf16 v181, v22, s57, v10\n"
      "v_mfma_f32_16x16x32_bf16 v[236:239], v[16:19], v[204:207], 0\n"
      "v_dot2_f32_bf16 v182, v23, s56, v10\n"
      "v_dot2_f32_bf16 v183, v23, s57, v10\n"
      "v_mfma_f32_16x16x32_bf16 v[240:243], v[16:19], v[208:211], 0\n"
      "v_cvt_pk_bf16_f32 v20, v176, v177\n"
      "v_cvt_pk_bf16_f32 v21, v178, v179\n"
      "v_mfma_f32_16x16x32_bf16 v[244:247], v[16:19], v[212:215], 0\n"
      "v_cvt_pk_bf16_f32 v22, v180, v181\n"
      "v_cvt_pk_bf16_f32 v23, v182, v183\n"
      "v_mfma_f32_16x16x32_bf16 v[248:251], v[16:19], v[216:219], 0\n"
      "v_mfma_f32_16x16x32_bf16 v[252:255], v[16:19], v[220:223], 0\n"
      "v_mfma_f32_16x16x32_bf16 v[224:227], v[20:23], v[64:67], v[224:227]\n"
      "s_waitcnt vmcnt(1)\n"
      "v_dot2_f32_bf16 v168, v24, s56, v10\n"
      "v_dot2_f32_bf16 v169, v24, s57, v10\n"
      "v_mfma_f32_16x16x32_bf16 v[228:231], v[20:23], v[68:71], v[228:231]\n"
      "v_dot2_f32_bf16 v170, v25, s56, v10\n"
      "v_dot2_f32_bf16 v171, v25, s57, v10\n"
      "v_mfma_f32_16x16x32_bf16 v[232:235], v[20:23], v[72:75], v[232:235]\n"
      "v_dot2_f32_bf16 v172, v26, s56, v10\n"
      "v_dot2_f32_bf16 v173, v26, s57, v10\n"
      "v_mfma_f32_16x16x32_bf16 v[236:239], v[20:23], v[76:79], v[236:239]\n"
      "v_dot2_f32_bf16 v174, v27, s56, v10\n"
      "v_dot2_f32_bf16 v175, v27, s57, v10\n"
      "v_mfma_f32_16x16x32_bf16 v[240:243], v[20:23], v[80:83], v[240:243]\n"
      "v_cvt_pk_bf16_f32 v24, v168, v169\n"
      "v_cvt_pk_bf16_f32 v25, v170, v171\n"
      "v_mfma_f32_16x16x32_bf16 v[244:247], v[20:23], v[84:87], v[244:247]\n"
      "v_cvt_pk_bf16_f32 v26, v172, v173\n"
      "v_cvt_pk_bf16_f32 v27, v174, v175\n"
      "v_mfma_f32_16x16x32_bf16 v[248:251], v[20:23], v[88:91], v[248:251]\n"
      "v_mfma_f32_16x16x32_bf16 v[252:255], v[20:23], v[92:95], v[252:255]\n"
      "v_mfma_f32_16x16x32_bf16 v[224:227], v[24:27], v[96:99], v[224:227]\n"
      "s_waitcnt vmcnt(0)\n"
      "v_dot2_f32_bf16 v176, v28, s56, v10\n"
      "v_dot2_f32_bf16 v177, v28, s57, v10\n"
      "v_mfma_f32_16x16x32_bf16 v[228:231], v[24:27], v[100:103], v[228:231]\n"
      "v_dot2_f32_bf16 v178, v29, s56, v10\n"
      "v_dot2_f32_bf16 v179, v29, s57, v10\n"
      "v_mfma_f32_16x16x32_bf16 v[232:235], v[24:27], v[104:107], v[232:235]\n"
      "v_dot2_f32_bf16 v180, v30, s56, v10\n"
      "v_dot2_f32_bf16 v181, v30, s57, v10\n"
      "v_mfma_f32_16x16x32_bf16 v[236:239], v[24:27], v[108:111], v[236:239]\n"
      "v_dot2_f32_bf16 v182, v31, s56, v10\n"
      "v_dot2_f32_bf16 v183, v31, s57, v10\n"
      "v_mfma_f32_16x16x32_bf16 v[240:243], v[24:27], v[112:115], v[240:243]\n"
      "v_cvt_pk_bf16_f32 v28, v176, v177\n"
      "v_cvt_pk_bf16_f32 v29, v178, v179\n"
      "v_mfma_f32_16x16x32_bf16 v[244:247], v[24:27], v[116:119], v[244:247]\n"
      "v_cvt_pk_bf16_f32 v30, v180, v181\n"
      "v_cvt_pk_bf16_f32 v31, v182, v183\n"
      "v_mfma_f32_16x16x32_bf16 v[248:251], v[24:27], v[120:123], v[248:251]\n"
      "v_mfma_f32_16x16x32_bf16 v[252:255], v[24:27], v[124:127], v[252:255]\n"
      "v_mfma_f32_16x16x32_bf16 v[224:227], v[28:31], v[128:131], v[224:227]\n"
      "v_mfma_f32_16x16x32_bf16 v[228:231], v[28:31], v[132:135], v[228:231]\n"
      "v_mfma_f32_16x16x32_bf16 v[232:235], v[28:31], v[136:139], v[232:235]\n"
      "v_mfma_f32_16x16x32_bf16 v[236:239], v[28:31], v[140:143], v[236:239]\n"
      "s_nop 4\n"
      "v_add_f32_dpp v160, v226, v224 row_ror:8 row_mask:0xf bank_mask:0x3\n"
      "v_add_f32_dpp v164, v227, v225 row_ror:8 row_mask:0xf bank_mask:0x3\n"
      "v_mfma_f32_16x16x32_bf16 v[240:243], v[28:31], v[144:147], v[240:243]\n"
      "v_add_f32_dpp v161, v230, v228 row_ror:8 row_mask:0xf bank_mask:0x3\n"
      "v_add_f32_dpp v165, v231, v229 row_ror:8 row_mask:0xf bank_mask:0x3\n"
      "v_mfma_f32_16x16x32_bf16 v[244:247], v[28:31], v[148:151], v[244:247]\n"
      "v_add_f32_dpp v162, v234, v232 row_ror:8 row_mask:0xf bank_mask:0x3\n"
      "v_add_f32_dpp v166, v235, v233 row_ror:8 row_mask:0xf bank_mask:0x3\n"
      "v_mfma_f32_16x16x32_bf16 v[248:251], v[28:31], v[152:155], v[248:251]\n"
      "v_add_f32_dpp v163, v238, v236 row_ror:8 row_mask:0xf bank_mask:0x3\n"
      "v_add_f32_dpp v167, v239, v237 row_ror:8 row_mask:0xf bank_mask:0x3\n"
      "v_mfma_f32_16x16x32_bf16 v[252:255], v[28:31], v[156:159], v[252:255]\n"
      "v_add_f32_dpp v160, v240, v242 row_ror:8 row_mask:0xf bank_mask:0xc\n"
      "v_add_f32_dpp v164, v241, v243 row_ror:8 row_mask:0xf bank_mask:0xc\n"
      "v_add_f32_dpp v161, v244, v246 row_ror:8 row_mask:0xf bank_mask:0xc\n"
      "v_add_f32_dpp v165, v245, v247 row_ror:8 row_mask:0xf bank_mask:0xc\n"
      "s_nop 0\n"
      "v_add_f32_dpp v162, v248, v250 row_ror:8 row_mask:0xf bank_mask:0xc\n"
      "v_add_f32_dpp v166, v249, v251 row_ror:8 row_mask:0xf bank_mask:0xc\n"
      "s_nop 0\n"
      "v_add_f32_dpp v163, v252, v254 row_ror:8 row_mask:0xf bank_mask:0xc\n"
      "v_add_f32_dpp v167, v253, v255 row_ror:8 row_mask:0xf bank_mask:0xc\n"
      "ds_write_b128 v34, v[160:163] \n"
      "ds_write_b128 v34, v[164:167] offset:256\n"
      "ds_write_b32 v41, v47 \n"
      "s_movk_i32 s59, 0x7fff\n"
      ".Lgw%=:\n"
      "ds_read_b128 v[72:75], v42 \n"
      "ds_read_b64 v[64:65], v35 offset:0\n"
      "ds_read_b64 v[66:67], v35 offset:2048\n"
      "ds_read_b64 v[68:69], v35 offset:4096\n"
      "ds_read_b64 v[70:71], v35 offset:6144\n"
      "s_sub_u32 s59, s59, 1\n"
      "s_cbranch_scc1 .Lgwo%=\n"
      "s_waitcnt lgkmcnt(4)\n"
      "v_xor_b32 v72, s15, v72\n"
      "v_bitop3_b32 v72, s15, v73, v72 bitop3:0xbe\n"
      "v_bitop3_b32 v72, s15, v74, v72 bitop3:0xbe\n"
      "v_bitop3_b32 v72, s15, v75, v72 bitop3:0xbe\n"
      "v_cmp_ne_u32 vcc, 0, v72\n"
      "s_waitcnt lgkmcnt(0)\n"
      "s_nop 0\n"
      "s_cbranch_vccnz .Lgw%=\n"
      ".Lgwo%=:\n"
      "s_waitcnt lgkmcnt(0)\n"
      "s_bitcmp1_b32 s49, 0\n"
      "v_pk_add_f32 v[64:65], v[64:65], v[66:67]\n"
      "v_pk_add_f32 v[68:69], v[68:69], v[70:71]\n"
      "v_pk_add_f32 v[64:65], v[64:65], v[68:69]\n"
      "v_mul_f32 v64, v64, v44\n"
      "v_mul_f32 v65, v65, v44\n"
      "v_cvt_pk_bf16_f32 v46, v64, v65\n"
      "s_cbranch_scc1 .Lgu%=\n"
      "v_lshlrev_b32 v64, 16, v46\n"
      "v_and_b32 v65, 0xffff0000, v46\n"
      "v_mul_f32 v66, 0xbfb8aa3b, v64\n"
      "v_mul_f32 v67, 0xbfb8aa3b, v65\n"
      "v_exp_f32 v66, v66\n"
      "v_exp_f32 v67, v67\n"
      "v_add_f32 v66, 1.0, v66\n"
      "v_add_f32 v67, 1.0, v67\n"
      "v_rcp_f32 v66, v66\n"
      "v_rcp_f32 v67, v67\n"
      "v_mul_f32 v64, v64, v66\n"
      "v_mul_f32 v65, v65, v67\n"
      "v_cvt_pk_bf16_f32 v46, v64, v65\n"
      ".Lgu%=:\n"
      "buffer_store_dwordx2 v[46:47], v37, s[16:19], s45 offen sc1\n"
      "s_endpgm\n"
      ".Lgss%=:\n"
      "s_sleep 1\n"
      "s_sub_u32 s59, s59, 1\n"
      "s_cbranch_scc1 .Lgso%=\n"
      "buffer_load_dwordx2 v[62:63], v45, s[16:19], s58 offen sc1\n"
      "s_waitcnt vmcnt(0)\n"
      "s_branch .Lgs%=\n"
      ".LD%=:\n"
      "s_waitcnt lgkmcnt(0)\n"
      "s_load_dword s15, s[88:89], 0x0\n"
      "s_mov_b32 s16, s88\n"
      "s_and_b32 s17, s89, 0xffff\n"
      "s_mov_b32 s18, 0x33c000\n"
      "s_mov_b32 s19, 0x20000\n"
      "s_mov_b32 s20, s64\n"
      "s_and_b32 s21, s65, 0xffff\n"
      "s_mov_b32 s22, 0x400000\n"
      "s_mov_b32 s23, 0x20000\n"
      "s_mov_b32 s28, s84\n"
      "s_and_b32 s29, s85, 0xffff\n"
      "s_mov_b32 s30, 0x700000\n"
      "s_mov_b32 s31, 0x20000\n"
      "s_mov_b32 s24, s86\n"
      "s_and_b32 s25, s87, 0xffff\n"
      "s_mov_b32 s26, 0x400000\n"
      "s_mov_b32 s27, 0x20000\n"
      "s_lshl_b32 s42, s34, 3\n"
      "s_add_u32 s42, s42, 4096\n"
      "s_lshl_b32 s43, s48, 8\n"
      "s_and_b32 s50, s49, 1\n"
      "s_lshl_b32 s50, s50, 7\n"
      "s_add_u32 s43, s43, s50\n"
      "s_add_u32 s43, s43, 40960\n"
      "s_waitcnt lgkmcnt(0)\n"
      "s_add_u32 s15, s15, 0x9e3779b1\n"
      "s_sleep 40\n"
      "v_mov_b32 v48, v43\n"
      "v_and_b32 v49, 7, v43\n"
      "v_lshrrev_b32 v50, 3, v43\n"
      "s_cmp_ge_u32 s34, 112\n"
      "s_cbranch_scc1 .LS%=\n"
      "s_setprio 0\n"
      "s_mul_i32 s35, s48, 14\n"
      "s_add_u32 s35, s35, s49\n"
      "s_lshr_b32 s52, s35, 2\n"
      "s_and_b32 s54, s35, 3\n"
      "v_lshlrev_b32 v42, 4, v49\n"
      "s_lshl_b32 s50, s52, 18\n"
      "s_lshl_b32 s51, s54, 9\n"
      "s_add_u32 s50, s50, s51\n"
      "s_lshl_b32 s51, s33, 7\n"
      "s_add_u32 s50, s50, s51\n"
      "v_lshl_add_u32 v32, v50, 14, v42\n"
      "v_add_u32 v32, s50, v32\n"
      "v_lshrrev_b32 v41, 1, v49\n"
      "v_and_b32 v43, 1, v41\n"
      "v_mul_u32_u24 v36, 0x120, v41\n"
      "v_lshlrev_b32 v43, 4, v43\n"
      "v_sub_u32 v36, v36, v43\n"
      "v_and_b32 v43, 1, v49\n"
      "v_lshl_add_u32 v36, v43, 7, v36\n"
      "v_lshrrev_b32 v41, 4, v48\n"
      "v_lshl_add_u32 v36, v41, 4, v36\n"
      "v_and_b32 v43, 8, v48\n"
      "v_add_u32 v36, v36, v43\n"
      "s_lshr_b32 s53, s33, 1\n"
      "s_mul_i32 s50, s53, 1152\n"
      "s_and_b32 s55, s33, 1\n"
      "s_lshl_b32 s51, s55, 6\n"
      "s_add_u32 s50, s50, s51\n"
      "s_add_u32 s50, s50, 149504\n"
      "v_add_u32 v36, s50, v36\n"
      "v_and_b32 v43, 1, v41\n"
      "v_mul_u32_u24 v37, 0x120, v41\n"
      "v_lshlrev_b32 v43, 4, v43\n"
      "v_sub_u32 v37, v37, v43\n"
      "v_and_b32 v43, 15, v48\n"
      "v_lshl_add_u32 v37, v43, 4, v37\n"
      "v_add_u32 v37, 0x24800, v37\n"
      "v_lshlrev_b32 v35, 4, v48\n"
      "s_lshl_b32 s46, s52, 13\n"
      "s_lshl_b32 s50, s53, 12\n"
      "s_add_u32 s46, s46, s50\n"
      "s_lshl_b32 s50, s55, 10\n"
      "s_add_u32 s46, s46, s50\n"
      "s_add_u32 s46, s46, 81920\n"
      "v_and_b32 v41, 3, v50\n"
      "v_lshrrev_b32 v43, 5, v48\n"
      "v_lshl_add_u32 v41, v41, 1, v43\n"
      "v_mul_u32_u24 v38, 0x1c00, v41\n"
      "v_add_u32 v38, v38, v42\n"
      "s_lshl_b32 s50, s54, 2\n"
      "s_add_u32 s50, s50, s33\n"
      "s_mul_i32 s47, s50, 57344\n"
      "s_lshl_b32 s50, s52, 8\n"
      "s_add_u32 s47, s47, s50\n"
      "s_add_u32 s47, s47, 311296\n"
      "s_mov_b32 s62, 0x05040100\n"
      "s_mov_b32 s63, 0x07060302\n"
      "s_lshl_b32 s50, s33, 2\n"
      "s_add_u32 s50, s50, 140864\n"
      "v_mov_b32 v41, s50\n"
      "v_mov_b32 v42, 0x22640\n"
      "s_movk_i32 s59, 0x3fff\n"
      ".Ldf%=:\n"
      "s_sleep 1\n"
      "ds_read_b128 v[240:243], v42\n"
      "s_sub_u32 s59, s59, 1\n"
      "s_cbranch_scc1 .Ldfo%=\n"
      "s_waitcnt lgkmcnt(0)\n"
      "v_xor_b32 v240, s15, v240\n"
      "v_bitop3_b32 v240, s15, v241, v240 bitop3:0xbe\n"
      "v_bitop3_b32 v240, s15, v242, v240 bitop3:0xbe\n"
      "v_bitop3_b32 v240, s15, v243, v240 bitop3:0xbe\n"
      "v_cmp_ne_u32 vcc, 0, v240\n"
      "s_nop 1\n"
      "s_cbranch_vccnz .Ldf%=\n"
      ".Ldfo%=:\n"
      "s_mov_b32 s60, 0\n"
      "buffer_load_dwordx4 v[128:131], v32, s[28:31], s60 offen\n"
      "s_mov_b32 s60, 2048\n"
      "buffer_load_dwordx4 v[132:135], v32, s[28:31], s60 offen\n"
      "s_mov_b32 s60, 4096\n"
      "buffer_load_dwordx4 v[136:139], v32, s[28:31], s60 offen\n"
      "s_mov_b32 s60, 6144\n"
      "buffer_load_dwordx4 v[140:143], v32, s[28:31], s60 offen\n"
      "s_mov_b32 s60, 8192\n"
      "buffer_load_dwordx4 v[144:147], v32, s[28:31], s60 offen\n"
      "s_mov_b32 s60, 10240\n"
      "buffer_load_dwordx4 v[148:151], v32, s[28:31], s60 offen\n"
      "s_mov_b32 s60, 12288\n"
      "buffer_load_dwordx4 v[152:155], v32, s[28:31], s60 offen\n"
      "s_mov_b32 s60, 14336\n"
      "buffer_load_dwordx4 v[156:159], v32, s[28:31], s60 offen\n"
      "s_waitcnt vmcnt(7)\n"
      "s_nop 0\n"
      "s_mov_b32 s60, 131072\n"
      "buffer_load_dwordx4 v[160:163], v32, s[28:31], s60 offen\n"
      "s_waitcnt vmcnt(7)\n"
      "s_nop 0\n"
      "s_mov_b32 s60, 133120\n"
      "buffer_load_dwordx4 v[164:167], v32, s[28:31], s60 offen\n"
      "v_perm_b32 v64, v132, v128, s62\n"
      "v_perm_b32 v68, v132, v128, s63\n"
      "v_perm_b32 v72, v133, v129, s62\n"
      "v_perm_b32 v76, v133, v129, s63\n"
      "v_perm_b32 v80, v134, v130, s62\n"
      "v_perm_b32 v84, v134, v130, s63\n"
      "v_perm_b32 v88, v135, v131, s62\n"
      "v_perm_b32 v92, v135, v131, s63\n"
      "s_waitcnt vmcnt(7)\n"
      "s_nop 0\n"
      "s_mov_b32 s60, 135168\n"
      "buffer_load_dwordx4 v[168:171], v32, s[28:31], s60 offen\n"
      "s_waitcnt vmcnt(7)\n"
      "s_nop 0\n"
      "s_mov_b32 s60, 137216\n"
      "buffer_load_dwordx4 v[172:175], v32, s[28:31], s60 offen\n"
      "v_perm_b32 v65, v140, v136, s62\n"
      "v_perm_b32 v69, v140, v136, s63\n"
      "v_perm_b32 v73, v141, v137, s62\n"
      "v_perm_b32 v77, v141, v137, s63\n"
      "v_perm_b32 v81, v142, v138, s62\n"
      "v_perm_b32 v85, v142, v138, s63\n"
      "v_perm_b32 v89, v143, v139, s62\n"
      "v_perm_b32 v93, v143, v139, s63\n"
      "s_waitcnt vmcnt(7)\n"
      "s_nop 0\n"
      "s_mov_b32 s60, 139264\n"
      "buffer_load_dwordx4 v[176:179], v32, s[28:31], s60 offen\n"
      "s_waitcnt vmcnt(7)\n"
      "s_nop 0\n"
      "s_mov_b32 s60, 141312\n"
      "buffer_load_dwordx4 v[180:183], v32, s[28:31], s60 offen\n"
      "v_perm_b32 v66, v148, v144, s62\n"
      "v_perm_b32 v70, v148, v144, s63\n"
      "v_perm_b32 v74, v149, v145, s62\n"
      "v_perm_b32 v78, v149, v145, s63\n"
      "v_perm_b32 v82, v150, v146, s62\n"
      "v_perm_b32 v86, v150, v146, s63\n"
      "v_perm_b32 v90, v151, v147, s62\n"
      "v_perm_b32 v94, v151, v147, s63\n"
      "s_waitcnt vmcnt(7)\n"
      "s_nop 0\n"
      "s_mov_b32 s60, 143360\n"
      "buffer_load_dwordx4 v[184:187], v32, s[28:31], s60 offen\n"
      "s_waitcnt vmcnt(7)\n"
      "s_nop 0\n"
      "s_mov_b32 s60, 145408\n"
      "buffer_load_dwordx4 v[188:191], v32, s[28:31], s60 offen\n"
      "v_perm_b32 v67, v156, v152, s62\n"
      "v_perm_b32 v71, v156, v152, s63\n"
      "v_perm_b32 v75, v157, v153, s62\n"
      "v_perm_b32 v79, v157, v153, s63\n"
      "v_perm_b32 v83, v158, v154, s62\n"
      "v_perm_b32 v87, v158, v154, s63\n"
      "v_perm_b32 v91, v159, v155, s62\n"
      "v_perm_b32 v95, v159, v155, s63\n"
      "s_waitcnt vmcnt(7)\n"
      "s_nop 0\n"
      "s_waitcnt vmcnt(6)\n"
      "s_nop 0\n"
      "v_perm_b32 v96, v164, v160, s62\n"
      "v_perm_b32 v100, v164, v160, s63\n"
      "v_perm_b32 v104, v165, v161, s62\n"
      "v_perm_b32 v108, v165, v161, s63\n"
      "v_perm_b32 v112, v166, v162, s62\n"
      "v_perm_b32 v116, v166, v162, s63\n"
      "v_perm_b32 v120, v167, v163, s62\n"
      "v_perm_b32 v124, v167, v163, s63\n"
      "s_waitcnt vmcnt(5)\n"
      "s_nop 0\n"
      "s_waitcnt vmcnt(4)\n"
      "s_nop 0\n"
      "v_perm_b32 v97, v172, v168, s62\n"
      "v_perm_b32 v101, v172, v168, s63\n"
      "v_perm_b32 v105, v173, v169, s62\n"
      "v_perm_b32 v109, v173, v169, s63\n"
      "v_perm_b32 v113, v174, v170, s62\n"
      "v_perm_b32 v117, v174, v170, s63\n"
      "v_perm_b32 v121, v175, v171, s62\n"
      "v_perm_b32 v125, v175, v171, s63\n"
      "s_waitcnt vmcnt(3)\n"
      "s_nop 0\n"
      "s_waitcnt vmcnt(2)\n"
      "s_nop 0\n"
      "v_perm_b32 v98, v180, v176, s62\n"
      "v_perm_b32 v102, v180, v176, s63\n"
      "v_perm_b32 v106, v181, v177, s62\n"
      "v_perm_b32 v110, v181, v177, s63\n"
      "v_perm_b32 v114, v182, v178, s62\n"
      "v_perm_b32 v118, v182, v178, s63\n"
      "v_perm_b32 v122, v183, v179, s62\n"
      "v_perm_b32 v126, v183, v179, s63\n"
      "s_waitcnt vmcnt(1)\n"
      "s_nop 0\n"
      "s_waitcnt vmcnt(0)\n"
      "s_nop 0\n"
      "v_perm_b32 v99, v188, v184, s62\n"
      "v_perm_b32 v103, v188, v184, s63\n"
      "v_perm_b32 v107, v189, v185, s62\n"
      "v_perm_b32 v111, v189, v185, s63\n"
      "v_perm_b32 v115, v190, v186, s62\n"
      "v_perm_b32 v119, v190, v186, s63\n"
      "v_perm_b32 v123, v191, v187, s62\n"
      "v_perm_b32 v127, v191, v187, s63\n"
      "s_movk_i32 s59, 0x3fff\n"
      "v_add_u32 v41, 0xffffffc0, v41\n"
      ".Lde%=:\n"
      "ds_read_b32 v43, v41\n"
      "s_sub_u32 s59, s59, 1\n"
      "s_cbranch_scc1 .Ldeo%=\n"
      "s_waitcnt lgkmcnt(0)\n"
      "v_cmp_ne_u32 vcc, s15, v43\n"
      "s_nop 1\n"
      "s_cbranch_vccz .Ldeo%=\n"
      "s_sleep 2\n"
      "s_branch .Lde%=\n"
      ".Ldeo%=:\n"
      "s_cmp_lg_u32 s59, 0x3ffe\n"
      "s_cbranch_scc0 .Ldep%=\n"
      "s_sleep 10\n"
      ".Ldep%=:\n"
      "buffer_load_dwordx4 v[224:227], v35, s[16:19], s46 offen sc1\n"
      "buffer_load_dwordx4 v[228:231], v35, s[16:19], s46 offen offset:2048 sc1\n"
      "s_movk_i32 s59, 0x7fff\n"
      "s_waitcnt vmcnt(0)\n"
      "s_nop 0\n"
      ".Ldg%=:\n"
      "v_xor_b32 v232, s15, v225\n"
      "v_bitop3_b32 v232, s15, v227, v232 bitop3:0xbe\n"
      "v_bitop3_b32 v232, s15, v229, v232 bitop3:0xbe\n"
      "v_bitop3_b32 v232, s15, v231, v232 bitop3:0xbe\n"
      "v_cmp_ne_u32 vcc, 0, v232\n"
      "v_lshlrev_b32 v232, 16, v224\n"
      "v_and_b32 v233, 0xffff0000, v224\n"
      "v_lshlrev_b32 v234, 16, v226\n"
      "v_and_b32 v235, 0xffff0000, v226\n"
      "s_cbranch_vccnz .Ldgs%=\n"
      ".Ldgo%=:\n"
      "v_lshlrev_b32 v236, 16, v228\n"
      "v_and_b32 v237, 0xffff0000, v228\n"
      "v_lshlrev_b32 v238, 16, v230\n"
      "v_and_b32 v239, 0xffff0000, v230\n"
      "v_pk_mul_f32 v[232:233], v[232:233], v[236:237]\n"
      "v_pk_mul_f32 v[234:235], v[234:235], v[238:239]\n"
      "v_cvt_pk_bf16_f32 v232, v232, v233\n"
      "v_cvt_pk_bf16_f32 v233, v234, v235\n"
      "ds_write_b64 v36, v[232:233] \n"
      "s_waitcnt lgkmcnt(0)\n"
      "s_barrier\n"
      "ds_read_b128 v[224:227], v37 \n"
      "ds_read_b128 v[228:231], v37 offset:1152\n"
      "v_mov_b32 v137, s15\n"
      "v_mov_b32 v139, s15\n"
      "v_mov_b32 v141, s15\n"
      "v_mov_b32 v143, s15\n"
      "s_waitcnt lgkmcnt(1)\n"
      "v_mfma_f32_16x16x32_bf16 v[192:195], v[64:67], v[224:227], 0\n"
      "s_waitcnt lgkmcnt(0)\n"
      "v_mfma_f32_16x16x32_bf16 v[192:195], v[96:99], v[228:231], v[192:195]\n"
      "v_mfma_f32_16x16x32_bf16 v[196:199], v[68:71], v[224:227], 0\n"
      "v_mfma_f32_16x16x32_bf16 v[196:199], v[100:103], v[228:231], v[196:199]\n"
      "v_mfma_f32_16x16x32_bf16 v[200:203], v[72:75], v[224:227], 0\n"
      "s_nop 4\n"
      "v_permlane32_swap_b32 v192, v193\n"
      "v_permlane32_swap_b32 v194, v195\n"
      "v_mfma_f32_16x16x32_bf16 v[200:203], v[104:107], v[228:231], v[200:203]\n"
      "v_mfma_f32_16x16x32_bf16 v[204:207], v[76:79], v[224:227], 0\n"
      "v_permlane32_swap_b32 v196, v197\n"
      "v_permlane32_swap_b32 v198, v199\n"
      "v_mfma_f32_16x16x32_bf16 v[204:207], v[108:111], v[228:231], v[204:207]\n"
      "v_add_f32_dpp v128, v193, v192 row_ror:8 row_mask:0xf bank_mask:0x3\n"
      "s_nop 1\n"
      "v_add_f32_dpp v128, v194, v195 row_ror:8 row_mask:0xf bank_mask:0xc\n"
      "v_mfma_f32_16x16x32_bf16 v[208:211], v[80:83], v[224:227], 0\n"
      "v_permlane32_swap_b32 v200, v201\n"
      "v_permlane32_swap_b32 v202, v203\n"
      "v_mfma_f32_16x16x32_bf16 v[208:211], v[112:115], v[228:231], v[208:211]\n"
      "v_add_f32_dpp v129, v197, v196 row_ror:8 row_mask:0xf bank_mask:0x3\n"
      "s_nop 1\n"
      "v_add_f32_dpp v129, v198, v199 row_ror:8 row_mask:0xf bank_mask:0xc\n"
      "v_mfma_f32_16x16x32_bf16 v[212:215], v[84:87], v[224:227], 0\n"
      "v_permlane32_swap_b32 v204, v205\n"
      "v_permlane32_swap_b32 v206, v207\n"
      "v_mfma_f32_16x16x32_bf16 v[212:215], v[116:119], v[228:231], v[212:215]\n"
      "v_add_f32_dpp v130, v201, v200 row_ror:8 row_mask:0xf bank_mask:0x3\n"
      "s_nop 1\n"
      "v_add_f32_dpp v130, v202, v203 row_ror:8 row_mask:0xf bank_mask:0xc\n"
      "v_mfma_f32_16x16x32_bf16 v[216:219], v[88:91], v[224:227], 0\n"
      "v_permlane32_swap_b32 v208, v209\n"
      "v_permlane32_swap_b32 v210, v211\n"
      "v_mfma_f32_16x16x32_bf16 v[216:219], v[120:123], v[228:231], v[216:219]\n"
      "v_add_f32_dpp v131, v205, v204 row_ror:8 row_mask:0xf bank_mask:0x3\n"
      "s_nop 1\n"
      "v_add_f32_dpp v131, v206, v207 row_ror:8 row_mask:0xf bank_mask:0xc\n"
      "v_mfma_f32_16x16x32_bf16 v[220:223], v[92:95], v[224:227], 0\n"
      "v_permlane32_swap_b32 v212, v213\n"
      "v_permlane32_swap_b32 v214, v215\n"
      "v_mfma_f32_16x16x32_bf16 v[220:223], v[124:127], v[228:231], v[220:223]\n"
      "v_add_f32_dpp v132, v209, v208 row_ror:8 row_mask:0xf bank_mask:0x3\n"
      "s_nop 1\n"
      "v_add_f32_dpp v132, v210, v211 row_ror:8 row_mask:0xf bank_mask:0xc\n"
      "v_cvt_pk_bf16_f32 v136, v128, v129\n"
      "v_cvt_pk_bf16_f32 v138, v130, v131\n"
      "buffer_store_dwordx4 v[136:139], v38, s[16:19], s47 offen offset:0 sc1\n"
      "v_permlane32_swap_b32 v216, v217\n"
      "v_permlane32_swap_b32 v218, v219\n"
      "v_add_f32_dpp v133, v213, v212 row_ror:8 row_mask:0xf bank_mask:0x3\n"
      "s_nop 1\n"
      "v_add_f32_dpp v133, v214, v215 row_ror:8 row_mask:0xf bank_mask:0xc\n"
      "v_permlane32_swap_b32 v220, v221\n"
      "v_permlane32_swap_b32 v222, v223\n"
      "v_add_f32_dpp v134, v217, v216 row_ror:8 row_mask:0xf bank_mask:0x3\n"
      "s_nop 1\n"
      "v_add_f32_dpp v134, v218, v219 row_ror:8 row_mask:0xf bank_mask:0xc\n"
      "v_add_f32_dpp v135, v221, v220 row_ror:8 row_mask:0xf bank_mask:0x3\n"
      "s_nop 1\n"
      "v_add_f32_dpp v135, v222, v223 row_ror:8 row_mask:0xf bank_mask:0xc\n"
      "v_cvt_pk_bf16_f32 v140, v132, v133\n"
      "v_cvt_pk_bf16_f32 v142, v134, v135\n"
      "buffer_store_dwordx4 v[140:143], v38, s[16:19], s47 offen offset:128 sc1\n"
      "s_cmp_lg_u32 s33, 0\n"
      "s_cbranch_scc1 .Lda%=\n"
      "v_mov_b32 v254, s15\n"
      "v_mov_b32 v255, s15\n"
      "buffer_store_dwordx2 v[254:255], off, s[16:19], s42 sc1\n"
      ".Lda%=:\n"
      "s_cmp_eq_u32 s33, 3\n"
      "s_cbranch_scc1 .LZ%=\n"
      "s_endpgm\n"
      ".LZ%=:\n"
      "v_lshlrev_b32 v108, 3, v49\n"
      "s_lshl4_add_u32 s35, s48, s49\n"
      "buffer_load_dwordx2 v[104:105], v108, s[16:19], s43 offen sc1\n"
      "s_lshl_b32 s41, s35, 4\n"
      "s_mul_i32 s44, s35, 7168\n"
      "s_add_u32 s44, s44, 311296\n"
      "s_add_u32 s67, s44, 0x1000\n"
      "s_lshl_b32 s66, s35, 9\n"
      "s_add_u32 s66, s66, 3325952\n"
      "v_lshrrev_b32 v109, 4, v48\n"
      "v_and_b32 v110, 8, v48\n"
      "v_lshl_add_u32 v109, v109, 1, v110\n"
      "v_lshlrev_b32 v113, 3, v48\n"
      "s_movk_i32 s59, 0x7fff\n"
      "s_waitcnt vmcnt(0)\n"
      "s_movk_i32 s61, 0x7ff\n"
      ".Ldzh%=:\n"
      "v_cmp_ne_u32 vcc, s15, v105\n"
      "v_bitop3_b32 v110, v104, s61, v104 bitop3:0x0c\n"
      "v_not_b32 v112, v104\n"
      "s_cbranch_vccnz .Ldzs%=\n"
      ".Ldzo%=:\n"
      "v_lshl_add_u32 v110, v110, 11, v109\n"
      "v_lshlrev_b32 v108, 4, v48\n"
      "buffer_load_ushort v111, v110, s[20:23], s41 offen\n"
      "v_ashrrev_i32 v112, 31, v112\n"
      "v_or_b32 v112, 0x80000000, v112\n"
      "v_xor_b32 v112, v112, v104\n"
      "v_and_b32 v112, 0xffff0000, v112\n"
      "v_mul_f32 v112, 0xbfb8aa3b, v112\n"
      "v_exp_f32 v112, v112\n"
      "s_nop 0\n"
      "v_add_f32 v112, 1.0, v112\n"
      "v_rcp_f32 v112, v112\n"
      "s_nop 0\n"
      "v_cvt_pk_bf16_f32 v112, v112, v112\n"
      "v_and_b32 v112, 0xffff0000, v112\n"
      "s_sleep 10\n"
      "buffer_load_dwordx4 v[64:67], v108, s[16:19], s44 offen offset:0 sc1\n"
      "buffer_load_dwordx4 v[68:71], v108, s[16:19], s44 offen offset:1024 sc1\n"
      "buffer_load_dwordx4 v[72:75], v108, s[16:19], s44 offen offset:2048 sc1\n"
      "buffer_load_dwordx4 v[76:79], v108, s[16:19], s44 offen offset:3072 sc1\n"
      "buffer_load_dwordx4 v[80:83], v108, s[16:19], s67 offen offset:0 sc1\n"
      "buffer_load_dwordx4 v[84:87], v108, s[16:19], s67 offen offset:1024 sc1\n"
      "buffer_load_dwordx4 v[88:91], v108, s[16:19], s67 offen offset:2048 sc1\n"
      "s_movk_i32 s59, 0x7fff\n"
      "s_waitcnt vmcnt(0)\n"
      "s_nop 0\n"
      "v_lshlrev_b32 v111, 16, v111\n"
      ".Ldq%=:\n"
      "v_xor_b32 v96, s15, v65\n"
      "v_xor_b32 v97, s15, v67\n"
      "v_bitop3_b32 v96, s15, v69, v96 bitop3:0xbe\n"
      "v_bitop3_b32 v97, s15, v71, v97 bitop3:0xbe\n"
      "v_bitop3_b32 v96, s15, v73, v96 bitop3:0xbe\n"
      "v_bitop3_b32 v97, s15, v75, v97 bitop3:0xbe\n"
      "v_bitop3_b32 v96, s15, v77, v96 bitop3:0xbe\n"
      "v_bitop3_b32 v97, s15, v79, v97 bitop3:0xbe\n"
      "v_bitop3_b32 v96, s15, v81, v96 bitop3:0xbe\n"
      "v_bitop3_b32 v97, s15, v83, v97 bitop3:0xbe\n"
      "v_bitop3_b32 v96, s15, v85, v96 bitop3:0xbe\n"
      "v_bitop3_b32 v97, s15, v87, v97 bitop3:0xbe\n"
      "v_bitop3_b32 v96, s15, v89, v96 bitop3:0xbe\n"
      "v_bitop3_b32 v97, s15, v91, v97 bitop3:0xbe\n"
      "v_or_b32 v96, v96, v97\n"
      "v_cmp_ne_u32 vcc, 0, v96\n"
      "v_lshlrev_b32 v96, 16, v64\n"
      "v_and_b32 v97, 0xffff0000, v64\n"
      "v_lshlrev_b32 v98, 16, v66\n"
      "v_and_b32 v99, 0xffff0000, v66\n"
      "s_cbranch_vccnz .Ldqs%=\n"
      ".Ldqo%=:\n"
      "buffer_load_dwordx2 v[106:107], v113, s[16:19], s66 offen sc1\n"
      "v_lshlrev_b32 v100, 16, v68\n"
      "v_and_b32 v101, 0xffff0000, v68\n"
      "v_lshlrev_b32 v102, 16, v70\n"
      "v_and_b32 v103, 0xffff0000, v70\n"
      "v_add_f32 v96, v96, v100\n"
      "v_add_f32 v97, v97, v101\n"
      "v_add_f32 v98, v98, v102\n"
      "v_add_f32 v99, v99, v103\n"
      "v_lshlrev_b32 v100, 16, v72\n"
      "v_and_b32 v101, 0xffff0000, v72\n"
      "v_lshlrev_b32 v102, 16, v74\n"
      "v_and_b32 v103, 0xffff0000, v74\n"
      "v_add_f32 v96, v96, v100\n"
      "v_add_f32 v97, v97, v101\n"
      "v_add_f32 v98, v98, v102\n"
      "v_add_f32 v99, v99, v103\n"
      "v_lshlrev_b32 v100, 16, v76\n"
      "v_and_b32 v101, 0xffff0000, v76\n"
      "v_lshlrev_b32 v102, 16, v78\n"
      "v_and_b32 v103, 0xffff0000, v78\n"
      "v_add_f32 v96, v96, v100\n"
      "v_add_f32 v97, v97, v101\n"
      "v_add_f32 v98, v98, v102\n"
      "v_add_f32 v99, v99, v103\n"
      "v_lshlrev_b32 v100, 16, v80\n"
      "v_and_b32 v101, 0xffff0000, v80\n"
      "v_lshlrev_b32 v102, 16, v82\n"
      "v_and_b32 v103, 0xffff0000, v82\n"
      "v_add_f32 v96, v96, v100\n"
      "v_add_f32 v97, v97, v101\n"
      "v_add_f32 v98, v98, v102\n"
      "v_add_f32 v99, v99, v103\n"
      "v_lshlrev_b32 v100, 16, v84\n"
      "v_and_b32 v101, 0xffff0000, v84\n"
      "v_lshlrev_b32 v102, 16, v86\n"
      "v_and_b32 v103, 0xffff0000, v86\n"
      "v_add_f32 v96, v96, v100\n"
      "v_add_f32 v97, v97, v101\n"
      "v_add_f32 v98, v98, v102\n"
      "v_add_f32 v99, v99, v103\n"
      "v_lshlrev_b32 v100, 16, v88\n"
      "v_and_b32 v101, 0xffff0000, v88\n"
      "v_lshlrev_b32 v102, 16, v90\n"
      "v_and_b32 v103, 0xffff0000, v90\n"
      "v_add_f32 v96, v96, v100\n"
      "v_add_f32 v97, v97, v101\n"
      "v_add_f32 v98, v98, v102\n"
      "v_add_f32 v99, v99, v103\n"
      "s_nop 0\n"
      "v_permlane32_swap_b32 v96, v98\n"
      "v_permlane32_swap_b32 v97, v99\n"
      "v_add_f32 v96, v96, v98\n"
      "v_add_f32 v97, v97, v99\n"
      "s_nop 1\n"
      "v_permlane16_swap_b32 v96, v97\n"
      "v_add_f32 v96, v96, v97\n"
      "v_cvt_pk_bf16_f32 v96, v96, v96\n"
      "v_and_b32 v96, 0xffff0000, v96\n"
      "v_mul_f32 v96, v96, v112\n"
      "v_cvt_pk_bf16_f32 v96, v96, v96\n"
      "v_and_b32 v96, 0xffff0000, v96\n"
      "v_add_f32 v111, v111, v96\n"
      "s_movk_i32 s59, 0x7fff\n"
      "s_waitcnt vmcnt(0)\n"
      "s_nop 0\n"
      ".Ldb%=:\n"
      "v_cmp_ne_u32 vcc, s15, v107\n"
      "v_add_f32 v97, v111, v106\n"
      "v_cvt_pk_bf16_f32 v97, v97, v97\n"
      "s_cbranch_vccnz .Ldbs%=\n"
      ".Ldbo%=:\n"
      "buffer_store_short v97, v110, s[24:27], s41 offen\n"
      "s_endpgm\n"
      ".Ldgs%=:\n"
      "s_sleep 1\n"
      "s_sub_u32 s59, s59, 1\n"
      "s_cbranch_scc1 .Ldgo%=\n"
      "buffer_load_dwordx4 v[224:227], v35, s[16:19], s46 offen sc1\n"
      "buffer_load_dwordx4 v[228:231], v35, s[16:19], s46 offen offset:2048 sc1\n"
      "s_waitcnt vmcnt(0)\n"
      "s_branch .Ldg%=\n"
      ".Ldzs%=:\n"
      "s_sleep 2\n"
      "s_sub_u32 s59, s59, 1\n"
      "s_cbranch_scc1 .Ldzo%=\n"
      "buffer_load_dwordx2 v[104:105], v108, s[16:19], s43 offen sc1\n"
      "s_waitcnt vmcnt(0)\n"
      "s_branch .Ldzh%=\n"
      ".Ldqs%=:\n"
      "s_sleep 4\n"
      "s_sub_u32 s59, s59, 1\n"
      "s_cbranch_scc1 .Ldqo%=\n"
      "v_cmp_ne_u32 vcc, s15, v65\n"
      "v_cmp_ne_u32 s[64:65], s15, v67\n"
      "s_nop 0\n"
      "s_or_b64 exec, vcc, s[64:65]\n"
      "s_cbranch_scc0 .Ldqk0%=\n"
      "buffer_load_dwordx4 v[64:67], v108, s[16:19], s44 offen offset:0 sc1\n"
      ".Ldqk0%=:\n"
      "s_mov_b64 exec, -1\n"
      "v_cmp_ne_u32 vcc, s15, v69\n"
      "v_cmp_ne_u32 s[64:65], s15, v71\n"
      "s_nop 0\n"
      "s_or_b64 exec, vcc, s[64:65]\n"
      "s_cbranch_scc0 .Ldqk1%=\n"
      "buffer_load_dwordx4 v[68:71], v108, s[16:19], s44 offen offset:1024 sc1\n"
      ".Ldqk1%=:\n"
      "s_mov_b64 exec, -1\n"
      "v_cmp_ne_u32 vcc, s15, v73\n"
      "v_cmp_ne_u32 s[64:65], s15, v75\n"
      "s_nop 0\n"
      "s_or_b64 exec, vcc, s[64:65]\n"
      "s_cbranch_scc0 .Ldqk2%=\n"
      "buffer_load_dwordx4 v[72:75], v108, s[16:19], s44 offen offset:2048 sc1\n"
      ".Ldqk2%=:\n"
      "s_mov_b64 exec, -1\n"
      "v_cmp_ne_u32 vcc, s15, v77\n"
      "v_cmp_ne_u32 s[64:65], s15, v79\n"
      "s_nop 0\n"
      "s_or_b64 exec, vcc, s[64:65]\n"
      "s_cbranch_scc0 .Ldqk3%=\n"
      "buffer_load_dwordx4 v[76:79], v108, s[16:19], s44 offen offset:3072 sc1\n"
      ".Ldqk3%=:\n"
      "s_mov_b64 exec, -1\n"
      "v_cmp_ne_u32 vcc, s15, v81\n"
      "v_cmp_ne_u32 s[64:65], s15, v83\n"
      "s_nop 0\n"
      "s_or_b64 exec, vcc, s[64:65]\n"
      "s_cbranch_scc0 .Ldqk4%=\n"
      "buffer_load_dwordx4 v[80:83], v108, s[16:19], s67 offen offset:0 sc1\n"
      ".Ldqk4%=:\n"
      "s_mov_b64 exec, -1\n"
      "v_cmp_ne_u32 vcc, s15, v85\n"
      "v_cmp_ne_u32 s[64:65], s15, v87\n"
      "s_nop 0\n"
      "s_or_b64 exec, vcc, s[64:65]\n"
      "s_cbranch_scc0 .Ldqk5%=\n"
      "buffer_load_dwordx4 v[84:87], v108, s[16:19], s67 offen offset:1024 sc1\n"
      ".Ldqk5%=:\n"
      "s_mov_b64 exec, -1\n"
      "v_cmp_ne_u32 vcc, s15, v89\n"
      "v_cmp_ne_u32 s[64:65], s15, v91\n"
      "s_nop 0\n"
      "s_or_b64 exec, vcc, s[64:65]\n"
      "s_cbranch_scc0 .Ldqk6%=\n"
      "buffer_load_dwordx4 v[88:91], v108, s[16:19], s67 offen offset:2048 sc1\n"
      ".Ldqk6%=:\n"
      "s_mov_b64 exec, -1\n"
      "s_waitcnt vmcnt(0)\n"
      "s_branch .Ldq%=\n"
      ".Ldbs%=:\n"
      "s_sleep 2\n"
      "s_sub_u32 s59, s59, 1\n"
      "s_cbranch_scc1 .Ldbo%=\n"
      "buffer_load_dwordx2 v[106:107], v113, s[16:19], s66 offen sc1\n"
      "s_waitcnt vmcnt(0)\n"
      "s_branch .Ldb%=\n"
      ".LT%=:\n"
      "v_mov_b32 v48, v43\n"
      "v_and_b32 v49, 7, v43\n"
      "v_lshrrev_b32 v50, 3, v43\n"
      "s_lshl_b32 s42, s34, 3\n"
      "s_add_u32 s42, s42, 4096\n"
      "s_lshl_b32 s43, s48, 8\n"
      "s_and_b32 s50, s49, 1\n"
      "s_lshl_b32 s50, s50, 7\n"
      "s_add_u32 s43, s43, s50\n"
      "s_add_u32 s43, s43, 40960\n"
      "s_mov_b32 s20, s64\n"
      "s_and_b32 s21, s65, 0xffff\n"
      "s_mov_b32 s22, 0x400000\n"
      "s_mov_b32 s23, 0x20000\n"
      "s_sleep 0\n"
      ".LS%=:\n"
      "v_lshlrev_b32 v112, 4, v0\n"
      "v_lshlrev_b32 v113, 2, v0\n"
      "v_sub_u32 v113, 0xfff, v113\n"
      "v_lshlrev_b32 v115, 2, v48\n"
      "v_mov_b32 v116, 0\n"
      "ds_write_b32 v115, v116 offset:26112\n"
      "ds_write_b32 v116, v116 offset:26400\n"
      "v_add_u32 v115, 0x140, v115\n"
      "v_mov_b32 v114, v115\n"
      "s_lshl_b32 s50, s33, 2\n"
      "s_add_u32 s50, s50, 256\n"
      "v_writelane_b32 v114, s50, 63\n"
      "s_lshl_b32 s50, s33, 5\n"
      "v_lshl_add_u32 v117, v49, 2, s50\n"
      "v_lshlrev_b32 v118, 5, v50\n"
      "s_and_b32 s50, s49, 1\n"
      "s_lshl_b32 s50, s50, 10\n"
      "v_lshl_add_u32 v119, v50, 7, s50\n"
      "s_movk_i32 s51, 24576\n"
      "s_mov_b32 s52, 0x80008000\n"
      "s_mov_b32 s53, 0xffff0000\n"
      "s_movk_i32 s59, 0x7fff\n"
      "s_sleep 20\n"
      "buffer_load_dwordx4 v[104:107], v112, s[16:19], s51 offen sc1\n"
      "s_sleep 4\n"
      "buffer_load_dwordx4 v[108:111], v112, s[16:19], s51 offen sc1\n"
      ".Lt_p%=:\n"
      "s_waitcnt vmcnt(1)\n"
      "v_cmp_ne_u32 vcc, s15, v105\n"
      "v_cmp_ne_u32 s[54:55], s15, v107\n"
      "v_mov_b32 v72, v104\n"
      "v_mov_b32 v74, v106\n"
      "s_or_b64 vcc, vcc, s[54:55]\n"
      "s_cbranch_scc0 .Lt_po%=\n"
      "buffer_load_dwordx4 v[104:107], v112, s[16:19], s51 offen sc1\n"
      "s_waitcnt vmcnt(1)\n"
      "v_cmp_ne_u32 vcc, s15, v109\n"
      "v_cmp_ne_u32 s[54:55], s15, v111\n"
      "v_mov_b32 v72, v108\n"
      "v_mov_b32 v74, v110\n"
      "s_or_b64 vcc, vcc, s[54:55]\n"
      "s_cbranch_scc0 .Lt_po%=\n"
      "buffer_load_dwordx4 v[108:111], v112, s[16:19], s51 offen sc1\n"
      "s_sub_u32 s59, s59, 1\n"
      "s_cbranch_scc0 .Lt_p%=\n"
      ".Lt_po%=:\n"
      "v_pk_ashrrev_i16 v81, 15, v72 op_sel_hi:[0,1]\n"
      "v_pk_ashrrev_i16 v82, 15, v74 op_sel_hi:[0,1]\n"
      "v_bitop3_b32 v81, v72, v81, s52 bitop3:0x1e\n"
      "v_bitop3_b32 v82, v74, v82, s52 bitop3:0x1e\n"
      "v_lshl_or_b32 v76, v81, 16, v113\n"
      "v_and_or_b32 v77, v81, s53, v113\n"
      "v_lshl_or_b32 v78, v82, 16, v113\n"
      "v_and_or_b32 v79, v82, s53, v113\n"
      "v_add_u32 v77, -1, v77\n"
      "v_add_u32 v78, -2, v78\n"
      "v_add_u32 v79, -3, v79\n"
      "v_max3_u32 v80, v76, v77, v78\n"
      "v_max_u32 v80, v80, v79\n"
      "s_nop 1\n"
      "v_max_u32_dpp v80, v80, v80 row_shr:1 row_mask:0xf bank_mask:0xf bound_ctrl:1\n"
      "s_nop 1\n"
      "v_max_u32_dpp v80, v80, v80 row_shr:2 row_mask:0xf bank_mask:0xf bound_ctrl:1\n"
      "s_nop 1\n"
      "v_max_u32_dpp v80, v80, v80 row_shr:4 row_mask:0xf bank_mask:0xf bound_ctrl:1\n"
      "s_nop 1\n"
      "v_max_u32_dpp v80, v80, v80 row_shr:8 row_mask:0xf bank_mask:0xf bound_ctrl:1\n"
      "s_nop 1\n"
      "v_max_u32_dpp v80, v80, v80 row_bcast:15 row_mask:0xa bank_mask:0xf\n"
      "s_nop 1\n"
      "v_max_u32_dpp v80, v80, v80 row_bcast:31 row_mask:0xc bank_mask:0xf\n"
      "ds_write_b32 v114, v80 offset:26112\n"
      "s_waitcnt lgkmcnt(0)\n"
      "s_barrier\n"
      "ds_read_b128 v[92:95], v116 offset:26368\n"
      "ds_read_b128 v[96:99], v116 offset:26384\n"
      "s_waitcnt lgkmcnt(1)\n"
      "v_min3_u32 v81, v92, v93, v94\n"
      "s_waitcnt lgkmcnt(0)\n"
      "v_min3_u32 v82, v96, v97, v98\n"
      "v_min3_u32 v81, v81, v95, v99\n"
      "v_min_u32 v81, v81, v82\n"
      "v_cmp_ge_u32 s[60:61], v76, v81\n"
      "v_cmp_ge_u32 s[62:63], v77, v81\n"
      "v_cmp_ge_u32 s[64:65], v78, v81\n"
      "v_cmp_ge_u32 s[66:67], v79, v81\n"
      "s_bcnt1_i32_b64 s68, s[60:61]\n"
      "s_bcnt1_i32_b64 s69, s[62:63]\n"
      "s_bcnt1_i32_b64 s70, s[64:65]\n"
      "s_bcnt1_i32_b64 s71, s[66:67]\n"
      "s_add_u32 s69, s69, s68\n"
      "s_add_u32 s70, s70, s69\n"
      "s_add_u32 s71, s71, s70\n"
      "v_mov_b32 v82, s71\n"
      "s_mov_b64 exec, 1\n"
      "ds_add_rtn_u32 v83, v116, v82 offset:26400\n"
      "s_mov_b64 exec, -1\n"
      "v_mbcnt_lo_u32_b32 v84, s60, 0\n"
      "v_mbcnt_hi_u32_b32 v84, s61, v84\n"
      "v_mbcnt_lo_u32_b32 v85, s62, 0\n"
      "v_mbcnt_hi_u32_b32 v85, s63, v85\n"
      "v_add_u32 v85, s68, v85\n"
      "v_mbcnt_lo_u32_b32 v86, s64, 0\n"
      "v_mbcnt_hi_u32_b32 v86, s65, v86\n"
      "v_add_u32 v86, s69, v86\n"
      "v_mbcnt_lo_u32_b32 v87, s66, 0\n"
      "v_mbcnt_hi_u32_b32 v87, s67, v87\n"
      "v_add_u32 v87, s70, v87\n"
      "s_waitcnt lgkmcnt(0)\n"
      "v_readfirstlane_b32 s72, v83\n"
      "s_nop 1\n"
      "v_add_lshl_u32 v84, v84, s72, 2\n"
      "v_add_lshl_u32 v85, v85, s72, 2\n"
      "v_add_lshl_u32 v86, v86, s72, 2\n"
      "v_add_lshl_u32 v87, v87, s72, 2\n"
      "v_and_b32 v84, 0xff, v84\n"
      "v_and_b32 v85, 0xff, v85\n"
      "v_and_b32 v86, 0xff, v86\n"
      "v_and_b32 v87, 0xff, v87\n"
      "v_cndmask_b32 v84, v115, v84, s[60:61]\n"
      "ds_write_b32 v84, v76 offset:26112\n"
      "v_cndmask_b32 v85, v115, v85, s[62:63]\n"
      "ds_write_b32 v85, v77 offset:26112\n"
      "v_cndmask_b32 v86, v115, v86, s[64:65]\n"
      "ds_write_b32 v86, v78 offset:26112\n"
      "v_cndmask_b32 v87, v115, v87, s[66:67]\n"
      "ds_write_b32 v87, v79 offset:26112\n"
      "s_waitcnt lgkmcnt(0)\n"
      "s_barrier\n"
      "ds_read_b32 v81, v116 offset:26400\n"
      ".Lt_r%=:\n"
      "ds_read_b32 v100, v117 offset:26112\n"
      "ds_read_b128 v[92:95], v118 offset:26112\n"
      "ds_read_b128 v[96:99], v118 offset:26128\n"
      "v_mov_b32 v101, s15\n"
      "v_mov_b32 v89, 0\n"
      "s_waitcnt lgkmcnt(3)\n"
      "v_cmp_lt_u32 vcc, 64, v81\n"
      "s_movk_i32 s50, 0x7ff\n"
      "s_cbranch_vccnz .Lt_m%=\n"
      "s_waitcnt lgkmcnt(1)\n"
      "v_cmp_gt_u32 s[60:61], v92, v100\n"
      "v_cmp_gt_u32 s[62:63], v93, v100\n"
      "v_cmp_gt_u32 s[64:65], v94, v100\n"
      "v_cmp_gt_u32 s[66:67], v95, v100\n"
      "s_waitcnt lgkmcnt(0)\n"
      "v_cmp_gt_u32 s[68:69], v96, v100\n"
      "v_cmp_gt_u32 s[70:71], v97, v100\n"
      "v_cmp_gt_u32 s[72:73], v98, v100\n"
      "v_cmp_gt_u32 s[74:75], v99, v100\n"
      "v_bitop3_b32 v81, v100, s50, v100 bitop3:0x0c\n"
      "v_addc_co_u32 v89, vcc, 0, v89, s[60:61]\n"
      "v_addc_co_u32 v89, vcc, 0, v89, s[62:63]\n"
      "v_addc_co_u32 v89, vcc, 0, v89, s[64:65]\n"
      "v_addc_co_u32 v89, vcc, 0, v89, s[66:67]\n"
      "v_addc_co_u32 v89, vcc, 0, v89, s[68:69]\n"
      "v_addc_co_u32 v89, vcc, 0, v89, s[70:71]\n"
      "v_addc_co_u32 v89, vcc, 0, v89, s[72:73]\n"
      "v_addc_co_u32 v89, vcc, 0, v89, s[74:75]\n"
      "v_lshl_add_u32 v82, v81, 11, v119\n"
      "v_lshlrev_b32 v81, 3, v81\n"
      "v_add_u32_dpp v89, v89, v89 row_ror:8 row_mask:0xf bank_mask:0xf\n"
      "v_mov_b32 v83, v89\n"
      "s_nop 1\n"
      "v_permlane16_swap_b32 v89, v83\n"
      "v_add_u32 v89, v89, v83\n"
      "v_mov_b32 v83, v89\n"
      "s_nop 1\n"
      "v_permlane32_swap_b32 v89, v83\n"
      "v_add_u32 v89, v89, v83\n"
      "v_cmp_gt_u32 vcc, 8, v89\n"
      "v_lshlrev_b32 v89, 3, v89\n"
      "s_movk_i32 s51, 8192\n"
      "s_cbranch_vccz .Lt_n%=\n"
      "s_and_b64 exec, vcc, 0xff\n"
      "ds_write_b32 v89, v100 offset:26752\n"
      "buffer_load_dword v84, v81, s[16:19], s51 offen sc1\n"
      "s_mov_b64 exec, vcc\n"
      "buffer_load_dword v85, v82, s[20:23], 0 offen\n"
      "s_mov_b64 exec, -1\n"
      ".Lt_n%=:\n"
      "v_lshlrev_b32 v89, 3, v49\n"
      "s_waitcnt lgkmcnt(0)\n"
      "s_barrier\n"
      "s_cmp_lg_u32 s33, 0\n"
      "s_cbranch_scc1 .Lt_a%=\n"
      "ds_read_b32 v100, v89 offset:26752\n"
      "s_mov_b64 exec, 0xff\n"
      "s_waitcnt lgkmcnt(0)\n"
      "buffer_store_dwordx2 v[100:101], v89, s[16:19], s43 offen sc1\n"
      "s_mov_b64 exec, -1\n"
      "s_nop 1\n"
      "v_mov_b32 v100, s15\n"
      "buffer_store_dwordx2 v[100:101], off, s[16:19], s42 sc1\n"
      ".Lt_a%=:\n"
      "s_cmp_lg_u32 s33, 3\n"
      "s_cbranch_scc1 .Lt_z%=\n"
      "s_sleep 100\n"
      "s_branch .LZ%=\n"
      ".Lt_z%=:\n"
      "s_cmp_lg_u32 s34, 127\n"
      "s_cbranch_scc1 .Lt_e%=\n"
      "s_cmp_lg_u32 s33, 0\n"
      "s_cbranch_scc1 .Lt_e%=\n"
      "s_movk_i32 s51, 4096\n"
      "s_movk_i32 s59, 0x7fff\n"
      ".Lt_k%=:\n"
      "s_sleep 16\n"
      "buffer_load_dwordx4 v[92:95], v112, s[16:19], s51 offen sc1\n"
      "buffer_load_dwordx4 v[96:99], v112, s[16:19], s51 offen offset:1024 sc1\n"
      "s_sub_u32 s59, s59, 1\n"
      "s_cbranch_scc1 .Lt_kb%=\n"
      "s_waitcnt vmcnt(0)\n"
      "v_xor_b32 v86, s15, v93\n"
      "v_bitop3_b32 v86, s15, v95, v86 bitop3:0xbe\n"
      "v_bitop3_b32 v86, s15, v97, v86 bitop3:0xbe\n"
      "v_bitop3_b32 v86, s15, v99, v86 bitop3:0xbe\n"
      "v_cmp_ne_u32 vcc, 0, v86\n"
      "s_nop 1\n"
      "s_cbranch_vccnz .Lt_k%=\n"
      ".Lt_kb%=:\n"
      "buffer_store_dword v101, off, s[16:19], 0 sc1\n"
      ".Lt_e%=:\n"
      "s_waitcnt vmcnt(0)\n"
      "s_endpgm\n"
      ".Lt_m%=:\n"
      "s_waitcnt lgkmcnt(0)\n"
      "s_barrier\n"
      "s_mov_b32 s59, 0\n"
      "v_mov_b32 v90, 0\n"
      ".Lt_ml%=:\n"
      "s_nop 1\n"
      "v_max3_u32 v80, v76, v77, v78\n"
      "v_max_u32 v80, v80, v79\n"
      "s_nop 1\n"
      "v_max_u32_dpp v80, v80, v80 row_shr:1 row_mask:0xf bank_mask:0xf bound_ctrl:1\n"
      "s_nop 1\n"
      "v_max_u32_dpp v80, v80, v80 row_shr:2 row_mask:0xf bank_mask:0xf bound_ctrl:1\n"
      "s_nop 1\n"
      "v_max_u32_dpp v80, v80, v80 row_shr:4 row_mask:0xf bank_mask:0xf bound_ctrl:1\n"
      "s_nop 1\n"
      "v_max_u32_dpp v80, v80, v80 row_shr:8 row_mask:0xf bank_mask:0xf bound_ctrl:1\n"
      "s_nop 1\n"
      "v_max_u32_dpp v80, v80, v80 row_bcast:15 row_mask:0xa bank_mask:0xf\n"
      "s_nop 1\n"
      "v_max_u32_dpp v80, v80, v80 row_bcast:31 row_mask:0xc bank_mask:0xf\n"
      "s_nop 0\n"
      "v_readlane_b32 s50, v80, 63\n"
      "s_mov_b32 m0, s59\n"
      "s_nop 0\n"
      "v_cmp_ne_u32 vcc, s50, v76\n"
      "s_nop 1\n"
      "v_cndmask_b32 v76, 0, v76, vcc\n"
      "v_cmp_ne_u32 vcc, s50, v77\n"
      "s_nop 1\n"
      "v_cndmask_b32 v77, 0, v77, vcc\n"
      "v_cmp_ne_u32 vcc, s50, v78\n"
      "s_nop 1\n"
      "v_cndmask_b32 v78, 0, v78, vcc\n"
      "v_cmp_ne_u32 vcc, s50, v79\n"
      "s_nop 1\n"
      "v_cndmask_b32 v79, 0, v79, vcc\n"
      "v_writelane_b32 v90, s50, m0\n"
      "s_add_u32 s59, s59, 1\n"
      "s_cmp_lt_u32 s59, 8\n"
      "s_cbranch_scc1 .Lt_ml%=\n"
      "s_mov_b64 exec, 0xff\n"
      "ds_write_b32 v117, v90 offset:26112\n"
      "s_mov_b64 exec, -1\n"
      "v_mov_b32 v81, 0\n"
      "s_waitcnt lgkmcnt(0)\n"
      "s_barrier\n"
      "s_branch .Lt_r%=\n"
      ".LEnd%=:\n"
      : "={s[80:95]}"(sts), "={s[96:99]}"(st2)
      : [ka] "s"(__builtin_amdgcn_kernarg_segment_ptr()), [bid] "s"(blockIdx.x), "{v0}"(threadIdx.x), "s"((u32)(uintptr_t)sm)
      : "v8", "v9", "v10", "v11", "v12", "v13", "v14", "v15", "v16", "v17", "v18", "v19", "v20", "v21", "v22", "v23", "v24", "v25", "v26", "v27",
        "v28", "v29", "v30", "v31", "v32", "v33", "v34", "v35", "v36", "v37", "v38", "v39", "v40", "v41", "v42", "v43", "v44", "v45", "v46", "v47",
        "v48", "v49", "v50", "v51", "v52", "v53", "v54", "v55", "v56", "v57", "v58", "v59", "v60", "v61", "v62", "v63", "v64", "v65", "v66", "v67",
        "v68", "v69", "v70", "v71", "v72", "v73", "v74", "v75", "v76", "v77", "v78", "v79", "v80", "v81", "v82", "v83", "v84", "v85", "v86", "v87",
        "v88", "v89", "v90", "v91", "v92", "v93", "v94", "v95", "v96", "v97", "v98", "v99", "v100", "v101", "v102", "v103", "v104", "v105", "v106", "v107",
        "v108", "v109", "v110", "v111", "v112", "v113", "v114", "v115", "v116", "v117", "v118", "v119", "v120", "v121", "v122", "v123", "v124", "v125", "v126", "v127",
        "v128", "v129", "v130", "v131", "v132", "v133", "v134", "v135", "v136", "v137", "v138", "v139", "v140", "v141", "v142", "v143", "v144", "v145", "v146", "v147",
        "v148", "v149", "v150", "v151", "v152", "v153", "v154", "v155", "v156", "v157", "v158", "v159", "v160", "v161", "v162", "v163", "v164", "v165", "v166", "v167",
        "v168", "v169", "v170", "v171", "v172", "v173", "v174", "v175", "v176", "v177", "v178", "v179", "v180", "v181", "v182", "v183", "v184", "v185", "v186", "v187",
        "v188", "v189", "v190", "v191", "v192", "v193", "v194", "v195", "v196", "v197", "v198", "v199", "v200", "v201", "v202", "v203", "v204", "v205", "v206", "v207",
        "v208", "v209", "v210", "v211", "v212", "v213", "v214", "v215", "v216", "v217", "v218", "v219", "v220", "v221", "v222", "v223", "v224", "v225", "v226", "v227",
        "v228", "v229", "v230", "v231", "v232", "v233", "v234", "v235", "v236", "v237", "v238", "v239", "v240", "v241", "v242", "v243", "v244", "v245", "v246", "v247",
        "v248", "v249", "v250", "v251", "v252", "v253", "v254", "v255", "s12", "s13", "s14", "s15", "s16", "s17", "s18", "s19", "s20", "s21", "s22", "s23",
        "s24", "s25", "s26", "s27", "s28", "s29", "s30", "s31", "s33", "s34", "s35", "s36", "s37", "s38", "s39", "s40", "s41", "s42", "s43", "s44",
        "s45", "s46", "s47", "s48", "s49", "s50", "s51", "s52", "s53", "s54", "s55", "s56", "s57", "s58", "s59", "s60", "s61", "s62", "s63", "s64",
        "s65", "s66", "s67", "s68", "s69", "s70", "s71", "s72", "s73", "s74", "s75", "s76", "s77", "s78", "s79",
        "vcc", "scc", "memory");
  const int b = blockIdx.x;
  GETST();
  STAMP(11);
  PEND();
}

extern "C" void kernel_launch(void* const* d_in, const int* in_sizes, int n_in, void* d_out, int out_size,
                              void* d_ws, size_t ws_size, hipStream_t stream) {
  const u16* const* in = (const u16* const*)d_in;
  fused<<<256, 512, 0, stream>>>(in[0], in[1], in[2], in[3], in[4], in[5], in[6], in[8], in[9], in[10], in[11],
                                 (u16*)d_out, (char*)d_ws);
}
```

```cpp
#include <hip/hip_runtime.h>
#include <stdint.h>

typedef unsigned short u16;
typedef unsigned int u32;
typedef unsigned long long u64;
typedef __attribute__((ext_vector_type(4))) u32 u32x4;
typedef __attribute__((ext_vector_type(16))) u32 u32x16;
#define AS3 __attribute__((address_space(3)))

#define STAMP(I)
#define PEND()
#define GETST()
constexpr int SPIN_MAX = 1 << 16;
constexpr int UNUSED = 0;

__global__ __launch_bounds__(512, 2) void fused(const u16* __restrict__ x, const u16* __restrict__ lar,
    const u16* __restrict__ aim, const u16* __restrict__ cre, const u16* __restrict__ cim,
    const u16* __restrict__ ldt, const u16* __restrict__ dsk, const u16* __restrict__ wr,
    const u16* __restrict__ wg, const u16* __restrict__ wu, const u16* __restrict__ wd,
    u16* __restrict__ out, char* ws) {
  __shared__ __attribute__((aligned(16))) char smem_[163840];
  AS3 char* sm = (AS3 char*)smem_;
  u64 ptm = 0, pc0 = 0, pt0 = 0, pte = 0, st_[16] = {};
  u32x16 sts;
  u32x4 st2;
  asm volatile(
      "s_memrealtime s[12:13]\n"
      "s_load_dwordx16 s[64:79], %[ka], 0x0\n"
      "s_load_dwordx8 s[80:87], %[ka], 0x40\n"
      "s_load_dwordx2 s[88:89], %[ka], 0x60\n"
      "v_readfirstlane_b32 s33, v0\n"
      "s_getpc_b64 s[50:51]\n"
      ".Lpc%=:\n"
      "s_mov_b32 s34, %[bid]\n"
      "v_and_b32 v43, 63, v0\n"
      "v_mov_b32 v15, 0\n"
      "s_lshr_b32 s33, s33, 6\n"
      "s_and_b32 s52, s34, -8\n"
      "s_add_u32 s52, s52, s33\n"
      "s_min_u32 s52, s52, (.LEnd%=-.Lpc%=)>>7\n"
      "s_lshl_b32 s52, s52, 7\n"
      "s_and_b32 s50, s50, 0xffffff80\n"
      "s_add_u32 s50, s50, s52\n"
      "s_addc_u32 s51, s51, 0\n"
      "global_load_dword v11, v15, s[50:51] \n"
      "s_and_b32 s48, s34, 7\n"
      "s_lshr_b32 s49, s34, 3\n"
      "s_and_b32 s55, s49, 15\n"
      "s_lshl4_add_u32 s35, s48, s55\n"
      "s_cmp_lt_u32 s34, 128\n"
      "s_cbranch_scc1 .LH%=\n"
      "s_lshl3_add_u32 s50, s35, s33\n"
      "s_lshr_b32 s51, s34, 5\n"
      "s_andn2_b32 s51, 4, s51\n"
      "s_add_u32 s50, s50, s51\n"
      "s_lshl_b32 s51, s50, 8\n"
      "s_lshl_b32 s50, s50, 1\n"
      "v_lshl_add_u32 v48, v43, 2, s51\n"
      "v_mov_b32 v49, s50\n"
      "s_waitcnt lgkmcnt(0)\n"
      "global_load_dword v16, v48, s[66:67] nt\n"
      "global_load_dword v17, v48, s[68:69] nt\n"
      "global_load_ushort v12, v49, s[74:75] \n"
      "global_load_ushort v13, v49, s[76:77] \n"
      "global_load_dword v18, v48, s[70:71] nt\n"
      "global_load_dword v19, v48, s[72:73] nt\n"
      "s_load_dword s15, s[88:89], 0x0\n"
      "s_setprio 3\n"
      "s_branch .LC%=\n"
      ".LH%=:\n"
      "s_cmp_ge_u32 s33, 4\n"
      "s_cbranch_scc1 .LG%=\n"
      ".LD%=:\n"
      "v_and_b32 v52, 48, v43\n"
      "v_and_b32 v53, 3, v43\n"
      "v_lshl_add_u32 v53, v53, 2, v52\n"
      "v_and_b32 v54, 12, v43\n"
      "v_lshlrev_b32 v53, 2, v53\n"
      "v_lshl_add_u32 v52, v54, 9, v53\n"
      "s_and_b32 s53, s34, 127\n"
      "s_and_b32 s52, s33, 3\n"
      "s_lshl2_add_u32 s14, s53, s52\n"
      "s_lshl_b32 s51, s14, 13\n"
      "v_lshl_add_u32 v53, v54, 6, v53\n"
      "s_waitcnt lgkmcnt(0)\n"
      "s_add_u32 s52, s64, s51\n"
      "s_addc_u32 s53, s65, 0\n"
      "global_load_dwordx4 v[224:227], v53, s[78:79] offset:0\n"
      "global_load_dwordx4 v[228:231], v53, s[78:79] offset:1024\n"
      "global_load_dwordx4 v[192:195], v52, s[52:53] offset:0\n"
      "global_load_dwordx4 v[196:199], v52, s[52:53] offset:256\n"
      "global_load_dwordx4 v[200:203], v52, s[52:53] offset:512\n"
      "global_load_dwordx4 v[204:207], v52, s[52:53] offset:768\n"
      "global_load_dwordx4 v[208:211], v52, s[52:53] offset:1024\n"
      "global_load_dwordx4 v[212:215], v52, s[52:53] offset:1280\n"
      "global_load_dwordx4 v[216:219], v52, s[52:53] offset:1536\n"
      "global_load_dwordx4 v[220:223], v52, s[52:53] offset:1792\n"
      "s_load_dword s15, s[88:89], 0x0\n"
      "s_setprio 3\n"
      "s_cmp_lg_u32 s33, 0\n"
      "s_cbranch_scc1 .Ltb%=\n"
      "v_mov_b32 v48, 0x22680\n"
      "v_mov_b32 v49, s12\n"
      "ds_write_b32 v48, v49 \n"
      ".Ltb%=:\n"
      "s_mov_b32 s16, s88\n"
      "s_and_b32 s17, s89, 0xffff\n"
      "s_mov_b32 s18, 0x32c000\n"
      "s_mov_b32 s19, 0x20000\n"
      "s_mov_b32 s20, s64\n"
      "s_and_b32 s21, s65, 0xffff\n"
      "s_mov_b32 s22, 0x400000\n"
      "s_mov_b32 s23, 0x20000\n"
      "s_mov_b32 s28, s84\n"
      "s_and_b32 s29, s85, 0xffff\n"
      "s_mov_b32 s30, 0x700000\n"
      "s_mov_b32 s31, 0x20000\n"
      "s_mov_b32 s24, s86\n"
      "s_and_b32 s25, s87, 0xffff\n"
      "s_mov_b32 s26, 0x400000\n"
      "s_mov_b32 s27, 0x20000\n"
      "s_lshl_b32 s42, s34, 3\n"
      "s_add_u32 s42, s42, 4096\n"
      "s_lshl_b32 s43, s48, 8\n"
      "s_and_b32 s50, s49, 1\n"
      "s_lshl_b32 s50, s50, 7\n"
      "s_add_u32 s43, s43, s50\n"
      "s_add_u32 s43, s43, 40960\n"
      "v_mov_b32 v20, 0x3f803f80\n"
      "v_mov_b32 v21, 0x3f803f80\n"
      "v_mov_b32 v22, 0x3f803f80\n"
      "v_mov_b32 v23, 0x3f803f80\n"
      "v_mov_b32 v44, 0x7f000000\n"
      "v_mov_b32 v25, 0\n"
      "s_lshl_b32 s68, s14, 5\n"
      "s_add_u32 s68, s68, 8192\n"
      "s_lshl_b32 s69, s14, 4\n"
      "s_add_u32 s69, s69, 24576\n"
      "v_writelane_b32 v44, s68, 0\n"
      "s_add_u32 s68, s68, 8\n"
      "v_writelane_b32 v44, s68, 20\n"
      "s_add_u32 s68, s68, 8\n"
      "v_writelane_b32 v44, s68, 40\n"
      "s_add_u32 s68, s68, 8\n"
      "v_writelane_b32 v44, s68, 60\n"
      "s_add_u32 s68, s68, 8\n"
      "v_writelane_b32 v44, s69, 1\n"
      "s_add_u32 s69, s69, 8\n"
      "v_writelane_b32 v44, s69, 2\n"
      "s_waitcnt lgkmcnt(0)\n"
      "s_add_u32 s15, s15, 0x9e3779b1\n"
      "v_mov_b32 v47, s15\n"
      "s_waitcnt vmcnt(9)\n"
      "v_mfma_f32_16x16x32_bf16 v[16:19], v[20:23], v[224:227], 0\n"
      "s_waitcnt vmcnt(8)\n"
      "v_mfma_f32_16x16x32_bf16 v[16:19], v[20:23], v[228:231], v[16:19]\n"
      "s_waitcnt vmcnt(7)\n"
      "v_mfma_f32_16x16x32_bf16 v[232:235], v[192:195], v[224:227], 0\n"
      "v_mfma_f32_16x16x32_bf16 v[252:255], v[192:195], v[192:195], 0\n"
      "v_mfma_f32_16x16x32_bf16 v[248:251], v[192:195], v[20:23], 0\n"
      "s_waitcnt vmcnt(6)\n"
      "v_mfma_f32_16x16x32_bf16 v[236:239], v[196:199], v[224:227], 0\n"
      "v_mfma_f32_16x16x32_bf16 v[252:255], v[196:199], v[196:199], v[252:255]\n"
      "v_mfma_f32_16x16x32_bf16 v[248:251], v[196:199], v[20:23], v[248:251]\n"
      "s_waitcnt vmcnt(5)\n"
      "v_mfma_f32_16x16x32_bf16 v[240:243], v[200:203], v[224:227], 0\n"
      "v_mfma_f32_16x16x32_bf16 v[252:255], v[200:203], v[200:203], v[252:255]\n"
      "v_mfma_f32_16x16x32_bf16 v[248:251], v[200:203], v[20:23], v[248:251]\n"
      "s_waitcnt vmcnt(4)\n"
      "v_mfma_f32_16x16x32_bf16 v[244:247], v[204:207], v[224:227], 0\n"
      "v_mfma_f32_16x16x32_bf16 v[252:255], v[204:207], v[204:207], v[252:255]\n"
      "v_mfma_f32_16x16x32_bf16 v[248:251], v[204:207], v[20:23], v[248:251]\n"
      "s_waitcnt vmcnt(3)\n"
      "v_mfma_f32_16x16x32_bf16 v[232:235], v[208:211], v[228:231], v[232:235]\n"
      "v_mfma_f32_16x16x32_bf16 v[252:255], v[208:211], v[208:211], v[252:255]\n"
      "v_mfma_f32_16x16x32_bf16 v[248:251], v[208:211], v[20:23], v[248:251]\n"
      "v_add_f32_dpp v16, v16, v16 quad_perm:[1,0,3,2] row_mask:0xf bank_mask:0xf\n"
      "s_waitcnt vmcnt(2)\n"
      "v_mfma_f32_16x16x32_bf16 v[236:239], v[212:215], v[228:231], v[236:239]\n"
      "v_mfma_f32_16x16x32_bf16 v[252:255], v[212:215], v[212:215], v[252:255]\n"
      "v_mfma_f32_16x16x32_bf16 v[248:251], v[212:215], v[20:23], v[248:251]\n"
      "v_add_f32_dpp v16, v16, v16 quad_perm:[2,3,0,1] row_mask:0xf bank_mask:0xf\n"
      "v_add_f32_dpp v25, v232, v25 quad_perm:[0,0,0,0] row_mask:0xf bank_mask:0x1\n"
      "s_nop 1\n"
      "v_add_f32_dpp v25, v233, v25 quad_perm:[1,1,1,1] row_mask:0xf bank_mask:0x1\n"
      "s_nop 1\n"
      "v_add_f32_dpp v25, v234, v25 quad_perm:[2,2,2,2] row_mask:0xf bank_mask:0x1\n"
      "s_nop 1\n"
      "v_add_f32_dpp v25, v235, v25 quad_perm:[3,3,3,3] row_mask:0xf bank_mask:0x1\n"
      "s_waitcnt vmcnt(1)\n"
      "v_mfma_f32_16x16x32_bf16 v[240:243], v[216:219], v[228:231], v[240:243]\n"
      "v_mfma_f32_16x16x32_bf16 v[252:255], v[216:219], v[216:219], v[252:255]\n"
      "v_mfma_f32_16x16x32_bf16 v[248:251], v[216:219], v[20:23], v[248:251]\n"
      "v_add_f32_dpp v16, v16, v16 row_ror:4 row_mask:0xf bank_mask:0xf\n"
      "v_add_f32_dpp v25, v236, v25 quad_perm:[0,0,0,0] row_mask:0xf bank_mask:0x2\n"
      "s_nop 1\n"
      "v_add_f32_dpp v25, v237, v25 quad_perm:[1,1,1,1] row_mask:0xf bank_mask:0x2\n"
      "s_nop 1\n"
      "v_add_f32_dpp v25, v238, v25 quad_perm:[2,2,2,2] row_mask:0xf bank_mask:0x2\n"
      "s_nop 1\n"
      "v_add_f32_dpp v25, v239, v25 quad_perm:[3,3,3,3] row_mask:0xf bank_mask:0x2\n"
      "s_waitcnt vmcnt(0)\n"
      "v_mfma_f32_16x16x32_bf16 v[244:247], v[220:223], v[228:231], v[244:247]\n"
      "v_mfma_f32_16x16x32_bf16 v[252:255], v[220:223], v[220:223], v[252:255]\n"
      "v_mfma_f32_16x16x32_bf16 v[248:251], v[220:223], v[20:23], v[248:251]\n"
      "v_add_f32_dpp v16, v16, v16 row_ror:8 row_mask:0xf bank_mask:0xf\n"
      "v_add_f32_dpp v25, v240, v25 quad_perm:[0,0,0,0] row_mask:0xf bank_mask:0x4\n"
      "s_nop 1\n"
      "v_add_f32_dpp v25, v241, v25 quad_perm:[1,1,1,1] row_mask:0xf bank_mask:0x4\n"
      "s_nop 1\n"
      "v_add_f32_dpp v25, v242, v25 quad_perm:[2,2,2,2] row_mask:0xf bank_mask:0x4\n"
      "s_nop 1\n"
      "v_add_f32_dpp v25, v243, v25 quad_perm:[3,3,3,3] row_mask:0xf bank_mask:0x4\n"
      "s_nop 1\n"
      "v_add_f32_dpp v25, v244, v25 quad_perm:[0,0,0,0] row_mask:0xf bank_mask:0x8\n"
      "v_mov_b32_dpp v24, v252 quad_perm:[0,0,0,0] row_mask:0xf bank_mask:0xf\n"
      "v_mov_b32_dpp v27, v254 quad_perm:[2,2,2,2] row_mask:0xf bank_mask:0xf\n"
      "v_add_f32_dpp v25, v245, v25 quad_perm:[1,1,1,1] row_mask:0xf bank_mask:0x8\n"
      "v_add_f32 v248, v248, v249\n"
      "v_add_f32 v250, v250, v251\n"
      "v_add_f32_dpp v24, v253, v24 quad_perm:[1,1,1,1] row_mask:0xf bank_mask:0xf\n"
      "v_add_f32_dpp v27, v255, v27 quad_perm:[3,3,3,3] row_mask:0xf bank_mask:0xf\n"
      "v_add_f32_dpp v25, v246, v25 quad_perm:[2,2,2,2] row_mask:0xf bank_mask:0x8\n"
      "v_add_f32 v248, v248, v250\n"
      "v_add_f32 v24, v24, v27\n"
      "v_add_f32_dpp v25, v247, v25 quad_perm:[3,3,3,3] row_mask:0xf bank_mask:0x8\n"
      "v_mul_f32 v248, 0x3a800000, v248\n"
      "v_mul_f32 v24, 0x3a800000, v24\n"
      "v_add_f32_dpp v25, v25, v25 row_ror:4 row_mask:0xf bank_mask:0xf\n"
      "v_fma_f32 v24, -v248, v248, v24\n"
      "v_max_f32 v24, 0, v24\n"
      "v_add_f32_dpp v25, v25, v25 row_ror:8 row_mask:0xf bank_mask:0xf\n"
      "v_cvt_pk_bf16_f32 v248, v248, v24\n"
      "v_and_b32 v24, 0xffff0000, v248\n"
      "v_add_f32 v24, 0x3727c5ac, v24\n"
      "v_lshlrev_b32 v26, 16, v248\n"
      "v_cvt_pk_bf16_f32 v24, v24, v24\n"
      "v_fma_f32 v25, -v26, v16, v25\n"
      "v_and_b32 v24, 0xffff0000, v24\n"
      "v_sqrt_f32 v24, v24\n"
      "s_nop 0\n"
      "v_cvt_pk_bf16_f32 v46, v26, v24\n"
      "v_and_b32 v24, 0xffff0000, v46\n"
      "v_rcp_f32 v24, v24\n"
      "s_nop 0\n"
      "v_mul_f32 v25, v25, v24\n"
      "v_cvt_pk_bf16_f32 v25, v25, v25\n"
      "s_nop 0\n"
      "v_readlane_b32 s68, v25, 0\n"
      "v_readlane_b32 s69, v25, 20\n"
      "v_readlane_b32 s70, v25, 40\n"
      "v_readlane_b32 s71, v25, 60\n"
      "s_pack_ll_b32_b16 s68, s68, s69\n"
      "s_pack_ll_b32_b16 s70, s70, s71\n"
      "v_writelane_b32 v46, s68, 1\n"
      "v_writelane_b32 v46, s70, 2\n"
      "buffer_store_dwordx2 v[46:47], v44, s[16:19], 0 offen sc1\n"
      "v_mov_b32 v48, v43\n"
      "v_and_b32 v49, 7, v43\n"
      "v_lshrrev_b32 v50, 3, v43\n"
      "s_cmp_ge_u32 s34, 112\n"
      "s_cbranch_scc1 .LS%=\n"
      "s_branch .LD2%=\n"
      ".LC%=:\n"
      "s_mov_b32 s16, s88\n"
      "s_and_b32 s17, s89, 0xffff\n"
      "s_mov_b32 s18, 0x32c000\n"
      "s_mov_b32 s19, 0x20000\n"
      "s_mov_b32 s20, s64\n"
      "s_and_b32 s21, s65, 0xffff\n"
      "s_mov_b32 s22, 0x400000\n"
      "s_mov_b32 s23, 0x20000\n"
      "s_mov_b32 s28, s84\n"
      "s_and_b32 s29, s85, 0xffff\n"
      "s_mov_b32 s30, 0x700000\n"
      "s_mov_b32 s31, 0x20000\n"
      "s_mul_i32 s39, s33, 6528\n"
      "s_mul_i32 s40, s33, 8768\n"
      "s_add_u32 s52, s40, 52224\n"
      "s_add_u32 s53, s52, 4160\n"
      "s_lshl_b32 s42, s34, 3\n"
      "s_add_u32 s42, s42, 4096\n"
      "s_lshl_b32 s43, s48, 8\n"
      "s_and_b32 s50, s49, 1\n"
      "s_lshl_b32 s50, s50, 7\n"
      "s_add_u32 s43, s43, s50\n"
      "s_add_u32 s43, s43, 40960\n"
      "s_and_b32 s50, s33, 3\n"
      "s_lshl2_add_u32 s45, s35, s50\n"
      "s_lshl_b32 s45, s45, 12\n"
      "s_add_u32 s45, s45, 1228800\n"
      "v_and_b32 v48, 31, v43\n"
      "v_lshrrev_b32 v49, 5, v43\n"
      "v_lshlrev_b32 v55, 4, v49\n"
      "v_lshrrev_b32 v52, 2, v48\n"
      "v_mul_u32_u24 v52, 0x110, v52\n"
      "v_add3_u32 v20, v52, v55, s52\n"
      "v_and_b32 v52, 3, v43\n"
      "v_mul_u32_u24 v52, 0x110, v52\n"
      "s_add_u32 s50, s52, 2176\n"
      "v_add3_u32 v178, v52, v55, s50\n"
      "v_lshl_add_u32 v52, v43, 2, s50\n"
      "v_mov_b32 v53, 0x3c00\n"
      "ds_write_b32 v52, v53 \n"
      "ds_write_b32 v52, v53 offset:3264\n"
      "v_and_b32 v52, 12, v43\n"
      "s_mov_b32 s50, 0x6600\n"
      "s_mov_b32 s51, 0x6600\n"
      "v_cndmask_b32 v52, v48, v52, s[50:51]\n"
      "s_mov_b32 s50, 0xff000000\n"
      "s_mov_b32 s51, 0xff000000\n"
      "v_cndmask_b32 v52, v52, 9, s[50:51]\n"
      "v_mul_u32_u24 v52, 0x110, v52\n"
      "v_add3_u32 v21, v52, v55, s39\n"
      "v_lshl_add_u32 v52, v43, 2, s39\n"
      "v_mov_b32 v53, 0x3c00\n"
      "ds_write_b32 v52, v53 \n"
      "s_cmp_lt_u32 s34, 128\n"
      "s_cselect_b64 vcc, -1, 0\n"
      "s_cbranch_scc1 .Lsc%=\n"
      "s_mov_b32 s24, s86\n"
      "s_and_b32 s25, s87, 0xffff\n"
      "s_mov_b32 s26, 0x400000\n"
      "s_mov_b32 s27, 0x20000\n"
      "s_mul_i32 s54, s33, 4864\n"
      "s_add_u32 s54, s54, 122368\n"
      "s_lshl_b32 s41, s35, 4\n"
      "s_and_b32 s43, s41, 0xffffff80\n"
      "s_add_u32 s43, s43, 43008\n"
      "s_mul_i32 s50, s48, 14\n"
      "s_add_u32 s50, s50, s55\n"
      "s_lshr_b32 s51, s50, 2\n"
      "s_lshl4_add_u32 s51, s51, s33\n"
      "s_lshl_b32 s46, s51, 14\n"
      "s_and_b32 s50, s50, 3\n"
      "s_lshl_b32 s50, s50, 9\n"
      "s_add_u32 s46, s46, s50\n"
      "s_cmp_lt_u32 s55, 14\n"
      "s_cselect_b32 s30, s30, 0\n"
      "s_cselect_b32 s47, s46, 0\n"
      "s_add_u32 s46, s47, 0x20000\n"
      "v_bfe_u32 v14, v43, 2, 3\n"
      "v_and_b32 v53, 3, v43\n"
      "v_lshl_add_u32 v14, v14, 4, v53\n"
      "v_lshlrev_b32 v14, 7, v14\n"
      "v_and_b32 v50, 15, v43\n"
      "v_lshrrev_b32 v51, 4, v43\n"
      "v_and_b32 v53, 1, v43\n"
      "s_and_b32 s58, s33, 3\n"
      "s_lshl4_add_u32 s50, s33, s55\n"
      "s_lshl_b32 s50, s50, 14\n"
      "s_and_b32 s51, s55, 8\n"
      "s_lshl_b32 s51, s51, 4\n"
      "s_sub_u32 s50, s50, s51\n"
      "v_bfe_u32 v52, v43, 1, 3\n"
      "v_lshl_add_u32 v52, v52, 4, v53\n"
      "v_lshlrev_b32 v52, 7, v52\n"
      "v_add_u32 v15, s50, v52\n"
      "v_max_i32 v15, 0, v15\n"
      "s_bitcmp1_b32 s55, 0\n"
      "s_cselect_b32 s56, s82, s80\n"
      "s_cselect_b32 s57, s83, s81\n"
      "s_lshr_b32 s50, s56, 7\n"
      "s_sub_u32 s50, s48, s50\n"
      "s_add_u32 s50, s50, 3\n"
      "s_and_b32 s50, s50, 7\n"
      "s_min_u32 s50, s50, 6\n"
      "s_mul_i32 s51, s48, 7\n"
      "s_add_u32 s50, s50, s51\n"
      "s_lshl_b32 s50, s50, 7\n"
      "s_lshr_b32 s51, s55, 1\n"
      "s_mul_i32 s51, s51, 24\n"
      "s_lshl_b32 s58, s58, 8\n"
      "s_add_u32 s51, s51, s58\n"
      "s_add_u32 s51, s51, 64\n"
      "s_mul_i32 s51, s51, 7168\n"
      "s_add_u32 s50, s50, s51\n"
      "v_min_u32 v57, 23, v43\n"
      "v_mul_u32_u24 v57, 0x1c00, v57\n"
      "v_add_u32 v57, s50, v57\n"
      "v_lshl_add_u32 v52, v48, 1, v49\n"
      "v_lshlrev_b32 v54, 3, v52\n"
      "s_add_u32 s50, s53, 570\n"
      "v_sub_u32 v22, s50, v54\n"
      "s_lshl_b32 s50, s33, 17\n"
      "v_lshl_add_u32 v8, v52, 11, s50\n"
      "v_mov_b32 v52, 0x63e\n"
      "v_mov_b32 v54, 0x6de\n"
      "s_mov_b32 s50, 0x55555555\n"
      "s_mov_b32 s51, 0x55555555\n"
      "v_cndmask_b32 v24, v52, v54, s[50:51]\n"
      "v_mov_b32 v52, 0x4de\n"
      "v_mov_b32 v54, 0x59e\n"
      "v_cndmask_b32 v52, v52, v54, s[50:51]\n"
      "s_mov_b32 s58, 0\n"
      "s_mov_b32 s59, -1\n"
      "v_cndmask_b32 v24, v24, v52, s[58:59]\n"
      "v_add_u32 v52, -1, v48\n"
      "v_and_b32 v52, -2, v52\n"
      "v_sub_u32 v24, v24, v52\n"
      "v_add_u32 v24, s53, v24\n"
      "v_add_u32 v23, 32, v24\n"
      "s_add_u32 s60, s53, 2048\n"
      "v_writelane_b32 v23, s60, 0\n"
      "v_writelane_b32 v23, s60, 32\n"
      "v_mul_u32_u24 v52, 0xc1, v53\n"
      "v_sub_u32 v52, v52, v48\n"
      "s_add_u32 s60, s53, 1150\n"
      "v_add_u32 v52, s60, v52\n"
      "v_add_u32 v25, 0xffffffc0, v24\n"
      "v_cndmask_b32 v25, v25, v52, s[58:59]\n"
      "v_and_b32 v52, -2, v43\n"
      "v_add_u32 v52, 58, v52\n"
      "v_mov_b32 v54, 0x810\n"
      "s_mov_b32 s60, 0xfffff000\n"
      "s_mov_b32 s61, 0xffffffff\n"
      "v_cndmask_b32 v52, v52, v54, s[60:61]\n"
      "s_movk_i32 s60, 1408\n"
      "s_movk_i32 s61, 1664\n"
      "v_writelane_b32 v52, s60, 8\n"
      "v_writelane_b32 v52, s60, 9\n"
      "v_writelane_b32 v52, s61, 10\n"
      "v_writelane_b32 v52, s61, 11\n"
      "v_mul_u32_u24 v54, 0x83e, v53\n"
      "v_add3_u32 v26, v52, v54, s53\n"
      "v_lshlrev_b32 v52, 1, v50\n"
      "v_add_u32 v52, v52, v54\n"
      "s_add_u32 s60, s53, 2140\n"
      "v_sub_u32 v52, s60, v52\n"
      "v_lshl_add_u32 v29, v51, 4, v52\n"
      "v_lshl_add_u32 v30, v51, 3, v52\n"
      "v_add_u32 v30, 0x440, v30\n"
      "v_bfe_u32 v52, v43, 2, 3\n"
      "v_max_u32 v54, 5, v52\n"
      "v_mul_u32_u24 v52, 0x60, v52\n"
      "v_lshl_add_u32 v52, v54, 6, v52\n"
      "v_add_u32 v52, 0x2a0, v52\n"
      "s_mov_b32 s60, 0xf\n"
      "s_mov_b32 s61, 0xf\n"
      "v_cndmask_b32 v52, v52, v55, s[60:61]\n"
      "v_mul_u32_u24 v54, 0x830, v49\n"
      "v_add3_u32 v27, v52, v54, s53\n"
      "v_and_b32 v55, 3, v43\n"
      "v_lshl_add_u32 v27, v55, 4, v27\n"
      "s_add_u32 s60, s39, 3104\n"
      "s_add_u32 s61, s52, 2080\n"
      "v_mov_b32 v52, s39\n"
      "v_mov_b32 v54, s60\n"
      "s_mov_b32 s50, 0xff00ff00\n"
      "s_mov_b32 s51, 0xff00ff00\n"
      "v_cndmask_b32 v28, v52, v54, s[50:51]\n"
      "v_mov_b32 v52, s52\n"
      "v_mov_b32 v54, s61\n"
      "v_cndmask_b32 v52, v52, v54, s[50:51]\n"
      "s_mov_b32 s50, 0xffff0000\n"
      "s_mov_b32 s51, 0xffff0000\n"
      "v_cndmask_b32 v28, v28, v52, s[50:51]\n"
      "v_and_b32 v52, 7, v43\n"
      "v_lshl_add_u32 v28, v52, 2, v28\n"
      "v_lshlrev_b32 v9, 3, v52\n"
      "v_bfe_u32 v10, v43, 3, 2\n"
      "v_lshlrev_b32 v10, 2, v10\n"
      "v_lshl_add_u32 v58, v49, 8, v48\n"
      "s_lshl_b32 s50, s33, 5\n"
      "v_add_u32 v58, s50, v58\n"
      "s_mov_b32 s50, 122912\n"
      "v_lshl_add_u32 v32, v58, 2, s50\n"
      "v_lshlrev_b32 v41, 12, v58\n"
      "v_lshlrev_b32 v42, 4, v58\n"
      "v_lshl_add_u32 v52, v50, 1, v51\n"
      "v_lshl_add_u32 v33, v52, 4, s54\n"
      "s_add_u32 s50, s54, 528\n"
      "v_lshl_add_u32 v34, v43, 4, s50\n"
      "v_lshl_add_u32 v35, v43, 3, s39\n"
      "v_lshl_add_u32 v36, v43, 3, s52\n"
      "s_add_u32 s50, s39, 32\n"
      "v_lshl_add_u32 v37, v43, 4, s50\n"
      "s_add_u32 s50, s52, 32\n"
      "v_lshl_add_u32 v38, v43, 4, s50\n"
      "v_lshl_add_u32 v52, v50, 2, v51\n"
      "v_lshl_add_u32 v39, v52, 2, s53\n"
      "v_bfe_u32 v52, v43, 2, 2\n"
      "v_mul_u32_u24 v52, 0x2240, v52\n"
      "v_lshl_add_u32 v52, v49, 11, v52\n"
      "v_lshl_add_u32 v52, v55, 3, v52\n"
      "v_and_b32 v54, 16, v43\n"
      "v_lshl_add_u32 v52, v54, 1, v52\n"
      "s_lshl_b32 s50, s33, 6\n"
      "s_add_u32 s50, s50, 56384\n"
      "v_add_u32 v40, s50, v52\n"
      "v_mov_b32 v54, 0\n"
      "v_mov_b32 v55, 0\n"
      "v_lshl_add_u32 v52, v43, 3, s54\n"
      "ds_write_b64 v52, v[54:55] offset:32\n"
      "s_mov_b32 s50, 161536\n"
      "v_lshl_add_u32 v52, v0, 2, s50\n"
      "ds_write_b32 v52, v54 \n"
      "v_mov_b32 v52, 0xffff\n"
      "v_mov_b32 v53, 0xffff0000\n"
      "s_mov_b32 s50, 0x0\n"
      "s_mov_b32 s51, 0x18000018\n"
      "v_cndmask_b32 v44, 0, v52, s[50:51]\n"
      "s_mov_b32 s60, 0x0\n"
      "s_mov_b32 s61, 0x30000030\n"
      "v_cndmask_b32 v54, 0, v53, s[60:61]\n"
      "v_or_b32 v44, v44, v54\n"
      "s_mov_b32 s50, 0x0\n"
      "s_mov_b32 s51, 0x60000060\n"
      "v_cndmask_b32 v45, 0, v52, s[50:51]\n"
      "s_mov_b32 s60, 0x0\n"
      "s_mov_b32 s61, 0xc00000c0\n"
      "v_cndmask_b32 v54, 0, v53, s[60:61]\n"
      "v_or_b32 v45, v45, v54\n"
      "s_mov_b32 s50, 0x10000\n"
      "s_mov_b32 s51, 0x80000180\n"
      "v_cndmask_b32 v46, 0, v52, s[50:51]\n"
      "s_mov_b32 s60, 0x30000\n"
      "s_mov_b32 s61, 0x300\n"
      "v_cndmask_b32 v54, 0, v53, s[60:61]\n"
      "v_or_b32 v46, v46, v54\n"
      "s_mov_b32 s50, 0x60000\n"
      "s_mov_b32 s51, 0x600\n"
      "v_cndmask_b32 v47, 0, v52, s[50:51]\n"
      "s_mov_b32 s60, 0xc0000\n"
      "s_mov_b32 s61, 0xc00\n"
      "v_cndmask_b32 v54, 0, v53, s[60:61]\n"
      "v_or_b32 v47, v47, v54\n"
      ".Lsc%=:\n"
      "s_waitcnt vmcnt(0)\n"
      "s_mov_b32 s50, 0x3fb8aa3b\n"
      "buffer_load_dword v11, v15, s[20:23], s41 offen\n"
      "s_cmp_ge_u32 s33, 4\n"
      "s_cbranch_scc1 .LR%=\n"
      "global_load_dword v11, v57, s[56:57]\n"
      ".LR%=:\n"
      "v_lshlrev_b32 v48, 16, v12\n"
      "v_and_b32 v52, 0x7fff7fff, v18\n"
      "v_and_b32 v53, 0x7fff7fff, v19\n"
      "v_mul_f32 v49, s50, v48\n"
      "v_pk_max_u16 v52, v52, v53\n"
      "v_fma_f32 v50, v48, s50, -v49\n"
      "v_rndne_f32 v51, v49\n"
      "v_lshlrev_b32 v53, 16, v52\n"
      "v_fmamk_f32 v50, v48, 0x32a5705f, v50\n"
      "v_sub_f32 v49, v49, v51\n"
      "v_max_u32 v52, v52, v53\n"
      "v_add_f32 v49, v49, v50\n"
      "v_cvt_i32_f32 v51, v51\n"
      "v_max_u32_dpp v52, v52, v52 row_shr:1 row_mask:0xf bank_mask:0xf bound_ctrl:1\n"
      "v_exp_f32 v49, v49\n"
      "v_lshlrev_b32 v54, 16, v13\n"
      "v_max_u32_dpp v52, v52, v52 row_shr:2 row_mask:0xf bank_mask:0xf bound_ctrl:1\n"
      "v_ldexp_f32 v49, v49, v51\n"
      "v_add_f32 v54, 1.0, v54\n"
      "v_max_u32_dpp v52, v52, v52 row_shr:4 row_mask:0xf bank_mask:0xf bound_ctrl:1\n"
      "v_cvt_pk_bf16_f32 v49, v49, v49\n"
      "s_waitcnt lgkmcnt(0)\n"
      "v_max_u32_dpp v52, v52, v52 row_shr:8 row_mask:0xf bank_mask:0xf bound_ctrl:1\n"
      "v_and_b32 v49, 0xffff0000, v49\n"
      "v_and_b32 v55, 0x7fffffff, v54\n"
      "v_max_u32_dpp v52, v52, v52 row_bcast:15 row_mask:0xa bank_mask:0xf\n"
      "v_mul_f32 v50, 0xbfb8aa3b, v49\n"
      "v_mul_f32 v51, 0xbe22f983, v49\n"
      "v_max_u32_dpp v52, v52, v52 row_bcast:31 row_mask:0xc bank_mask:0xf\n"
      "v_readfirstlane_b32 s36, v50\n"
      "v_readlane_b32 s50, v52, 63\n"
      "v_readfirstlane_b32 s37, v51\n"
      "v_mul_f32 v55, 0x3b800000, v55\n"
      "v_mul_f32 v52, s50, v49\n"
      "s_add_u32 s15, s15, 0x9e3779b1\n"
      "v_max_f32 v52, v52, v55\n"
      "v_frexp_exp_i32_f32 v52, v52\n"
      "s_nop 0\n"
      "v_readfirstlane_b32 s50, v52\n"
      "s_nop 0\n"
      "s_sub_i32 s50, 7, s50\n"
      "s_max_i32 s50, s50, -107\n"
      "s_min_i32 s50, s50, 93\n"
      "v_ldexp_f32 v49, v49, s50\n"
      "s_sub_i32 s51, s50, 1\n"
      "v_ldexp_f32 v54, v54, s51\n"
      "s_sub_i32 s14, 128, s50\n"
      "s_lshl_b32 s14, s14, 23\n"
      "v_readfirstlane_b32 s38, v49\n"
      "s_mov_b64 s[60:61], 1\n"
      "v_cndmask_b32 v31, 0, v54, s[60:61]\n"
      "s_lshl_b32 s50, s33, 2\n"
      "s_add_u32 s50, s50, 161280\n"
      "v_mov_b32 v50, s50\n"
      "v_mov_b32 v51, s14\n"
      "ds_write_b32 v50, v51 \n"
      "s_mov_b32 s60, s41\n"
      "s_add_u32 s61, s41, 0x200000\n"
      "s_movk_i32 s62, 8192\n"
      "s_movk_i32 s63, 16384\n"
      "s_mov_b32 m0, s40\n"
      "s_add_u32 s13, s40, 52224\n"
      "s_add_u32 s12, s39, 272\n"
      "v_lshlrev_b32 v72, 16, v16\n"
      "v_and_b32 v73, 0xffff0000, v16\n"
      "v_lshlrev_b32 v74, 16, v17\n"
      "v_and_b32 v75, 0xffff0000, v17\n"
      "s_mov_b32 s64, 0x3fb8aa3b\n"
      "s_mov_b32 s66, 0x43800000\n"
      "s_mov_b32 s68, 0x42000000\n"
      "s_mov_b32 s70, s37\n"
      "v_pk_mul_f32 v[72:73], s[64:65], v[72:73] op_sel_hi:[0,1]\n"
      "v_pk_mul_f32 v[74:75], s[70:71], v[74:75] op_sel_hi:[0,1]\n"
      "v_exp_f32 v72, v72\n"
      "v_exp_f32 v73, v73\n"
      "v_pk_mul_f32 v[78:79], s[66:67], v[74:75] op_sel_hi:[0,1]\n"
      "v_pk_mul_f32 v[80:81], s[68:69], v[74:75] op_sel_hi:[0,1]\n"
      "v_fract_f32 v76, v74\n"
      "v_fract_f32 v77, v75\n"
      "v_cvt_pk_bf16_f32 v72, v72, v73\n"
      "v_cos_f32 v90, v76\n"
      "v_cos_f32 v91, v77\n"
      "v_fract_f32 v78, v78\n"
      "v_fract_f32 v79, v79\n"
      "v_and_b32 v73, 0xffff0000, v72\n"
      "v_lshlrev_b32 v72, 16, v72\n"
      "v_sin_f32 v92, v76\n"
      "v_sin_f32 v93, v77\n"
      "v_pk_mul_f32 v[82:83], s[36:37], v[72:73] op_sel_hi:[0,1]\n"
      "v_fract_f32 v80, v80\n"
      "v_fract_f32 v81, v81\n"
      "v_exp_f32 v84, v82\n"
      "v_exp_f32 v85, v83\n"
      "v_pk_mul_f32 v[86:87], s[66:67], v[82:83] op_sel_hi:[0,1]\n"
      "v_cos_f32 v94, v78\n"
      "v_cos_f32 v95, v79\n"
      "v_pk_mul_f32 v[88:89], s[68:69], v[82:83] op_sel_hi:[0,1]\n"
      "v_sin_f32 v96, v78\n"
      "v_sin_f32 v97, v79\n"
      "v_pk_mul_f32 v[90:91], v[90:91], v[84:85]\n"
      "v_exp_f32 v86, v86\n"
      "v_exp_f32 v87, v87\n"
      "v_pk_mul_f32 v[92:93], v[92:93], v[84:85]\n"
      "v_pk_add_f32 v[82:83], v[90:91], v[92:93]\n"
      "v_cos_f32 v202, v80\n"
      "v_pk_add_f32 v[84:85], v[90:91], v[92:93] neg_lo:[0,1] neg_hi:[0,1]\n"
      "v_cos_f32 v150, v81\n"
      "v_pk_mul_f32 v[100:101], v[90:91], v[92:93]\n"
      "v_pk_mul_f32 v[94:95], v[94:95], v[86:87]\n"
      "v_pk_mul_f32 v[98:99], v[82:83], v[84:85]\n"
      "v_pk_mul_f32 v[96:97], v[96:97], v[86:87]\n"
      "v_pk_add_f32 v[100:101], v[100:101], v[100:101]\n"
      "v_sin_f32 v203, v80\n"
      "v_cvt_pk_f16_f32 v208, v90, v92\n"
      "v_sin_f32 v151, v81\n"
      "v_cvt_pk_f16_f32 v156, v91, v93\n"
      "v_exp_f32 v88, v88\n"
      "v_pk_add_f32 v[82:83], v[98:99], v[100:101]\n"
      "v_exp_f32 v89, v89\n"
      "v_pk_add_f32 v[84:85], v[98:99], v[100:101] neg_lo:[0,1] neg_hi:[0,1]\n"
      "v_pk_add_f32 v[114:115], v[94:95], v[96:97]\n"
      "v_pk_mul_f32 v[104:105], v[98:99], v[100:101]\n"
      "v_pk_add_f32 v[116:117], v[94:95], v[96:97] neg_lo:[0,1] neg_hi:[0,1]\n"
      "v_pk_mul_f32 v[102:103], v[82:83], v[84:85]\n"
      "v_pk_mul_f32 v[108:109], v[94:95], v[96:97]\n"
      "v_pk_add_f32 v[104:105], v[104:105], v[104:105]\n"
      "v_pk_mul_f32 v[106:107], v[114:115], v[116:117]\n"
      "v_cvt_pk_f16_f32 v209, v98, v100\n"
      "v_pk_add_f32 v[108:109], v[108:109], v[108:109]\n"
      "v_cvt_pk_f16_f32 v157, v99, v101\n"
      "v_cvt_pk_f16_f32 v212, v94, v96\n"
      "v_pk_add_f32 v[82:83], v[102:103], v[104:105]\n"
      "v_cvt_pk_f16_f32 v160, v95, v97\n"
      "v_pk_add_f32 v[84:85], v[102:103], v[104:105] neg_lo:[0,1] neg_hi:[0,1]\n"
      "v_pk_mul_f32 v[202:203], v[202:203], v[88:89] op_sel:[0,0] op_sel_hi:[1,0]\n"
      "v_pk_mul_f32 v[92:93], v[102:103], v[104:105]\n"
      "v_pk_mul_f32 v[150:151], v[150:151], v[88:89] op_sel:[0,1] op_sel_hi:[1,1]\n"
      "v_pk_mul_f32 v[90:91], v[82:83], v[84:85]\n"
      "v_pk_mul_f32 v[110:111], v[108:109], v[96:97]\n"
      "v_pk_add_f32 v[92:93], v[92:93], v[92:93]\n"
      "v_pk_mul_f32 v[112:113], v[108:109], v[94:95]\n"
      "v_cvt_pk_f16_f32 v210, v102, v104\n"
      "v_pk_fma_f32 v[110:111], v[106:107], v[94:95], v[110:111] neg_lo:[0,0,1] neg_hi:[0,0,1]\n"
      "v_cvt_pk_f16_f32 v158, v103, v105\n"
      "v_pk_fma_f32 v[112:113], v[106:107], v[96:97], v[112:113]\n"
      "v_pk_add_f32 v[82:83], v[90:91], v[92:93]\n"
      "v_cvt_pk_f16_f32 v213, v106, v108\n"
      "v_pk_add_f32 v[84:85], v[90:91], v[92:93] neg_lo:[0,1] neg_hi:[0,1]\n"
      "v_cvt_pk_f16_f32 v161, v107, v109\n"
      "v_pk_mul_f32 v[74:75], v[90:91], v[92:93]\n"
      "v_pk_add_f32 v[114:115], v[106:107], v[108:109]\n"
      "v_pk_mul_f32 v[72:73], v[82:83], v[84:85]\n"
      "v_pk_add_f32 v[116:117], v[106:107], v[108:109] neg_lo:[0,1] neg_hi:[0,1]\n"
      "v_pk_add_f32 v[74:75], v[74:75], v[74:75]\n"
      "v_pk_mul_f32 v[96:97], v[106:107], v[108:109]\n"
      "v_cvt_pk_f16_f32 v205, v90, v92\n"
      "v_pk_mul_f32 v[94:95], v[114:115], v[116:117]\n"
      "v_cvt_pk_f16_f32 v153, v91, v93\n"
      "v_pk_add_f32 v[96:97], v[96:97], v[96:97]\n"
      "v_pk_mul_f32 v[82:83], v[74:75], v[92:93]\n"
      "v_cvt_pk_f16_f32 v214, v110, v112\n"
      "v_pk_mul_f32 v[84:85], v[74:75], v[90:91]\n"
      "v_cvt_pk_f16_f32 v162, v111, v113\n"
      "v_pk_fma_f32 v[82:83], v[72:73], v[90:91], v[82:83] neg_lo:[0,0,1] neg_hi:[0,0,1]\n"
      "v_pk_mul_f32 v[76:77], v[100:101], v[104:105]\n"
      "v_pk_fma_f32 v[84:85], v[72:73], v[92:93], v[84:85]\n"
      "v_pk_mul_f32 v[78:79], v[100:101], v[102:103]\n"
      "v_cvt_pk_f16_f32 v206, v72, v74\n"
      "v_pk_fma_f32 v[76:77], v[98:99], v[102:103], v[76:77] neg_lo:[0,0,1] neg_hi:[0,0,1]\n"
      "v_cvt_pk_f16_f32 v154, v73, v75\n"
      "v_pk_fma_f32 v[78:79], v[98:99], v[104:105], v[78:79]\n"
      "v_cvt_pk_f16_f32 v207, v82, v84\n"
      "v_cvt_pk_f16_f32 v215, v94, v96\n"
      "v_cvt_pk_f16_f32 v155, v83, v85\n"
      "v_cvt_pk_f16_f32 v163, v95, v97\n"
      "v_cvt_pk_f16_f32 v211, v76, v78\n"
      "v_cvt_pk_f16_f32 v159, v77, v79\n"
      "v_lshlrev_b32 v198, 16, v18\n"
      "v_lshlrev_b32 v199, 16, v19\n"
      "v_pk_mul_f32 v[198:199], s[38:39], v[198:199] op_sel_hi:[0,1]\n"
      "v_and_b32 v146, 0xffff0000, v18\n"
      "v_and_b32 v147, 0xffff0000, v19\n"
      "v_pk_mul_f32 v[146:147], s[38:39], v[146:147] op_sel_hi:[0,1]\n"
      "v_cvt_pk_f16_f32 v240, v198, v199\n"
      "v_cvt_pk_f16_f32 v241, v146, v147\n"
      "v_pk_mul_f32 v[200:201], v[198:199], v[202:203] op_sel_hi:[1,0]\n"
      "v_pk_mul_f32 v[148:149], v[146:147], v[150:151] op_sel_hi:[1,0]\n"
      "ds_write_addtid_b32 v240 offset:52224\n"
      "ds_write_addtid_b32 v241 offset:55488\n"
      "v_pk_fma_f32 v[200:201], v[198:199], v[202:203], v[200:201] op_sel:[1,1,0] op_sel_hi:[0,1,1] neg_hi:[0,1,0]\n"
      "v_pk_fma_f32 v[148:149], v[146:147], v[150:151], v[148:149] op_sel:[1,1,0] op_sel_hi:[0,1,1] neg_hi:[0,1,0]\n"
      "ds_write_addtid_b32 v205 offset:54672\n"
      "v_cvt_pk_f16_f32 v242, v200, v201\n"
      "v_cvt_pk_f16_f32 v243, v148, v149\n"
      "v_pk_mul_f32 v[198:199], v[200:201], v[202:203] op_sel_hi:[1,0]\n"
      "v_pk_mul_f32 v[146:147], v[148:149], v[150:151] op_sel_hi:[1,0]\n"
      "ds_write_addtid_b32 v242 offset:52496\n"
      "ds_write_addtid_b32 v243 offset:55760\n"
      "v_pk_fma_f32 v[198:199], v[200:201], v[202:203], v[198:199] op_sel:[1,1,0] op_sel_hi:[0,1,1] neg_hi:[0,1,0]\n"
      "v_pk_fma_f32 v[146:147], v[148:149], v[150:151], v[146:147] op_sel:[1,1,0] op_sel_hi:[0,1,1] neg_hi:[0,1,0]\n"
      "ds_write_addtid_b32 v153 offset:57936\n"
      "v_cvt_pk_f16_f32 v240, v198, v199\n"
      "v_cvt_pk_f16_f32 v241, v146, v147\n"
      "v_pk_mul_f32 v[200:201], v[198:199], v[202:203] op_sel_hi:[1,0]\n"
      "v_pk_mul_f32 v[148:149], v[146:147], v[150:151] op_sel_hi:[1,0]\n"
      "ds_write_addtid_b32 v240 offset:52768\n"
      "ds_write_addtid_b32 v241 offset:56032\n"
      "v_pk_fma_f32 v[200:201], v[198:199], v[202:203], v[200:201] op_sel:[1,1,0] op_sel_hi:[0,1,1] neg_hi:[0,1,0]\n"
      "v_pk_fma_f32 v[148:149], v[146:147], v[150:151], v[148:149] op_sel:[1,1,0] op_sel_hi:[0,1,1] neg_hi:[0,1,0]\n"
      "ds_write_addtid_b32 v206 offset:54944\n"
      "v_cvt_pk_f16_f32 v242, v200, v201\n"
      "v_cvt_pk_f16_f32 v243, v148, v149\n"
      "v_pk_mul_f32 v[198:199], v[200:201], v[202:203] op_sel_hi:[1,0]\n"
      "v_pk_mul_f32 v[146:147], v[148:149], v[150:151] op_sel_hi:[1,0]\n"
      "ds_write_addtid_b32 v242 offset:53040\n"
      "ds_write_addtid_b32 v243 offset:56304\n"
      "v_pk_fma_f32 v[198:199], v[200:201], v[202:203], v[198:199] op_sel:[1,1,0] op_sel_hi:[0,1,1] neg_hi:[0,1,0]\n"
      "v_pk_fma_f32 v[146:147], v[148:149], v[150:151], v[146:147] op_sel:[1,1,0] op_sel_hi:[0,1,1] neg_hi:[0,1,0]\n"
      "ds_write_addtid_b32 v154 offset:58208\n"
      "v_cvt_pk_f16_f32 v240, v198, v199\n"
      "v_cvt_pk_f16_f32 v241, v146, v147\n"
      "v_pk_mul_f32 v[200:201], v[198:199], v[202:203] op_sel_hi:[1,0]\n"
      "v_pk_mul_f32 v[148:149], v[146:147], v[150:151] op_sel_hi:[1,0]\n"
      "ds_write_addtid_b32 v240 offset:53312\n"
      "ds_write_addtid_b32 v241 offset:56576\n"
      "v_pk_fma_f32 v[200:201], v[198:199], v[202:203], v[200:201] op_sel:[1,1,0] op_sel_hi:[0,1,1] neg_hi:[0,1,0]\n"
      "v_pk_fma_f32 v[148:149], v[146:147], v[150:151], v[148:149] op_sel:[1,1,0] op_sel_hi:[0,1,1] neg_hi:[0,1,0]\n"
      "ds_write_addtid_b32 v207 offset:55216\n"
      "v_cvt_pk_f16_f32 v242, v200, v201\n"
      "v_cvt_pk_f16_f32 v243, v148, v149\n"
      "v_pk_mul_f32 v[198:199], v[200:201], v[202:203] op_sel_hi:[1,0]\n"
      "v_pk_mul_f32 v[146:147], v[148:149], v[150:151] op_sel_hi:[1,0]\n"
      "ds_write_addtid_b32 v242 offset:53584\n"
      "ds_write_addtid_b32 v243 offset:56848\n"
      "v_pk_fma_f32 v[198:199], v[200:201], v[202:203], v[198:199] op_sel:[1,1,0] op_sel_hi:[0,1,1] neg_hi:[0,1,0]\n"
      "v_pk_fma_f32 v[146:147], v[148:149], v[150:151], v[146:147] op_sel:[1,1,0] op_sel_hi:[0,1,1] neg_hi:[0,1,0]\n"
      "ds_write_addtid_b32 v155 offset:58480\n"
      "v_cvt_pk_f16_f32 v240, v198, v199\n"
      "v_cvt_pk_f16_f32 v241, v146, v147\n"
      "v_pk_mul_f32 v[200:201], v[198:199], v[202:203] op_sel_hi:[1,0]\n"
      "v_pk_mul_f32 v[148:149], v[146:147], v[150:151] op_sel_hi:[1,0]\n"
      "ds_write_addtid_b32 v240 offset:53856\n"
      "ds_write_addtid_b32 v241 offset:57120\n"
      "v_pk_fma_f32 v[200:201], v[198:199], v[202:203], v[200:201] op_sel:[1,1,0] op_sel_hi:[0,1,1] neg_hi:[0,1,0]\n"
      "v_pk_fma_f32 v[148:149], v[146:147], v[150:151], v[148:149] op_sel:[1,1,0] op_sel_hi:[0,1,1] neg_hi:[0,1,0]\n"
      "v_cvt_pk_f16_f32 v242, v200, v201\n"
      "v_cvt_pk_f16_f32 v243, v148, v149\n"
      "ds_write_addtid_b32 v242 offset:54128\n"
      "ds_write_addtid_b32 v243 offset:57392\n"
      "ds_read_b128 v[104:107], v20 offset:0\n"
      "ds_read_b128 v[108:111], v178 offset:0\n"
      "ds_read_b128 v[112:115], v20 offset:32\n"
      "ds_read_b128 v[116:119], v178 offset:32\n"
      "s_mov_b32 m0, s39\n"
      "s_setprio 2\n"
      "v_pk_mul_f16 v240, v209, v208 op_sel_hi:[0,1]\n"
      "v_pk_mul_f16 v241, v210, v208 op_sel_hi:[0,1]\n"
      "v_pk_mul_f16 v242, v211, v208 op_sel_hi:[0,1]\n"
      "ds_write_addtid_b32 v208 offset:272\n"
      "v_pk_fma_f16 v240, v209, v208, v240 op_sel:[1,1,0] op_sel_hi:[1,0,1] neg_lo:[0,1,0]\n"
      "ds_write_addtid_b32 v209 offset:544\n"
      "v_pk_fma_f16 v241, v210, v208, v241 op_sel:[1,1,0] op_sel_hi:[1,0,1] neg_lo:[0,1,0]\n"
      "ds_write_addtid_b32 v240 offset:816\n"
      "v_pk_fma_f16 v242, v211, v208, v242 op_sel:[1,1,0] op_sel_hi:[1,0,1] neg_lo:[0,1,0]\n"
      "ds_write_addtid_b32 v241 offset:1360\n"
      "ds_write_addtid_b32 v242 offset:1904\n"
      "v_pk_mul_f16 v243, v212, v209 op_sel_hi:[0,1]\n"
      "v_pk_mul_f16 v244, v212, v210 op_sel_hi:[0,1]\n"
      "v_pk_mul_f16 v245, v212, v211 op_sel_hi:[0,1]\n"
      "ds_write_addtid_b32 v210 offset:1088\n"
      "v_pk_fma_f16 v243, v212, v209, v243 op_sel:[1,1,0] op_sel_hi:[1,0,1] neg_lo:[0,1,0]\n"
      "ds_write_addtid_b32 v211 offset:1632\n"
      "v_pk_fma_f16 v244, v212, v210, v244 op_sel:[1,1,0] op_sel_hi:[1,0,1] neg_lo:[0,1,0]\n"
      "ds_write_addtid_b32 v243 offset:2992\n"
      "v_pk_fma_f16 v245, v212, v211, v245 op_sel:[1,1,0] op_sel_hi:[1,0,1] neg_lo:[0,1,0]\n"
      "ds_write_addtid_b32 v244 offset:3264\n"
      "ds_write_addtid_b32 v245 offset:4080\n"
      "v_pk_mul_f16 v246, v213, v210 op_sel_hi:[0,1]\n"
      "v_pk_mul_f16 v247, v214, v210 op_sel_hi:[0,1]\n"
      "v_pk_mul_f16 v248, v215, v215 op_sel_hi:[0,1]\n"
      "ds_write_addtid_b32 v212 offset:2176\n"
      "v_pk_fma_f16 v246, v213, v210, v246 op_sel:[1,1,0] op_sel_hi:[1,0,1] neg_lo:[0,1,0]\n"
      "ds_write_addtid_b32 v213 offset:5440\n"
      "v_pk_fma_f16 v247, v214, v210, v247 op_sel:[1,1,0] op_sel_hi:[1,0,1] neg_lo:[0,1,0]\n"
      "ds_write_addtid_b32 v246 offset:5984\n"
      "v_pk_fma_f16 v248, v215, v215, v248 op_sel:[1,1,0] op_sel_hi:[1,0,1] neg_lo:[0,1,0]\n"
      "ds_write_addtid_b32 v247 offset:6256\n"
      "ds_write_addtid_b32 v248 offset:2448\n"
      "v_pk_mul_f16 v249, v215, v212 op_sel_hi:[0,1]\n"
      "v_pk_mul_f16 v250, v215, v213 op_sel_hi:[0,1]\n"
      "v_pk_mul_f16 v251, v215, v214 op_sel_hi:[0,1]\n"
      "ds_write_addtid_b32 v214 offset:5712\n"
      "v_pk_fma_f16 v249, v215, v212, v249 op_sel:[1,1,0] op_sel_hi:[1,0,1] neg_lo:[0,1,0]\n"
      "ds_write_addtid_b32 v215 offset:4352\n"
      "v_pk_fma_f16 v250, v215, v213, v250 op_sel:[1,1,0] op_sel_hi:[1,0,1] neg_lo:[0,1,0]\n"
      "ds_write_addtid_b32 v249 offset:4624\n"
      "v_pk_fma_f16 v251, v215, v214, v251 op_sel:[1,1,0] op_sel_hi:[1,0,1] neg_lo:[0,1,0]\n"
      "ds_write_addtid_b32 v250 offset:4896\n"
      "ds_write_addtid_b32 v251 offset:5168\n"
      "ds_read_b128 v[72:75], v21 offset:0\n"
      "ds_read_b128 v[76:79], v21 offset:32\n"
      "ds_read_b128 v[80:83], v21 offset:64\n"
      "ds_read_b128 v[84:87], v21 offset:96\n"
      "ds_read_b128 v[88:91], v21 offset:128\n"
      "ds_read_b128 v[92:95], v21 offset:160\n"
      "ds_read_b128 v[96:99], v21 offset:192\n"
      "ds_read_b128 v[100:103], v21 offset:224\n"
      "ds_read_b128 v[120:123], v20 offset:64\n"
      "ds_read_b128 v[124:127], v178 offset:64\n"
      "s_waitcnt lgkmcnt(15)\n"
      "v_pk_mul_f16 v164, v104, v108 op_sel_hi:[0,1] neg_hi:[0,1]\n"
      "v_pk_mul_f16 v165, v105, v109 op_sel_hi:[0,1] neg_hi:[0,1]\n"
      "v_pk_mul_f16 v166, v106, v110 op_sel_hi:[0,1] neg_hi:[0,1]\n"
      "v_pk_mul_f16 v167, v107, v111 op_sel_hi:[0,1] neg_hi:[0,1]\n"
      "v_pk_fma_f16 v164, v104, v108, v164 op_sel:[1,1,0] op_sel_hi:[1,0,1]\n"
      "v_pk_fma_f16 v165, v105, v109, v165 op_sel:[1,1,0] op_sel_hi:[1,0,1]\n"
      "v_pk_fma_f16 v166, v106, v110, v166 op_sel:[1,1,0] op_sel_hi:[1,0,1]\n"
      "v_pk_fma_f16 v167, v107, v111, v167 op_sel:[1,1,0] op_sel_hi:[1,0,1]\n"
      "s_waitcnt lgkmcnt(9)\n"
      "s_nop 0\n"
      "v_mfma_f32_32x32x16_f16 v[180:195], v[72:75], v[164:167], 0\n"
      "v_pk_mul_f16 v240, v157, v156 op_sel_hi:[0,1]\n"
      "v_pk_mul_f16 v241, v158, v156 op_sel_hi:[0,1]\n"
      "v_pk_mul_f16 v242, v159, v156 op_sel_hi:[0,1]\n"
      "v_pk_mul_f16 v168, v112, v116 op_sel_hi:[0,1] neg_hi:[0,1]\n"
      "v_pk_mul_f16 v169, v113, v117 op_sel_hi:[0,1] neg_hi:[0,1]\n"
      "v_pk_mul_f16 v170, v114, v118 op_sel_hi:[0,1] neg_hi:[0,1]\n"
      "v_pk_mul_f16 v171, v115, v119 op_sel_hi:[0,1] neg_hi:[0,1]\n"
      "ds_write_addtid_b32 v156 offset:272\n"
      "v_pk_fma_f16 v240, v157, v156, v240 op_sel:[1,1,0] op_sel_hi:[1,0,1] neg_lo:[0,1,0]\n"
      "ds_write_addtid_b32 v157 offset:544\n"
      "v_pk_fma_f16 v168, v112, v116, v168 op_sel:[1,1,0] op_sel_hi:[1,0,1]\n"
      "v_pk_fma_f16 v169, v113, v117, v169 op_sel:[1,1,0] op_sel_hi:[1,0,1]\n"
      "v_pk_fma_f16 v170, v114, v118, v170 op_sel:[1,1,0] op_sel_hi:[1,0,1]\n"
      "v_pk_fma_f16 v171, v115, v119, v171 op_sel:[1,1,0] op_sel_hi:[1,0,1]\n"
      "ds_read_b128 v[104:107], v20 offset:96\n"
      "ds_read_b128 v[108:111], v178 offset:96\n"
      "s_waitcnt lgkmcnt(12)\n"
      "v_mfma_f32_32x32x16_f16 v[180:195], v[76:79], v[168:171], v[180:195]\n"
      "v_pk_fma_f16 v241, v158, v156, v241 op_sel:[1,1,0] op_sel_hi:[1,0,1] neg_lo:[0,1,0]\n"
      "ds_write_addtid_b32 v240 offset:816\n"
      "v_pk_fma_f16 v242, v159, v156, v242 op_sel:[1,1,0] op_sel_hi:[1,0,1] neg_lo:[0,1,0]\n"
      "s_waitcnt lgkmcnt(5)\n"
      "v_pk_mul_f16 v164, v120, v124 op_sel_hi:[0,1] neg_hi:[0,1]\n"
      "v_pk_mul_f16 v165, v121, v125 op_sel_hi:[0,1] neg_hi:[0,1]\n"
      "v_pk_mul_f16 v166, v122, v126 op_sel_hi:[0,1] neg_hi:[0,1]\n"
      "v_pk_mul_f16 v167, v123, v127 op_sel_hi:[0,1] neg_hi:[0,1]\n"
      "ds_write_addtid_b32 v241 offset:1360\n"
      "ds_write_addtid_b32 v242 offset:1904\n"
      "v_pk_mul_f16 v243, v160, v157 op_sel_hi:[0,1]\n"
      "v_pk_fma_f16 v164, v120, v124, v164 op_sel:[1,1,0] op_sel_hi:[1,0,1]\n"
      "v_pk_fma_f16 v165, v121, v125, v165 op_sel:[1,1,0] op_sel_hi:[1,0,1]\n"
      "v_pk_fma_f16 v166, v122, v126, v166 op_sel:[1,1,0] op_sel_hi:[1,0,1]\n"
      "v_pk_fma_f16 v167, v123, v127, v167 op_sel:[1,1,0] op_sel_hi:[1,0,1]\n"
      "ds_read_b128 v[112:115], v20 offset:128\n"
      "ds_read_b128 v[116:119], v178 offset:128\n"
      "v_mfma_f32_32x32x16_f16 v[180:195], v[80:83], v[164:167], v[180:195]\n"
      "v_pk_mul_f16 v244, v160, v158 op_sel_hi:[0,1]\n"
      "v_pk_mul_f16 v245, v160, v159 op_sel_hi:[0,1]\n"
      "ds_write_addtid_b32 v158 offset:1088\n"
      "s_waitcnt lgkmcnt(6)\n"
      "v_pk_mul_f16 v168, v104, v108 op_sel_hi:[0,1] neg_hi:[0,1]\n"
      "v_pk_mul_f16 v169, v105, v109 op_sel_hi:[0,1] neg_hi:[0,1]\n"
      "v_pk_mul_f16 v170, v106, v110 op_sel_hi:[0,1] neg_hi:[0,1]\n"
      "v_pk_mul_f16 v171, v107, v111 op_sel_hi:[0,1] neg_hi:[0,1]\n"
      "v_pk_fma_f16 v243, v160, v157, v243 op_sel:[1,1,0] op_sel_hi:[1,0,1] neg_lo:[0,1,0]\n"
      "ds_write_addtid_b32 v159 offset:1632\n"
      "v_pk_fma_f16 v244, v160, v158, v244 op_sel:[1,1,0] op_sel_hi:[1,0,1] neg_lo:[0,1,0]\n"
      "v_pk_fma_f16 v168, v104, v108, v168 op_sel:[1,1,0] op_sel_hi:[1,0,1]\n"
      "v_pk_fma_f16 v169, v105, v109, v169 op_sel:[1,1,0] op_sel_hi:[1,0,1]\n"
      "v_pk_fma_f16 v170, v106, v110, v170 op_sel:[1,1,0] op_sel_hi:[1,0,1]\n"
      "v_pk_fma_f16 v171, v107, v111, v171 op_sel:[1,1,0] op_sel_hi:[1,0,1]\n"
      "ds_read_b128 v[120:123], v20 offset:160\n"
      "ds_read_b128 v[124:127], v178 offset:160\n"
      "buffer_load_dwordx4 v[48:51], v41, s[20:23], s60 offen nt\n"
      "v_mfma_f32_32x32x16_f16 v[180:195], v[84:87], v[168:171], v[180:195]\n"
      "ds_write_addtid_b32 v243 offset:2992\n"
      "v_pk_fma_f16 v245, v160, v159, v245 op_sel:[1,1,0] op_sel_hi:[1,0,1] neg_lo:[0,1,0]\n"
      "ds_write_addtid_b32 v244 offset:3264\n"
      "s_waitcnt lgkmcnt(6)\n"
      "v_pk_mul_f16 v164, v112, v116 op_sel_hi:[0,1] neg_hi:[0,1]\n"
      "v_pk_mul_f16 v165, v113, v117 op_sel_hi:[0,1] neg_hi:[0,1]\n"
      "v_pk_mul_f16 v166, v114, v118 op_sel_hi:[0,1] neg_hi:[0,1]\n"
      "v_pk_mul_f16 v167, v115, v119 op_sel_hi:[0,1] neg_hi:[0,1]\n"
      "ds_write_addtid_b32 v245 offset:4080\n"
      "v_pk_mul_f16 v246, v161, v158 op_sel_hi:[0,1]\n"
      "v_pk_mul_f16 v247, v162, v158 op_sel_hi:[0,1]\n"
      "v_pk_fma_f16 v164, v112, v116, v164 op_sel:[1,1,0] op_sel_hi:[1,0,1]\n"
      "v_pk_fma_f16 v165, v113, v117, v165 op_sel:[1,1,0] op_sel_hi:[1,0,1]\n"
      "v_pk_fma_f16 v166, v114, v118, v166 op_sel:[1,1,0] op_sel_hi:[1,0,1]\n"
      "v_pk_fma_f16 v167, v115, v119, v167 op_sel:[1,1,0] op_sel_hi:[1,0,1]\n"
      "ds_read_b128 v[104:107], v20 offset:192\n"
      "ds_read_b128 v[108:111], v178 offset:192\n"
      "v_mfma_f32_32x32x16_f16 v[180:195], v[88:91], v[164:167], v[180:195]\n"
      "v_pk_mul_f16 v248, v163, v163 op_sel_hi:[0,1]\n"
      "ds_write_addtid_b32 v160 offset:2176\n"
      "v_pk_fma_f16 v246, v161, v158, v246 op_sel:[1,1,0] op_sel_hi:[1,0,1] neg_lo:[0,1,0]\n"
      "s_waitcnt lgkmcnt(6)\n"
      "v_pk_mul_f16 v168, v120, v124 op_sel_hi:[0,1] neg_hi:[0,1]\n"
      "v_pk_mul_f16 v169, v121, v125 op_sel_hi:[0,1] neg_hi:[0,1]\n"
      "v_pk_mul_f16 v170, v122, v126 op_sel_hi:[0,1] neg_hi:[0,1]\n"
      "v_pk_mul_f16 v171, v123, v127 op_sel_hi:[0,1] neg_hi:[0,1]\n"
      "ds_write_addtid_b32 v161 offset:5440\n"
      "v_pk_fma_f16 v247, v162, v158, v247 op_sel:[1,1,0] op_sel_hi:[1,0,1] neg_lo:[0,1,0]\n"
      "ds_write_addtid_b32 v246 offset:5984\n"
      "v_pk_fma_f16 v168, v120, v124, v168 op_sel:[1,1,0] op_sel_hi:[1,0,1]\n"
      "v_pk_fma_f16 v169, v121, v125, v169 op_sel:[1,1,0] op_sel_hi:[1,0,1]\n"
      "v_pk_fma_f16 v170, v122, v126, v170 op_sel:[1,1,0] op_sel_hi:[1,0,1]\n"
      "v_pk_fma_f16 v171, v123, v127, v171 op_sel:[1,1,0] op_sel_hi:[1,0,1]\n"
      "ds_read_b128 v[112:115], v20 offset:224\n"
      "ds_read_b128 v[116:119], v178 offset:224\n"
      "v_mfma_f32_32x32x16_f16 v[180:195], v[92:95], v[168:171], v[180:195]\n"
      "v_pk_fma_f16 v248, v163, v163, v248 op_sel:[1,1,0] op_sel_hi:[1,0,1] neg_lo:[0,1,0]\n"
      "ds_write_addtid_b32 v247 offset:6256\n"
      "ds_write_addtid_b32 v248 offset:2448\n"
      "s_waitcnt lgkmcnt(7)\n"
      "v_pk_mul_f16 v164, v104, v108 op_sel_hi:[0,1] neg_hi:[0,1]\n"
      "v_pk_mul_f16 v165, v105, v109 op_sel_hi:[0,1] neg_hi:[0,1]\n"
      "v_pk_mul_f16 v166, v106, v110 op_sel_hi:[0,1] neg_hi:[0,1]\n"
      "v_pk_mul_f16 v167, v107, v111 op_sel_hi:[0,1] neg_hi:[0,1]\n"
      "v_pk_mul_f16 v249, v163, v160 op_sel_hi:[0,1]\n"
      "v_pk_mul_f16 v250, v163, v161 op_sel_hi:[0,1]\n"
      "v_pk_mul_f16 v251, v163, v162 op_sel_hi:[0,1]\n"
      "v_pk_fma_f16 v164, v104, v108, v164 op_sel:[1,1,0] op_sel_hi:[1,0,1]\n"
      "v_pk_fma_f16 v165, v105, v109, v165 op_sel:[1,1,0] op_sel_hi:[1,0,1]\n"
      "v_pk_fma_f16 v166, v106, v110, v166 op_sel:[1,1,0] op_sel_hi:[1,0,1]\n"
      "v_pk_fma_f16 v167, v107, v111, v167 op_sel:[1,1,0] op_sel_hi:[1,0,1]\n"
      "ds_read_b128 v[120:123], v20 offset:3264\n"
      "ds_read_b128 v[124:127], v178 offset:3264\n"
      "v_mfma_f32_32x32x16_f16 v[180:195], v[96:99], v[164:167], v[180:195]\n"
      "ds_write_addtid_b32 v162 offset:5712\n"
      "v_pk_fma_f16 v249, v163, v160, v249 op_sel:[1,1,0] op_sel_hi:[1,0,1] neg_lo:[0,1,0]\n"
      "ds_write_addtid_b32 v163 offset:4352\n"
      "s_waitcnt lgkmcnt(6)\n"
      "v_pk_mul_f16 v168, v112, v116 op_sel_hi:[0,1] neg_hi:[0,1]\n"
      "v_pk_mul_f16 v169, v113, v117 op_sel_hi:[0,1] neg_hi:[0,1]\n"
      "v_pk_mul_f16 v170, v114, v118 op_sel_hi:[0,1] neg_hi:[0,1]\n"
      "v_pk_mul_f16 v171, v115, v119 op_sel_hi:[0,1] neg_hi:[0,1]\n"
      "v_pk_fma_f16 v250, v163, v161, v250 op_sel:[1,1,0] op_sel_hi:[1,0,1] neg_lo:[0,1,0]\n"
      "ds_write_addtid_b32 v249 offset:4624\n"
      "v_pk_fma_f16 v251, v163, v162, v251 op_sel:[1,1,0] op_sel_hi:[1,0,1] neg_lo:[0,1,0]\n"
      "v_pk_fma_f16 v168, v112, v116, v168 op_sel:[1,1,0] op_sel_hi:[1,0,1]\n"
      "v_pk_fma_f16 v169, v113, v117, v169 op_sel:[1,1,0] op_sel_hi:[1,0,1]\n"
      "v_pk_fma_f16 v170, v114, v118, v170 op_sel:[1,1,0] op_sel_hi:[1,0,1]\n"
      "v_pk_fma_f16 v171, v115, v119, v171 op_sel:[1,1,0] op_sel_hi:[1,0,1]\n"
      "ds_read_b128 v[104:107], v20 offset:3296\n"
      "ds_read_b128 v[108:111], v178 offset:3296\n"
      "v_mfma_f32_32x32x16_f16 v[180:195], v[100:103], v[168:171], v[180:195]\n"
      "ds_write_addtid_b32 v250 offset:4896\n"
      "ds_write_addtid_b32 v251 offset:5168\n"
      "s_waitcnt lgkmcnt(7)\n"
      "v_pk_mul_f16 v164, v120, v124 op_sel_hi:[0,1] neg_hi:[0,1]\n"
      "v_pk_mul_f16 v165, v121, v125 op_sel_hi:[0,1] neg_hi:[0,1]\n"
      "v_pk_mul_f16 v166, v122, v126 op_sel_hi:[0,1] neg_hi:[0,1]\n"
      "v_pk_mul_f16 v167, v123, v127 op_sel_hi:[0,1] neg_hi:[0,1]\n"
      "ds_read_b128 v[72:75], v21 offset:0\n"
      "ds_read_b128 v[76:79], v21 offset:32\n"
      "ds_read_b128 v[80:83], v21 offset:64\n"
      "ds_read_b128 v[84:87], v21 offset:96\n"
      "ds_read_b128 v[88:91], v21 offset:128\n"
      "ds_read_b128 v[92:95], v21 offset:160\n"
      "ds_read_b128 v[96:99], v21 offset:192\n"
      "ds_read_b128 v[100:103], v21 offset:224\n"
      "v_pk_fma_f16 v164, v120, v124, v164 op_sel:[1,1,0] op_sel_hi:[1,0,1]\n"
      "v_pk_fma_f16 v165, v121, v125, v165 op_sel:[1,1,0] op_sel_hi:[1,0,1]\n"
      "v_pk_fma_f16 v166, v122, v126, v166 op_sel:[1,1,0] op_sel_hi:[1,0,1]\n"
      "v_pk_fma_f16 v167, v123, v127, v167 op_sel:[1,1,0] op_sel_hi:[1,0,1]\n"
      "ds_read_b128 v[112:115], v20 offset:3328\n"
      "ds_read_b128 v[116:119], v178 offset:3328\n"
      "s_setprio 1\n"
      "s_waitcnt lgkmcnt(9)\n"
      "v_mfma_f32_32x32x16_f16 v[180:195], v[72:75], v[164:167], v[180:195]\n"
      "v_pk_mul_f16 v168, v104, v108 op_sel_hi:[0,1] neg_hi:[0,1]\n"
      "v_pk_mul_f16 v169, v105, v109 op_sel_hi:[0,1] neg_hi:[0,1]\n"
      "v_pk_mul_f16 v170, v106, v110 op_sel_hi:[0,1] neg_hi:[0,1]\n"
      "v_pk_mul_f16 v171, v107, v111 op_sel_hi:[0,1] neg_hi:[0,1]\n"
      "v_pk_fma_f16 v168, v104, v108, v168 op_sel:[1,1,0] op_sel_hi:[1,0,1]\n"
      "v_pk_fma_f16 v169, v105, v109, v169 op_sel:[1,1,0] op_sel_hi:[1,0,1]\n"
      "v_pk_fma_f16 v170, v106, v110, v170 op_sel:[1,1,0] op_sel_hi:[1,0,1]\n"
      "v_pk_fma_f16 v171, v107, v111, v171 op_sel:[1,1,0] op_sel_hi:[1,0,1]\n"
      "ds_read_b128 v[120:123], v20 offset:3360\n"
      "ds_read_b128 v[124:127], v178 offset:3360\n"
      "buffer_load_dwordx4 v[52:55], v41, s[20:23], s60 offen offset:2048 nt\n"
      "s_waitcnt lgkmcnt(10)\n"
      "v_mfma_f32_32x32x16_f16 v[180:195], v[76:79], v[168:171], v[180:195]\n"
      "s_waitcnt lgkmcnt(2)\n"
      "v_pk_mul_f16 v164, v112, v116 op_sel_hi:[0,1] neg_hi:[0,1]\n"
      "v_pk_mul_f16 v165, v113, v117 op_sel_hi:[0,1] neg_hi:[0,1]\n"
      "v_pk_mul_f16 v166, v114, v118 op_sel_hi:[0,1] neg_hi:[0,1]\n"
      "v_pk_mul_f16 v167, v115, v119 op_sel_hi:[0,1] neg_hi:[0,1]\n"
      "v_pk_fma_f16 v164, v112, v116, v164 op_sel:[1,1,0] op_sel_hi:[1,0,1]\n"
      "v_pk_fma_f16 v165, v113, v117, v165 op_sel:[1,1,0] op_sel_hi:[1,0,1]\n"
      "v_pk_fma_f16 v166, v114, v118, v166 op_sel:[1,1,0] op_sel_hi:[1,0,1]\n"
      "v_pk_fma_f16 v167, v115, v119, v167 op_sel:[1,1,0] op_sel_hi:[1,0,1]\n"
      "ds_read_b128 v[104:107], v20 offset:3392\n"
      "ds_read_b128 v[108:111], v178 offset:3392\n"
      "v_mfma_f32_32x32x16_f16 v[180:195], v[80:83], v[164:167], v[180:195]\n"
      "s_waitcnt lgkmcnt(2)\n"
      "v_pk_mul_f16 v168, v120, v124 op_sel_hi:[0,1] neg_hi:[0,1]\n"
      "v_pk_mul_f16 v169, v121, v125 op_sel_hi:[0,1] neg_hi:[0,1]\n"
      "v_pk_mul_f16 v170, v122, v126 op_sel_hi:[0,1] neg_hi:[0,1]\n"
      "v_pk_mul_f16 v171, v123, v127 op_sel_hi:[0,1] neg_hi:[0,1]\n"
      "v_pk_fma_f16 v168, v120, v124, v168 op_sel:[1,1,0] op_sel_hi:[1,0,1]\n"
      "v_pk_fma_f16 v169, v121, v125, v169 op_sel:[1,1,0] op_sel_hi:[1,0,1]\n"
      "v_pk_fma_f16 v170, v122, v126, v170 op_sel:[1,1,0] op_sel_hi:[1,0,1]\n"
      "v_pk_fma_f16 v171, v123, v127, v171 op_sel:[1,1,0] op_sel_hi:[1,0,1]\n"
      "ds_read_b128 v[112:115], v20 offset:3424\n"
      "ds_read_b128 v[116:119], v178 offset:3424\n"
      "v_mfma_f32_32x32x16_f16 v[180:195], v[84:87], v[168:171], v[180:195]\n"
      "s_waitcnt lgkmcnt(2)\n"
      "v_pk_mul_f16 v164, v104, v108 op_sel_hi:[0,1] neg_hi:[0,1]\n"
      "v_pk_mul_f16 v165, v105, v109 op_sel_hi:[0,1] neg_hi:[0,1]\n"
      "v_pk_mul_f16 v166, v106, v110 op_sel_hi:[0,1] neg_hi:[0,1]\n"
      "v_pk_mul_f16 v167, v107, v111 op_sel_hi:[0,1] neg_hi:[0,1]\n"
      "v_pk_fma_f16 v164, v104, v108, v164 op_sel:[1,1,0] op_sel_hi:[1,0,1]\n"
      "v_pk_fma_f16 v165, v105, v109, v165 op_sel:[1,1,0] op_sel_hi:[1,0,1]\n"
      "v_pk_fma_f16 v166, v106, v110, v166 op_sel:[1,1,0] op_sel_hi:[1,0,1]\n"
      "v_pk_fma_f16 v167, v107, v111, v167 op_sel:[1,1,0] op_sel_hi:[1,0,1]\n"
      "ds_read_b128 v[120:123], v20 offset:3456\n"
      "ds_read_b128 v[124:127], v178 offset:3456\n"
      "v_mfma_f32_32x32x16_f16 v[180:195], v[88:91], v[164:167], v[180:195]\n"
      "s_waitcnt lgkmcnt(2)\n"
      "v_pk_mul_f16 v168, v112, v116 op_sel_hi:[0,1] neg_hi:[0,1]\n"
      "v_pk_mul_f16 v169, v113, v117 op_sel_hi:[0,1] neg_hi:[0,1]\n"
      "v_pk_mul_f16 v170, v114, v118 op_sel_hi:[0,1] neg_hi:[0,1]\n"
      "v_pk_mul_f16 v171, v115, v119 op_sel_hi:[0,1] neg_hi:[0,1]\n"
      "v_pk_fma_f16 v168, v112, v116, v168 op_sel:[1,1,0] op_sel_hi:[1,0,1]\n"
      "v_pk_fma_f16 v169, v113, v117, v169 op_sel:[1,1,0] op_sel_hi:[1,0,1]\n"
      "v_pk_fma_f16 v170, v114, v118, v170 op_sel:[1,1,0] op_sel_hi:[1,0,1]\n"
      "v_pk_fma_f16 v171, v115, v119, v171 op_sel:[1,1,0] op_sel_hi:[1,0,1]\n"
      "ds_read_b128 v[104:107], v20 offset:3488\n"
      "ds_read_b128 v[108:111], v178 offset:3488\n"
      "buffer_load_dwordx4 v[64:67], v42, s[16:19], s62 offen sc1\n"
      "buffer_load_dwordx4 v[68:71], v42, s[16:19], s63 offen sc1\n"
      "v_mov_b32 v140, 0\n"
      "v_mov_b32 v141, 0\n"
      "v_mov_b32 v142, 0\n"
      "v_mov_b32 v143, 0\n"
      "ds_write_b128 v27, v[140:143] \n"
      "ds_write_b32 v28, v140 \n"
      "v_mfma_f32_32x32x16_f16 v[180:195], v[92:95], v[168:171], v[180:195]\n"
      "s_waitcnt lgkmcnt(2)\n"
      "v_pk_mul_f16 v164, v120, v124 op_sel_hi:[0,1] neg_hi:[0,1]\n"
      "v_pk_mul_f16 v165, v121, v125 op_sel_hi:[0,1] neg_hi:[0,1]\n"
      "v_pk_mul_f16 v166, v122, v126 op_sel_hi:[0,1] neg_hi:[0,1]\n"
      "v_pk_mul_f16 v167, v123, v127 op_sel_hi:[0,1] neg_hi:[0,1]\n"
      "v_pk_fma_f16 v164, v120, v124, v164 op_sel:[1,1,0] op_sel_hi:[1,0,1]\n"
      "v_pk_fma_f16 v165, v121, v125, v165 op_sel:[1,1,0] op_sel_hi:[1,0,1]\n"
      "v_pk_fma_f16 v166, v122, v126, v166 op_sel:[1,1,0] op_sel_hi:[1,0,1]\n"
      "v_pk_fma_f16 v167, v123, v127, v167 op_sel:[1,1,0] op_sel_hi:[1,0,1]\n"
      "s_nop 1\n"
      "v_mfma_f32_32x32x16_f16 v[180:195], v[96:99], v[164:167], v[180:195]\n"
      "s_waitcnt lgkmcnt(0)\n"
      "v_pk_mul_f16 v168, v104, v108 op_sel_hi:[0,1] neg_hi:[0,1]\n"
      "v_pk_mul_f16 v169, v105, v109 op_sel_hi:[0,1] neg_hi:[0,1]\n"
      "v_pk_mul_f16 v170, v106, v110 op_sel_hi:[0,1] neg_hi:[0,1]\n"
      "v_pk_mul_f16 v171, v107, v111 op_sel_hi:[0,1] neg_hi:[0,1]\n"
      "v_pk_fma_f16 v168, v104, v108, v168 op_sel:[1,1,0] op_sel_hi:[1,0,1]\n"
      "v_pk_fma_f16 v169, v105, v109, v169 op_sel:[1,1,0] op_sel_hi:[1,0,1]\n"
      "v_pk_fma_f16 v170, v106, v110, v170 op_sel:[1,1,0] op_sel_hi:[1,0,1]\n"
      "v_pk_fma_f16 v171, v107, v111, v171 op_sel:[1,1,0] op_sel_hi:[1,0,1]\n"
      "s_nop 1\n"
      "v_mfma_f32_32x32x16_f16 v[180:195], v[100:103], v[168:171], v[180:195]\n"
      "buffer_load_dword v11, v15, s[20:23], s61 offen\n"
      "s_nop 11\n"
      "v_add_f32 v180, v180, v31\n"
      "v_cvt_pk_f16_f32 v222, v187, v186\n"
      "v_cvt_pk_f16_f32 v223, v185, v184\n"
      "v_cvt_pk_f16_f32 v225, v186, v185\n"
      "v_cvt_pk_f16_f32 v226, v188, v189\n"
      "ds_write_b64 v22, v[222:223] offset:2110\n"
      "v_cvt_pk_f16_f32 v228, v192, v192\n"
      "ds_write_b16 v22, v222 \n"
      "v_cvt_pk_f16_f32 v220, v183, v182\n"
      "ds_write_b32 v22, v225 offset:2\n"
      "v_cvt_pk_f16_f32 v221, v181, v180\n"
      "ds_write_b16_d16_hi v22, v223 offset:6\n"
      "v_cvt_pk_f16_f32 v224, v182, v181\n"
      "v_readlane_b32 s64, v226, 0\n"
      "v_readlane_b32 s65, v226, 32\n"
      "v_readlane_b32 s66, v228, 0\n"
      "ds_write_b64 v22, v[220:221] offset:510\n"
      "v_cvt_pk_f16_f32 v227, v190, v191\n"
      "ds_write_b16 v22, v220 offset:2620\n"
      "v_mov_b32 v229, s65\n"
      "ds_write_b32 v22, v224 offset:2622\n"
      "v_writelane_b32 v229, s64, 8\n"
      "v_writelane_b32 v229, s64, 9\n"
      "ds_write_b16_d16_hi v22, v221 offset:2626\n"
      "v_writelane_b32 v229, s66, 10\n"
      "v_writelane_b32 v229, s66, 11\n"
      "s_pack_ll_b32_b16 s64, s64, s64\n"
      "ds_write_b16 v26, v229 \n"
      "v_add_u32 v230, 0x240, v29\n"
      "s_setprio 0\n"
      "v_and_b32 v44, s64, v44\n"
      "v_and_b32 v45, s64, v45\n"
      "v_and_b32 v46, s64, v46\n"
      "v_and_b32 v47, s64, v47\n"
      "ds_write_b16 v23, v226 \n"
      "ds_write_b16 v23, v226 offset:2110\n"
      "ds_write_b16_d16_hi v24, v226 \n"
      "ds_write_b16_d16_hi v24, v226 offset:2110\n"
      "ds_write_b16 v25, v227 offset:32\n"
      "ds_write_b16 v25, v227 offset:2142\n"
      "ds_write_b16_d16_hi v25, v227 \n"
      "ds_write_b16_d16_hi v25, v227 offset:2110\n"
      "ds_read2_b32 v[108:109], v230 offset0:0 offset1:1\n"
      "ds_read2_b32 v[110:111], v230 offset0:2 offset1:3\n"
      "ds_read2_b32 v[76:77], v29 offset0:16 offset1:17\n"
      "ds_read2_b32 v[78:79], v29 offset0:18 offset1:19\n"
      "ds_read2_b32 v[112:113], v230 offset0:16 offset1:17\n"
      "ds_read2_b32 v[114:115], v230 offset0:18 offset1:19\n"
      "ds_read2_b32 v[80:81], v29 offset0:32 offset1:33\n"
      "ds_read2_b32 v[82:83], v29 offset0:34 offset1:35\n"
      "ds_read2_b32 v[116:117], v230 offset0:32 offset1:33\n"
      "ds_read2_b32 v[118:119], v230 offset0:34 offset1:35\n"
      "ds_read2_b32 v[84:85], v29 offset0:48 offset1:49\n"
      "ds_read2_b32 v[86:87], v29 offset0:50 offset1:51\n"
      "s_movk_i32 s59, 0x7fff\n"
      "s_waitcnt vmcnt(1)\n"
      "s_nop 0\n"
      ".Lcsgk%=:\n"
      "v_cmp_ne_u32 s[64:65], s15, v65\n"
      "v_cmp_ne_u32 vcc, s15, v67\n"
      "s_nop 0\n"
      "s_or_b64 vcc, vcc, s[64:65]\n"
      "s_cbranch_scc1 .Lcsgsk%=\n"
      ".Lcsgok%=:\n"
      "v_and_b32 v196, 0xffff0000, v64\n"
      "v_lshlrev_b32 v198, 16, v64\n"
      "v_rcp_f32 v196, v196\n"
      "v_and_b32 v197, 0xffff0000, v66\n"
      "v_lshlrev_b32 v199, 16, v66\n"
      "v_rcp_f32 v197, v197\n"
      "v_mul_f32 v198, -v198, v196\n"
      "v_mul_f32 v199, -v199, v197\n"
      "ds_read2_b32 v[120:121], v230 offset0:48 offset1:49\n"
      "ds_read2_b32 v[122:123], v230 offset0:50 offset1:51\n"
      "v_lshlrev_b32 v204, 16, v48\n"
      "v_lshlrev_b32 v205, 16, v52\n"
      "v_and_b32 v206, 0xffff0000, v48\n"
      "v_and_b32 v207, 0xffff0000, v52\n"
      "v_pk_fma_f32 v[204:205], v[204:205], v[196:197], v[198:199]\n"
      "v_pk_fma_f32 v[206:207], v[206:207], v[196:197], v[198:199]\n"
      "v_cvt_pk_f16_f32 v204, v204, v205\n"
      "v_cvt_pk_f16_f32 v206, v206, v207\n"
      "ds_read2_b32 v[88:89], v29 offset0:64 offset1:65\n"
      "ds_read2_b32 v[90:91], v29 offset0:66 offset1:67\n"
      "ds_write2st64_b32 v32, v204, v206 offset0:0 offset1:19\n"
      "v_lshlrev_b32 v208, 16, v49\n"
      "v_lshlrev_b32 v209, 16, v53\n"
      "v_and_b32 v210, 0xffff0000, v49\n"
      "v_and_b32 v211, 0xffff0000, v53\n"
      "v_pk_fma_f32 v[208:209], v[208:209], v[196:197], v[198:199]\n"
      "v_pk_fma_f32 v[210:211], v[210:211], v[196:197], v[198:199]\n"
      "v_cvt_pk_f16_f32 v208, v208, v209\n"
      "ds_read2_b32 v[124:125], v230 offset0:64 offset1:65\n"
      "ds_read2_b32 v[126:127], v230 offset0:66 offset1:67\n"
      "v_cvt_pk_f16_f32 v210, v210, v211\n"
      "ds_write2st64_b32 v32, v208, v210 offset0:38 offset1:57\n"
      "v_lshlrev_b32 v204, 16, v50\n"
      "v_lshlrev_b32 v205, 16, v54\n"
      "v_and_b32 v206, 0xffff0000, v50\n"
      "v_and_b32 v207, 0xffff0000, v54\n"
      "v_pk_fma_f32 v[204:205], v[204:205], v[196:197], v[198:199]\n"
      "v_pk_fma_f32 v[206:207], v[206:207], v[196:197], v[198:199]\n"
      "ds_read2_b32 v[92:93], v29 offset0:80 offset1:81\n"
      "ds_read2_b32 v[94:95], v29 offset0:82 offset1:83\n"
      "v_cvt_pk_f16_f32 v204, v204, v205\n"
      "v_cvt_pk_f16_f32 v206, v206, v207\n"
      "ds_write2st64_b32 v32, v204, v206 offset0:76 offset1:95\n"
      "v_lshlrev_b32 v208, 16, v51\n"
      "v_lshlrev_b32 v209, 16, v55\n"
      "v_and_b32 v210, 0xffff0000, v51\n"
      "v_and_b32 v211, 0xffff0000, v55\n"
      "v_pk_fma_f32 v[208:209], v[208:209], v[196:197], v[198:199]\n"
      "ds_read2_b32 v[128:129], v230 offset0:80 offset1:81\n"
      "ds_read2_b32 v[130:131], v230 offset0:82 offset1:83\n"
      "v_pk_fma_f32 v[210:211], v[210:211], v[196:197], v[198:199]\n"
      "v_cvt_pk_f16_f32 v208, v208, v209\n"
      "v_cvt_pk_f16_f32 v210, v210, v211\n"
      "ds_write2st64_b32 v32, v208, v210 offset0:114 offset1:133\n"
      "s_mov_b32 s54, s41\n"
      "s_add_u32 s55, s41, 0x100000\n"
      "s_add_u32 s56, s41, 0x200000\n"
      "s_add_u32 s57, s41, 0x300000\n"
      "ds_read2_b32 v[96:97], v29 offset0:96 offset1:97\n"
      "ds_read2_b32 v[98:99], v29 offset0:98 offset1:99\n"
      "s_mov_b32 s58, 0x40000000\n"
      "v_lshrrev_b32 v14, 9, v8\n"
      "v_add_u32 v14, 0x27600, v14\n"
      "s_lshr_b32 s13, s33, 0\n"
      "ds_read2_b32 v[132:133], v230 offset0:96 offset1:97\n"
      "ds_read2_b32 v[134:135], v230 offset0:98 offset1:99\n"
      "ds_read2_b32 v[100:101], v29 offset0:112 offset1:113\n"
      "ds_read2_b32 v[102:103], v29 offset0:114 offset1:115\n"
      "ds_read2_b32 v[136:137], v230 offset0:112 offset1:113\n"
      "ds_read2_b32 v[138:139], v230 offset0:114 offset1:115\n"
      "ds_read2_b32 v[104:105], v29 offset0:128 offset1:129\n"
      "ds_read2_b32 v[106:107], v29 offset0:130 offset1:131\n"
      "ds_read2_b32 v[72:73], v29 offset0:0 offset1:1\n"
      "ds_read2_b32 v[74:75], v29 offset0:2 offset1:3\n"
      "s_waitcnt lgkmcnt(12)\n"
      ".Lccv%=:\n"
      "s_barrier\n"
      "s_setprio 3\n"
      "ds_read_b128 v[216:219], v33 offset:64\n"
      "ds_read_b128 v[220:223], v33 offset:128\n"
      "ds_read_b128 v[224:227], v33 offset:192\n"
      "ds_read_b128 v[212:215], v33 offset:256\n"
      "v_permlane32_swap_b32 v48, v52\n"
      "v_permlane32_swap_b32 v49, v53\n"
      "v_permlane32_swap_b32 v50, v54\n"
      "v_permlane32_swap_b32 v51, v55\n"
      "s_cmp_lg_u32 s33, 0\n"
      "s_cbranch_scc1 .Lca%=\n"
      "v_mov_b32 v16, s15\n"
      "v_mov_b32 v17, s15\n"
      "buffer_store_dwordx2 v[16:17], off, s[16:19], s42 sc1\n"
      ".Lca%=:\n"
      "s_waitcnt lgkmcnt(3)\n"
      "v_mfma_f32_16x16x32_f16 v[180:183], v[108:111], v[216:219], 0\n"
      "ds_read_b128 v[236:239], v34 offset:0\n"
      "ds_read_b128 v[240:243], v34 offset:16\n"
      "v_lshlrev_b32 v29, 2, v43\n"
      "v_add_u32 v29, 0x27600, v29\n"
      "ds_read_b32 v255, v29 \n"
      "v_mfma_f32_16x16x32_f16 v[184:187], v[76:79], v[216:219], 0\n"
      "ds_read2_b32 v[140:141], v30 offset0:16 offset1:17\n"
      "ds_read_b128 v[216:219], v33 offset:320\n"
      "s_waitcnt lgkmcnt(7)\n"
      "v_mfma_f32_16x16x32_f16 v[180:183], v[112:115], v[220:223], v[180:183]\n"
      "v_mfma_f32_16x16x32_f16 v[184:187], v[80:83], v[220:223], v[184:187]\n"
      "ds_read2_b32 v[142:143], v30 offset0:64 offset1:65\n"
      "ds_read_b128 v[220:223], v33 offset:384\n"
      "s_waitcnt lgkmcnt(8)\n"
      "v_mfma_f32_16x16x32_f16 v[180:183], v[116:119], v[224:227], v[180:183]\n"
      "s_waitcnt lgkmcnt(6)\n"
      "v_alignbit_b32 v245, v238, v237, 16\n"
      "v_mfma_f32_16x16x32_f16 v[184:187], v[84:87], v[224:227], v[184:187]\n"
      "ds_read2_b32 v[144:145], v30 offset0:8 offset1:9\n"
      "ds_read_b128 v[224:227], v33 offset:448\n"
      "v_alignbit_b32 v246, v239, v238, 16\n"
      "v_mfma_f32_16x16x32_f16 v[180:183], v[120:123], v[212:215], v[180:183]\n"
      "s_waitcnt lgkmcnt(7)\n"
      "v_alignbit_b32 v247, v240, v239, 16\n"
      "s_waitcnt lgkmcnt(6)\n"
      "v_readlane_b32 s72, v255, 0\n"
      "v_mfma_f32_16x16x32_f16 v[184:187], v[88:91], v[212:215], v[184:187]\n"
      "ds_read2_b32 v[146:147], v30 offset0:56 offset1:57\n"
      "ds_read_b128 v[212:215], v33 offset:512\n"
      "v_alignbit_b32 v248, v241, v240, 16\n"
      "s_waitcnt lgkmcnt(6)\n"
      "v_mfma_f32_16x16x32_f16 v[180:183], v[124:127], v[216:219], v[180:183]\n"
      "v_alignbit_b32 v249, v242, v241, 16\n"
      "v_readlane_b32 s73, v255, 1\n"
      "v_mfma_f32_16x16x32_f16 v[184:187], v[92:95], v[216:219], v[184:187]\n"
      "ds_read2_b32 v[148:149], v30 offset0:0 offset1:1\n"
      "v_pk_add_f16 v16, v245, v246\n"
      "s_waitcnt lgkmcnt(5)\n"
      "v_mfma_f32_16x16x32_f16 v[180:183], v[128:131], v[220:223], v[180:183]\n"
      "v_pk_add_f16 v17, v246, v247\n"
      "v_readlane_b32 s74, v255, 2\n"
      "v_mfma_f32_16x16x32_f16 v[184:187], v[96:99], v[220:223], v[184:187]\n"
      "ds_read2_b32 v[150:151], v30 offset0:48 offset1:49\n"
      "ds_read_b128 v[216:219], v33 offset:576\n"
      "v_pk_add_f16 v18, v247, v248\n"
      "s_waitcnt lgkmcnt(5)\n"
      "v_mfma_f32_16x16x32_f16 v[180:183], v[132:135], v[224:227], v[180:183]\n"
      "v_pk_add_f16 v19, v248, v249\n"
      "v_readlane_b32 s75, v255, 3\n"
      "v_mfma_f32_16x16x32_f16 v[184:187], v[100:103], v[224:227], v[184:187]\n"
      "ds_read2_b32 v[152:153], v30 offset0:40 offset1:41\n"
      "ds_read_b128 v[220:223], v33 offset:640\n"
      "v_pk_add_f16 v20, v16, v238\n"
      "s_waitcnt lgkmcnt(5)\n"
      "v_mfma_f32_16x16x32_f16 v[180:183], v[136:139], v[212:215], v[180:183]\n"
      "v_pk_add_f16 v21, v17, v239\n"
      "v_readlane_b32 s76, v255, 4\n"
      "v_mfma_f32_16x16x32_f16 v[184:187], v[104:107], v[212:215], v[184:187]\n"
      "ds_read2_b32 v[154:155], v30 offset0:88 offset1:89\n"
      "ds_read_b128 v[224:227], v33 offset:704\n"
      "v_pk_add_f16 v22, v18, v240\n"
      "v_mfma_f32_16x16x32_f16 v[188:191], v[72:75], v[212:215], 0\n"
      "v_pk_add_f16 v23, v19, v241\n"
      "v_readlane_b32 s77, v255, 5\n"
      "v_mfma_f32_16x16x32_f16 v[196:199], v[44:47], v[212:215], 0\n"
      "ds_read2_b32 v[156:157], v30 offset0:32 offset1:33\n"
      "ds_read_b128 v[212:215], v33 offset:768\n"
      "ds_write_b128 v37, v[20:23] offset:0\n"
      "v_pk_add_f16 v16, v237, v239\n"
      "s_waitcnt lgkmcnt(7)\n"
      "v_mfma_f32_16x16x32_f16 v[184:187], v[108:111], v[216:219], v[184:187]\n"
      "v_pk_add_f16 v17, v238, v240\n"
      "v_readlane_b32 s78, v255, 6\n"
      "v_cvt_pk_bf16_f32 v24, v180, v182\n"
      "v_cvt_pk_bf16_f32 v25, v181, v183\n"
      "ds_write2st64_b32 v39, v24, v25 offset0:0 offset1:8\n"
      "v_mfma_f32_16x16x32_f16 v[188:191], v[76:79], v[216:219], v[188:191]\n"
      "ds_read2_b32 v[158:159], v30 offset0:80 offset1:81\n"
      "ds_read_b128 v[216:219], v33 offset:832\n"
      "v_pk_add_f16 v18, v239, v241\n"
      "s_waitcnt lgkmcnt(8)\n"
      "v_mfma_f32_16x16x32_f16 v[184:187], v[112:115], v[220:223], v[184:187]\n"
      "buffer_load_dwordx4 v[56:59], v41, s[20:23], s61 offen nt\n"
      "v_pk_add_f16 v19, v240, v242\n"
      "v_readlane_b32 s79, v255, 7\n"
      "v_mfma_f32_16x16x32_f16 v[188:191], v[80:83], v[220:223], v[188:191]\n"
      "ds_read2_b32 v[160:161], v30 offset0:24 offset1:25\n"
      "ds_read_b128 v[220:223], v33 offset:896\n"
      "v_pk_add_f16 v20, v20, v16\n"
      "s_waitcnt lgkmcnt(8)\n"
      "v_mfma_f32_16x16x32_f16 v[184:187], v[116:119], v[224:227], v[184:187]\n"
      "v_pk_add_f16 v21, v21, v17\n"
      "s_setprio 2\n"
      "v_mfma_f32_16x16x32_f16 v[188:191], v[84:87], v[224:227], v[188:191]\n"
      "ds_read2_b32 v[162:163], v30 offset0:72 offset1:73\n"
      "ds_read_b128 v[224:227], v33 offset:960\n"
      "v_pk_add_f16 v22, v22, v18\n"
      "s_waitcnt lgkmcnt(8)\n"
      "v_mfma_f32_16x16x32_f16 v[184:187], v[120:123], v[212:215], v[184:187]\n"
      "v_pk_add_f16 v23, v23, v19\n"
      "v_mfma_f32_16x16x32_f16 v[188:191], v[88:91], v[212:215], v[188:191]\n"
      "ds_read2_b32 v[164:165], v30 offset0:128 offset1:129\n"
      "ds_read_b128 v[212:215], v33 offset:1024\n"
      "ds_write_b128 v37, v[20:23] offset:3104\n"
      "v_alignbit_b32 v244, v237, v236, 16\n"
      "s_waitcnt lgkmcnt(7)\n"
      "v_mfma_f32_16x16x32_f16 v[184:187], v[124:127], v[216:219], v[184:187]\n"
      "v_alignbit_b32 v250, v243, v242, 16\n"
      "v_mfma_f32_16x16x32_f16 v[188:191], v[92:95], v[216:219], v[188:191]\n"
      "v_pk_add_f16 v16, v244, v247\n"
      "s_waitcnt lgkmcnt(5)\n"
      "v_mfma_f32_16x16x32_f16 v[184:187], v[128:131], v[220:223], v[184:187]\n"
      "v_pk_add_f16 v17, v245, v248\n"
      "v_mfma_f32_16x16x32_f16 v[188:191], v[96:99], v[220:223], v[188:191]\n"
      "ds_read2_b32 v[166:167], v30 offset0:168 offset1:169\n"
      "ds_read_b64_tr_b16 v[228:229], v35 offset:32\n"
      "ds_read_b64_tr_b16 v[230:231], v35 offset:16\n"
      "v_pk_add_f16 v18, v246, v249\n"
      "s_waitcnt lgkmcnt(6)\n"
      "v_mfma_f32_16x16x32_f16 v[184:187], v[132:135], v[224:227], v[184:187]\n"
      "v_pk_add_f16 v19, v247, v250\n"
      "v_mfma_f32_16x16x32_f16 v[188:191], v[100:103], v[224:227], v[188:191]\n"
      "v_pk_add_f16 v20, v20, v16\n"
      "s_waitcnt lgkmcnt(4)\n"
      "v_mfma_f32_16x16x32_f16 v[184:187], v[136:139], v[212:215], v[184:187]\n"
      "ds_read_b128 v[216:219], v33 offset:1088\n"
      "buffer_load_dwordx4 v[60:63], v41, s[20:23], s61 offen offset:2048 nt\n"
      "v_pk_add_f16 v21, v21, v17\n"
      "v_mfma_f32_16x16x32_f16 v[188:191], v[104:107], v[212:215], v[188:191]\n"
      "ds_read2_b32 v[168:169], v30 offset0:120 offset1:121\n"
      "v_pk_add_f16 v22, v22, v18\n"
      "v_mfma_f32_16x16x32_f16 v[192:195], v[72:75], v[212:215], 0\n"
      "ds_read_b128 v[220:223], v33 offset:1152\n"
      "v_pk_add_f16 v23, v23, v19\n"
      "v_mfma_f32_16x16x32_f16 v[200:203], v[44:47], v[212:215], 0\n"
      "ds_write_b128 v38, v[20:23] offset:0\n"
      "v_pk_add_f16 v16, v236, v240\n"
      "s_waitcnt lgkmcnt(4)\n"
      "v_mfma_f32_16x16x32_f16 v[188:191], v[228:231], v[140:143], v[188:191]\n"
      "ds_read_b128 v[224:227], v33 offset:1216\n"
      "v_pk_add_f16 v17, v237, v241\n"
      "v_cvt_pk_bf16_f32 v24, v184, v186\n"
      "v_cvt_pk_bf16_f32 v25, v185, v187\n"
      "ds_write2st64_b32 v39, v24, v25 offset0:1 offset1:9\n"
      "v_mfma_f32_16x16x32_f16 v[192:195], v[228:231], v[144:147], v[192:195]\n"
      "ds_read2_b32 v[170:171], v30 offset0:160 offset1:161\n"
      "v_pk_add_f16 v18, v238, v242\n"
      "v_mfma_f32_16x16x32_f16 v[196:199], v[228:231], v[148:151], v[196:199]\n"
      "ds_read_b64_tr_b16 v[232:233], v35 offset:3128\n"
      "ds_read_b64_tr_b16 v[234:235], v35 offset:3112\n"
      "v_pk_add_f16 v19, v239, v243\n"
      "s_waitcnt lgkmcnt(8)\n"
      "v_mfma_f32_16x16x32_f16 v[188:191], v[108:111], v[216:219], v[188:191]\n"
      "v_pk_add_f16 v20, v20, v16\n"
      "ds_read_b128 v[236:239], v34 offset:1024\n"
      "ds_read_b128 v[240:243], v34 offset:1040\n"
      "v_mfma_f32_16x16x32_f16 v[192:195], v[76:79], v[216:219], v[192:195]\n"
      "v_pk_add_f16 v21, v21, v17\n"
      "s_waitcnt lgkmcnt(8)\n"
      "v_mfma_f32_16x16x32_f16 v[188:191], v[112:115], v[220:223], v[188:191]\n"
      "ds_read2_b32 v[172:173], v30 offset0:112 offset1:113\n"
      "ds_read_b128 v[212:215], v33 offset:1280\n"
      "v_pk_add_f16 v22, v22, v18\n"
      "v_mfma_f32_16x16x32_f16 v[192:195], v[80:83], v[220:223], v[192:195]\n"
      "v_pk_add_f16 v23, v23, v19\n"
      "s_waitcnt lgkmcnt(8)\n"
      "v_mfma_f32_16x16x32_f16 v[188:191], v[116:119], v[224:227], v[188:191]\n"
      "ds_read_b128 v[216:219], v33 offset:1344\n"
      "ds_write_b128 v38, v[20:23] offset:2080\n"
      "v_mfma_f32_16x16x32_f16 v[192:195], v[84:87], v[224:227], v[192:195]\n"
      "s_waitcnt lgkmcnt(5)\n"
      "v_alignbit_b32 v245, v238, v237, 16\n"
      "s_setprio 1\n"
      "v_mfma_f32_16x16x32_f16 v[188:191], v[232:235], v[152:155], v[188:191]\n"
      "ds_read2_b32 v[174:175], v30 offset0:152 offset1:153\n"
      "ds_read_b128 v[220:223], v33 offset:1408\n"
      "v_alignbit_b32 v246, v239, v238, 16\n"
      "v_mfma_f32_16x16x32_f16 v[192:195], v[232:235], v[156:159], v[192:195]\n"
      "s_waitcnt lgkmcnt(6)\n"
      "v_alignbit_b32 v247, v240, v239, 16\n"
      "v_mfma_f32_16x16x32_f16 v[196:199], v[232:235], v[160:163], v[196:199]\n"
      "ds_read_b128 v[224:227], v33 offset:1472\n"
      "v_alignbit_b32 v248, v241, v240, 16\n"
      "s_waitcnt lgkmcnt(5)\n"
      "v_mfma_f32_16x16x32_f16 v[188:191], v[120:123], v[212:215], v[188:191]\n"
      "v_alignbit_b32 v249, v242, v241, 16\n"
      "v_mfma_f32_16x16x32_f16 v[192:195], v[88:91], v[212:215], v[192:195]\n"
      "ds_read2_b32 v[176:177], v30 offset0:104 offset1:105\n"
      "ds_read_b128 v[212:215], v33 offset:1536\n"
      "v_pk_add_f16 v16, v245, v246\n"
      "s_waitcnt lgkmcnt(6)\n"
      "v_mfma_f32_16x16x32_f16 v[188:191], v[124:127], v[216:219], v[188:191]\n"
      "v_pk_add_f16 v17, v246, v247\n"
      "v_mfma_f32_16x16x32_f16 v[192:195], v[92:95], v[216:219], v[192:195]\n"
      "v_pk_add_f16 v18, v247, v248\n"
      "s_waitcnt lgkmcnt(3)\n"
      "v_mfma_f32_16x16x32_f16 v[188:191], v[128:131], v[220:223], v[188:191]\n"
      "v_pk_add_f16 v19, v248, v249\n"
      "v_mfma_f32_16x16x32_f16 v[192:195], v[96:99], v[220:223], v[192:195]\n"
      "ds_read2_b32 v[178:179], v30 offset0:144 offset1:145\n"
      "ds_read_b64_tr_b16 v[228:229], v35 offset:544\n"
      "ds_read_b64_tr_b16 v[230:231], v35 offset:528\n"
      "v_pk_add_f16 v20, v16, v238\n"
      "s_waitcnt lgkmcnt(5)\n"
      "v_mfma_f32_16x16x32_f16 v[188:191], v[132:135], v[224:227], v[188:191]\n"
      "v_pk_add_f16 v21, v17, v239\n"
      "v_mfma_f32_16x16x32_f16 v[192:195], v[100:103], v[224:227], v[192:195]\n"
      "v_pk_add_f16 v22, v18, v240\n"
      "s_waitcnt lgkmcnt(3)\n"
      "v_mfma_f32_16x16x32_f16 v[188:191], v[136:139], v[212:215], v[188:191]\n"
      "ds_read_b128 v[216:219], v33 offset:1600\n"
      "v_pk_add_f16 v23, v19, v241\n"
      "v_mfma_f32_16x16x32_f16 v[192:195], v[104:107], v[212:215], v[192:195]\n"
      "ds_write_b128 v37, v[20:23] offset:1024\n"
      "v_pk_add_f16 v16, v237, v239\n"
      "v_mfma_f32_16x16x32_f16 v[196:199], v[72:75], v[212:215], v[196:199]\n"
      "ds_read_b128 v[220:223], v33 offset:1664\n"
      "v_pk_add_f16 v17, v238, v240\n"
      "v_mfma_f32_16x16x32_f16 v[204:207], v[44:47], v[212:215], 0\n"
      "v_pk_add_f16 v18, v239, v241\n"
      "s_waitcnt lgkmcnt(3)\n"
      "v_mfma_f32_16x16x32_f16 v[192:195], v[228:231], v[140:143], v[192:195]\n"
      "ds_read_b128 v[224:227], v33 offset:1728\n"
      "s_movk_i32 s59, 0x7fff\n"
      "s_waitcnt vmcnt(2)\n"
      "s_nop 0\n"
      ".Lcsgh%=:\n"
      "v_cmp_ne_u32 s[64:65], s15, v69\n"
      "v_cmp_ne_u32 vcc, s15, v71\n"
      "s_nop 0\n"
      "s_or_b64 vcc, vcc, s[64:65]\n"
      "s_cbranch_scc1 .Lcsgsh%=\n"
      ".Lcsgoh%=:\n"
      "v_mov_b32 v42, v8\n"
      "v_and_b32 v26, 0xffff0000, v68\n"
      "v_lshlrev_b32 v28, 16, v68\n"
      "v_pk_add_f16 v19, v240, v242\n"
      "v_cvt_pk_bf16_f32 v24, v188, v190\n"
      "v_cvt_pk_bf16_f32 v25, v189, v191\n"
      "ds_write2st64_b32 v39, v24, v25 offset0:2 offset1:10\n"
      "v_mfma_f32_16x16x32_f16 v[196:199], v[228:231], v[144:147], v[196:199]\n"
      "v_rcp_f32 v26, v26\n"
      "v_and_b32 v27, 0xffff0000, v70\n"
      "v_pk_add_f16 v20, v20, v16\n"
      "v_mfma_f32_16x16x32_f16 v[200:203], v[228:231], v[148:151], v[200:203]\n"
      "ds_read_b64_tr_b16 v[232:233], v35 offset:3640\n"
      "ds_read_b64_tr_b16 v[234:235], v35 offset:3624\n"
      "v_lshlrev_b32 v29, 16, v70\n"
      "v_rcp_f32 v27, v27\n"
      "v_pk_add_f16 v21, v21, v17\n"
      "s_waitcnt lgkmcnt(6)\n"
      "v_mfma_f32_16x16x32_f16 v[192:195], v[108:111], v[216:219], v[192:195]\n"
      "v_mul_f32 v28, -v28, v26\n"
      "v_mul_f32 v29, -v29, v27\n"
      "v_pk_add_f16 v22, v22, v18\n"
      "v_mfma_f32_16x16x32_f16 v[196:199], v[76:79], v[216:219], v[196:199]\n"
      "s_waitcnt vmcnt(1)\n"
      "v_lshlrev_b32 v180, 16, v56\n"
      "s_waitcnt vmcnt(0)\n"
      "v_lshlrev_b32 v181, 16, v60\n"
      "v_pk_add_f16 v23, v23, v19\n"
      "s_setprio 0\n"
      "s_waitcnt lgkmcnt(4)\n"
      "v_mfma_f32_16x16x32_f16 v[192:195], v[112:115], v[220:223], v[192:195]\n"
      "ds_read_b128 v[212:215], v33 offset:1792\n"
      "v_and_b32 v182, 0xffff0000, v56\n"
      "v_and_b32 v183, 0xffff0000, v60\n"
      "ds_write_b128 v37, v[20:23] offset:4128\n"
      "v_alignbit_b32 v244, v237, v236, 16\n"
      "v_mfma_f32_16x16x32_f16 v[196:199], v[80:83], v[220:223], v[196:199]\n"
      "v_fma_f32 v180, v180, v26, v28\n"
      "v_fma_f32 v181, v181, v27, v29\n"
      "v_alignbit_b32 v250, v243, v242, 16\n"
      "s_waitcnt lgkmcnt(5)\n"
      "v_mfma_f32_16x16x32_f16 v[192:195], v[116:119], v[224:227], v[192:195]\n"
      "ds_read_b128 v[216:219], v33 offset:1856\n"
      "v_fma_f32 v182, v182, v26, v28\n"
      "v_fma_f32 v183, v183, v27, v29\n"
      "v_pk_add_f16 v16, v244, v247\n"
      "v_mfma_f32_16x16x32_f16 v[196:199], v[84:87], v[224:227], v[196:199]\n"
      "v_cvt_pk_f16_f32 v180, v180, v181\n"
      "v_cvt_pk_f16_f32 v182, v182, v183\n"
      "v_pk_add_f16 v17, v245, v248\n"
      "s_waitcnt lgkmcnt(3)\n"
      "v_mfma_f32_16x16x32_f16 v[192:195], v[232:235], v[152:155], v[192:195]\n"
      "ds_read_b128 v[220:223], v33 offset:1920\n"
      "ds_write2st64_b32 v32, v180, v182 offset0:8 offset1:27\n"
      "v_lshlrev_b32 v184, 16, v57\n"
      "v_lshlrev_b32 v185, 16, v61\n"
      "v_pk_add_f16 v18, v246, v249\n"
      "v_mfma_f32_16x16x32_f16 v[196:199], v[232:235], v[156:159], v[196:199]\n"
      "v_and_b32 v186, 0xffff0000, v57\n"
      "v_and_b32 v187, 0xffff0000, v61\n"
      "v_pk_add_f16 v19, v247, v250\n"
      "s_cmp_lg_u32 s33, 0\n"
      "s_cbranch_scc1 .Lcg%=\n"
      "buffer_load_dwordx2 v[252:253], v9, s[16:19], s43 offen sc1\n"
      ".Lcg%=:\n"
      "v_mfma_f32_16x16x32_f16 v[200:203], v[232:235], v[160:163], v[200:203]\n"
      "ds_read_b128 v[224:227], v33 offset:1984\n"
      "v_fma_f32 v184, v184, v26, v28\n"
      "v_fma_f32 v185, v185, v27, v29\n"
      "v_pk_add_f16 v20, v20, v16\n"
      "s_waitcnt lgkmcnt(5)\n"
      "v_mfma_f32_16x16x32_f16 v[192:195], v[120:123], v[212:215], v[192:195]\n"
      "v_fma_f32 v186, v186, v26, v28\n"
      "v_fma_f32 v187, v187, v27, v29\n"
      "v_pk_add_f16 v21, v21, v17\n"
      "v_mfma_f32_16x16x32_f16 v[196:199], v[88:91], v[212:215], v[196:199]\n"
      "ds_read_b128 v[212:215], v33 offset:2048\n"
      "v_cvt_pk_f16_f32 v184, v184, v185\n"
      "v_cvt_pk_f16_f32 v186, v186, v187\n"
      "v_pk_add_f16 v22, v22, v18\n"
      "s_waitcnt lgkmcnt(4)\n"
      "v_mfma_f32_16x16x32_f16 v[192:195], v[124:127], v[216:219], v[192:195]\n"
      "ds_write2st64_b32 v32, v184, v186 offset0:46 offset1:65\n"
      "v_lshlrev_b32 v180, 16, v58\n"
      "v_lshlrev_b32 v181, 16, v62\n"
      "v_pk_add_f16 v23, v23, v19\n"
      "v_mfma_f32_16x16x32_f16 v[196:199], v[92:95], v[216:219], v[196:199]\n"
      "v_and_b32 v182, 0xffff0000, v58\n"
      "v_and_b32 v183, 0xffff0000, v62\n"
      "ds_write_b128 v38, v[20:23] offset:1024\n"
      "v_pk_add_f16 v16, v236, v240\n"
      "s_waitcnt lgkmcnt(5)\n"
      "v_mfma_f32_16x16x32_f16 v[192:195], v[128:131], v[220:223], v[192:195]\n"
      "v_fma_f32 v180, v180, v26, v28\n"
      "v_fma_f32 v181, v181, v27, v29\n"
      "v_pk_add_f16 v17, v237, v241\n"
      "v_mfma_f32_16x16x32_f16 v[196:199], v[96:99], v[220:223], v[196:199]\n"
      "ds_read_b64_tr_b16 v[228:229], v36 offset:24\n"
      "ds_read_b64_tr_b16 v[230:231], v36 offset:2088\n"
      "v_fma_f32 v182, v182, v26, v28\n"
      "v_fma_f32 v183, v183, v27, v29\n"
      "v_pk_add_f16 v18, v238, v242\n"
      "s_waitcnt lgkmcnt(5)\n"
      "v_mfma_f32_16x16x32_f16 v[192:195], v[132:135], v[224:227], v[192:195]\n"
      "v_cvt_pk_f16_f32 v180, v180, v181\n"
      "v_cvt_pk_f16_f32 v182, v182, v183\n"
      "v_pk_add_f16 v19, v239, v243\n"
      "v_mfma_f32_16x16x32_f16 v[196:199], v[100:103], v[224:227], v[196:199]\n"
      "ds_write2st64_b32 v32, v180, v182 offset0:84 offset1:103\n"
      "v_lshlrev_b32 v184, 16, v59\n"
      "v_lshlrev_b32 v185, 16, v63\n"
      "v_pk_add_f16 v20, v20, v16\n"
      "s_waitcnt lgkmcnt(5)\n"
      "v_mfma_f32_16x16x32_f16 v[192:195], v[136:139], v[212:215], v[192:195]\n"
      "v_and_b32 v186, 0xffff0000, v59\n"
      "v_and_b32 v187, 0xffff0000, v63\n"
      "v_pk_add_f16 v21, v21, v17\n"
      "v_mfma_f32_16x16x32_f16 v[196:199], v[104:107], v[212:215], v[196:199]\n"
      "ds_read_b64_tr_b16 v[232:233], v36 offset:536\n"
      "ds_read_b64_tr_b16 v[234:235], v36 offset:2600\n"
      "v_fma_f32 v184, v184, v26, v28\n"
      "v_fma_f32 v185, v185, v27, v29\n"
      "v_pk_add_f16 v22, v22, v18\n"
      "v_mfma_f32_16x16x32_f16 v[200:203], v[72:75], v[212:215], v[200:203]\n"
      "v_fma_f32 v186, v186, v26, v28\n"
      "v_fma_f32 v187, v187, v27, v29\n"
      "v_pk_add_f16 v23, v23, v19\n"
      "v_mfma_f32_16x16x32_f16 v[208:211], v[44:47], v[212:215], 0\n"
      "v_cvt_pk_f16_f32 v184, v184, v185\n"
      "v_cvt_pk_f16_f32 v186, v186, v187\n"
      "ds_write_b128 v38, v[20:23] offset:3104\n"
      "s_waitcnt lgkmcnt(4)\n"
      "v_mfma_f32_16x16x32_f16 v[196:199], v[228:231], v[164:167], v[196:199]\n"
      "ds_write2st64_b32 v32, v184, v186 offset0:122 offset1:141\n"
      "v_permlane32_swap_b32 v56, v60\n"
      "v_permlane32_swap_b32 v57, v61\n"
      "v_cvt_pk_bf16_f32 v24, v192, v194\n"
      "v_cvt_pk_bf16_f32 v25, v193, v195\n"
      "ds_write2st64_b32 v39, v24, v25 offset0:3 offset1:11\n"
      "v_mfma_f32_16x16x32_f16 v[200:203], v[228:231], v[168:171], v[200:203]\n"
      "v_permlane32_swap_b32 v58, v62\n"
      "v_permlane32_swap_b32 v59, v63\n"
      "v_mfma_f32_16x16x32_f16 v[204:207], v[228:231], v[172:175], v[204:207]\n"
      "v_mfma_f32_16x16x32_f16 v[208:211], v[228:231], v[176:179], v[208:211]\n"
      "ds_read_b64_tr_b16 v[228:229], v35 offset:1056\n"
      "ds_read_b64_tr_b16 v[230:231], v35 offset:1040\n"
      "s_cmp_eq_u32 s33, 0\n"
      "s_cbranch_scc1 .Lch%=\n"
      ".Lcho%=:\n"
      "s_waitcnt lgkmcnt(2)\n"
      "s_barrier\n"
      "s_setprio 3\n"
      "v_mfma_f32_16x16x32_f16 v[200:203], v[232:235], v[164:167], v[200:203]\n"
      "ds_read_b64_tr_b16 v[64:65], v40 offset:0\n"
      "ds_read_b64_tr_b16 v[66:67], v40 offset:35072\n"
      "v_lshlrev_b32 v68, 16, v48\n"
      "v_and_b32 v69, 0xffff0000, v48\n"
      "v_mfma_f32_16x16x32_f16 v[204:207], v[232:235], v[168:171], v[204:207]\n"
      "v_lshlrev_b32 v70, 16, v49\n"
      "v_and_b32 v71, 0xffff0000, v49\n"
      "ds_read_b128 v[236:239], v34 offset:2048\n"
      "ds_read_b128 v[240:243], v34 offset:2064\n"
      "ds_read_b32 v12, v14 offset:256\n"
      "v_mfma_f32_16x16x32_f16 v[208:211], v[232:235], v[172:175], v[208:211]\n"
      "ds_read_b128 v[216:219], v33 offset:2112\n"
      "ds_read_b64_tr_b16 v[232:233], v35 offset:4152\n"
      "ds_read_b64_tr_b16 v[234:235], v35 offset:4136\n"
      "v_lshlrev_b32 v26, 16, v50\n"
      "v_and_b32 v27, 0xffff0000, v50\n"
      "s_waitcnt lgkmcnt(8)\n"
      "v_mfma_f32_16x16x32_f16 v[196:199], v[228:231], v[140:143], v[196:199]\n"
      "v_lshlrev_b32 v28, 16, v51\n"
      "v_and_b32 v29, 0xffff0000, v51\n"
      "v_mfma_f32_16x16x32_f16 v[200:203], v[228:231], v[144:147], v[200:203]\n"
      "ds_read_b128 v[220:223], v33 offset:2176\n"
      "s_waitcnt lgkmcnt(8)\n"
      "v_lshlrev_b32 v48, 16, v64\n"
      "v_and_b32 v64, 0xffff0000, v64\n"
      "v_mfma_f32_16x16x32_f16 v[204:207], v[228:231], v[148:151], v[204:207]\n"
      "v_fmac_f32 v68, s72, v48\n"
      "v_fmac_f32 v69, s73, v64\n"
      "s_waitcnt lgkmcnt(1)\n"
      "v_mfma_f32_16x16x32_f16 v[196:199], v[232:235], v[152:155], v[196:199]\n"
      "ds_read_b128 v[224:227], v33 offset:2240\n"
      "v_lshlrev_b32 v49, 16, v65\n"
      "v_and_b32 v65, 0xffff0000, v65\n"
      "v_mfma_f32_16x16x32_f16 v[200:203], v[232:235], v[156:159], v[200:203]\n"
      "v_fmac_f32 v70, s74, v49\n"
      "v_fmac_f32 v71, s75, v65\n"
      "v_mfma_f32_16x16x32_f16 v[204:207], v[232:235], v[160:163], v[204:207]\n"
      "ds_read_b128 v[212:215], v33 offset:2304\n"
      "v_lshlrev_b32 v50, 16, v66\n"
      "v_and_b32 v66, 0xffff0000, v66\n"
      "v_mfma_f32_16x16x32_f16 v[196:199], v[108:111], v[216:219], v[196:199]\n"
      "v_fmac_f32 v26, s76, v50\n"
      "v_fmac_f32 v27, s77, v66\n"
      "v_mfma_f32_16x16x32_f16 v[200:203], v[76:79], v[216:219], v[200:203]\n"
      "ds_read_b64_tr_b16 v[228:229], v35 offset:1568\n"
      "ds_read_b64_tr_b16 v[230:231], v35 offset:1552\n"
      "v_lshlrev_b32 v51, 16, v67\n"
      "v_and_b32 v67, 0xffff0000, v67\n"
      "s_waitcnt lgkmcnt(4)\n"
      "v_mfma_f32_16x16x32_f16 v[196:199], v[112:115], v[220:223], v[196:199]\n"
      "v_fmac_f32 v28, s78, v51\n"
      "v_fmac_f32 v29, s79, v67\n"
      "v_mfma_f32_16x16x32_f16 v[200:203], v[80:83], v[220:223], v[200:203]\n"
      "v_cvt_pk_bf16_f32 v48, v68, v69\n"
      "s_waitcnt lgkmcnt(3)\n"
      "v_mfma_f32_16x16x32_f16 v[196:199], v[116:119], v[224:227], v[196:199]\n"
      "ds_read_b128 v[216:219], v33 offset:2368\n"
      "v_cvt_pk_bf16_f32 v49, v70, v71\n"
      "s_setprio 2\n"
      "v_mfma_f32_16x16x32_f16 v[200:203], v[84:87], v[224:227], v[200:203]\n"
      "v_cvt_pk_bf16_f32 v50, v26, v27\n"
      "s_waitcnt lgkmcnt(3)\n"
      "v_mfma_f32_16x16x32_f16 v[196:199], v[120:123], v[212:215], v[196:199]\n"
      "ds_read_b128 v[220:223], v33 offset:2432\n"
      "v_cvt_pk_bf16_f32 v51, v28, v29\n"
      "v_lshlrev_b32 v13, 24, v12\n"
      "v_and_or_b32 v13, v13, s58, v42\n"
      "v_mfma_f32_16x16x32_f16 v[200:203], v[88:91], v[212:215], v[200:203]\n"
      "s_cmp_lg_u32 s13, 0\n"
      "s_cbranch_scc1 .Lcs00%=\n"
      "buffer_store_dwordx4 v[48:51], v13, s[24:27], s54 offen\n"
      ".Lcs00%=:\n"
      "v_alignbit_b32 v245, v238, v237, 16\n"
      "s_waitcnt lgkmcnt(2)\n"
      "v_mfma_f32_16x16x32_f16 v[200:203], v[228:231], v[140:143], v[200:203]\n"
      "ds_read_b128 v[224:227], v33 offset:2496\n"
      "v_alignbit_b32 v246, v239, v238, 16\n"
      "v_mfma_f32_16x16x32_f16 v[204:207], v[228:231], v[144:147], v[204:207]\n"
      "s_cmp_lg_u32 s13, 1\n"
      "s_cbranch_scc1 .Lcs01%=\n"
      "buffer_store_dwordx4 v[48:51], v13, s[24:27], s54 offen\n"
      ".Lcs01%=:\n"
      "v_alignbit_b32 v247, v240, v239, 16\n"
      "v_mfma_f32_16x16x32_f16 v[208:211], v[228:231], v[148:151], v[208:211]\n"
      "ds_read_b128 v[212:215], v33 offset:2560\n"
      "v_alignbit_b32 v248, v241, v240, 16\n"
      "s_waitcnt lgkmcnt(3)\n"
      "v_mfma_f32_16x16x32_f16 v[196:199], v[124:127], v[216:219], v[196:199]\n"
      "s_cmp_lg_u32 s13, 2\n"
      "s_cbranch_scc1 .Lcs02%=\n"
      "buffer_store_dwordx4 v[48:51], v13, s[24:27], s54 offen\n"
      ".Lcs02%=:\n"
      "v_alignbit_b32 v249, v242, v241, 16\n"
      "v_mfma_f32_16x16x32_f16 v[200:203], v[92:95], v[216:219], v[200:203]\n"
      "v_pk_add_f16 v16, v245, v246\n"
      "s_waitcnt lgkmcnt(2)\n"
      "v_mfma_f32_16x16x32_f16 v[196:199], v[128:131], v[220:223], v[196:199]\n"
      "ds_read_b64_tr_b16 v[232:233], v35 offset:4664\n"
      "ds_read_b64_tr_b16 v[234:235], v35 offset:4648\n"
      "s_cmp_lg_u32 s13, 3\n"
      "s_cbranch_scc1 .Lcs03%=\n"
      "buffer_store_dwordx4 v[48:51], v13, s[24:27], s54 offen\n"
      ".Lcs03%=:\n"
      "ds_read_b64_tr_b16 v[64:65], v40 offset:512\n"
      "ds_read_b64_tr_b16 v[66:67], v40 offset:35584\n"
      "v_lshlrev_b32 v68, 16, v52\n"
      "v_and_b32 v69, 0xffff0000, v52\n"
      "v_mfma_f32_16x16x32_f16 v[200:203], v[96:99], v[220:223], v[200:203]\n"
      "v_lshlrev_b32 v70, 16, v53\n"
      "v_and_b32 v71, 0xffff0000, v53\n"
      "s_waitcnt lgkmcnt(5)\n"
      "v_mfma_f32_16x16x32_f16 v[196:199], v[132:135], v[224:227], v[196:199]\n"
      "s_cmp_lg_u32 s13, 4\n"
      "s_cbranch_scc1 .Lcs04%=\n"
      "buffer_store_dwordx4 v[48:51], v13, s[24:27], s54 offen\n"
      ".Lcs04%=:\n"
      "v_lshlrev_b32 v26, 16, v54\n"
      "v_and_b32 v27, 0xffff0000, v54\n"
      "v_mfma_f32_16x16x32_f16 v[200:203], v[100:103], v[224:227], v[200:203]\n"
      "ds_read_b128 v[216:219], v33 offset:2624\n"
      "v_lshlrev_b32 v28, 16, v55\n"
      "v_and_b32 v29, 0xffff0000, v55\n"
      "s_waitcnt lgkmcnt(5)\n"
      "v_mfma_f32_16x16x32_f16 v[196:199], v[136:139], v[212:215], v[196:199]\n"
      "s_cmp_lg_u32 s13, 5\n"
      "s_cbranch_scc1 .Lcs05%=\n"
      "buffer_store_dwordx4 v[48:51], v13, s[24:27], s54 offen\n"
      ".Lcs05%=:\n"
      "s_waitcnt lgkmcnt(2)\n"
      "v_lshlrev_b32 v52, 16, v64\n"
      "v_and_b32 v64, 0xffff0000, v64\n"
      "v_mfma_f32_16x16x32_f16 v[200:203], v[104:107], v[212:215], v[200:203]\n"
      "ds_read_b128 v[220:223], v33 offset:2688\n"
      "v_fmac_f32 v68, s72, v52\n"
      "v_fmac_f32 v69, s73, v64\n"
      "s_setprio 1\n"
      "v_mfma_f32_16x16x32_f16 v[204:207], v[72:75], v[212:215], v[204:207]\n"
      "s_cmp_lg_u32 s13, 6\n"
      "s_cbranch_scc1 .Lcs06%=\n"
      "buffer_store_dwordx4 v[48:51], v13, s[24:27], s54 offen\n"
      ".Lcs06%=:\n"
      "v_lshlrev_b32 v53, 16, v65\n"
      "v_and_b32 v65, 0xffff0000, v65\n"
      "v_mfma_f32_16x16x32_f16 v[200:203], v[232:235], v[152:155], v[200:203]\n"
      "ds_read_b128 v[224:227], v33 offset:2752\n"
      "v_fmac_f32 v70, s74, v53\n"
      "v_fmac_f32 v71, s75, v65\n"
      "v_mfma_f32_16x16x32_f16 v[204:207], v[232:235], v[156:159], v[204:207]\n"
      "s_cmp_lg_u32 s13, 7\n"
      "s_cbranch_scc1 .Lcs07%=\n"
      "buffer_store_dwordx4 v[48:51], v13, s[24:27], s54 offen\n"
      ".Lcs07%=:\n"
      "s_waitcnt lgkmcnt(3)\n"
      "v_lshlrev_b32 v54, 16, v66\n"
      "v_and_b32 v66, 0xffff0000, v66\n"
      "v_cvt_pk_bf16_f32 v24, v196, v198\n"
      "v_cvt_pk_bf16_f32 v25, v197, v199\n"
      "ds_write2st64_b32 v39, v24, v25 offset0:4 offset1:12\n"
      "v_mfma_f32_16x16x32_f16 v[208:211], v[232:235], v[160:163], v[208:211]\n"
      "ds_read_b128 v[212:215], v33 offset:2816\n"
      "v_fmac_f32 v26, s76, v54\n"
      "v_fmac_f32 v27, s77, v66\n"
      "s_waitcnt lgkmcnt(4)\n"
      "v_mfma_f32_16x16x32_f16 v[200:203], v[108:111], v[216:219], v[200:203]\n"
      "v_lshlrev_b32 v55, 16, v67\n"
      "v_and_b32 v67, 0xffff0000, v67\n"
      "v_mfma_f32_16x16x32_f16 v[204:207], v[76:79], v[216:219], v[204:207]\n"
      "ds_read_b128 v[216:219], v33 offset:2880\n"
      "v_fmac_f32 v28, s78, v55\n"
      "v_fmac_f32 v29, s79, v67\n"
      "s_waitcnt lgkmcnt(4)\n"
      "v_mfma_f32_16x16x32_f16 v[200:203], v[112:115], v[220:223], v[200:203]\n"
      "v_cvt_pk_bf16_f32 v52, v68, v69\n"
      "v_mfma_f32_16x16x32_f16 v[204:207], v[80:83], v[220:223], v[204:207]\n"
      "ds_read_b128 v[220:223], v33 offset:2944\n"
      "v_cvt_pk_bf16_f32 v53, v70, v71\n"
      "s_waitcnt lgkmcnt(4)\n"
      "v_mfma_f32_16x16x32_f16 v[200:203], v[116:119], v[224:227], v[200:203]\n"
      "v_cvt_pk_bf16_f32 v54, v26, v27\n"
      "v_mfma_f32_16x16x32_f16 v[204:207], v[84:87], v[224:227], v[204:207]\n"
      "ds_read_b128 v[224:227], v33 offset:3008\n"
      "v_cvt_pk_bf16_f32 v55, v28, v29\n"
      "v_lshlrev_b32 v13, 16, v12\n"
      "v_and_or_b32 v13, v13, s58, v42\n"
      "s_waitcnt lgkmcnt(3)\n"
      "v_mfma_f32_16x16x32_f16 v[200:203], v[120:123], v[212:215], v[200:203]\n"
      "s_cmp_lg_u32 s13, 0\n"
      "s_cbranch_scc1 .Lcs10%=\n"
      "buffer_store_dwordx4 v[52:55], v13, s[24:27], s55 offen\n"
      ".Lcs10%=:\n"
      "v_pk_add_f16 v17, v246, v247\n"
      "s_setprio 0\n"
      "v_mfma_f32_16x16x32_f16 v[204:207], v[88:91], v[212:215], v[204:207]\n"
      "ds_read_b128 v[212:215], v33 offset:3072\n"
      "v_pk_add_f16 v18, v247, v248\n"
      "s_waitcnt lgkmcnt(3)\n"
      "v_mfma_f32_16x16x32_f16 v[200:203], v[124:127], v[216:219], v[200:203]\n"
      "s_cmp_lg_u32 s13, 1\n"
      "s_cbranch_scc1 .Lcs11%=\n"
      "buffer_store_dwordx4 v[52:55], v13, s[24:27], s55 offen\n"
      ".Lcs11%=:\n"
      "v_pk_add_f16 v19, v248, v249\n"
      "v_mfma_f32_16x16x32_f16 v[204:207], v[92:95], v[216:219], v[204:207]\n"
      "v_pk_add_f16 v20, v16, v238\n"
      "s_waitcnt lgkmcnt(2)\n"
      "v_mfma_f32_16x16x32_f16 v[200:203], v[128:131], v[220:223], v[200:203]\n"
      "ds_read_b64_tr_b16 v[228:229], v36 offset:1048\n"
      "ds_read_b64_tr_b16 v[230:231], v36 offset:3112\n"
      "s_cmp_lg_u32 s13, 2\n"
      "s_cbranch_scc1 .Lcs12%=\n"
      "buffer_store_dwordx4 v[52:55], v13, s[24:27], s55 offen\n"
      ".Lcs12%=:\n"
      "v_pk_add_f16 v21, v17, v239\n"
      "v_mfma_f32_16x16x32_f16 v[204:207], v[96:99], v[220:223], v[204:207]\n"
      "v_pk_add_f16 v22, v18, v240\n"
      "s_waitcnt lgkmcnt(3)\n"
      "v_mfma_f32_16x16x32_f16 v[200:203], v[132:135], v[224:227], v[200:203]\n"
      "ds_read_b128 v[216:219], v33 offset:3136\n"
      "s_cmp_lg_u32 s13, 3\n"
      "s_cbranch_scc1 .Lcs13%=\n"
      "buffer_store_dwordx4 v[52:55], v13, s[24:27], s55 offen\n"
      ".Lcs13%=:\n"
      "v_pk_add_f16 v23, v19, v241\n"
      "v_mfma_f32_16x16x32_f16 v[204:207], v[100:103], v[224:227], v[204:207]\n"
      "ds_write_b128 v37, v[20:23] offset:2048\n"
      "v_pk_add_f16 v16, v237, v239\n"
      "s_waitcnt lgkmcnt(4)\n"
      "v_mfma_f32_16x16x32_f16 v[200:203], v[136:139], v[212:215], v[200:203]\n"
      "ds_read_b128 v[220:223], v33 offset:3200\n"
      "s_cmp_lg_u32 s13, 4\n"
      "s_cbranch_scc1 .Lcs14%=\n"
      "buffer_store_dwordx4 v[52:55], v13, s[24:27], s55 offen\n"
      ".Lcs14%=:\n"
      "v_pk_add_f16 v17, v238, v240\n"
      "v_mfma_f32_16x16x32_f16 v[204:207], v[104:107], v[212:215], v[204:207]\n"
      "v_pk_add_f16 v18, v239, v241\n"
      "v_mfma_f32_16x16x32_f16 v[208:211], v[72:75], v[212:215], v[208:211]\n"
      "ds_read_b128 v[224:227], v33 offset:3264\n"
      "s_cmp_lg_u32 s13, 5\n"
      "s_cbranch_scc1 .Lcs15%=\n"
      "buffer_store_dwordx4 v[52:55], v13, s[24:27], s55 offen\n"
      ".Lcs15%=:\n"
      "v_pk_add_f16 v19, v240, v242\n"
      "s_waitcnt lgkmcnt(4)\n"
      "v_mfma_f32_16x16x32_f16 v[204:207], v[228:231], v[164:167], v[204:207]\n"
      "v_pk_add_f16 v20, v20, v16\n"
      "v_mfma_f32_16x16x32_f16 v[208:211], v[228:231], v[168:171], v[208:211]\n"
      "ds_read_b64_tr_b16 v[232:233], v36 offset:1560\n"
      "ds_read_b64_tr_b16 v[234:235], v36 offset:3624\n"
      "s_cmp_lg_u32 s13, 6\n"
      "s_cbranch_scc1 .Lcs16%=\n"
      "buffer_store_dwordx4 v[52:55], v13, s[24:27], s55 offen\n"
      ".Lcs16%=:\n"
      "v_pk_add_f16 v21, v21, v17\n"
      "v_cvt_pk_bf16_f32 v24, v200, v202\n"
      "v_cvt_pk_bf16_f32 v25, v201, v203\n"
      "ds_write2st64_b32 v39, v24, v25 offset0:5 offset1:13\n"
      "s_waitcnt lgkmcnt(6)\n"
      "v_mfma_f32_16x16x32_f16 v[204:207], v[108:111], v[216:219], v[204:207]\n"
      "ds_read_b128 v[212:215], v33 offset:3328\n"
      "v_pk_add_f16 v22, v22, v18\n"
      "v_mfma_f32_16x16x32_f16 v[208:211], v[76:79], v[216:219], v[208:211]\n"
      "s_cmp_lg_u32 s13, 7\n"
      "s_cbranch_scc1 .Lcs17%=\n"
      "buffer_store_dwordx4 v[52:55], v13, s[24:27], s55 offen\n"
      ".Lcs17%=:\n"
      "v_pk_add_f16 v23, v23, v19\n"
      "s_waitcnt lgkmcnt(5)\n"
      "v_mfma_f32_16x16x32_f16 v[204:207], v[112:115], v[220:223], v[204:207]\n"
      "ds_read_b128 v[216:219], v33 offset:3392\n"
      "ds_write_b128 v37, v[20:23] offset:5152\n"
      "s_waitcnt lgkmcnt(3)\n"
      "s_barrier\n"
      "s_setprio 3\n"
      "v_mfma_f32_16x16x32_f16 v[208:211], v[80:83], v[220:223], v[208:211]\n"
      "ds_read_b64_tr_b16 v[64:65], v40 offset:1024\n"
      "ds_read_b64_tr_b16 v[66:67], v40 offset:36096\n"
      "v_lshlrev_b32 v68, 16, v56\n"
      "v_and_b32 v69, 0xffff0000, v56\n"
      "v_mfma_f32_16x16x32_f16 v[204:207], v[116:119], v[224:227], v[204:207]\n"
      "ds_read_b128 v[220:223], v33 offset:3456\n"
      "v_lshlrev_b32 v70, 16, v57\n"
      "v_and_b32 v71, 0xffff0000, v57\n"
      "v_mfma_f32_16x16x32_f16 v[208:211], v[84:87], v[224:227], v[208:211]\n"
      "v_lshlrev_b32 v26, 16, v58\n"
      "v_and_b32 v27, 0xffff0000, v58\n"
      "v_mfma_f32_16x16x32_f16 v[208:211], v[232:235], v[164:167], v[208:211]\n"
      "ds_read_b64_tr_b16 v[228:229], v35 offset:2080\n"
      "ds_read_b64_tr_b16 v[230:231], v35 offset:2064\n"
      "v_lshlrev_b32 v28, 16, v59\n"
      "v_and_b32 v29, 0xffff0000, v59\n"
      "s_waitcnt lgkmcnt(7)\n"
      "v_mfma_f32_16x16x32_f16 v[204:207], v[120:123], v[212:215], v[204:207]\n"
      "s_waitcnt lgkmcnt(4)\n"
      "v_lshlrev_b32 v56, 16, v64\n"
      "v_and_b32 v64, 0xffff0000, v64\n"
      "v_mfma_f32_16x16x32_f16 v[208:211], v[88:91], v[212:215], v[208:211]\n"
      "ds_read_b128 v[224:227], v33 offset:3520\n"
      "v_fmac_f32 v68, s72, v56\n"
      "v_fmac_f32 v69, s73, v64\n"
      "v_mfma_f32_16x16x32_f16 v[204:207], v[124:127], v[216:219], v[204:207]\n"
      "v_lshlrev_b32 v57, 16, v65\n"
      "v_and_b32 v65, 0xffff0000, v65\n"
      "v_mfma_f32_16x16x32_f16 v[208:211], v[92:95], v[216:219], v[208:211]\n"
      "ds_read_b128 v[212:215], v33 offset:3584\n"
      "v_fmac_f32 v70, s74, v57\n"
      "v_fmac_f32 v71, s75, v65\n"
      "s_setprio 2\n"
      "s_waitcnt lgkmcnt(4)\n"
      "v_mfma_f32_16x16x32_f16 v[204:207], v[128:131], v[220:223], v[204:207]\n"
      "v_lshlrev_b32 v58, 16, v66\n"
      "v_and_b32 v66, 0xffff0000, v66\n"
      "v_mfma_f32_16x16x32_f16 v[208:211], v[96:99], v[220:223], v[208:211]\n"
      "ds_read_b128 v[216:219], v33 offset:3648\n"
      "v_fmac_f32 v26, s76, v58\n"
      "v_fmac_f32 v27, s77, v66\n"
      "s_waitcnt lgkmcnt(3)\n"
      "v_mfma_f32_16x16x32_f16 v[204:207], v[228:231], v[140:143], v[204:207]\n"
      "ds_read_b64_tr_b16 v[232:233], v35 offset:5176\n"
      "ds_read_b64_tr_b16 v[234:235], v35 offset:5160\n"
      "v_lshlrev_b32 v59, 16, v67\n"
      "v_and_b32 v67, 0xffff0000, v67\n"
      "v_mfma_f32_16x16x32_f16 v[208:211], v[228:231], v[144:147], v[208:211]\n"
      "v_fmac_f32 v28, s78, v59\n"
      "v_fmac_f32 v29, s79, v67\n"
      "s_waitcnt lgkmcnt(4)\n"
      "v_mfma_f32_16x16x32_f16 v[204:207], v[132:135], v[224:227], v[204:207]\n"
      "ds_read_b128 v[220:223], v33 offset:3712\n"
      "v_cvt_pk_bf16_f32 v56, v68, v69\n"
      "v_mfma_f32_16x16x32_f16 v[208:211], v[100:103], v[224:227], v[208:211]\n"
      "ds_read_b128 v[224:227], v33 offset:3776\n"
      "v_cvt_pk_bf16_f32 v57, v70, v71\n"
      "s_waitcnt lgkmcnt(5)\n"
      "v_mfma_f32_16x16x32_f16 v[204:207], v[136:139], v[212:215], v[204:207]\n"
      "v_cvt_pk_bf16_f32 v58, v26, v27\n"
      "s_setprio 1\n"
      "v_mfma_f32_16x16x32_f16 v[208:211], v[104:107], v[212:215], v[208:211]\n"
      "ds_read_b64_tr_b16 v[228:229], v35 offset:2592\n"
      "ds_read_b64_tr_b16 v[230:231], v35 offset:2576\n"
      "ds_read_b128 v[212:215], v33 offset:3840\n"
      "v_cvt_pk_bf16_f32 v59, v28, v29\n"
      "v_lshlrev_b32 v13, 8, v12\n"
      "v_and_or_b32 v13, v13, s58, v42\n"
      "s_waitcnt lgkmcnt(7)\n"
      "v_mfma_f32_16x16x32_f16 v[208:211], v[108:111], v[216:219], v[208:211]\n"
      "ds_read_b128 v[216:219], v33 offset:3904\n"
      "s_cmp_lg_u32 s13, 0\n"
      "s_cbranch_scc1 .Lcs20%=\n"
      "buffer_store_dwordx4 v[56:59], v13, s[24:27], s56 offen\n"
      ".Lcs20%=:\n"
      "s_waitcnt lgkmcnt(6)\n"
      "v_mfma_f32_16x16x32_f16 v[204:207], v[232:235], v[152:155], v[204:207]\n"
      "v_mfma_f32_16x16x32_f16 v[208:211], v[232:235], v[156:159], v[208:211]\n"
      "ds_read_b64_tr_b16 v[232:233], v35 offset:5688\n"
      "ds_read_b64_tr_b16 v[234:235], v35 offset:5672\n"
      "s_cmp_lg_u32 s13, 1\n"
      "s_cbranch_scc1 .Lcs21%=\n"
      "buffer_store_dwordx4 v[56:59], v13, s[24:27], s56 offen\n"
      ".Lcs21%=:\n"
      "s_waitcnt lgkmcnt(7)\n"
      "v_mfma_f32_16x16x32_f16 v[208:211], v[112:115], v[220:223], v[208:211]\n"
      "ds_read_b128 v[220:223], v33 offset:3968\n"
      "s_setprio 0\n"
      "s_waitcnt lgkmcnt(7)\n"
      "v_mfma_f32_16x16x32_f16 v[208:211], v[116:119], v[224:227], v[208:211]\n"
      "ds_read_b128 v[224:227], v33 offset:4032\n"
      "s_cmp_lg_u32 s13, 2\n"
      "s_cbranch_scc1 .Lcs22%=\n"
      "buffer_store_dwordx4 v[56:59], v13, s[24:27], s56 offen\n"
      ".Lcs22%=:\n"
      "s_waitcnt lgkmcnt(5)\n"
      "v_mfma_f32_16x16x32_f16 v[208:211], v[120:123], v[212:215], v[208:211]\n"
      "ds_read_b128 v[212:215], v33 offset:4096\n"
      "v_cvt_pk_bf16_f32 v24, v204, v206\n"
      "v_cvt_pk_bf16_f32 v25, v205, v207\n"
      "ds_write2st64_b32 v39, v24, v25 offset0:6 offset1:14\n"
      "v_mfma_f32_16x16x32_f16 v[208:211], v[228:231], v[140:143], v[208:211]\n"
      "s_cmp_lg_u32 s13, 3\n"
      "s_cbranch_scc1 .Lcs23%=\n"
      "buffer_store_dwordx4 v[56:59], v13, s[24:27], s56 offen\n"
      ".Lcs23%=:\n"
      "s_waitcnt lgkmcnt(6)\n"
      "v_mfma_f32_16x16x32_f16 v[208:211], v[124:127], v[216:219], v[208:211]\n"
      "s_waitcnt lgkmcnt(3)\n"
      "v_mfma_f32_16x16x32_f16 v[208:211], v[128:131], v[220:223], v[208:211]\n"
      "s_cmp_lg_u32 s13, 4\n"
      "s_cbranch_scc1 .Lcs24%=\n"
      "buffer_store_dwordx4 v[56:59], v13, s[24:27], s56 offen\n"
      ".Lcs24%=:\n"
      "v_mfma_f32_16x16x32_f16 v[208:211], v[232:235], v[152:155], v[208:211]\n"
      "s_waitcnt lgkmcnt(2)\n"
      "v_mfma_f32_16x16x32_f16 v[208:211], v[132:135], v[224:227], v[208:211]\n"
      "s_cmp_lg_u32 s13, 5\n"
      "s_cbranch_scc1 .Lcs25%=\n"
      "buffer_store_dwordx4 v[56:59], v13, s[24:27], s56 offen\n"
      ".Lcs25%=:\n"
      "s_waitcnt lgkmcnt(1)\n"
      "v_mfma_f32_16x16x32_f16 v[208:211], v[136:139], v[212:215], v[208:211]\n"
      "s_cmp_lg_u32 s13, 6\n"
      "s_cbranch_scc1 .Lcs26%=\n"
      "buffer_store_dwordx4 v[56:59], v13, s[24:27], s56 offen\n"
      ".Lcs26%=:\n"
      "s_cmp_lg_u32 s13, 7\n"
      "s_cbranch_scc1 .Lcs27%=\n"
      "buffer_store_dwordx4 v[56:59], v13, s[24:27], s56 offen\n"
      ".Lcs27%=:\n"
      "v_lshlrev_b32 v68, 16, v60\n"
      "v_and_b32 v69, 0xffff0000, v60\n"
      "v_lshlrev_b32 v70, 16, v61\n"
      "v_and_b32 v71, 0xffff0000, v61\n"
      "v_lshlrev_b32 v26, 16, v62\n"
      "v_and_b32 v27, 0xffff0000, v62\n"
      "v_lshlrev_b32 v28, 16, v63\n"
      "v_and_b32 v29, 0xffff0000, v63\n"
      "v_cvt_pk_bf16_f32 v24, v208, v210\n"
      "v_cvt_pk_bf16_f32 v25, v209, v211\n"
      "ds_write2st64_b32 v39, v24, v25 offset0:7 offset1:15\n"
      "s_waitcnt lgkmcnt(0)\n"
      "s_barrier\n"
      "ds_read_b64_tr_b16 v[64:65], v40 offset:1536\n"
      "ds_read_b64_tr_b16 v[66:67], v40 offset:36608\n"
      "s_cmp_lg_u32 s33, 0\n"
      "s_cbranch_scc1 .Lcb%=\n"
      "ds_read_u16_d16_hi v252, v8 offset:56384\n"
      "ds_read_u16_d16_hi v253, v8 offset:65152\n"
      "s_waitcnt lgkmcnt(0)\n"
      "v_pk_mul_f32 v[252:253], v[252:253], v[254:255]\n"
      "v_cvt_pk_bf16_f32 v252, v252, v253\n"
      "buffer_atomic_pk_add_bf16 v252, v251, s[24:27], s41 offen\n"
      ".Lcb%=:\n"
      "s_waitcnt lgkmcnt(1)\n"
      "v_lshlrev_b32 v60, 16, v64\n"
      "v_and_b32 v64, 0xffff0000, v64\n"
      "v_fmac_f32 v68, s72, v60\n"
      "v_fmac_f32 v69, s73, v64\n"
      "v_lshlrev_b32 v61, 16, v65\n"
      "v_and_b32 v65, 0xffff0000, v65\n"
      "v_fmac_f32 v70, s74, v61\n"
      "v_fmac_f32 v71, s75, v65\n"
      "s_waitcnt lgkmcnt(0)\n"
      "v_lshlrev_b32 v62, 16, v66\n"
      "v_and_b32 v66, 0xffff0000, v66\n"
      "v_fmac_f32 v26, s76, v62\n"
      "v_fmac_f32 v27, s77, v66\n"
      "v_lshlrev_b32 v63, 16, v67\n"
      "v_and_b32 v67, 0xffff0000, v67\n"
      "v_fmac_f32 v28, s78, v63\n"
      "v_fmac_f32 v29, s79, v67\n"
      "v_cvt_pk_bf16_f32 v60, v68, v69\n"
      "v_cvt_pk_bf16_f32 v61, v70, v71\n"
      "v_cvt_pk_bf16_f32 v62, v26, v27\n"
      "v_cvt_pk_bf16_f32 v63, v28, v29\n"
      "v_and_or_b32 v13, v12, s58, v42\n"
      "buffer_store_dwordx4 v[60:63], v13, s[24:27], s57 offen\n"
      ".LEx%=:\ns_endpgm\n"
      ".Lcsgsk%=:\n"
      "s_sleep 4\n"
      "s_sub_u32 s59, s59, 1\n"
      "s_cbranch_scc1 .Lcsgok%=\n"
      "buffer_load_dwordx4 v[64:67], v42, s[16:19], s62 offen sc1\n"
      "s_waitcnt vmcnt(0)\n"
      "s_branch .Lcsgk%=\n"
      ".Lcsgsh%=:\n"
      "s_sleep 4\n"
      "s_sub_u32 s59, s59, 1\n"
      "s_cbranch_scc1 .Lcsgoh%=\n"
      "buffer_load_dwordx4 v[68:71], v42, s[16:19], s63 offen sc1\n"
      "s_waitcnt vmcnt(0)\n"
      "s_branch .Lcsgh%=\n"
      ".Lch%=:\n"
      "s_movk_i32 s59, 0x7fff\n"
      ".Lchl%=:\n"
      "s_waitcnt vmcnt(0)\n"
      "v_cmp_ne_u32 vcc, s15, v253\n"
      "v_not_b32 v255, v252\n"
      "s_sub_u32 s59, s59, 1\n"
      "s_cbranch_scc1 .Lchk%=\n"
      "s_cbranch_vccz .Lchk%=\n"
      "s_sleep 1\n"
      "buffer_load_dwordx2 v[252:253], v9, s[16:19], s43 offen sc1\n"
      "s_branch .Lchl%=\n"
      ".Lchk%=:\n"
      "v_and_b32 v31, 0x1ff, v255\n"
      "v_bfe_u32 v8, v255, 9, 2\n"
      "v_lshl_add_u32 v31, v31, 2, v8\n"
      "v_mov_b32 v8, 64\n"
      "v_add_u32 v31, 0x27700, v31\n"
      "ds_write_b8 v31, v8 \n"
      "v_and_b32 v31, 1, v255\n"
      "v_and_b32 v8, 0x7fe, v255\n"
      "v_lshl_add_u32 v8, v31, 11, v8\n"
      "v_mul_u32_u24 v31, 0x1120, v10\n"
      "v_add_u32 v8, v8, v31\n"
      "v_lshlrev_b32 v31, 1, v10\n"
      "v_add_u32 v31, 0x27600, v31\n"
      "v_and_b32 v255, 0x7ff, v255\n"
      "v_lshl_add_u32 v251, v255, 11, v10\n"
      "v_and_b32 v252, 32, v43\n"
      "v_lshl_or_b32 v251, v252, 25, v251\n"
      "ds_read_b64 v[254:255], v31 \n"
      "v_mov_b32 v252, 0\n"
      "v_mov_b32 v253, 0\n"
      "s_waitcnt lgkmcnt(0)\n"
      "s_branch .Lcho%=\n"
      ".LG%=:\n"
      "s_setprio 3\n"
      "s_mul_i32 s35, s48, 14\n"
      "s_add_u32 s35, s35, s49\n"
      "s_and_b32 s55, s33, 3\n"
      "s_mul_i32 s50, s55, 1835008\n"
      "s_lshr_b32 s51, s35, 1\n"
      "s_lshl_b32 s51, s51, 7\n"
      "s_add_u32 s50, s50, s51\n"
      "v_lshrrev_b32 v37, 3, v43\n"
      "v_and_b32 v38, 7, v43\n"
      "v_mul_u32_u24 v37, 0xe000, v37\n"
      "v_lshlrev_b32 v38, 4, v38\n"
      "v_add3_u32 v32, v37, v38, s50\n"
      "s_cmp_lt_u32 s34, 112\n"
      "s_cselect_b32 s26, 0x700000, 0\n"
      "s_mov_b32 s27, 0x20000\n"
      "s_bitcmp1_b32 s49, 0\n"
      "s_waitcnt lgkmcnt(0)\n"
      "s_load_dword s15, s[88:89], 0x0\n"
      "s_cselect_b32 s24, s82, s80\n"
      "s_cselect_b32 s25, s83, s81\n"
      "s_and_b32 s25, s25, 0xffff\n"
      "s_mov_b32 s16, s88\n"
      "s_and_b32 s17, s89, 0xffff\n"
      "s_mov_b32 s18, 0x32c000\n"
      "s_mov_b32 s19, 0x20000\n"
      "s_mov_b32 s36, s86\n"
      "s_and_b32 s37, s87, 0xffff\n"
      "s_sleep 16\n"
      "s_mov_b32 s60, 0\n"
      "buffer_load_dwordx4 v[64:67], v32, s[24:27], s60 offen nt\n"
      "s_sleep 2\n"
      "s_mov_b32 s60, 7168\n"
      "buffer_load_dwordx4 v[68:71], v32, s[24:27], s60 offen nt\n"
      "s_sleep 2\n"
      "s_mov_b32 s60, 14336\n"
      "buffer_load_dwordx4 v[72:75], v32, s[24:27], s60 offen nt\n"
      "s_sleep 2\n"
      "s_mov_b32 s60, 21504\n"
      "buffer_load_dwordx4 v[76:79], v32, s[24:27], s60 offen nt\n"
      "s_sleep 2\n"
      "s_mov_b32 s60, 28672\n"
      "buffer_load_dwordx4 v[80:83], v32, s[24:27], s60 offen nt\n"
      "s_sleep 2\n"
      "s_mov_b32 s60, 35840\n"
      "buffer_load_dwordx4 v[84:87], v32, s[24:27], s60 offen nt\n"
      "s_sleep 2\n"
      "s_mov_b32 s60, 43008\n"
      "buffer_load_dwordx4 v[88:91], v32, s[24:27], s60 offen nt\n"
      "s_sleep 2\n"
      "s_mov_b32 s60, 50176\n"
      "buffer_load_dwordx4 v[92:95], v32, s[24:27], s60 offen nt\n"
      "s_sleep 2\n"
      "s_waitcnt lgkmcnt(0)\n"
      "s_add_u32 s15, s15, 0x9e3779b1\n"
      "v_mov_b32 v47, s15\n"
      "s_cmp_ge_u32 s34, 112\n"
      "s_cbranch_scc1 .LT%=\n"
      "v_mov_b32 v48, 0x22680\n"
      "ds_read_b32 v48, v48 \n"
      "s_waitcnt lgkmcnt(0)\n"
      "v_readfirstlane_b32 s12, v48\n"
      "s_mov_b32 s62, 0x05040100\n"
      "s_mov_b32 s63, 0x07060302\n"
      "s_lshl_b32 s42, s34, 3\n"
      "s_add_u32 s42, s42, 4096\n"
      "s_lshl_b32 s43, s48, 8\n"
      "s_and_b32 s50, s49, 1\n"
      "s_lshl_b32 s50, s50, 7\n"
      "s_add_u32 s43, s43, s50\n"
      "s_add_u32 s43, s43, 40960\n"
      "s_mov_b32 s20, s64\n"
      "s_and_b32 s21, s65, 0xffff\n"
      "s_mov_b32 s22, 0x400000\n"
      "s_mov_b32 s23, 0x20000\n"
      "v_lshrrev_b32 v39, 4, v43\n"
      "v_and_b32 v38, 15, v43\n"
      "v_lshlrev_b32 v38, 4, v38\n"
      "v_bfe_u32 v33, v43, 2, 2\n"
      "v_and_b32 v37, 1, v43\n"
      "v_lshl_add_u32 v33, v33, 1, v37\n"
      "v_lshlrev_b32 v33, 3, v33\n"
      "v_and_b32 v37, 2, v43\n"
      "v_lshl_add_u32 v37, v39, 2, v37\n"
      "s_lshl_b32 s50, s55, 9\n"
      "v_lshl_add_u32 v36, v37, 3, s50\n"
      "s_lshl_b32 s51, s55, 4\n"
      "v_lshrrev_b32 v40, 5, v43\n"
      "v_lshl_add_u32 v40, v40, 2, s51\n"
      "s_lshl_b32 s51, s55, 2\n"
      "s_add_u32 s51, s51, 140800\n"
      "v_mov_b32 v41, s51\n"
      "v_mov_b32 v42, 0x22600\n"
      "v_lshl_add_u32 v37, v43, 3, s50\n"
      "v_add_u32 v35, 0x22700, v37\n"
      "s_lshl_b32 s50, s55, 11\n"
      "s_add_u32 s50, s50, 141056\n"
      "v_lshl_add_u32 v34, v39, 9, v38\n"
      "v_add_u32 v34, s50, v34\n"
      "s_lshl_b32 s45, s35, 11\n"
      "s_add_u32 s45, s45, 81920\n"
      "s_mov_b32 s56, 0x3f80\n"
      "s_mov_b32 s57, 0x3f800000\n"
      "s_movk_i32 s61, 0x7ff\n"
      "s_movk_i32 s58, 8192\n"
      "s_mov_b32 s54, 0\n"
      "s_mov_b64 s[66:67], 0\n"
      "s_waitcnt vmcnt(7)\n"
      "s_nop 0\n"
      "s_mov_b32 s60, 458752\n"
      "buffer_load_dwordx4 v[96:99], v32, s[24:27], s60 offen nt\n"
      "s_waitcnt vmcnt(7)\n"
      "s_nop 0\n"
      "s_mov_b32 s60, 465920\n"
      "buffer_load_dwordx4 v[100:103], v32, s[24:27], s60 offen nt\n"
      "v_perm_b32 v192, v68, v64, s62\n"
      "v_perm_b32 v196, v68, v64, s63\n"
      "v_perm_b32 v200, v69, v65, s62\n"
      "v_perm_b32 v204, v69, v65, s63\n"
      "v_perm_b32 v208, v70, v66, s62\n"
      "v_perm_b32 v212, v70, v66, s63\n"
      "v_perm_b32 v216, v71, v67, s62\n"
      "v_perm_b32 v220, v71, v67, s63\n"
      "s_waitcnt vmcnt(7)\n"
      "s_nop 0\n"
      "s_mov_b32 s60, 473088\n"
      "buffer_load_dwordx4 v[104:107], v32, s[24:27], s60 offen nt\n"
      "s_waitcnt vmcnt(7)\n"
      "s_nop 0\n"
      "s_mov_b32 s60, 480256\n"
      "buffer_load_dwordx4 v[108:111], v32, s[24:27], s60 offen nt\n"
      "v_perm_b32 v193, v76, v72, s62\n"
      "v_perm_b32 v197, v76, v72, s63\n"
      "v_perm_b32 v201, v77, v73, s62\n"
      "v_perm_b32 v205, v77, v73, s63\n"
      "v_perm_b32 v209, v78, v74, s62\n"
      "v_perm_b32 v213, v78, v74, s63\n"
      "v_perm_b32 v217, v79, v75, s62\n"
      "v_perm_b32 v221, v79, v75, s63\n"
      "s_waitcnt vmcnt(7)\n"
      "s_nop 0\n"
      "s_mov_b32 s60, 487424\n"
      "buffer_load_dwordx4 v[112:115], v32, s[24:27], s60 offen nt\n"
      "s_waitcnt vmcnt(7)\n"
      "s_nop 0\n"
      "s_mov_b32 s60, 494592\n"
      "buffer_load_dwordx4 v[116:119], v32, s[24:27], s60 offen nt\n"
      "v_perm_b32 v194, v84, v80, s62\n"
      "v_perm_b32 v198, v84, v80, s63\n"
      "v_perm_b32 v202, v85, v81, s62\n"
      "v_perm_b32 v206, v85, v81, s63\n"
      "v_perm_b32 v210, v86, v82, s62\n"
      "v_perm_b32 v214, v86, v82, s63\n"
      "v_perm_b32 v218, v87, v83, s62\n"
      "v_perm_b32 v222, v87, v83, s63\n"
      "s_waitcnt vmcnt(7)\n"
      "s_nop 0\n"
      "s_mov_b32 s60, 501760\n"
      "buffer_load_dwordx4 v[120:123], v32, s[24:27], s60 offen nt\n"
      "s_waitcnt vmcnt(7)\n"
      "s_nop 0\n"
      "s_mov_b32 s60, 508928\n"
      "buffer_load_dwordx4 v[124:127], v32, s[24:27], s60 offen nt\n"
      "v_perm_b32 v195, v92, v88, s62\n"
      "v_perm_b32 v199, v92, v88, s63\n"
      "v_perm_b32 v203, v93, v89, s62\n"
      "v_perm_b32 v207, v93, v89, s63\n"
      "v_perm_b32 v211, v94, v90, s62\n"
      "v_perm_b32 v215, v94, v90, s63\n"
      "v_perm_b32 v219, v95, v91, s62\n"
      "v_perm_b32 v223, v95, v91, s63\n"
      "s_waitcnt vmcnt(7)\n"
      "s_nop 0\n"
      "s_mov_b32 s60, 917504\n"
      "buffer_load_dwordx4 v[128:131], v32, s[24:27], s60 offen nt\n"
      "s_waitcnt vmcnt(7)\n"
      "s_nop 0\n"
      "s_mov_b32 s60, 924672\n"
      "buffer_load_dwordx4 v[132:135], v32, s[24:27], s60 offen nt\n"
      "v_perm_b32 v64, v100, v96, s62\n"
      "v_perm_b32 v68, v100, v96, s63\n"
      "v_perm_b32 v72, v101, v97, s62\n"
      "v_perm_b32 v76, v101, v97, s63\n"
      "v_perm_b32 v80, v102, v98, s62\n"
      "v_perm_b32 v84, v102, v98, s63\n"
      "v_perm_b32 v88, v103, v99, s62\n"
      "v_perm_b32 v92, v103, v99, s63\n"
      "s_waitcnt vmcnt(7)\n"
      "s_nop 0\n"
      "s_mov_b32 s60, 931840\n"
      "buffer_load_dwordx4 v[136:139], v32, s[24:27], s60 offen nt\n"
      "s_waitcnt vmcnt(7)\n"
      "s_nop 0\n"
      "s_mov_b32 s60, 939008\n"
      "buffer_load_dwordx4 v[140:143], v32, s[24:27], s60 offen nt\n"
      "v_perm_b32 v65, v108, v104, s62\n"
      "v_perm_b32 v69, v108, v104, s63\n"
      "v_perm_b32 v73, v109, v105, s62\n"
      "v_perm_b32 v77, v109, v105, s63\n"
      "v_perm_b32 v81, v110, v106, s62\n"
      "v_perm_b32 v85, v110, v106, s63\n"
      "v_perm_b32 v89, v111, v107, s62\n"
      "v_perm_b32 v93, v111, v107, s63\n"
      "s_waitcnt vmcnt(7)\n"
      "s_nop 0\n"
      "s_mov_b32 s60, 946176\n"
      "buffer_load_dwordx4 v[144:147], v32, s[24:27], s60 offen nt\n"
      "s_waitcnt vmcnt(7)\n"
      "s_nop 0\n"
      "s_mov_b32 s60, 953344\n"
      "buffer_load_dwordx4 v[148:151], v32, s[24:27], s60 offen nt\n"
      "v_perm_b32 v66, v116, v112, s62\n"
      "v_perm_b32 v70, v116, v112, s63\n"
      "v_perm_b32 v74, v117, v113, s62\n"
      "v_perm_b32 v78, v117, v113, s63\n"
      "v_perm_b32 v82, v118, v114, s62\n"
      "v_perm_b32 v86, v118, v114, s63\n"
      "v_perm_b32 v90, v119, v115, s62\n"
      "v_perm_b32 v94, v119, v115, s63\n"
      "s_waitcnt vmcnt(7)\n"
      "s_nop 0\n"
      "s_mov_b32 s60, 960512\n"
      "buffer_load_dwordx4 v[152:155], v32, s[24:27], s60 offen nt\n"
      "s_waitcnt vmcnt(7)\n"
      "s_nop 0\n"
      "s_mov_b32 s60, 967680\n"
      "buffer_load_dwordx4 v[156:159], v32, s[24:27], s60 offen nt\n"
      "v_perm_b32 v67, v124, v120, s62\n"
      "v_perm_b32 v71, v124, v120, s63\n"
      "v_perm_b32 v75, v125, v121, s62\n"
      "v_perm_b32 v79, v125, v121, s63\n"
      "v_perm_b32 v83, v126, v122, s62\n"
      "v_perm_b32 v87, v126, v122, s63\n"
      "v_perm_b32 v91, v127, v123, s62\n"
      "v_perm_b32 v95, v127, v123, s63\n"
      "s_waitcnt vmcnt(7)\n"
      "s_nop 0\n"
      "s_mov_b32 s60, 1376256\n"
      "buffer_load_dwordx4 v[160:163], v32, s[24:27], s60 offen nt\n"
      "s_waitcnt vmcnt(7)\n"
      "s_nop 0\n"
      "s_mov_b32 s60, 1383424\n"
      "buffer_load_dwordx4 v[164:167], v32, s[24:27], s60 offen nt\n"
      "v_perm_b32 v96, v132, v128, s62\n"
      "v_perm_b32 v100, v132, v128, s63\n"
      "v_perm_b32 v104, v133, v129, s62\n"
      "v_perm_b32 v108, v133, v129, s63\n"
      "v_perm_b32 v112, v134, v130, s62\n"
      "v_perm_b32 v116, v134, v130, s63\n"
      "v_perm_b32 v120, v135, v131, s62\n"
      "v_perm_b32 v124, v135, v131, s63\n"
      "s_waitcnt vmcnt(7)\n"
      "s_nop 0\n"
      "s_mov_b32 s60, 1390592\n"
      "buffer_load_dwordx4 v[168:171], v32, s[24:27], s60 offen nt\n"
      "s_waitcnt vmcnt(7)\n"
      "s_nop 0\n"
      "s_mov_b32 s60, 1397760\n"
      "buffer_load_dwordx4 v[172:175], v32, s[24:27], s60 offen nt\n"
      "v_perm_b32 v97, v140, v136, s62\n"
      "v_perm_b32 v101, v140, v136, s63\n"
      "v_perm_b32 v105, v141, v137, s62\n"
      "v_perm_b32 v109, v141, v137, s63\n"
      "v_perm_b32 v113, v142, v138, s62\n"
      "v_perm_b32 v117, v142, v138, s63\n"
      "v_perm_b32 v121, v143, v139, s62\n"
      "v_perm_b32 v125, v143, v139, s63\n"
      "s_waitcnt vmcnt(7)\n"
      "s_nop 0\n"
      "s_mov_b32 s60, 1404928\n"
      "buffer_load_dwordx4 v[176:179], v32, s[24:27], s60 offen nt\n"
      "s_waitcnt vmcnt(7)\n"
      "s_nop 0\n"
      "s_mov_b32 s60, 1412096\n"
      "buffer_load_dwordx4 v[180:183], v32, s[24:27], s60 offen nt\n"
      "v_perm_b32 v98, v148, v144, s62\n"
      "v_perm_b32 v102, v148, v144, s63\n"
      "v_perm_b32 v106, v149, v145, s62\n"
      "v_perm_b32 v110, v149, v145, s63\n"
      "v_perm_b32 v114, v150, v146, s62\n"
      "v_perm_b32 v118, v150, v146, s63\n"
      "v_perm_b32 v122, v151, v147, s62\n"
      "v_perm_b32 v126, v151, v147, s63\n"
      "s_waitcnt vmcnt(7)\n"
      "s_nop 0\n"
      "s_mov_b32 s60, 1419264\n"
      "buffer_load_dwordx4 v[184:187], v32, s[24:27], s60 offen nt\n"
      "s_waitcnt vmcnt(7)\n"
      "s_nop 0\n"
      "s_mov_b32 s60, 1426432\n"
      "buffer_load_dwordx4 v[188:191], v32, s[24:27], s60 offen nt\n"
      "ds_write_b32 v41, v47 offset:80\n"
      "v_perm_b32 v99, v156, v152, s62\n"
      "v_perm_b32 v103, v156, v152, s63\n"
      "v_perm_b32 v107, v157, v153, s62\n"
      "v_perm_b32 v111, v157, v153, s63\n"
      "v_perm_b32 v115, v158, v154, s62\n"
      "v_perm_b32 v119, v158, v154, s63\n"
      "v_perm_b32 v123, v159, v155, s62\n"
      "v_perm_b32 v127, v159, v155, s63\n"
      "s_waitcnt vmcnt(7)\n"
      "s_nop 0\n"
      "s_waitcnt vmcnt(6)\n"
      "s_nop 0\n"
      "v_perm_b32 v128, v164, v160, s62\n"
      "v_perm_b32 v132, v164, v160, s63\n"
      "v_perm_b32 v136, v165, v161, s62\n"
      "v_perm_b32 v140, v165, v161, s63\n"
      "v_perm_b32 v144, v166, v162, s62\n"
      "v_perm_b32 v148, v166, v162, s63\n"
      "v_perm_b32 v152, v167, v163, s62\n"
      "v_perm_b32 v156, v167, v163, s63\n"
      "s_waitcnt vmcnt(5)\n"
      "s_nop 0\n"
      "s_waitcnt vmcnt(4)\n"
      "s_nop 0\n"
      "buffer_load_dwordx2 v[8:9], v33, s[16:19], s43 offen sc1\n"
      "v_perm_b32 v129, v172, v168, s62\n"
      "v_perm_b32 v133, v172, v168, s63\n"
      "v_perm_b32 v137, v173, v169, s62\n"
      "v_perm_b32 v141, v173, v169, s63\n"
      "v_perm_b32 v145, v174, v170, s62\n"
      "v_perm_b32 v149, v174, v170, s63\n"
      "v_perm_b32 v153, v175, v171, s62\n"
      "v_perm_b32 v157, v175, v171, s63\n"
      "s_waitcnt vmcnt(3)\n"
      "s_nop 0\n"
      "s_waitcnt vmcnt(2)\n"
      "s_nop 0\n"
      "v_perm_b32 v130, v180, v176, s62\n"
      "v_perm_b32 v134, v180, v176, s63\n"
      "v_perm_b32 v138, v181, v177, s62\n"
      "v_perm_b32 v142, v181, v177, s63\n"
      "v_perm_b32 v146, v182, v178, s62\n"
      "v_perm_b32 v150, v182, v178, s63\n"
      "v_perm_b32 v154, v183, v179, s62\n"
      "v_perm_b32 v158, v183, v179, s63\n"
      "s_waitcnt vmcnt(1)\n"
      "s_nop 0\n"
      "s_waitcnt vmcnt(0)\n"
      "s_nop 0\n"
      "v_perm_b32 v131, v188, v184, s62\n"
      "v_perm_b32 v135, v188, v184, s63\n"
      "v_perm_b32 v139, v189, v185, s62\n"
      "v_perm_b32 v143, v189, v185, s63\n"
      "v_perm_b32 v147, v190, v186, s62\n"
      "v_perm_b32 v151, v190, v186, s63\n"
      "v_perm_b32 v155, v191, v187, s62\n"
      "v_perm_b32 v159, v191, v187, s63\n"
      "s_cmp_lg_u32 s54, 0\n"
      "s_cbranch_scc1 .Lghx%=\n"
      "s_movk_i32 s59, 0x3fff\n"
      "buffer_load_dwordx2 v[12:13], v33, s[16:19], s43 offen sc1\n"
      ".Lgh%=:\n"
      "s_waitcnt vmcnt(1)\n"
      "v_cmp_ne_u32 vcc, s15, v9\n"
      "v_bitop3_b32 v10, v8, s61, v8 bitop3:0x0c\n"
      "s_nop 0\n"
      "s_cbranch_vccz .Lgho%=\n"
      "s_sleep 2\n"
      "buffer_load_dwordx2 v[8:9], v33, s[16:19], s43 offen sc1\n"
      "s_waitcnt vmcnt(1)\n"
      "v_cmp_ne_u32 vcc, s15, v13\n"
      "v_bitop3_b32 v10, v12, s61, v12 bitop3:0x0c\n"
      "s_nop 0\n"
      "s_cbranch_vccz .Lgho%=\n"
      "s_sleep 2\n"
      "buffer_load_dwordx2 v[12:13], v33, s[16:19], s43 offen sc1\n"
      "s_sub_u32 s59, s59, 1\n"
      "s_cbranch_scc0 .Lgh%=\n"
      ".Lgho%=:\n"
      "v_lshl_add_u32 v11, v10, 11, v36\n"
      "v_lshlrev_b32 v45, 3, v10\n"
      "buffer_load_dwordx2 v[62:63], v45, s[16:19], s58 offen sc1\n"
      "buffer_load_dwordx4 v[16:19], v11, s[20:23], 0 offen offset:0\n"
      "buffer_load_dwordx4 v[20:23], v11, s[20:23], 0 offen offset:128\n"
      "buffer_load_dwordx4 v[24:27], v11, s[20:23], 0 offen offset:256\n"
      "buffer_load_dwordx4 v[28:31], v11, s[20:23], 0 offen offset:384\n"
      "ds_write_b32 v41, v47 offset:64\n"
      ".Lghx%=:\n"
      "s_movk_i32 s59, 0x7fff\n"
      "s_waitcnt vmcnt(4)\n"
      "s_nop 0\n"
      ".Lgs%=:\n"
      "v_cmp_ne_u32 vcc, s15, v63\n"
      "v_lshlrev_b32 v10, 16, v62\n"
      "v_and_b32 v44, 0xffff0000, v62\n"
      "s_cbranch_vccnz .Lgss%=\n"
      ".Lgso%=:\n"
      "v_rcp_f32 v44, v44\n"
      "v_xor_b32 v10, 0x80000000, v10\n"
      "s_waitcnt vmcnt(3)\n"
      "v_dot2_f32_bf16 v168, v16, s56, v10\n"
      "v_dot2_f32_bf16 v169, v16, s57, v10\n"
      "v_dot2_f32_bf16 v170, v17, s56, v10\n"
      "v_dot2_f32_bf16 v171, v17, s57, v10\n"
      "v_dot2_f32_bf16 v172, v18, s56, v10\n"
      "v_dot2_f32_bf16 v173, v18, s57, v10\n"
      "v_dot2_f32_bf16 v174, v19, s56, v10\n"
      "v_dot2_f32_bf16 v175, v19, s57, v10\n"
      "v_cvt_pk_bf16_f32 v16, v168, v169\n"
      "v_cvt_pk_bf16_f32 v17, v170, v171\n"
      "v_cvt_pk_bf16_f32 v18, v172, v173\n"
      "v_cvt_pk_bf16_f32 v19, v174, v175\n"
      "ds_bpermute_b32 v44, v40, v44\n"
      "s_waitcnt vmcnt(2)\n"
      "v_dot2_f32_bf16 v176, v20, s56, v10\n"
      "v_dot2_f32_bf16 v177, v20, s57, v10\n"
      "v_dot2_f32_bf16 v178, v21, s56, v10\n"
      "v_dot2_f32_bf16 v179, v21, s57, v10\n"
      "v_dot2_f32_bf16 v180, v22, s56, v10\n"
      "v_dot2_f32_bf16 v181, v22, s57, v10\n"
      "v_dot2_f32_bf16 v182, v23, s56, v10\n"
      "v_dot2_f32_bf16 v183, v23, s57, v10\n"
      "v_cvt_pk_bf16_f32 v20, v176, v177\n"
      "v_cvt_pk_bf16_f32 v21, v178, v179\n"
      "v_cvt_pk_bf16_f32 v22, v180, v181\n"
      "v_cvt_pk_bf16_f32 v23, v182, v183\n"
      "s_waitcnt vmcnt(1)\n"
      "v_dot2_f32_bf16 v168, v24, s56, v10\n"
      "v_dot2_f32_bf16 v169, v24, s57, v10\n"
      "v_dot2_f32_bf16 v170, v25, s56, v10\n"
      "v_dot2_f32_bf16 v171, v25, s57, v10\n"
      "v_dot2_f32_bf16 v172, v26, s56, v10\n"
      "v_dot2_f32_bf16 v173, v26, s57, v10\n"
      "v_dot2_f32_bf16 v174, v27, s56, v10\n"
      "v_dot2_f32_bf16 v175, v27, s57, v10\n"
      "v_cvt_pk_bf16_f32 v24, v168, v169\n"
      "v_cvt_pk_bf16_f32 v25, v170, v171\n"
      "v_cvt_pk_bf16_f32 v26, v172, v173\n"
      "v_cvt_pk_bf16_f32 v27, v174, v175\n"
      "s_waitcnt vmcnt(0)\n"
      "v_dot2_f32_bf16 v176, v28, s56, v10\n"
      "v_dot2_f32_bf16 v177, v28, s57, v10\n"
      "v_dot2_f32_bf16 v178, v29, s56, v10\n"
      "v_dot2_f32_bf16 v179, v29, s57, v10\n"
      "v_dot2_f32_bf16 v180, v30, s56, v10\n"
      "v_dot2_f32_bf16 v181, v30, s57, v10\n"
      "v_dot2_f32_bf16 v182, v31, s56, v10\n"
      "v_dot2_f32_bf16 v183, v31, s57, v10\n"
      "v_cvt_pk_bf16_f32 v28, v176, v177\n"
      "v_cvt_pk_bf16_f32 v29, v178, v179\n"
      "v_cvt_pk_bf16_f32 v30, v180, v181\n"
      "v_cvt_pk_bf16_f32 v31, v182, v183\n"
      "v_mfma_f32_16x16x32_bf16 v[224:227], v[16:19], v[192:195], 0\n"
      "v_mfma_f32_16x16x32_bf16 v[228:231], v[16:19], v[196:199], 0\n"
      "v_mfma_f32_16x16x32_bf16 v[232:235], v[16:19], v[200:203], 0\n"
      "v_mfma_f32_16x16x32_bf16 v[236:239], v[16:19], v[204:207], 0\n"
      "v_mfma_f32_16x16x32_bf16 v[240:243], v[16:19], v[208:211], 0\n"
      "v_mfma_f32_16x16x32_bf16 v[244:247], v[16:19], v[212:215], 0\n"
      "v_mfma_f32_16x16x32_bf16 v[248:251], v[16:19], v[216:219], 0\n"
      "v_mfma_f32_16x16x32_bf16 v[252:255], v[16:19], v[220:223], 0\n"
      "v_mfma_f32_16x16x32_bf16 v[224:227], v[20:23], v[64:67], v[224:227]\n"
      "v_mfma_f32_16x16x32_bf16 v[228:231], v[20:23], v[68:71], v[228:231]\n"
      "v_mfma_f32_16x16x32_bf16 v[232:235], v[20:23], v[72:75], v[232:235]\n"
      "v_mfma_f32_16x16x32_bf16 v[236:239], v[20:23], v[76:79], v[236:239]\n"
      "v_mfma_f32_16x16x32_bf16 v[240:243], v[20:23], v[80:83], v[240:243]\n"
      "v_mfma_f32_16x16x32_bf16 v[244:247], v[20:23], v[84:87], v[244:247]\n"
      "v_mfma_f32_16x16x32_bf16 v[248:251], v[20:23], v[88:91], v[248:251]\n"
      "v_mfma_f32_16x16x32_bf16 v[252:255], v[20:23], v[92:95], v[252:255]\n"
      "v_mfma_f32_16x16x32_bf16 v[224:227], v[24:27], v[96:99], v[224:227]\n"
      "v_mfma_f32_16x16x32_bf16 v[228:231], v[24:27], v[100:103], v[228:231]\n"
      "v_mfma_f32_16x16x32_bf16 v[232:235], v[24:27], v[104:107], v[232:235]\n"
      "v_mfma_f32_16x16x32_bf16 v[236:239], v[24:27], v[108:111], v[236:239]\n"
      "v_mfma_f32_16x16x32_bf16 v[240:243], v[24:27], v[112:115], v[240:243]\n"
      "v_mfma_f32_16x16x32_bf16 v[244:247], v[24:27], v[116:119], v[244:247]\n"
      "v_mfma_f32_16x16x32_bf16 v[248:251], v[24:27], v[120:123], v[248:251]\n"
      "v_mfma_f32_16x16x32_bf16 v[252:255], v[24:27], v[124:127], v[252:255]\n"
      "v_mfma_f32_16x16x32_bf16 v[224:227], v[28:31], v[128:131], v[224:227]\n"
      "v_mfma_f32_16x16x32_bf16 v[228:231], v[28:31], v[132:135], v[228:231]\n"
      "v_mfma_f32_16x16x32_bf16 v[232:235], v[28:31], v[136:139], v[232:235]\n"
      "v_mfma_f32_16x16x32_bf16 v[236:239], v[28:31], v[140:143], v[236:239]\n"
      "s_nop 4\n"
      "v_add_f32_dpp v160, v226, v224 row_ror:8 row_mask:0xf bank_mask:0x3\n"
      "v_add_f32_dpp v164, v227, v225 row_ror:8 row_mask:0xf bank_mask:0x3\n"
      "v_mfma_f32_16x16x32_bf16 v[240:243], v[28:31], v[144:147], v[240:243]\n"
      "v_add_f32_dpp v161, v230, v228 row_ror:8 row_mask:0xf bank_mask:0x3\n"
      "v_add_f32_dpp v165, v231, v229 row_ror:8 row_mask:0xf bank_mask:0x3\n"
      "v_mfma_f32_16x16x32_bf16 v[244:247], v[28:31], v[148:151], v[244:247]\n"
      "v_add_f32_dpp v162, v234, v232 row_ror:8 row_mask:0xf bank_mask:0x3\n"
      "v_add_f32_dpp v166, v235, v233 row_ror:8 row_mask:0xf bank_mask:0x3\n"
      "v_mfma_f32_16x16x32_bf16 v[248:251], v[28:31], v[152:155], v[248:251]\n"
      "v_add_f32_dpp v163, v238, v236 row_ror:8 row_mask:0xf bank_mask:0x3\n"
      "v_add_f32_dpp v167, v239, v237 row_ror:8 row_mask:0xf bank_mask:0x3\n"
      "v_mfma_f32_16x16x32_bf16 v[252:255], v[28:31], v[156:159], v[252:255]\n"
      "v_add_f32_dpp v160, v240, v242 row_ror:8 row_mask:0xf bank_mask:0xc\n"
      "v_add_f32_dpp v164, v241, v243 row_ror:8 row_mask:0xf bank_mask:0xc\n"
      "v_add_f32_dpp v161, v244, v246 row_ror:8 row_mask:0xf bank_mask:0xc\n"
      "v_add_f32_dpp v165, v245, v247 row_ror:8 row_mask:0xf bank_mask:0xc\n"
      "s_nop 0\n"
      "v_add_f32_dpp v162, v248, v250 row_ror:8 row_mask:0xf bank_mask:0xc\n"
      "v_add_f32_dpp v166, v249, v251 row_ror:8 row_mask:0xf bank_mask:0xc\n"
      "s_nop 0\n"
      "v_add_f32_dpp v163, v252, v254 row_ror:8 row_mask:0xf bank_mask:0xc\n"
      "v_add_f32_dpp v167, v253, v255 row_ror:8 row_mask:0xf bank_mask:0xc\n"
      "ds_write_b128 v34, v[160:163] \n"
      "ds_write_b128 v34, v[164:167] offset:256\n"
      "ds_write_b32 v41, v47 \n"
      "s_movk_i32 s59, 0x7fff\n"
      ".Lgw%=:\n"
      "ds_read_b128 v[72:75], v42 \n"
      "ds_read_b64 v[64:65], v35 offset:0\n"
      "ds_read_b64 v[66:67], v35 offset:2048\n"
      "ds_read_b64 v[68:69], v35 offset:4096\n"
      "ds_read_b64 v[70:71], v35 offset:6144\n"
      "s_sub_u32 s59, s59, 1\n"
      "s_cbranch_scc1 .Lgwo%=\n"
      "s_waitcnt lgkmcnt(4)\n"
      "v_xor_b32 v72, s15, v72\n"
      "v_bitop3_b32 v72, s15, v73, v72 bitop3:0xbe\n"
      "v_bitop3_b32 v72, s15, v74, v72 bitop3:0xbe\n"
      "v_bitop3_b32 v72, s15, v75, v72 bitop3:0xbe\n"
      "v_cmp_ne_u32 vcc, 0, v72\n"
      "s_waitcnt lgkmcnt(0)\n"
      "s_nop 0\n"
      "s_cbranch_vccnz .Lgw%=\n"
      ".Lgwo%=:\n"
      "s_waitcnt lgkmcnt(0)\n"
      "s_bitcmp1_b32 s49, 0\n"
      "v_pk_add_f32 v[64:65], v[64:65], v[66:67]\n"
      "v_pk_add_f32 v[68:69], v[68:69], v[70:71]\n"
      "v_pk_add_f32 v[64:65], v[64:65], v[68:69]\n"
      "v_mul_f32 v64, v64, v44\n"
      "v_mul_f32 v65, v65, v44\n"
      "v_cvt_pk_bf16_f32 v46, v64, v65\n"
      "s_cbranch_scc1 .Lgu%=\n"
      "v_lshlrev_b32 v64, 16, v46\n"
      "v_and_b32 v65, 0xffff0000, v46\n"
      "v_mul_f32 v66, 0xbfb8aa3b, v64\n"
      "v_mul_f32 v67, 0xbfb8aa3b, v65\n"
      "v_exp_f32 v66, v66\n"
      "v_exp_f32 v67, v67\n"
      "v_add_f32 v66, 1.0, v66\n"
      "v_add_f32 v67, 1.0, v67\n"
      "v_rcp_f32 v66, v66\n"
      "v_rcp_f32 v67, v67\n"
      "v_mul_f32 v64, v64, v66\n"
      "v_mul_f32 v65, v65, v67\n"
      "v_cvt_pk_bf16_f32 v46, v64, v65\n"
      ".Lgu%=:\n"
      "buffer_store_dwordx2 v[46:47], v37, s[16:19], s45 offen sc1\n"
      "s_cmp_lg_u32 s33, 4\n"
      "s_cbranch_scc1 .Lga%=\n"
      "v_mov_b32 v64, s15\n"
      "v_mov_b32 v65, s15\n"
      "buffer_store_dwordx2 v[64:65], off, s[16:19], s42 sc1\n"
      ".Lga%=:\n"
      "s_cmp_eq_u32 s33, 7\n"
      "s_mov_b32 s24, s36\n"
      "s_mov_b32 s25, s37\n"
      "s_mov_b32 s26, 0x400000\n"
      "s_cbranch_scc1 .LZ%=\n"
      "s_endpgm\n"
      ".Lgss%=:\n"
      "s_sleep 1\n"
      "s_sub_u32 s59, s59, 1\n"
      "s_cbranch_scc1 .Lgso%=\n"
      "buffer_load_dwordx2 v[62:63], v45, s[16:19], s58 offen sc1\n"
      "s_waitcnt vmcnt(0)\n"
      "s_branch .Lgs%=\n"
      ".LD2%=:\n"
      "s_setprio 0\n"
      "s_mul_i32 s35, s48, 14\n"
      "s_add_u32 s35, s35, s49\n"
      "s_lshr_b32 s52, s35, 2\n"
      "s_and_b32 s54, s35, 3\n"
      "v_lshlrev_b32 v42, 4, v49\n"
      "s_lshl_b32 s50, s52, 18\n"
      "s_lshl_b32 s51, s54, 9\n"
      "s_add_u32 s50, s50, s51\n"
      "s_lshl_b32 s51, s33, 7\n"
      "s_add_u32 s50, s50, s51\n"
      "v_lshl_add_u32 v32, v50, 14, v42\n"
      "v_add_u32 v32, s50, v32\n"
      "v_lshrrev_b32 v41, 1, v49\n"
      "v_and_b32 v43, 1, v41\n"
      "v_mul_u32_u24 v36, 0x120, v41\n"
      "v_lshlrev_b32 v43, 4, v43\n"
      "v_sub_u32 v36, v36, v43\n"
      "v_and_b32 v43, 1, v49\n"
      "v_lshl_add_u32 v36, v43, 7, v36\n"
      "v_lshrrev_b32 v41, 4, v48\n"
      "v_lshl_add_u32 v36, v41, 4, v36\n"
      "v_and_b32 v43, 8, v48\n"
      "v_add_u32 v36, v36, v43\n"
      "s_lshr_b32 s53, s33, 1\n"
      "s_mul_i32 s50, s53, 1152\n"
      "s_and_b32 s55, s33, 1\n"
      "s_lshl_b32 s51, s55, 6\n"
      "s_add_u32 s50, s50, s51\n"
      "s_add_u32 s50, s50, 149504\n"
      "v_add_u32 v36, s50, v36\n"
      "v_and_b32 v43, 1, v41\n"
      "v_mul_u32_u24 v37, 0x120, v41\n"
      "v_lshlrev_b32 v43, 4, v43\n"
      "v_sub_u32 v37, v37, v43\n"
      "v_and_b32 v43, 15, v48\n"
      "v_lshl_add_u32 v37, v43, 4, v37\n"
      "v_add_u32 v37, 0x24800, v37\n"
      "v_lshlrev_b32 v35, 4, v48\n"
      "s_lshl_b32 s46, s52, 13\n"
      "s_lshl_b32 s50, s53, 12\n"
      "s_add_u32 s46, s46, s50\n"
      "s_lshl_b32 s50, s55, 10\n"
      "s_add_u32 s46, s46, s50\n"
      "s_add_u32 s46, s46, 81920\n"
      "v_and_b32 v41, 3, v50\n"
      "v_lshrrev_b32 v43, 5, v48\n"
      "v_lshl_add_u32 v41, v41, 1, v43\n"
      "v_mul_u32_u24 v38, 0x1c00, v41\n"
      "v_add_u32 v38, v38, v42\n"
      "s_lshl_b32 s50, s54, 2\n"
      "s_add_u32 s50, s50, s33\n"
      "s_mul_i32 s47, s50, 57344\n"
      "s_lshl_b32 s50, s52, 8\n"
      "s_add_u32 s47, s47, s50\n"
      "s_add_u32 s47, s47, 311296\n"
      "s_mov_b32 s62, 0x05040100\n"
      "s_mov_b32 s63, 0x07060302\n"
      "s_lshl_b32 s50, s33, 2\n"
      "s_add_u32 s50, s50, 140864\n"
      "v_mov_b32 v41, s50\n"
      "v_mov_b32 v42, 0x22640\n"
      "s_movk_i32 s59, 0x3fff\n"
      ".Ldf%=:\n"
      "s_sleep 1\n"
      "ds_read_b128 v[240:243], v42\n"
      "ds_read_b32 v43, v41 offset:16\n"
      "s_sub_u32 s59, s59, 1\n"
      "s_cbranch_scc1 .Ldfo%=\n"
      "s_waitcnt lgkmcnt(0)\n"
      "v_xor_b32 v240, s15, v240\n"
      "v_bitop3_b32 v240, s15, v241, v240 bitop3:0xbe\n"
      "v_bitop3_b32 v240, s15, v242, v240 bitop3:0xbe\n"
      "v_bitop3_b32 v240, s15, v243, v240 bitop3:0xbe\n"
      "v_bitop3_b32 v240, s15, v43, v240 bitop3:0xbe\n"
      "v_cmp_ne_u32 vcc, 0, v240\n"
      "s_nop 1\n"
      "s_cbranch_vccnz .Ldf%=\n"
      ".Ldfo%=:\n"
      "s_mov_b32 s60, 0\n"
      "buffer_load_dwordx4 v[128:131], v32, s[28:31], s60 offen\n"
      "s_mov_b32 s60, 2048\n"
      "buffer_load_dwordx4 v[132:135], v32, s[28:31], s60 offen\n"
      "s_mov_b32 s60, 4096\n"
      "buffer_load_dwordx4 v[136:139], v32, s[28:31], s60 offen\n"
      "s_mov_b32 s60, 6144\n"
      "buffer_load_dwordx4 v[140:143], v32, s[28:31], s60 offen\n"
      "s_mov_b32 s60, 8192\n"
      "buffer_load_dwordx4 v[144:147], v32, s[28:31], s60 offen\n"
      "s_mov_b32 s60, 10240\n"
      "buffer_load_dwordx4 v[148:151], v32, s[28:31], s60 offen\n"
      "s_mov_b32 s60, 12288\n"
      "buffer_load_dwordx4 v[152:155], v32, s[28:31], s60 offen\n"
      "s_mov_b32 s60, 14336\n"
      "buffer_load_dwordx4 v[156:159], v32, s[28:31], s60 offen\n"
      "s_waitcnt vmcnt(7)\n"
      "s_nop 0\n"
      "s_mov_b32 s60, 131072\n"
      "buffer_load_dwordx4 v[160:163], v32, s[28:31], s60 offen\n"
      "s_waitcnt vmcnt(7)\n"
      "s_nop 0\n"
      "s_mov_b32 s60, 133120\n"
      "buffer_load_dwordx4 v[164:167], v32, s[28:31], s60 offen\n"
      "v_perm_b32 v64, v132, v128, s62\n"
      "v_perm_b32 v68, v132, v128, s63\n"
      "v_perm_b32 v72, v133, v129, s62\n"
      "v_perm_b32 v76, v133, v129, s63\n"
      "v_perm_b32 v80, v134, v130, s62\n"
      "v_perm_b32 v84, v134, v130, s63\n"
      "v_perm_b32 v88, v135, v131, s62\n"
      "v_perm_b32 v92, v135, v131, s63\n"
      "s_waitcnt vmcnt(7)\n"
      "s_nop 0\n"
      "s_mov_b32 s60, 135168\n"
      "buffer_load_dwordx4 v[168:171], v32, s[28:31], s60 offen\n"
      "s_waitcnt vmcnt(7)\n"
      "s_nop 0\n"
      "s_mov_b32 s60, 137216\n"
      "buffer_load_dwordx4 v[172:175], v32, s[28:31], s60 offen\n"
      "v_perm_b32 v65, v140, v136, s62\n"
      "v_perm_b32 v69, v140, v136, s63\n"
      "v_perm_b32 v73, v141, v137, s62\n"
      "v_perm_b32 v77, v141, v137, s63\n"
      "v_perm_b32 v81, v142, v138, s62\n"
      "v_perm_b32 v85, v142, v138, s63\n"
      "v_perm_b32 v89, v143, v139, s62\n"
      "v_perm_b32 v93, v143, v139, s63\n"
      "s_waitcnt vmcnt(7)\n"
      "s_nop 0\n"
      "s_mov_b32 s60, 139264\n"
      "buffer_load_dwordx4 v[176:179], v32, s[28:31], s60 offen\n"
      "s_waitcnt vmcnt(7)\n"
      "s_nop 0\n"
      "s_mov_b32 s60, 141312\n"
      "buffer_load_dwordx4 v[180:183], v32, s[28:31], s60 offen\n"
      "v_perm_b32 v66, v148, v144, s62\n"
      "v_perm_b32 v70, v148, v144, s63\n"
      "v_perm_b32 v74, v149, v145, s62\n"
      "v_perm_b32 v78, v149, v145, s63\n"
      "v_perm_b32 v82, v150, v146, s62\n"
      "v_perm_b32 v86, v150, v146, s63\n"
      "v_perm_b32 v90, v151, v147, s62\n"
      "v_perm_b32 v94, v151, v147, s63\n"
      "s_waitcnt vmcnt(7)\n"
      "s_nop 0\n"
      "s_mov_b32 s60, 143360\n"
      "buffer_load_dwordx4 v[184:187], v32, s[28:31], s60 offen\n"
      "s_waitcnt vmcnt(7)\n"
      "s_nop 0\n"
      "s_mov_b32 s60, 145408\n"
      "buffer_load_dwordx4 v[188:191], v32, s[28:31], s60 offen\n"
      "v_perm_b32 v67, v156, v152, s62\n"
      "v_perm_b32 v71, v156, v152, s63\n"
      "v_perm_b32 v75, v157, v153, s62\n"
      "v_perm_b32 v79, v157, v153, s63\n"
      "v_perm_b32 v83, v158, v154, s62\n"
      "v_perm_b32 v87, v158, v154, s63\n"
      "v_perm_b32 v91, v159, v155, s62\n"
      "v_perm_b32 v95, v159, v155, s63\n"
      "s_waitcnt vmcnt(7)\n"
      "s_nop 0\n"
      "s_waitcnt vmcnt(6)\n"
      "s_nop 0\n"
      "v_perm_b32 v96, v164, v160, s62\n"
      "v_perm_b32 v100, v164, v160, s63\n"
      "v_perm_b32 v104, v165, v161, s62\n"
      "v_perm_b32 v108, v165, v161, s63\n"
      "v_perm_b32 v112, v166, v162, s62\n"
      "v_perm_b32 v116, v166, v162, s63\n"
      "v_perm_b32 v120, v167, v163, s62\n"
      "v_perm_b32 v124, v167, v163, s63\n"
      "s_waitcnt vmcnt(5)\n"
      "s_nop 0\n"
      "s_waitcnt vmcnt(4)\n"
      "s_nop 0\n"
      "v_perm_b32 v97, v172, v168, s62\n"
      "v_perm_b32 v101, v172, v168, s63\n"
      "v_perm_b32 v105, v173, v169, s62\n"
      "v_perm_b32 v109, v173, v169, s63\n"
      "v_perm_b32 v113, v174, v170, s62\n"
      "v_perm_b32 v117, v174, v170, s63\n"
      "v_perm_b32 v121, v175, v171, s62\n"
      "v_perm_b32 v125, v175, v171, s63\n"
      "s_waitcnt vmcnt(3)\n"
      "s_nop 0\n"
      "s_waitcnt vmcnt(2)\n"
      "s_nop 0\n"
      "v_perm_b32 v98, v180, v176, s62\n"
      "v_perm_b32 v102, v180, v176, s63\n"
      "v_perm_b32 v106, v181, v177, s62\n"
      "v_perm_b32 v110, v181, v177, s63\n"
      "v_perm_b32 v114, v182, v178, s62\n"
      "v_perm_b32 v118, v182, v178, s63\n"
      "v_perm_b32 v122, v183, v179, s62\n"
      "v_perm_b32 v126, v183, v179, s63\n"
      "s_waitcnt vmcnt(1)\n"
      "s_nop 0\n"
      "s_waitcnt vmcnt(0)\n"
      "s_nop 0\n"
      "v_perm_b32 v99, v188, v184, s62\n"
      "v_perm_b32 v103, v188, v184, s63\n"
      "v_perm_b32 v107, v189, v185, s62\n"
      "v_perm_b32 v111, v189, v185, s63\n"
      "v_perm_b32 v115, v190, v186, s62\n"
      "v_perm_b32 v119, v190, v186, s63\n"
      "v_perm_b32 v123, v191, v187, s62\n"
      "v_perm_b32 v127, v191, v187, s63\n"
      "s_movk_i32 s59, 0x3fff\n"
      "v_add_u32 v41, 0xffffffc0, v41\n"
      ".Lde%=:\n"
      "ds_read_b32 v43, v41\n"
      "s_sub_u32 s59, s59, 1\n"
      "s_cbranch_scc1 .Ldeo%=\n"
      "s_waitcnt lgkmcnt(0)\n"
      "v_cmp_ne_u32 vcc, s15, v43\n"
      "s_nop 1\n"
      "s_cbranch_vccz .Ldeo%=\n"
      "s_sleep 2\n"
      "s_branch .Lde%=\n"
      ".Ldeo%=:\n"
      "s_cmp_lg_u32 s59, 0x3ffe\n"
      "s_cbranch_scc0 .Ldep%=\n"
      "s_sleep 6\n"
      ".Ldep%=:\n"
      "buffer_load_dwordx4 v[224:227], v35, s[16:19], s46 offen sc1\n"
      "buffer_load_dwordx4 v[228:231], v35, s[16:19], s46 offen offset:2048 sc1\n"
      "v_mov_b32 v137, s15\n"
      "v_mov_b32 v139, s15\n"
      "v_mov_b32 v141, s15\n"
      "v_mov_b32 v143, s15\n"
      "s_movk_i32 s59, 0x7fff\n"
      "s_waitcnt vmcnt(0)\n"
      "s_nop 0\n"
      ".Ldg%=:\n"
      "v_xor_b32 v232, s15, v225\n"
      "v_bitop3_b32 v232, s15, v227, v232 bitop3:0xbe\n"
      "v_bitop3_b32 v232, s15, v229, v232 bitop3:0xbe\n"
      "v_bitop3_b32 v232, s15, v231, v232 bitop3:0xbe\n"
      "v_cmp_ne_u32 vcc, 0, v232\n"
      "v_lshlrev_b32 v232, 16, v224\n"
      "v_and_b32 v233, 0xffff0000, v224\n"
      "v_lshlrev_b32 v234, 16, v226\n"
      "v_and_b32 v235, 0xffff0000, v226\n"
      "s_cbranch_vccnz .Ldgs%=\n"
      ".Ldgo%=:\n"
      "v_lshlrev_b32 v236, 16, v228\n"
      "v_and_b32 v237, 0xffff0000, v228\n"
      "v_lshlrev_b32 v238, 16, v230\n"
      "v_and_b32 v239, 0xffff0000, v230\n"
      "v_pk_mul_f32 v[232:233], v[232:233], v[236:237]\n"
      "v_pk_mul_f32 v[234:235], v[234:235], v[238:239]\n"
      "v_cvt_pk_bf16_f32 v232, v232, v233\n"
      "v_cvt_pk_bf16_f32 v233, v234, v235\n"
      "ds_write_b64 v36, v[232:233] \n"
      "ds_write_b32 v41, v137 offset:96\n"
      "ds_read_b128 v[240:243], v42 offset:32\n"
      "ds_read_b128 v[224:227], v37 \n"
      "ds_read_b128 v[228:231], v37 offset:1152\n"
      "s_movk_i32 s59, 0x7fff\n"
      ".Ldv%=:\n"
      "s_waitcnt lgkmcnt(2)\n"
      "v_xor_b32 v240, s15, v240\n"
      "v_bitop3_b32 v240, s15, v241, v240 bitop3:0xbe\n"
      "v_bitop3_b32 v240, s15, v242, v240 bitop3:0xbe\n"
      "v_bitop3_b32 v240, s15, v243, v240 bitop3:0xbe\n"
      "v_cmp_ne_u32 vcc, 0, v240\n"
      "s_nop 1\n"
      "s_cbranch_vccnz .Ldvs%=\n"
      ".Ldvo%=:\n"
      "s_waitcnt lgkmcnt(1)\n"
      "v_mfma_f32_16x16x32_bf16 v[192:195], v[64:67], v[224:227], 0\n"
      "s_waitcnt lgkmcnt(0)\n"
      "v_mfma_f32_16x16x32_bf16 v[192:195], v[96:99], v[228:231], v[192:195]\n"
      "v_mfma_f32_16x16x32_bf16 v[196:199], v[68:71], v[224:227], 0\n"
      "v_mfma_f32_16x16x32_bf16 v[196:199], v[100:103], v[228:231], v[196:199]\n"
      "v_mfma_f32_16x16x32_bf16 v[200:203], v[72:75], v[224:227], 0\n"
      "s_nop 4\n"
      "v_permlane32_swap_b32 v192, v193\n"
      "v_permlane32_swap_b32 v194, v195\n"
      "v_mfma_f32_16x16x32_bf16 v[200:203], v[104:107], v[228:231], v[200:203]\n"
      "v_mfma_f32_16x16x32_bf16 v[204:207], v[76:79], v[224:227], 0\n"
      "v_permlane32_swap_b32 v196, v197\n"
      "v_permlane32_swap_b32 v198, v199\n"
      "v_mfma_f32_16x16x32_bf16 v[204:207], v[108:111], v[228:231], v[204:207]\n"
      "v_add_f32_dpp v128, v193, v192 row_ror:8 row_mask:0xf bank_mask:0x3\n"
      "s_nop 1\n"
      "v_add_f32_dpp v128, v194, v195 row_ror:8 row_mask:0xf bank_mask:0xc\n"
      "v_mfma_f32_16x16x32_bf16 v[208:211], v[80:83], v[224:227], 0\n"
      "v_permlane32_swap_b32 v200, v201\n"
      "v_permlane32_swap_b32 v202, v203\n"
      "v_mfma_f32_16x16x32_bf16 v[208:211], v[112:115], v[228:231], v[208:211]\n"
      "v_add_f32_dpp v129, v197, v196 row_ror:8 row_mask:0xf bank_mask:0x3\n"
      "s_nop 1\n"
      "v_add_f32_dpp v129, v198, v199 row_ror:8 row_mask:0xf bank_mask:0xc\n"
      "v_mfma_f32_16x16x32_bf16 v[212:215], v[84:87], v[224:227], 0\n"
      "v_permlane32_swap_b32 v204, v205\n"
      "v_permlane32_swap_b32 v206, v207\n"
      "v_mfma_f32_16x16x32_bf16 v[212:215], v[116:119], v[228:231], v[212:215]\n"
      "v_add_f32_dpp v130, v201, v200 row_ror:8 row_mask:0xf bank_mask:0x3\n"
      "s_nop 1\n"
      "v_add_f32_dpp v130, v202, v203 row_ror:8 row_mask:0xf bank_mask:0xc\n"
      "v_mfma_f32_16x16x32_bf16 v[216:219], v[88:91], v[224:227], 0\n"
      "v_permlane32_swap_b32 v208, v209\n"
      "v_permlane32_swap_b32 v210, v211\n"
      "v_mfma_f32_16x16x32_bf16 v[216:219], v[120:123], v[228:231], v[216:219]\n"
      "v_add_f32_dpp v131, v205, v204 row_ror:8 row_mask:0xf bank_mask:0x3\n"
      "s_nop 1\n"
      "v_add_f32_dpp v131, v206, v207 row_ror:8 row_mask:0xf bank_mask:0xc\n"
      "v_mfma_f32_16x16x32_bf16 v[220:223], v[92:95], v[224:227], 0\n"
      "v_permlane32_swap_b32 v212, v213\n"
      "v_permlane32_swap_b32 v214, v215\n"
      "v_mfma_f32_16x16x32_bf16 v[220:223], v[124:127], v[228:231], v[220:223]\n"
      "v_add_f32_dpp v132, v209, v208 row_ror:8 row_mask:0xf bank_mask:0x3\n"
      "s_nop 1\n"
      "v_add_f32_dpp v132, v210, v211 row_ror:8 row_mask:0xf bank_mask:0xc\n"
      "v_cvt_pk_bf16_f32 v136, v128, v129\n"
      "v_cvt_pk_bf16_f32 v138, v130, v131\n"
      "buffer_store_dwordx4 v[136:139], v38, s[16:19], s47 offen offset:0 sc1\n"
      "v_permlane32_swap_b32 v216, v217\n"
      "v_permlane32_swap_b32 v218, v219\n"
      "v_add_f32_dpp v133, v213, v212 row_ror:8 row_mask:0xf bank_mask:0x3\n"
      "s_nop 1\n"
      "v_add_f32_dpp v133, v214, v215 row_ror:8 row_mask:0xf bank_mask:0xc\n"
      "v_permlane32_swap_b32 v220, v221\n"
      "v_permlane32_swap_b32 v222, v223\n"
      "v_add_f32_dpp v134, v217, v216 row_ror:8 row_mask:0xf bank_mask:0x3\n"
      "s_nop 1\n"
      "v_add_f32_dpp v134, v218, v219 row_ror:8 row_mask:0xf bank_mask:0xc\n"
      "v_add_f32_dpp v135, v221, v220 row_ror:8 row_mask:0xf bank_mask:0x3\n"
      "s_nop 1\n"
      "v_add_f32_dpp v135, v222, v223 row_ror:8 row_mask:0xf bank_mask:0xc\n"
      "v_cvt_pk_bf16_f32 v140, v132, v133\n"
      "v_cvt_pk_bf16_f32 v142, v134, v135\n"
      "buffer_store_dwordx4 v[140:143], v38, s[16:19], s47 offen offset:128 sc1\n"
      "s_endpgm\n"
      ".LZ%=:\n"
      "s_setprio 0\n"
      "v_and_b32 v48, 63, v0\n"
      "v_and_b32 v49, 7, v0\n"
      "v_lshlrev_b32 v108, 3, v49\n"
      "s_lshl4_add_u32 s35, s48, s49\n"
      "s_lshl_b32 s41, s35, 4\n"
      "s_and_b32 s43, s41, 0xffffff80\n"
      "s_add_u32 s43, s43, 43008\n"
      "buffer_load_dwordx2 v[104:105], v108, s[16:19], s43 offen sc1\n"
      "s_mul_i32 s44, s35, 7168\n"
      "s_add_u32 s44, s44, 311296\n"
      "s_add_u32 s67, s44, 0x1000\n"
      "v_lshrrev_b32 v109, 5, v48\n"
      "v_and_b32 v110, 8, v48\n"
      "v_lshl_add_u32 v109, v109, 2, v110\n"
      "v_and_b32 v113, 16, v48\n"
      "s_movk_i32 s59, 0x7fff\n"
      "s_waitcnt vmcnt(0)\n"
      "s_movk_i32 s61, 0x7ff\n"
      ".Ldzh%=:\n"
      "v_cmp_ne_u32 vcc, s15, v105\n"
      "v_bitop3_b32 v110, v104, s61, v104 bitop3:0x0c\n"
      "v_not_b32 v112, v104\n"
      "s_cbranch_vccnz .Ldzs%=\n"
      ".Ldzo%=:\n"
      "v_lshl_add_u32 v110, v110, 11, v109\n"
      "v_lshlrev_b32 v108, 4, v48\n"
      "buffer_load_dword v111, v110, s[20:23], s41 offen\n"
      "v_lshl_or_b32 v113, v113, 26, v110\n"
      "v_ashrrev_i32 v112, 31, v112\n"
      "v_or_b32 v112, 0x80000000, v112\n"
      "v_xor_b32 v112, v112, v104\n"
      "v_and_b32 v112, 0xffff0000, v112\n"
      "v_mul_f32 v112, 0xbfb8aa3b, v112\n"
      "v_exp_f32 v112, v112\n"
      "s_nop 0\n"
      "v_add_f32 v112, 1.0, v112\n"
      "v_rcp_f32 v112, v112\n"
      "s_nop 0\n"
      "v_cvt_pk_bf16_f32 v112, v112, v112\n"
      "v_and_b32 v112, 0xffff0000, v112\n"
      ".Ldpc1_%=:\n"
      "s_memrealtime s[64:65]\n"
      "s_waitcnt lgkmcnt(0)\n"
      "s_sub_u32 s64, s64, s12\n"
      "s_cmp_ge_u32 s64, 830\n"
      "s_cbranch_scc1 .Ldpd1_%=\n"
      "s_sleep 1\n"
      "s_branch .Ldpc1_%=\n"
      ".Ldpd1_%=:\n"
      "buffer_load_dwordx4 v[64:67], v108, s[16:19], s44 offen offset:0 sc1\n"
      "buffer_load_dwordx4 v[68:71], v108, s[16:19], s44 offen offset:1024 sc1\n"
      "buffer_load_dwordx4 v[72:75], v108, s[16:19], s44 offen offset:2048 sc1\n"
      "buffer_load_dwordx4 v[76:79], v108, s[16:19], s44 offen offset:3072 sc1\n"
      "buffer_load_dwordx4 v[80:83], v108, s[16:19], s67 offen offset:0 sc1\n"
      "buffer_load_dwordx4 v[84:87], v108, s[16:19], s67 offen offset:1024 sc1\n"
      "buffer_load_dwordx4 v[88:91], v108, s[16:19], s67 offen offset:2048 sc1\n"
      "s_movk_i32 s59, 0x7fff\n"
      "s_waitcnt vmcnt(0)\n"
      "s_nop 0\n"
      "v_lshlrev_b32 v106, 16, v111\n"
      "v_and_b32 v107, 0xffff0000, v111\n"
      ".Ldq%=:\n"
      "v_xor_b32 v96, s15, v65\n"
      "v_xor_b32 v97, s15, v67\n"
      "v_bitop3_b32 v96, s15, v69, v96 bitop3:0xbe\n"
      "v_bitop3_b32 v97, s15, v71, v97 bitop3:0xbe\n"
      "v_bitop3_b32 v96, s15, v73, v96 bitop3:0xbe\n"
      "v_bitop3_b32 v97, s15, v75, v97 bitop3:0xbe\n"
      "v_bitop3_b32 v96, s15, v77, v96 bitop3:0xbe\n"
      "v_bitop3_b32 v97, s15, v79, v97 bitop3:0xbe\n"
      "v_bitop3_b32 v96, s15, v81, v96 bitop3:0xbe\n"
      "v_bitop3_b32 v97, s15, v83, v97 bitop3:0xbe\n"
      "v_bitop3_b32 v96, s15, v85, v96 bitop3:0xbe\n"
      "v_bitop3_b32 v97, s15, v87, v97 bitop3:0xbe\n"
      "v_bitop3_b32 v96, s15, v89, v96 bitop3:0xbe\n"
      "v_bitop3_b32 v97, s15, v91, v97 bitop3:0xbe\n"
      "v_or_b32 v96, v96, v97\n"
      "v_cmp_ne_u32 vcc, 0, v96\n"
      "v_lshlrev_b32 v96, 16, v64\n"
      "v_and_b32 v97, 0xffff0000, v64\n"
      "v_lshlrev_b32 v98, 16, v66\n"
      "v_and_b32 v99, 0xffff0000, v66\n"
      "s_cbranch_vccnz .Ldqs%=\n"
      ".Ldqo%=:\n"
      "v_lshlrev_b32 v100, 16, v68\n"
      "v_and_b32 v101, 0xffff0000, v68\n"
      "v_lshlrev_b32 v102, 16, v70\n"
      "v_and_b32 v103, 0xffff0000, v70\n"
      "v_add_f32 v96, v96, v100\n"
      "v_add_f32 v97, v97, v101\n"
      "v_add_f32 v98, v98, v102\n"
      "v_add_f32 v99, v99, v103\n"
      "v_lshlrev_b32 v100, 16, v72\n"
      "v_and_b32 v101, 0xffff0000, v72\n"
      "v_lshlrev_b32 v102, 16, v74\n"
      "v_and_b32 v103, 0xffff0000, v74\n"
      "v_add_f32 v96, v96, v100\n"
      "v_add_f32 v97, v97, v101\n"
      "v_add_f32 v98, v98, v102\n"
      "v_add_f32 v99, v99, v103\n"
      "v_lshlrev_b32 v100, 16, v76\n"
      "v_and_b32 v101, 0xffff0000, v76\n"
      "v_lshlrev_b32 v102, 16, v78\n"
      "v_and_b32 v103, 0xffff0000, v78\n"
      "v_add_f32 v96, v96, v100\n"
      "v_add_f32 v97, v97, v101\n"
      "v_add_f32 v98, v98, v102\n"
      "v_add_f32 v99, v99, v103\n"
      "v_lshlrev_b32 v100, 16, v80\n"
      "v_and_b32 v101, 0xffff0000, v80\n"
      "v_lshlrev_b32 v102, 16, v82\n"
      "v_and_b32 v103, 0xffff0000, v82\n"
      "v_add_f32 v96, v96, v100\n"
      "v_add_f32 v97, v97, v101\n"
      "v_add_f32 v98, v98, v102\n"
      "v_add_f32 v99, v99, v103\n"
      "v_lshlrev_b32 v100, 16, v84\n"
      "v_and_b32 v101, 0xffff0000, v84\n"
      "v_lshlrev_b32 v102, 16, v86\n"
      "v_and_b32 v103, 0xffff0000, v86\n"
      "v_add_f32 v96, v96, v100\n"
      "v_add_f32 v97, v97, v101\n"
      "v_add_f32 v98, v98, v102\n"
      "v_add_f32 v99, v99, v103\n"
      "v_lshlrev_b32 v100, 16, v88\n"
      "v_and_b32 v101, 0xffff0000, v88\n"
      "v_lshlrev_b32 v102, 16, v90\n"
      "v_and_b32 v103, 0xffff0000, v90\n"
      "v_add_f32 v96, v96, v100\n"
      "v_add_f32 v97, v97, v101\n"
      "v_add_f32 v98, v98, v102\n"
      "v_add_f32 v99, v99, v103\n"
      "s_nop 0\n"
      "v_permlane32_swap_b32 v96, v98\n"
      "v_permlane32_swap_b32 v97, v99\n"
      "v_add_f32 v96, v96, v98\n"
      "v_add_f32 v97, v97, v99\n"
      "v_mov_b32 v98, v96\n"
      "v_mov_b32 v99, v97\n"
      "s_nop 0\n"
      "v_permlane16_swap_b32 v96, v98\n"
      "v_permlane16_swap_b32 v97, v99\n"
      "v_add_f32 v96, v96, v98\n"
      "v_add_f32 v97, v97, v99\n"
      "v_cvt_pk_bf16_f32 v96, v96, v97\n"
      "v_and_b32 v97, 0xffff0000, v96\n"
      "v_lshlrev_b32 v96, 16, v96\n"
      "v_mul_f32 v97, v97, v112\n"
      "v_mul_f32 v96, v96, v112\n"
      "v_cvt_pk_bf16_f32 v96, v96, v97\n"
      "v_and_b32 v97, 0xffff0000, v96\n"
      "v_lshlrev_b32 v96, 16, v96\n"
      "v_add_f32 v97, v97, v107\n"
      "v_add_f32 v96, v96, v106\n"
      "v_cvt_pk_bf16_f32 v96, v96, v97\n"
      "buffer_atomic_pk_add_bf16 v96, v113, s[24:27], s41 offen\n"
      "s_endpgm\n"
      ".Ldgs%=:\n"
      "s_sleep 1\n"
      "s_sub_u32 s59, s59, 1\n"
      "s_cbranch_scc1 .Ldgo%=\n"
      "buffer_load_dwordx4 v[224:227], v35, s[16:19], s46 offen sc1\n"
      "buffer_load_dwordx4 v[228:231], v35, s[16:19], s46 offen offset:2048 sc1\n"
      "s_waitcnt vmcnt(0)\n"
      "s_branch .Ldg%=\n"
      ".Ldvs%=:\n"
      "s_sub_u32 s59, s59, 1\n"
      "s_cbranch_scc1 .Ldvo%=\n"
      "s_waitcnt lgkmcnt(0)\n"
      "ds_read_b128 v[240:243], v42 offset:32\n"
      "ds_read_b128 v[224:227], v37 \n"
      "ds_read_b128 v[228:231], v37 offset:1152\n"
      "s_waitcnt lgkmcnt(0)\n"
      "s_branch .Ldv%=\n"
      ".Ldzs%=:\n"
      "s_sleep 2\n"
      "s_sub_u32 s59, s59, 1\n"
      "s_cbranch_scc1 .Ldzo%=\n"
      "buffer_load_dwordx2 v[104:105], v108, s[16:19], s43 offen sc1\n"
      "s_waitcnt vmcnt(0)\n"
      "s_branch .Ldzh%=\n"
      ".Ldqs%=:\n"
      "s_sleep 1\n"
      "s_sub_u32 s59, s59, 1\n"
      "s_cbranch_scc1 .Ldqo%=\n"
      "v_cmp_ne_u32 vcc, s15, v65\n"
      "v_cmp_ne_u32 s[64:65], s15, v67\n"
      "s_nop 0\n"
      "s_or_b64 exec, vcc, s[64:65]\n"
      "s_cbranch_scc0 .Ldqk0%=\n"
      "buffer_load_dwordx4 v[64:67], v108, s[16:19], s44 offen offset:0 sc1\n"
      ".Ldqk0%=:\n"
      "s_mov_b64 exec, -1\n"
      "v_cmp_ne_u32 vcc, s15, v69\n"
      "v_cmp_ne_u32 s[64:65], s15, v71\n"
      "s_nop 0\n"
      "s_or_b64 exec, vcc, s[64:65]\n"
      "s_cbranch_scc0 .Ldqk1%=\n"
      "buffer_load_dwordx4 v[68:71], v108, s[16:19], s44 offen offset:1024 sc1\n"
      ".Ldqk1%=:\n"
      "s_mov_b64 exec, -1\n"
      "v_cmp_ne_u32 vcc, s15, v73\n"
      "v_cmp_ne_u32 s[64:65], s15, v75\n"
      "s_nop 0\n"
      "s_or_b64 exec, vcc, s[64:65]\n"
      "s_cbranch_scc0 .Ldqk2%=\n"
      "buffer_load_dwordx4 v[72:75], v108, s[16:19], s44 offen offset:2048 sc1\n"
      ".Ldqk2%=:\n"
      "s_mov_b64 exec, -1\n"
      "v_cmp_ne_u32 vcc, s15, v77\n"
      "v_cmp_ne_u32 s[64:65], s15, v79\n"
      "s_nop 0\n"
      "s_or_b64 exec, vcc, s[64:65]\n"
      "s_cbranch_scc0 .Ldqk3%=\n"
      "buffer_load_dwordx4 v[76:79], v108, s[16:19], s44 offen offset:3072 sc1\n"
      ".Ldqk3%=:\n"
      "s_mov_b64 exec, -1\n"
      "v_cmp_ne_u32 vcc, s15, v81\n"
      "v_cmp_ne_u32 s[64:65], s15, v83\n"
      "s_nop 0\n"
      "s_or_b64 exec, vcc, s[64:65]\n"
      "s_cbranch_scc0 .Ldqk4%=\n"
      "buffer_load_dwordx4 v[80:83], v108, s[16:19], s67 offen offset:0 sc1\n"
      ".Ldqk4%=:\n"
      "s_mov_b64 exec, -1\n"
      "v_cmp_ne_u32 vcc, s15, v85\n"
      "v_cmp_ne_u32 s[64:65], s15, v87\n"
      "s_nop 0\n"
      "s_or_b64 exec, vcc, s[64:65]\n"
      "s_cbranch_scc0 .Ldqk5%=\n"
      "buffer_load_dwordx4 v[84:87], v108, s[16:19], s67 offen offset:1024 sc1\n"
      ".Ldqk5%=:\n"
      "s_mov_b64 exec, -1\n"
      "v_cmp_ne_u32 vcc, s15, v89\n"
      "v_cmp_ne_u32 s[64:65], s15, v91\n"
      "s_nop 0\n"
      "s_or_b64 exec, vcc, s[64:65]\n"
      "s_cbranch_scc0 .Ldqk6%=\n"
      "buffer_load_dwordx4 v[88:91], v108, s[16:19], s67 offen offset:2048 sc1\n"
      ".Ldqk6%=:\n"
      "s_mov_b64 exec, -1\n"
      "s_waitcnt vmcnt(0)\n"
      "s_branch .Ldq%=\n"
      ".LT%=:\n"
      "v_mov_b32 v48, 0x22680\n"
      "ds_read_b32 v48, v48 \n"
      "s_waitcnt lgkmcnt(0)\n"
      "v_readfirstlane_b32 s12, v48\n"
      "v_mov_b32 v48, v43\n"
      "v_and_b32 v49, 7, v43\n"
      "v_lshrrev_b32 v50, 3, v43\n"
      "s_lshl_b32 s42, s34, 3\n"
      "s_add_u32 s42, s42, 4096\n"
      "s_lshl_b32 s43, s48, 8\n"
      "s_and_b32 s50, s49, 1\n"
      "s_lshl_b32 s50, s50, 7\n"
      "s_add_u32 s43, s43, s50\n"
      "s_add_u32 s43, s43, 40960\n"
      "s_mov_b32 s20, s64\n"
      "s_and_b32 s21, s65, 0xffff\n"
      "s_mov_b32 s22, 0x400000\n"
      "s_mov_b32 s23, 0x20000\n"
      "s_sleep 0\n"
      ".LS%=:\n"
      "v_lshlrev_b32 v112, 4, v0\n"
      "v_lshlrev_b32 v113, 2, v0\n"
      "v_sub_u32 v113, 0xfff, v113\n"
      "v_lshlrev_b32 v115, 2, v48\n"
      "v_mov_b32 v116, 0\n"
      "ds_write_b32 v115, v116 offset:26112\n"
      "ds_write_b32 v116, v116 offset:26400\n"
      "v_add_u32 v115, 0x140, v115\n"
      "v_mov_b32 v114, v115\n"
      "s_lshl_b32 s50, s33, 2\n"
      "s_add_u32 s50, s50, 256\n"
      "v_writelane_b32 v114, s50, 63\n"
      "s_lshl_b32 s50, s33, 5\n"
      "v_lshl_add_u32 v117, v49, 2, s50\n"
      "v_lshlrev_b32 v118, 5, v50\n"
      "s_and_b32 s50, s49, 1\n"
      "s_lshl_b32 s50, s50, 10\n"
      "v_lshl_add_u32 v119, v50, 7, s50\n"
      "s_movk_i32 s51, 24576\n"
      "s_mov_b32 s52, 0x80008000\n"
      "s_mov_b32 s53, 0xffff0000\n"
      "s_movk_i32 s59, 0x7fff\n"
      ".Lt_pc2_%=:\n"
      "s_memrealtime s[64:65]\n"
      "s_waitcnt lgkmcnt(0)\n"
      "s_sub_u32 s64, s64, s12\n"
      "s_cmp_ge_u32 s64, 290\n"
      "s_cbranch_scc1 .Lt_pd2_%=\n"
      "s_sleep 1\n"
      "s_branch .Lt_pc2_%=\n"
      ".Lt_pd2_%=:\n"
      ".Lt_p%=:\n"
      "buffer_load_dwordx4 v[72:75], v112, s[16:19], s51 offen sc1\n"
      "s_waitcnt vmcnt(0)\n"
      "v_cmp_ne_u32 vcc, s15, v73\n"
      "v_cmp_ne_u32 s[54:55], s15, v75\n"
      "v_pk_ashrrev_i16 v81, 15, v72 op_sel_hi:[0,1]\n"
      "v_pk_ashrrev_i16 v82, 15, v74 op_sel_hi:[0,1]\n"
      "s_or_b64 vcc, vcc, s[54:55]\n"
      "s_cbranch_scc1 .Lt_ps%=\n"
      ".Lt_po%=:\n"
      "v_bitop3_b32 v81, v72, v81, s52 bitop3:0x1e\n"
      "v_bitop3_b32 v82, v74, v82, s52 bitop3:0x1e\n"
      "v_lshl_or_b32 v76, v81, 16, v113\n"
      "v_and_or_b32 v77, v81, s53, v113\n"
      "v_lshl_or_b32 v78, v82, 16, v113\n"
      "v_and_or_b32 v79, v82, s53, v113\n"
      "v_add_u32 v77, -1, v77\n"
      "v_add_u32 v78, -2, v78\n"
      "v_add_u32 v79, -3, v79\n"
      "v_max3_u32 v80, v76, v77, v78\n"
      "v_max_u32 v80, v80, v79\n"
      "s_nop 1\n"
      "v_max_u32_dpp v80, v80, v80 row_shr:1 row_mask:0xf bank_mask:0xf bound_ctrl:1\n"
      "s_nop 1\n"
      "v_max_u32_dpp v80, v80, v80 row_shr:2 row_mask:0xf bank_mask:0xf bound_ctrl:1\n"
      "s_nop 1\n"
      "v_max_u32_dpp v80, v80, v80 row_shr:4 row_mask:0xf bank_mask:0xf bound_ctrl:1\n"
      "s_nop 1\n"
      "v_max_u32_dpp v80, v80, v80 row_shr:8 row_mask:0xf bank_mask:0xf bound_ctrl:1\n"
      "s_nop 1\n"
      "v_max_u32_dpp v80, v80, v80 row_bcast:15 row_mask:0xa bank_mask:0xf\n"
      "s_nop 1\n"
      "v_max_u32_dpp v80, v80, v80 row_bcast:31 row_mask:0xc bank_mask:0xf\n"
      "ds_write_b32 v114, v80 offset:26112\n"
      "s_waitcnt lgkmcnt(0)\n"
      "s_barrier\n"
      "ds_read_b128 v[92:95], v116 offset:26368\n"
      "ds_read_b128 v[96:99], v116 offset:26384\n"
      "s_waitcnt lgkmcnt(1)\n"
      "v_min3_u32 v81, v92, v93, v94\n"
      "s_waitcnt lgkmcnt(0)\n"
      "v_min3_u32 v82, v96, v97, v98\n"
      "v_min3_u32 v81, v81, v95, v99\n"
      "v_min_u32 v81, v81, v82\n"
      "v_cmp_ge_u32 s[60:61], v76, v81\n"
      "v_cmp_ge_u32 s[62:63], v77, v81\n"
      "v_cmp_ge_u32 s[64:65], v78, v81\n"
      "v_cmp_ge_u32 s[66:67], v79, v81\n"
      "s_bcnt1_i32_b64 s68, s[60:61]\n"
      "s_bcnt1_i32_b64 s69, s[62:63]\n"
      "s_bcnt1_i32_b64 s70, s[64:65]\n"
      "s_bcnt1_i32_b64 s71, s[66:67]\n"
      "s_add_u32 s69, s69, s68\n"
      "s_add_u32 s70, s70, s69\n"
      "s_add_u32 s71, s71, s70\n"
      "v_mov_b32 v82, s71\n"
      "s_mov_b64 exec, 1\n"
      "ds_add_rtn_u32 v83, v116, v82 offset:26400\n"
      "s_mov_b64 exec, -1\n"
      "v_mbcnt_lo_u32_b32 v84, s60, 0\n"
      "v_mbcnt_hi_u32_b32 v84, s61, v84\n"
      "v_mbcnt_lo_u32_b32 v85, s62, 0\n"
      "v_mbcnt_hi_u32_b32 v85, s63, v85\n"
      "v_add_u32 v85, s68, v85\n"
      "v_mbcnt_lo_u32_b32 v86, s64, 0\n"
      "v_mbcnt_hi_u32_b32 v86, s65, v86\n"
      "v_add_u32 v86, s69, v86\n"
      "v_mbcnt_lo_u32_b32 v87, s66, 0\n"
      "v_mbcnt_hi_u32_b32 v87, s67, v87\n"
      "v_add_u32 v87, s70, v87\n"
      "s_waitcnt lgkmcnt(0)\n"
      "v_readfirstlane_b32 s72, v83\n"
      "s_nop 1\n"
      "v_add_lshl_u32 v84, v84, s72, 2\n"
      "v_add_lshl_u32 v85, v85, s72, 2\n"
      "v_add_lshl_u32 v86, v86, s72, 2\n"
      "v_add_lshl_u32 v87, v87, s72, 2\n"
      "v_and_b32 v84, 0xff, v84\n"
      "v_and_b32 v85, 0xff, v85\n"
      "v_and_b32 v86, 0xff, v86\n"
      "v_and_b32 v87, 0xff, v87\n"
      "v_cndmask_b32 v84, v115, v84, s[60:61]\n"
      "ds_write_b32 v84, v76 offset:26112\n"
      "v_cndmask_b32 v85, v115, v85, s[62:63]\n"
      "ds_write_b32 v85, v77 offset:26112\n"
      "v_cndmask_b32 v86, v115, v86, s[64:65]\n"
      "ds_write_b32 v86, v78 offset:26112\n"
      "v_cndmask_b32 v87, v115, v87, s[66:67]\n"
      "ds_write_b32 v87, v79 offset:26112\n"
      "s_waitcnt lgkmcnt(0)\n"
      "s_barrier\n"
      "ds_read_b32 v81, v116 offset:26400\n"
      ".Lt_r%=:\n"
      "ds_read_b32 v100, v117 offset:26112\n"
      "ds_read_b128 v[92:95], v118 offset:26112\n"
      "ds_read_b128 v[96:99], v118 offset:26128\n"
      "v_mov_b32 v101, s15\n"
      "v_mov_b32 v89, 0\n"
      "s_waitcnt lgkmcnt(3)\n"
      "v_cmp_lt_u32 vcc, 64, v81\n"
      "s_movk_i32 s50, 0x7ff\n"
      "s_cbranch_vccnz .Lt_m%=\n"
      "s_waitcnt lgkmcnt(1)\n"
      "v_cmp_gt_u32 s[60:61], v92, v100\n"
      "v_cmp_gt_u32 s[62:63], v93, v100\n"
      "v_cmp_gt_u32 s[64:65], v94, v100\n"
      "v_cmp_gt_u32 s[66:67], v95, v100\n"
      "s_waitcnt lgkmcnt(0)\n"
      "v_cmp_gt_u32 s[68:69], v96, v100\n"
      "v_cmp_gt_u32 s[70:71], v97, v100\n"
      "v_cmp_gt_u32 s[72:73], v98, v100\n"
      "v_cmp_gt_u32 s[74:75], v99, v100\n"
      "v_bitop3_b32 v81, v100, s50, v100 bitop3:0x0c\n"
      "v_addc_co_u32 v89, vcc, 0, v89, s[60:61]\n"
      "v_addc_co_u32 v89, vcc, 0, v89, s[62:63]\n"
      "v_addc_co_u32 v89, vcc, 0, v89, s[64:65]\n"
      "v_addc_co_u32 v89, vcc, 0, v89, s[66:67]\n"
      "v_addc_co_u32 v89, vcc, 0, v89, s[68:69]\n"
      "v_addc_co_u32 v89, vcc, 0, v89, s[70:71]\n"
      "v_addc_co_u32 v89, vcc, 0, v89, s[72:73]\n"
      "v_addc_co_u32 v89, vcc, 0, v89, s[74:75]\n"
      "v_lshl_add_u32 v82, v81, 11, v119\n"
      "v_lshlrev_b32 v81, 3, v81\n"
      "v_add_u32_dpp v89, v89, v89 row_ror:8 row_mask:0xf bank_mask:0xf\n"
      "v_mov_b32 v83, v89\n"
      "s_nop 1\n"
      "v_permlane16_swap_b32 v89, v83\n"
      "v_add_u32 v89, v89, v83\n"
      "v_mov_b32 v83, v89\n"
      "s_nop 1\n"
      "v_permlane32_swap_b32 v89, v83\n"
      "v_add_u32 v89, v89, v83\n"
      "v_cmp_gt_u32 vcc, 8, v89\n"
      "v_lshlrev_b32 v89, 3, v89\n"
      "s_movk_i32 s51, 8192\n"
      "s_cbranch_vccz .Lt_n%=\n"
      "s_and_b64 exec, vcc, 0xff\n"
      "ds_write_b32 v89, v100 offset:26752\n"
      "buffer_load_dword v84, v81, s[16:19], s51 offen sc1\n"
      "s_mov_b64 exec, vcc\n"
      "buffer_load_dword v85, v82, s[20:23], 0 offen\n"
      "s_mov_b64 exec, -1\n"
      ".Lt_n%=:\n"
      "v_lshlrev_b32 v89, 3, v49\n"
      "s_waitcnt lgkmcnt(0)\n"
      "s_barrier\n"
      "s_cmp_lg_u32 s33, 0\n"
      "s_cbranch_scc1 .Lt_a%=\n"
      "ds_read_b32 v100, v89 offset:26752\n"
      "s_mov_b64 exec, 0xff\n"
      "s_waitcnt lgkmcnt(0)\n"
      "buffer_store_dwordx2 v[100:101], v89, s[16:19], s43 offen sc1\n"
      "s_mov_b64 exec, -1\n"
      "s_nop 1\n"
      "v_lshlrev_b32 v81, 3, v50\n"
      "ds_read_b32 v81, v81 offset:26752\n"
      "v_mov_b32 v92, 0\n"
      "v_mov_b32 v93, 0\n"
      "v_mov_b32 v94, 0\n"
      "v_mov_b32 v95, 0\n"
      "s_and_b32 s51, s43, 0x7ff\n"
      "v_lshlrev_b32 v82, 4, v49\n"
      "s_waitcnt lgkmcnt(0)\n"
      "v_bitop3_b32 v81, v81, s50, v81 bitop3:0x0c\n"
      "v_lshl_add_u32 v81, v81, 11, v82\n"
      "buffer_store_dwordx4 v[92:95], v81, s[24:27], s51 offen sc1\n"
      "s_waitcnt vmcnt(0)\n"
      "s_mov_b64 exec, 0xff\n"
      "buffer_store_dwordx2 v[100:101], v89, s[16:19], s43 offen offset:2048 sc1\n"
      "s_mov_b64 exec, -1\n"
      "s_nop 1\n"
      "v_mov_b32 v100, s15\n"
      "buffer_store_dwordx2 v[100:101], off, s[16:19], s42 sc1\n"
      ".Lt_a%=:\n"
      "s_cmp_lg_u32 s33, 3\n"
      "s_cbranch_scc1 .Lt_z%=\n"
      "s_sleep 55\n"
      "s_branch .LZ%=\n"
      ".Lt_z%=:\n"
      "s_cmp_lg_u32 s34, 127\n"
      "s_cbranch_scc1 .Lt_e%=\n"
      "s_cmp_lg_u32 s33, 0\n"
      "s_cbranch_scc1 .Lt_e%=\n"
      "s_movk_i32 s51, 4096\n"
      "s_movk_i32 s59, 0x7fff\n"
      ".Lt_k%=:\n"
      "s_sleep 16\n"
      "buffer_load_dwordx4 v[92:95], v112, s[16:19], s51 offen sc1\n"
      "buffer_load_dwordx4 v[96:99], v112, s[16:19], s51 offen offset:1024 sc1\n"
      "s_sub_u32 s59, s59, 1\n"
      "s_cbranch_scc1 .Lt_kb%=\n"
      "s_waitcnt vmcnt(0)\n"
      "v_xor_b32 v86, s15, v93\n"
      "v_bitop3_b32 v86, s15, v95, v86 bitop3:0xbe\n"
      "v_bitop3_b32 v86, s15, v97, v86 bitop3:0xbe\n"
      "v_bitop3_b32 v86, s15, v99, v86 bitop3:0xbe\n"
      "v_cmp_ne_u32 vcc, 0, v86\n"
      "s_nop 1\n"
      "s_cbranch_vccnz .Lt_k%=\n"
      ".Lt_kb%=:\n"
      "buffer_store_dword v101, off, s[16:19], 0 sc1\n"
      ".Lt_e%=:\n"
      "s_waitcnt vmcnt(0)\n"
      "s_endpgm\n"
      ".Lt_ps%=:\n"
      "s_sleep 1\n"
      "s_sub_u32 s59, s59, 1\n"
      "s_cbranch_scc0 .Lt_p%=\n"
      "s_branch .Lt_po%=\n"
      ".Lt_m%=:\n"
      "s_waitcnt lgkmcnt(0)\n"
      "s_barrier\n"
      "s_mov_b32 s59, 0\n"
      "v_mov_b32 v90, 0\n"
      ".Lt_ml%=:\n"
      "s_nop 1\n"
      "v_max3_u32 v80, v76, v77, v78\n"
      "v_max_u32 v80, v80, v79\n"
      "s_nop 1\n"
      "v_max_u32_dpp v80, v80, v80 row_shr:1 row_mask:0xf bank_mask:0xf bound_ctrl:1\n"
      "s_nop 1\n"
      "v_max_u32_dpp v80, v80, v80 row_shr:2 row_mask:0xf bank_mask:0xf bound_ctrl:1\n"
      "s_nop 1\n"
      "v_max_u32_dpp v80, v80, v80 row_shr:4 row_mask:0xf bank_mask:0xf bound_ctrl:1\n"
      "s_nop 1\n"
      "v_max_u32_dpp v80, v80, v80 row_shr:8 row_mask:0xf bank_mask:0xf bound_ctrl:1\n"
      "s_nop 1\n"
      "v_max_u32_dpp v80, v80, v80 row_bcast:15 row_mask:0xa bank_mask:0xf\n"
      "s_nop 1\n"
      "v_max_u32_dpp v80, v80, v80 row_bcast:31 row_mask:0xc bank_mask:0xf\n"
      "s_nop 0\n"
      "v_readlane_b32 s50, v80, 63\n"
      "s_mov_b32 m0, s59\n"
      "s_nop 0\n"
      "v_cmp_ne_u32 vcc, s50, v76\n"
      "s_nop 1\n"
      "v_cndmask_b32 v76, 0, v76, vcc\n"
      "v_cmp_ne_u32 vcc, s50, v77\n"
      "s_nop 1\n"
      "v_cndmask_b32 v77, 0, v77, vcc\n"
      "v_cmp_ne_u32 vcc, s50, v78\n"
      "s_nop 1\n"
      "v_cndmask_b32 v78, 0, v78, vcc\n"
      "v_cmp_ne_u32 vcc, s50, v79\n"
      "s_nop 1\n"
      "v_cndmask_b32 v79, 0, v79, vcc\n"
      "v_writelane_b32 v90, s50, m0\n"
      "s_add_u32 s59, s59, 1\n"
      "s_cmp_lt_u32 s59, 8\n"
      "s_cbranch_scc1 .Lt_ml%=\n"
      "s_mov_b64 exec, 0xff\n"
      "ds_write_b32 v117, v90 offset:26112\n"
      "s_mov_b64 exec, -1\n"
      "v_mov_b32 v81, 0\n"
      "s_waitcnt lgkmcnt(0)\n"
      "s_barrier\n"
      "s_branch .Lt_r%=\n"
      ".LEnd%=:\n"
      : "={s[80:95]}"(sts), "={s[96:99]}"(st2)
      : [ka] "s"(__builtin_amdgcn_kernarg_segment_ptr()), [bid] "s"(blockIdx.x), "{v0}"(threadIdx.x), "s"((u32)(uintptr_t)sm)
      : "v8", "v9", "v10", "v11", "v12", "v13", "v14", "v15", "v16", "v17", "v18", "v19", "v20", "v21", "v22", "v23", "v24", "v25", "v26", "v27",
        "v28", "v29", "v30", "v31", "v32", "v33", "v34", "v35", "v36", "v37", "v38", "v39", "v40", "v41", "v42", "v43", "v44", "v45", "v46", "v47",
        "v48", "v49", "v50", "v51", "v52", "v53", "v54", "v55", "v56", "v57", "v58", "v59", "v60", "v61", "v62", "v63", "v64", "v65", "v66", "v67",
        "v68", "v69", "v70", "v71", "v72", "v73", "v74", "v75", "v76", "v77", "v78", "v79", "v80", "v81", "v82", "v83", "v84", "v85", "v86", "v87",
        "v88", "v89", "v90", "v91", "v92", "v93", "v94", "v95", "v96", "v97", "v98", "v99", "v100", "v101", "v102", "v103", "v104", "v105", "v106", "v107",
        "v108", "v109", "v110", "v111", "v112", "v113", "v114", "v115", "v116", "v117", "v118", "v119", "v120", "v121", "v122", "v123", "v124", "v125", "v126", "v127",
        "v128", "v129", "v130", "v131", "v132", "v133", "v134", "v135", "v136", "v137", "v138", "v139", "v140", "v141", "v142", "v143", "v144", "v145", "v146", "v147",
        "v148", "v149", "v150", "v151", "v152", "v153", "v154", "v155", "v156", "v157", "v158", "v159", "v160", "v161", "v162", "v163", "v164", "v165", "v166", "v167",
        "v168", "v169", "v170", "v171", "v172", "v173", "v174", "v175", "v176", "v177", "v178", "v179", "v180", "v181", "v182", "v183", "v184", "v185", "v186", "v187",
        "v188", "v189", "v190", "v191", "v192", "v193", "v194", "v195", "v196", "v197", "v198", "v199", "v200", "v201", "v202", "v203", "v204", "v205", "v206", "v207",
        "v208", "v209", "v210", "v211", "v212", "v213", "v214", "v215", "v216", "v217", "v218", "v219", "v220", "v221", "v222", "v223", "v224", "v225", "v226", "v227",
        "v228", "v229", "v230", "v231", "v232", "v233", "v234", "v235", "v236", "v237", "v238", "v239", "v240", "v241", "v242", "v243", "v244", "v245", "v246", "v247",
        "v248", "v249", "v250", "v251", "v252", "v253", "v254", "v255", "s12", "s13", "s14", "s15", "s16", "s17", "s18", "s19", "s20", "s21", "s22", "s23",
        "s24", "s25", "s26", "s27", "s28", "s29", "s30", "s31", "s33", "s34", "s35", "s36", "s37", "s38", "s39", "s40", "s41", "s42", "s43", "s44",
        "s45", "s46", "s47", "s48", "s49", "s50", "s51", "s52", "s53", "s54", "s55", "s56", "s57", "s58", "s59", "s60", "s61", "s62", "s63", "s64",
        "s65", "s66", "s67", "s68", "s69", "s70", "s71", "s72", "s73", "s74", "s75", "s76", "s77", "s78", "s79",
        "vcc", "scc", "memory");
  const int b = blockIdx.x;
  GETST();
  STAMP(11);
  PEND();
}

extern "C" void kernel_launch(void* const* d_in, const int* in_sizes, int n_in, void* d_out, int out_size,
                              void* d_ws, size_t ws_size, hipStream_t stream) {
  const u16* const* in = (const u16* const*)d_in;
  fused<<<256, 512, 0, stream>>>(in[0], in[1], in[2], in[3], in[4], in[5], in[6], in[8], in[9], in[10], in[11],
                                 (u16*)d_out, (char*)d_ws);
}
```

```cpp
#include <hip/hip_runtime.h>
#include <stdint.h>

typedef unsigned short u16;
typedef unsigned int u32;
typedef unsigned long long u64;
typedef __attribute__((ext_vector_type(4))) u32 u32x4;
typedef __attribute__((ext_vector_type(16))) u32 u32x16;
#define AS3 __attribute__((address_space(3)))

#define STAMP(I)
#define PEND()
#define GETST()
constexpr int SPIN_MAX = 1 << 16;
constexpr int UNUSED = 0;

__global__ __launch_bounds__(512, 2) void fused(const u16* __restrict__ x, const u16* __restrict__ lar,
    const u16* __restrict__ aim, const u16* __restrict__ cre, const u16* __restrict__ cim,
    const u16* __restrict__ ldt, const u16* __restrict__ wr, const u16* __restrict__ dsk,
    const u16* __restrict__ wg, const u16* __restrict__ wu, const u16* __restrict__ wd,
    u16* __restrict__ out, char* ws) {
  __shared__ __attribute__((aligned(16))) char smem_[163840];
  AS3 char* sm = (AS3 char*)smem_;
  u64 ptm = 0, pc0 = 0, pt0 = 0, pte = 0, st_[16] = {};
  u32x16 sts;
  u32x4 st2;
  asm volatile(
      "s_memrealtime s[12:13]\n"
      "s_load_dwordx16 s[64:79], %[ka], 0x0\n"
      "s_load_dwordx8 s[80:87], %[ka], 0x40\n"
      "s_load_dwordx2 s[88:89], %[ka], 0x60\n"
      "v_readfirstlane_b32 s33, v0\n"
      "s_getpc_b64 s[50:51]\n"
      ".Lpc%=:\n"
      "s_mov_b32 s34, %[bid]\n"
      "v_and_b32 v43, 63, v0\n"
      "v_mov_b32 v15, 0\n"
      "s_lshr_b32 s33, s33, 6\n"
      "s_and_b32 s52, s34, -8\n"
      "s_add_u32 s52, s52, s33\n"
      "s_min_u32 s52, s52, (.LEnd%=-.Lpc%=)>>7\n"
      "s_lshl_b32 s52, s52, 7\n"
      "s_and_b32 s50, s50, 0xffffff80\n"
      "s_add_u32 s50, s50, s52\n"
      "s_addc_u32 s51, s51, 0\n"
      "global_load_dword v11, v15, s[50:51] \n"
      "s_and_b32 s48, s34, 7\n"
      "s_lshr_b32 s49, s34, 3\n"
      "s_and_b32 s55, s49, 15\n"
      "s_lshl4_add_u32 s35, s48, s55\n"
      "s_cmp_lt_u32 s34, 128\n"
      "s_cbranch_scc1 .LH%=\n"
      "s_lshl3_add_u32 s50, s35, s33\n"
      "s_lshr_b32 s51, s34, 5\n"
      "s_andn2_b32 s51, 4, s51\n"
      "s_add_u32 s50, s50, s51\n"
      "s_lshl_b32 s51, s50, 8\n"
      "s_lshl_b32 s50, s50, 1\n"
      "v_lshl_add_u32 v48, v43, 2, s51\n"
      "v_mov_b32 v49, s50\n"
      "s_waitcnt lgkmcnt(0)\n"
      "global_load_dword v16, v48, s[66:67] nt\n"
      "global_load_dword v17, v48, s[68:69] nt\n"
      "global_load_ushort v12, v49, s[74:75] \n"
      "global_load_dword v18, v48, s[70:71] nt\n"
      "global_load_dword v19, v48, s[72:73] nt\n"
      "global_load_ushort v13, v49, s[78:79] \n"
      "s_load_dword s15, s[88:89], 0x0\n"
      "s_setprio 3\n"
      "s_branch .LC%=\n"
      ".LH%=:\n"
      "s_cmp_ge_u32 s33, 4\n"
      "s_cbranch_scc1 .LG%=\n"
      ".LD%=:\n"
      "v_and_b32 v52, 48, v43\n"
      "v_and_b32 v53, 3, v43\n"
      "v_lshl_add_u32 v53, v53, 2, v52\n"
      "v_and_b32 v54, 12, v43\n"
      "v_lshlrev_b32 v53, 2, v53\n"
      "v_lshl_add_u32 v52, v54, 9, v53\n"
      "s_and_b32 s53, s34, 127\n"
      "s_and_b32 s52, s33, 3\n"
      "s_lshl2_add_u32 s14, s53, s52\n"
      "s_lshl_b32 s51, s14, 13\n"
      "v_lshl_add_u32 v53, v54, 6, v53\n"
      "s_waitcnt lgkmcnt(0)\n"
      "s_add_u32 s52, s64, s51\n"
      "s_addc_u32 s53, s65, 0\n"
      "global_load_dwordx4 v[224:227], v53, s[76:77] offset:0\n"
      "global_load_dwordx4 v[228:231], v53, s[76:77] offset:1024\n"
      "global_load_dwordx4 v[192:195], v52, s[52:53] offset:0\n"
      "global_load_dwordx4 v[196:199], v52, s[52:53] offset:256\n"
      "global_load_dwordx4 v[200:203], v52, s[52:53] offset:512\n"
      "global_load_dwordx4 v[204:207], v52, s[52:53] offset:768\n"
      "global_load_dwordx4 v[208:211], v52, s[52:53] offset:1024\n"
      "global_load_dwordx4 v[212:215], v52, s[52:53] offset:1280\n"
      "global_load_dwordx4 v[216:219], v52, s[52:53] offset:1536\n"
      "global_load_dwordx4 v[220:223], v52, s[52:53] offset:1792\n"
      "s_load_dword s15, s[88:89], 0x0\n"
      "s_setprio 3\n"
      "s_cmp_lg_u32 s33, 0\n"
      "s_cbranch_scc1 .Ltb%=\n"
      "v_mov_b32 v48, 0x22680\n"
      "v_mov_b32 v49, s12\n"
      "ds_write_b32 v48, v49 \n"
      ".Ltb%=:\n"
      "s_mov_b32 s16, s88\n"
      "s_and_b32 s17, s89, 0xffff\n"
      "s_mov_b32 s18, 0x6e0000\n"
      "s_mov_b32 s19, 0x20000\n"
      "s_mov_b32 s20, s64\n"
      "s_and_b32 s21, s65, 0xffff\n"
      "s_mov_b32 s22, 0x400000\n"
      "s_mov_b32 s23, 0x20000\n"
      "s_mov_b32 s28, s84\n"
      "s_and_b32 s29, s85, 0xffff\n"
      "s_mov_b32 s30, 0x700000\n"
      "s_mov_b32 s31, 0x20000\n"
      "s_mov_b32 s24, s86\n"
      "s_and_b32 s25, s87, 0xffff\n"
      "s_mov_b32 s26, 0x400000\n"
      "s_mov_b32 s27, 0x20000\n"
      "s_lshl_b32 s42, s34, 3\n"
      "s_add_u32 s42, s42, 2048\n"
      "s_lshl_b32 s43, s48, 8\n"
      "s_and_b32 s50, s49, 1\n"
      "s_lshl_b32 s50, s50, 7\n"
      "s_add_u32 s43, s43, s50\n"
      "s_add_u32 s43, s43, 65536\n"
      "v_mov_b32 v20, 0x3f803f80\n"
      "v_mov_b32 v21, 0x3f803f80\n"
      "v_mov_b32 v22, 0x3f803f80\n"
      "v_mov_b32 v23, 0x3f803f80\n"
      "v_mov_b32 v44, 0x7f000000\n"
      "v_mov_b32 v25, 0\n"
      "s_lshr_b32 s70, s14, 7\n"
      "s_mul_i32 s70, s70, 61440\n"
      "s_lshl_b32 s68, s14, 5\n"
      "s_add_u32 s68, s68, s70\n"
      "s_add_u32 s68, s68, 131072\n"
      "s_lshr_b32 s70, s14, 8\n"
      "s_mul_i32 s70, s70, 61440\n"
      "s_lshl4_add_u32 s69, s14, s70\n"
      "s_add_u32 s69, s69, 393216\n"
      "v_writelane_b32 v44, s68, 0\n"
      "s_add_u32 s68, s68, 8\n"
      "v_writelane_b32 v44, s68, 20\n"
      "s_add_u32 s68, s68, 8\n"
      "v_writelane_b32 v44, s68, 40\n"
      "s_add_u32 s68, s68, 8\n"
      "v_writelane_b32 v44, s68, 60\n"
      "s_add_u32 s68, s68, 8\n"
      "v_writelane_b32 v44, s69, 1\n"
      "s_add_u32 s69, s69, 8\n"
      "v_writelane_b32 v44, s69, 2\n"
      "s_waitcnt lgkmcnt(0)\n"
      "s_add_u32 s15, s15, 0x9e3779b1\n"
      "v_mov_b32 v47, s15\n"
      "s_waitcnt vmcnt(9)\n"
      "v_mfma_f32_16x16x32_bf16 v[16:19], v[20:23], v[224:227], 0\n"
      "s_waitcnt vmcnt(8)\n"
      "v_mfma_f32_16x16x32_bf16 v[16:19], v[20:23], v[228:231], v[16:19]\n"
      "s_waitcnt vmcnt(7)\n"
      "v_mfma_f32_16x16x32_bf16 v[232:235], v[192:195], v[224:227], 0\n"
      "v_mfma_f32_16x16x32_bf16 v[252:255], v[192:195], v[192:195], 0\n"
      "v_mfma_f32_16x16x32_bf16 v[248:251], v[192:195], v[20:23], 0\n"
      "s_waitcnt vmcnt(6)\n"
      "v_mfma_f32_16x16x32_bf16 v[236:239], v[196:199], v[224:227], 0\n"
      "v_mfma_f32_16x16x32_bf16 v[252:255], v[196:199], v[196:199], v[252:255]\n"
      "v_mfma_f32_16x16x32_bf16 v[248:251], v[196:199], v[20:23], v[248:251]\n"
      "s_waitcnt vmcnt(5)\n"
      "v_mfma_f32_16x16x32_bf16 v[240:243], v[200:203], v[224:227], 0\n"
      "v_mfma_f32_16x16x32_bf16 v[252:255], v[200:203], v[200:203], v[252:255]\n"
      "v_mfma_f32_16x16x32_bf16 v[248:251], v[200:203], v[20:23], v[248:251]\n"
      "s_waitcnt vmcnt(4)\n"
      "v_mfma_f32_16x16x32_bf16 v[244:247], v[204:207], v[224:227], 0\n"
      "v_mfma_f32_16x16x32_bf16 v[252:255], v[204:207], v[204:207], v[252:255]\n"
      "v_mfma_f32_16x16x32_bf16 v[248:251], v[204:207], v[20:23], v[248:251]\n"
      "s_waitcnt vmcnt(3)\n"
      "v_mfma_f32_16x16x32_bf16 v[232:235], v[208:211], v[228:231], v[232:235]\n"
      "v_mfma_f32_16x16x32_bf16 v[252:255], v[208:211], v[208:211], v[252:255]\n"
      "v_mfma_f32_16x16x32_bf16 v[248:251], v[208:211], v[20:23], v[248:251]\n"
      "v_add_f32_dpp v16, v16, v16 quad_perm:[1,0,3,2] row_mask:0xf bank_mask:0xf\n"
      "s_waitcnt vmcnt(2)\n"
      "v_mfma_f32_16x16x32_bf16 v[236:239], v[212:215], v[228:231], v[236:239]\n"
      "v_mfma_f32_16x16x32_bf16 v[252:255], v[212:215], v[212:215], v[252:255]\n"
      "v_mfma_f32_16x16x32_bf16 v[248:251], v[212:215], v[20:23], v[248:251]\n"
      "v_add_f32_dpp v16, v16, v16 quad_perm:[2,3,0,1] row_mask:0xf bank_mask:0xf\n"
      "v_add_f32_dpp v25, v232, v25 quad_perm:[0,0,0,0] row_mask:0xf bank_mask:0x1\n"
      "s_nop 1\n"
      "v_add_f32_dpp v25, v233, v25 quad_perm:[1,1,1,1] row_mask:0xf bank_mask:0x1\n"
      "s_nop 1\n"
      "v_add_f32_dpp v25, v234, v25 quad_perm:[2,2,2,2] row_mask:0xf bank_mask:0x1\n"
      "s_nop 1\n"
      "v_add_f32_dpp v25, v235, v25 quad_perm:[3,3,3,3] row_mask:0xf bank_mask:0x1\n"
      "s_waitcnt vmcnt(1)\n"
      "v_mfma_f32_16x16x32_bf16 v[240:243], v[216:219], v[228:231], v[240:243]\n"
      "v_mfma_f32_16x16x32_bf16 v[252:255], v[216:219], v[216:219], v[252:255]\n"
      "v_mfma_f32_16x16x32_bf16 v[248:251], v[216:219], v[20:23], v[248:251]\n"
      "v_add_f32_dpp v16, v16, v16 row_ror:4 row_mask:0xf bank_mask:0xf\n"
      "v_add_f32_dpp v25, v236, v25 quad_perm:[0,0,0,0] row_mask:0xf bank_mask:0x2\n"
      "s_nop 1\n"
      "v_add_f32_dpp v25, v237, v25 quad_perm:[1,1,1,1] row_mask:0xf bank_mask:0x2\n"
      "s_nop 1\n"
      "v_add_f32_dpp v25, v238, v25 quad_perm:[2,2,2,2] row_mask:0xf bank_mask:0x2\n"
      "s_nop 1\n"
      "v_add_f32_dpp v25, v239, v25 quad_perm:[3,3,3,3] row_mask:0xf bank_mask:0x2\n"
      "s_waitcnt vmcnt(0)\n"
      "v_mfma_f32_16x16x32_bf16 v[244:247], v[220:223], v[228:231], v[244:247]\n"
      "v_mfma_f32_16x16x32_bf16 v[252:255], v[220:223], v[220:223], v[252:255]\n"
      "v_mfma_f32_16x16x32_bf16 v[248:251], v[220:223], v[20:23], v[248:251]\n"
      "v_add_f32_dpp v16, v16, v16 row_ror:8 row_mask:0xf bank_mask:0xf\n"
      "v_add_f32_dpp v25, v240, v25 quad_perm:[0,0,0,0] row_mask:0xf bank_mask:0x4\n"
      "s_nop 1\n"
      "v_add_f32_dpp v25, v241, v25 quad_perm:[1,1,1,1] row_mask:0xf bank_mask:0x4\n"
      "s_nop 1\n"
      "v_add_f32_dpp v25, v242, v25 quad_perm:[2,2,2,2] row_mask:0xf bank_mask:0x4\n"
      "s_nop 1\n"
      "v_add_f32_dpp v25, v243, v25 quad_perm:[3,3,3,3] row_mask:0xf bank_mask:0x4\n"
      "s_nop 1\n"
      "v_add_f32_dpp v25, v244, v25 quad_perm:[0,0,0,0] row_mask:0xf bank_mask:0x8\n"
      "v_mov_b32_dpp v24, v252 quad_perm:[0,0,0,0] row_mask:0xf bank_mask:0xf\n"
      "v_mov_b32_dpp v27, v254 quad_perm:[2,2,2,2] row_mask:0xf bank_mask:0xf\n"
      "v_add_f32_dpp v25, v245, v25 quad_perm:[1,1,1,1] row_mask:0xf bank_mask:0x8\n"
      "v_add_f32 v248, v248, v249\n"
      "v_add_f32 v250, v250, v251\n"
      "v_add_f32_dpp v24, v253, v24 quad_perm:[1,1,1,1] row_mask:0xf bank_mask:0xf\n"
      "v_add_f32_dpp v27, v255, v27 quad_perm:[3,3,3,3] row_mask:0xf bank_mask:0xf\n"
      "v_add_f32_dpp v25, v246, v25 quad_perm:[2,2,2,2] row_mask:0xf bank_mask:0x8\n"
      "v_add_f32 v248, v248, v250\n"
      "v_add_f32 v24, v24, v27\n"
      "v_add_f32_dpp v25, v247, v25 quad_perm:[3,3,3,3] row_mask:0xf bank_mask:0x8\n"
      "v_mul_f32 v248, 0x3a800000, v248\n"
      "v_mul_f32 v24, 0x3a800000, v24\n"
      "v_add_f32_dpp v25, v25, v25 row_ror:4 row_mask:0xf bank_mask:0xf\n"
      "v_fma_f32 v24, -v248, v248, v24\n"
      "v_max_f32 v24, 0, v24\n"
      "v_add_f32_dpp v25, v25, v25 row_ror:8 row_mask:0xf bank_mask:0xf\n"
      "v_cvt_pk_bf16_f32 v248, v248, v24\n"
      "v_and_b32 v24, 0xffff0000, v248\n"
      "v_add_f32 v24, 0x3727c5ac, v24\n"
      "v_lshlrev_b32 v26, 16, v248\n"
      "v_cvt_pk_bf16_f32 v24, v24, v24\n"
      "v_fma_f32 v25, -v26, v16, v25\n"
      "v_and_b32 v24, 0xffff0000, v24\n"
      "v_sqrt_f32 v24, v24\n"
      "s_nop 0\n"
      "v_cvt_pk_bf16_f32 v46, v26, v24\n"
      "v_and_b32 v24, 0xffff0000, v46\n"
      "v_rcp_f32 v24, v24\n"
      "s_nop 0\n"
      "v_mul_f32 v25, v25, v24\n"
      "v_cvt_pk_bf16_f32 v25, v25, v25\n"
      "s_nop 0\n"
      "v_readlane_b32 s68, v25, 0\n"
      "v_readlane_b32 s69, v25, 20\n"
      "v_readlane_b32 s70, v25, 40\n"
      "v_readlane_b32 s71, v25, 60\n"
      "s_pack_ll_b32_b16 s68, s68, s69\n"
      "s_pack_ll_b32_b16 s70, s70, s71\n"
      "v_writelane_b32 v46, s68, 1\n"
      "v_writelane_b32 v46, s70, 2\n"
      "buffer_store_dwordx2 v[46:47], v44, s[16:19], 0 offen sc1\n"
      "v_mov_b32 v48, v43\n"
      "v_and_b32 v49, 7, v43\n"
      "v_lshrrev_b32 v50, 3, v43\n"
      "s_cmp_ge_u32 s34, 112\n"
      "s_cbranch_scc1 .LS%=\n"
      "s_branch .LD2%=\n"
      ".LC%=:\n"
      "s_mov_b32 s16, s88\n"
      "s_and_b32 s17, s89, 0xffff\n"
      "s_mov_b32 s18, 0x6e0000\n"
      "s_mov_b32 s19, 0x20000\n"
      "s_mov_b32 s20, s64\n"
      "s_and_b32 s21, s65, 0xffff\n"
      "s_mov_b32 s22, 0x400000\n"
      "s_mov_b32 s23, 0x20000\n"
      "s_mul_i32 s39, s33, 6528\n"
      "s_mul_i32 s40, s33, 8768\n"
      "s_add_u32 s52, s40, 52224\n"
      "s_add_u32 s53, s52, 4160\n"
      "s_lshl_b32 s42, s34, 3\n"
      "s_add_u32 s42, s42, 2048\n"
      "v_and_b32 v48, 31, v43\n"
      "v_lshrrev_b32 v49, 5, v43\n"
      "v_lshlrev_b32 v55, 4, v49\n"
      "v_lshrrev_b32 v52, 2, v48\n"
      "v_mul_u32_u24 v52, 0x110, v52\n"
      "v_add3_u32 v20, v52, v55, s52\n"
      "v_and_b32 v52, 3, v43\n"
      "v_mul_u32_u24 v52, 0x110, v52\n"
      "s_add_u32 s50, s52, 2176\n"
      "v_add3_u32 v178, v52, v55, s50\n"
      "v_lshl_add_u32 v52, v43, 2, s50\n"
      "v_mov_b32 v53, 0x3c00\n"
      "ds_write_b32 v52, v53 \n"
      "ds_write_b32 v52, v53 offset:3264\n"
      "v_and_b32 v52, 12, v43\n"
      "s_mov_b32 s50, 0x6600\n"
      "s_mov_b32 s51, 0x6600\n"
      "v_cndmask_b32 v52, v48, v52, s[50:51]\n"
      "s_mov_b32 s50, 0xff000000\n"
      "s_mov_b32 s51, 0xff000000\n"
      "v_cndmask_b32 v52, v52, 9, s[50:51]\n"
      "v_mul_u32_u24 v52, 0x110, v52\n"
      "v_add3_u32 v21, v52, v55, s39\n"
      "v_lshl_add_u32 v52, v43, 2, s39\n"
      "v_mov_b32 v53, 0x3c00\n"
      "ds_write_b32 v52, v53 \n"
      "s_mov_b32 s24, s86\n"
      "s_and_b32 s25, s87, 0xffff\n"
      "s_mov_b32 s26, 0x400000\n"
      "s_mov_b32 s27, 0x20000\n"
      "s_mul_i32 s54, s33, 4864\n"
      "s_add_u32 s54, s54, 122368\n"
      "s_lshl_b32 s41, s35, 4\n"
      "s_and_b32 s43, s41, 0xffffff80\n"
      "s_add_u32 s43, s43, 67584\n"
      "v_and_b32 v50, 15, v43\n"
      "v_lshrrev_b32 v51, 4, v43\n"
      "v_and_b32 v53, 1, v43\n"
      "s_and_b32 s58, s33, 3\n"
      "s_lshl4_add_u32 s50, s33, s55\n"
      "s_lshl_b32 s50, s50, 14\n"
      "s_and_b32 s51, s55, 8\n"
      "s_lshl_b32 s51, s51, 4\n"
      "s_sub_u32 s50, s50, s51\n"
      "v_bfe_u32 v52, v43, 1, 3\n"
      "v_lshl_add_u32 v52, v52, 4, v53\n"
      "v_lshlrev_b32 v52, 7, v52\n"
      "v_add_u32 v15, s50, v52\n"
      "s_bitcmp1_b32 s55, 0\n"
      "s_cselect_b32 s56, s82, s80\n"
      "s_cselect_b32 s57, s83, s81\n"
      "s_lshr_b32 s50, s56, 7\n"
      "s_sub_u32 s50, s48, s50\n"
      "s_add_u32 s50, s50, 3\n"
      "s_and_b32 s50, s50, 7\n"
      "s_min_u32 s50, s50, 6\n"
      "s_mul_i32 s51, s48, 7\n"
      "s_add_u32 s50, s50, s51\n"
      "s_lshl_b32 s50, s50, 7\n"
      "s_lshr_b32 s51, s55, 1\n"
      "s_mul_i32 s51, s51, 24\n"
      "s_lshl_b32 s58, s58, 8\n"
      "s_add_u32 s51, s51, s58\n"
      "s_add_u32 s51, s51, 64\n"
      "s_mul_i32 s51, s51, 7168\n"
      "s_add_u32 s50, s50, s51\n"
      "v_min_u32 v57, 23, v43\n"
      "v_mul_u32_u24 v57, 0x1c00, v57\n"
      "v_add_u32 v57, s50, v57\n"
      "v_lshl_add_u32 v52, v48, 1, v49\n"
      "v_lshlrev_b32 v54, 3, v52\n"
      "s_add_u32 s50, s53, 570\n"
      "v_sub_u32 v22, s50, v54\n"
      "s_lshl_b32 s50, s33, 17\n"
      "v_lshl_add_u32 v8, v52, 11, s50\n"
      "v_mov_b32 v52, 0x63e\n"
      "v_mov_b32 v54, 0x6de\n"
      "s_mov_b32 s50, 0x55555555\n"
      "s_mov_b32 s51, 0x55555555\n"
      "v_cndmask_b32 v24, v52, v54, s[50:51]\n"
      "v_mov_b32 v52, 0x4de\n"
      "v_mov_b32 v54, 0x59e\n"
      "v_cndmask_b32 v52, v52, v54, s[50:51]\n"
      "s_mov_b32 s58, 0\n"
      "s_mov_b32 s59, -1\n"
      "v_cndmask_b32 v24, v24, v52, s[58:59]\n"
      "v_add_u32 v52, -1, v48\n"
      "v_and_b32 v52, -2, v52\n"
      "v_sub_u32 v24, v24, v52\n"
      "v_add_u32 v24, s53, v24\n"
      "v_add_u32 v23, 32, v24\n"
      "s_add_u32 s60, s53, 2048\n"
      "v_writelane_b32 v23, s60, 0\n"
      "v_writelane_b32 v23, s60, 32\n"
      "v_mul_u32_u24 v52, 0xc1, v53\n"
      "v_sub_u32 v52, v52, v48\n"
      "s_add_u32 s60, s53, 1150\n"
      "v_add_u32 v52, s60, v52\n"
      "v_add_u32 v25, 0xffffffc0, v24\n"
      "v_cndmask_b32 v25, v25, v52, s[58:59]\n"
      "v_and_b32 v52, -2, v43\n"
      "v_add_u32 v52, 58, v52\n"
      "v_mov_b32 v54, 0x810\n"
      "s_mov_b32 s60, 0xfffff000\n"
      "s_mov_b32 s61, 0xffffffff\n"
      "v_cndmask_b32 v52, v52, v54, s[60:61]\n"
      "s_movk_i32 s60, 1408\n"
      "s_movk_i32 s61, 1664\n"
      "v_writelane_b32 v52, s60, 8\n"
      "v_writelane_b32 v52, s60, 9\n"
      "v_writelane_b32 v52, s61, 10\n"
      "v_writelane_b32 v52, s61, 11\n"
      "v_mul_u32_u24 v54, 0x83e, v53\n"
      "v_add3_u32 v26, v52, v54, s53\n"
      "v_lshlrev_b32 v52, 1, v50\n"
      "v_add_u32 v52, v52, v54\n"
      "s_add_u32 s60, s53, 2140\n"
      "v_sub_u32 v52, s60, v52\n"
      "v_lshl_add_u32 v29, v51, 4, v52\n"
      "v_lshl_add_u32 v30, v51, 3, v52\n"
      "v_add_u32 v30, 0x440, v30\n"
      "v_bfe_u32 v52, v43, 2, 3\n"
      "v_max_u32 v54, 5, v52\n"
      "v_mul_u32_u24 v52, 0x60, v52\n"
      "v_lshl_add_u32 v52, v54, 6, v52\n"
      "v_add_u32 v52, 0x2a0, v52\n"
      "s_mov_b32 s60, 0xf\n"
      "s_mov_b32 s61, 0xf\n"
      "v_cndmask_b32 v52, v52, v55, s[60:61]\n"
      "v_mul_u32_u24 v54, 0x830, v49\n"
      "v_add3_u32 v27, v52, v54, s53\n"
      "v_and_b32 v55, 3, v43\n"
      "v_lshl_add_u32 v27, v55, 4, v27\n"
      "s_add_u32 s60, s39, 3104\n"
      "s_add_u32 s61, s52, 2080\n"
      "v_mov_b32 v52, s39\n"
      "v_mov_b32 v54, s60\n"
      "s_mov_b32 s50, 0xff00ff00\n"
      "s_mov_b32 s51, 0xff00ff00\n"
      "v_cndmask_b32 v28, v52, v54, s[50:51]\n"
      "v_mov_b32 v52, s52\n"
      "v_mov_b32 v54, s61\n"
      "v_cndmask_b32 v52, v52, v54, s[50:51]\n"
      "s_mov_b32 s50, 0xffff0000\n"
      "s_mov_b32 s51, 0xffff0000\n"
      "v_cndmask_b32 v28, v28, v52, s[50:51]\n"
      "v_and_b32 v52, 7, v43\n"
      "v_lshl_add_u32 v28, v52, 2, v28\n"
      "v_lshlrev_b32 v9, 3, v52\n"
      "v_bfe_u32 v10, v43, 3, 2\n"
      "v_lshlrev_b32 v10, 2, v10\n"
      "v_lshl_add_u32 v58, v49, 8, v48\n"
      "s_lshl_b32 s50, s33, 5\n"
      "v_add_u32 v58, s50, v58\n"
      "s_mov_b32 s50, 122912\n"
      "v_lshl_add_u32 v32, v58, 2, s50\n"
      "v_lshlrev_b32 v41, 12, v58\n"
      "v_mul_u32_u24 v42, 0xf000, v49\n"
      "v_lshl_add_u32 v42, v58, 4, v42\n"
      "v_lshl_add_u32 v52, v50, 1, v51\n"
      "v_lshl_add_u32 v33, v52, 4, s54\n"
      "s_add_u32 s50, s54, 528\n"
      "v_lshl_add_u32 v34, v43, 4, s50\n"
      "v_lshl_add_u32 v35, v43, 3, s39\n"
      "v_lshl_add_u32 v36, v43, 3, s52\n"
      "s_add_u32 s50, s39, 32\n"
      "v_lshl_add_u32 v37, v43, 4, s50\n"
      "s_add_u32 s50, s52, 32\n"
      "v_lshl_add_u32 v38, v43, 4, s50\n"
      "v_lshl_add_u32 v52, v50, 2, v51\n"
      "v_lshl_add_u32 v39, v52, 2, s53\n"
      "v_bfe_u32 v52, v43, 2, 2\n"
      "v_mul_u32_u24 v52, 0x2240, v52\n"
      "v_lshl_add_u32 v52, v49, 11, v52\n"
      "v_lshl_add_u32 v52, v55, 3, v52\n"
      "v_and_b32 v54, 16, v43\n"
      "v_lshl_add_u32 v52, v54, 1, v52\n"
      "s_lshl_b32 s50, s33, 6\n"
      "s_add_u32 s50, s50, 56384\n"
      "v_add_u32 v40, s50, v52\n"
      "v_mov_b32 v54, 0\n"
      "v_mov_b32 v55, 0\n"
      "v_lshl_add_u32 v52, v43, 3, s54\n"
      "ds_write_b64 v52, v[54:55] offset:32\n"
      "s_mov_b32 s50, 161536\n"
      "v_lshl_add_u32 v52, v0, 2, s50\n"
      "ds_write_b32 v52, v54 \n"
      "v_mov_b32 v52, 0xffff\n"
      "v_mov_b32 v53, 0xffff0000\n"
      "s_mov_b32 s50, 0x0\n"
      "s_mov_b32 s51, 0x18000018\n"
      "v_cndmask_b32 v44, 0, v52, s[50:51]\n"
      "s_mov_b32 s60, 0x0\n"
      "s_mov_b32 s61, 0x30000030\n"
      "v_cndmask_b32 v54, 0, v53, s[60:61]\n"
      "v_or_b32 v44, v44, v54\n"
      "s_mov_b32 s50, 0x0\n"
      "s_mov_b32 s51, 0x60000060\n"
      "v_cndmask_b32 v45, 0, v52, s[50:51]\n"
      "s_mov_b32 s60, 0x0\n"
      "s_mov_b32 s61, 0xc00000c0\n"
      "v_cndmask_b32 v54, 0, v53, s[60:61]\n"
      "v_or_b32 v45, v45, v54\n"
      "s_mov_b32 s50, 0x10000\n"
      "s_mov_b32 s51, 0x80000180\n"
      "v_cndmask_b32 v46, 0, v52, s[50:51]\n"
      "s_mov_b32 s60, 0x30000\n"
      "s_mov_b32 s61, 0x300\n"
      "v_cndmask_b32 v54, 0, v53, s[60:61]\n"
      "v_or_b32 v46, v46, v54\n"
      "s_mov_b32 s50, 0x60000\n"
      "s_mov_b32 s51, 0x600\n"
      "v_cndmask_b32 v47, 0, v52, s[50:51]\n"
      "s_mov_b32 s60, 0xc0000\n"
      "s_mov_b32 s61, 0xc00\n"
      "v_cndmask_b32 v54, 0, v53, s[60:61]\n"
      "v_or_b32 v47, v47, v54\n"
      ".Lsc%=:\n"
      "s_waitcnt vmcnt(0)\n"
      "s_mov_b32 s50, 0x3fb8aa3b\n"
      "buffer_load_dword v11, v15, s[20:23], s41 offen\n"
      "s_cmp_ge_u32 s33, 4\n"
      "s_cbranch_scc1 .LR%=\n"
      "global_load_dword v11, v57, s[56:57]\n"
      ".LR%=:\n"
      "v_lshlrev_b32 v48, 16, v12\n"
      "v_and_b32 v52, 0x7fff7fff, v18\n"
      "v_and_b32 v53, 0x7fff7fff, v19\n"
      "v_mul_f32 v49, s50, v48\n"
      "v_pk_max_u16 v52, v52, v53\n"
      "v_fma_f32 v50, v48, s50, -v49\n"
      "v_rndne_f32 v51, v49\n"
      "v_lshlrev_b32 v53, 16, v52\n"
      "v_fmamk_f32 v50, v48, 0x32a5705f, v50\n"
      "v_sub_f32 v49, v49, v51\n"
      "v_max_u32 v52, v52, v53\n"
      "v_add_f32 v49, v49, v50\n"
      "v_cvt_i32_f32 v51, v51\n"
      "v_max_u32_dpp v52, v52, v52 row_shr:1 row_mask:0xf bank_mask:0xf bound_ctrl:1\n"
      "v_exp_f32 v49, v49\n"
      "v_lshlrev_b32 v54, 16, v13\n"
      "v_max_u32_dpp v52, v52, v52 row_shr:2 row_mask:0xf bank_mask:0xf bound_ctrl:1\n"
      "v_ldexp_f32 v49, v49, v51\n"
      "v_add_f32 v54, 1.0, v54\n"
      "v_max_u32_dpp v52, v52, v52 row_shr:4 row_mask:0xf bank_mask:0xf bound_ctrl:1\n"
      "v_cvt_pk_bf16_f32 v49, v49, v49\n"
      "s_waitcnt lgkmcnt(0)\n"
      "v_max_u32_dpp v52, v52, v52 row_shr:8 row_mask:0xf bank_mask:0xf bound_ctrl:1\n"
      "v_and_b32 v49, 0xffff0000, v49\n"
      "v_and_b32 v55, 0x7fffffff, v54\n"
      "v_max_u32_dpp v52, v52, v52 row_bcast:15 row_mask:0xa bank_mask:0xf\n"
      "v_mul_f32 v50, 0xbfb8aa3b, v49\n"
      "v_mul_f32 v51, 0xbe22f983, v49\n"
      "v_max_u32_dpp v52, v52, v52 row_bcast:31 row_mask:0xc bank_mask:0xf\n"
      "v_readfirstlane_b32 s36, v50\n"
      "v_readlane_b32 s50, v52, 63\n"
      "v_readfirstlane_b32 s37, v51\n"
      "v_mul_f32 v55, 0x3b800000, v55\n"
      "v_mul_f32 v52, s50, v49\n"
      "s_add_u32 s15, s15, 0x9e3779b1\n"
      "v_max_f32 v52, v52, v55\n"
      "v_frexp_exp_i32_f32 v52, v52\n"
      "s_nop 0\n"
      "v_readfirstlane_b32 s50, v52\n"
      "s_nop 0\n"
      "s_sub_i32 s50, 7, s50\n"
      "s_max_i32 s50, s50, -107\n"
      "s_min_i32 s50, s50, 93\n"
      "v_ldexp_f32 v49, v49, s50\n"
      "s_sub_i32 s51, s50, 1\n"
      "v_ldexp_f32 v54, v54, s51\n"
      "s_sub_i32 s14, 128, s50\n"
      "s_lshl_b32 s14, s14, 23\n"
      "v_readfirstlane_b32 s38, v49\n"
      "s_mov_b64 s[60:61], 1\n"
      "v_cndmask_b32 v31, 0, v54, s[60:61]\n"
      "s_lshl_b32 s50, s33, 2\n"
      "s_add_u32 s50, s50, 161280\n"
      "v_mov_b32 v50, s50\n"
      "v_mov_b32 v51, s14\n"
      "ds_write_b32 v50, v51 \n"
      "s_mov_b32 s60, s41\n"
      "s_add_u32 s61, s41, 0x200000\n"
      "s_mov_b32 s62, 131072\n"
      "s_mov_b32 s63, 262144\n"
      "s_mov_b32 m0, s40\n"
      "s_add_u32 s13, s40, 52224\n"
      "s_add_u32 s12, s39, 272\n"
      "v_lshlrev_b32 v72, 16, v16\n"
      "v_and_b32 v73, 0xffff0000, v16\n"
      "v_lshlrev_b32 v74, 16, v17\n"
      "v_and_b32 v75, 0xffff0000, v17\n"
      "s_mov_b32 s64, 0x3fb8aa3b\n"
      "s_mov_b32 s66, 0x43800000\n"
      "s_mov_b32 s68, 0x42000000\n"
      "s_mov_b32 s70, s37\n"
      "v_pk_mul_f32 v[72:73], s[64:65], v[72:73] op_sel_hi:[0,1]\n"
      "v_pk_mul_f32 v[74:75], s[70:71], v[74:75] op_sel_hi:[0,1]\n"
      "v_exp_f32 v72, v72\n"
      "v_exp_f32 v73, v73\n"
      "v_pk_mul_f32 v[78:79], s[66:67], v[74:75] op_sel_hi:[0,1]\n"
      "v_pk_mul_f32 v[80:81], s[68:69], v[74:75] op_sel_hi:[0,1]\n"
      "v_fract_f32 v76, v74\n"
      "v_fract_f32 v77, v75\n"
      "v_cvt_pk_bf16_f32 v72, v72, v73\n"
      "v_cos_f32 v90, v76\n"
      "v_cos_f32 v91, v77\n"
      "v_fract_f32 v78, v78\n"
      "v_fract_f32 v79, v79\n"
      "v_and_b32 v73, 0xffff0000, v72\n"
      "v_lshlrev_b32 v72, 16, v72\n"
      "v_sin_f32 v92, v76\n"
      "v_sin_f32 v93, v77\n"
      "v_pk_mul_f32 v[82:83], s[36:37], v[72:73] op_sel_hi:[0,1]\n"
      "v_fract_f32 v80, v80\n"
      "v_fract_f32 v81, v81\n"
      "v_exp_f32 v84, v82\n"
      "v_exp_f32 v85, v83\n"
      "v_pk_mul_f32 v[86:87], s[66:67], v[82:83] op_sel_hi:[0,1]\n"
      "v_cos_f32 v94, v78\n"
      "v_cos_f32 v95, v79\n"
      "v_pk_mul_f32 v[88:89], s[68:69], v[82:83] op_sel_hi:[0,1]\n"
      "v_sin_f32 v96, v78\n"
      "v_sin_f32 v97, v79\n"
      "v_pk_mul_f32 v[90:91], v[90:91], v[84:85]\n"
      "v_exp_f32 v86, v86\n"
      "v_exp_f32 v87, v87\n"
      "v_pk_mul_f32 v[92:93], v[92:93], v[84:85]\n"
      "v_pk_add_f32 v[82:83], v[90:91], v[92:93]\n"
      "v_cos_f32 v202, v80\n"
      "v_pk_add_f32 v[84:85], v[90:91], v[92:93] neg_lo:[0,1] neg_hi:[0,1]\n"
      "v_cos_f32 v150, v81\n"
      "v_pk_mul_f32 v[100:101], v[90:91], v[92:93]\n"
      "v_pk_mul_f32 v[94:95], v[94:95], v[86:87]\n"
      "v_pk_mul_f32 v[98:99], v[82:83], v[84:85]\n"
      "v_pk_mul_f32 v[96:97], v[96:97], v[86:87]\n"
      "v_pk_add_f32 v[100:101], v[100:101], v[100:101]\n"
      "v_sin_f32 v203, v80\n"
      "v_cvt_pk_f16_f32 v208, v90, v92\n"
      "v_sin_f32 v151, v81\n"
      "v_cvt_pk_f16_f32 v156, v91, v93\n"
      "v_exp_f32 v88, v88\n"
      "v_pk_add_f32 v[82:83], v[98:99], v[100:101]\n"
      "v_exp_f32 v89, v89\n"
      "v_pk_add_f32 v[84:85], v[98:99], v[100:101] neg_lo:[0,1] neg_hi:[0,1]\n"
      "v_pk_add_f32 v[114:115], v[94:95], v[96:97]\n"
      "v_pk_mul_f32 v[104:105], v[98:99], v[100:101]\n"
      "v_pk_add_f32 v[116:117], v[94:95], v[96:97] neg_lo:[0,1] neg_hi:[0,1]\n"
      "v_pk_mul_f32 v[102:103], v[82:83], v[84:85]\n"
      "v_pk_mul_f32 v[108:109], v[94:95], v[96:97]\n"
      "v_pk_add_f32 v[104:105], v[104:105], v[104:105]\n"
      "v_pk_mul_f32 v[106:107], v[114:115], v[116:117]\n"
      "v_cvt_pk_f16_f32 v209, v98, v100\n"
      "v_pk_add_f32 v[108:109], v[108:109], v[108:109]\n"
      "v_cvt_pk_f16_f32 v157, v99, v101\n"
      "v_cvt_pk_f16_f32 v212, v94, v96\n"
      "v_pk_add_f32 v[82:83], v[102:103], v[104:105]\n"
      "v_cvt_pk_f16_f32 v160, v95, v97\n"
      "v_pk_add_f32 v[84:85], v[102:103], v[104:105] neg_lo:[0,1] neg_hi:[0,1]\n"
      "v_pk_mul_f32 v[202:203], v[202:203], v[88:89] op_sel:[0,0] op_sel_hi:[1,0]\n"
      "v_pk_mul_f32 v[92:93], v[102:103], v[104:105]\n"
      "v_pk_mul_f32 v[150:151], v[150:151], v[88:89] op_sel:[0,1] op_sel_hi:[1,1]\n"
      "v_pk_mul_f32 v[90:91], v[82:83], v[84:85]\n"
      "v_pk_mul_f32 v[110:111], v[108:109], v[96:97]\n"
      "v_pk_add_f32 v[92:93], v[92:93], v[92:93]\n"
      "v_pk_mul_f32 v[112:113], v[108:109], v[94:95]\n"
      "v_cvt_pk_f16_f32 v210, v102, v104\n"
      "v_pk_fma_f32 v[110:111], v[106:107], v[94:95], v[110:111] neg_lo:[0,0,1] neg_hi:[0,0,1]\n"
      "v_cvt_pk_f16_f32 v158, v103, v105\n"
      "v_pk_fma_f32 v[112:113], v[106:107], v[96:97], v[112:113]\n"
      "v_pk_add_f32 v[82:83], v[90:91], v[92:93]\n"
      "v_cvt_pk_f16_f32 v213, v106, v108\n"
      "v_pk_add_f32 v[84:85], v[90:91], v[92:93] neg_lo:[0,1] neg_hi:[0,1]\n"
      "v_cvt_pk_f16_f32 v161, v107, v109\n"
      "v_pk_mul_f32 v[74:75], v[90:91], v[92:93]\n"
      "v_pk_add_f32 v[114:115], v[106:107], v[108:109]\n"
      "v_pk_mul_f32 v[72:73], v[82:83], v[84:85]\n"
      "v_pk_add_f32 v[116:117], v[106:107], v[108:109] neg_lo:[0,1] neg_hi:[0,1]\n"
      "v_pk_add_f32 v[74:75], v[74:75], v[74:75]\n"
      "v_pk_mul_f32 v[96:97], v[106:107], v[108:109]\n"
      "v_cvt_pk_f16_f32 v205, v90, v92\n"
      "v_pk_mul_f32 v[94:95], v[114:115], v[116:117]\n"
      "v_cvt_pk_f16_f32 v153, v91, v93\n"
      "v_pk_add_f32 v[96:97], v[96:97], v[96:97]\n"
      "v_pk_mul_f32 v[82:83], v[74:75], v[92:93]\n"
      "v_cvt_pk_f16_f32 v214, v110, v112\n"
      "v_pk_mul_f32 v[84:85], v[74:75], v[90:91]\n"
      "v_cvt_pk_f16_f32 v162, v111, v113\n"
      "v_pk_fma_f32 v[82:83], v[72:73], v[90:91], v[82:83] neg_lo:[0,0,1] neg_hi:[0,0,1]\n"
      "v_pk_mul_f32 v[76:77], v[100:101], v[104:105]\n"
      "v_pk_fma_f32 v[84:85], v[72:73], v[92:93], v[84:85]\n"
      "v_pk_mul_f32 v[78:79], v[100:101], v[102:103]\n"
      "v_cvt_pk_f16_f32 v206, v72, v74\n"
      "v_pk_fma_f32 v[76:77], v[98:99], v[102:103], v[76:77] neg_lo:[0,0,1] neg_hi:[0,0,1]\n"
      "v_cvt_pk_f16_f32 v154, v73, v75\n"
      "v_pk_fma_f32 v[78:79], v[98:99], v[104:105], v[78:79]\n"
      "v_cvt_pk_f16_f32 v207, v82, v84\n"
      "v_cvt_pk_f16_f32 v215, v94, v96\n"
      "v_cvt_pk_f16_f32 v155, v83, v85\n"
      "v_cvt_pk_f16_f32 v163, v95, v97\n"
      "v_cvt_pk_f16_f32 v211, v76, v78\n"
      "v_cvt_pk_f16_f32 v159, v77, v79\n"
      "v_lshlrev_b32 v198, 16, v18\n"
      "v_lshlrev_b32 v199, 16, v19\n"
      "v_pk_mul_f32 v[198:199], s[38:39], v[198:199] op_sel_hi:[0,1]\n"
      "v_and_b32 v146, 0xffff0000, v18\n"
      "v_and_b32 v147, 0xffff0000, v19\n"
      "v_pk_mul_f32 v[146:147], s[38:39], v[146:147] op_sel_hi:[0,1]\n"
      "v_cvt_pk_f16_f32 v240, v198, v199\n"
      "v_cvt_pk_f16_f32 v241, v146, v147\n"
      "v_pk_mul_f32 v[200:201], v[198:199], v[202:203] op_sel_hi:[1,0]\n"
      "v_pk_mul_f32 v[148:149], v[146:147], v[150:151] op_sel_hi:[1,0]\n"
      "ds_write_addtid_b32 v240 offset:52224\n"
      "ds_write_addtid_b32 v241 offset:55488\n"
      "v_pk_fma_f32 v[200:201], v[198:199], v[202:203], v[200:201] op_sel:[1,1,0] op_sel_hi:[0,1,1] neg_hi:[0,1,0]\n"
      "v_pk_fma_f32 v[148:149], v[146:147], v[150:151], v[148:149] op_sel:[1,1,0] op_sel_hi:[0,1,1] neg_hi:[0,1,0]\n"
      "ds_write_addtid_b32 v205 offset:54672\n"
      "v_cvt_pk_f16_f32 v242, v200, v201\n"
      "v_cvt_pk_f16_f32 v243, v148, v149\n"
      "v_pk_mul_f32 v[198:199], v[200:201], v[202:203] op_sel_hi:[1,0]\n"
      "v_pk_mul_f32 v[146:147], v[148:149], v[150:151] op_sel_hi:[1,0]\n"
      "ds_write_addtid_b32 v242 offset:52496\n"
      "ds_write_addtid_b32 v243 offset:55760\n"
      "v_pk_fma_f32 v[198:199], v[200:201], v[202:203], v[198:199] op_sel:[1,1,0] op_sel_hi:[0,1,1] neg_hi:[0,1,0]\n"
      "v_pk_fma_f32 v[146:147], v[148:149], v[150:151], v[146:147] op_sel:[1,1,0] op_sel_hi:[0,1,1] neg_hi:[0,1,0]\n"
      "ds_write_addtid_b32 v153 offset:57936\n"
      "v_cvt_pk_f16_f32 v240, v198, v199\n"
      "v_cvt_pk_f16_f32 v241, v146, v147\n"
      "v_pk_mul_f32 v[200:201], v[198:199], v[202:203] op_sel_hi:[1,0]\n"
      "v_pk_mul_f32 v[148:149], v[146:147], v[150:151] op_sel_hi:[1,0]\n"
      "ds_write_addtid_b32 v240 offset:52768\n"
      "ds_write_addtid_b32 v241 offset:56032\n"
      "v_pk_fma_f32 v[200:201], v[198:199], v[202:203], v[200:201] op_sel:[1,1,0] op_sel_hi:[0,1,1] neg_hi:[0,1,0]\n"
      "v_pk_fma_f32 v[148:149], v[146:147], v[150:151], v[148:149] op_sel:[1,1,0] op_sel_hi:[0,1,1] neg_hi:[0,1,0]\n"
      "ds_write_addtid_b32 v206 offset:54944\n"
      "v_cvt_pk_f16_f32 v242, v200, v201\n"
      "v_cvt_pk_f16_f32 v243, v148, v149\n"
      "v_pk_mul_f32 v[198:199], v[200:201], v[202:203] op_sel_hi:[1,0]\n"
      "v_pk_mul_f32 v[146:147], v[148:149], v[150:151] op_sel_hi:[1,0]\n"
      "ds_write_addtid_b32 v242 offset:53040\n"
      "ds_write_addtid_b32 v243 offset:56304\n"
      "v_pk_fma_f32 v[198:199], v[200:201], v[202:203], v[198:199] op_sel:[1,1,0] op_sel_hi:[0,1,1] neg_hi:[0,1,0]\n"
      "v_pk_fma_f32 v[146:147], v[148:149], v[150:151], v[146:147] op_sel:[1,1,0] op_sel_hi:[0,1,1] neg_hi:[0,1,0]\n"
      "ds_write_addtid_b32 v154 offset:58208\n"
      "v_cvt_pk_f16_f32 v240, v198, v199\n"
      "v_cvt_pk_f16_f32 v241, v146, v147\n"
      "v_pk_mul_f32 v[200:201], v[198:199], v[202:203] op_sel_hi:[1,0]\n"
      "v_pk_mul_f32 v[148:149], v[146:147], v[150:151] op_sel_hi:[1,0]\n"
      "ds_write_addtid_b32 v240 offset:53312\n"
      "ds_write_addtid_b32 v241 offset:56576\n"
      "v_pk_fma_f32 v[200:201], v[198:199], v[202:203], v[200:201] op_sel:[1,1,0] op_sel_hi:[0,1,1] neg_hi:[0,1,0]\n"
      "v_pk_fma_f32 v[148:149], v[146:147], v[150:151], v[148:149] op_sel:[1,1,0] op_sel_hi:[0,1,1] neg_hi:[0,1,0]\n"
      "ds_write_addtid_b32 v207 offset:55216\n"
      "v_cvt_pk_f16_f32 v242, v200, v201\n"
      "v_cvt_pk_f16_f32 v243, v148, v149\n"
      "v_pk_mul_f32 v[198:199], v[200:201], v[202:203] op_sel_hi:[1,0]\n"
      "v_pk_mul_f32 v[146:147], v[148:149], v[150:151] op_sel_hi:[1,0]\n"
      "ds_write_addtid_b32 v242 offset:53584\n"
      "ds_write_addtid_b32 v243 offset:56848\n"
      "v_pk_fma_f32 v[198:199], v[200:201], v[202:203], v[198:199] op_sel:[1,1,0] op_sel_hi:[0,1,1] neg_hi:[0,1,0]\n"
      "v_pk_fma_f32 v[146:147], v[148:149], v[150:151], v[146:147] op_sel:[1,1,0] op_sel_hi:[0,1,1] neg_hi:[0,1,0]\n"
      "ds_write_addtid_b32 v155 offset:58480\n"
      "v_cvt_pk_f16_f32 v240, v198, v199\n"
      "v_cvt_pk_f16_f32 v241, v146, v147\n"
      "v_pk_mul_f32 v[200:201], v[198:199], v[202:203] op_sel_hi:[1,0]\n"
      "v_pk_mul_f32 v[148:149], v[146:147], v[150:151] op_sel_hi:[1,0]\n"
      "ds_write_addtid_b32 v240 offset:53856\n"
      "ds_write_addtid_b32 v241 offset:57120\n"
      "v_pk_fma_f32 v[200:201], v[198:199], v[202:203], v[200:201] op_sel:[1,1,0] op_sel_hi:[0,1,1] neg_hi:[0,1,0]\n"
      "v_pk_fma_f32 v[148:149], v[146:147], v[150:151], v[148:149] op_sel:[1,1,0] op_sel_hi:[0,1,1] neg_hi:[0,1,0]\n"
      "v_cvt_pk_f16_f32 v242, v200, v201\n"
      "v_cvt_pk_f16_f32 v243, v148, v149\n"
      "ds_write_addtid_b32 v242 offset:54128\n"
      "ds_write_addtid_b32 v243 offset:57392\n"
      "ds_read_b128 v[104:107], v20 offset:0\n"
      "ds_read_b128 v[108:111], v178 offset:0\n"
      "ds_read_b128 v[112:115], v20 offset:32\n"
      "ds_read_b128 v[116:119], v178 offset:32\n"
      "s_mov_b32 m0, s39\n"
      "s_setprio 2\n"
      "v_pk_mul_f16 v240, v209, v208 op_sel_hi:[0,1]\n"
      "v_pk_mul_f16 v241, v210, v208 op_sel_hi:[0,1]\n"
      "v_pk_mul_f16 v242, v211, v208 op_sel_hi:[0,1]\n"
      "ds_write_addtid_b32 v208 offset:272\n"
      "v_pk_fma_f16 v240, v209, v208, v240 op_sel:[1,1,0] op_sel_hi:[1,0,1] neg_lo:[0,1,0]\n"
      "ds_write_addtid_b32 v209 offset:544\n"
      "v_pk_fma_f16 v241, v210, v208, v241 op_sel:[1,1,0] op_sel_hi:[1,0,1] neg_lo:[0,1,0]\n"
      "ds_write_addtid_b32 v240 offset:816\n"
      "v_pk_fma_f16 v242, v211, v208, v242 op_sel:[1,1,0] op_sel_hi:[1,0,1] neg_lo:[0,1,0]\n"
      "ds_write_addtid_b32 v241 offset:1360\n"
      "ds_write_addtid_b32 v242 offset:1904\n"
      "v_pk_mul_f16 v243, v212, v209 op_sel_hi:[0,1]\n"
      "v_pk_mul_f16 v244, v212, v210 op_sel_hi:[0,1]\n"
      "v_pk_mul_f16 v245, v212, v211 op_sel_hi:[0,1]\n"
      "ds_write_addtid_b32 v210 offset:1088\n"
      "v_pk_fma_f16 v243, v212, v209, v243 op_sel:[1,1,0] op_sel_hi:[1,0,1] neg_lo:[0,1,0]\n"
      "ds_write_addtid_b32 v211 offset:1632\n"
      "v_pk_fma_f16 v244, v212, v210, v244 op_sel:[1,1,0] op_sel_hi:[1,0,1] neg_lo:[0,1,0]\n"
      "ds_write_addtid_b32 v243 offset:2992\n"
      "v_pk_fma_f16 v245, v212, v211, v245 op_sel:[1,1,0] op_sel_hi:[1,0,1] neg_lo:[0,1,0]\n"
      "ds_write_addtid_b32 v244 offset:3264\n"
      "ds_write_addtid_b32 v245 offset:4080\n"
      "v_pk_mul_f16 v246, v213, v210 op_sel_hi:[0,1]\n"
      "v_pk_mul_f16 v247, v214, v210 op_sel_hi:[0,1]\n"
      "v_pk_mul_f16 v248, v215, v215 op_sel_hi:[0,1]\n"
      "ds_write_addtid_b32 v212 offset:2176\n"
      "v_pk_fma_f16 v246, v213, v210, v246 op_sel:[1,1,0] op_sel_hi:[1,0,1] neg_lo:[0,1,0]\n"
      "ds_write_addtid_b32 v213 offset:5440\n"
      "v_pk_fma_f16 v247, v214, v210, v247 op_sel:[1,1,0] op_sel_hi:[1,0,1] neg_lo:[0,1,0]\n"
      "ds_write_addtid_b32 v246 offset:5984\n"
      "v_pk_fma_f16 v248, v215, v215, v248 op_sel:[1,1,0] op_sel_hi:[1,0,1] neg_lo:[0,1,0]\n"
      "ds_write_addtid_b32 v247 offset:6256\n"
      "ds_write_addtid_b32 v248 offset:2448\n"
      "v_pk_mul_f16 v249, v215, v212 op_sel_hi:[0,1]\n"
      "v_pk_mul_f16 v250, v215, v213 op_sel_hi:[0,1]\n"
      "v_pk_mul_f16 v251, v215, v214 op_sel_hi:[0,1]\n"
      "ds_write_addtid_b32 v214 offset:5712\n"
      "v_pk_fma_f16 v249, v215, v212, v249 op_sel:[1,1,0] op_sel_hi:[1,0,1] neg_lo:[0,1,0]\n"
      "ds_write_addtid_b32 v215 offset:4352\n"
      "v_pk_fma_f16 v250, v215, v213, v250 op_sel:[1,1,0] op_sel_hi:[1,0,1] neg_lo:[0,1,0]\n"
      "ds_write_addtid_b32 v249 offset:4624\n"
      "v_pk_fma_f16 v251, v215, v214, v251 op_sel:[1,1,0] op_sel_hi:[1,0,1] neg_lo:[0,1,0]\n"
      "ds_write_addtid_b32 v250 offset:4896\n"
      "ds_write_addtid_b32 v251 offset:5168\n"
      "ds_read_b128 v[72:75], v21 offset:0\n"
      "ds_read_b128 v[76:79], v21 offset:32\n"
      "ds_read_b128 v[80:83], v21 offset:64\n"
      "ds_read_b128 v[84:87], v21 offset:96\n"
      "ds_read_b128 v[88:91], v21 offset:128\n"
      "ds_read_b128 v[92:95], v21 offset:160\n"
      "ds_read_b128 v[96:99], v21 offset:192\n"
      "ds_read_b128 v[100:103], v21 offset:224\n"
      "ds_read_b128 v[120:123], v20 offset:64\n"
      "ds_read_b128 v[124:127], v178 offset:64\n"
      "s_waitcnt lgkmcnt(15)\n"
      "v_pk_mul_f16 v164, v104, v108 op_sel_hi:[0,1] neg_hi:[0,1]\n"
      "v_pk_mul_f16 v165, v105, v109 op_sel_hi:[0,1] neg_hi:[0,1]\n"
      "v_pk_mul_f16 v166, v106, v110 op_sel_hi:[0,1] neg_hi:[0,1]\n"
      "v_pk_mul_f16 v167, v107, v111 op_sel_hi:[0,1] neg_hi:[0,1]\n"
      "v_pk_fma_f16 v164, v104, v108, v164 op_sel:[1,1,0] op_sel_hi:[1,0,1]\n"
      "v_pk_fma_f16 v165, v105, v109, v165 op_sel:[1,1,0] op_sel_hi:[1,0,1]\n"
      "v_pk_fma_f16 v166, v106, v110, v166 op_sel:[1,1,0] op_sel_hi:[1,0,1]\n"
      "v_pk_fma_f16 v167, v107, v111, v167 op_sel:[1,1,0] op_sel_hi:[1,0,1]\n"
      "s_waitcnt lgkmcnt(9)\n"
      "s_nop 0\n"
      "v_mfma_f32_32x32x16_f16 v[180:195], v[72:75], v[164:167], 0\n"
      "v_pk_mul_f16 v240, v157, v156 op_sel_hi:[0,1]\n"
      "v_pk_mul_f16 v241, v158, v156 op_sel_hi:[0,1]\n"
      "v_pk_mul_f16 v242, v159, v156 op_sel_hi:[0,1]\n"
      "v_pk_mul_f16 v168, v112, v116 op_sel_hi:[0,1] neg_hi:[0,1]\n"
      "v_pk_mul_f16 v169, v113, v117 op_sel_hi:[0,1] neg_hi:[0,1]\n"
      "v_pk_mul_f16 v170, v114, v118 op_sel_hi:[0,1] neg_hi:[0,1]\n"
      "v_pk_mul_f16 v171, v115, v119 op_sel_hi:[0,1] neg_hi:[0,1]\n"
      "ds_write_addtid_b32 v156 offset:272\n"
      "v_pk_fma_f16 v240, v157, v156, v240 op_sel:[1,1,0] op_sel_hi:[1,0,1] neg_lo:[0,1,0]\n"
      "ds_write_addtid_b32 v157 offset:544\n"
      "v_pk_fma_f16 v168, v112, v116, v168 op_sel:[1,1,0] op_sel_hi:[1,0,1]\n"
      "v_pk_fma_f16 v169, v113, v117, v169 op_sel:[1,1,0] op_sel_hi:[1,0,1]\n"
      "v_pk_fma_f16 v170, v114, v118, v170 op_sel:[1,1,0] op_sel_hi:[1,0,1]\n"
      "v_pk_fma_f16 v171, v115, v119, v171 op_sel:[1,1,0] op_sel_hi:[1,0,1]\n"
      "ds_read_b128 v[104:107], v20 offset:96\n"
      "ds_read_b128 v[108:111], v178 offset:96\n"
      "s_waitcnt lgkmcnt(12)\n"
      "v_mfma_f32_32x32x16_f16 v[180:195], v[76:79], v[168:171], v[180:195]\n"
      "v_pk_fma_f16 v241, v158, v156, v241 op_sel:[1,1,0] op_sel_hi:[1,0,1] neg_lo:[0,1,0]\n"
      "ds_write_addtid_b32 v240 offset:816\n"
      "v_pk_fma_f16 v242, v159, v156, v242 op_sel:[1,1,0] op_sel_hi:[1,0,1] neg_lo:[0,1,0]\n"
      "s_waitcnt lgkmcnt(5)\n"
      "v_pk_mul_f16 v164, v120, v124 op_sel_hi:[0,1] neg_hi:[0,1]\n"
      "v_pk_mul_f16 v165, v121, v125 op_sel_hi:[0,1] neg_hi:[0,1]\n"
      "v_pk_mul_f16 v166, v122, v126 op_sel_hi:[0,1] neg_hi:[0,1]\n"
      "v_pk_mul_f16 v167, v123, v127 op_sel_hi:[0,1] neg_hi:[0,1]\n"
      "ds_write_addtid_b32 v241 offset:1360\n"
      "ds_write_addtid_b32 v242 offset:1904\n"
      "v_pk_mul_f16 v243, v160, v157 op_sel_hi:[0,1]\n"
      "v_pk_fma_f16 v164, v120, v124, v164 op_sel:[1,1,0] op_sel_hi:[1,0,1]\n"
      "v_pk_fma_f16 v165, v121, v125, v165 op_sel:[1,1,0] op_sel_hi:[1,0,1]\n"
      "v_pk_fma_f16 v166, v122, v126, v166 op_sel:[1,1,0] op_sel_hi:[1,0,1]\n"
      "v_pk_fma_f16 v167, v123, v127, v167 op_sel:[1,1,0] op_sel_hi:[1,0,1]\n"
      "ds_read_b128 v[112:115], v20 offset:128\n"
      "ds_read_b128 v[116:119], v178 offset:128\n"
      "v_mfma_f32_32x32x16_f16 v[180:195], v[80:83], v[164:167], v[180:195]\n"
      "v_pk_mul_f16 v244, v160, v158 op_sel_hi:[0,1]\n"
      "v_pk_mul_f16 v245, v160, v159 op_sel_hi:[0,1]\n"
      "ds_write_addtid_b32 v158 offset:1088\n"
      "s_waitcnt lgkmcnt(6)\n"
      "v_pk_mul_f16 v168, v104, v108 op_sel_hi:[0,1] neg_hi:[0,1]\n"
      "v_pk_mul_f16 v169, v105, v109 op_sel_hi:[0,1] neg_hi:[0,1]\n"
      "v_pk_mul_f16 v170, v106, v110 op_sel_hi:[0,1] neg_hi:[0,1]\n"
      "v_pk_mul_f16 v171, v107, v111 op_sel_hi:[0,1] neg_hi:[0,1]\n"
      "v_pk_fma_f16 v243, v160, v157, v243 op_sel:[1,1,0] op_sel_hi:[1,0,1] neg_lo:[0,1,0]\n"
      "ds_write_addtid_b32 v159 offset:1632\n"
      "v_pk_fma_f16 v244, v160, v158, v244 op_sel:[1,1,0] op_sel_hi:[1,0,1] neg_lo:[0,1,0]\n"
      "v_pk_fma_f16 v168, v104, v108, v168 op_sel:[1,1,0] op_sel_hi:[1,0,1]\n"
      "v_pk_fma_f16 v169, v105, v109, v169 op_sel:[1,1,0] op_sel_hi:[1,0,1]\n"
      "v_pk_fma_f16 v170, v106, v110, v170 op_sel:[1,1,0] op_sel_hi:[1,0,1]\n"
      "v_pk_fma_f16 v171, v107, v111, v171 op_sel:[1,1,0] op_sel_hi:[1,0,1]\n"
      "ds_read_b128 v[120:123], v20 offset:160\n"
      "ds_read_b128 v[124:127], v178 offset:160\n"
      "buffer_load_dwordx4 v[48:51], v41, s[20:23], s60 offen nt\n"
      "v_mfma_f32_32x32x16_f16 v[180:195], v[84:87], v[168:171], v[180:195]\n"
      "ds_write_addtid_b32 v243 offset:2992\n"
      "v_pk_fma_f16 v245, v160, v159, v245 op_sel:[1,1,0] op_sel_hi:[1,0,1] neg_lo:[0,1,0]\n"
      "ds_write_addtid_b32 v244 offset:3264\n"
      "s_waitcnt lgkmcnt(6)\n"
      "v_pk_mul_f16 v164, v112, v116 op_sel_hi:[0,1] neg_hi:[0,1]\n"
      "v_pk_mul_f16 v165, v113, v117 op_sel_hi:[0,1] neg_hi:[0,1]\n"
      "v_pk_mul_f16 v166, v114, v118 op_sel_hi:[0,1] neg_hi:[0,1]\n"
      "v_pk_mul_f16 v167, v115, v119 op_sel_hi:[0,1] neg_hi:[0,1]\n"
      "ds_write_addtid_b32 v245 offset:4080\n"
      "v_pk_mul_f16 v246, v161, v158 op_sel_hi:[0,1]\n"
      "v_pk_mul_f16 v247, v162, v158 op_sel_hi:[0,1]\n"
      "v_pk_fma_f16 v164, v112, v116, v164 op_sel:[1,1,0] op_sel_hi:[1,0,1]\n"
      "v_pk_fma_f16 v165, v113, v117, v165 op_sel:[1,1,0] op_sel_hi:[1,0,1]\n"
      "v_pk_fma_f16 v166, v114, v118, v166 op_sel:[1,1,0] op_sel_hi:[1,0,1]\n"
      "v_pk_fma_f16 v167, v115, v119, v167 op_sel:[1,1,0] op_sel_hi:[1,0,1]\n"
      "ds_read_b128 v[104:107], v20 offset:192\n"
      "ds_read_b128 v[108:111], v178 offset:192\n"
      "v_mfma_f32_32x32x16_f16 v[180:195], v[88:91], v[164:167], v[180:195]\n"
      "v_pk_mul_f16 v248, v163, v163 op_sel_hi:[0,1]\n"
      "ds_write_addtid_b32 v160 offset:2176\n"
      "v_pk_fma_f16 v246, v161, v158, v246 op_sel:[1,1,0] op_sel_hi:[1,0,1] neg_lo:[0,1,0]\n"
      "s_waitcnt lgkmcnt(6)\n"
      "v_pk_mul_f16 v168, v120, v124 op_sel_hi:[0,1] neg_hi:[0,1]\n"
      "v_pk_mul_f16 v169, v121, v125 op_sel_hi:[0,1] neg_hi:[0,1]\n"
      "v_pk_mul_f16 v170, v122, v126 op_sel_hi:[0,1] neg_hi:[0,1]\n"
      "v_pk_mul_f16 v171, v123, v127 op_sel_hi:[0,1] neg_hi:[0,1]\n"
      "ds_write_addtid_b32 v161 offset:5440\n"
      "v_pk_fma_f16 v247, v162, v158, v247 op_sel:[1,1,0] op_sel_hi:[1,0,1] neg_lo:[0,1,0]\n"
      "ds_write_addtid_b32 v246 offset:5984\n"
      "v_pk_fma_f16 v168, v120, v124, v168 op_sel:[1,1,0] op_sel_hi:[1,0,1]\n"
      "v_pk_fma_f16 v169, v121, v125, v169 op_sel:[1,1,0] op_sel_hi:[1,0,1]\n"
      "v_pk_fma_f16 v170, v122, v126, v170 op_sel:[1,1,0] op_sel_hi:[1,0,1]\n"
      "v_pk_fma_f16 v171, v123, v127, v171 op_sel:[1,1,0] op_sel_hi:[1,0,1]\n"
      "ds_read_b128 v[112:115], v20 offset:224\n"
      "ds_read_b128 v[116:119], v178 offset:224\n"
      "v_mfma_f32_32x32x16_f16 v[180:195], v[92:95], v[168:171], v[180:195]\n"
      "v_pk_fma_f16 v248, v163, v163, v248 op_sel:[1,1,0] op_sel_hi:[1,0,1] neg_lo:[0,1,0]\n"
      "ds_write_addtid_b32 v247 offset:6256\n"
      "ds_write_addtid_b32 v248 offset:2448\n"
      "s_waitcnt lgkmcnt(7)\n"
      "v_pk_mul_f16 v164, v104, v108 op_sel_hi:[0,1] neg_hi:[0,1]\n"
      "v_pk_mul_f16 v165, v105, v109 op_sel_hi:[0,1] neg_hi:[0,1]\n"
      "v_pk_mul_f16 v166, v106, v110 op_sel_hi:[0,1] neg_hi:[0,1]\n"
      "v_pk_mul_f16 v167, v107, v111 op_sel_hi:[0,1] neg_hi:[0,1]\n"
      "v_pk_mul_f16 v249, v163, v160 op_sel_hi:[0,1]\n"
      "v_pk_mul_f16 v250, v163, v161 op_sel_hi:[0,1]\n"
      "v_pk_mul_f16 v251, v163, v162 op_sel_hi:[0,1]\n"
      "v_pk_fma_f16 v164, v104, v108, v164 op_sel:[1,1,0] op_sel_hi:[1,0,1]\n"
      "v_pk_fma_f16 v165, v105, v109, v165 op_sel:[1,1,0] op_sel_hi:[1,0,1]\n"
      "v_pk_fma_f16 v166, v106, v110, v166 op_sel:[1,1,0] op_sel_hi:[1,0,1]\n"
      "v_pk_fma_f16 v167, v107, v111, v167 op_sel:[1,1,0] op_sel_hi:[1,0,1]\n"
      "ds_read_b128 v[120:123], v20 offset:3264\n"
      "ds_read_b128 v[124:127], v178 offset:3264\n"
      "v_mfma_f32_32x32x16_f16 v[180:195], v[96:99], v[164:167], v[180:195]\n"
      "ds_write_addtid_b32 v162 offset:5712\n"
      "v_pk_fma_f16 v249, v163, v160, v249 op_sel:[1,1,0] op_sel_hi:[1,0,1] neg_lo:[0,1,0]\n"
      "ds_write_addtid_b32 v163 offset:4352\n"
      "s_waitcnt lgkmcnt(6)\n"
      "v_pk_mul_f16 v168, v112, v116 op_sel_hi:[0,1] neg_hi:[0,1]\n"
      "v_pk_mul_f16 v169, v113, v117 op_sel_hi:[0,1] neg_hi:[0,1]\n"
      "v_pk_mul_f16 v170, v114, v118 op_sel_hi:[0,1] neg_hi:[0,1]\n"
      "v_pk_mul_f16 v171, v115, v119 op_sel_hi:[0,1] neg_hi:[0,1]\n"
      "v_pk_fma_f16 v250, v163, v161, v250 op_sel:[1,1,0] op_sel_hi:[1,0,1] neg_lo:[0,1,0]\n"
      "ds_write_addtid_b32 v249 offset:4624\n"
      "v_pk_fma_f16 v251, v163, v162, v251 op_sel:[1,1,0] op_sel_hi:[1,0,1] neg_lo:[0,1,0]\n"
      "v_pk_fma_f16 v168, v112, v116, v168 op_sel:[1,1,0] op_sel_hi:[1,0,1]\n"
      "v_pk_fma_f16 v169, v113, v117, v169 op_sel:[1,1,0] op_sel_hi:[1,0,1]\n"
      "v_pk_fma_f16 v170, v114, v118, v170 op_sel:[1,1,0] op_sel_hi:[1,0,1]\n"
      "v_pk_fma_f16 v171, v115, v119, v171 op_sel:[1,1,0] op_sel_hi:[1,0,1]\n"
      "ds_read_b128 v[104:107], v20 offset:3296\n"
      "ds_read_b128 v[108:111], v178 offset:3296\n"
      "v_mfma_f32_32x32x16_f16 v[180:195], v[100:103], v[168:171], v[180:195]\n"
      "ds_write_addtid_b32 v250 offset:4896\n"
      "ds_write_addtid_b32 v251 offset:5168\n"
      "s_waitcnt lgkmcnt(7)\n"
      "v_pk_mul_f16 v164, v120, v124 op_sel_hi:[0,1] neg_hi:[0,1]\n"
      "v_pk_mul_f16 v165, v121, v125 op_sel_hi:[0,1] neg_hi:[0,1]\n"
      "v_pk_mul_f16 v166, v122, v126 op_sel_hi:[0,1] neg_hi:[0,1]\n"
      "v_pk_mul_f16 v167, v123, v127 op_sel_hi:[0,1] neg_hi:[0,1]\n"
      "ds_read_b128 v[72:75], v21 offset:0\n"
      "ds_read_b128 v[76:79], v21 offset:32\n"
      "ds_read_b128 v[80:83], v21 offset:64\n"
      "ds_read_b128 v[84:87], v21 offset:96\n"
      "ds_read_b128 v[88:91], v21 offset:128\n"
      "ds_read_b128 v[92:95], v21 offset:160\n"
      "ds_read_b128 v[96:99], v21 offset:192\n"
      "ds_read_b128 v[100:103], v21 offset:224\n"
      "v_pk_fma_f16 v164, v120, v124, v164 op_sel:[1,1,0] op_sel_hi:[1,0,1]\n"
      "v_pk_fma_f16 v165, v121, v125, v165 op_sel:[1,1,0] op_sel_hi:[1,0,1]\n"
      "v_pk_fma_f16 v166, v122, v126, v166 op_sel:[1,1,0] op_sel_hi:[1,0,1]\n"
      "v_pk_fma_f16 v167, v123, v127, v167 op_sel:[1,1,0] op_sel_hi:[1,0,1]\n"
      "ds_read_b128 v[112:115], v20 offset:3328\n"
      "ds_read_b128 v[116:119], v178 offset:3328\n"
      "s_setprio 1\n"
      "s_waitcnt lgkmcnt(9)\n"
      "v_mfma_f32_32x32x16_f16 v[180:195], v[72:75], v[164:167], v[180:195]\n"
      "v_pk_mul_f16 v168, v104, v108 op_sel_hi:[0,1] neg_hi:[0,1]\n"
      "v_pk_mul_f16 v169, v105, v109 op_sel_hi:[0,1] neg_hi:[0,1]\n"
      "v_pk_mul_f16 v170, v106, v110 op_sel_hi:[0,1] neg_hi:[0,1]\n"
      "v_pk_mul_f16 v171, v107, v111 op_sel_hi:[0,1] neg_hi:[0,1]\n"
      "v_pk_fma_f16 v168, v104, v108, v168 op_sel:[1,1,0] op_sel_hi:[1,0,1]\n"
      "v_pk_fma_f16 v169, v105, v109, v169 op_sel:[1,1,0] op_sel_hi:[1,0,1]\n"
      "v_pk_fma_f16 v170, v106, v110, v170 op_sel:[1,1,0] op_sel_hi:[1,0,1]\n"
      "v_pk_fma_f16 v171, v107, v111, v171 op_sel:[1,1,0] op_sel_hi:[1,0,1]\n"
      "ds_read_b128 v[120:123], v20 offset:3360\n"
      "ds_read_b128 v[124:127], v178 offset:3360\n"
      "buffer_load_dwordx4 v[52:55], v41, s[20:23], s60 offen offset:2048 nt\n"
      "s_waitcnt lgkmcnt(10)\n"
      "v_mfma_f32_32x32x16_f16 v[180:195], v[76:79], v[168:171], v[180:195]\n"
      "s_waitcnt lgkmcnt(2)\n"
      "v_pk_mul_f16 v164, v112, v116 op_sel_hi:[0,1] neg_hi:[0,1]\n"
      "v_pk_mul_f16 v165, v113, v117 op_sel_hi:[0,1] neg_hi:[0,1]\n"
      "v_pk_mul_f16 v166, v114, v118 op_sel_hi:[0,1] neg_hi:[0,1]\n"
      "v_pk_mul_f16 v167, v115, v119 op_sel_hi:[0,1] neg_hi:[0,1]\n"
      "v_pk_fma_f16 v164, v112, v116, v164 op_sel:[1,1,0] op_sel_hi:[1,0,1]\n"
      "v_pk_fma_f16 v165, v113, v117, v165 op_sel:[1,1,0] op_sel_hi:[1,0,1]\n"
      "v_pk_fma_f16 v166, v114, v118, v166 op_sel:[1,1,0] op_sel_hi:[1,0,1]\n"
      "v_pk_fma_f16 v167, v115, v119, v167 op_sel:[1,1,0] op_sel_hi:[1,0,1]\n"
      "ds_read_b128 v[104:107], v20 offset:3392\n"
      "ds_read_b128 v[108:111], v178 offset:3392\n"
      "v_mfma_f32_32x32x16_f16 v[180:195], v[80:83], v[164:167], v[180:195]\n"
      "s_waitcnt lgkmcnt(2)\n"
      "v_pk_mul_f16 v168, v120, v124 op_sel_hi:[0,1] neg_hi:[0,1]\n"
      "v_pk_mul_f16 v169, v121, v125 op_sel_hi:[0,1] neg_hi:[0,1]\n"
      "v_pk_mul_f16 v170, v122, v126 op_sel_hi:[0,1] neg_hi:[0,1]\n"
      "v_pk_mul_f16 v171, v123, v127 op_sel_hi:[0,1] neg_hi:[0,1]\n"
      "v_pk_fma_f16 v168, v120, v124, v168 op_sel:[1,1,0] op_sel_hi:[1,0,1]\n"
      "v_pk_fma_f16 v169, v121, v125, v169 op_sel:[1,1,0] op_sel_hi:[1,0,1]\n"
      "v_pk_fma_f16 v170, v122, v126, v170 op_sel:[1,1,0] op_sel_hi:[1,0,1]\n"
      "v_pk_fma_f16 v171, v123, v127, v171 op_sel:[1,1,0] op_sel_hi:[1,0,1]\n"
      "ds_read_b128 v[112:115], v20 offset:3424\n"
      "ds_read_b128 v[116:119], v178 offset:3424\n"
      "v_mfma_f32_32x32x16_f16 v[180:195], v[84:87], v[168:171], v[180:195]\n"
      "s_waitcnt lgkmcnt(2)\n"
      "v_pk_mul_f16 v164, v104, v108 op_sel_hi:[0,1] neg_hi:[0,1]\n"
      "v_pk_mul_f16 v165, v105, v109 op_sel_hi:[0,1] neg_hi:[0,1]\n"
      "v_pk_mul_f16 v166, v106, v110 op_sel_hi:[0,1] neg_hi:[0,1]\n"
      "v_pk_mul_f16 v167, v107, v111 op_sel_hi:[0,1] neg_hi:[0,1]\n"
      "v_pk_fma_f16 v164, v104, v108, v164 op_sel:[1,1,0] op_sel_hi:[1,0,1]\n"
      "v_pk_fma_f16 v165, v105, v109, v165 op_sel:[1,1,0] op_sel_hi:[1,0,1]\n"
      "v_pk_fma_f16 v166, v106, v110, v166 op_sel:[1,1,0] op_sel_hi:[1,0,1]\n"
      "v_pk_fma_f16 v167, v107, v111, v167 op_sel:[1,1,0] op_sel_hi:[1,0,1]\n"
      "ds_read_b128 v[120:123], v20 offset:3456\n"
      "ds_read_b128 v[124:127], v178 offset:3456\n"
      "v_mfma_f32_32x32x16_f16 v[180:195], v[88:91], v[164:167], v[180:195]\n"
      "s_waitcnt lgkmcnt(2)\n"
      "v_pk_mul_f16 v168, v112, v116 op_sel_hi:[0,1] neg_hi:[0,1]\n"
      "v_pk_mul_f16 v169, v113, v117 op_sel_hi:[0,1] neg_hi:[0,1]\n"
      "v_pk_mul_f16 v170, v114, v118 op_sel_hi:[0,1] neg_hi:[0,1]\n"
      "v_pk_mul_f16 v171, v115, v119 op_sel_hi:[0,1] neg_hi:[0,1]\n"
      "v_pk_fma_f16 v168, v112, v116, v168 op_sel:[1,1,0] op_sel_hi:[1,0,1]\n"
      "v_pk_fma_f16 v169, v113, v117, v169 op_sel:[1,1,0] op_sel_hi:[1,0,1]\n"
      "v_pk_fma_f16 v170, v114, v118, v170 op_sel:[1,1,0] op_sel_hi:[1,0,1]\n"
      "v_pk_fma_f16 v171, v115, v119, v171 op_sel:[1,1,0] op_sel_hi:[1,0,1]\n"
      "ds_read_b128 v[104:107], v20 offset:3488\n"
      "ds_read_b128 v[108:111], v178 offset:3488\n"
      "v_mov_b32 v140, 0\n"
      "v_mov_b32 v141, 0\n"
      "v_mov_b32 v142, 0\n"
      "v_mov_b32 v143, 0\n"
      "ds_write_b128 v27, v[140:143] \n"
      "ds_write_b32 v28, v140 \n"
      "v_mfma_f32_32x32x16_f16 v[180:195], v[92:95], v[168:171], v[180:195]\n"
      "s_waitcnt lgkmcnt(2)\n"
      "v_pk_mul_f16 v164, v120, v124 op_sel_hi:[0,1] neg_hi:[0,1]\n"
      "v_pk_mul_f16 v165, v121, v125 op_sel_hi:[0,1] neg_hi:[0,1]\n"
      "v_pk_mul_f16 v166, v122, v126 op_sel_hi:[0,1] neg_hi:[0,1]\n"
      "v_pk_mul_f16 v167, v123, v127 op_sel_hi:[0,1] neg_hi:[0,1]\n"
      "v_pk_fma_f16 v164, v120, v124, v164 op_sel:[1,1,0] op_sel_hi:[1,0,1]\n"
      "v_pk_fma_f16 v165, v121, v125, v165 op_sel:[1,1,0] op_sel_hi:[1,0,1]\n"
      "v_pk_fma_f16 v166, v122, v126, v166 op_sel:[1,1,0] op_sel_hi:[1,0,1]\n"
      "v_pk_fma_f16 v167, v123, v127, v167 op_sel:[1,1,0] op_sel_hi:[1,0,1]\n"
      "s_nop 1\n"
      "v_mfma_f32_32x32x16_f16 v[180:195], v[96:99], v[164:167], v[180:195]\n"
      "s_waitcnt lgkmcnt(0)\n"
      "v_pk_mul_f16 v168, v104, v108 op_sel_hi:[0,1] neg_hi:[0,1]\n"
      "v_pk_mul_f16 v169, v105, v109 op_sel_hi:[0,1] neg_hi:[0,1]\n"
      "v_pk_mul_f16 v170, v106, v110 op_sel_hi:[0,1] neg_hi:[0,1]\n"
      "v_pk_mul_f16 v171, v107, v111 op_sel_hi:[0,1] neg_hi:[0,1]\n"
      "v_pk_fma_f16 v168, v104, v108, v168 op_sel:[1,1,0] op_sel_hi:[1,0,1]\n"
      "v_pk_fma_f16 v169, v105, v109, v169 op_sel:[1,1,0] op_sel_hi:[1,0,1]\n"
      "v_pk_fma_f16 v170, v106, v110, v170 op_sel:[1,1,0] op_sel_hi:[1,0,1]\n"
      "v_pk_fma_f16 v171, v107, v111, v171 op_sel:[1,1,0] op_sel_hi:[1,0,1]\n"
      "s_nop 1\n"
      "v_mfma_f32_32x32x16_f16 v[180:195], v[100:103], v[168:171], v[180:195]\n"
      "buffer_load_dword v11, v15, s[20:23], s61 offen\n"
      "s_nop 11\n"
      "v_add_f32 v180, v180, v31\n"
      "buffer_load_dwordx4 v[64:67], v42, s[16:19], s62 offen sc1\n"
      "v_cvt_pk_f16_f32 v222, v187, v186\n"
      "v_cvt_pk_f16_f32 v223, v185, v184\n"
      "v_cvt_pk_f16_f32 v225, v186, v185\n"
      "v_cvt_pk_f16_f32 v226, v188, v189\n"
      "ds_write_b64 v22, v[222:223] offset:2110\n"
      "v_cvt_pk_f16_f32 v228, v192, v192\n"
      "ds_write_b16 v22, v222 \n"
      "v_cvt_pk_f16_f32 v220, v183, v182\n"
      "ds_write_b32 v22, v225 offset:2\n"
      "v_cvt_pk_f16_f32 v221, v181, v180\n"
      "ds_write_b16_d16_hi v22, v223 offset:6\n"
      "v_cvt_pk_f16_f32 v224, v182, v181\n"
      "v_readlane_b32 s64, v226, 0\n"
      "v_readlane_b32 s65, v226, 32\n"
      "v_readlane_b32 s66, v228, 0\n"
      "ds_write_b64 v22, v[220:221] offset:510\n"
      "v_cvt_pk_f16_f32 v227, v190, v191\n"
      "ds_write_b16 v22, v220 offset:2620\n"
      "v_mov_b32 v229, s65\n"
      "ds_write_b32 v22, v224 offset:2622\n"
      "v_writelane_b32 v229, s64, 8\n"
      "v_writelane_b32 v229, s64, 9\n"
      "ds_write_b16_d16_hi v22, v221 offset:2626\n"
      "v_writelane_b32 v229, s66, 10\n"
      "v_writelane_b32 v229, s66, 11\n"
      "s_pack_ll_b32_b16 s64, s64, s64\n"
      "ds_write_b16 v26, v229 \n"
      "v_add_u32 v230, 0x240, v29\n"
      "s_setprio 0\n"
      "v_and_b32 v44, s64, v44\n"
      "v_and_b32 v45, s64, v45\n"
      "v_and_b32 v46, s64, v46\n"
      "v_and_b32 v47, s64, v47\n"
      "ds_write_b16 v23, v226 \n"
      "ds_write_b16 v23, v226 offset:2110\n"
      "ds_write_b16_d16_hi v24, v226 \n"
      "ds_write_b16_d16_hi v24, v226 offset:2110\n"
      "ds_write_b16 v25, v227 offset:32\n"
      "ds_write_b16 v25, v227 offset:2142\n"
      "ds_write_b16_d16_hi v25, v227 \n"
      "ds_write_b16_d16_hi v25, v227 offset:2110\n"
      "ds_read2_b32 v[108:109], v230 offset0:0 offset1:1\n"
      "ds_read2_b32 v[110:111], v230 offset0:2 offset1:3\n"
      "ds_read2_b32 v[76:77], v29 offset0:16 offset1:17\n"
      "ds_read2_b32 v[78:79], v29 offset0:18 offset1:19\n"
      "ds_read2_b32 v[112:113], v230 offset0:16 offset1:17\n"
      "ds_read2_b32 v[114:115], v230 offset0:18 offset1:19\n"
      "ds_read2_b32 v[80:81], v29 offset0:32 offset1:33\n"
      "ds_read2_b32 v[82:83], v29 offset0:34 offset1:35\n"
      "ds_read2_b32 v[116:117], v230 offset0:32 offset1:33\n"
      "ds_read2_b32 v[118:119], v230 offset0:34 offset1:35\n"
      "ds_read2_b32 v[84:85], v29 offset0:48 offset1:49\n"
      "ds_read2_b32 v[86:87], v29 offset0:50 offset1:51\n"
      "s_movk_i32 s59, 0x7fff\n"
      "s_waitcnt vmcnt(0)\n"
      "s_nop 0\n"
      ".Lcsgk%=:\n"
      "v_cmp_ne_u32 s[64:65], s15, v65\n"
      "v_cmp_ne_u32 vcc, s15, v67\n"
      "s_nop 0\n"
      "s_or_b64 vcc, vcc, s[64:65]\n"
      "s_cbranch_scc1 .Lcsgsk%=\n"
      ".Lcsgok%=:\n"
      "v_and_b32 v196, 0xffff0000, v64\n"
      "v_lshlrev_b32 v198, 16, v64\n"
      "v_rcp_f32 v196, v196\n"
      "v_and_b32 v197, 0xffff0000, v66\n"
      "v_lshlrev_b32 v199, 16, v66\n"
      "v_rcp_f32 v197, v197\n"
      "v_mul_f32 v198, -v198, v196\n"
      "v_mul_f32 v199, -v199, v197\n"
      "ds_read2_b32 v[120:121], v230 offset0:48 offset1:49\n"
      "ds_read2_b32 v[122:123], v230 offset0:50 offset1:51\n"
      "v_lshlrev_b32 v204, 16, v48\n"
      "v_lshlrev_b32 v205, 16, v52\n"
      "v_and_b32 v206, 0xffff0000, v48\n"
      "v_and_b32 v207, 0xffff0000, v52\n"
      "v_pk_fma_f32 v[204:205], v[204:205], v[196:197], v[198:199]\n"
      "v_pk_fma_f32 v[206:207], v[206:207], v[196:197], v[198:199]\n"
      "v_cvt_pk_f16_f32 v204, v204, v205\n"
      "v_cvt_pk_f16_f32 v206, v206, v207\n"
      "ds_read2_b32 v[88:89], v29 offset0:64 offset1:65\n"
      "ds_read2_b32 v[90:91], v29 offset0:66 offset1:67\n"
      "ds_write2st64_b32 v32, v204, v206 offset0:0 offset1:19\n"
      "v_lshlrev_b32 v208, 16, v49\n"
      "v_lshlrev_b32 v209, 16, v53\n"
      "v_and_b32 v210, 0xffff0000, v49\n"
      "v_and_b32 v211, 0xffff0000, v53\n"
      "v_pk_fma_f32 v[208:209], v[208:209], v[196:197], v[198:199]\n"
      "v_pk_fma_f32 v[210:211], v[210:211], v[196:197], v[198:199]\n"
      "v_cvt_pk_f16_f32 v208, v208, v209\n"
      "ds_read2_b32 v[124:125], v230 offset0:64 offset1:65\n"
      "ds_read2_b32 v[126:127], v230 offset0:66 offset1:67\n"
      "v_cvt_pk_f16_f32 v210, v210, v211\n"
      "ds_write2st64_b32 v32, v208, v210 offset0:38 offset1:57\n"
      "v_lshlrev_b32 v204, 16, v50\n"
      "v_lshlrev_b32 v205, 16, v54\n"
      "v_and_b32 v206, 0xffff0000, v50\n"
      "v_and_b32 v207, 0xffff0000, v54\n"
      "v_pk_fma_f32 v[204:205], v[204:205], v[196:197], v[198:199]\n"
      "v_pk_fma_f32 v[206:207], v[206:207], v[196:197], v[198:199]\n"
      "ds_read2_b32 v[92:93], v29 offset0:80 offset1:81\n"
      "ds_read2_b32 v[94:95], v29 offset0:82 offset1:83\n"
      "v_cvt_pk_f16_f32 v204, v204, v205\n"
      "v_cvt_pk_f16_f32 v206, v206, v207\n"
      "ds_write2st64_b32 v32, v204, v206 offset0:76 offset1:95\n"
      "v_lshlrev_b32 v208, 16, v51\n"
      "v_lshlrev_b32 v209, 16, v55\n"
      "v_and_b32 v210, 0xffff0000, v51\n"
      "v_and_b32 v211, 0xffff0000, v55\n"
      "v_pk_fma_f32 v[208:209], v[208:209], v[196:197], v[198:199]\n"
      "ds_read2_b32 v[128:129], v230 offset0:80 offset1:81\n"
      "ds_read2_b32 v[130:131], v230 offset0:82 offset1:83\n"
      "v_pk_fma_f32 v[210:211], v[210:211], v[196:197], v[198:199]\n"
      "v_cvt_pk_f16_f32 v208, v208, v209\n"
      "v_cvt_pk_f16_f32 v210, v210, v211\n"
      "ds_write2st64_b32 v32, v208, v210 offset0:114 offset1:133\n"
      "s_mov_b32 s54, s41\n"
      "s_add_u32 s55, s41, 0x100000\n"
      "s_add_u32 s56, s41, 0x200000\n"
      "s_add_u32 s57, s41, 0x300000\n"
      "ds_read2_b32 v[96:97], v29 offset0:96 offset1:97\n"
      "ds_read2_b32 v[98:99], v29 offset0:98 offset1:99\n"
      "s_mov_b32 s58, 0x40000000\n"
      "v_lshrrev_b32 v14, 9, v8\n"
      "v_add_u32 v14, 0x27600, v14\n"
      "s_lshr_b32 s13, s33, 1\n"
      "ds_read2_b32 v[132:133], v230 offset0:96 offset1:97\n"
      "ds_read2_b32 v[134:135], v230 offset0:98 offset1:99\n"
      "ds_read2_b32 v[100:101], v29 offset0:112 offset1:113\n"
      "ds_read2_b32 v[102:103], v29 offset0:114 offset1:115\n"
      "ds_read2_b32 v[136:137], v230 offset0:112 offset1:113\n"
      "ds_read2_b32 v[138:139], v230 offset0:114 offset1:115\n"
      "ds_read2_b32 v[104:105], v29 offset0:128 offset1:129\n"
      "ds_read2_b32 v[106:107], v29 offset0:130 offset1:131\n"
      "ds_read2_b32 v[72:73], v29 offset0:0 offset1:1\n"
      "ds_read2_b32 v[74:75], v29 offset0:2 offset1:3\n"
      "s_waitcnt lgkmcnt(12)\n"
      ".Lccv%=:\n"
      "s_barrier\n"
      "s_setprio 3\n"
      "ds_read_b128 v[216:219], v33 offset:64\n"
      "ds_read_b128 v[220:223], v33 offset:128\n"
      "ds_read_b128 v[224:227], v33 offset:192\n"
      "ds_read_b128 v[212:215], v33 offset:256\n"
      "v_permlane32_swap_b32 v48, v52\n"
      "v_permlane32_swap_b32 v49, v53\n"
      "v_permlane32_swap_b32 v50, v54\n"
      "v_permlane32_swap_b32 v51, v55\n"
      "s_cmp_lg_u32 s33, 0\n"
      "s_cbranch_scc1 .Lca%=\n"
      "v_mov_b32 v16, s15\n"
      "v_mov_b32 v17, s15\n"
      "buffer_store_dwordx2 v[16:17], off, s[16:19], s42 sc1\n"
      ".Lca%=:\n"
      "s_waitcnt lgkmcnt(2)\n"
      "v_mfma_f32_16x16x32_f16 v[180:183], v[108:111], v[216:219], 0\n"
      "v_lshlrev_b32 v29, 2, v43\n"
      "v_add_u32 v29, 0x27600, v29\n"
      "ds_read_b32 v255, v29 \n"
      "ds_read_b128 v[236:239], v34 offset:0\n"
      "ds_read_b128 v[240:243], v34 offset:16\n"
      "v_mfma_f32_16x16x32_f16 v[184:187], v[76:79], v[216:219], 0\n"
      "ds_read2_b32 v[140:141], v30 offset0:16 offset1:17\n"
      "ds_read_b128 v[216:219], v33 offset:320\n"
      "v_mfma_f32_16x16x32_f16 v[180:183], v[112:115], v[220:223], v[180:183]\n"
      "v_mfma_f32_16x16x32_f16 v[184:187], v[80:83], v[220:223], v[184:187]\n"
      "ds_read2_b32 v[142:143], v30 offset0:64 offset1:65\n"
      "ds_read_b128 v[220:223], v33 offset:384\n"
      "s_waitcnt lgkmcnt(7)\n"
      "v_mfma_f32_16x16x32_f16 v[180:183], v[116:119], v[224:227], v[180:183]\n"
      "s_waitcnt lgkmcnt(5)\n"
      "v_alignbit_b32 v245, v238, v237, 16\n"
      "v_alignbit_b32 v246, v239, v238, 16\n"
      "v_mfma_f32_16x16x32_f16 v[184:187], v[84:87], v[224:227], v[184:187]\n"
      "ds_read2_b32 v[144:145], v30 offset0:8 offset1:9\n"
      "ds_read_b128 v[224:227], v33 offset:448\n"
      "s_waitcnt lgkmcnt(4)\n"
      "v_alignbit_b32 v247, v240, v239, 16\n"
      "v_alignbit_b32 v248, v241, v240, 16\n"
      "v_mfma_f32_16x16x32_f16 v[180:183], v[120:123], v[212:215], v[180:183]\n"
      "v_alignbit_b32 v249, v242, v241, 16\n"
      "v_pk_add_f16 v16, v245, v246\n"
      "v_mfma_f32_16x16x32_f16 v[184:187], v[88:91], v[212:215], v[184:187]\n"
      "ds_read2_b32 v[146:147], v30 offset0:56 offset1:57\n"
      "ds_read_b128 v[212:215], v33 offset:512\n"
      "v_pk_add_f16 v17, v246, v247\n"
      "v_pk_add_f16 v18, v247, v248\n"
      "v_mfma_f32_16x16x32_f16 v[180:183], v[124:127], v[216:219], v[180:183]\n"
      "v_pk_add_f16 v19, v248, v249\n"
      "v_pk_add_f16 v20, v16, v238\n"
      "v_mfma_f32_16x16x32_f16 v[184:187], v[92:95], v[216:219], v[184:187]\n"
      "ds_read2_b32 v[148:149], v30 offset0:0 offset1:1\n"
      "v_pk_add_f16 v21, v17, v239\n"
      "v_pk_add_f16 v22, v18, v240\n"
      "s_waitcnt lgkmcnt(3)\n"
      "v_mfma_f32_16x16x32_f16 v[180:183], v[128:131], v[220:223], v[180:183]\n"
      "v_pk_add_f16 v23, v19, v241\n"
      "ds_write_b128 v37, v[20:23] offset:0\n"
      "v_pk_add_f16 v16, v237, v239\n"
      "v_mfma_f32_16x16x32_f16 v[184:187], v[96:99], v[220:223], v[184:187]\n"
      "ds_read2_b32 v[150:151], v30 offset0:48 offset1:49\n"
      "ds_read_b128 v[216:219], v33 offset:576\n"
      "v_pk_add_f16 v17, v238, v240\n"
      "v_pk_add_f16 v18, v239, v241\n"
      "v_mfma_f32_16x16x32_f16 v[180:183], v[132:135], v[224:227], v[180:183]\n"
      "v_pk_add_f16 v19, v240, v242\n"
      "v_pk_add_f16 v20, v20, v16\n"
      "v_mfma_f32_16x16x32_f16 v[184:187], v[100:103], v[224:227], v[184:187]\n"
      "ds_read2_b32 v[152:153], v30 offset0:40 offset1:41\n"
      "ds_read_b128 v[220:223], v33 offset:640\n"
      "v_pk_add_f16 v21, v21, v17\n"
      "v_pk_add_f16 v22, v22, v18\n"
      "s_waitcnt lgkmcnt(4)\n"
      "v_mfma_f32_16x16x32_f16 v[180:183], v[136:139], v[212:215], v[180:183]\n"
      "v_pk_add_f16 v23, v23, v19\n"
      "ds_write_b128 v37, v[20:23] offset:3104\n"
      "v_alignbit_b32 v244, v237, v236, 16\n"
      "v_mfma_f32_16x16x32_f16 v[184:187], v[104:107], v[212:215], v[184:187]\n"
      "ds_read2_b32 v[154:155], v30 offset0:88 offset1:89\n"
      "ds_read_b128 v[224:227], v33 offset:704\n"
      "v_alignbit_b32 v250, v243, v242, 16\n"
      "v_pk_add_f16 v16, v244, v247\n"
      "v_mfma_f32_16x16x32_f16 v[188:191], v[72:75], v[212:215], 0\n"
      "v_pk_add_f16 v17, v245, v248\n"
      "v_pk_add_f16 v18, v246, v249\n"
      "v_mfma_f32_16x16x32_f16 v[196:199], v[44:47], v[212:215], 0\n"
      "ds_read2_b32 v[156:157], v30 offset0:32 offset1:33\n"
      "ds_read_b128 v[212:215], v33 offset:768\n"
      "v_pk_add_f16 v19, v247, v250\n"
      "v_pk_add_f16 v20, v20, v16\n"
      "s_waitcnt lgkmcnt(4)\n"
      "v_mfma_f32_16x16x32_f16 v[184:187], v[108:111], v[216:219], v[184:187]\n"
      "v_cvt_pk_bf16_f32 v24, v180, v182\n"
      "v_cvt_pk_bf16_f32 v25, v181, v183\n"
      "ds_write2st64_b32 v39, v24, v25 offset0:0 offset1:8\n"
      "v_mfma_f32_16x16x32_f16 v[188:191], v[76:79], v[216:219], v[188:191]\n"
      "ds_read2_b32 v[158:159], v30 offset0:80 offset1:81\n"
      "ds_read_b128 v[216:219], v33 offset:832\n"
      "v_pk_add_f16 v21, v21, v17\n"
      "v_pk_add_f16 v22, v22, v18\n"
      "v_mfma_f32_16x16x32_f16 v[184:187], v[112:115], v[220:223], v[184:187]\n"
      "buffer_load_dwordx4 v[56:59], v41, s[20:23], s61 offen nt\n"
      "buffer_load_dwordx4 v[68:71], v42, s[16:19], s63 offen sc1\n"
      "v_pk_add_f16 v23, v23, v19\n"
      "ds_write_b128 v38, v[20:23] offset:0\n"
      "v_pk_add_f16 v16, v236, v240\n"
      "v_mfma_f32_16x16x32_f16 v[188:191], v[80:83], v[220:223], v[188:191]\n"
      "ds_read2_b32 v[160:161], v30 offset0:24 offset1:25\n"
      "ds_read_b128 v[220:223], v33 offset:896\n"
      "v_pk_add_f16 v17, v237, v241\n"
      "v_pk_add_f16 v18, v238, v242\n"
      "s_waitcnt lgkmcnt(3)\n"
      "v_mfma_f32_16x16x32_f16 v[184:187], v[116:119], v[224:227], v[184:187]\n"
      "v_pk_add_f16 v19, v239, v243\n"
      "v_pk_add_f16 v20, v20, v16\n"
      "s_setprio 2\n"
      "v_mfma_f32_16x16x32_f16 v[188:191], v[84:87], v[224:227], v[188:191]\n"
      "ds_read2_b32 v[162:163], v30 offset0:72 offset1:73\n"
      "ds_read_b128 v[224:227], v33 offset:960\n"
      "v_pk_add_f16 v21, v21, v17\n"
      "v_pk_add_f16 v22, v22, v18\n"
      "ds_read_b128 v[236:239], v34 offset:1024\n"
      "ds_read_b128 v[240:243], v34 offset:1040\n"
      "v_mfma_f32_16x16x32_f16 v[184:187], v[120:123], v[212:215], v[184:187]\n"
      "v_pk_add_f16 v23, v23, v19\n"
      "ds_write_b128 v38, v[20:23] offset:2080\n"
      "v_mfma_f32_16x16x32_f16 v[188:191], v[88:91], v[212:215], v[188:191]\n"
      "ds_read2_b32 v[164:165], v30 offset0:128 offset1:129\n"
      "ds_read_b128 v[212:215], v33 offset:1024\n"
      "v_mfma_f32_16x16x32_f16 v[184:187], v[124:127], v[216:219], v[184:187]\n"
      "v_mfma_f32_16x16x32_f16 v[188:191], v[92:95], v[216:219], v[188:191]\n"
      "s_waitcnt lgkmcnt(4)\n"
      "v_alignbit_b32 v245, v238, v237, 16\n"
      "v_alignbit_b32 v246, v239, v238, 16\n"
      "v_mfma_f32_16x16x32_f16 v[184:187], v[128:131], v[220:223], v[184:187]\n"
      "s_waitcnt lgkmcnt(3)\n"
      "v_alignbit_b32 v247, v240, v239, 16\n"
      "v_alignbit_b32 v248, v241, v240, 16\n"
      "v_mfma_f32_16x16x32_f16 v[188:191], v[96:99], v[220:223], v[188:191]\n"
      "ds_read2_b32 v[166:167], v30 offset0:168 offset1:169\n"
      "ds_read_b64_tr_b16 v[228:229], v35 offset:32\n"
      "ds_read_b64_tr_b16 v[230:231], v35 offset:16\n"
      "v_alignbit_b32 v249, v242, v241, 16\n"
      "v_pk_add_f16 v16, v245, v246\n"
      "v_mfma_f32_16x16x32_f16 v[184:187], v[132:135], v[224:227], v[184:187]\n"
      "v_pk_add_f16 v17, v246, v247\n"
      "v_pk_add_f16 v18, v247, v248\n"
      "v_mfma_f32_16x16x32_f16 v[188:191], v[100:103], v[224:227], v[188:191]\n"
      "v_pk_add_f16 v19, v248, v249\n"
      "v_pk_add_f16 v20, v16, v238\n"
      "s_waitcnt lgkmcnt(3)\n"
      "v_mfma_f32_16x16x32_f16 v[184:187], v[136:139], v[212:215], v[184:187]\n"
      "ds_read_b128 v[216:219], v33 offset:1088\n"
      "buffer_load_dwordx4 v[60:63], v41, s[20:23], s61 offen offset:2048 nt\n"
      "v_pk_add_f16 v21, v17, v239\n"
      "v_pk_add_f16 v22, v18, v240\n"
      "v_mfma_f32_16x16x32_f16 v[188:191], v[104:107], v[212:215], v[188:191]\n"
      "ds_read2_b32 v[168:169], v30 offset0:120 offset1:121\n"
      "v_pk_add_f16 v23, v19, v241\n"
      "ds_write_b128 v37, v[20:23] offset:1024\n"
      "v_pk_add_f16 v16, v237, v239\n"
      "v_mfma_f32_16x16x32_f16 v[192:195], v[72:75], v[212:215], 0\n"
      "ds_read_b128 v[220:223], v33 offset:1152\n"
      "v_pk_add_f16 v17, v238, v240\n"
      "v_pk_add_f16 v18, v239, v241\n"
      "v_mfma_f32_16x16x32_f16 v[200:203], v[44:47], v[212:215], 0\n"
      "v_pk_add_f16 v19, v240, v242\n"
      "v_pk_add_f16 v20, v20, v16\n"
      "s_waitcnt lgkmcnt(3)\n"
      "v_mfma_f32_16x16x32_f16 v[188:191], v[228:231], v[140:143], v[188:191]\n"
      "ds_read_b128 v[224:227], v33 offset:1216\n"
      "v_cvt_pk_bf16_f32 v24, v184, v186\n"
      "v_cvt_pk_bf16_f32 v25, v185, v187\n"
      "ds_write2st64_b32 v39, v24, v25 offset0:1 offset1:9\n"
      "v_mfma_f32_16x16x32_f16 v[192:195], v[228:231], v[144:147], v[192:195]\n"
      "ds_read2_b32 v[170:171], v30 offset0:160 offset1:161\n"
      "v_pk_add_f16 v21, v21, v17\n"
      "v_pk_add_f16 v22, v22, v18\n"
      "v_mfma_f32_16x16x32_f16 v[196:199], v[228:231], v[148:151], v[196:199]\n"
      "ds_read_b64_tr_b16 v[232:233], v35 offset:3128\n"
      "ds_read_b64_tr_b16 v[234:235], v35 offset:3112\n"
      "v_pk_add_f16 v23, v23, v19\n"
      "ds_write_b128 v37, v[20:23] offset:4128\n"
      "v_alignbit_b32 v244, v237, v236, 16\n"
      "v_mfma_f32_16x16x32_f16 v[188:191], v[108:111], v[216:219], v[188:191]\n"
      "v_alignbit_b32 v250, v243, v242, 16\n"
      "v_pk_add_f16 v16, v244, v247\n"
      "v_mfma_f32_16x16x32_f16 v[192:195], v[76:79], v[216:219], v[192:195]\n"
      "v_pk_add_f16 v17, v245, v248\n"
      "v_pk_add_f16 v18, v246, v249\n"
      "s_waitcnt lgkmcnt(3)\n"
      "v_mfma_f32_16x16x32_f16 v[188:191], v[112:115], v[220:223], v[188:191]\n"
      "ds_read2_b32 v[172:173], v30 offset0:112 offset1:113\n"
      "ds_read_b128 v[212:215], v33 offset:1280\n"
      "v_pk_add_f16 v19, v247, v250\n"
      "v_pk_add_f16 v20, v20, v16\n"
      "v_mfma_f32_16x16x32_f16 v[192:195], v[80:83], v[220:223], v[192:195]\n"
      "v_pk_add_f16 v21, v21, v17\n"
      "v_pk_add_f16 v22, v22, v18\n"
      "v_mfma_f32_16x16x32_f16 v[188:191], v[116:119], v[224:227], v[188:191]\n"
      "ds_read_b128 v[216:219], v33 offset:1344\n"
      "v_pk_add_f16 v23, v23, v19\n"
      "ds_write_b128 v38, v[20:23] offset:1024\n"
      "v_pk_add_f16 v16, v236, v240\n"
      "v_mfma_f32_16x16x32_f16 v[192:195], v[84:87], v[224:227], v[192:195]\n"
      "v_pk_add_f16 v17, v237, v241\n"
      "v_pk_add_f16 v18, v238, v242\n"
      "s_setprio 1\n"
      "s_waitcnt lgkmcnt(2)\n"
      "v_mfma_f32_16x16x32_f16 v[188:191], v[232:235], v[152:155], v[188:191]\n"
      "ds_read2_b32 v[174:175], v30 offset0:152 offset1:153\n"
      "ds_read_b128 v[220:223], v33 offset:1408\n"
      "v_pk_add_f16 v19, v239, v243\n"
      "v_pk_add_f16 v20, v20, v16\n"
      "v_mfma_f32_16x16x32_f16 v[192:195], v[232:235], v[156:159], v[192:195]\n"
      "v_pk_add_f16 v21, v21, v17\n"
      "v_pk_add_f16 v22, v22, v18\n"
      "v_mfma_f32_16x16x32_f16 v[196:199], v[232:235], v[160:163], v[196:199]\n"
      "ds_read_b128 v[224:227], v33 offset:1472\n"
      "v_pk_add_f16 v23, v23, v19\n"
      "ds_write_b128 v38, v[20:23] offset:3104\n"
      "v_mfma_f32_16x16x32_f16 v[188:191], v[120:123], v[212:215], v[188:191]\n"
      "v_readlane_b32 s72, v255, 0\n"
      "v_mfma_f32_16x16x32_f16 v[192:195], v[88:91], v[212:215], v[192:195]\n"
      "ds_read2_b32 v[176:177], v30 offset0:104 offset1:105\n"
      "ds_read_b128 v[212:215], v33 offset:1536\n"
      "v_readlane_b32 s73, v255, 1\n"
      "s_waitcnt lgkmcnt(4)\n"
      "v_mfma_f32_16x16x32_f16 v[188:191], v[124:127], v[216:219], v[188:191]\n"
      "v_readlane_b32 s74, v255, 2\n"
      "v_mfma_f32_16x16x32_f16 v[192:195], v[92:95], v[216:219], v[192:195]\n"
      "v_readlane_b32 s75, v255, 3\n"
      "v_mfma_f32_16x16x32_f16 v[188:191], v[128:131], v[220:223], v[188:191]\n"
      "v_readlane_b32 s76, v255, 4\n"
      "v_mfma_f32_16x16x32_f16 v[192:195], v[96:99], v[220:223], v[192:195]\n"
      "ds_read2_b32 v[178:179], v30 offset0:144 offset1:145\n"
      "ds_read_b64_tr_b16 v[228:229], v35 offset:544\n"
      "ds_read_b64_tr_b16 v[230:231], v35 offset:528\n"
      "v_readlane_b32 s77, v255, 5\n"
      "s_waitcnt lgkmcnt(3)\n"
      "v_mfma_f32_16x16x32_f16 v[188:191], v[132:135], v[224:227], v[188:191]\n"
      "v_readlane_b32 s78, v255, 6\n"
      "v_mfma_f32_16x16x32_f16 v[192:195], v[100:103], v[224:227], v[192:195]\n"
      "v_readlane_b32 s79, v255, 7\n"
      "v_mfma_f32_16x16x32_f16 v[188:191], v[136:139], v[212:215], v[188:191]\n"
      "ds_read_b128 v[216:219], v33 offset:1600\n"
      "s_movk_i32 s59, 0x7fff\n"
      "s_waitcnt vmcnt(1)\n"
      "s_nop 0\n"
      ".Lcsgh%=:\n"
      "v_cmp_ne_u32 s[64:65], s15, v69\n"
      "v_cmp_ne_u32 vcc, s15, v71\n"
      "s_nop 0\n"
      "s_or_b64 vcc, vcc, s[64:65]\n"
      "s_cbranch_scc1 .Lcsgsh%=\n"
      ".Lcsgoh%=:\n"
      "v_mov_b32 v42, v8\n"
      "v_mfma_f32_16x16x32_f16 v[192:195], v[104:107], v[212:215], v[192:195]\n"
      "v_and_b32 v26, 0xffff0000, v68\n"
      "v_lshlrev_b32 v28, 16, v68\n"
      "v_mfma_f32_16x16x32_f16 v[196:199], v[72:75], v[212:215], v[196:199]\n"
      "ds_read_b128 v[220:223], v33 offset:1664\n"
      "v_rcp_f32 v26, v26\n"
      "v_and_b32 v27, 0xffff0000, v70\n"
      "v_mfma_f32_16x16x32_f16 v[204:207], v[44:47], v[212:215], 0\n"
      "v_lshlrev_b32 v29, 16, v70\n"
      "v_rcp_f32 v27, v27\n"
      "s_waitcnt lgkmcnt(1)\n"
      "v_mfma_f32_16x16x32_f16 v[192:195], v[228:231], v[140:143], v[192:195]\n"
      "ds_read_b128 v[224:227], v33 offset:1728\n"
      "v_cvt_pk_bf16_f32 v24, v188, v190\n"
      "v_cvt_pk_bf16_f32 v25, v189, v191\n"
      "ds_write2st64_b32 v39, v24, v25 offset0:2 offset1:10\n"
      "v_mfma_f32_16x16x32_f16 v[196:199], v[228:231], v[144:147], v[196:199]\n"
      "v_mul_f32 v28, -v28, v26\n"
      "v_mul_f32 v29, -v29, v27\n"
      "v_mfma_f32_16x16x32_f16 v[200:203], v[228:231], v[148:151], v[200:203]\n"
      "ds_read_b64_tr_b16 v[232:233], v35 offset:3640\n"
      "ds_read_b64_tr_b16 v[234:235], v35 offset:3624\n"
      "v_lshlrev_b32 v180, 16, v56\n"
      "s_waitcnt vmcnt(0)\n"
      "v_lshlrev_b32 v181, 16, v60\n"
      "v_mfma_f32_16x16x32_f16 v[192:195], v[108:111], v[216:219], v[192:195]\n"
      "v_and_b32 v182, 0xffff0000, v56\n"
      "v_and_b32 v183, 0xffff0000, v60\n"
      "v_mfma_f32_16x16x32_f16 v[196:199], v[76:79], v[216:219], v[196:199]\n"
      "v_fma_f32 v180, v180, v26, v28\n"
      "v_fma_f32 v181, v181, v27, v29\n"
      "s_setprio 0\n"
      "s_waitcnt lgkmcnt(2)\n"
      "v_mfma_f32_16x16x32_f16 v[192:195], v[112:115], v[220:223], v[192:195]\n"
      "ds_read_b128 v[212:215], v33 offset:1792\n"
      "v_fma_f32 v182, v182, v26, v28\n"
      "v_fma_f32 v183, v183, v27, v29\n"
      "v_mfma_f32_16x16x32_f16 v[196:199], v[80:83], v[220:223], v[196:199]\n"
      "v_cvt_pk_f16_f32 v180, v180, v181\n"
      "v_cvt_pk_f16_f32 v182, v182, v183\n"
      "ds_write2st64_b32 v32, v180, v182 offset0:8 offset1:27\n"
      "v_mfma_f32_16x16x32_f16 v[192:195], v[116:119], v[224:227], v[192:195]\n"
      "ds_read_b128 v[216:219], v33 offset:1856\n"
      "v_lshlrev_b32 v184, 16, v57\n"
      "v_lshlrev_b32 v185, 16, v61\n"
      "v_mfma_f32_16x16x32_f16 v[196:199], v[84:87], v[224:227], v[196:199]\n"
      "v_and_b32 v186, 0xffff0000, v57\n"
      "v_and_b32 v187, 0xffff0000, v61\n"
      "s_waitcnt lgkmcnt(2)\n"
      "v_mfma_f32_16x16x32_f16 v[192:195], v[232:235], v[152:155], v[192:195]\n"
      "ds_read_b128 v[220:223], v33 offset:1920\n"
      "v_fma_f32 v184, v184, v26, v28\n"
      "v_fma_f32 v185, v185, v27, v29\n"
      "v_mfma_f32_16x16x32_f16 v[196:199], v[232:235], v[156:159], v[196:199]\n"
      "v_fma_f32 v186, v186, v26, v28\n"
      "v_fma_f32 v187, v187, v27, v29\n"
      "s_cmp_lg_u32 s33, 0\n"
      "s_cbranch_scc1 .Lcg%=\n"
      "buffer_load_dwordx2 v[252:253], v9, s[16:19], s43 offen sc1\n"
      ".Lcg%=:\n"
      "v_mfma_f32_16x16x32_f16 v[200:203], v[232:235], v[160:163], v[200:203]\n"
      "ds_read_b128 v[224:227], v33 offset:1984\n"
      "v_cvt_pk_f16_f32 v184, v184, v185\n"
      "v_cvt_pk_f16_f32 v186, v186, v187\n"
      "ds_write2st64_b32 v32, v184, v186 offset0:46 offset1:65\n"
      "v_mfma_f32_16x16x32_f16 v[192:195], v[120:123], v[212:215], v[192:195]\n"
      "v_lshlrev_b32 v180, 16, v58\n"
      "v_lshlrev_b32 v181, 16, v62\n"
      "v_mfma_f32_16x16x32_f16 v[196:199], v[88:91], v[212:215], v[196:199]\n"
      "ds_read_b128 v[212:215], v33 offset:2048\n"
      "v_and_b32 v182, 0xffff0000, v58\n"
      "v_and_b32 v183, 0xffff0000, v62\n"
      "s_waitcnt lgkmcnt(3)\n"
      "v_mfma_f32_16x16x32_f16 v[192:195], v[124:127], v[216:219], v[192:195]\n"
      "v_fma_f32 v180, v180, v26, v28\n"
      "v_fma_f32 v181, v181, v27, v29\n"
      "v_mfma_f32_16x16x32_f16 v[196:199], v[92:95], v[216:219], v[196:199]\n"
      "v_fma_f32 v182, v182, v26, v28\n"
      "v_fma_f32 v183, v183, v27, v29\n"
      "v_mfma_f32_16x16x32_f16 v[192:195], v[128:131], v[220:223], v[192:195]\n"
      "v_cvt_pk_f16_f32 v180, v180, v181\n"
      "v_cvt_pk_f16_f32 v182, v182, v183\n"
      "ds_write2st64_b32 v32, v180, v182 offset0:84 offset1:103\n"
      "v_mfma_f32_16x16x32_f16 v[196:199], v[96:99], v[220:223], v[196:199]\n"
      "ds_read_b64_tr_b16 v[228:229], v36 offset:24\n"
      "ds_read_b64_tr_b16 v[230:231], v36 offset:2088\n"
      "v_lshlrev_b32 v184, 16, v59\n"
      "v_lshlrev_b32 v185, 16, v63\n"
      "s_waitcnt lgkmcnt(3)\n"
      "v_mfma_f32_16x16x32_f16 v[192:195], v[132:135], v[224:227], v[192:195]\n"
      "v_and_b32 v186, 0xffff0000, v59\n"
      "v_and_b32 v187, 0xffff0000, v63\n"
      "v_mfma_f32_16x16x32_f16 v[196:199], v[100:103], v[224:227], v[196:199]\n"
      "v_fma_f32 v184, v184, v26, v28\n"
      "v_fma_f32 v185, v185, v27, v29\n"
      "v_mfma_f32_16x16x32_f16 v[192:195], v[136:139], v[212:215], v[192:195]\n"
      "v_fma_f32 v186, v186, v26, v28\n"
      "v_fma_f32 v187, v187, v27, v29\n"
      "v_mfma_f32_16x16x32_f16 v[196:199], v[104:107], v[212:215], v[196:199]\n"
      "ds_read_b64_tr_b16 v[232:233], v36 offset:536\n"
      "ds_read_b64_tr_b16 v[234:235], v36 offset:2600\n"
      "v_cvt_pk_f16_f32 v184, v184, v185\n"
      "v_cvt_pk_f16_f32 v186, v186, v187\n"
      "ds_write2st64_b32 v32, v184, v186 offset0:122 offset1:141\n"
      "v_mfma_f32_16x16x32_f16 v[200:203], v[72:75], v[212:215], v[200:203]\n"
      "v_permlane32_swap_b32 v56, v60\n"
      "v_permlane32_swap_b32 v57, v61\n"
      "v_mfma_f32_16x16x32_f16 v[208:211], v[44:47], v[212:215], 0\n"
      "v_permlane32_swap_b32 v58, v62\n"
      "v_permlane32_swap_b32 v59, v63\n"
      "s_waitcnt lgkmcnt(3)\n"
      "v_mfma_f32_16x16x32_f16 v[196:199], v[228:231], v[164:167], v[196:199]\n"
      "v_cvt_pk_bf16_f32 v24, v192, v194\n"
      "v_cvt_pk_bf16_f32 v25, v193, v195\n"
      "ds_write2st64_b32 v39, v24, v25 offset0:3 offset1:11\n"
      "v_mfma_f32_16x16x32_f16 v[200:203], v[228:231], v[168:171], v[200:203]\n"
      "v_mfma_f32_16x16x32_f16 v[204:207], v[228:231], v[172:175], v[204:207]\n"
      "v_mfma_f32_16x16x32_f16 v[208:211], v[228:231], v[176:179], v[208:211]\n"
      "ds_read_b64_tr_b16 v[228:229], v35 offset:1056\n"
      "ds_read_b64_tr_b16 v[230:231], v35 offset:1040\n"
      "s_cmp_eq_u32 s33, 0\n"
      "s_cbranch_scc1 .Lch%=\n"
      ".Lcho%=:\n"
      "s_waitcnt lgkmcnt(2)\n"
      "s_barrier\n"
      "s_setprio 3\n"
      "v_mfma_f32_16x16x32_f16 v[200:203], v[232:235], v[164:167], v[200:203]\n"
      "ds_read_b64_tr_b16 v[64:65], v40 offset:0\n"
      "ds_read_b64_tr_b16 v[66:67], v40 offset:35072\n"
      "v_lshlrev_b32 v68, 16, v48\n"
      "v_and_b32 v69, 0xffff0000, v48\n"
      "v_mfma_f32_16x16x32_f16 v[204:207], v[232:235], v[168:171], v[204:207]\n"
      "v_lshlrev_b32 v70, 16, v49\n"
      "v_and_b32 v71, 0xffff0000, v49\n"
      "ds_read_b128 v[236:239], v34 offset:2048\n"
      "ds_read_b128 v[240:243], v34 offset:2064\n"
      "ds_read_b32 v12, v14 offset:256\n"
      "v_mfma_f32_16x16x32_f16 v[208:211], v[232:235], v[172:175], v[208:211]\n"
      "ds_read_b128 v[216:219], v33 offset:2112\n"
      "ds_read_b64_tr_b16 v[232:233], v35 offset:4152\n"
      "ds_read_b64_tr_b16 v[234:235], v35 offset:4136\n"
      "v_lshlrev_b32 v26, 16, v50\n"
      "v_and_b32 v27, 0xffff0000, v50\n"
      "s_waitcnt lgkmcnt(6)\n"
      "v_mfma_f32_16x16x32_f16 v[196:199], v[228:231], v[140:143], v[196:199]\n"
      "v_lshlrev_b32 v28, 16, v51\n"
      "v_and_b32 v29, 0xffff0000, v51\n"
      "v_mfma_f32_16x16x32_f16 v[200:203], v[228:231], v[144:147], v[200:203]\n"
      "ds_read_b128 v[220:223], v33 offset:2176\n"
      "v_lshlrev_b32 v48, 16, v64\n"
      "v_and_b32 v64, 0xffff0000, v64\n"
      "v_mfma_f32_16x16x32_f16 v[204:207], v[228:231], v[148:151], v[204:207]\n"
      "v_fmac_f32 v68, s72, v48\n"
      "v_fmac_f32 v69, s73, v64\n"
      "s_waitcnt lgkmcnt(1)\n"
      "v_mfma_f32_16x16x32_f16 v[196:199], v[232:235], v[152:155], v[196:199]\n"
      "ds_read_b128 v[224:227], v33 offset:2240\n"
      "v_lshlrev_b32 v49, 16, v65\n"
      "v_and_b32 v65, 0xffff0000, v65\n"
      "v_mfma_f32_16x16x32_f16 v[200:203], v[232:235], v[156:159], v[200:203]\n"
      "v_fmac_f32 v70, s74, v49\n"
      "v_fmac_f32 v71, s75, v65\n"
      "v_mfma_f32_16x16x32_f16 v[204:207], v[232:235], v[160:163], v[204:207]\n"
      "ds_read_b128 v[212:215], v33 offset:2304\n"
      "v_lshlrev_b32 v50, 16, v66\n"
      "v_and_b32 v66, 0xffff0000, v66\n"
      "v_mfma_f32_16x16x32_f16 v[196:199], v[108:111], v[216:219], v[196:199]\n"
      "v_fmac_f32 v26, s76, v50\n"
      "v_fmac_f32 v27, s77, v66\n"
      "v_mfma_f32_16x16x32_f16 v[200:203], v[76:79], v[216:219], v[200:203]\n"
      "ds_read_b64_tr_b16 v[228:229], v35 offset:1568\n"
      "ds_read_b64_tr_b16 v[230:231], v35 offset:1552\n"
      "v_lshlrev_b32 v51, 16, v67\n"
      "v_and_b32 v67, 0xffff0000, v67\n"
      "s_waitcnt lgkmcnt(3)\n"
      "v_mfma_f32_16x16x32_f16 v[196:199], v[112:115], v[220:223], v[196:199]\n"
      "v_fmac_f32 v28, s78, v51\n"
      "v_fmac_f32 v29, s79, v67\n"
      "v_mfma_f32_16x16x32_f16 v[200:203], v[80:83], v[220:223], v[200:203]\n"
      "v_cvt_pk_bf16_f32 v48, v68, v69\n"
      "v_cvt_pk_bf16_f32 v49, v70, v71\n"
      "v_mfma_f32_16x16x32_f16 v[196:199], v[116:119], v[224:227], v[196:199]\n"
      "ds_read_b128 v[216:219], v33 offset:2368\n"
      "v_cvt_pk_bf16_f32 v50, v26, v27\n"
      "v_cvt_pk_bf16_f32 v51, v28, v29\n"
      "s_setprio 2\n"
      "v_mfma_f32_16x16x32_f16 v[200:203], v[84:87], v[224:227], v[200:203]\n"
      "v_lshlrev_b32 v13, 24, v12\n"
      "v_and_or_b32 v13, v13, s58, v42\n"
      "s_waitcnt lgkmcnt(1)\n"
      "v_mfma_f32_16x16x32_f16 v[196:199], v[120:123], v[212:215], v[196:199]\n"
      "ds_read_b128 v[220:223], v33 offset:2432\n"
      "s_cmp_lg_u32 s13, 0\n"
      "s_cbranch_scc1 .Lcs00%=\n"
      "buffer_store_dwordx4 v[48:51], v13, s[24:27], s54 offen\n"
      ".Lcs00%=:\n"
      "v_alignbit_b32 v245, v238, v237, 16\n"
      "v_alignbit_b32 v246, v239, v238, 16\n"
      "v_mfma_f32_16x16x32_f16 v[200:203], v[88:91], v[212:215], v[200:203]\n"
      "v_alignbit_b32 v247, v240, v239, 16\n"
      "v_alignbit_b32 v248, v241, v240, 16\n"
      "v_mfma_f32_16x16x32_f16 v[200:203], v[228:231], v[140:143], v[200:203]\n"
      "ds_read_b128 v[224:227], v33 offset:2496\n"
      "v_alignbit_b32 v249, v242, v241, 16\n"
      "v_pk_add_f16 v16, v245, v246\n"
      "v_mfma_f32_16x16x32_f16 v[204:207], v[228:231], v[144:147], v[204:207]\n"
      "v_pk_add_f16 v17, v246, v247\n"
      "v_pk_add_f16 v18, v247, v248\n"
      "v_mfma_f32_16x16x32_f16 v[208:211], v[228:231], v[148:151], v[208:211]\n"
      "ds_read_b128 v[212:215], v33 offset:2560\n"
      "s_cmp_lg_u32 s13, 1\n"
      "s_cbranch_scc1 .Lcs01%=\n"
      "buffer_store_dwordx4 v[48:51], v13, s[24:27], s54 offen\n"
      ".Lcs01%=:\n"
      "v_pk_add_f16 v19, v248, v249\n"
      "v_pk_add_f16 v20, v16, v238\n"
      "s_waitcnt lgkmcnt(1)\n"
      "v_mfma_f32_16x16x32_f16 v[196:199], v[124:127], v[216:219], v[196:199]\n"
      "v_pk_add_f16 v21, v17, v239\n"
      "v_pk_add_f16 v22, v18, v240\n"
      "v_mfma_f32_16x16x32_f16 v[200:203], v[92:95], v[216:219], v[200:203]\n"
      "v_pk_add_f16 v23, v19, v241\n"
      "ds_write_b128 v37, v[20:23] offset:2048\n"
      "v_pk_add_f16 v16, v237, v239\n"
      "v_mfma_f32_16x16x32_f16 v[196:199], v[128:131], v[220:223], v[196:199]\n"
      "ds_read_b64_tr_b16 v[232:233], v35 offset:4664\n"
      "ds_read_b64_tr_b16 v[234:235], v35 offset:4648\n"
      "ds_read_b64_tr_b16 v[64:65], v40 offset:512\n"
      "ds_read_b64_tr_b16 v[66:67], v40 offset:35584\n"
      "v_lshlrev_b32 v68, 16, v52\n"
      "v_and_b32 v69, 0xffff0000, v52\n"
      "v_mfma_f32_16x16x32_f16 v[200:203], v[96:99], v[220:223], v[200:203]\n"
      "s_cmp_lg_u32 s13, 2\n"
      "s_cbranch_scc1 .Lcs02%=\n"
      "buffer_store_dwordx4 v[48:51], v13, s[24:27], s54 offen\n"
      ".Lcs02%=:\n"
      "v_lshlrev_b32 v70, 16, v53\n"
      "v_and_b32 v71, 0xffff0000, v53\n"
      "v_mfma_f32_16x16x32_f16 v[196:199], v[132:135], v[224:227], v[196:199]\n"
      "v_lshlrev_b32 v26, 16, v54\n"
      "v_and_b32 v27, 0xffff0000, v54\n"
      "v_mfma_f32_16x16x32_f16 v[200:203], v[100:103], v[224:227], v[200:203]\n"
      "ds_read_b128 v[216:219], v33 offset:2624\n"
      "v_lshlrev_b32 v28, 16, v55\n"
      "v_and_b32 v29, 0xffff0000, v55\n"
      "s_waitcnt lgkmcnt(1)\n"
      "v_mfma_f32_16x16x32_f16 v[196:199], v[136:139], v[212:215], v[196:199]\n"
      "v_lshlrev_b32 v52, 16, v64\n"
      "v_and_b32 v64, 0xffff0000, v64\n"
      "v_mfma_f32_16x16x32_f16 v[200:203], v[104:107], v[212:215], v[200:203]\n"
      "ds_read_b128 v[220:223], v33 offset:2688\n"
      "s_cmp_lg_u32 s13, 3\n"
      "s_cbranch_scc1 .Lcs03%=\n"
      "buffer_store_dwordx4 v[48:51], v13, s[24:27], s54 offen\n"
      ".Lcs03%=:\n"
      "v_fmac_f32 v68, s72, v52\n"
      "v_fmac_f32 v69, s73, v64\n"
      "s_setprio 1\n"
      "v_mfma_f32_16x16x32_f16 v[204:207], v[72:75], v[212:215], v[204:207]\n"
      "v_lshlrev_b32 v53, 16, v65\n"
      "v_and_b32 v65, 0xffff0000, v65\n"
      "v_mfma_f32_16x16x32_f16 v[200:203], v[232:235], v[152:155], v[200:203]\n"
      "ds_read_b128 v[224:227], v33 offset:2752\n"
      "v_fmac_f32 v70, s74, v53\n"
      "v_fmac_f32 v71, s75, v65\n"
      "v_mfma_f32_16x16x32_f16 v[204:207], v[232:235], v[156:159], v[204:207]\n"
      "v_cvt_pk_bf16_f32 v24, v196, v198\n"
      "v_cvt_pk_bf16_f32 v25, v197, v199\n"
      "ds_write2st64_b32 v39, v24, v25 offset0:4 offset1:12\n"
      "v_mfma_f32_16x16x32_f16 v[208:211], v[232:235], v[160:163], v[208:211]\n"
      "ds_read_b128 v[212:215], v33 offset:2816\n"
      "v_lshlrev_b32 v54, 16, v66\n"
      "v_and_b32 v66, 0xffff0000, v66\n"
      "s_waitcnt lgkmcnt(3)\n"
      "v_mfma_f32_16x16x32_f16 v[200:203], v[108:111], v[216:219], v[200:203]\n"
      "v_fmac_f32 v26, s76, v54\n"
      "v_fmac_f32 v27, s77, v66\n"
      "v_mfma_f32_16x16x32_f16 v[204:207], v[76:79], v[216:219], v[204:207]\n"
      "ds_read_b128 v[216:219], v33 offset:2880\n"
      "v_lshlrev_b32 v55, 16, v67\n"
      "v_and_b32 v67, 0xffff0000, v67\n"
      "v_mfma_f32_16x16x32_f16 v[200:203], v[112:115], v[220:223], v[200:203]\n"
      "v_fmac_f32 v28, s78, v55\n"
      "v_fmac_f32 v29, s79, v67\n"
      "v_mfma_f32_16x16x32_f16 v[204:207], v[80:83], v[220:223], v[204:207]\n"
      "ds_read_b128 v[220:223], v33 offset:2944\n"
      "v_cvt_pk_bf16_f32 v52, v68, v69\n"
      "v_cvt_pk_bf16_f32 v53, v70, v71\n"
      "s_waitcnt lgkmcnt(2)\n"
      "v_mfma_f32_16x16x32_f16 v[200:203], v[116:119], v[224:227], v[200:203]\n"
      "v_cvt_pk_bf16_f32 v54, v26, v27\n"
      "v_cvt_pk_bf16_f32 v55, v28, v29\n"
      "v_mfma_f32_16x16x32_f16 v[204:207], v[84:87], v[224:227], v[204:207]\n"
      "ds_read_b128 v[224:227], v33 offset:3008\n"
      "v_lshlrev_b32 v13, 16, v12\n"
      "v_and_or_b32 v13, v13, s58, v42\n"
      "v_mfma_f32_16x16x32_f16 v[200:203], v[120:123], v[212:215], v[200:203]\n"
      "s_cmp_lg_u32 s13, 0\n"
      "s_cbranch_scc1 .Lcs10%=\n"
      "buffer_store_dwordx4 v[52:55], v13, s[24:27], s55 offen\n"
      ".Lcs10%=:\n"
      "v_pk_add_f16 v17, v238, v240\n"
      "v_pk_add_f16 v18, v239, v241\n"
      "s_setprio 0\n"
      "v_mfma_f32_16x16x32_f16 v[204:207], v[88:91], v[212:215], v[204:207]\n"
      "ds_read_b128 v[212:215], v33 offset:3072\n"
      "v_pk_add_f16 v19, v240, v242\n"
      "v_pk_add_f16 v20, v20, v16\n"
      "s_waitcnt lgkmcnt(1)\n"
      "v_mfma_f32_16x16x32_f16 v[200:203], v[124:127], v[216:219], v[200:203]\n"
      "v_pk_add_f16 v21, v21, v17\n"
      "v_pk_add_f16 v22, v22, v18\n"
      "v_mfma_f32_16x16x32_f16 v[204:207], v[92:95], v[216:219], v[204:207]\n"
      "v_pk_add_f16 v23, v23, v19\n"
      "ds_write_b128 v37, v[20:23] offset:5152\n"
      "v_mfma_f32_16x16x32_f16 v[200:203], v[128:131], v[220:223], v[200:203]\n"
      "ds_read_b64_tr_b16 v[228:229], v36 offset:1048\n"
      "ds_read_b64_tr_b16 v[230:231], v36 offset:3112\n"
      "s_cmp_lg_u32 s13, 1\n"
      "s_cbranch_scc1 .Lcs11%=\n"
      "buffer_store_dwordx4 v[52:55], v13, s[24:27], s55 offen\n"
      ".Lcs11%=:\n"
      "v_mfma_f32_16x16x32_f16 v[204:207], v[96:99], v[220:223], v[204:207]\n"
      "v_mfma_f32_16x16x32_f16 v[200:203], v[132:135], v[224:227], v[200:203]\n"
      "ds_read_b128 v[216:219], v33 offset:3136\n"
      "v_mfma_f32_16x16x32_f16 v[204:207], v[100:103], v[224:227], v[204:207]\n"
      "s_waitcnt lgkmcnt(4)\n"
      "v_mfma_f32_16x16x32_f16 v[200:203], v[136:139], v[212:215], v[200:203]\n"
      "ds_read_b128 v[220:223], v33 offset:3200\n"
      "s_cmp_lg_u32 s13, 2\n"
      "s_cbranch_scc1 .Lcs12%=\n"
      "buffer_store_dwordx4 v[52:55], v13, s[24:27], s55 offen\n"
      ".Lcs12%=:\n"
      "v_mfma_f32_16x16x32_f16 v[204:207], v[104:107], v[212:215], v[204:207]\n"
      "v_mfma_f32_16x16x32_f16 v[208:211], v[72:75], v[212:215], v[208:211]\n"
      "ds_read_b128 v[224:227], v33 offset:3264\n"
      "s_waitcnt lgkmcnt(3)\n"
      "v_mfma_f32_16x16x32_f16 v[204:207], v[228:231], v[164:167], v[204:207]\n"
      "v_mfma_f32_16x16x32_f16 v[208:211], v[228:231], v[168:171], v[208:211]\n"
      "ds_read_b64_tr_b16 v[232:233], v36 offset:1560\n"
      "ds_read_b64_tr_b16 v[234:235], v36 offset:3624\n"
      "v_cvt_pk_bf16_f32 v24, v200, v202\n"
      "v_cvt_pk_bf16_f32 v25, v201, v203\n"
      "ds_write2st64_b32 v39, v24, v25 offset0:5 offset1:13\n"
      "s_cmp_lg_u32 s13, 3\n"
      "s_cbranch_scc1 .Lcs13%=\n"
      "buffer_store_dwordx4 v[52:55], v13, s[24:27], s55 offen\n"
      ".Lcs13%=:\n"
      "s_waitcnt lgkmcnt(3)\n"
      "v_mfma_f32_16x16x32_f16 v[204:207], v[108:111], v[216:219], v[204:207]\n"
      "ds_read_b128 v[212:215], v33 offset:3328\n"
      "v_mfma_f32_16x16x32_f16 v[208:211], v[76:79], v[216:219], v[208:211]\n"
      "v_mfma_f32_16x16x32_f16 v[204:207], v[112:115], v[220:223], v[204:207]\n"
      "ds_read_b128 v[216:219], v33 offset:3392\n"
      "s_waitcnt lgkmcnt(2)\n"
      "s_barrier\n"
      "s_setprio 3\n"
      "v_mfma_f32_16x16x32_f16 v[208:211], v[80:83], v[220:223], v[208:211]\n"
      "ds_read_b64_tr_b16 v[64:65], v40 offset:1024\n"
      "ds_read_b64_tr_b16 v[66:67], v40 offset:36096\n"
      "v_lshlrev_b32 v68, 16, v56\n"
      "v_and_b32 v69, 0xffff0000, v56\n"
      "v_mfma_f32_16x16x32_f16 v[204:207], v[116:119], v[224:227], v[204:207]\n"
      "ds_read_b128 v[220:223], v33 offset:3456\n"
      "v_lshlrev_b32 v70, 16, v57\n"
      "v_and_b32 v71, 0xffff0000, v57\n"
      "v_mfma_f32_16x16x32_f16 v[208:211], v[84:87], v[224:227], v[208:211]\n"
      "v_lshlrev_b32 v26, 16, v58\n"
      "v_and_b32 v27, 0xffff0000, v58\n"
      "v_mfma_f32_16x16x32_f16 v[208:211], v[232:235], v[164:167], v[208:211]\n"
      "ds_read_b64_tr_b16 v[228:229], v35 offset:2080\n"
      "ds_read_b64_tr_b16 v[230:231], v35 offset:2064\n"
      "v_lshlrev_b32 v28, 16, v59\n"
      "v_and_b32 v29, 0xffff0000, v59\n"
      "s_waitcnt lgkmcnt(3)\n"
      "v_mfma_f32_16x16x32_f16 v[204:207], v[120:123], v[212:215], v[204:207]\n"
      "v_lshlrev_b32 v56, 16, v64\n"
      "v_and_b32 v64, 0xffff0000, v64\n"
      "v_mfma_f32_16x16x32_f16 v[208:211], v[88:91], v[212:215], v[208:211]\n"
      "ds_read_b128 v[224:227], v33 offset:3520\n"
      "v_fmac_f32 v68, s72, v56\n"
      "v_fmac_f32 v69, s73, v64\n"
      "v_mfma_f32_16x16x32_f16 v[204:207], v[124:127], v[216:219], v[204:207]\n"
      "v_lshlrev_b32 v57, 16, v65\n"
      "v_and_b32 v65, 0xffff0000, v65\n"
      "v_mfma_f32_16x16x32_f16 v[208:211], v[92:95], v[216:219], v[208:211]\n"
      "ds_read_b128 v[212:215], v33 offset:3584\n"
      "v_fmac_f32 v70, s74, v57\n"
      "v_fmac_f32 v71, s75, v65\n"
      "s_setprio 2\n"
      "s_waitcnt lgkmcnt(2)\n"
      "v_mfma_f32_16x16x32_f16 v[204:207], v[128:131], v[220:223], v[204:207]\n"
      "v_lshlrev_b32 v58, 16, v66\n"
      "v_and_b32 v66, 0xffff0000, v66\n"
      "v_mfma_f32_16x16x32_f16 v[208:211], v[96:99], v[220:223], v[208:211]\n"
      "ds_read_b128 v[216:219], v33 offset:3648\n"
      "v_fmac_f32 v26, s76, v58\n"
      "v_fmac_f32 v27, s77, v66\n"
      "v_mfma_f32_16x16x32_f16 v[204:207], v[228:231], v[140:143], v[204:207]\n"
      "ds_read_b64_tr_b16 v[232:233], v35 offset:5176\n"
      "ds_read_b64_tr_b16 v[234:235], v35 offset:5160\n"
      "v_lshlrev_b32 v59, 16, v67\n"
      "v_and_b32 v67, 0xffff0000, v67\n"
      "v_mfma_f32_16x16x32_f16 v[208:211], v[228:231], v[144:147], v[208:211]\n"
      "v_fmac_f32 v28, s78, v59\n"
      "v_fmac_f32 v29, s79, v67\n"
      "s_waitcnt lgkmcnt(3)\n"
      "v_mfma_f32_16x16x32_f16 v[204:207], v[132:135], v[224:227], v[204:207]\n"
      "ds_read_b128 v[220:223], v33 offset:3712\n"
      "v_cvt_pk_bf16_f32 v56, v68, v69\n"
      "v_cvt_pk_bf16_f32 v57, v70, v71\n"
      "v_mfma_f32_16x16x32_f16 v[208:211], v[100:103], v[224:227], v[208:211]\n"
      "ds_read_b128 v[224:227], v33 offset:3776\n"
      "v_cvt_pk_bf16_f32 v58, v26, v27\n"
      "v_cvt_pk_bf16_f32 v59, v28, v29\n"
      "v_mfma_f32_16x16x32_f16 v[204:207], v[136:139], v[212:215], v[204:207]\n"
      "v_lshlrev_b32 v13, 8, v12\n"
      "v_and_or_b32 v13, v13, s58, v42\n"
      "s_setprio 1\n"
      "v_mfma_f32_16x16x32_f16 v[208:211], v[104:107], v[212:215], v[208:211]\n"
      "ds_read_b64_tr_b16 v[228:229], v35 offset:2592\n"
      "ds_read_b64_tr_b16 v[230:231], v35 offset:2576\n"
      "ds_read_b128 v[212:215], v33 offset:3840\n"
      "s_cmp_lg_u32 s13, 0\n"
      "s_cbranch_scc1 .Lcs20%=\n"
      "buffer_store_dwordx4 v[56:59], v13, s[24:27], s56 offen\n"
      ".Lcs20%=:\n"
      "v_lshlrev_b32 v68, 16, v60\n"
      "v_and_b32 v69, 0xffff0000, v60\n"
      "s_waitcnt lgkmcnt(3)\n"
      "v_mfma_f32_16x16x32_f16 v[208:211], v[108:111], v[216:219], v[208:211]\n"
      "ds_read_b128 v[216:219], v33 offset:3904\n"
      "v_lshlrev_b32 v70, 16, v61\n"
      "v_and_b32 v71, 0xffff0000, v61\n"
      "v_mfma_f32_16x16x32_f16 v[204:207], v[232:235], v[152:155], v[204:207]\n"
      "v_lshlrev_b32 v26, 16, v62\n"
      "v_and_b32 v27, 0xffff0000, v62\n"
      "v_mfma_f32_16x16x32_f16 v[208:211], v[232:235], v[156:159], v[208:211]\n"
      "ds_read_b64_tr_b16 v[232:233], v35 offset:5688\n"
      "ds_read_b64_tr_b16 v[234:235], v35 offset:5672\n"
      "v_lshlrev_b32 v28, 16, v63\n"
      "v_and_b32 v29, 0xffff0000, v63\n"
      "v_mfma_f32_16x16x32_f16 v[208:211], v[112:115], v[220:223], v[208:211]\n"
      "ds_read_b128 v[220:223], v33 offset:3968\n"
      "s_cmp_lg_u32 s13, 1\n"
      "s_cbranch_scc1 .Lcs21%=\n"
      "buffer_store_dwordx4 v[56:59], v13, s[24:27], s56 offen\n"
      ".Lcs21%=:\n"
      "s_setprio 0\n"
      "v_mfma_f32_16x16x32_f16 v[208:211], v[116:119], v[224:227], v[208:211]\n"
      "ds_read_b128 v[224:227], v33 offset:4032\n"
      "s_waitcnt lgkmcnt(3)\n"
      "v_mfma_f32_16x16x32_f16 v[208:211], v[120:123], v[212:215], v[208:211]\n"
      "ds_read_b128 v[212:215], v33 offset:4096\n"
      "v_cvt_pk_bf16_f32 v24, v204, v206\n"
      "v_cvt_pk_bf16_f32 v25, v205, v207\n"
      "ds_write2st64_b32 v39, v24, v25 offset0:6 offset1:14\n"
      "v_mfma_f32_16x16x32_f16 v[208:211], v[228:231], v[140:143], v[208:211]\n"
      "v_mfma_f32_16x16x32_f16 v[208:211], v[124:127], v[216:219], v[208:211]\n"
      "s_cmp_lg_u32 s13, 2\n"
      "s_cbranch_scc1 .Lcs22%=\n"
      "buffer_store_dwordx4 v[56:59], v13, s[24:27], s56 offen\n"
      ".Lcs22%=:\n"
      "s_waitcnt lgkmcnt(2)\n"
      "v_mfma_f32_16x16x32_f16 v[208:211], v[128:131], v[220:223], v[208:211]\n"
      "v_mfma_f32_16x16x32_f16 v[208:211], v[232:235], v[152:155], v[208:211]\n"
      "v_mfma_f32_16x16x32_f16 v[208:211], v[132:135], v[224:227], v[208:211]\n"
      "s_waitcnt lgkmcnt(1)\n"
      "v_mfma_f32_16x16x32_f16 v[208:211], v[136:139], v[212:215], v[208:211]\n"
      "s_cmp_lg_u32 s13, 3\n"
      "s_cbranch_scc1 .Lcs23%=\n"
      "buffer_store_dwordx4 v[56:59], v13, s[24:27], s56 offen\n"
      ".Lcs23%=:\n"
      "s_nop 5\n"
      "v_cvt_pk_bf16_f32 v24, v208, v210\n"
      "v_cvt_pk_bf16_f32 v25, v209, v211\n"
      "ds_write2st64_b32 v39, v24, v25 offset0:7 offset1:15\n"
      "s_waitcnt lgkmcnt(0)\n"
      "s_barrier\n"
      "ds_read_b64_tr_b16 v[64:65], v40 offset:1536\n"
      "ds_read_b64_tr_b16 v[66:67], v40 offset:36608\n"
      "s_cmp_lg_u32 s33, 0\n"
      "s_cbranch_scc1 .Lcb%=\n"
      "ds_read_u16_d16_hi v252, v8 offset:56384\n"
      "ds_read_u16_d16_hi v253, v8 offset:65152\n"
      "s_waitcnt lgkmcnt(0)\n"
      "v_pk_mul_f32 v[252:253], v[252:253], v[254:255]\n"
      "v_cvt_pk_bf16_f32 v252, v252, v253\n"
      "buffer_atomic_pk_add_bf16 v252, v251, s[24:27], s41 offen\n"
      ".Lcb%=:\n"
      "s_waitcnt lgkmcnt(1)\n"
      "v_lshlrev_b32 v60, 16, v64\n"
      "v_and_b32 v64, 0xffff0000, v64\n"
      "v_fmac_f32 v68, s72, v60\n"
      "v_fmac_f32 v69, s73, v64\n"
      "v_lshlrev_b32 v61, 16, v65\n"
      "v_and_b32 v65, 0xffff0000, v65\n"
      "v_fmac_f32 v70, s74, v61\n"
      "v_fmac_f32 v71, s75, v65\n"
      "s_waitcnt lgkmcnt(0)\n"
      "v_lshlrev_b32 v62, 16, v66\n"
      "v_and_b32 v66, 0xffff0000, v66\n"
      "v_fmac_f32 v26, s76, v62\n"
      "v_fmac_f32 v27, s77, v66\n"
      "v_lshlrev_b32 v63, 16, v67\n"
      "v_and_b32 v67, 0xffff0000, v67\n"
      "v_fmac_f32 v28, s78, v63\n"
      "v_fmac_f32 v29, s79, v67\n"
      "v_cvt_pk_bf16_f32 v60, v68, v69\n"
      "v_cvt_pk_bf16_f32 v61, v70, v71\n"
      "v_cvt_pk_bf16_f32 v62, v26, v27\n"
      "v_cvt_pk_bf16_f32 v63, v28, v29\n"
      "v_and_or_b32 v13, v12, s58, v42\n"
      "buffer_store_dwordx4 v[60:63], v13, s[24:27], s57 offen\n"
      ".LEx%=:\ns_endpgm\n"
      ".Lcsgsk%=:\n"
      "s_sleep 4\n"
      "s_sub_u32 s59, s59, 1\n"
      "s_cbranch_scc1 .Lcsgok%=\n"
      "buffer_load_dwordx4 v[64:67], v42, s[16:19], s62 offen sc1\n"
      "s_waitcnt vmcnt(0)\n"
      "s_branch .Lcsgk%=\n"
      ".Lcsgsh%=:\n"
      "s_sleep 4\n"
      "s_sub_u32 s59, s59, 1\n"
      "s_cbranch_scc1 .Lcsgoh%=\n"
      "buffer_load_dwordx4 v[68:71], v42, s[16:19], s63 offen sc1\n"
      "s_waitcnt vmcnt(0)\n"
      "s_branch .Lcsgh%=\n"
      ".Lch%=:\n"
      "s_movk_i32 s59, 0x7fff\n"
      ".Lchl%=:\n"
      "s_waitcnt vmcnt(0)\n"
      "v_cmp_ne_u32 vcc, s15, v253\n"
      "v_not_b32 v255, v252\n"
      "s_sub_u32 s59, s59, 1\n"
      "s_cbranch_scc1 .Lchk%=\n"
      "s_cbranch_vccz .Lchk%=\n"
      "s_sleep 1\n"
      "buffer_load_dwordx2 v[252:253], v9, s[16:19], s43 offen sc1\n"
      "s_branch .Lchl%=\n"
      ".Lchk%=:\n"
      "v_and_b32 v31, 0x1ff, v255\n"
      "v_bfe_u32 v8, v255, 9, 2\n"
      "v_lshl_add_u32 v31, v31, 2, v8\n"
      "v_mov_b32 v8, 64\n"
      "v_add_u32 v31, 0x27700, v31\n"
      "ds_write_b8 v31, v8 \n"
      "v_and_b32 v31, 1, v255\n"
      "v_and_b32 v8, 0x7fe, v255\n"
      "v_lshl_add_u32 v8, v31, 11, v8\n"
      "v_mul_u32_u24 v31, 0x1120, v10\n"
      "v_add_u32 v8, v8, v31\n"
      "v_lshlrev_b32 v31, 1, v10\n"
      "v_add_u32 v31, 0x27600, v31\n"
      "v_and_b32 v255, 0x7ff, v255\n"
      "v_lshl_add_u32 v251, v255, 11, v10\n"
      "v_and_b32 v252, 32, v43\n"
      "v_lshl_or_b32 v251, v252, 25, v251\n"
      "ds_read_b64 v[254:255], v31 \n"
      "v_mov_b32 v252, 0\n"
      "v_mov_b32 v253, 0\n"
      "s_waitcnt lgkmcnt(0)\n"
      "s_branch .Lcho%=\n"
      ".LG%=:\n"
      "s_setprio 3\n"
      "s_mul_i32 s35, s48, 14\n"
      "s_add_u32 s35, s35, s49\n"
      "s_and_b32 s55, s33, 3\n"
      "s_mul_i32 s50, s55, 1835008\n"
      "s_lshr_b32 s51, s35, 1\n"
      "s_lshl_b32 s51, s51, 7\n"
      "s_add_u32 s50, s50, s51\n"
      "v_lshrrev_b32 v37, 3, v43\n"
      "v_and_b32 v38, 7, v43\n"
      "v_mul_u32_u24 v37, 0xe000, v37\n"
      "v_lshlrev_b32 v38, 4, v38\n"
      "v_add3_u32 v32, v37, v38, s50\n"
      "s_cmp_lt_u32 s34, 112\n"
      "s_cselect_b32 s26, 0x700000, 0\n"
      "s_mov_b32 s27, 0x20000\n"
      "s_bitcmp1_b32 s49, 0\n"
      "s_waitcnt lgkmcnt(0)\n"
      "s_load_dword s15, s[88:89], 0x0\n"
      "s_cselect_b32 s24, s82, s80\n"
      "s_cselect_b32 s25, s83, s81\n"
      "s_and_b32 s25, s25, 0xffff\n"
      "s_mov_b32 s16, s88\n"
      "s_and_b32 s17, s89, 0xffff\n"
      "s_mov_b32 s18, 0x6e0000\n"
      "s_mov_b32 s19, 0x20000\n"
      "s_mov_b32 s36, s86\n"
      "s_and_b32 s37, s87, 0xffff\n"
      "s_sleep 16\n"
      "s_mov_b32 s60, 0\n"
      "buffer_load_dwordx4 v[64:67], v32, s[24:27], s60 offen nt\n"
      "s_sleep 2\n"
      "s_mov_b32 s60, 7168\n"
      "buffer_load_dwordx4 v[68:71], v32, s[24:27], s60 offen nt\n"
      "s_sleep 2\n"
      "s_mov_b32 s60, 14336\n"
      "buffer_load_dwordx4 v[72:75], v32, s[24:27], s60 offen nt\n"
      "s_sleep 2\n"
      "s_mov_b32 s60, 21504\n"
      "buffer_load_dwordx4 v[76:79], v32, s[24:27], s60 offen nt\n"
      "s_sleep 2\n"
      "s_mov_b32 s60, 28672\n"
      "buffer_load_dwordx4 v[80:83], v32, s[24:27], s60 offen nt\n"
      "s_sleep 2\n"
      "s_mov_b32 s60, 35840\n"
      "buffer_load_dwordx4 v[84:87], v32, s[24:27], s60 offen nt\n"
      "s_sleep 2\n"
      "s_mov_b32 s60, 43008\n"
      "buffer_load_dwordx4 v[88:91], v32, s[24:27], s60 offen nt\n"
      "s_sleep 2\n"
      "s_mov_b32 s60, 50176\n"
      "buffer_load_dwordx4 v[92:95], v32, s[24:27], s60 offen nt\n"
      "s_sleep 2\n"
      "s_waitcnt lgkmcnt(0)\n"
      "s_add_u32 s15, s15, 0x9e3779b1\n"
      "v_mov_b32 v47, s15\n"
      "s_cmp_ge_u32 s34, 112\n"
      "s_cbranch_scc1 .LT%=\n"
      "v_mov_b32 v48, 0x22680\n"
      "ds_read_b32 v48, v48 \n"
      "s_waitcnt lgkmcnt(0)\n"
      "v_readfirstlane_b32 s12, v48\n"
      "s_mov_b32 s62, 0x05040100\n"
      "s_mov_b32 s63, 0x07060302\n"
      "s_lshl_b32 s42, s34, 3\n"
      "s_add_u32 s42, s42, 2048\n"
      "s_lshl_b32 s43, s48, 8\n"
      "s_and_b32 s50, s49, 1\n"
      "s_lshl_b32 s50, s50, 7\n"
      "s_add_u32 s43, s43, s50\n"
      "s_add_u32 s43, s43, 65536\n"
      "s_mov_b32 s20, s64\n"
      "s_and_b32 s21, s65, 0xffff\n"
      "s_mov_b32 s22, 0x400000\n"
      "s_mov_b32 s23, 0x20000\n"
      "v_lshrrev_b32 v39, 4, v43\n"
      "v_and_b32 v38, 15, v43\n"
      "v_lshlrev_b32 v38, 4, v38\n"
      "v_bfe_u32 v33, v43, 2, 2\n"
      "v_and_b32 v37, 1, v43\n"
      "v_lshl_add_u32 v33, v33, 1, v37\n"
      "v_lshlrev_b32 v33, 3, v33\n"
      "v_and_b32 v37, 2, v43\n"
      "v_lshl_add_u32 v37, v39, 2, v37\n"
      "s_lshl_b32 s50, s55, 9\n"
      "v_lshl_add_u32 v36, v37, 3, s50\n"
      "s_lshl_b32 s51, s55, 4\n"
      "v_lshrrev_b32 v40, 5, v43\n"
      "v_lshl_add_u32 v40, v40, 2, s51\n"
      "s_lshl_b32 s51, s55, 2\n"
      "s_add_u32 s51, s51, 140800\n"
      "v_mov_b32 v41, s51\n"
      "v_mov_b32 v42, 0x22600\n"
      "v_lshl_add_u32 v37, v43, 3, s50\n"
      "v_add_u32 v35, 0x22700, v37\n"
      "s_lshl_b32 s50, s55, 11\n"
      "s_add_u32 s50, s50, 141056\n"
      "v_lshl_add_u32 v34, v39, 9, v38\n"
      "v_add_u32 v34, s50, v34\n"
      "s_lshr_b32 s45, s35, 1\n"
      "s_lshl_b32 s45, s45, 16\n"
      "s_and_b32 s50, s35, 1\n"
      "s_lshl_b32 s50, s50, 11\n"
      "s_add_u32 s45, s45, s50\n"
      "s_add_u32 s45, s45, 524288\n"
      "s_mov_b32 s56, 0x3f80\n"
      "s_mov_b32 s57, 0x3f800000\n"
      "s_movk_i32 s61, 0x7ff\n"
      "s_mov_b32 s58, 131072\n"
      "s_mov_b32 s54, 0\n"
      "s_mov_b64 s[66:67], 0\n"
      "s_movk_i32 s70, 427\n"
      "s_waitcnt vmcnt(7)\n"
      "s_nop 0\n"
      "s_mov_b32 s60, 458752\n"
      "buffer_load_dwordx4 v[96:99], v32, s[24:27], s60 offen nt\n"
      "s_waitcnt vmcnt(7)\n"
      "s_nop 0\n"
      "s_mov_b32 s60, 465920\n"
      "buffer_load_dwordx4 v[100:103], v32, s[24:27], s60 offen nt\n"
      "v_perm_b32 v192, v68, v64, s62\n"
      "v_perm_b32 v196, v68, v64, s63\n"
      "v_perm_b32 v200, v69, v65, s62\n"
      "v_perm_b32 v204, v69, v65, s63\n"
      "v_perm_b32 v208, v70, v66, s62\n"
      "v_perm_b32 v212, v70, v66, s63\n"
      "v_perm_b32 v216, v71, v67, s62\n"
      "v_perm_b32 v220, v71, v67, s63\n"
      "s_waitcnt vmcnt(7)\n"
      "s_nop 0\n"
      "s_mov_b32 s60, 473088\n"
      "buffer_load_dwordx4 v[104:107], v32, s[24:27], s60 offen nt\n"
      "s_waitcnt vmcnt(7)\n"
      "s_nop 0\n"
      "s_mov_b32 s60, 480256\n"
      "buffer_load_dwordx4 v[108:111], v32, s[24:27], s60 offen nt\n"
      "v_perm_b32 v193, v76, v72, s62\n"
      "v_perm_b32 v197, v76, v72, s63\n"
      "v_perm_b32 v201, v77, v73, s62\n"
      "v_perm_b32 v205, v77, v73, s63\n"
      "v_perm_b32 v209, v78, v74, s62\n"
      "v_perm_b32 v213, v78, v74, s63\n"
      "v_perm_b32 v217, v79, v75, s62\n"
      "v_perm_b32 v221, v79, v75, s63\n"
      "s_waitcnt vmcnt(7)\n"
      "s_nop 0\n"
      "s_mov_b32 s60, 487424\n"
      "buffer_load_dwordx4 v[112:115], v32, s[24:27], s60 offen nt\n"
      "s_waitcnt vmcnt(7)\n"
      "s_nop 0\n"
      "s_mov_b32 s60, 494592\n"
      "buffer_load_dwordx4 v[116:119], v32, s[24:27], s60 offen nt\n"
      "v_perm_b32 v194, v84, v80, s62\n"
      "v_perm_b32 v198, v84, v80, s63\n"
      "v_perm_b32 v202, v85, v81, s62\n"
      "v_perm_b32 v206, v85, v81, s63\n"
      "v_perm_b32 v210, v86, v82, s62\n"
      "v_perm_b32 v214, v86, v82, s63\n"
      "v_perm_b32 v218, v87, v83, s62\n"
      "v_perm_b32 v222, v87, v83, s63\n"
      "s_waitcnt vmcnt(7)\n"
      "s_nop 0\n"
      "s_mov_b32 s60, 501760\n"
      "buffer_load_dwordx4 v[120:123], v32, s[24:27], s60 offen nt\n"
      "s_waitcnt vmcnt(7)\n"
      "s_nop 0\n"
      "s_mov_b32 s60, 508928\n"
      "buffer_load_dwordx4 v[124:127], v32, s[24:27], s60 offen nt\n"
      "v_perm_b32 v195, v92, v88, s62\n"
      "v_perm_b32 v199, v92, v88, s63\n"
      "v_perm_b32 v203, v93, v89, s62\n"
      "v_perm_b32 v207, v93, v89, s63\n"
      "v_perm_b32 v211, v94, v90, s62\n"
      "v_perm_b32 v215, v94, v90, s63\n"
      "v_perm_b32 v219, v95, v91, s62\n"
      "v_perm_b32 v223, v95, v91, s63\n"
      "s_waitcnt vmcnt(7)\n"
      "s_nop 0\n"
      "s_mov_b32 s60, 917504\n"
      "buffer_load_dwordx4 v[128:131], v32, s[24:27], s60 offen nt\n"
      "s_waitcnt vmcnt(7)\n"
      "s_nop 0\n"
      "s_mov_b32 s60, 924672\n"
      "buffer_load_dwordx4 v[132:135], v32, s[24:27], s60 offen nt\n"
      "v_perm_b32 v64, v100, v96, s62\n"
      "v_perm_b32 v68, v100, v96, s63\n"
      "v_perm_b32 v72, v101, v97, s62\n"
      "v_perm_b32 v76, v101, v97, s63\n"
      "v_perm_b32 v80, v102, v98, s62\n"
      "v_perm_b32 v84, v102, v98, s63\n"
      "v_perm_b32 v88, v103, v99, s62\n"
      "v_perm_b32 v92, v103, v99, s63\n"
      "s_waitcnt vmcnt(7)\n"
      "s_nop 0\n"
      "s_mov_b32 s60, 931840\n"
      "buffer_load_dwordx4 v[136:139], v32, s[24:27], s60 offen nt\n"
      "s_waitcnt vmcnt(7)\n"
      "s_nop 0\n"
      "s_mov_b32 s60, 939008\n"
      "buffer_load_dwordx4 v[140:143], v32, s[24:27], s60 offen nt\n"
      "v_perm_b32 v65, v108, v104, s62\n"
      "v_perm_b32 v69, v108, v104, s63\n"
      "v_perm_b32 v73, v109, v105, s62\n"
      "v_perm_b32 v77, v109, v105, s63\n"
      "v_perm_b32 v81, v110, v106, s62\n"
      "v_perm_b32 v85, v110, v106, s63\n"
      "v_perm_b32 v89, v111, v107, s62\n"
      "v_perm_b32 v93, v111, v107, s63\n"
      "s_waitcnt vmcnt(7)\n"
      "s_nop 0\n"
      "s_mov_b32 s60, 946176\n"
      "buffer_load_dwordx4 v[144:147], v32, s[24:27], s60 offen nt\n"
      "s_waitcnt vmcnt(7)\n"
      "s_nop 0\n"
      "s_mov_b32 s60, 953344\n"
      "buffer_load_dwordx4 v[148:151], v32, s[24:27], s60 offen nt\n"
      "v_perm_b32 v66, v116, v112, s62\n"
      "v_perm_b32 v70, v116, v112, s63\n"
      "v_perm_b32 v74, v117, v113, s62\n"
      "v_perm_b32 v78, v117, v113, s63\n"
      "v_perm_b32 v82, v118, v114, s62\n"
      "v_perm_b32 v86, v118, v114, s63\n"
      "v_perm_b32 v90, v119, v115, s62\n"
      "v_perm_b32 v94, v119, v115, s63\n"
      "s_waitcnt vmcnt(7)\n"
      "s_nop 0\n"
      "s_mov_b32 s60, 960512\n"
      "buffer_load_dwordx4 v[152:155], v32, s[24:27], s60 offen nt\n"
      "s_waitcnt vmcnt(7)\n"
      "s_nop 0\n"
      "s_mov_b32 s60, 967680\n"
      "buffer_load_dwordx4 v[156:159], v32, s[24:27], s60 offen nt\n"
      "v_perm_b32 v67, v124, v120, s62\n"
      "v_perm_b32 v71, v124, v120, s63\n"
      "v_perm_b32 v75, v125, v121, s62\n"
      "v_perm_b32 v79, v125, v121, s63\n"
      "v_perm_b32 v83, v126, v122, s62\n"
      "v_perm_b32 v87, v126, v122, s63\n"
      "v_perm_b32 v91, v127, v123, s62\n"
      "v_perm_b32 v95, v127, v123, s63\n"
      "s_waitcnt vmcnt(7)\n"
      "s_nop 0\n"
      "s_mov_b32 s60, 1376256\n"
      "buffer_load_dwordx4 v[160:163], v32, s[24:27], s60 offen nt\n"
      "s_waitcnt vmcnt(7)\n"
      "s_nop 0\n"
      "s_mov_b32 s60, 1383424\n"
      "buffer_load_dwordx4 v[164:167], v32, s[24:27], s60 offen nt\n"
      "v_perm_b32 v96, v132, v128, s62\n"
      "v_perm_b32 v100, v132, v128, s63\n"
      "v_perm_b32 v104, v133, v129, s62\n"
      "v_perm_b32 v108, v133, v129, s63\n"
      "v_perm_b32 v112, v134, v130, s62\n"
      "v_perm_b32 v116, v134, v130, s63\n"
      "v_perm_b32 v120, v135, v131, s62\n"
      "v_perm_b32 v124, v135, v131, s63\n"
      "s_waitcnt vmcnt(7)\n"
      "s_nop 0\n"
      "s_mov_b32 s60, 1390592\n"
      "buffer_load_dwordx4 v[168:171], v32, s[24:27], s60 offen nt\n"
      "s_waitcnt vmcnt(7)\n"
      "s_nop 0\n"
      "s_mov_b32 s60, 1397760\n"
      "buffer_load_dwordx4 v[172:175], v32, s[24:27], s60 offen nt\n"
      "v_perm_b32 v97, v140, v136, s62\n"
      "v_perm_b32 v101, v140, v136, s63\n"
      "v_perm_b32 v105, v141, v137, s62\n"
      "v_perm_b32 v109, v141, v137, s63\n"
      "v_perm_b32 v113, v142, v138, s62\n"
      "v_perm_b32 v117, v142, v138, s63\n"
      "v_perm_b32 v121, v143, v139, s62\n"
      "v_perm_b32 v125, v143, v139, s63\n"
      "s_waitcnt vmcnt(7)\n"
      "s_nop 0\n"
      "s_mov_b32 s60, 1404928\n"
      "buffer_load_dwordx4 v[176:179], v32, s[24:27], s60 offen nt\n"
      "s_waitcnt vmcnt(7)\n"
      "s_nop 0\n"
      "s_mov_b32 s60, 1412096\n"
      "buffer_load_dwordx4 v[180:183], v32, s[24:27], s60 offen nt\n"
      "v_perm_b32 v98, v148, v144, s62\n"
      "v_perm_b32 v102, v148, v144, s63\n"
      "v_perm_b32 v106, v149, v145, s62\n"
      "v_perm_b32 v110, v149, v145, s63\n"
      "v_perm_b32 v114, v150, v146, s62\n"
      "v_perm_b32 v118, v150, v146, s63\n"
      "v_perm_b32 v122, v151, v147, s62\n"
      "v_perm_b32 v126, v151, v147, s63\n"
      "s_waitcnt vmcnt(7)\n"
      "s_nop 0\n"
      "s_mov_b32 s60, 1419264\n"
      "buffer_load_dwordx4 v[184:187], v32, s[24:27], s60 offen nt\n"
      "s_waitcnt vmcnt(7)\n"
      "s_nop 0\n"
      "s_mov_b32 s60, 1426432\n"
      "buffer_load_dwordx4 v[188:191], v32, s[24:27], s60 offen nt\n"
      "ds_write_b32 v41, v47 offset:80\n"
      "v_perm_b32 v99, v156, v152, s62\n"
      "v_perm_b32 v103, v156, v152, s63\n"
      "v_perm_b32 v107, v157, v153, s62\n"
      "v_perm_b32 v111, v157, v153, s63\n"
      "v_perm_b32 v115, v158, v154, s62\n"
      "v_perm_b32 v119, v158, v154, s63\n"
      "v_perm_b32 v123, v159, v155, s62\n"
      "v_perm_b32 v127, v159, v155, s63\n"
      "s_waitcnt vmcnt(7)\n"
      "s_nop 0\n"
      "s_memrealtime s[68:69]\n"
      "s_waitcnt vmcnt(6)\n"
      "s_nop 0\n"
      "s_waitcnt lgkmcnt(0)\n"
      "s_sub_u32 s68, s68, s12\n"
      "s_cmp_lt_u32 s68, s70\n"
      "s_cbranch_scc1 .Lgn25%=\n"
      "buffer_load_dwordx2 v[8:9], v33, s[16:19], s43 offen sc1\n"
      "s_mov_b32 s70, -1\n"
      ".Lgn25%=:\n"
      "s_memrealtime s[68:69]\n"
      "v_perm_b32 v128, v164, v160, s62\n"
      "v_perm_b32 v132, v164, v160, s63\n"
      "v_perm_b32 v136, v165, v161, s62\n"
      "v_perm_b32 v140, v165, v161, s63\n"
      "v_perm_b32 v144, v166, v162, s62\n"
      "v_perm_b32 v148, v166, v162, s63\n"
      "v_perm_b32 v152, v167, v163, s62\n"
      "v_perm_b32 v156, v167, v163, s63\n"
      "s_waitcnt vmcnt(5)\n"
      "s_nop 0\n"
      "s_waitcnt lgkmcnt(0)\n"
      "s_sub_u32 s68, s68, s12\n"
      "s_cmp_lt_u32 s68, s70\n"
      "s_cbranch_scc1 .Lgn26%=\n"
      "buffer_load_dwordx2 v[8:9], v33, s[16:19], s43 offen sc1\n"
      "s_mov_b32 s70, -1\n"
      ".Lgn26%=:\n"
      "s_memrealtime s[68:69]\n"
      "s_waitcnt vmcnt(4)\n"
      "s_nop 0\n"
      "s_waitcnt lgkmcnt(0)\n"
      "s_sub_u32 s68, s68, s12\n"
      "s_cmp_lt_u32 s68, s70\n"
      "s_cbranch_scc1 .Lgn27%=\n"
      "buffer_load_dwordx2 v[8:9], v33, s[16:19], s43 offen sc1\n"
      "s_mov_b32 s70, -1\n"
      ".Lgn27%=:\n"
      "s_memrealtime s[68:69]\n"
      "v_perm_b32 v129, v172, v168, s62\n"
      "v_perm_b32 v133, v172, v168, s63\n"
      "v_perm_b32 v137, v173, v169, s62\n"
      "v_perm_b32 v141, v173, v169, s63\n"
      "v_perm_b32 v145, v174, v170, s62\n"
      "v_perm_b32 v149, v174, v170, s63\n"
      "v_perm_b32 v153, v175, v171, s62\n"
      "v_perm_b32 v157, v175, v171, s63\n"
      "s_waitcnt vmcnt(3)\n"
      "s_nop 0\n"
      "s_waitcnt lgkmcnt(0)\n"
      "s_sub_u32 s68, s68, s12\n"
      "s_cmp_lt_u32 s68, s70\n"
      "s_cbranch_scc1 .Lgn28%=\n"
      "buffer_load_dwordx2 v[8:9], v33, s[16:19], s43 offen sc1\n"
      "s_mov_b32 s70, -1\n"
      ".Lgn28%=:\n"
      "s_memrealtime s[68:69]\n"
      "s_waitcnt vmcnt(2)\n"
      "s_nop 0\n"
      "s_waitcnt lgkmcnt(0)\n"
      "s_sub_u32 s68, s68, s12\n"
      "s_cmp_lt_u32 s68, s70\n"
      "s_cbranch_scc1 .Lgn29%=\n"
      "buffer_load_dwordx2 v[8:9], v33, s[16:19], s43 offen sc1\n"
      "s_mov_b32 s70, -1\n"
      ".Lgn29%=:\n"
      "s_memrealtime s[68:69]\n"
      "v_perm_b32 v130, v180, v176, s62\n"
      "v_perm_b32 v134, v180, v176, s63\n"
      "v_perm_b32 v138, v181, v177, s62\n"
      "v_perm_b32 v142, v181, v177, s63\n"
      "v_perm_b32 v146, v182, v178, s62\n"
      "v_perm_b32 v150, v182, v178, s63\n"
      "v_perm_b32 v154, v183, v179, s62\n"
      "v_perm_b32 v158, v183, v179, s63\n"
      "s_waitcnt vmcnt(1)\n"
      "s_nop 0\n"
      "s_waitcnt lgkmcnt(0)\n"
      "s_sub_u32 s68, s68, s12\n"
      "s_cmp_lt_u32 s68, s70\n"
      "s_cbranch_scc1 .Lgn30%=\n"
      "buffer_load_dwordx2 v[8:9], v33, s[16:19], s43 offen sc1\n"
      "s_mov_b32 s70, -1\n"
      ".Lgn30%=:\n"
      "s_memrealtime s[68:69]\n"
      "s_waitcnt vmcnt(0)\n"
      "s_nop 0\n"
      "s_waitcnt lgkmcnt(0)\n"
      "s_sub_u32 s68, s68, s12\n"
      "s_cmp_lt_u32 s68, s70\n"
      "s_cbranch_scc1 .Lgn31%=\n"
      "buffer_load_dwordx2 v[8:9], v33, s[16:19], s43 offen sc1\n"
      "s_mov_b32 s70, -1\n"
      ".Lgn31%=:\n"
      "v_perm_b32 v131, v188, v184, s62\n"
      "v_perm_b32 v135, v188, v184, s63\n"
      "v_perm_b32 v139, v189, v185, s62\n"
      "v_perm_b32 v143, v189, v185, s63\n"
      "v_perm_b32 v147, v190, v186, s62\n"
      "v_perm_b32 v151, v190, v186, s63\n"
      "v_perm_b32 v155, v191, v187, s62\n"
      "v_perm_b32 v159, v191, v187, s63\n"
      "s_movk_i32 s59, 0x3fff\n"
      "s_cmp_eq_u32 s70, -1\n"
      "s_cbranch_scc1 .Lghc%=\n"
      ".Lgpc3_%=:\n"
      "s_memrealtime s[68:69]\n"
      "s_waitcnt lgkmcnt(0)\n"
      "s_sub_u32 s68, s68, s12\n"
      "s_cmp_ge_u32 s68, 435\n"
      "s_cbranch_scc1 .Lgpd3_%=\n"
      "s_sleep 1\n"
      "s_branch .Lgpc3_%=\n"
      ".Lgpd3_%=:\n"
      ".Lgh%=:\n"
      "buffer_load_dwordx2 v[8:9], v33, s[16:19], s43 offen sc1\n"
      ".Lghc%=:\n"
      "s_waitcnt vmcnt(0)\n"
      "v_cmp_ne_u32 vcc, s15, v9\n"
      "v_bitop3_b32 v10, v8, s61, v8 bitop3:0x0c\n"
      "s_sub_u32 s59, s59, 1\n"
      "s_cbranch_vccz .Lgho%=\n"
      "s_sleep 2\n"
      "s_cbranch_scc0 .Lgh%=\n"
      ".Lgho%=:\n"
      "v_lshrrev_b32 v45, 9, v10\n"
      "v_lshl_add_u32 v11, v10, 11, v36\n"
      "v_mul_u32_u24 v45, 0xf000, v45\n"
      "v_lshl_add_u32 v45, v10, 3, v45\n"
      "buffer_load_dwordx2 v[62:63], v45, s[16:19], s58 offen sc1\n"
      "buffer_load_dwordx4 v[16:19], v11, s[20:23], 0 offen offset:0\n"
      "buffer_load_dwordx4 v[20:23], v11, s[20:23], 0 offen offset:128\n"
      "buffer_load_dwordx4 v[24:27], v11, s[20:23], 0 offen offset:256\n"
      "buffer_load_dwordx4 v[28:31], v11, s[20:23], 0 offen offset:384\n"
      "ds_write_b32 v41, v47 offset:64\n"
      ".Lghx%=:\n"
      "s_movk_i32 s59, 0x7fff\n"
      "s_waitcnt vmcnt(4)\n"
      "s_nop 0\n"
      ".Lgs%=:\n"
      "v_cmp_ne_u32 vcc, s15, v63\n"
      "v_lshlrev_b32 v10, 16, v62\n"
      "v_and_b32 v44, 0xffff0000, v62\n"
      "s_cbranch_vccnz .Lgss%=\n"
      ".Lgso%=:\n"
      "v_rcp_f32 v44, v44\n"
      "v_xor_b32 v10, 0x80000000, v10\n"
      "s_waitcnt vmcnt(3)\n"
      "v_dot2_f32_bf16 v168, v16, s56, v10\n"
      "v_dot2_f32_bf16 v169, v16, s57, v10\n"
      "v_dot2_f32_bf16 v170, v17, s56, v10\n"
      "v_dot2_f32_bf16 v171, v17, s57, v10\n"
      "v_dot2_f32_bf16 v172, v18, s56, v10\n"
      "v_dot2_f32_bf16 v173, v18, s57, v10\n"
      "v_dot2_f32_bf16 v174, v19, s56, v10\n"
      "v_dot2_f32_bf16 v175, v19, s57, v10\n"
      "v_cvt_pk_bf16_f32 v16, v168, v169\n"
      "v_cvt_pk_bf16_f32 v17, v170, v171\n"
      "v_cvt_pk_bf16_f32 v18, v172, v173\n"
      "v_cvt_pk_bf16_f32 v19, v174, v175\n"
      "ds_bpermute_b32 v44, v40, v44\n"
      "s_waitcnt vmcnt(2)\n"
      "v_dot2_f32_bf16 v176, v20, s56, v10\n"
      "v_dot2_f32_bf16 v177, v20, s57, v10\n"
      "v_dot2_f32_bf16 v178, v21, s56, v10\n"
      "v_dot2_f32_bf16 v179, v21, s57, v10\n"
      "v_dot2_f32_bf16 v180, v22, s56, v10\n"
      "v_dot2_f32_bf16 v181, v22, s57, v10\n"
      "v_dot2_f32_bf16 v182, v23, s56, v10\n"
      "v_dot2_f32_bf16 v183, v23, s57, v10\n"
      "v_cvt_pk_bf16_f32 v20, v176, v177\n"
      "v_cvt_pk_bf16_f32 v21, v178, v179\n"
      "v_cvt_pk_bf16_f32 v22, v180, v181\n"
      "v_cvt_pk_bf16_f32 v23, v182, v183\n"
      "s_waitcnt vmcnt(1)\n"
      "v_dot2_f32_bf16 v168, v24, s56, v10\n"
      "v_dot2_f32_bf16 v169, v24, s57, v10\n"
      "v_dot2_f32_bf16 v170, v25, s56, v10\n"
      "v_dot2_f32_bf16 v171, v25, s57, v10\n"
      "v_dot2_f32_bf16 v172, v26, s56, v10\n"
      "v_dot2_f32_bf16 v173, v26, s57, v10\n"
      "v_dot2_f32_bf16 v174, v27, s56, v10\n"
      "v_dot2_f32_bf16 v175, v27, s57, v10\n"
      "v_cvt_pk_bf16_f32 v24, v168, v169\n"
      "v_cvt_pk_bf16_f32 v25, v170, v171\n"
      "v_cvt_pk_bf16_f32 v26, v172, v173\n"
      "v_cvt_pk_bf16_f32 v27, v174, v175\n"
      "s_waitcnt vmcnt(0)\n"
      "v_dot2_f32_bf16 v176, v28, s56, v10\n"
      "v_dot2_f32_bf16 v177, v28, s57, v10\n"
      "v_dot2_f32_bf16 v178, v29, s56, v10\n"
      "v_dot2_f32_bf16 v179, v29, s57, v10\n"
      "v_dot2_f32_bf16 v180, v30, s56, v10\n"
      "v_dot2_f32_bf16 v181, v30, s57, v10\n"
      "v_dot2_f32_bf16 v182, v31, s56, v10\n"
      "v_dot2_f32_bf16 v183, v31, s57, v10\n"
      "v_cvt_pk_bf16_f32 v28, v176, v177\n"
      "v_cvt_pk_bf16_f32 v29, v178, v179\n"
      "v_cvt_pk_bf16_f32 v30, v180, v181\n"
      "v_cvt_pk_bf16_f32 v31, v182, v183\n"
      "v_mfma_f32_16x16x32_bf16 v[224:227], v[16:19], v[192:195], 0\n"
      "v_mfma_f32_16x16x32_bf16 v[228:231], v[16:19], v[196:199], 0\n"
      "v_mfma_f32_16x16x32_bf16 v[232:235], v[16:19], v[200:203], 0\n"
      "v_mfma_f32_16x16x32_bf16 v[236:239], v[16:19], v[204:207], 0\n"
      "v_mfma_f32_16x16x32_bf16 v[240:243], v[16:19], v[208:211], 0\n"
      "v_mfma_f32_16x16x32_bf16 v[244:247], v[16:19], v[212:215], 0\n"
      "v_mfma_f32_16x16x32_bf16 v[248:251], v[16:19], v[216:219], 0\n"
      "v_mfma_f32_16x16x32_bf16 v[252:255], v[16:19], v[220:223], 0\n"
      "v_mfma_f32_16x16x32_bf16 v[224:227], v[20:23], v[64:67], v[224:227]\n"
      "v_mfma_f32_16x16x32_bf16 v[228:231], v[20:23], v[68:71], v[228:231]\n"
      "v_mfma_f32_16x16x32_bf16 v[232:235], v[20:23], v[72:75], v[232:235]\n"
      "v_mfma_f32_16x16x32_bf16 v[236:239], v[20:23], v[76:79], v[236:239]\n"
      "v_mfma_f32_16x16x32_bf16 v[240:243], v[20:23], v[80:83], v[240:243]\n"
      "v_mfma_f32_16x16x32_bf16 v[244:247], v[20:23], v[84:87], v[244:247]\n"
      "v_mfma_f32_16x16x32_bf16 v[248:251], v[20:23], v[88:91], v[248:251]\n"
      "v_mfma_f32_16x16x32_bf16 v[252:255], v[20:23], v[92:95], v[252:255]\n"
      "v_mfma_f32_16x16x32_bf16 v[224:227], v[24:27], v[96:99], v[224:227]\n"
      "v_mfma_f32_16x16x32_bf16 v[228:231], v[24:27], v[100:103], v[228:231]\n"
      "v_mfma_f32_16x16x32_bf16 v[232:235], v[24:27], v[104:107], v[232:235]\n"
      "v_mfma_f32_16x16x32_bf16 v[236:239], v[24:27], v[108:111], v[236:239]\n"
      "v_mfma_f32_16x16x32_bf16 v[240:243], v[24:27], v[112:115], v[240:243]\n"
      "v_mfma_f32_16x16x32_bf16 v[244:247], v[24:27], v[116:119], v[244:247]\n"
      "v_mfma_f32_16x16x32_bf16 v[248:251], v[24:27], v[120:123], v[248:251]\n"
      "v_mfma_f32_16x16x32_bf16 v[252:255], v[24:27], v[124:127], v[252:255]\n"
      "v_mfma_f32_16x16x32_bf16 v[224:227], v[28:31], v[128:131], v[224:227]\n"
      "v_mfma_f32_16x16x32_bf16 v[228:231], v[28:31], v[132:135], v[228:231]\n"
      "v_mfma_f32_16x16x32_bf16 v[232:235], v[28:31], v[136:139], v[232:235]\n"
      "v_mfma_f32_16x16x32_bf16 v[236:239], v[28:31], v[140:143], v[236:239]\n"
      "s_nop 4\n"
      "v_add_f32_dpp v160, v226, v224 row_ror:8 row_mask:0xf bank_mask:0x3\n"
      "v_add_f32_dpp v164, v227, v225 row_ror:8 row_mask:0xf bank_mask:0x3\n"
      "v_mfma_f32_16x16x32_bf16 v[240:243], v[28:31], v[144:147], v[240:243]\n"
      "v_add_f32_dpp v161, v230, v228 row_ror:8 row_mask:0xf bank_mask:0x3\n"
      "v_add_f32_dpp v165, v231, v229 row_ror:8 row_mask:0xf bank_mask:0x3\n"
      "v_mfma_f32_16x16x32_bf16 v[244:247], v[28:31], v[148:151], v[244:247]\n"
      "v_add_f32_dpp v162, v234, v232 row_ror:8 row_mask:0xf bank_mask:0x3\n"
      "v_add_f32_dpp v166, v235, v233 row_ror:8 row_mask:0xf bank_mask:0x3\n"
      "v_mfma_f32_16x16x32_bf16 v[248:251], v[28:31], v[152:155], v[248:251]\n"
      "v_add_f32_dpp v163, v238, v236 row_ror:8 row_mask:0xf bank_mask:0x3\n"
      "v_add_f32_dpp v167, v239, v237 row_ror:8 row_mask:0xf bank_mask:0x3\n"
      "v_mfma_f32_16x16x32_bf16 v[252:255], v[28:31], v[156:159], v[252:255]\n"
      "v_add_f32_dpp v160, v240, v242 row_ror:8 row_mask:0xf bank_mask:0xc\n"
      "v_add_f32_dpp v164, v241, v243 row_ror:8 row_mask:0xf bank_mask:0xc\n"
      "v_add_f32_dpp v161, v244, v246 row_ror:8 row_mask:0xf bank_mask:0xc\n"
      "v_add_f32_dpp v165, v245, v247 row_ror:8 row_mask:0xf bank_mask:0xc\n"
      "s_nop 0\n"
      "v_add_f32_dpp v162, v248, v250 row_ror:8 row_mask:0xf bank_mask:0xc\n"
      "v_add_f32_dpp v166, v249, v251 row_ror:8 row_mask:0xf bank_mask:0xc\n"
      "s_nop 0\n"
      "v_add_f32_dpp v163, v252, v254 row_ror:8 row_mask:0xf bank_mask:0xc\n"
      "v_add_f32_dpp v167, v253, v255 row_ror:8 row_mask:0xf bank_mask:0xc\n"
      "ds_write_b128 v34, v[160:163] \n"
      "ds_write_b128 v34, v[164:167] offset:256\n"
      "ds_write_b32 v41, v47 \n"
      "s_movk_i32 s59, 0x7fff\n"
      ".Lgw%=:\n"
      "ds_read_b128 v[72:75], v42 \n"
      "ds_read_b64 v[64:65], v35 offset:0\n"
      "ds_read_b64 v[66:67], v35 offset:2048\n"
      "ds_read_b64 v[68:69], v35 offset:4096\n"
      "ds_read_b64 v[70:71], v35 offset:6144\n"
      "s_sub_u32 s59, s59, 1\n"
      "s_cbranch_scc1 .Lgwo%=\n"
      "s_waitcnt lgkmcnt(4)\n"
      "v_xor_b32 v72, s15, v72\n"
      "v_bitop3_b32 v72, s15, v73, v72 bitop3:0xbe\n"
      "v_bitop3_b32 v72, s15, v74, v72 bitop3:0xbe\n"
      "v_bitop3_b32 v72, s15, v75, v72 bitop3:0xbe\n"
      "v_cmp_ne_u32 vcc, 0, v72\n"
      "s_waitcnt lgkmcnt(0)\n"
      "s_nop 0\n"
      "s_cbranch_vccnz .Lgw%=\n"
      ".Lgwo%=:\n"
      "s_waitcnt lgkmcnt(0)\n"
      "s_bitcmp1_b32 s49, 0\n"
      "v_pk_add_f32 v[64:65], v[64:65], v[66:67]\n"
      "v_pk_add_f32 v[68:69], v[68:69], v[70:71]\n"
      "v_pk_add_f32 v[64:65], v[64:65], v[68:69]\n"
      "v_mul_f32 v64, v64, v44\n"
      "v_mul_f32 v65, v65, v44\n"
      "v_cvt_pk_bf16_f32 v46, v64, v65\n"
      "s_cbranch_scc1 .Lgu%=\n"
      "v_lshlrev_b32 v64, 16, v46\n"
      "v_and_b32 v65, 0xffff0000, v46\n"
      "v_mul_f32 v66, 0xbfb8aa3b, v64\n"
      "v_mul_f32 v67, 0xbfb8aa3b, v65\n"
      "v_exp_f32 v66, v66\n"
      "v_exp_f32 v67, v67\n"
      "v_add_f32 v66, 1.0, v66\n"
      "v_add_f32 v67, 1.0, v67\n"
      "v_rcp_f32 v66, v66\n"
      "v_rcp_f32 v67, v67\n"
      "v_mul_f32 v64, v64, v66\n"
      "v_mul_f32 v65, v65, v67\n"
      "v_cvt_pk_bf16_f32 v46, v64, v65\n"
      ".Lgu%=:\n"
      "buffer_store_dwordx2 v[46:47], v37, s[16:19], s45 offen sc1\n"
      "s_cmp_lg_u32 s33, 4\n"
      "s_cbranch_scc1 .Lga%=\n"
      "v_mov_b32 v64, s15\n"
      "v_mov_b32 v65, s15\n"
      "buffer_store_dwordx2 v[64:65], off, s[16:19], s42 sc1\n"
      ".Lga%=:\n"
      "s_cmp_eq_u32 s33, 7\n"
      "s_mov_b32 s24, s36\n"
      "s_mov_b32 s25, s37\n"
      "s_mov_b32 s26, 0x400000\n"
      "s_cbranch_scc1 .LZ%=\n"
      "s_endpgm\n"
      ".Lgss%=:\n"
      "s_sleep 1\n"
      "s_sub_u32 s59, s59, 1\n"
      "s_cbranch_scc1 .Lgso%=\n"
      "buffer_load_dwordx2 v[62:63], v45, s[16:19], s58 offen sc1\n"
      "s_waitcnt vmcnt(0)\n"
      "s_branch .Lgs%=\n"
      ".LD2%=:\n"
      "s_setprio 0\n"
      "s_mul_i32 s35, s48, 14\n"
      "s_add_u32 s35, s35, s49\n"
      "s_lshr_b32 s52, s35, 2\n"
      "s_and_b32 s54, s35, 3\n"
      "v_lshlrev_b32 v42, 4, v49\n"
      "s_lshl_b32 s50, s52, 18\n"
      "s_lshl_b32 s51, s54, 9\n"
      "s_add_u32 s50, s50, s51\n"
      "s_lshl_b32 s51, s33, 7\n"
      "s_add_u32 s50, s50, s51\n"
      "v_lshl_add_u32 v32, v50, 14, v42\n"
      "v_add_u32 v32, s50, v32\n"
      "v_lshrrev_b32 v41, 1, v49\n"
      "v_and_b32 v43, 1, v41\n"
      "v_mul_u32_u24 v36, 0x120, v41\n"
      "v_lshlrev_b32 v43, 4, v43\n"
      "v_sub_u32 v36, v36, v43\n"
      "v_and_b32 v43, 1, v49\n"
      "v_lshl_add_u32 v36, v43, 7, v36\n"
      "v_lshrrev_b32 v41, 4, v48\n"
      "v_lshl_add_u32 v36, v41, 4, v36\n"
      "v_and_b32 v43, 8, v48\n"
      "v_add_u32 v36, v36, v43\n"
      "s_lshr_b32 s53, s33, 1\n"
      "s_mul_i32 s50, s53, 1152\n"
      "s_and_b32 s55, s33, 1\n"
      "s_lshl_b32 s51, s55, 6\n"
      "s_add_u32 s50, s50, s51\n"
      "s_add_u32 s50, s50, 149504\n"
      "v_add_u32 v36, s50, v36\n"
      "v_and_b32 v43, 1, v41\n"
      "v_mul_u32_u24 v37, 0x120, v41\n"
      "v_lshlrev_b32 v43, 4, v43\n"
      "v_sub_u32 v37, v37, v43\n"
      "v_and_b32 v43, 15, v48\n"
      "v_lshl_add_u32 v37, v43, 4, v37\n"
      "v_add_u32 v37, 0x24800, v37\n"
      "v_lshlrev_b32 v35, 4, v48\n"
      "s_lshl_b32 s46, s52, 17\n"
      "s_lshl_b32 s50, s53, 16\n"
      "s_add_u32 s46, s46, s50\n"
      "s_lshl_b32 s50, s55, 10\n"
      "s_add_u32 s46, s46, s50\n"
      "s_add_u32 s46, s46, 524288\n"
      "v_and_b32 v41, 3, v50\n"
      "v_lshrrev_b32 v43, 5, v48\n"
      "v_lshl_add_u32 v41, v41, 1, v43\n"
      "v_mul_u32_u24 v38, 0x1c00, v41\n"
      "v_add_u32 v38, v38, v42\n"
      "s_lshl_b32 s50, s54, 2\n"
      "s_add_u32 s50, s50, s33\n"
      "s_mul_i32 s47, s50, 57344\n"
      "s_lshl_b32 s50, s52, 8\n"
      "s_add_u32 s47, s47, s50\n"
      "s_add_u32 s47, s47, 4194304\n"
      "s_mov_b32 s62, 0x05040100\n"
      "s_mov_b32 s63, 0x07060302\n"
      "s_lshl_b32 s50, s33, 2\n"
      "s_add_u32 s50, s50, 140864\n"
      "v_mov_b32 v41, s50\n"
      "v_mov_b32 v42, 0x22640\n"
      "s_movk_i32 s59, 0x3fff\n"
      ".Ldf%=:\n"
      "s_sleep 1\n"
      "ds_read_b128 v[240:243], v42\n"
      "ds_read_b32 v43, v41 offset:16\n"
      "s_sub_u32 s59, s59, 1\n"
      "s_cbranch_scc1 .Ldfo%=\n"
      "s_waitcnt lgkmcnt(0)\n"
      "v_xor_b32 v240, s15, v240\n"
      "v_bitop3_b32 v240, s15, v241, v240 bitop3:0xbe\n"
      "v_bitop3_b32 v240, s15, v242, v240 bitop3:0xbe\n"
      "v_bitop3_b32 v240, s15, v243, v240 bitop3:0xbe\n"
      "v_bitop3_b32 v240, s15, v43, v240 bitop3:0xbe\n"
      "v_cmp_ne_u32 vcc, 0, v240\n"
      "s_nop 1\n"
      "s_cbranch_vccnz .Ldf%=\n"
      ".Ldfo%=:\n"
      "s_mov_b32 s60, 0\n"
      "buffer_load_dwordx4 v[128:131], v32, s[28:31], s60 offen\n"
      "s_mov_b32 s60, 2048\n"
      "buffer_load_dwordx4 v[132:135], v32, s[28:31], s60 offen\n"
      "s_mov_b32 s60, 4096\n"
      "buffer_load_dwordx4 v[136:139], v32, s[28:31], s60 offen\n"
      "s_mov_b32 s60, 6144\n"
      "buffer_load_dwordx4 v[140:143], v32, s[28:31], s60 offen\n"
      "s_mov_b32 s60, 8192\n"
      "buffer_load_dwordx4 v[144:147], v32, s[28:31], s60 offen\n"
      "s_mov_b32 s60, 10240\n"
      "buffer_load_dwordx4 v[148:151], v32, s[28:31], s60 offen\n"
      "s_mov_b32 s60, 12288\n"
      "buffer_load_dwordx4 v[152:155], v32, s[28:31], s60 offen\n"
      "s_mov_b32 s60, 14336\n"
      "buffer_load_dwordx4 v[156:159], v32, s[28:31], s60 offen\n"
      "s_waitcnt vmcnt(7)\n"
      "s_nop 0\n"
      "s_mov_b32 s60, 131072\n"
      "buffer_load_dwordx4 v[160:163], v32, s[28:31], s60 offen\n"
      "s_waitcnt vmcnt(7)\n"
      "s_nop 0\n"
      "s_mov_b32 s60, 133120\n"
      "buffer_load_dwordx4 v[164:167], v32, s[28:31], s60 offen\n"
      "v_perm_b32 v64, v132, v128, s62\n"
      "v_perm_b32 v68, v132, v128, s63\n"
      "v_perm_b32 v72, v133, v129, s62\n"
      "v_perm_b32 v76, v133, v129, s63\n"
      "v_perm_b32 v80, v134, v130, s62\n"
      "v_perm_b32 v84, v134, v130, s63\n"
      "v_perm_b32 v88, v135, v131, s62\n"
      "v_perm_b32 v92, v135, v131, s63\n"
      "s_waitcnt vmcnt(7)\n"
      "s_nop 0\n"
      "s_mov_b32 s60, 135168\n"
      "buffer_load_dwordx4 v[168:171], v32, s[28:31], s60 offen\n"
      "s_waitcnt vmcnt(7)\n"
      "s_nop 0\n"
      "s_mov_b32 s60, 137216\n"
      "buffer_load_dwordx4 v[172:175], v32, s[28:31], s60 offen\n"
      "v_perm_b32 v65, v140, v136, s62\n"
      "v_perm_b32 v69, v140, v136, s63\n"
      "v_perm_b32 v73, v141, v137, s62\n"
      "v_perm_b32 v77, v141, v137, s63\n"
      "v_perm_b32 v81, v142, v138, s62\n"
      "v_perm_b32 v85, v142, v138, s63\n"
      "v_perm_b32 v89, v143, v139, s62\n"
      "v_perm_b32 v93, v143, v139, s63\n"
      "s_waitcnt vmcnt(7)\n"
      "s_nop 0\n"
      "s_mov_b32 s60, 139264\n"
      "buffer_load_dwordx4 v[176:179], v32, s[28:31], s60 offen\n"
      "s_waitcnt vmcnt(7)\n"
      "s_nop 0\n"
      "s_mov_b32 s60, 141312\n"
      "buffer_load_dwordx4 v[180:183], v32, s[28:31], s60 offen\n"
      "v_perm_b32 v66, v148, v144, s62\n"
      "v_perm_b32 v70, v148, v144, s63\n"
      "v_perm_b32 v74, v149, v145, s62\n"
      "v_perm_b32 v78, v149, v145, s63\n"
      "v_perm_b32 v82, v150, v146, s62\n"
      "v_perm_b32 v86, v150, v146, s63\n"
      "v_perm_b32 v90, v151, v147, s62\n"
      "v_perm_b32 v94, v151, v147, s63\n"
      "s_waitcnt vmcnt(7)\n"
      "s_nop 0\n"
      "s_mov_b32 s60, 143360\n"
      "buffer_load_dwordx4 v[184:187], v32, s[28:31], s60 offen\n"
      "s_waitcnt vmcnt(7)\n"
      "s_nop 0\n"
      "s_mov_b32 s60, 145408\n"
      "buffer_load_dwordx4 v[188:191], v32, s[28:31], s60 offen\n"
      "v_perm_b32 v67, v156, v152, s62\n"
      "v_perm_b32 v71, v156, v152, s63\n"
      "v_perm_b32 v75, v157, v153, s62\n"
      "v_perm_b32 v79, v157, v153, s63\n"
      "v_perm_b32 v83, v158, v154, s62\n"
      "v_perm_b32 v87, v158, v154, s63\n"
      "v_perm_b32 v91, v159, v155, s62\n"
      "v_perm_b32 v95, v159, v155, s63\n"
      "s_waitcnt vmcnt(7)\n"
      "s_nop 0\n"
      "s_movk_i32 s59, 0x3fff\n"
      "v_add_u32 v41, 0xffffffc0, v41\n"
      ".Lde%=:\n"
      "ds_read_b32 v43, v41\n"
      "s_sub_u32 s59, s59, 1\n"
      "s_cbranch_scc1 .Ldeo%=\n"
      "s_waitcnt lgkmcnt(0)\n"
      "v_cmp_ne_u32 vcc, s15, v43\n"
      "s_nop 1\n"
      "s_cbranch_vccz .Ldeo%=\n"
      "s_sleep 2\n"
      "s_branch .Lde%=\n"
      ".Ldeo%=:\n"
      "s_cmp_lg_u32 s59, 0x3ffe\n"
      "s_cbranch_scc0 .Ldep%=\n"
      "s_sleep 6\n"
      ".Ldep%=:\n"
      ".Ldpc1_%=:\n"
      "s_memrealtime s[64:65]\n"
      "s_waitcnt lgkmcnt(0)\n"
      "s_sub_u32 s64, s64, s12\n"
      "s_cmp_ge_u32 s64, 640\n"
      "s_cbranch_scc1 .Ldpd1_%=\n"
      "s_sleep 1\n"
      "s_branch .Ldpc1_%=\n"
      ".Ldpd1_%=:\n"
      "buffer_load_dwordx4 v[224:227], v35, s[16:19], s46 offen sc1\n"
      "buffer_load_dwordx4 v[228:231], v35, s[16:19], s46 offen offset:2048 sc1\n"
      "s_waitcnt vmcnt(8)\n"
      "s_nop 0\n"
      "v_perm_b32 v96, v164, v160, s62\n"
      "v_perm_b32 v100, v164, v160, s63\n"
      "v_perm_b32 v104, v165, v161, s62\n"
      "v_perm_b32 v108, v165, v161, s63\n"
      "v_perm_b32 v112, v166, v162, s62\n"
      "v_perm_b32 v116, v166, v162, s63\n"
      "v_perm_b32 v120, v167, v163, s62\n"
      "v_perm_b32 v124, v167, v163, s63\n"
      "s_waitcnt vmcnt(7)\n"
      "s_nop 0\n"
      "s_waitcnt vmcnt(6)\n"
      "s_nop 0\n"
      "v_perm_b32 v97, v172, v168, s62\n"
      "v_perm_b32 v101, v172, v168, s63\n"
      "v_perm_b32 v105, v173, v169, s62\n"
      "v_perm_b32 v109, v173, v169, s63\n"
      "v_perm_b32 v113, v174, v170, s62\n"
      "v_perm_b32 v117, v174, v170, s63\n"
      "v_perm_b32 v121, v175, v171, s62\n"
      "v_perm_b32 v125, v175, v171, s63\n"
      "s_waitcnt vmcnt(5)\n"
      "s_nop 0\n"
      "s_waitcnt vmcnt(4)\n"
      "s_nop 0\n"
      "v_perm_b32 v98, v180, v176, s62\n"
      "v_perm_b32 v102, v180, v176, s63\n"
      "v_perm_b32 v106, v181, v177, s62\n"
      "v_perm_b32 v110, v181, v177, s63\n"
      "v_perm_b32 v114, v182, v178, s62\n"
      "v_perm_b32 v118, v182, v178, s63\n"
      "v_perm_b32 v122, v183, v179, s62\n"
      "v_perm_b32 v126, v183, v179, s63\n"
      "s_waitcnt vmcnt(3)\n"
      "s_nop 0\n"
      "s_waitcnt vmcnt(2)\n"
      "s_nop 0\n"
      "v_perm_b32 v99, v188, v184, s62\n"
      "v_perm_b32 v103, v188, v184, s63\n"
      "v_perm_b32 v107, v189, v185, s62\n"
      "v_perm_b32 v111, v189, v185, s63\n"
      "v_perm_b32 v115, v190, v186, s62\n"
      "v_perm_b32 v119, v190, v186, s63\n"
      "v_perm_b32 v123, v191, v187, s62\n"
      "v_perm_b32 v127, v191, v187, s63\n"
      "v_mov_b32 v137, s15\n"
      "v_mov_b32 v139, s15\n"
      "v_mov_b32 v141, s15\n"
      "v_mov_b32 v143, s15\n"
      "s_movk_i32 s59, 0x7fff\n"
      "s_waitcnt vmcnt(0)\n"
      "s_nop 0\n"
      ".Ldg%=:\n"
      "v_xor_b32 v232, s15, v225\n"
      "v_bitop3_b32 v232, s15, v227, v232 bitop3:0xbe\n"
      "v_bitop3_b32 v232, s15, v229, v232 bitop3:0xbe\n"
      "v_bitop3_b32 v232, s15, v231, v232 bitop3:0xbe\n"
      "v_cmp_ne_u32 vcc, 0, v232\n"
      "v_lshlrev_b32 v232, 16, v224\n"
      "v_and_b32 v233, 0xffff0000, v224\n"
      "v_lshlrev_b32 v234, 16, v226\n"
      "v_and_b32 v235, 0xffff0000, v226\n"
      "s_cbranch_vccnz .Ldgs%=\n"
      ".Ldgo%=:\n"
      "v_lshlrev_b32 v236, 16, v228\n"
      "v_and_b32 v237, 0xffff0000, v228\n"
      "v_lshlrev_b32 v238, 16, v230\n"
      "v_and_b32 v239, 0xffff0000, v230\n"
      "v_pk_mul_f32 v[232:233], v[232:233], v[236:237]\n"
      "v_pk_mul_f32 v[234:235], v[234:235], v[238:239]\n"
      "v_cvt_pk_bf16_f32 v232, v232, v233\n"
      "v_cvt_pk_bf16_f32 v233, v234, v235\n"
      "ds_write_b64 v36, v[232:233] \n"
      "ds_write_b32 v41, v137 offset:96\n"
      "ds_read_b128 v[240:243], v42 offset:32\n"
      "ds_read_b128 v[224:227], v37 \n"
      "ds_read_b128 v[228:231], v37 offset:1152\n"
      "s_movk_i32 s59, 0x7fff\n"
      ".Ldv%=:\n"
      "s_waitcnt lgkmcnt(2)\n"
      "v_xor_b32 v240, s15, v240\n"
      "v_bitop3_b32 v240, s15, v241, v240 bitop3:0xbe\n"
      "v_bitop3_b32 v240, s15, v242, v240 bitop3:0xbe\n"
      "v_bitop3_b32 v240, s15, v243, v240 bitop3:0xbe\n"
      "v_cmp_ne_u32 vcc, 0, v240\n"
      "s_nop 1\n"
      "s_cbranch_vccnz .Ldvs%=\n"
      ".Ldvo%=:\n"
      "s_waitcnt lgkmcnt(1)\n"
      "v_mfma_f32_16x16x32_bf16 v[192:195], v[64:67], v[224:227], 0\n"
      "s_waitcnt lgkmcnt(0)\n"
      "v_mfma_f32_16x16x32_bf16 v[192:195], v[96:99], v[228:231], v[192:195]\n"
      "v_mfma_f32_16x16x32_bf16 v[196:199], v[68:71], v[224:227], 0\n"
      "v_mfma_f32_16x16x32_bf16 v[196:199], v[100:103], v[228:231], v[196:199]\n"
      "v_mfma_f32_16x16x32_bf16 v[200:203], v[72:75], v[224:227], 0\n"
      "s_nop 4\n"
      "v_permlane32_swap_b32 v192, v193\n"
      "v_permlane32_swap_b32 v194, v195\n"
      "v_mfma_f32_16x16x32_bf16 v[200:203], v[104:107], v[228:231], v[200:203]\n"
      "v_mfma_f32_16x16x32_bf16 v[204:207], v[76:79], v[224:227], 0\n"
      "v_permlane32_swap_b32 v196, v197\n"
      "v_permlane32_swap_b32 v198, v199\n"
      "v_mfma_f32_16x16x32_bf16 v[204:207], v[108:111], v[228:231], v[204:207]\n"
      "v_add_f32_dpp v128, v193, v192 row_ror:8 row_mask:0xf bank_mask:0x3\n"
      "s_nop 1\n"
      "v_add_f32_dpp v128, v194, v195 row_ror:8 row_mask:0xf bank_mask:0xc\n"
      "v_mfma_f32_16x16x32_bf16 v[208:211], v[80:83], v[224:227], 0\n"
      "v_permlane32_swap_b32 v200, v201\n"
      "v_permlane32_swap_b32 v202, v203\n"
      "v_mfma_f32_16x16x32_bf16 v[208:211], v[112:115], v[228:231], v[208:211]\n"
      "v_add_f32_dpp v129, v197, v196 row_ror:8 row_mask:0xf bank_mask:0x3\n"
      "s_nop 1\n"
      "v_add_f32_dpp v129, v198, v199 row_ror:8 row_mask:0xf bank_mask:0xc\n"
      "v_mfma_f32_16x16x32_bf16 v[212:215], v[84:87], v[224:227], 0\n"
      "v_permlane32_swap_b32 v204, v205\n"
      "v_permlane32_swap_b32 v206, v207\n"
      "v_mfma_f32_16x16x32_bf16 v[212:215], v[116:119], v[228:231], v[212:215]\n"
      "v_add_f32_dpp v130, v201, v200 row_ror:8 row_mask:0xf bank_mask:0x3\n"
      "s_nop 1\n"
      "v_add_f32_dpp v130, v202, v203 row_ror:8 row_mask:0xf bank_mask:0xc\n"
      "v_mfma_f32_16x16x32_bf16 v[216:219], v[88:91], v[224:227], 0\n"
      "v_permlane32_swap_b32 v208, v209\n"
      "v_permlane32_swap_b32 v210, v211\n"
      "v_mfma_f32_16x16x32_bf16 v[216:219], v[120:123], v[228:231], v[216:219]\n"
      "v_add_f32_dpp v131, v205, v204 row_ror:8 row_mask:0xf bank_mask:0x3\n"
      "s_nop 1\n"
      "v_add_f32_dpp v131, v206, v207 row_ror:8 row_mask:0xf bank_mask:0xc\n"
      "v_mfma_f32_16x16x32_bf16 v[220:223], v[92:95], v[224:227], 0\n"
      "v_permlane32_swap_b32 v212, v213\n"
      "v_permlane32_swap_b32 v214, v215\n"
      "v_mfma_f32_16x16x32_bf16 v[220:223], v[124:127], v[228:231], v[220:223]\n"
      "v_add_f32_dpp v132, v209, v208 row_ror:8 row_mask:0xf bank_mask:0x3\n"
      "s_nop 1\n"
      "v_add_f32_dpp v132, v210, v211 row_ror:8 row_mask:0xf bank_mask:0xc\n"
      "v_cvt_pk_bf16_f32 v136, v128, v129\n"
      "v_cvt_pk_bf16_f32 v138, v130, v131\n"
      "buffer_store_dwordx4 v[136:139], v38, s[16:19], s47 offen offset:0 sc1\n"
      "v_permlane32_swap_b32 v216, v217\n"
      "v_permlane32_swap_b32 v218, v219\n"
      "v_add_f32_dpp v133, v213, v212 row_ror:8 row_mask:0xf bank_mask:0x3\n"
      "s_nop 1\n"
      "v_add_f32_dpp v133, v214, v215 row_ror:8 row_mask:0xf bank_mask:0xc\n"
      "v_permlane32_swap_b32 v220, v221\n"
      "v_permlane32_swap_b32 v222, v223\n"
      "v_add_f32_dpp v134, v217, v216 row_ror:8 row_mask:0xf bank_mask:0x3\n"
      "s_nop 1\n"
      "v_add_f32_dpp v134, v218, v219 row_ror:8 row_mask:0xf bank_mask:0xc\n"
      "v_add_f32_dpp v135, v221, v220 row_ror:8 row_mask:0xf bank_mask:0x3\n"
      "s_nop 1\n"
      "v_add_f32_dpp v135, v222, v223 row_ror:8 row_mask:0xf bank_mask:0xc\n"
      "v_cvt_pk_bf16_f32 v140, v132, v133\n"
      "v_cvt_pk_bf16_f32 v142, v134, v135\n"
      "buffer_store_dwordx4 v[140:143], v38, s[16:19], s47 offen offset:128 sc1\n"
      "s_endpgm\n"
      ".LZ%=:\n"
      "s_setprio 0\n"
      "v_and_b32 v48, 63, v0\n"
      "v_and_b32 v49, 7, v0\n"
      "v_lshlrev_b32 v108, 3, v49\n"
      "s_lshl4_add_u32 s35, s48, s49\n"
      "s_lshl_b32 s41, s35, 4\n"
      "s_and_b32 s43, s41, 0xffffff80\n"
      "s_add_u32 s43, s43, 67584\n"
      "buffer_load_dwordx2 v[104:105], v108, s[16:19], s43 offen sc1\n"
      "s_mul_i32 s44, s35, 7168\n"
      "s_add_u32 s44, s44, 4194304\n"
      "s_add_u32 s67, s44, 0x1000\n"
      "v_lshrrev_b32 v109, 5, v48\n"
      "v_and_b32 v110, 8, v48\n"
      "v_lshl_add_u32 v109, v109, 2, v110\n"
      "v_and_b32 v113, 16, v48\n"
      "s_movk_i32 s59, 0x7fff\n"
      "s_waitcnt vmcnt(0)\n"
      "s_movk_i32 s61, 0x7ff\n"
      ".Ldzh%=:\n"
      "v_cmp_ne_u32 vcc, s15, v105\n"
      "v_bitop3_b32 v110, v104, s61, v104 bitop3:0x0c\n"
      "v_not_b32 v112, v104\n"
      "s_cbranch_vccnz .Ldzs%=\n"
      ".Ldzo%=:\n"
      "v_lshl_add_u32 v110, v110, 11, v109\n"
      "v_lshlrev_b32 v108, 4, v48\n"
      "buffer_load_dword v111, v110, s[20:23], s41 offen\n"
      "v_lshl_or_b32 v113, v113, 26, v110\n"
      "v_ashrrev_i32 v112, 31, v112\n"
      "v_or_b32 v112, 0x80000000, v112\n"
      "v_xor_b32 v112, v112, v104\n"
      "v_and_b32 v112, 0xffff0000, v112\n"
      "v_mul_f32 v112, 0xbfb8aa3b, v112\n"
      "v_exp_f32 v112, v112\n"
      "s_nop 0\n"
      "v_add_f32 v112, 1.0, v112\n"
      "v_rcp_f32 v112, v112\n"
      "s_nop 0\n"
      "v_cvt_pk_bf16_f32 v112, v112, v112\n"
      "v_and_b32 v112, 0xffff0000, v112\n"
      ".Ldpc2_%=:\n"
      "s_memrealtime s[64:65]\n"
      "s_waitcnt lgkmcnt(0)\n"
      "s_sub_u32 s64, s64, s12\n"
      "s_cmp_ge_u32 s64, 825\n"
      "s_cbranch_scc1 .Ldpd2_%=\n"
      "s_sleep 1\n"
      "s_branch .Ldpc2_%=\n"
      ".Ldpd2_%=:\n"
      "buffer_load_dwordx4 v[64:67], v108, s[16:19], s44 offen offset:0 sc1\n"
      "buffer_load_dwordx4 v[68:71], v108, s[16:19], s44 offen offset:1024 sc1\n"
      "buffer_load_dwordx4 v[72:75], v108, s[16:19], s44 offen offset:2048 sc1\n"
      "buffer_load_dwordx4 v[76:79], v108, s[16:19], s44 offen offset:3072 sc1\n"
      "buffer_load_dwordx4 v[80:83], v108, s[16:19], s67 offen offset:0 sc1\n"
      "buffer_load_dwordx4 v[84:87], v108, s[16:19], s67 offen offset:1024 sc1\n"
      "buffer_load_dwordx4 v[88:91], v108, s[16:19], s67 offen offset:2048 sc1\n"
      "s_movk_i32 s59, 0x7fff\n"
      "s_waitcnt vmcnt(0)\n"
      "s_nop 0\n"
      "v_lshlrev_b32 v106, 16, v111\n"
      "v_and_b32 v107, 0xffff0000, v111\n"
      ".Ldq%=:\n"
      "v_xor_b32 v96, s15, v65\n"
      "v_xor_b32 v97, s15, v67\n"
      "v_bitop3_b32 v96, s15, v69, v96 bitop3:0xbe\n"
      "v_bitop3_b32 v97, s15, v71, v97 bitop3:0xbe\n"
      "v_bitop3_b32 v96, s15, v73, v96 bitop3:0xbe\n"
      "v_bitop3_b32 v97, s15, v75, v97 bitop3:0xbe\n"
      "v_bitop3_b32 v96, s15, v77, v96 bitop3:0xbe\n"
      "v_bitop3_b32 v97, s15, v79, v97 bitop3:0xbe\n"
      "v_bitop3_b32 v96, s15, v81, v96 bitop3:0xbe\n"
      "v_bitop3_b32 v97, s15, v83, v97 bitop3:0xbe\n"
      "v_bitop3_b32 v96, s15, v85, v96 bitop3:0xbe\n"
      "v_bitop3_b32 v97, s15, v87, v97 bitop3:0xbe\n"
      "v_bitop3_b32 v96, s15, v89, v96 bitop3:0xbe\n"
      "v_bitop3_b32 v97, s15, v91, v97 bitop3:0xbe\n"
      "v_or_b32 v96, v96, v97\n"
      "v_cmp_ne_u32 vcc, 0, v96\n"
      "v_lshlrev_b32 v96, 16, v64\n"
      "v_and_b32 v97, 0xffff0000, v64\n"
      "v_lshlrev_b32 v98, 16, v66\n"
      "v_and_b32 v99, 0xffff0000, v66\n"
      "s_cbranch_vccnz .Ldqs%=\n"
      ".Ldqo%=:\n"
      "v_lshlrev_b32 v100, 16, v68\n"
      "v_and_b32 v101, 0xffff0000, v68\n"
      "v_lshlrev_b32 v102, 16, v70\n"
      "v_and_b32 v103, 0xffff0000, v70\n"
      "v_add_f32 v96, v96, v100\n"
      "v_add_f32 v97, v97, v101\n"
      "v_add_f32 v98, v98, v102\n"
      "v_add_f32 v99, v99, v103\n"
      "v_lshlrev_b32 v100, 16, v72\n"
      "v_and_b32 v101, 0xffff0000, v72\n"
      "v_lshlrev_b32 v102, 16, v74\n"
      "v_and_b32 v103, 0xffff0000, v74\n"
      "v_add_f32 v96, v96, v100\n"
      "v_add_f32 v97, v97, v101\n"
      "v_add_f32 v98, v98, v102\n"
      "v_add_f32 v99, v99, v103\n"
      "v_lshlrev_b32 v100, 16, v76\n"
      "v_and_b32 v101, 0xffff0000, v76\n"
      "v_lshlrev_b32 v102, 16, v78\n"
      "v_and_b32 v103, 0xffff0000, v78\n"
      "v_add_f32 v96, v96, v100\n"
      "v_add_f32 v97, v97, v101\n"
      "v_add_f32 v98, v98, v102\n"
      "v_add_f32 v99, v99, v103\n"
      "v_lshlrev_b32 v100, 16, v80\n"
      "v_and_b32 v101, 0xffff0000, v80\n"
      "v_lshlrev_b32 v102, 16, v82\n"
      "v_and_b32 v103, 0xffff0000, v82\n"
      "v_add_f32 v96, v96, v100\n"
      "v_add_f32 v97, v97, v101\n"
      "v_add_f32 v98, v98, v102\n"
      "v_add_f32 v99, v99, v103\n"
      "v_lshlrev_b32 v100, 16, v84\n"
      "v_and_b32 v101, 0xffff0000, v84\n"
      "v_lshlrev_b32 v102, 16, v86\n"
      "v_and_b32 v103, 0xffff0000, v86\n"
      "v_add_f32 v96, v96, v100\n"
      "v_add_f32 v97, v97, v101\n"
      "v_add_f32 v98, v98, v102\n"
      "v_add_f32 v99, v99, v103\n"
      "v_lshlrev_b32 v100, 16, v88\n"
      "v_and_b32 v101, 0xffff0000, v88\n"
      "v_lshlrev_b32 v102, 16, v90\n"
      "v_and_b32 v103, 0xffff0000, v90\n"
      "v_add_f32 v96, v96, v100\n"
      "v_add_f32 v97, v97, v101\n"
      "v_add_f32 v98, v98, v102\n"
      "v_add_f32 v99, v99, v103\n"
      "s_nop 0\n"
      "v_permlane32_swap_b32 v96, v98\n"
      "v_permlane32_swap_b32 v97, v99\n"
      "v_add_f32 v96, v96, v98\n"
      "v_add_f32 v97, v97, v99\n"
      "v_mov_b32 v98, v96\n"
      "v_mov_b32 v99, v97\n"
      "s_nop 0\n"
      "v_permlane16_swap_b32 v96, v98\n"
      "v_permlane16_swap_b32 v97, v99\n"
      "v_add_f32 v96, v96, v98\n"
      "v_add_f32 v97, v97, v99\n"
      "v_cvt_pk_bf16_f32 v96, v96, v97\n"
      "v_and_b32 v97, 0xffff0000, v96\n"
      "v_lshlrev_b32 v96, 16, v96\n"
      "v_mul_f32 v97, v97, v112\n"
      "v_mul_f32 v96, v96, v112\n"
      "v_cvt_pk_bf16_f32 v96, v96, v97\n"
      "v_and_b32 v97, 0xffff0000, v96\n"
      "v_lshlrev_b32 v96, 16, v96\n"
      "v_add_f32 v97, v97, v107\n"
      "v_add_f32 v96, v96, v106\n"
      "v_cvt_pk_bf16_f32 v96, v96, v97\n"
      "buffer_atomic_pk_add_bf16 v96, v113, s[24:27], s41 offen\n"
      "s_endpgm\n"
      ".Ldgs%=:\n"
      "s_sleep 1\n"
      "s_sub_u32 s59, s59, 1\n"
      "s_cbranch_scc1 .Ldgo%=\n"
      "buffer_load_dwordx4 v[224:227], v35, s[16:19], s46 offen sc1\n"
      "buffer_load_dwordx4 v[228:231], v35, s[16:19], s46 offen offset:2048 sc1\n"
      "s_waitcnt vmcnt(0)\n"
      "s_branch .Ldg%=\n"
      ".Ldvs%=:\n"
      "s_sub_u32 s59, s59, 1\n"
      "s_cbranch_scc1 .Ldvo%=\n"
      "s_waitcnt lgkmcnt(0)\n"
      "ds_read_b128 v[240:243], v42 offset:32\n"
      "ds_read_b128 v[224:227], v37 \n"
      "ds_read_b128 v[228:231], v37 offset:1152\n"
      "s_waitcnt lgkmcnt(0)\n"
      "s_branch .Ldv%=\n"
      ".Ldzs%=:\n"
      "s_sleep 2\n"
      "s_sub_u32 s59, s59, 1\n"
      "s_cbranch_scc1 .Ldzo%=\n"
      "buffer_load_dwordx2 v[104:105], v108, s[16:19], s43 offen sc1\n"
      "s_waitcnt vmcnt(0)\n"
      "s_branch .Ldzh%=\n"
      ".Ldqs%=:\n"
      "s_sleep 1\n"
      "s_sub_u32 s59, s59, 1\n"
      "s_cbranch_scc1 .Ldqo%=\n"
      "v_cmp_ne_u32 vcc, s15, v65\n"
      "v_cmp_ne_u32 s[64:65], s15, v67\n"
      "s_nop 0\n"
      "s_or_b64 exec, vcc, s[64:65]\n"
      "s_cbranch_scc0 .Ldqk0%=\n"
      "buffer_load_dwordx4 v[64:67], v108, s[16:19], s44 offen offset:0 sc1\n"
      ".Ldqk0%=:\n"
      "s_mov_b64 exec, -1\n"
      "v_cmp_ne_u32 vcc, s15, v69\n"
      "v_cmp_ne_u32 s[64:65], s15, v71\n"
      "s_nop 0\n"
      "s_or_b64 exec, vcc, s[64:65]\n"
      "s_cbranch_scc0 .Ldqk1%=\n"
      "buffer_load_dwordx4 v[68:71], v108, s[16:19], s44 offen offset:1024 sc1\n"
      ".Ldqk1%=:\n"
      "s_mov_b64 exec, -1\n"
      "v_cmp_ne_u32 vcc, s15, v73\n"
      "v_cmp_ne_u32 s[64:65], s15, v75\n"
      "s_nop 0\n"
      "s_or_b64 exec, vcc, s[64:65]\n"
      "s_cbranch_scc0 .Ldqk2%=\n"
      "buffer_load_dwordx4 v[72:75], v108, s[16:19], s44 offen offset:2048 sc1\n"
      ".Ldqk2%=:\n"
      "s_mov_b64 exec, -1\n"
      "v_cmp_ne_u32 vcc, s15, v77\n"
      "v_cmp_ne_u32 s[64:65], s15, v79\n"
      "s_nop 0\n"
      "s_or_b64 exec, vcc, s[64:65]\n"
      "s_cbranch_scc0 .Ldqk3%=\n"
      "buffer_load_dwordx4 v[76:79], v108, s[16:19], s44 offen offset:3072 sc1\n"
      ".Ldqk3%=:\n"
      "s_mov_b64 exec, -1\n"
      "v_cmp_ne_u32 vcc, s15, v81\n"
      "v_cmp_ne_u32 s[64:65], s15, v83\n"
      "s_nop 0\n"
      "s_or_b64 exec, vcc, s[64:65]\n"
      "s_cbranch_scc0 .Ldqk4%=\n"
      "buffer_load_dwordx4 v[80:83], v108, s[16:19], s67 offen offset:0 sc1\n"
      ".Ldqk4%=:\n"
      "s_mov_b64 exec, -1\n"
      "v_cmp_ne_u32 vcc, s15, v85\n"
      "v_cmp_ne_u32 s[64:65], s15, v87\n"
      "s_nop 0\n"
      "s_or_b64 exec, vcc, s[64:65]\n"
      "s_cbranch_scc0 .Ldqk5%=\n"
      "buffer_load_dwordx4 v[84:87], v108, s[16:19], s67 offen offset:1024 sc1\n"
      ".Ldqk5%=:\n"
      "s_mov_b64 exec, -1\n"
      "v_cmp_ne_u32 vcc, s15, v89\n"
      "v_cmp_ne_u32 s[64:65], s15, v91\n"
      "s_nop 0\n"
      "s_or_b64 exec, vcc, s[64:65]\n"
      "s_cbranch_scc0 .Ldqk6%=\n"
      "buffer_load_dwordx4 v[88:91], v108, s[16:19], s67 offen offset:2048 sc1\n"
      ".Ldqk6%=:\n"
      "s_mov_b64 exec, -1\n"
      "s_waitcnt vmcnt(0)\n"
      "s_branch .Ldq%=\n"
      ".LT%=:\n"
      "v_mov_b32 v48, 0x22680\n"
      "ds_read_b32 v48, v48 \n"
      "s_waitcnt lgkmcnt(0)\n"
      "v_readfirstlane_b32 s12, v48\n"
      "v_mov_b32 v48, v43\n"
      "v_and_b32 v49, 7, v43\n"
      "v_lshrrev_b32 v50, 3, v43\n"
      "s_lshl_b32 s42, s34, 3\n"
      "s_add_u32 s42, s42, 2048\n"
      "s_lshl_b32 s43, s48, 8\n"
      "s_and_b32 s50, s49, 1\n"
      "s_lshl_b32 s50, s50, 7\n"
      "s_add_u32 s43, s43, s50\n"
      "s_add_u32 s43, s43, 65536\n"
      "s_mov_b32 s20, s64\n"
      "s_and_b32 s21, s65, 0xffff\n"
      "s_mov_b32 s22, 0x400000\n"
      "s_mov_b32 s23, 0x20000\n"
      "s_mov_b32 s28, s84\n"
      "s_and_b32 s29, s85, 0xffff\n"
      "s_mov_b32 s30, 0x700000\n"
      "s_mov_b32 s31, 0x20000\n"
      "s_sleep 0\n"
      ".LS%=:\n"
      "v_lshlrev_b32 v112, 4, v0\n"
      "v_lshlrev_b32 v113, 2, v0\n"
      "v_sub_u32 v113, 0xfff, v113\n"
      "v_lshlrev_b32 v115, 2, v48\n"
      "v_mov_b32 v116, 0\n"
      "ds_write_b32 v115, v116 offset:27136\n"
      "ds_write_b32 v116, v116 offset:26400\n"
      "v_add_u32 v115, 0x140, v115\n"
      "v_mov_b32 v114, v115\n"
      "s_lshl_b32 s50, s33, 2\n"
      "s_add_u32 s50, s50, 256\n"
      "v_writelane_b32 v114, s50, 63\n"
      "s_lshl_b32 s50, s33, 5\n"
      "v_lshl_add_u32 v117, v49, 2, s50\n"
      "v_lshlrev_b32 v118, 5, v50\n"
      "s_and_b32 s50, s49, 1\n"
      "s_lshl_b32 s50, s50, 10\n"
      "v_lshl_add_u32 v119, v50, 7, s50\n"
      "s_lshr_b32 s51, s33, 2\n"
      "s_mul_i32 s51, s51, 61440\n"
      "s_add_u32 s51, s51, 393216\n"
      "s_mov_b32 s52, 0x80008000\n"
      "s_mov_b32 s53, 0xffff0000\n"
      "s_movk_i32 s59, 0x7fff\n"
      ".Lt_pc4_%=:\n"
      "s_memrealtime s[64:65]\n"
      "s_waitcnt lgkmcnt(0)\n"
      "s_sub_u32 s64, s64, s12\n"
      "s_cmp_ge_u32 s64, 300\n"
      "s_cbranch_scc1 .Lt_pd4_%=\n"
      "s_sleep 1\n"
      "s_branch .Lt_pc4_%=\n"
      ".Lt_pd4_%=:\n"
      ".Lt_p%=:\n"
      "buffer_load_dwordx4 v[72:75], v112, s[16:19], s51 offen sc1\n"
      "s_waitcnt vmcnt(0)\n"
      "v_cmp_ne_u32 vcc, s15, v73\n"
      "v_cmp_ne_u32 s[54:55], s15, v75\n"
      "v_pk_ashrrev_i16 v81, 15, v72 op_sel_hi:[0,1]\n"
      "v_pk_ashrrev_i16 v82, 15, v74 op_sel_hi:[0,1]\n"
      "s_or_b64 vcc, vcc, s[54:55]\n"
      "s_cbranch_scc1 .Lt_ps%=\n"
      ".Lt_po%=:\n"
      "v_bitop3_b32 v81, v72, v81, s52 bitop3:0x1e\n"
      "v_bitop3_b32 v82, v74, v82, s52 bitop3:0x1e\n"
      "v_lshl_or_b32 v76, v81, 16, v113\n"
      "v_and_or_b32 v77, v81, s53, v113\n"
      "v_lshl_or_b32 v78, v82, 16, v113\n"
      "v_and_or_b32 v79, v82, s53, v113\n"
      "v_add_u32 v77, -1, v77\n"
      "v_add_u32 v78, -2, v78\n"
      "v_add_u32 v79, -3, v79\n"
      "v_max3_u32 v80, v76, v77, v78\n"
      "v_max_u32 v80, v80, v79\n"
      "v_mov_b32 v104, v80\n"
      "v_med3_u32 v105, v76, v77, v78\n"
      "v_max_u32_dpp v80, v80, v80 row_shr:1 row_mask:0xf bank_mask:0xf bound_ctrl:1\n"
      "v_max3_u32 v83, v76, v77, v78\n"
      "v_min3_u32 v106, v76, v77, v78\n"
      "v_max_u32_dpp v80, v80, v80 row_shr:2 row_mask:0xf bank_mask:0xf bound_ctrl:1\n"
      "v_med3_u32 v106, v105, v106, v79\n"
      "v_med3_u32 v105, v83, v105, v79\n"
      "v_max_u32_dpp v80, v80, v80 row_shr:4 row_mask:0xf bank_mask:0xf bound_ctrl:1\n"
      "v_mov_b32 v107, 0x100\n"
      "s_nop 0\n"
      "v_max_u32_dpp v80, v80, v80 row_shr:8 row_mask:0xf bank_mask:0xf bound_ctrl:1\n"
      "s_nop 1\n"
      "v_max_u32_dpp v80, v80, v80 row_bcast:15 row_mask:0xa bank_mask:0xf\n"
      "s_nop 1\n"
      "v_max_u32_dpp v80, v80, v80 row_bcast:31 row_mask:0xc bank_mask:0xf\n"
      "ds_write_b32 v114, v80 offset:26112\n"
      "s_waitcnt lgkmcnt(0)\n"
      "s_barrier\n"
      "ds_read_b128 v[92:95], v116 offset:26368\n"
      "ds_read_b128 v[96:99], v116 offset:26384\n"
      "s_waitcnt lgkmcnt(1)\n"
      "v_min3_u32 v81, v92, v93, v94\n"
      "s_waitcnt lgkmcnt(0)\n"
      "v_min3_u32 v82, v96, v97, v98\n"
      "v_min3_u32 v81, v81, v95, v99\n"
      "v_min_u32 v81, v81, v82\n"
      "v_cmp_ge_u32 s[62:63], v105, v81\n"
      "v_cmp_ge_u32 vcc, v106, v81\n"
      "v_cmp_ge_u32 s[60:61], v104, v81\n"
      "v_cndmask_b32 v82, 1, 2, s[62:63]\n"
      "v_cndmask_b32 v82, v82, v107, vcc\n"
      "s_mov_b64 exec, s[60:61]\n"
      "ds_add_rtn_u32 v83, v116, v82 offset:26400\n"
      "s_waitcnt lgkmcnt(0)\n"
      "v_min_u32 v83, 0x50, v83\n"
      "v_lshlrev_b32 v83, 2, v83\n"
      "ds_write_b32 v83, v104 offset:27136\n"
      "s_mov_b64 exec, s[62:63]\n"
      "ds_write_b32 v83, v105 offset:27140\n"
      "s_mov_b64 exec, -1\n"
      "s_waitcnt lgkmcnt(0)\n"
      "s_barrier\n"
      "ds_read_b32 v81, v116 offset:26400\n"
      ".Lt_r%=:\n"
      "ds_read_b32 v100, v117 offset:27136\n"
      "ds_read_b128 v[92:95], v118 offset:27136\n"
      "ds_read_b128 v[96:99], v118 offset:27152\n"
      "v_mov_b32 v101, s15\n"
      "v_mov_b32 v89, 0\n"
      "s_waitcnt lgkmcnt(3)\n"
      "v_cmp_lt_u32 vcc, 64, v81\n"
      "s_movk_i32 s50, 0x7ff\n"
      "s_cbranch_vccnz .Lt_m%=\n"
      "s_waitcnt lgkmcnt(1)\n"
      "v_cmp_gt_u32 s[60:61], v92, v100\n"
      "v_cmp_gt_u32 s[62:63], v93, v100\n"
      "v_cmp_gt_u32 s[64:65], v94, v100\n"
      "v_cmp_gt_u32 s[66:67], v95, v100\n"
      "s_waitcnt lgkmcnt(0)\n"
      "v_cmp_gt_u32 s[68:69], v96, v100\n"
      "v_cmp_gt_u32 s[70:71], v97, v100\n"
      "v_cmp_gt_u32 s[72:73], v98, v100\n"
      "v_cmp_gt_u32 s[74:75], v99, v100\n"
      "v_bitop3_b32 v81, v100, s50, v100 bitop3:0x0c\n"
      "v_addc_co_u32 v89, vcc, 0, v89, s[60:61]\n"
      "v_addc_co_u32 v89, vcc, 0, v89, s[62:63]\n"
      "v_addc_co_u32 v89, vcc, 0, v89, s[64:65]\n"
      "v_addc_co_u32 v89, vcc, 0, v89, s[66:67]\n"
      "v_addc_co_u32 v89, vcc, 0, v89, s[68:69]\n"
      "v_addc_co_u32 v89, vcc, 0, v89, s[70:71]\n"
      "v_addc_co_u32 v89, vcc, 0, v89, s[72:73]\n"
      "v_addc_co_u32 v89, vcc, 0, v89, s[74:75]\n"
      "v_lshl_add_u32 v82, v81, 11, v119\n"
      "v_lshrrev_b32 v86, 9, v81\n"
      "v_mul_u32_u24 v86, 0xf000, v86\n"
      "v_lshl_add_u32 v81, v81, 3, v86\n"
      "v_add_u32_dpp v89, v89, v89 row_ror:8 row_mask:0xf bank_mask:0xf\n"
      "v_mov_b32 v83, v89\n"
      "s_nop 1\n"
      "v_permlane16_swap_b32 v89, v83\n"
      "v_add_u32 v89, v89, v83\n"
      "v_mov_b32 v83, v89\n"
      "s_nop 1\n"
      "v_permlane32_swap_b32 v89, v83\n"
      "v_add_u32 v89, v89, v83\n"
      "v_cmp_gt_u32 vcc, 8, v89\n"
      "v_lshlrev_b32 v89, 3, v89\n"
      "s_mov_b32 s51, 131072\n"
      "s_cbranch_vccz .Lt_n%=\n"
      "s_and_b64 exec, vcc, 0xff\n"
      "ds_write_b32 v89, v100 offset:26752\n"
      "buffer_load_dword v84, v81, s[16:19], s51 offen sc1\n"
      "s_mov_b64 exec, vcc\n"
      "buffer_load_dword v85, v82, s[20:23], 0 offen\n"
      "s_mov_b64 exec, -1\n"
      ".Lt_n%=:\n"
      "v_lshlrev_b32 v89, 3, v49\n"
      "s_waitcnt lgkmcnt(0)\n"
      "s_barrier\n"
      "s_cmp_lg_u32 s33, 0\n"
      "s_cbranch_scc1 .Lt_a%=\n"
      "ds_read_b32 v100, v89 offset:26752\n"
      "s_mov_b64 exec, 0xff\n"
      "s_waitcnt lgkmcnt(0)\n"
      "buffer_store_dwordx2 v[100:101], v89, s[16:19], s43 offen sc1\n"
      "s_mov_b64 exec, -1\n"
      "s_nop 1\n"
      "v_mov_b32 v96, s15\n"
      "v_mov_b32 v97, s15\n"
      "buffer_store_dwordx2 v[96:97], off, s[16:19], s42 sc1\n"
      ".Lt_a%=:\n"
      "s_cmp_lg_u32 s33, 1\n"
      "s_cbranch_scc1 .Lt_c%=\n"
      "ds_read_b32 v100, v89 offset:26752\n"
      "v_lshlrev_b32 v81, 3, v50\n"
      "ds_read_b32 v81, v81 offset:26752\n"
      "v_mov_b32 v92, 0\n"
      "v_mov_b32 v93, 0\n"
      "v_mov_b32 v94, 0\n"
      "v_mov_b32 v95, 0\n"
      "s_and_b32 s51, s43, 0x7ff\n"
      "v_lshlrev_b32 v82, 4, v49\n"
      "s_waitcnt lgkmcnt(0)\n"
      "v_bitop3_b32 v81, v81, s50, v81 bitop3:0x0c\n"
      "v_lshl_add_u32 v81, v81, 11, v82\n"
      "buffer_store_dwordx4 v[92:95], v81, s[24:27], s51 offen sc1\n"
      "s_waitcnt vmcnt(0)\n"
      "s_mov_b64 exec, 0xff\n"
      "buffer_store_dwordx2 v[100:101], v89, s[16:19], s43 offen offset:2048 sc1\n"
      "s_mov_b64 exec, -1\n"
      "s_nop 1\n"
      ".Lt_c%=:\n"
      "s_cmp_lg_u32 s33, 3\n"
      "s_cbranch_scc1 .Lt_z%=\n"
      "s_sleep 55\n"
      "s_branch .LZ%=\n"
      ".Lt_z%=:\n"
      "s_cmp_lg_u32 s34, 127\n"
      "s_cbranch_scc1 .Lt_e%=\n"
      "s_cmp_lg_u32 s33, 0\n"
      "s_cbranch_scc1 .Lt_e%=\n"
      "s_movk_i32 s51, 2048\n"
      "s_movk_i32 s59, 0x7fff\n"
      ".Lt_k%=:\n"
      "s_sleep 16\n"
      "buffer_load_dwordx4 v[92:95], v112, s[16:19], s51 offen sc1\n"
      "buffer_load_dwordx4 v[96:99], v112, s[16:19], s51 offen offset:1024 sc1\n"
      "s_sub_u32 s59, s59, 1\n"
      "s_cbranch_scc1 .Lt_kb%=\n"
      "s_waitcnt vmcnt(0)\n"
      "v_xor_b32 v86, s15, v93\n"
      "v_bitop3_b32 v86, s15, v95, v86 bitop3:0xbe\n"
      "v_bitop3_b32 v86, s15, v97, v86 bitop3:0xbe\n"
      "v_bitop3_b32 v86, s15, v99, v86 bitop3:0xbe\n"
      "v_cmp_ne_u32 vcc, 0, v86\n"
      "s_nop 1\n"
      "s_cbranch_vccnz .Lt_k%=\n"
      ".Lt_kb%=:\n"
      "buffer_store_dword v101, off, s[16:19], 0 sc1\n"
      ".Lt_e%=:\n"
      "s_waitcnt vmcnt(0)\n"
      "s_endpgm\n"
      ".Lt_ps%=:\n"
      "s_sleep 1\n"
      "s_sub_u32 s59, s59, 1\n"
      "s_cbranch_scc0 .Lt_p%=\n"
      "s_branch .Lt_po%=\n"
      ".Lt_m%=:\n"
      "s_waitcnt lgkmcnt(0)\n"
      "s_barrier\n"
      "s_mov_b32 s59, 0\n"
      "v_mov_b32 v90, 0\n"
      ".Lt_ml%=:\n"
      "s_nop 1\n"
      "v_max3_u32 v80, v76, v77, v78\n"
      "v_max_u32 v80, v80, v79\n"
      "s_nop 1\n"
      "v_max_u32_dpp v80, v80, v80 row_shr:1 row_mask:0xf bank_mask:0xf bound_ctrl:1\n"
      "s_nop 1\n"
      "v_max_u32_dpp v80, v80, v80 row_shr:2 row_mask:0xf bank_mask:0xf bound_ctrl:1\n"
      "s_nop 1\n"
      "v_max_u32_dpp v80, v80, v80 row_shr:4 row_mask:0xf bank_mask:0xf bound_ctrl:1\n"
      "s_nop 1\n"
      "v_max_u32_dpp v80, v80, v80 row_shr:8 row_mask:0xf bank_mask:0xf bound_ctrl:1\n"
      "s_nop 1\n"
      "v_max_u32_dpp v80, v80, v80 row_bcast:15 row_mask:0xa bank_mask:0xf\n"
      "s_nop 1\n"
      "v_max_u32_dpp v80, v80, v80 row_bcast:31 row_mask:0xc bank_mask:0xf\n"
      "s_nop 0\n"
      "v_readlane_b32 s50, v80, 63\n"
      "s_mov_b32 m0, s59\n"
      "s_nop 0\n"
      "v_cmp_ne_u32 vcc, s50, v76\n"
      "s_nop 1\n"
      "v_cndmask_b32 v76, 0, v76, vcc\n"
      "v_cmp_ne_u32 vcc, s50, v77\n"
      "s_nop 1\n"
      "v_cndmask_b32 v77, 0, v77, vcc\n"
      "v_cmp_ne_u32 vcc, s50, v78\n"
      "s_nop 1\n"
      "v_cndmask_b32 v78, 0, v78, vcc\n"
      "v_cmp_ne_u32 vcc, s50, v79\n"
      "s_nop 1\n"
      "v_cndmask_b32 v79, 0, v79, vcc\n"
      "v_writelane_b32 v90, s50, m0\n"
      "s_add_u32 s59, s59, 1\n"
      "s_cmp_lt_u32 s59, 8\n"
      "s_cbranch_scc1 .Lt_ml%=\n"
      "s_mov_b64 exec, 0xff\n"
      "ds_write_b32 v117, v90 offset:27136\n"
      "s_mov_b64 exec, -1\n"
      "v_mov_b32 v81, 0\n"
      "s_waitcnt lgkmcnt(0)\n"
      "s_barrier\n"
      "s_branch .Lt_r%=\n"
      ".LEnd%=:\n"
      : "={s[80:95]}"(sts), "={s[96:99]}"(st2)
      : [ka] "s"(__builtin_amdgcn_kernarg_segment_ptr()), [bid] "s"(blockIdx.x), "{v0}"(threadIdx.x), "s"((u32)(uintptr_t)sm)
      : "v8", "v9", "v10", "v11", "v12", "v13", "v14", "v15", "v16", "v17", "v18", "v19", "v20", "v21", "v22", "v23", "v24", "v25", "v26", "v27",
        "v28", "v29", "v30", "v31", "v32", "v33", "v34", "v35", "v36", "v37", "v38", "v39", "v40", "v41", "v42", "v43", "v44", "v45", "v46", "v47",
        "v48", "v49", "v50", "v51", "v52", "v53", "v54", "v55", "v56", "v57", "v58", "v59", "v60", "v61", "v62", "v63", "v64", "v65", "v66", "v67",
        "v68", "v69", "v70", "v71", "v72", "v73", "v74", "v75", "v76", "v77", "v78", "v79", "v80", "v81", "v82", "v83", "v84", "v85", "v86", "v87",
        "v88", "v89", "v90", "v91", "v92", "v93", "v94", "v95", "v96", "v97", "v98", "v99", "v100", "v101", "v102", "v103", "v104", "v105", "v106", "v107",
        "v108", "v109", "v110", "v111", "v112", "v113", "v114", "v115", "v116", "v117", "v118", "v119", "v120", "v121", "v122", "v123", "v124", "v125", "v126", "v127",
        "v128", "v129", "v130", "v131", "v132", "v133", "v134", "v135", "v136", "v137", "v138", "v139", "v140", "v141", "v142", "v143", "v144", "v145", "v146", "v147",
        "v148", "v149", "v150", "v151", "v152", "v153", "v154", "v155", "v156", "v157", "v158", "v159", "v160", "v161", "v162", "v163", "v164", "v165", "v166", "v167",
        "v168", "v169", "v170", "v171", "v172", "v173", "v174", "v175", "v176", "v177", "v178", "v179", "v180", "v181", "v182", "v183", "v184", "v185", "v186", "v187",
        "v188", "v189", "v190", "v191", "v192", "v193", "v194", "v195", "v196", "v197", "v198", "v199", "v200", "v201", "v202", "v203", "v204", "v205", "v206", "v207",
        "v208", "v209", "v210", "v211", "v212", "v213", "v214", "v215", "v216", "v217", "v218", "v219", "v220", "v221", "v222", "v223", "v224", "v225", "v226", "v227",
        "v228", "v229", "v230", "v231", "v232", "v233", "v234", "v235", "v236", "v237", "v238", "v239", "v240", "v241", "v242", "v243", "v244", "v245", "v246", "v247",
        "v248", "v249", "v250", "v251", "v252", "v253", "v254", "v255", "s12", "s13", "s14", "s15", "s16", "s17", "s18", "s19", "s20", "s21", "s22", "s23",
        "s24", "s25", "s26", "s27", "s28", "s29", "s30", "s31", "s33", "s34", "s35", "s36", "s37", "s38", "s39", "s40", "s41", "s42", "s43", "s44",
        "s45", "s46", "s47", "s48", "s49", "s50", "s51", "s52", "s53", "s54", "s55", "s56", "s57", "s58", "s59", "s60", "s61", "s62", "s63", "s64",
        "s65", "s66", "s67", "s68", "s69", "s70", "s71", "s72", "s73", "s74", "s75", "s76", "s77", "s78", "s79",
        "vcc", "scc", "memory");
  const int b = blockIdx.x;
  GETST();
  STAMP(11);
  PEND();
}

extern "C" void kernel_launch(void* const* d_in, const int* in_sizes, int n_in, void* d_out, int out_size,
                              void* d_ws, size_t ws_size, hipStream_t stream) {
  const u16* const* in = (const u16* const*)d_in;
  fused<<<256, 512, 0, stream>>>(in[0], in[1], in[2], in[3], in[4], in[5], in[8], in[6], in[9], in[10], in[11],
                                 (u16*)d_out, (char*)d_ws);
}
```

```cpp
#include <hip/hip_runtime.h>
#include <stdint.h>

typedef unsigned short u16;
typedef unsigned int u32;
typedef unsigned long long u64;
typedef __attribute__((ext_vector_type(4))) u32 u32x4;
typedef __attribute__((ext_vector_type(16))) u32 u32x16;
#define AS3 __attribute__((address_space(3)))

#define STAMP(I)
#define PEND()
#define GETST()
constexpr int SPIN_MAX = 1 << 16;
constexpr int UNUSED = 0;

__global__ __launch_bounds__(512, 2) void fused(const u16* __restrict__ x, const u16* __restrict__ lar,
    const u16* __restrict__ aim, const u16* __restrict__ cre, const u16* __restrict__ cim,
    const u16* __restrict__ ldt, const u16* __restrict__ wr, const u16* __restrict__ dsk,
    const u16* __restrict__ wg, const u16* __restrict__ wu, const u16* __restrict__ wd,
    u16* __restrict__ out, char* ws) {
  __shared__ __attribute__((aligned(16))) char smem_[163840];
  AS3 char* sm = (AS3 char*)smem_;
  u64 ptm = 0, pc0 = 0, pt0 = 0, pte = 0, st_[16] = {};
  u32x16 sts;
  u32x4 st2;
  asm volatile(
      "s_memrealtime s[12:13]\n"
      "s_load_dwordx16 s[64:79], %[ka], 0x0\n"
      "s_load_dwordx8 s[80:87], %[ka], 0x40\n"
      "s_load_dwordx2 s[88:89], %[ka], 0x60\n"
      "v_readfirstlane_b32 s33, v0\n"
      "s_getpc_b64 s[50:51]\n"
      ".Lpc%=:\n"
      "s_mov_b32 s34, %[bid]\n"
      "v_and_b32 v43, 63, v0\n"
      "v_mov_b32 v15, 0\n"
      "s_lshr_b32 s33, s33, 6\n"
      "s_and_b32 s52, s34, -8\n"
      "s_add_u32 s52, s52, s33\n"
      "s_min_u32 s52, s52, (.LEnd%=-.Lpc%=)>>7\n"
      "s_lshl_b32 s52, s52, 7\n"
      "s_and_b32 s50, s50, 0xffffff80\n"
      "s_add_u32 s50, s50, s52\n"
      "s_addc_u32 s51, s51, 0\n"
      "global_load_dword v11, v15, s[50:51] \n"
      "s_and_b32 s48, s34, 7\n"
      "s_lshr_b32 s49, s34, 3\n"
      "s_and_b32 s55, s49, 15\n"
      "s_lshl4_add_u32 s35, s48, s55\n"
      "s_cmp_lt_u32 s34, 128\n"
      "s_cbranch_scc1 .LH%=\n"
      "s_lshl3_add_u32 s50, s35, s33\n"
      "s_lshr_b32 s51, s34, 5\n"
      "s_andn2_b32 s51, 4, s51\n"
      "s_add_u32 s50, s50, s51\n"
      "s_lshl_b32 s51, s50, 8\n"
      "s_lshl_b32 s50, s50, 1\n"
      "v_lshl_add_u32 v48, v43, 2, s51\n"
      "v_mov_b32 v49, s50\n"
      "s_waitcnt lgkmcnt(0)\n"
      "global_load_dword v16, v48, s[66:67] nt\n"
      "global_load_dword v17, v48, s[68:69] nt\n"
      "global_load_ushort v12, v49, s[74:75] \n"
      "global_load_dword v18, v48, s[70:71] nt\n"
      "global_load_dword v19, v48, s[72:73] nt\n"
      "global_load_ushort v13, v49, s[78:79] \n"
      "s_load_dword s15, s[88:89], 0x0\n"
      "s_setprio 3\n"
      "s_branch .LC%=\n"
      ".LH%=:\n"
      "s_cmp_ge_u32 s33, 4\n"
      "s_cbranch_scc1 .LG%=\n"
      ".LD%=:\n"
      "v_and_b32 v52, 48, v43\n"
      "v_and_b32 v53, 3, v43\n"
      "v_lshl_add_u32 v53, v53, 2, v52\n"
      "v_and_b32 v54, 12, v43\n"
      "v_lshlrev_b32 v53, 2, v53\n"
      "v_lshl_add_u32 v52, v54, 9, v53\n"
      "s_and_b32 s53, s34, 127\n"
      "s_and_b32 s52, s33, 3\n"
      "s_lshl2_add_u32 s14, s53, s52\n"
      "s_lshl_b32 s51, s14, 13\n"
      "v_lshl_add_u32 v53, v54, 6, v53\n"
      "s_waitcnt lgkmcnt(0)\n"
      "s_add_u32 s52, s64, s51\n"
      "s_addc_u32 s53, s65, 0\n"
      "global_load_dwordx4 v[224:227], v53, s[76:77] offset:0\n"
      "global_load_dwordx4 v[228:231], v53, s[76:77] offset:1024\n"
      "global_load_dwordx4 v[192:195], v52, s[52:53] offset:0\n"
      "global_load_dwordx4 v[196:199], v52, s[52:53] offset:256\n"
      "global_load_dwordx4 v[200:203], v52, s[52:53] offset:512\n"
      "global_load_dwordx4 v[204:207], v52, s[52:53] offset:768\n"
      "global_load_dwordx4 v[208:211], v52, s[52:53] offset:1024\n"
      "global_load_dwordx4 v[212:215], v52, s[52:53] offset:1280\n"
      "global_load_dwordx4 v[216:219], v52, s[52:53] offset:1536\n"
      "global_load_dwordx4 v[220:223], v52, s[52:53] offset:1792\n"
      "s_load_dword s15, s[88:89], 0x0\n"
      "s_setprio 3\n"
      "s_cmp_lg_u32 s33, 0\n"
      "s_cbranch_scc1 .Ltb%=\n"
      "v_mov_b32 v48, 0x22680\n"
      "v_mov_b32 v49, s12\n"
      "ds_write_b32 v48, v49 \n"
      ".Ltb%=:\n"
      "s_mov_b32 s16, s88\n"
      "s_and_b32 s17, s89, 0xffff\n"
      "s_mov_b32 s18, 0x6e0000\n"
      "s_mov_b32 s19, 0x20000\n"
      "s_mov_b32 s20, s64\n"
      "s_and_b32 s21, s65, 0xffff\n"
      "s_mov_b32 s22, 0x400000\n"
      "s_mov_b32 s23, 0x20000\n"
      "s_mov_b32 s28, s84\n"
      "s_and_b32 s29, s85, 0xffff\n"
      "s_mov_b32 s30, 0x700000\n"
      "s_mov_b32 s31, 0x20000\n"
      "s_mov_b32 s24, s86\n"
      "s_and_b32 s25, s87, 0xffff\n"
      "s_mov_b32 s26, 0x400000\n"
      "s_mov_b32 s27, 0x20000\n"
      "s_lshl_b32 s42, s34, 3\n"
      "s_add_u32 s42, s42, 2048\n"
      "s_lshl_b32 s43, s48, 8\n"
      "s_and_b32 s50, s49, 1\n"
      "s_lshl_b32 s50, s50, 7\n"
      "s_add_u32 s43, s43, s50\n"
      "s_add_u32 s43, s43, 65536\n"
      "v_mov_b32 v20, 0x3f803f80\n"
      "v_mov_b32 v21, 0x3f803f80\n"
      "v_mov_b32 v22, 0x3f803f80\n"
      "v_mov_b32 v23, 0x3f803f80\n"
      "v_mov_b32 v44, 0x7f000000\n"
      "v_mov_b32 v25, 0\n"
      "s_lshr_b32 s70, s14, 7\n"
      "s_mul_i32 s70, s70, 61440\n"
      "s_lshl_b32 s68, s14, 5\n"
      "s_add_u32 s68, s68, s70\n"
      "s_add_u32 s68, s68, 131072\n"
      "s_lshr_b32 s70, s14, 8\n"
      "s_mul_i32 s70, s70, 61440\n"
      "s_lshl4_add_u32 s69, s14, s70\n"
      "s_add_u32 s69, s69, 393216\n"
      "v_writelane_b32 v44, s68, 0\n"
      "s_add_u32 s68, s68, 8\n"
      "v_writelane_b32 v44, s68, 20\n"
      "s_add_u32 s68, s68, 8\n"
      "v_writelane_b32 v44, s68, 40\n"
      "s_add_u32 s68, s68, 8\n"
      "v_writelane_b32 v44, s68, 60\n"
      "s_add_u32 s68, s68, 8\n"
      "v_writelane_b32 v44, s69, 1\n"
      "s_add_u32 s69, s69, 8\n"
      "v_writelane_b32 v44, s69, 2\n"
      "s_waitcnt lgkmcnt(0)\n"
      "s_add_u32 s15, s15, 0x9e3779b1\n"
      "v_mov_b32 v47, s15\n"
      "s_waitcnt vmcnt(9)\n"
      "v_mfma_f32_16x16x32_bf16 v[16:19], v[20:23], v[224:227], 0\n"
      "s_waitcnt vmcnt(8)\n"
      "v_mfma_f32_16x16x32_bf16 v[16:19], v[20:23], v[228:231], v[16:19]\n"
      "s_waitcnt vmcnt(7)\n"
      "v_mfma_f32_16x16x32_bf16 v[232:235], v[192:195], v[224:227], 0\n"
      "v_mfma_f32_16x16x32_bf16 v[252:255], v[192:195], v[192:195], 0\n"
      "v_mfma_f32_16x16x32_bf16 v[248:251], v[192:195], v[20:23], 0\n"
      "s_waitcnt vmcnt(6)\n"
      "v_mfma_f32_16x16x32_bf16 v[236:239], v[196:199], v[224:227], 0\n"
      "v_mfma_f32_16x16x32_bf16 v[252:255], v[196:199], v[196:199], v[252:255]\n"
      "v_mfma_f32_16x16x32_bf16 v[248:251], v[196:199], v[20:23], v[248:251]\n"
      "s_waitcnt vmcnt(5)\n"
      "v_mfma_f32_16x16x32_bf16 v[240:243], v[200:203], v[224:227], 0\n"
      "v_mfma_f32_16x16x32_bf16 v[252:255], v[200:203], v[200:203], v[252:255]\n"
      "v_mfma_f32_16x16x32_bf16 v[248:251], v[200:203], v[20:23], v[248:251]\n"
      "s_waitcnt vmcnt(4)\n"
      "v_mfma_f32_16x16x32_bf16 v[244:247], v[204:207], v[224:227], 0\n"
      "v_mfma_f32_16x16x32_bf16 v[252:255], v[204:207], v[204:207], v[252:255]\n"
      "v_mfma_f32_16x16x32_bf16 v[248:251], v[204:207], v[20:23], v[248:251]\n"
      "s_waitcnt vmcnt(3)\n"
      "v_mfma_f32_16x16x32_bf16 v[232:235], v[208:211], v[228:231], v[232:235]\n"
      "v_mfma_f32_16x16x32_bf16 v[252:255], v[208:211], v[208:211], v[252:255]\n"
      "v_mfma_f32_16x16x32_bf16 v[248:251], v[208:211], v[20:23], v[248:251]\n"
      "v_add_f32_dpp v16, v16, v16 quad_perm:[1,0,3,2] row_mask:0xf bank_mask:0xf\n"
      "s_waitcnt vmcnt(2)\n"
      "v_mfma_f32_16x16x32_bf16 v[236:239], v[212:215], v[228:231], v[236:239]\n"
      "v_mfma_f32_16x16x32_bf16 v[252:255], v[212:215], v[212:215], v[252:255]\n"
      "v_mfma_f32_16x16x32_bf16 v[248:251], v[212:215], v[20:23], v[248:251]\n"
      "v_add_f32_dpp v16, v16, v16 quad_perm:[2,3,0,1] row_mask:0xf bank_mask:0xf\n"
      "v_add_f32_dpp v25, v232, v25 quad_perm:[0,0,0,0] row_mask:0xf bank_mask:0x1\n"
      "s_nop 1\n"
      "v_add_f32_dpp v25, v233, v25 quad_perm:[1,1,1,1] row_mask:0xf bank_mask:0x1\n"
      "s_nop 1\n"
      "v_add_f32_dpp v25, v234, v25 quad_perm:[2,2,2,2] row_mask:0xf bank_mask:0x1\n"
      "s_nop 1\n"
      "v_add_f32_dpp v25, v235, v25 quad_perm:[3,3,3,3] row_mask:0xf bank_mask:0x1\n"
      "s_waitcnt vmcnt(1)\n"
      "v_mfma_f32_16x16x32_bf16 v[240:243], v[216:219], v[228:231], v[240:243]\n"
      "v_mfma_f32_16x16x32_bf16 v[252:255], v[216:219], v[216:219], v[252:255]\n"
      "v_mfma_f32_16x16x32_bf16 v[248:251], v[216:219], v[20:23], v[248:251]\n"
      "v_add_f32_dpp v16, v16, v16 row_ror:4 row_mask:0xf bank_mask:0xf\n"
      "v_add_f32_dpp v25, v236, v25 quad_perm:[0,0,0,0] row_mask:0xf bank_mask:0x2\n"
      "s_nop 1\n"
      "v_add_f32_dpp v25, v237, v25 quad_perm:[1,1,1,1] row_mask:0xf bank_mask:0x2\n"
      "s_nop 1\n"
      "v_add_f32_dpp v25, v238, v25 quad_perm:[2,2,2,2] row_mask:0xf bank_mask:0x2\n"
      "s_nop 1\n"
      "v_add_f32_dpp v25, v239, v25 quad_perm:[3,3,3,3] row_mask:0xf bank_mask:0x2\n"
      "s_waitcnt vmcnt(0)\n"
      "v_mfma_f32_16x16x32_bf16 v[244:247], v[220:223], v[228:231], v[244:247]\n"
      "v_mfma_f32_16x16x32_bf16 v[252:255], v[220:223], v[220:223], v[252:255]\n"
      "v_mfma_f32_16x16x32_bf16 v[248:251], v[220:223], v[20:23], v[248:251]\n"
      "v_add_f32_dpp v16, v16, v16 row_ror:8 row_mask:0xf bank_mask:0xf\n"
      "v_add_f32_dpp v25, v240, v25 quad_perm:[0,0,0,0] row_mask:0xf bank_mask:0x4\n"
      "s_nop 1\n"
      "v_add_f32_dpp v25, v241, v25 quad_perm:[1,1,1,1] row_mask:0xf bank_mask:0x4\n"
      "s_nop 1\n"
      "v_add_f32_dpp v25, v242, v25 quad_perm:[2,2,2,2] row_mask:0xf bank_mask:0x4\n"
      "s_nop 1\n"
      "v_add_f32_dpp v25, v243, v25 quad_perm:[3,3,3,3] row_mask:0xf bank_mask:0x4\n"
      "s_nop 1\n"
      "v_add_f32_dpp v25, v244, v25 quad_perm:[0,0,0,0] row_mask:0xf bank_mask:0x8\n"
      "v_mov_b32_dpp v24, v252 quad_perm:[0,0,0,0] row_mask:0xf bank_mask:0xf\n"
      "v_mov_b32_dpp v27, v254 quad_perm:[2,2,2,2] row_mask:0xf bank_mask:0xf\n"
      "v_add_f32_dpp v25, v245, v25 quad_perm:[1,1,1,1] row_mask:0xf bank_mask:0x8\n"
      "v_add_f32 v248, v248, v249\n"
      "v_add_f32 v250, v250, v251\n"
      "v_add_f32_dpp v24, v253, v24 quad_perm:[1,1,1,1] row_mask:0xf bank_mask:0xf\n"
      "v_add_f32_dpp v27, v255, v27 quad_perm:[3,3,3,3] row_mask:0xf bank_mask:0xf\n"
      "v_add_f32_dpp v25, v246, v25 quad_perm:[2,2,2,2] row_mask:0xf bank_mask:0x8\n"
      "v_add_f32 v248, v248, v250\n"
      "v_add_f32 v24, v24, v27\n"
      "v_add_f32_dpp v25, v247, v25 quad_perm:[3,3,3,3] row_mask:0xf bank_mask:0x8\n"
      "v_mul_f32 v248, 0x3a800000, v248\n"
      "v_mul_f32 v24, 0x3a800000, v24\n"
      "v_add_f32_dpp v25, v25, v25 row_ror:4 row_mask:0xf bank_mask:0xf\n"
      "v_fma_f32 v24, -v248, v248, v24\n"
      "v_max_f32 v24, 0, v24\n"
      "v_add_f32_dpp v25, v25, v25 row_ror:8 row_mask:0xf bank_mask:0xf\n"
      "v_cvt_pk_bf16_f32 v248, v248, v24\n"
      "v_and_b32 v24, 0xffff0000, v248\n"
      "v_add_f32 v24, 0x3727c5ac, v24\n"
      "v_lshlrev_b32 v26, 16, v248\n"
      "v_cvt_pk_bf16_f32 v24, v24, v24\n"
      "v_fma_f32 v25, -v26, v16, v25\n"
      "v_and_b32 v24, 0xffff0000, v24\n"
      "v_sqrt_f32 v24, v24\n"
      "s_nop 0\n"
      "v_cvt_pk_bf16_f32 v46, v26, v24\n"
      "v_and_b32 v24, 0xffff0000, v46\n"
      "v_rcp_f32 v24, v24\n"
      "s_nop 0\n"
      "v_mul_f32 v25, v25, v24\n"
      "v_cvt_pk_bf16_f32 v25, v25, v25\n"
      "s_nop 0\n"
      "v_readlane_b32 s68, v25, 0\n"
      "v_readlane_b32 s69, v25, 20\n"
      "v_readlane_b32 s70, v25, 40\n"
      "v_readlane_b32 s71, v25, 60\n"
      "s_pack_ll_b32_b16 s68, s68, s69\n"
      "s_pack_ll_b32_b16 s70, s70, s71\n"
      "v_writelane_b32 v46, s68, 1\n"
      "v_writelane_b32 v46, s70, 2\n"
      "buffer_store_dwordx2 v[46:47], v44, s[16:19], 0 offen sc1\n"
      "v_mov_b32 v48, v43\n"
      "v_and_b32 v49, 7, v43\n"
      "v_lshrrev_b32 v50, 3, v43\n"
      "s_cmp_ge_u32 s34, 112\n"
      "s_cbranch_scc1 .LS%=\n"
      "s_branch .LD2%=\n"
      ".LC%=:\n"
      "s_mov_b32 s16, s88\n"
      "s_and_b32 s17, s89, 0xffff\n"
      "s_mov_b32 s18, 0x6e0000\n"
      "s_mov_b32 s19, 0x20000\n"
      "s_mov_b32 s20, s64\n"
      "s_and_b32 s21, s65, 0xffff\n"
      "s_mov_b32 s22, 0x400000\n"
      "s_mov_b32 s23, 0x20000\n"
      "s_mul_i32 s39, s33, 6528\n"
      "s_mul_i32 s40, s33, 8768\n"
      "s_add_u32 s52, s40, 52224\n"
      "s_add_u32 s53, s52, 4160\n"
      "s_lshl_b32 s42, s34, 3\n"
      "s_add_u32 s42, s42, 2048\n"
      "v_and_b32 v48, 31, v43\n"
      "v_lshrrev_b32 v49, 5, v43\n"
      "v_lshlrev_b32 v55, 4, v49\n"
      "v_lshrrev_b32 v52, 2, v48\n"
      "v_mul_u32_u24 v52, 0x110, v52\n"
      "v_add3_u32 v20, v52, v55, s52\n"
      "v_and_b32 v52, 3, v43\n"
      "v_mul_u32_u24 v52, 0x110, v52\n"
      "s_add_u32 s50, s52, 2176\n"
      "v_add3_u32 v178, v52, v55, s50\n"
      "v_lshl_add_u32 v52, v43, 2, s50\n"
      "v_mov_b32 v53, 0x3c00\n"
      "ds_write_b32 v52, v53 \n"
      "ds_write_b32 v52, v53 offset:3264\n"
      "v_and_b32 v52, 12, v43\n"
      "s_mov_b32 s50, 0x6600\n"
      "s_mov_b32 s51, 0x6600\n"
      "v_cndmask_b32 v52, v48, v52, s[50:51]\n"
      "s_mov_b32 s50, 0xff000000\n"
      "s_mov_b32 s51, 0xff000000\n"
      "v_cndmask_b32 v52, v52, 9, s[50:51]\n"
      "v_mul_u32_u24 v52, 0x110, v52\n"
      "v_add3_u32 v21, v52, v55, s39\n"
      "v_lshl_add_u32 v52, v43, 2, s39\n"
      "v_mov_b32 v53, 0x3c00\n"
      "ds_write_b32 v52, v53 \n"
      "s_mov_b32 s24, s86\n"
      "s_and_b32 s25, s87, 0xffff\n"
      "s_mov_b32 s26, 0x400000\n"
      "s_mov_b32 s27, 0x20000\n"
      "s_mul_i32 s54, s33, 4864\n"
      "s_add_u32 s54, s54, 122368\n"
      "s_lshl_b32 s41, s35, 4\n"
      "s_and_b32 s43, s41, 0xffffff80\n"
      "s_add_u32 s43, s43, 67584\n"
      "v_and_b32 v50, 15, v43\n"
      "v_lshrrev_b32 v51, 4, v43\n"
      "v_and_b32 v53, 1, v43\n"
      "s_and_b32 s58, s33, 3\n"
      "s_lshl4_add_u32 s50, s33, s55\n"
      "s_lshl_b32 s50, s50, 15\n"
      "s_and_b32 s51, s55, 8\n"
      "s_lshl_b32 s51, s51, 4\n"
      "s_sub_u32 s50, s50, s51\n"
      "v_bfe_u32 v52, v43, 1, 4\n"
      "v_lshl_add_u32 v52, v52, 4, v53\n"
      "v_lshlrev_b32 v52, 7, v52\n"
      "v_add_u32 v15, s50, v52\n"
      "s_bitcmp1_b32 s55, 0\n"
      "s_cselect_b32 s56, s82, s80\n"
      "s_cselect_b32 s57, s83, s81\n"
      "s_lshr_b32 s50, s56, 7\n"
      "s_sub_u32 s50, s48, s50\n"
      "s_add_u32 s50, s50, 3\n"
      "s_and_b32 s50, s50, 7\n"
      "s_min_u32 s50, s50, 6\n"
      "s_mul_i32 s51, s48, 7\n"
      "s_add_u32 s50, s50, s51\n"
      "s_lshl_b32 s50, s50, 7\n"
      "s_lshr_b32 s51, s55, 1\n"
      "s_mul_i32 s51, s51, 24\n"
      "s_lshl_b32 s58, s58, 8\n"
      "s_add_u32 s51, s51, s58\n"
      "s_add_u32 s51, s51, 64\n"
      "s_mul_i32 s51, s51, 7168\n"
      "s_add_u32 s50, s50, s51\n"
      "v_min_u32 v57, 23, v43\n"
      "v_mul_u32_u24 v57, 0x1c00, v57\n"
      "v_add_u32 v57, s50, v57\n"
      "v_lshl_add_u32 v52, v48, 1, v49\n"
      "v_lshlrev_b32 v54, 3, v52\n"
      "s_add_u32 s50, s53, 570\n"
      "v_sub_u32 v22, s50, v54\n"
      "s_lshl_b32 s50, s33, 17\n"
      "v_lshl_add_u32 v8, v52, 11, s50\n"
      "v_mov_b32 v52, 0x63e\n"
      "v_mov_b32 v54, 0x6de\n"
      "s_mov_b32 s50, 0x55555555\n"
      "s_mov_b32 s51, 0x55555555\n"
      "v_cndmask_b32 v24, v52, v54, s[50:51]\n"
      "v_mov_b32 v52, 0x4de\n"
      "v_mov_b32 v54, 0x59e\n"
      "v_cndmask_b32 v52, v52, v54, s[50:51]\n"
      "s_mov_b32 s58, 0\n"
      "s_mov_b32 s59, -1\n"
      "v_cndmask_b32 v24, v24, v52, s[58:59]\n"
      "v_add_u32 v52, -1, v48\n"
      "v_and_b32 v52, -2, v52\n"
      "v_sub_u32 v24, v24, v52\n"
      "v_add_u32 v24, s53, v24\n"
      "v_add_u32 v23, 32, v24\n"
      "s_add_u32 s60, s53, 2048\n"
      "v_writelane_b32 v23, s60, 0\n"
      "v_writelane_b32 v23, s60, 32\n"
      "v_mul_u32_u24 v52, 0xc1, v53\n"
      "v_sub_u32 v52, v52, v48\n"
      "s_add_u32 s60, s53, 1150\n"
      "v_add_u32 v52, s60, v52\n"
      "v_add_u32 v25, 0xffffffc0, v24\n"
      "v_cndmask_b32 v25, v25, v52, s[58:59]\n"
      "v_and_b32 v52, -2, v43\n"
      "v_add_u32 v52, 58, v52\n"
      "v_mov_b32 v54, 0x810\n"
      "s_mov_b32 s60, 0xfffff000\n"
      "s_mov_b32 s61, 0xffffffff\n"
      "v_cndmask_b32 v52, v52, v54, s[60:61]\n"
      "s_movk_i32 s60, 1408\n"
      "s_movk_i32 s61, 1664\n"
      "v_writelane_b32 v52, s60, 8\n"
      "v_writelane_b32 v52, s60, 9\n"
      "v_writelane_b32 v52, s61, 10\n"
      "v_writelane_b32 v52, s61, 11\n"
      "v_mul_u32_u24 v54, 0x83e, v53\n"
      "v_add3_u32 v26, v52, v54, s53\n"
      "v_lshlrev_b32 v52, 1, v50\n"
      "v_add_u32 v52, v52, v54\n"
      "s_add_u32 s60, s53, 2140\n"
      "v_sub_u32 v52, s60, v52\n"
      "v_lshl_add_u32 v29, v51, 4, v52\n"
      "v_lshl_add_u32 v30, v51, 3, v52\n"
      "v_add_u32 v30, 0x440, v30\n"
      "v_bfe_u32 v52, v43, 2, 3\n"
      "v_max_u32 v54, 5, v52\n"
      "v_mul_u32_u24 v52, 0x60, v52\n"
      "v_lshl_add_u32 v52, v54, 6, v52\n"
      "v_add_u32 v52, 0x2a0, v52\n"
      "s_mov_b32 s60, 0xf\n"
      "s_mov_b32 s61, 0xf\n"
      "v_cndmask_b32 v52, v52, v55, s[60:61]\n"
      "v_mul_u32_u24 v54, 0x830, v49\n"
      "v_add3_u32 v27, v52, v54, s53\n"
      "v_and_b32 v55, 3, v43\n"
      "v_lshl_add_u32 v27, v55, 4, v27\n"
      "s_add_u32 s60, s39, 3104\n"
      "s_add_u32 s61, s52, 2080\n"
      "v_mov_b32 v52, s39\n"
      "v_mov_b32 v54, s60\n"
      "s_mov_b32 s50, 0xff00ff00\n"
      "s_mov_b32 s51, 0xff00ff00\n"
      "v_cndmask_b32 v28, v52, v54, s[50:51]\n"
      "v_mov_b32 v52, s52\n"
      "v_mov_b32 v54, s61\n"
      "v_cndmask_b32 v52, v52, v54, s[50:51]\n"
      "s_mov_b32 s50, 0xffff0000\n"
      "s_mov_b32 s51, 0xffff0000\n"
      "v_cndmask_b32 v28, v28, v52, s[50:51]\n"
      "v_and_b32 v52, 7, v43\n"
      "v_lshl_add_u32 v28, v52, 2, v28\n"
      "v_lshlrev_b32 v9, 3, v52\n"
      "v_bfe_u32 v10, v43, 3, 2\n"
      "v_lshlrev_b32 v10, 2, v10\n"
      "v_lshl_add_u32 v58, v49, 8, v48\n"
      "s_lshl_b32 s50, s33, 5\n"
      "v_add_u32 v58, s50, v58\n"
      "s_mov_b32 s50, 122912\n"
      "v_lshl_add_u32 v32, v58, 2, s50\n"
      "v_lshlrev_b32 v41, 12, v58\n"
      "v_mul_u32_u24 v42, 0xf000, v49\n"
      "v_lshl_add_u32 v42, v58, 4, v42\n"
      "v_lshl_add_u32 v52, v50, 1, v51\n"
      "v_lshl_add_u32 v33, v52, 4, s54\n"
      "s_add_u32 s50, s54, 528\n"
      "v_lshl_add_u32 v34, v43, 4, s50\n"
      "v_lshl_add_u32 v35, v43, 3, s39\n"
      "v_lshl_add_u32 v36, v43, 3, s52\n"
      "s_add_u32 s50, s39, 32\n"
      "v_lshl_add_u32 v37, v43, 4, s50\n"
      "s_add_u32 s50, s52, 32\n"
      "v_lshl_add_u32 v38, v43, 4, s50\n"
      "v_lshl_add_u32 v52, v50, 2, v51\n"
      "v_lshl_add_u32 v39, v52, 2, s53\n"
      "v_bfe_u32 v52, v43, 2, 2\n"
      "v_mul_u32_u24 v52, 0x2240, v52\n"
      "v_lshl_add_u32 v52, v49, 11, v52\n"
      "v_lshl_add_u32 v52, v55, 3, v52\n"
      "v_and_b32 v54, 16, v43\n"
      "v_lshl_add_u32 v52, v54, 1, v52\n"
      "s_lshl_b32 s50, s33, 6\n"
      "s_add_u32 s50, s50, 56384\n"
      "v_add_u32 v40, s50, v52\n"
      "v_mov_b32 v54, 0\n"
      "v_mov_b32 v55, 0\n"
      "v_lshl_add_u32 v52, v43, 3, s54\n"
      "ds_write_b64 v52, v[54:55] offset:32\n"
      "s_mov_b32 s50, 161536\n"
      "v_lshl_add_u32 v52, v0, 2, s50\n"
      "ds_write_b32 v52, v54 \n"
      "v_mov_b32 v52, 0xffff\n"
      "v_mov_b32 v53, 0xffff0000\n"
      "s_mov_b32 s50, 0x0\n"
      "s_mov_b32 s51, 0x18000018\n"
      "v_cndmask_b32 v44, 0, v52, s[50:51]\n"
      "s_mov_b32 s60, 0x0\n"
      "s_mov_b32 s61, 0x30000030\n"
      "v_cndmask_b32 v54, 0, v53, s[60:61]\n"
      "v_or_b32 v44, v44, v54\n"
      "s_mov_b32 s50, 0x0\n"
      "s_mov_b32 s51, 0x60000060\n"
      "v_cndmask_b32 v45, 0, v52, s[50:51]\n"
      "s_mov_b32 s60, 0x0\n"
      "s_mov_b32 s61, 0xc00000c0\n"
      "v_cndmask_b32 v54, 0, v53, s[60:61]\n"
      "v_or_b32 v45, v45, v54\n"
      "s_mov_b32 s50, 0x10000\n"
      "s_mov_b32 s51, 0x80000180\n"
      "v_cndmask_b32 v46, 0, v52, s[50:51]\n"
      "s_mov_b32 s60, 0x30000\n"
      "s_mov_b32 s61, 0x300\n"
      "v_cndmask_b32 v54, 0, v53, s[60:61]\n"
      "v_or_b32 v46, v46, v54\n"
      "s_mov_b32 s50, 0x60000\n"
      "s_mov_b32 s51, 0x600\n"
      "v_cndmask_b32 v47, 0, v52, s[50:51]\n"
      "s_mov_b32 s60, 0xc0000\n"
      "s_mov_b32 s61, 0xc00\n"
      "v_cndmask_b32 v54, 0, v53, s[60:61]\n"
      "v_or_b32 v47, v47, v54\n"
      ".Lsc%=:\n"
      "s_waitcnt vmcnt(0)\n"
      "s_mov_b32 s50, 0x3fb8aa3b\n"
      "v_mov_b32 v14, v57\n"
      "v_lshlrev_b32 v48, 16, v12\n"
      "v_and_b32 v52, 0x7fff7fff, v18\n"
      "v_and_b32 v53, 0x7fff7fff, v19\n"
      "v_mul_f32 v49, s50, v48\n"
      "v_pk_max_u16 v52, v52, v53\n"
      "v_fma_f32 v50, v48, s50, -v49\n"
      "v_rndne_f32 v51, v49\n"
      "v_lshlrev_b32 v53, 16, v52\n"
      "v_fmamk_f32 v50, v48, 0x32a5705f, v50\n"
      "v_sub_f32 v49, v49, v51\n"
      "v_max_u32 v52, v52, v53\n"
      "v_add_f32 v49, v49, v50\n"
      "v_cvt_i32_f32 v51, v51\n"
      "v_max_u32_dpp v52, v52, v52 row_shr:1 row_mask:0xf bank_mask:0xf bound_ctrl:1\n"
      "v_exp_f32 v49, v49\n"
      "v_lshlrev_b32 v54, 16, v13\n"
      "v_max_u32_dpp v52, v52, v52 row_shr:2 row_mask:0xf bank_mask:0xf bound_ctrl:1\n"
      "v_ldexp_f32 v49, v49, v51\n"
      "v_add_f32 v54, 1.0, v54\n"
      "v_max_u32_dpp v52, v52, v52 row_shr:4 row_mask:0xf bank_mask:0xf bound_ctrl:1\n"
      "v_cvt_pk_bf16_f32 v49, v49, v49\n"
      "s_waitcnt lgkmcnt(0)\n"
      "v_max_u32_dpp v52, v52, v52 row_shr:8 row_mask:0xf bank_mask:0xf bound_ctrl:1\n"
      "v_and_b32 v49, 0xffff0000, v49\n"
      "v_and_b32 v55, 0x7fffffff, v54\n"
      "v_max_u32_dpp v52, v52, v52 row_bcast:15 row_mask:0xa bank_mask:0xf\n"
      "v_mul_f32 v50, 0xbfb8aa3b, v49\n"
      "v_mul_f32 v51, 0xbe22f983, v49\n"
      "v_max_u32_dpp v52, v52, v52 row_bcast:31 row_mask:0xc bank_mask:0xf\n"
      "v_readfirstlane_b32 s36, v50\n"
      "v_readlane_b32 s50, v52, 63\n"
      "v_readfirstlane_b32 s37, v51\n"
      "v_mul_f32 v55, 0x3b800000, v55\n"
      "v_mul_f32 v52, s50, v49\n"
      "s_add_u32 s15, s15, 0x9e3779b1\n"
      "v_max_f32 v52, v52, v55\n"
      "v_frexp_exp_i32_f32 v52, v52\n"
      "s_nop 0\n"
      "v_readfirstlane_b32 s50, v52\n"
      "s_nop 0\n"
      "s_sub_i32 s50, 7, s50\n"
      "s_max_i32 s50, s50, -107\n"
      "s_min_i32 s50, s50, 93\n"
      "v_ldexp_f32 v49, v49, s50\n"
      "s_sub_i32 s51, s50, 1\n"
      "v_ldexp_f32 v54, v54, s51\n"
      "s_sub_i32 s14, 128, s50\n"
      "s_lshl_b32 s14, s14, 23\n"
      "v_readfirstlane_b32 s38, v49\n"
      "s_mov_b64 s[60:61], 1\n"
      "v_cndmask_b32 v31, 0, v54, s[60:61]\n"
      "s_lshl_b32 s50, s33, 2\n"
      "s_add_u32 s50, s50, 161280\n"
      "v_mov_b32 v50, s50\n"
      "v_mov_b32 v51, s14\n"
      "ds_write_b32 v50, v51 \n"
      "s_mov_b32 s60, s41\n"
      "s_add_u32 s61, s41, 0x200000\n"
      "s_mov_b32 s62, 131072\n"
      "s_mov_b32 s63, 262144\n"
      "s_mov_b32 m0, s40\n"
      "s_add_u32 s13, s40, 52224\n"
      "s_add_u32 s12, s39, 272\n"
      "v_lshlrev_b32 v72, 16, v16\n"
      "v_and_b32 v73, 0xffff0000, v16\n"
      "v_lshlrev_b32 v74, 16, v17\n"
      "v_and_b32 v75, 0xffff0000, v17\n"
      "s_mov_b32 s64, 0x3fb8aa3b\n"
      "s_mov_b32 s66, 0x43800000\n"
      "s_mov_b32 s68, 0x42000000\n"
      "s_mov_b32 s70, s37\n"
      "v_pk_mul_f32 v[72:73], s[64:65], v[72:73] op_sel_hi:[0,1]\n"
      "v_pk_mul_f32 v[74:75], s[70:71], v[74:75] op_sel_hi:[0,1]\n"
      "v_exp_f32 v72, v72\n"
      "v_exp_f32 v73, v73\n"
      "v_pk_mul_f32 v[78:79], s[66:67], v[74:75] op_sel_hi:[0,1]\n"
      "v_pk_mul_f32 v[80:81], s[68:69], v[74:75] op_sel_hi:[0,1]\n"
      "v_fract_f32 v76, v74\n"
      "v_fract_f32 v77, v75\n"
      "v_cvt_pk_bf16_f32 v72, v72, v73\n"
      "v_cos_f32 v90, v76\n"
      "v_cos_f32 v91, v77\n"
      "v_fract_f32 v78, v78\n"
      "v_fract_f32 v79, v79\n"
      "v_and_b32 v73, 0xffff0000, v72\n"
      "v_lshlrev_b32 v72, 16, v72\n"
      "v_sin_f32 v92, v76\n"
      "v_sin_f32 v93, v77\n"
      "v_pk_mul_f32 v[82:83], s[36:37], v[72:73] op_sel_hi:[0,1]\n"
      "v_fract_f32 v80, v80\n"
      "v_fract_f32 v81, v81\n"
      "v_exp_f32 v84, v82\n"
      "v_exp_f32 v85, v83\n"
      "v_pk_mul_f32 v[86:87], s[66:67], v[82:83] op_sel_hi:[0,1]\n"
      "v_cos_f32 v94, v78\n"
      "v_cos_f32 v95, v79\n"
      "v_pk_mul_f32 v[88:89], s[68:69], v[82:83] op_sel_hi:[0,1]\n"
      "v_sin_f32 v96, v78\n"
      "v_sin_f32 v97, v79\n"
      "v_pk_mul_f32 v[90:91], v[90:91], v[84:85]\n"
      "v_exp_f32 v86, v86\n"
      "v_exp_f32 v87, v87\n"
      "v_pk_mul_f32 v[92:93], v[92:93], v[84:85]\n"
      "s_cmp_ge_u32 s33, 4\n"
      "s_cbranch_scc1 .LcR%=\n"
      "buffer_load_dword v11, v15, s[20:23], s41 offen\n"
      "global_load_dword v11, v14, s[56:57]\n"
      ".LcR%=:\n"
      "v_pk_mul_f32 v[82:83], v[92:93], v[92:93]\n"
      "v_cos_f32 v202, v80\n"
      "v_pk_add_f32 v[84:85], v[90:91], v[90:91]\n"
      "v_cos_f32 v150, v81\n"
      "v_pk_fma_f32 v[98:99], v[90:91], v[90:91], v[82:83] neg_lo:[0,0,1] neg_hi:[0,0,1]\n"
      "v_pk_mul_f32 v[94:95], v[94:95], v[86:87]\n"
      "v_pk_mul_f32 v[100:101], v[84:85], v[92:93]\n"
      "v_pk_mul_f32 v[96:97], v[96:97], v[86:87]\n"
      "v_cvt_pk_f16_f32 v208, v90, v92\n"
      "v_sin_f32 v203, v80\n"
      "v_cvt_pk_f16_f32 v156, v91, v93\n"
      "v_sin_f32 v151, v81\n"
      "v_pk_mul_f32 v[82:83], v[100:101], v[100:101]\n"
      "v_exp_f32 v88, v88\n"
      "v_pk_add_f32 v[84:85], v[98:99], v[98:99]\n"
      "v_exp_f32 v89, v89\n"
      "v_pk_fma_f32 v[102:103], v[98:99], v[98:99], v[82:83] neg_lo:[0,0,1] neg_hi:[0,0,1]\n"
      "v_pk_mul_f32 v[114:115], v[96:97], v[96:97]\n"
      "v_pk_mul_f32 v[104:105], v[84:85], v[100:101]\n"
      "v_pk_add_f32 v[116:117], v[94:95], v[94:95]\n"
      "v_cvt_pk_f16_f32 v209, v98, v100\n"
      "v_pk_fma_f32 v[106:107], v[94:95], v[94:95], v[114:115] neg_lo:[0,0,1] neg_hi:[0,0,1]\n"
      "v_cvt_pk_f16_f32 v157, v99, v101\n"
      "v_pk_mul_f32 v[108:109], v[116:117], v[96:97]\n"
      "v_pk_mul_f32 v[82:83], v[104:105], v[104:105]\n"
      "v_cvt_pk_f16_f32 v212, v94, v96\n"
      "v_pk_add_f32 v[84:85], v[102:103], v[102:103]\n"
      "v_cvt_pk_f16_f32 v160, v95, v97\n"
      "v_pk_fma_f32 v[90:91], v[102:103], v[102:103], v[82:83] neg_lo:[0,0,1] neg_hi:[0,0,1]\n"
      "v_pk_mul_f32 v[202:203], v[202:203], v[88:89] op_sel:[0,0] op_sel_hi:[1,0]\n"
      "v_pk_mul_f32 v[92:93], v[84:85], v[104:105]\n"
      "v_pk_mul_f32 v[150:151], v[150:151], v[88:89] op_sel:[0,1] op_sel_hi:[1,1]\n"
      "v_cvt_pk_f16_f32 v210, v102, v104\n"
      "v_cvt_pk_f16_f32 v213, v106, v108\n"
      "v_cvt_pk_f16_f32 v158, v103, v105\n"
      "v_cvt_pk_f16_f32 v161, v107, v109\n"
      "v_pk_mul_f32 v[82:83], v[92:93], v[92:93]\n"
      "v_pk_mul_f32 v[114:115], v[108:109], v[108:109]\n"
      "v_pk_add_f32 v[84:85], v[90:91], v[90:91]\n"
      "v_pk_add_f32 v[116:117], v[106:107], v[106:107]\n"
      "v_pk_fma_f32 v[72:73], v[90:91], v[90:91], v[82:83] neg_lo:[0,0,1] neg_hi:[0,0,1]\n"
      "v_pk_fma_f32 v[94:95], v[106:107], v[106:107], v[114:115] neg_lo:[0,0,1] neg_hi:[0,0,1]\n"
      "v_pk_mul_f32 v[74:75], v[84:85], v[92:93]\n"
      "v_pk_mul_f32 v[96:97], v[116:117], v[108:109]\n"
      "v_cvt_pk_f16_f32 v205, v90, v92\n"
      "v_pk_mul_f16 v214, v212, v213 op_sel_hi:[0,1]\n"
      "v_cvt_pk_f16_f32 v153, v91, v93\n"
      "v_pk_mul_f16 v162, v160, v161 op_sel_hi:[0,1]\n"
      "v_pk_mul_f16 v211, v209, v210 op_sel_hi:[0,1]\n"
      "v_pk_fma_f16 v214, v212, v213, v214 op_sel:[1,1,0] op_sel_hi:[1,0,1] neg_lo:[0,1,0]\n"
      "v_pk_mul_f16 v159, v157, v158 op_sel_hi:[0,1]\n"
      "v_pk_fma_f16 v162, v160, v161, v162 op_sel:[1,1,0] op_sel_hi:[1,0,1] neg_lo:[0,1,0]\n"
      "v_pk_fma_f16 v211, v209, v210, v211 op_sel:[1,1,0] op_sel_hi:[1,0,1] neg_lo:[0,1,0]\n"
      "v_cvt_pk_f16_f32 v215, v94, v96\n"
      "v_pk_fma_f16 v159, v157, v158, v159 op_sel:[1,1,0] op_sel_hi:[1,0,1] neg_lo:[0,1,0]\n"
      "v_cvt_pk_f16_f32 v163, v95, v97\n"
      "v_cvt_pk_f16_f32 v206, v72, v74\n"
      "v_lshlrev_b32 v198, 16, v18\n"
      "v_cvt_pk_f16_f32 v154, v73, v75\n"
      "v_lshlrev_b32 v199, 16, v19\n"
      "v_pk_mul_f16 v207, v205, v206 op_sel_hi:[0,1]\n"
      "v_pk_mul_f32 v[198:199], s[38:39], v[198:199] op_sel_hi:[0,1]\n"
      "v_pk_mul_f16 v155, v153, v154 op_sel_hi:[0,1]\n"
      "v_and_b32 v146, 0xffff0000, v18\n"
      "v_pk_fma_f16 v207, v205, v206, v207 op_sel:[1,1,0] op_sel_hi:[1,0,1] neg_lo:[0,1,0]\n"
      "v_and_b32 v147, 0xffff0000, v19\n"
      "v_pk_fma_f16 v155, v153, v154, v155 op_sel:[1,1,0] op_sel_hi:[1,0,1] neg_lo:[0,1,0]\n"
      "v_pk_mul_f32 v[146:147], s[38:39], v[146:147] op_sel_hi:[0,1]\n"
      "v_cvt_pk_f16_f32 v240, v198, v199\n"
      "v_cvt_pk_f16_f32 v241, v146, v147\n"
      "v_pk_mul_f32 v[200:201], v[198:199], v[202:203] op_sel_hi:[1,0]\n"
      "v_pk_mul_f32 v[148:149], v[146:147], v[150:151] op_sel_hi:[1,0]\n"
      "ds_write_addtid_b32 v240 offset:52224\n"
      "ds_write_addtid_b32 v241 offset:55488\n"
      "v_pk_fma_f32 v[200:201], v[198:199], v[202:203], v[200:201] op_sel:[1,1,0] op_sel_hi:[0,1,1] neg_hi:[0,1,0]\n"
      "v_pk_fma_f32 v[148:149], v[146:147], v[150:151], v[148:149] op_sel:[1,1,0] op_sel_hi:[0,1,1] neg_hi:[0,1,0]\n"
      "ds_write_addtid_b32 v205 offset:54672\n"
      "v_cvt_pk_f16_f32 v242, v200, v201\n"
      "v_cvt_pk_f16_f32 v243, v148, v149\n"
      "v_pk_mul_f32 v[198:199], v[200:201], v[202:203] op_sel_hi:[1,0]\n"
      "v_pk_mul_f32 v[146:147], v[148:149], v[150:151] op_sel_hi:[1,0]\n"
      "ds_write_addtid_b32 v242 offset:52496\n"
      "ds_write_addtid_b32 v243 offset:55760\n"
      "v_pk_fma_f32 v[198:199], v[200:201], v[202:203], v[198:199] op_sel:[1,1,0] op_sel_hi:[0,1,1] neg_hi:[0,1,0]\n"
      "v_pk_fma_f32 v[146:147], v[148:149], v[150:151], v[146:147] op_sel:[1,1,0] op_sel_hi:[0,1,1] neg_hi:[0,1,0]\n"
      "ds_write_addtid_b32 v153 offset:57936\n"
      "v_cvt_pk_f16_f32 v240, v198, v199\n"
      "v_cvt_pk_f16_f32 v241, v146, v147\n"
      "v_pk_mul_f32 v[200:201], v[198:199], v[202:203] op_sel_hi:[1,0]\n"
      "v_pk_mul_f32 v[148:149], v[146:147], v[150:151] op_sel_hi:[1,0]\n"
      "ds_write_addtid_b32 v240 offset:52768\n"
      "ds_write_addtid_b32 v241 offset:56032\n"
      "v_pk_fma_f32 v[200:201], v[198:199], v[202:203], v[200:201] op_sel:[1,1,0] op_sel_hi:[0,1,1] neg_hi:[0,1,0]\n"
      "v_pk_fma_f32 v[148:149], v[146:147], v[150:151], v[148:149] op_sel:[1,1,0] op_sel_hi:[0,1,1] neg_hi:[0,1,0]\n"
      "ds_write_addtid_b32 v206 offset:54944\n"
      "v_cvt_pk_f16_f32 v242, v200, v201\n"
      "v_cvt_pk_f16_f32 v243, v148, v149\n"
      "v_pk_mul_f32 v[198:199], v[200:201], v[202:203] op_sel_hi:[1,0]\n"
      "v_pk_mul_f32 v[146:147], v[148:149], v[150:151] op_sel_hi:[1,0]\n"
      "ds_write_addtid_b32 v242 offset:53040\n"
      "ds_write_addtid_b32 v243 offset:56304\n"
      "v_pk_fma_f32 v[198:199], v[200:201], v[202:203], v[198:199] op_sel:[1,1,0] op_sel_hi:[0,1,1] neg_hi:[0,1,0]\n"
      "v_pk_fma_f32 v[146:147], v[148:149], v[150:151], v[146:147] op_sel:[1,1,0] op_sel_hi:[0,1,1] neg_hi:[0,1,0]\n"
      "ds_write_addtid_b32 v154 offset:58208\n"
      "v_cvt_pk_f16_f32 v240, v198, v199\n"
      "v_cvt_pk_f16_f32 v241, v146, v147\n"
      "v_pk_mul_f32 v[200:201], v[198:199], v[202:203] op_sel_hi:[1,0]\n"
      "v_pk_mul_f32 v[148:149], v[146:147], v[150:151] op_sel_hi:[1,0]\n"
      "ds_write_addtid_b32 v240 offset:53312\n"
      "ds_write_addtid_b32 v241 offset:56576\n"
      "v_pk_fma_f32 v[200:201], v[198:199], v[202:203], v[200:201] op_sel:[1,1,0] op_sel_hi:[0,1,1] neg_hi:[0,1,0]\n"
      "v_pk_fma_f32 v[148:149], v[146:147], v[150:151], v[148:149] op_sel:[1,1,0] op_sel_hi:[0,1,1] neg_hi:[0,1,0]\n"
      "ds_write_addtid_b32 v207 offset:55216\n"
      "v_cvt_pk_f16_f32 v242, v200, v201\n"
      "v_cvt_pk_f16_f32 v243, v148, v149\n"
      "v_pk_mul_f32 v[198:199], v[200:201], v[202:203] op_sel_hi:[1,0]\n"
      "v_pk_mul_f32 v[146:147], v[148:149], v[150:151] op_sel_hi:[1,0]\n"
      "ds_write_addtid_b32 v242 offset:53584\n"
      "ds_write_addtid_b32 v243 offset:56848\n"
      "v_pk_fma_f32 v[198:199], v[200:201], v[202:203], v[198:199] op_sel:[1,1,0] op_sel_hi:[0,1,1] neg_hi:[0,1,0]\n"
      "v_pk_fma_f32 v[146:147], v[148:149], v[150:151], v[146:147] op_sel:[1,1,0] op_sel_hi:[0,1,1] neg_hi:[0,1,0]\n"
      "ds_write_addtid_b32 v155 offset:58480\n"
      "v_cvt_pk_f16_f32 v240, v198, v199\n"
      "v_cvt_pk_f16_f32 v241, v146, v147\n"
      "v_pk_mul_f32 v[200:201], v[198:199], v[202:203] op_sel_hi:[1,0]\n"
      "v_pk_mul_f32 v[148:149], v[146:147], v[150:151] op_sel_hi:[1,0]\n"
      "ds_write_addtid_b32 v240 offset:53856\n"
      "ds_write_addtid_b32 v241 offset:57120\n"
      "v_pk_fma_f32 v[200:201], v[198:199], v[202:203], v[200:201] op_sel:[1,1,0] op_sel_hi:[0,1,1] neg_hi:[0,1,0]\n"
      "v_pk_fma_f32 v[148:149], v[146:147], v[150:151], v[148:149] op_sel:[1,1,0] op_sel_hi:[0,1,1] neg_hi:[0,1,0]\n"
      "v_cvt_pk_f16_f32 v242, v200, v201\n"
      "v_cvt_pk_f16_f32 v243, v148, v149\n"
      "ds_write_addtid_b32 v242 offset:54128\n"
      "ds_write_addtid_b32 v243 offset:57392\n"
      "ds_read_b128 v[104:107], v20 offset:0\n"
      "ds_read_b128 v[108:111], v178 offset:0\n"
      "ds_read_b128 v[112:115], v20 offset:32\n"
      "ds_read_b128 v[116:119], v178 offset:32\n"
      "s_mov_b32 m0, s39\n"
      "s_setprio 2\n"
      "v_pk_mul_f16 v240, v209, v208 op_sel_hi:[0,1]\n"
      "v_pk_mul_f16 v241, v210, v208 op_sel_hi:[0,1]\n"
      "v_pk_mul_f16 v242, v211, v208 op_sel_hi:[0,1]\n"
      "ds_write_addtid_b32 v208 offset:272\n"
      "v_pk_fma_f16 v240, v209, v208, v240 op_sel:[1,1,0] op_sel_hi:[1,0,1] neg_lo:[0,1,0]\n"
      "ds_write_addtid_b32 v209 offset:544\n"
      "v_pk_fma_f16 v241, v210, v208, v241 op_sel:[1,1,0] op_sel_hi:[1,0,1] neg_lo:[0,1,0]\n"
      "ds_write_addtid_b32 v240 offset:816\n"
      "v_pk_fma_f16 v242, v211, v208, v242 op_sel:[1,1,0] op_sel_hi:[1,0,1] neg_lo:[0,1,0]\n"
      "ds_write_addtid_b32 v241 offset:1360\n"
      "ds_write_addtid_b32 v242 offset:1904\n"
      "v_pk_mul_f16 v243, v212, v209 op_sel_hi:[0,1]\n"
      "v_pk_mul_f16 v244, v212, v210 op_sel_hi:[0,1]\n"
      "v_pk_mul_f16 v245, v212, v211 op_sel_hi:[0,1]\n"
      "ds_write_addtid_b32 v210 offset:1088\n"
      "v_pk_fma_f16 v243, v212, v209, v243 op_sel:[1,1,0] op_sel_hi:[1,0,1] neg_lo:[0,1,0]\n"
      "ds_write_addtid_b32 v211 offset:1632\n"
      "v_pk_fma_f16 v244, v212, v210, v244 op_sel:[1,1,0] op_sel_hi:[1,0,1] neg_lo:[0,1,0]\n"
      "ds_write_addtid_b32 v243 offset:2992\n"
      "v_pk_fma_f16 v245, v212, v211, v245 op_sel:[1,1,0] op_sel_hi:[1,0,1] neg_lo:[0,1,0]\n"
      "ds_write_addtid_b32 v244 offset:3264\n"
      "ds_write_addtid_b32 v245 offset:4080\n"
      "v_pk_mul_f16 v246, v213, v210 op_sel_hi:[0,1]\n"
      "v_pk_mul_f16 v247, v214, v210 op_sel_hi:[0,1]\n"
      "v_pk_mul_f16 v248, v215, v215 op_sel_hi:[0,1]\n"
      "ds_write_addtid_b32 v212 offset:2176\n"
      "v_pk_fma_f16 v246, v213, v210, v246 op_sel:[1,1,0] op_sel_hi:[1,0,1] neg_lo:[0,1,0]\n"
      "ds_write_addtid_b32 v213 offset:5440\n"
      "v_pk_fma_f16 v247, v214, v210, v247 op_sel:[1,1,0] op_sel_hi:[1,0,1] neg_lo:[0,1,0]\n"
      "ds_write_addtid_b32 v246 offset:5984\n"
      "v_pk_fma_f16 v248, v215, v215, v248 op_sel:[1,1,0] op_sel_hi:[1,0,1] neg_lo:[0,1,0]\n"
      "ds_write_addtid_b32 v247 offset:6256\n"
      "ds_write_addtid_b32 v248 offset:2448\n"
      "v_pk_mul_f16 v249, v215, v212 op_sel_hi:[0,1]\n"
      "v_pk_mul_f16 v250, v215, v213 op_sel_hi:[0,1]\n"
      "v_pk_mul_f16 v251, v215, v214 op_sel_hi:[0,1]\n"
      "ds_write_addtid_b32 v214 offset:5712\n"
      "v_pk_fma_f16 v249, v215, v212, v249 op_sel:[1,1,0] op_sel_hi:[1,0,1] neg_lo:[0,1,0]\n"
      "ds_write_addtid_b32 v215 offset:4352\n"
      "v_pk_fma_f16 v250, v215, v213, v250 op_sel:[1,1,0] op_sel_hi:[1,0,1] neg_lo:[0,1,0]\n"
      "ds_write_addtid_b32 v249 offset:4624\n"
      "v_pk_fma_f16 v251, v215, v214, v251 op_sel:[1,1,0] op_sel_hi:[1,0,1] neg_lo:[0,1,0]\n"
      "ds_write_addtid_b32 v250 offset:4896\n"
      "ds_write_addtid_b32 v251 offset:5168\n"
      "ds_read_b128 v[72:75], v21 offset:0\n"
      "ds_read_b128 v[76:79], v21 offset:32\n"
      "ds_read_b128 v[80:83], v21 offset:64\n"
      "ds_read_b128 v[84:87], v21 offset:96\n"
      "ds_read_b128 v[88:91], v21 offset:128\n"
      "ds_read_b128 v[92:95], v21 offset:160\n"
      "ds_read_b128 v[96:99], v21 offset:192\n"
      "ds_read_b128 v[100:103], v21 offset:224\n"
      "ds_read_b128 v[120:123], v20 offset:64\n"
      "ds_read_b128 v[124:127], v178 offset:64\n"
      "s_waitcnt lgkmcnt(15)\n"
      "v_pk_mul_f16 v164, v104, v108 op_sel_hi:[0,1] neg_hi:[0,1]\n"
      "v_pk_mul_f16 v165, v105, v109 op_sel_hi:[0,1] neg_hi:[0,1]\n"
      "v_pk_mul_f16 v166, v106, v110 op_sel_hi:[0,1] neg_hi:[0,1]\n"
      "v_pk_mul_f16 v167, v107, v111 op_sel_hi:[0,1] neg_hi:[0,1]\n"
      "v_pk_fma_f16 v164, v104, v108, v164 op_sel:[1,1,0] op_sel_hi:[1,0,1]\n"
      "v_pk_fma_f16 v165, v105, v109, v165 op_sel:[1,1,0] op_sel_hi:[1,0,1]\n"
      "v_pk_fma_f16 v166, v106, v110, v166 op_sel:[1,1,0] op_sel_hi:[1,0,1]\n"
      "v_pk_fma_f16 v167, v107, v111, v167 op_sel:[1,1,0] op_sel_hi:[1,0,1]\n"
      "s_waitcnt lgkmcnt(9)\n"
      "s_nop 0\n"
      "v_mfma_f32_32x32x16_f16 v[180:195], v[72:75], v[164:167], 0\n"
      "v_pk_mul_f16 v240, v157, v156 op_sel_hi:[0,1]\n"
      "v_pk_mul_f16 v241, v158, v156 op_sel_hi:[0,1]\n"
      "v_pk_mul_f16 v242, v159, v156 op_sel_hi:[0,1]\n"
      "v_pk_mul_f16 v168, v112, v116 op_sel_hi:[0,1] neg_hi:[0,1]\n"
      "v_pk_mul_f16 v169, v113, v117 op_sel_hi:[0,1] neg_hi:[0,1]\n"
      "v_pk_mul_f16 v170, v114, v118 op_sel_hi:[0,1] neg_hi:[0,1]\n"
      "v_pk_mul_f16 v171, v115, v119 op_sel_hi:[0,1] neg_hi:[0,1]\n"
      "ds_write_addtid_b32 v156 offset:272\n"
      "v_pk_fma_f16 v240, v157, v156, v240 op_sel:[1,1,0] op_sel_hi:[1,0,1] neg_lo:[0,1,0]\n"
      "ds_write_addtid_b32 v157 offset:544\n"
      "v_pk_fma_f16 v168, v112, v116, v168 op_sel:[1,1,0] op_sel_hi:[1,0,1]\n"
      "v_pk_fma_f16 v169, v113, v117, v169 op_sel:[1,1,0] op_sel_hi:[1,0,1]\n"
      "v_pk_fma_f16 v170, v114, v118, v170 op_sel:[1,1,0] op_sel_hi:[1,0,1]\n"
      "v_pk_fma_f16 v171, v115, v119, v171 op_sel:[1,1,0] op_sel_hi:[1,0,1]\n"
      "ds_read_b128 v[104:107], v20 offset:96\n"
      "ds_read_b128 v[108:111], v178 offset:96\n"
      "s_waitcnt lgkmcnt(12)\n"
      "v_mfma_f32_32x32x16_f16 v[180:195], v[76:79], v[168:171], v[180:195]\n"
      "v_pk_fma_f16 v241, v158, v156, v241 op_sel:[1,1,0] op_sel_hi:[1,0,1] neg_lo:[0,1,0]\n"
      "ds_write_addtid_b32 v240 offset:816\n"
      "v_pk_fma_f16 v242, v159, v156, v242 op_sel:[1,1,0] op_sel_hi:[1,0,1] neg_lo:[0,1,0]\n"
      "s_waitcnt lgkmcnt(5)\n"
      "v_pk_mul_f16 v164, v120, v124 op_sel_hi:[0,1] neg_hi:[0,1]\n"
      "v_pk_mul_f16 v165, v121, v125 op_sel_hi:[0,1] neg_hi:[0,1]\n"
      "v_pk_mul_f16 v166, v122, v126 op_sel_hi:[0,1] neg_hi:[0,1]\n"
      "v_pk_mul_f16 v167, v123, v127 op_sel_hi:[0,1] neg_hi:[0,1]\n"
      "ds_write_addtid_b32 v241 offset:1360\n"
      "ds_write_addtid_b32 v242 offset:1904\n"
      "v_pk_mul_f16 v243, v160, v157 op_sel_hi:[0,1]\n"
      "v_pk_fma_f16 v164, v120, v124, v164 op_sel:[1,1,0] op_sel_hi:[1,0,1]\n"
      "v_pk_fma_f16 v165, v121, v125, v165 op_sel:[1,1,0] op_sel_hi:[1,0,1]\n"
      "v_pk_fma_f16 v166, v122, v126, v166 op_sel:[1,1,0] op_sel_hi:[1,0,1]\n"
      "v_pk_fma_f16 v167, v123, v127, v167 op_sel:[1,1,0] op_sel_hi:[1,0,1]\n"
      "ds_read_b128 v[112:115], v20 offset:128\n"
      "ds_read_b128 v[116:119], v178 offset:128\n"
      "v_mfma_f32_32x32x16_f16 v[180:195], v[80:83], v[164:167], v[180:195]\n"
      "v_pk_mul_f16 v244, v160, v158 op_sel_hi:[0,1]\n"
      "v_pk_mul_f16 v245, v160, v159 op_sel_hi:[0,1]\n"
      "ds_write_addtid_b32 v158 offset:1088\n"
      "s_waitcnt lgkmcnt(6)\n"
      "v_pk_mul_f16 v168, v104, v108 op_sel_hi:[0,1] neg_hi:[0,1]\n"
      "v_pk_mul_f16 v169, v105, v109 op_sel_hi:[0,1] neg_hi:[0,1]\n"
      "v_pk_mul_f16 v170, v106, v110 op_sel_hi:[0,1] neg_hi:[0,1]\n"
      "v_pk_mul_f16 v171, v107, v111 op_sel_hi:[0,1] neg_hi:[0,1]\n"
      "v_pk_fma_f16 v243, v160, v157, v243 op_sel:[1,1,0] op_sel_hi:[1,0,1] neg_lo:[0,1,0]\n"
      "ds_write_addtid_b32 v159 offset:1632\n"
      "v_pk_fma_f16 v244, v160, v158, v244 op_sel:[1,1,0] op_sel_hi:[1,0,1] neg_lo:[0,1,0]\n"
      "v_pk_fma_f16 v168, v104, v108, v168 op_sel:[1,1,0] op_sel_hi:[1,0,1]\n"
      "v_pk_fma_f16 v169, v105, v109, v169 op_sel:[1,1,0] op_sel_hi:[1,0,1]\n"
      "v_pk_fma_f16 v170, v106, v110, v170 op_sel:[1,1,0] op_sel_hi:[1,0,1]\n"
      "v_pk_fma_f16 v171, v107, v111, v171 op_sel:[1,1,0] op_sel_hi:[1,0,1]\n"
      "ds_read_b128 v[120:123], v20 offset:160\n"
      "ds_read_b128 v[124:127], v178 offset:160\n"
      "buffer_load_dwordx4 v[48:51], v41, s[20:23], s60 offen nt\n"
      "v_mfma_f32_32x32x16_f16 v[180:195], v[84:87], v[168:171], v[180:195]\n"
      "ds_write_addtid_b32 v243 offset:2992\n"
      "v_pk_fma_f16 v245, v160, v159, v245 op_sel:[1,1,0] op_sel_hi:[1,0,1] neg_lo:[0,1,0]\n"
      "ds_write_addtid_b32 v244 offset:3264\n"
      "s_waitcnt lgkmcnt(6)\n"
      "v_pk_mul_f16 v164, v112, v116 op_sel_hi:[0,1] neg_hi:[0,1]\n"
      "v_pk_mul_f16 v165, v113, v117 op_sel_hi:[0,1] neg_hi:[0,1]\n"
      "v_pk_mul_f16 v166, v114, v118 op_sel_hi:[0,1] neg_hi:[0,1]\n"
      "v_pk_mul_f16 v167, v115, v119 op_sel_hi:[0,1] neg_hi:[0,1]\n"
      "ds_write_addtid_b32 v245 offset:4080\n"
      "v_pk_mul_f16 v246, v161, v158 op_sel_hi:[0,1]\n"
      "v_pk_mul_f16 v247, v162, v158 op_sel_hi:[0,1]\n"
      "v_pk_fma_f16 v164, v112, v116, v164 op_sel:[1,1,0] op_sel_hi:[1,0,1]\n"
      "v_pk_fma_f16 v165, v113, v117, v165 op_sel:[1,1,0] op_sel_hi:[1,0,1]\n"
      "v_pk_fma_f16 v166, v114, v118, v166 op_sel:[1,1,0] op_sel_hi:[1,0,1]\n"
      "v_pk_fma_f16 v167, v115, v119, v167 op_sel:[1,1,0] op_sel_hi:[1,0,1]\n"
      "ds_read_b128 v[104:107], v20 offset:192\n"
      "ds_read_b128 v[108:111], v178 offset:192\n"
      "v_mfma_f32_32x32x16_f16 v[180:195], v[88:91], v[164:167], v[180:195]\n"
      "v_pk_mul_f16 v248, v163, v163 op_sel_hi:[0,1]\n"
      "ds_write_addtid_b32 v160 offset:2176\n"
      "v_pk_fma_f16 v246, v161, v158, v246 op_sel:[1,1,0] op_sel_hi:[1,0,1] neg_lo:[0,1,0]\n"
      "s_waitcnt lgkmcnt(6)\n"
      "v_pk_mul_f16 v168, v120, v124 op_sel_hi:[0,1] neg_hi:[0,1]\n"
      "v_pk_mul_f16 v169, v121, v125 op_sel_hi:[0,1] neg_hi:[0,1]\n"
      "v_pk_mul_f16 v170, v122, v126 op_sel_hi:[0,1] neg_hi:[0,1]\n"
      "v_pk_mul_f16 v171, v123, v127 op_sel_hi:[0,1] neg_hi:[0,1]\n"
      "ds_write_addtid_b32 v161 offset:5440\n"
      "v_pk_fma_f16 v247, v162, v158, v247 op_sel:[1,1,0] op_sel_hi:[1,0,1] neg_lo:[0,1,0]\n"
      "ds_write_addtid_b32 v246 offset:5984\n"
      "v_pk_fma_f16 v168, v120, v124, v168 op_sel:[1,1,0] op_sel_hi:[1,0,1]\n"
      "v_pk_fma_f16 v169, v121, v125, v169 op_sel:[1,1,0] op_sel_hi:[1,0,1]\n"
      "v_pk_fma_f16 v170, v122, v126, v170 op_sel:[1,1,0] op_sel_hi:[1,0,1]\n"
      "v_pk_fma_f16 v171, v123, v127, v171 op_sel:[1,1,0] op_sel_hi:[1,0,1]\n"
      "ds_read_b128 v[112:115], v20 offset:224\n"
      "ds_read_b128 v[116:119], v178 offset:224\n"
      "v_mfma_f32_32x32x16_f16 v[180:195], v[92:95], v[168:171], v[180:195]\n"
      "v_pk_fma_f16 v248, v163, v163, v248 op_sel:[1,1,0] op_sel_hi:[1,0,1] neg_lo:[0,1,0]\n"
      "ds_write_addtid_b32 v247 offset:6256\n"
      "ds_write_addtid_b32 v248 offset:2448\n"
      "s_waitcnt lgkmcnt(7)\n"
      "v_pk_mul_f16 v164, v104, v108 op_sel_hi:[0,1] neg_hi:[0,1]\n"
      "v_pk_mul_f16 v165, v105, v109 op_sel_hi:[0,1] neg_hi:[0,1]\n"
      "v_pk_mul_f16 v166, v106, v110 op_sel_hi:[0,1] neg_hi:[0,1]\n"
      "v_pk_mul_f16 v167, v107, v111 op_sel_hi:[0,1] neg_hi:[0,1]\n"
      "v_pk_mul_f16 v249, v163, v160 op_sel_hi:[0,1]\n"
      "v_pk_mul_f16 v250, v163, v161 op_sel_hi:[0,1]\n"
      "v_pk_mul_f16 v251, v163, v162 op_sel_hi:[0,1]\n"
      "v_pk_fma_f16 v164, v104, v108, v164 op_sel:[1,1,0] op_sel_hi:[1,0,1]\n"
      "v_pk_fma_f16 v165, v105, v109, v165 op_sel:[1,1,0] op_sel_hi:[1,0,1]\n"
      "v_pk_fma_f16 v166, v106, v110, v166 op_sel:[1,1,0] op_sel_hi:[1,0,1]\n"
      "v_pk_fma_f16 v167, v107, v111, v167 op_sel:[1,1,0] op_sel_hi:[1,0,1]\n"
      "ds_read_b128 v[120:123], v20 offset:3264\n"
      "ds_read_b128 v[124:127], v178 offset:3264\n"
      "v_mfma_f32_32x32x16_f16 v[180:195], v[96:99], v[164:167], v[180:195]\n"
      "ds_write_addtid_b32 v162 offset:5712\n"
      "v_pk_fma_f16 v249, v163, v160, v249 op_sel:[1,1,0] op_sel_hi:[1,0,1] neg_lo:[0,1,0]\n"
      "ds_write_addtid_b32 v163 offset:4352\n"
      "s_waitcnt lgkmcnt(6)\n"
      "v_pk_mul_f16 v168, v112, v116 op_sel_hi:[0,1] neg_hi:[0,1]\n"
      "v_pk_mul_f16 v169, v113, v117 op_sel_hi:[0,1] neg_hi:[0,1]\n"
      "v_pk_mul_f16 v170, v114, v118 op_sel_hi:[0,1] neg_hi:[0,1]\n"
      "v_pk_mul_f16 v171, v115, v119 op_sel_hi:[0,1] neg_hi:[0,1]\n"
      "v_pk_fma_f16 v250, v163, v161, v250 op_sel:[1,1,0] op_sel_hi:[1,0,1] neg_lo:[0,1,0]\n"
      "ds_write_addtid_b32 v249 offset:4624\n"
      "v_pk_fma_f16 v251, v163, v162, v251 op_sel:[1,1,0] op_sel_hi:[1,0,1] neg_lo:[0,1,0]\n"
      "v_pk_fma_f16 v168, v112, v116, v168 op_sel:[1,1,0] op_sel_hi:[1,0,1]\n"
      "v_pk_fma_f16 v169, v113, v117, v169 op_sel:[1,1,0] op_sel_hi:[1,0,1]\n"
      "v_pk_fma_f16 v170, v114, v118, v170 op_sel:[1,1,0] op_sel_hi:[1,0,1]\n"
      "v_pk_fma_f16 v171, v115, v119, v171 op_sel:[1,1,0] op_sel_hi:[1,0,1]\n"
      "ds_read_b128 v[104:107], v20 offset:3296\n"
      "ds_read_b128 v[108:111], v178 offset:3296\n"
      "v_mfma_f32_32x32x16_f16 v[180:195], v[100:103], v[168:171], v[180:195]\n"
      "ds_write_addtid_b32 v250 offset:4896\n"
      "ds_write_addtid_b32 v251 offset:5168\n"
      "s_waitcnt lgkmcnt(7)\n"
      "v_pk_mul_f16 v164, v120, v124 op_sel_hi:[0,1] neg_hi:[0,1]\n"
      "v_pk_mul_f16 v165, v121, v125 op_sel_hi:[0,1] neg_hi:[0,1]\n"
      "v_pk_mul_f16 v166, v122, v126 op_sel_hi:[0,1] neg_hi:[0,1]\n"
      "v_pk_mul_f16 v167, v123, v127 op_sel_hi:[0,1] neg_hi:[0,1]\n"
      "ds_read_b128 v[72:75], v21 offset:0\n"
      "ds_read_b128 v[76:79], v21 offset:32\n"
      "ds_read_b128 v[80:83], v21 offset:64\n"
      "ds_read_b128 v[84:87], v21 offset:96\n"
      "ds_read_b128 v[88:91], v21 offset:128\n"
      "ds_read_b128 v[92:95], v21 offset:160\n"
      "ds_read_b128 v[96:99], v21 offset:192\n"
      "ds_read_b128 v[100:103], v21 offset:224\n"
      "v_pk_fma_f16 v164, v120, v124, v164 op_sel:[1,1,0] op_sel_hi:[1,0,1]\n"
      "v_pk_fma_f16 v165, v121, v125, v165 op_sel:[1,1,0] op_sel_hi:[1,0,1]\n"
      "v_pk_fma_f16 v166, v122, v126, v166 op_sel:[1,1,0] op_sel_hi:[1,0,1]\n"
      "v_pk_fma_f16 v167, v123, v127, v167 op_sel:[1,1,0] op_sel_hi:[1,0,1]\n"
      "ds_read_b128 v[112:115], v20 offset:3328\n"
      "ds_read_b128 v[116:119], v178 offset:3328\n"
      "s_setprio 1\n"
      "s_waitcnt lgkmcnt(9)\n"
      "v_mfma_f32_32x32x16_f16 v[180:195], v[72:75], v[164:167], v[180:195]\n"
      "v_pk_mul_f16 v168, v104, v108 op_sel_hi:[0,1] neg_hi:[0,1]\n"
      "v_pk_mul_f16 v169, v105, v109 op_sel_hi:[0,1] neg_hi:[0,1]\n"
      "v_pk_mul_f16 v170, v106, v110 op_sel_hi:[0,1] neg_hi:[0,1]\n"
      "v_pk_mul_f16 v171, v107, v111 op_sel_hi:[0,1] neg_hi:[0,1]\n"
      "v_pk_fma_f16 v168, v104, v108, v168 op_sel:[1,1,0] op_sel_hi:[1,0,1]\n"
      "v_pk_fma_f16 v169, v105, v109, v169 op_sel:[1,1,0] op_sel_hi:[1,0,1]\n"
      "v_pk_fma_f16 v170, v106, v110, v170 op_sel:[1,1,0] op_sel_hi:[1,0,1]\n"
      "v_pk_fma_f16 v171, v107, v111, v171 op_sel:[1,1,0] op_sel_hi:[1,0,1]\n"
      "ds_read_b128 v[120:123], v20 offset:3360\n"
      "ds_read_b128 v[124:127], v178 offset:3360\n"
      "buffer_load_dwordx4 v[52:55], v41, s[20:23], s60 offen offset:2048 nt\n"
      "s_waitcnt lgkmcnt(10)\n"
      "v_mfma_f32_32x32x16_f16 v[180:195], v[76:79], v[168:171], v[180:195]\n"
      "s_waitcnt lgkmcnt(2)\n"
      "v_pk_mul_f16 v164, v112, v116 op_sel_hi:[0,1] neg_hi:[0,1]\n"
      "v_pk_mul_f16 v165, v113, v117 op_sel_hi:[0,1] neg_hi:[0,1]\n"
      "v_pk_mul_f16 v166, v114, v118 op_sel_hi:[0,1] neg_hi:[0,1]\n"
      "v_pk_mul_f16 v167, v115, v119 op_sel_hi:[0,1] neg_hi:[0,1]\n"
      "v_pk_fma_f16 v164, v112, v116, v164 op_sel:[1,1,0] op_sel_hi:[1,0,1]\n"
      "v_pk_fma_f16 v165, v113, v117, v165 op_sel:[1,1,0] op_sel_hi:[1,0,1]\n"
      "v_pk_fma_f16 v166, v114, v118, v166 op_sel:[1,1,0] op_sel_hi:[1,0,1]\n"
      "v_pk_fma_f16 v167, v115, v119, v167 op_sel:[1,1,0] op_sel_hi:[1,0,1]\n"
      "ds_read_b128 v[104:107], v20 offset:3392\n"
      "ds_read_b128 v[108:111], v178 offset:3392\n"
      "v_mfma_f32_32x32x16_f16 v[180:195], v[80:83], v[164:167], v[180:195]\n"
      "s_waitcnt lgkmcnt(2)\n"
      "v_pk_mul_f16 v168, v120, v124 op_sel_hi:[0,1] neg_hi:[0,1]\n"
      "v_pk_mul_f16 v169, v121, v125 op_sel_hi:[0,1] neg_hi:[0,1]\n"
      "v_pk_mul_f16 v170, v122, v126 op_sel_hi:[0,1] neg_hi:[0,1]\n"
      "v_pk_mul_f16 v171, v123, v127 op_sel_hi:[0,1] neg_hi:[0,1]\n"
      "v_pk_fma_f16 v168, v120, v124, v168 op_sel:[1,1,0] op_sel_hi:[1,0,1]\n"
      "v_pk_fma_f16 v169, v121, v125, v169 op_sel:[1,1,0] op_sel_hi:[1,0,1]\n"
      "v_pk_fma_f16 v170, v122, v126, v170 op_sel:[1,1,0] op_sel_hi:[1,0,1]\n"
      "v_pk_fma_f16 v171, v123, v127, v171 op_sel:[1,1,0] op_sel_hi:[1,0,1]\n"
      "ds_read_b128 v[112:115], v20 offset:3424\n"
      "ds_read_b128 v[116:119], v178 offset:3424\n"
      "v_mfma_f32_32x32x16_f16 v[180:195], v[84:87], v[168:171], v[180:195]\n"
      "s_waitcnt lgkmcnt(2)\n"
      "v_pk_mul_f16 v164, v104, v108 op_sel_hi:[0,1] neg_hi:[0,1]\n"
      "v_pk_mul_f16 v165, v105, v109 op_sel_hi:[0,1] neg_hi:[0,1]\n"
      "v_pk_mul_f16 v166, v106, v110 op_sel_hi:[0,1] neg_hi:[0,1]\n"
      "v_pk_mul_f16 v167, v107, v111 op_sel_hi:[0,1] neg_hi:[0,1]\n"
      "v_pk_fma_f16 v164, v104, v108, v164 op_sel:[1,1,0] op_sel_hi:[1,0,1]\n"
      "v_pk_fma_f16 v165, v105, v109, v165 op_sel:[1,1,0] op_sel_hi:[1,0,1]\n"
      "v_pk_fma_f16 v166, v106, v110, v166 op_sel:[1,1,0] op_sel_hi:[1,0,1]\n"
      "v_pk_fma_f16 v167, v107, v111, v167 op_sel:[1,1,0] op_sel_hi:[1,0,1]\n"
      "ds_read_b128 v[120:123], v20 offset:3456\n"
      "ds_read_b128 v[124:127], v178 offset:3456\n"
      "v_mfma_f32_32x32x16_f16 v[180:195], v[88:91], v[164:167], v[180:195]\n"
      "s_waitcnt lgkmcnt(2)\n"
      "v_pk_mul_f16 v168, v112, v116 op_sel_hi:[0,1] neg_hi:[0,1]\n"
      "v_pk_mul_f16 v169, v113, v117 op_sel_hi:[0,1] neg_hi:[0,1]\n"
      "v_pk_mul_f16 v170, v114, v118 op_sel_hi:[0,1] neg_hi:[0,1]\n"
      "v_pk_mul_f16 v171, v115, v119 op_sel_hi:[0,1] neg_hi:[0,1]\n"
      "v_pk_fma_f16 v168, v112, v116, v168 op_sel:[1,1,0] op_sel_hi:[1,0,1]\n"
      "v_pk_fma_f16 v169, v113, v117, v169 op_sel:[1,1,0] op_sel_hi:[1,0,1]\n"
      "v_pk_fma_f16 v170, v114, v118, v170 op_sel:[1,1,0] op_sel_hi:[1,0,1]\n"
      "v_pk_fma_f16 v171, v115, v119, v171 op_sel:[1,1,0] op_sel_hi:[1,0,1]\n"
      "ds_read_b128 v[104:107], v20 offset:3488\n"
      "ds_read_b128 v[108:111], v178 offset:3488\n"
      "v_mov_b32 v140, 0\n"
      "v_mov_b32 v141, 0\n"
      "v_mov_b32 v142, 0\n"
      "v_mov_b32 v143, 0\n"
      "ds_write_b128 v27, v[140:143] \n"
      "ds_write_b32 v28, v140 \n"
      "v_mfma_f32_32x32x16_f16 v[180:195], v[92:95], v[168:171], v[180:195]\n"
      "s_waitcnt lgkmcnt(2)\n"
      "v_pk_mul_f16 v164, v120, v124 op_sel_hi:[0,1] neg_hi:[0,1]\n"
      "v_pk_mul_f16 v165, v121, v125 op_sel_hi:[0,1] neg_hi:[0,1]\n"
      "v_pk_mul_f16 v166, v122, v126 op_sel_hi:[0,1] neg_hi:[0,1]\n"
      "v_pk_mul_f16 v167, v123, v127 op_sel_hi:[0,1] neg_hi:[0,1]\n"
      "v_pk_fma_f16 v164, v120, v124, v164 op_sel:[1,1,0] op_sel_hi:[1,0,1]\n"
      "v_pk_fma_f16 v165, v121, v125, v165 op_sel:[1,1,0] op_sel_hi:[1,0,1]\n"
      "v_pk_fma_f16 v166, v122, v126, v166 op_sel:[1,1,0] op_sel_hi:[1,0,1]\n"
      "v_pk_fma_f16 v167, v123, v127, v167 op_sel:[1,1,0] op_sel_hi:[1,0,1]\n"
      "s_nop 1\n"
      "v_mfma_f32_32x32x16_f16 v[180:195], v[96:99], v[164:167], v[180:195]\n"
      "s_waitcnt lgkmcnt(0)\n"
      "v_pk_mul_f16 v168, v104, v108 op_sel_hi:[0,1] neg_hi:[0,1]\n"
      "v_pk_mul_f16 v169, v105, v109 op_sel_hi:[0,1] neg_hi:[0,1]\n"
      "v_pk_mul_f16 v170, v106, v110 op_sel_hi:[0,1] neg_hi:[0,1]\n"
      "v_pk_mul_f16 v171, v107, v111 op_sel_hi:[0,1] neg_hi:[0,1]\n"
      "v_pk_fma_f16 v168, v104, v108, v168 op_sel:[1,1,0] op_sel_hi:[1,0,1]\n"
      "v_pk_fma_f16 v169, v105, v109, v169 op_sel:[1,1,0] op_sel_hi:[1,0,1]\n"
      "v_pk_fma_f16 v170, v106, v110, v170 op_sel:[1,1,0] op_sel_hi:[1,0,1]\n"
      "v_pk_fma_f16 v171, v107, v111, v171 op_sel:[1,1,0] op_sel_hi:[1,0,1]\n"
      "s_nop 1\n"
      "v_mfma_f32_32x32x16_f16 v[180:195], v[100:103], v[168:171], v[180:195]\n"
      "s_cmp_lt_u32 s33, 4\n"
      "s_cbranch_scc1 .Lct2%=\n"
      "buffer_load_dword v11, v15, s[20:23], s60 offen\n"
      ".Lct2%=:\n"
      "s_nop 10\n"
      "v_add_f32 v180, v180, v31\n"
      "buffer_load_dwordx4 v[64:67], v42, s[16:19], s62 offen sc1\n"
      "v_cvt_pk_f16_f32 v222, v187, v186\n"
      "v_cvt_pk_f16_f32 v223, v185, v184\n"
      "v_cvt_pk_f16_f32 v225, v186, v185\n"
      "v_cvt_pk_f16_f32 v226, v188, v189\n"
      "ds_write_b64 v22, v[222:223] offset:2110\n"
      "v_cvt_pk_f16_f32 v228, v192, v192\n"
      "ds_write_b16 v22, v222 \n"
      "v_cvt_pk_f16_f32 v220, v183, v182\n"
      "ds_write_b32 v22, v225 offset:2\n"
      "v_cvt_pk_f16_f32 v221, v181, v180\n"
      "ds_write_b16_d16_hi v22, v223 offset:6\n"
      "v_cvt_pk_f16_f32 v224, v182, v181\n"
      "v_readlane_b32 s64, v226, 0\n"
      "v_readlane_b32 s65, v226, 32\n"
      "v_readlane_b32 s66, v228, 0\n"
      "ds_write_b64 v22, v[220:221] offset:510\n"
      "v_cvt_pk_f16_f32 v227, v190, v191\n"
      "ds_write_b16 v22, v220 offset:2620\n"
      "v_mov_b32 v229, s65\n"
      "ds_write_b32 v22, v224 offset:2622\n"
      "v_writelane_b32 v229, s64, 8\n"
      "v_writelane_b32 v229, s64, 9\n"
      "ds_write_b16_d16_hi v22, v221 offset:2626\n"
      "v_writelane_b32 v229, s66, 10\n"
      "v_writelane_b32 v229, s66, 11\n"
      "s_pack_ll_b32_b16 s64, s64, s64\n"
      "ds_write_b16 v26, v229 \n"
      "v_add_u32 v230, 0x240, v29\n"
      "s_setprio 0\n"
      "v_and_b32 v44, s64, v44\n"
      "v_and_b32 v45, s64, v45\n"
      "v_and_b32 v46, s64, v46\n"
      "v_and_b32 v47, s64, v47\n"
      "ds_write_b16 v23, v226 \n"
      "ds_write_b16 v23, v226 offset:2110\n"
      "ds_write_b16_d16_hi v24, v226 \n"
      "ds_write_b16_d16_hi v24, v226 offset:2110\n"
      "ds_write_b16 v25, v227 offset:32\n"
      "ds_write_b16 v25, v227 offset:2142\n"
      "ds_write_b16_d16_hi v25, v227 \n"
      "ds_write_b16_d16_hi v25, v227 offset:2110\n"
      "ds_read2_b32 v[108:109], v230 offset0:0 offset1:1\n"
      "ds_read2_b32 v[110:111], v230 offset0:2 offset1:3\n"
      "ds_read2_b32 v[76:77], v29 offset0:16 offset1:17\n"
      "ds_read2_b32 v[78:79], v29 offset0:18 offset1:19\n"
      "ds_read2_b32 v[112:113], v230 offset0:16 offset1:17\n"
      "ds_read2_b32 v[114:115], v230 offset0:18 offset1:19\n"
      "ds_read2_b32 v[80:81], v29 offset0:32 offset1:33\n"
      "ds_read2_b32 v[82:83], v29 offset0:34 offset1:35\n"
      "ds_read2_b32 v[116:117], v230 offset0:32 offset1:33\n"
      "ds_read2_b32 v[118:119], v230 offset0:34 offset1:35\n"
      "ds_read2_b32 v[84:85], v29 offset0:48 offset1:49\n"
      "ds_read2_b32 v[86:87], v29 offset0:50 offset1:51\n"
      "s_movk_i32 s59, 0x7fff\n"
      "s_waitcnt vmcnt(0)\n"
      "s_nop 0\n"
      ".Lcsgk%=:\n"
      "v_cmp_ne_u32 s[64:65], s15, v65\n"
      "v_cmp_ne_u32 vcc, s15, v67\n"
      "s_nop 0\n"
      "s_or_b64 vcc, vcc, s[64:65]\n"
      "s_cbranch_scc1 .Lcsgsk%=\n"
      ".Lcsgok%=:\n"
      "v_and_b32 v196, 0xffff0000, v64\n"
      "v_lshlrev_b32 v198, 16, v64\n"
      "v_rcp_f32 v196, v196\n"
      "v_and_b32 v197, 0xffff0000, v66\n"
      "v_lshlrev_b32 v199, 16, v66\n"
      "v_rcp_f32 v197, v197\n"
      "v_mul_f32 v198, -v198, v196\n"
      "v_mul_f32 v199, -v199, v197\n"
      "ds_read2_b32 v[120:121], v230 offset0:48 offset1:49\n"
      "ds_read2_b32 v[122:123], v230 offset0:50 offset1:51\n"
      "v_lshlrev_b32 v204, 16, v48\n"
      "v_lshlrev_b32 v205, 16, v52\n"
      "v_and_b32 v206, 0xffff0000, v48\n"
      "v_and_b32 v207, 0xffff0000, v52\n"
      "v_pk_fma_f32 v[204:205], v[204:205], v[196:197], v[198:199]\n"
      "v_pk_fma_f32 v[206:207], v[206:207], v[196:197], v[198:199]\n"
      "v_cvt_pk_f16_f32 v204, v204, v205\n"
      "v_cvt_pk_f16_f32 v206, v206, v207\n"
      "ds_read2_b32 v[88:89], v29 offset0:64 offset1:65\n"
      "ds_read2_b32 v[90:91], v29 offset0:66 offset1:67\n"
      "ds_write2st64_b32 v32, v204, v206 offset0:0 offset1:19\n"
      "v_lshlrev_b32 v208, 16, v49\n"
      "v_lshlrev_b32 v209, 16, v53\n"
      "v_and_b32 v210, 0xffff0000, v49\n"
      "v_and_b32 v211, 0xffff0000, v53\n"
      "v_pk_fma_f32 v[208:209], v[208:209], v[196:197], v[198:199]\n"
      "v_pk_fma_f32 v[210:211], v[210:211], v[196:197], v[198:199]\n"
      "v_cvt_pk_f16_f32 v208, v208, v209\n"
      "ds_read2_b32 v[124:125], v230 offset0:64 offset1:65\n"
      "ds_read2_b32 v[126:127], v230 offset0:66 offset1:67\n"
      "v_cvt_pk_f16_f32 v210, v210, v211\n"
      "ds_write2st64_b32 v32, v208, v210 offset0:38 offset1:57\n"
      "v_lshlrev_b32 v204, 16, v50\n"
      "v_lshlrev_b32 v205, 16, v54\n"
      "v_and_b32 v206, 0xffff0000, v50\n"
      "v_and_b32 v207, 0xffff0000, v54\n"
      "v_pk_fma_f32 v[204:205], v[204:205], v[196:197], v[198:199]\n"
      "v_pk_fma_f32 v[206:207], v[206:207], v[196:197], v[198:199]\n"
      "ds_read2_b32 v[92:93], v29 offset0:80 offset1:81\n"
      "ds_read2_b32 v[94:95], v29 offset0:82 offset1:83\n"
      "v_cvt_pk_f16_f32 v204, v204, v205\n"
      "v_cvt_pk_f16_f32 v206, v206, v207\n"
      "ds_write2st64_b32 v32, v204, v206 offset0:76 offset1:95\n"
      "v_lshlrev_b32 v208, 16, v51\n"
      "v_lshlrev_b32 v209, 16, v55\n"
      "v_and_b32 v210, 0xffff0000, v51\n"
      "v_and_b32 v211, 0xffff0000, v55\n"
      "v_pk_fma_f32 v[208:209], v[208:209], v[196:197], v[198:199]\n"
      "ds_read2_b32 v[128:129], v230 offset0:80 offset1:81\n"
      "ds_read2_b32 v[130:131], v230 offset0:82 offset1:83\n"
      "v_pk_fma_f32 v[210:211], v[210:211], v[196:197], v[198:199]\n"
      "v_cvt_pk_f16_f32 v208, v208, v209\n"
      "v_cvt_pk_f16_f32 v210, v210, v211\n"
      "ds_write2st64_b32 v32, v208, v210 offset0:114 offset1:133\n"
      "s_mov_b32 s54, s41\n"
      "s_add_u32 s55, s41, 0x100000\n"
      "s_add_u32 s56, s41, 0x200000\n"
      "s_add_u32 s57, s41, 0x300000\n"
      "ds_read2_b32 v[96:97], v29 offset0:96 offset1:97\n"
      "ds_read2_b32 v[98:99], v29 offset0:98 offset1:99\n"
      "s_mov_b32 s58, 0x40000000\n"
      "v_lshrrev_b32 v14, 9, v8\n"
      "v_add_u32 v14, 0x27600, v14\n"
      "s_lshr_b32 s13, s33, 1\n"
      "ds_read2_b32 v[132:133], v230 offset0:96 offset1:97\n"
      "ds_read2_b32 v[134:135], v230 offset0:98 offset1:99\n"
      "ds_read2_b32 v[100:101], v29 offset0:112 offset1:113\n"
      "ds_read2_b32 v[102:103], v29 offset0:114 offset1:115\n"
      "ds_read2_b32 v[136:137], v230 offset0:112 offset1:113\n"
      "ds_read2_b32 v[138:139], v230 offset0:114 offset1:115\n"
      "ds_read2_b32 v[104:105], v29 offset0:128 offset1:129\n"
      "ds_read2_b32 v[106:107], v29 offset0:130 offset1:131\n"
      "ds_read2_b32 v[72:73], v29 offset0:0 offset1:1\n"
      "ds_read2_b32 v[74:75], v29 offset0:2 offset1:3\n"
      "s_waitcnt lgkmcnt(12)\n"
      ".Lccv%=:\n"
      "s_barrier\n"
      "s_setprio 3\n"
      "ds_read_b128 v[216:219], v33 offset:64\n"
      "ds_read_b128 v[220:223], v33 offset:128\n"
      "ds_read_b128 v[224:227], v33 offset:192\n"
      "ds_read_b128 v[212:215], v33 offset:256\n"
      "v_permlane32_swap_b32 v48, v52\n"
      "v_permlane32_swap_b32 v49, v53\n"
      "v_permlane32_swap_b32 v50, v54\n"
      "v_permlane32_swap_b32 v51, v55\n"
      "s_cmp_lg_u32 s33, 0\n"
      "s_cbranch_scc1 .Lca%=\n"
      "v_mov_b32 v16, s15\n"
      "v_mov_b32 v17, s15\n"
      "buffer_store_dwordx2 v[16:17], off, s[16:19], s42 sc1\n"
      ".Lca%=:\n"
      "s_waitcnt lgkmcnt(2)\n"
      "v_mfma_f32_16x16x32_f16 v[180:183], v[108:111], v[216:219], 0\n"
      "v_lshlrev_b32 v29, 2, v43\n"
      "v_add_u32 v29, 0x27600, v29\n"
      "ds_read_b32 v255, v29 \n"
      "ds_read_b128 v[236:239], v34 offset:0\n"
      "ds_read_b128 v[240:243], v34 offset:16\n"
      "v_mfma_f32_16x16x32_f16 v[184:187], v[76:79], v[216:219], 0\n"
      "ds_read2_b32 v[140:141], v30 offset0:16 offset1:17\n"
      "ds_read_b128 v[216:219], v33 offset:320\n"
      "v_mfma_f32_16x16x32_f16 v[180:183], v[112:115], v[220:223], v[180:183]\n"
      "v_mfma_f32_16x16x32_f16 v[184:187], v[80:83], v[220:223], v[184:187]\n"
      "ds_read2_b32 v[142:143], v30 offset0:64 offset1:65\n"
      "ds_read_b128 v[220:223], v33 offset:384\n"
      "s_waitcnt lgkmcnt(7)\n"
      "v_mfma_f32_16x16x32_f16 v[180:183], v[116:119], v[224:227], v[180:183]\n"
      "s_waitcnt lgkmcnt(5)\n"
      "v_alignbit_b32 v245, v238, v237, 16\n"
      "v_alignbit_b32 v246, v239, v238, 16\n"
      "v_mfma_f32_16x16x32_f16 v[184:187], v[84:87], v[224:227], v[184:187]\n"
      "ds_read2_b32 v[144:145], v30 offset0:8 offset1:9\n"
      "ds_read_b128 v[224:227], v33 offset:448\n"
      "s_waitcnt lgkmcnt(4)\n"
      "v_alignbit_b32 v247, v240, v239, 16\n"
      "v_alignbit_b32 v248, v241, v240, 16\n"
      "v_mfma_f32_16x16x32_f16 v[180:183], v[120:123], v[212:215], v[180:183]\n"
      "v_alignbit_b32 v249, v242, v241, 16\n"
      "v_pk_add_f16 v16, v245, v246\n"
      "v_mfma_f32_16x16x32_f16 v[184:187], v[88:91], v[212:215], v[184:187]\n"
      "ds_read2_b32 v[146:147], v30 offset0:56 offset1:57\n"
      "ds_read_b128 v[212:215], v33 offset:512\n"
      "v_pk_add_f16 v17, v246, v247\n"
      "v_pk_add_f16 v18, v247, v248\n"
      "v_mfma_f32_16x16x32_f16 v[180:183], v[124:127], v[216:219], v[180:183]\n"
      "v_pk_add_f16 v19, v248, v249\n"
      "v_pk_add_f16 v20, v16, v238\n"
      "v_mfma_f32_16x16x32_f16 v[184:187], v[92:95], v[216:219], v[184:187]\n"
      "ds_read2_b32 v[148:149], v30 offset0:0 offset1:1\n"
      "v_pk_add_f16 v21, v17, v239\n"
      "v_pk_add_f16 v22, v18, v240\n"
      "s_waitcnt lgkmcnt(3)\n"
      "v_mfma_f32_16x16x32_f16 v[180:183], v[128:131], v[220:223], v[180:183]\n"
      "v_pk_add_f16 v23, v19, v241\n"
      "ds_write_b128 v37, v[20:23] offset:0\n"
      "v_pk_add_f16 v16, v237, v239\n"
      "v_mfma_f32_16x16x32_f16 v[184:187], v[96:99], v[220:223], v[184:187]\n"
      "ds_read2_b32 v[150:151], v30 offset0:48 offset1:49\n"
      "ds_read_b128 v[216:219], v33 offset:576\n"
      "v_pk_add_f16 v17, v238, v240\n"
      "v_pk_add_f16 v18, v239, v241\n"
      "v_mfma_f32_16x16x32_f16 v[180:183], v[132:135], v[224:227], v[180:183]\n"
      "v_pk_add_f16 v19, v240, v242\n"
      "v_pk_add_f16 v20, v20, v16\n"
      "v_mfma_f32_16x16x32_f16 v[184:187], v[100:103], v[224:227], v[184:187]\n"
      "ds_read2_b32 v[152:153], v30 offset0:40 offset1:41\n"
      "ds_read_b128 v[220:223], v33 offset:640\n"
      "v_pk_add_f16 v21, v21, v17\n"
      "v_pk_add_f16 v22, v22, v18\n"
      "s_waitcnt lgkmcnt(4)\n"
      "v_mfma_f32_16x16x32_f16 v[180:183], v[136:139], v[212:215], v[180:183]\n"
      "v_pk_add_f16 v23, v23, v19\n"
      "ds_write_b128 v37, v[20:23] offset:3104\n"
      "v_alignbit_b32 v244, v237, v236, 16\n"
      "v_mfma_f32_16x16x32_f16 v[184:187], v[104:107], v[212:215], v[184:187]\n"
      "ds_read2_b32 v[154:155], v30 offset0:88 offset1:89\n"
      "ds_read_b128 v[224:227], v33 offset:704\n"
      "v_alignbit_b32 v250, v243, v242, 16\n"
      "v_pk_add_f16 v16, v244, v247\n"
      "v_mfma_f32_16x16x32_f16 v[188:191], v[72:75], v[212:215], 0\n"
      "v_pk_add_f16 v17, v245, v248\n"
      "v_pk_add_f16 v18, v246, v249\n"
      "v_mfma_f32_16x16x32_f16 v[196:199], v[44:47], v[212:215], 0\n"
      "ds_read2_b32 v[156:157], v30 offset0:32 offset1:33\n"
      "ds_read_b128 v[212:215], v33 offset:768\n"
      "v_pk_add_f16 v19, v247, v250\n"
      "v_pk_add_f16 v20, v20, v16\n"
      "s_waitcnt lgkmcnt(4)\n"
      "v_mfma_f32_16x16x32_f16 v[184:187], v[108:111], v[216:219], v[184:187]\n"
      "v_cvt_pk_bf16_f32 v24, v180, v182\n"
      "v_cvt_pk_bf16_f32 v25, v181, v183\n"
      "ds_write2st64_b32 v39, v24, v25 offset0:0 offset1:8\n"
      "v_mfma_f32_16x16x32_f16 v[188:191], v[76:79], v[216:219], v[188:191]\n"
      "ds_read2_b32 v[158:159], v30 offset0:80 offset1:81\n"
      "ds_read_b128 v[216:219], v33 offset:832\n"
      "v_pk_add_f16 v21, v21, v17\n"
      "v_pk_add_f16 v22, v22, v18\n"
      "v_mfma_f32_16x16x32_f16 v[184:187], v[112:115], v[220:223], v[184:187]\n"
      "buffer_load_dwordx4 v[56:59], v41, s[20:23], s61 offen nt\n"
      "buffer_load_dwordx4 v[68:71], v42, s[16:19], s63 offen sc1\n"
      "v_pk_add_f16 v23, v23, v19\n"
      "ds_write_b128 v38, v[20:23] offset:0\n"
      "v_pk_add_f16 v16, v236, v240\n"
      "v_mfma_f32_16x16x32_f16 v[188:191], v[80:83], v[220:223], v[188:191]\n"
      "ds_read2_b32 v[160:161], v30 offset0:24 offset1:25\n"
      "ds_read_b128 v[220:223], v33 offset:896\n"
      "v_pk_add_f16 v17, v237, v241\n"
      "v_pk_add_f16 v18, v238, v242\n"
      "s_waitcnt lgkmcnt(3)\n"
      "v_mfma_f32_16x16x32_f16 v[184:187], v[116:119], v[224:227], v[184:187]\n"
      "v_pk_add_f16 v19, v239, v243\n"
      "v_pk_add_f16 v20, v20, v16\n"
      "s_setprio 2\n"
      "v_mfma_f32_16x16x32_f16 v[188:191], v[84:87], v[224:227], v[188:191]\n"
      "ds_read2_b32 v[162:163], v30 offset0:72 offset1:73\n"
      "ds_read_b128 v[224:227], v33 offset:960\n"
      "v_pk_add_f16 v21, v21, v17\n"
      "v_pk_add_f16 v22, v22, v18\n"
      "ds_read_b128 v[236:239], v34 offset:1024\n"
      "ds_read_b128 v[240:243], v34 offset:1040\n"
      "v_mfma_f32_16x16x32_f16 v[184:187], v[120:123], v[212:215], v[184:187]\n"
      "v_pk_add_f16 v23, v23, v19\n"
      "ds_write_b128 v38, v[20:23] offset:2080\n"
      "v_mfma_f32_16x16x32_f16 v[188:191], v[88:91], v[212:215], v[188:191]\n"
      "ds_read2_b32 v[164:165], v30 offset0:128 offset1:129\n"
      "ds_read_b128 v[212:215], v33 offset:1024\n"
      "v_mfma_f32_16x16x32_f16 v[184:187], v[124:127], v[216:219], v[184:187]\n"
      "v_mfma_f32_16x16x32_f16 v[188:191], v[92:95], v[216:219], v[188:191]\n"
      "s_waitcnt lgkmcnt(4)\n"
      "v_alignbit_b32 v245, v238, v237, 16\n"
      "v_alignbit_b32 v246, v239, v238, 16\n"
      "v_mfma_f32_16x16x32_f16 v[184:187], v[128:131], v[220:223], v[184:187]\n"
      "s_waitcnt lgkmcnt(3)\n"
      "v_alignbit_b32 v247, v240, v239, 16\n"
      "v_alignbit_b32 v248, v241, v240, 16\n"
      "v_mfma_f32_16x16x32_f16 v[188:191], v[96:99], v[220:223], v[188:191]\n"
      "ds_read2_b32 v[166:167], v30 offset0:168 offset1:169\n"
      "ds_read_b64_tr_b16 v[228:229], v35 offset:32\n"
      "ds_read_b64_tr_b16 v[230:231], v35 offset:16\n"
      "v_alignbit_b32 v249, v242, v241, 16\n"
      "v_pk_add_f16 v16, v245, v246\n"
      "v_mfma_f32_16x16x32_f16 v[184:187], v[132:135], v[224:227], v[184:187]\n"
      "v_pk_add_f16 v17, v246, v247\n"
      "v_pk_add_f16 v18, v247, v248\n"
      "v_mfma_f32_16x16x32_f16 v[188:191], v[100:103], v[224:227], v[188:191]\n"
      "v_pk_add_f16 v19, v248, v249\n"
      "v_pk_add_f16 v20, v16, v238\n"
      "s_waitcnt lgkmcnt(3)\n"
      "v_mfma_f32_16x16x32_f16 v[184:187], v[136:139], v[212:215], v[184:187]\n"
      "ds_read_b128 v[216:219], v33 offset:1088\n"
      "buffer_load_dwordx4 v[60:63], v41, s[20:23], s61 offen offset:2048 nt\n"
      "v_pk_add_f16 v21, v17, v239\n"
      "v_pk_add_f16 v22, v18, v240\n"
      "v_mfma_f32_16x16x32_f16 v[188:191], v[104:107], v[212:215], v[188:191]\n"
      "ds_read2_b32 v[168:169], v30 offset0:120 offset1:121\n"
      "v_pk_add_f16 v23, v19, v241\n"
      "ds_write_b128 v37, v[20:23] offset:1024\n"
      "v_pk_add_f16 v16, v237, v239\n"
      "v_mfma_f32_16x16x32_f16 v[192:195], v[72:75], v[212:215], 0\n"
      "ds_read_b128 v[220:223], v33 offset:1152\n"
      "v_pk_add_f16 v17, v238, v240\n"
      "v_pk_add_f16 v18, v239, v241\n"
      "v_mfma_f32_16x16x32_f16 v[200:203], v[44:47], v[212:215], 0\n"
      "v_pk_add_f16 v19, v240, v242\n"
      "v_pk_add_f16 v20, v20, v16\n"
      "s_waitcnt lgkmcnt(3)\n"
      "v_mfma_f32_16x16x32_f16 v[188:191], v[228:231], v[140:143], v[188:191]\n"
      "ds_read_b128 v[224:227], v33 offset:1216\n"
      "v_cvt_pk_bf16_f32 v24, v184, v186\n"
      "v_cvt_pk_bf16_f32 v25, v185, v187\n"
      "ds_write2st64_b32 v39, v24, v25 offset0:1 offset1:9\n"
      "v_mfma_f32_16x16x32_f16 v[192:195], v[228:231], v[144:147], v[192:195]\n"
      "ds_read2_b32 v[170:171], v30 offset0:160 offset1:161\n"
      "v_pk_add_f16 v21, v21, v17\n"
      "v_pk_add_f16 v22, v22, v18\n"
      "v_mfma_f32_16x16x32_f16 v[196:199], v[228:231], v[148:151], v[196:199]\n"
      "ds_read_b64_tr_b16 v[232:233], v35 offset:3128\n"
      "ds_read_b64_tr_b16 v[234:235], v35 offset:3112\n"
      "v_pk_add_f16 v23, v23, v19\n"
      "ds_write_b128 v37, v[20:23] offset:4128\n"
      "v_alignbit_b32 v244, v237, v236, 16\n"
      "v_mfma_f32_16x16x32_f16 v[188:191], v[108:111], v[216:219], v[188:191]\n"
      "v_alignbit_b32 v250, v243, v242, 16\n"
      "v_pk_add_f16 v16, v244, v247\n"
      "v_mfma_f32_16x16x32_f16 v[192:195], v[76:79], v[216:219], v[192:195]\n"
      "v_pk_add_f16 v17, v245, v248\n"
      "v_pk_add_f16 v18, v246, v249\n"
      "s_waitcnt lgkmcnt(3)\n"
      "v_mfma_f32_16x16x32_f16 v[188:191], v[112:115], v[220:223], v[188:191]\n"
      "ds_read2_b32 v[172:173], v30 offset0:112 offset1:113\n"
      "ds_read_b128 v[212:215], v33 offset:1280\n"
      "v_pk_add_f16 v19, v247, v250\n"
      "v_pk_add_f16 v20, v20, v16\n"
      "v_mfma_f32_16x16x32_f16 v[192:195], v[80:83], v[220:223], v[192:195]\n"
      "v_pk_add_f16 v21, v21, v17\n"
      "v_pk_add_f16 v22, v22, v18\n"
      "v_mfma_f32_16x16x32_f16 v[188:191], v[116:119], v[224:227], v[188:191]\n"
      "ds_read_b128 v[216:219], v33 offset:1344\n"
      "v_pk_add_f16 v23, v23, v19\n"
      "ds_write_b128 v38, v[20:23] offset:1024\n"
      "v_pk_add_f16 v16, v236, v240\n"
      "v_mfma_f32_16x16x32_f16 v[192:195], v[84:87], v[224:227], v[192:195]\n"
      "v_pk_add_f16 v17, v237, v241\n"
      "v_pk_add_f16 v18, v238, v242\n"
      "s_setprio 1\n"
      "s_waitcnt lgkmcnt(2)\n"
      "v_mfma_f32_16x16x32_f16 v[188:191], v[232:235], v[152:155], v[188:191]\n"
      "ds_read2_b32 v[174:175], v30 offset0:152 offset1:153\n"
      "ds_read_b128 v[220:223], v33 offset:1408\n"
      "v_pk_add_f16 v19, v239, v243\n"
      "v_pk_add_f16 v20, v20, v16\n"
      "v_mfma_f32_16x16x32_f16 v[192:195], v[232:235], v[156:159], v[192:195]\n"
      "v_pk_add_f16 v21, v21, v17\n"
      "v_pk_add_f16 v22, v22, v18\n"
      "v_mfma_f32_16x16x32_f16 v[196:199], v[232:235], v[160:163], v[196:199]\n"
      "ds_read_b128 v[224:227], v33 offset:1472\n"
      "v_pk_add_f16 v23, v23, v19\n"
      "ds_write_b128 v38, v[20:23] offset:3104\n"
      "v_mfma_f32_16x16x32_f16 v[188:191], v[120:123], v[212:215], v[188:191]\n"
      "v_readlane_b32 s72, v255, 0\n"
      "v_mfma_f32_16x16x32_f16 v[192:195], v[88:91], v[212:215], v[192:195]\n"
      "ds_read2_b32 v[176:177], v30 offset0:104 offset1:105\n"
      "ds_read_b128 v[212:215], v33 offset:1536\n"
      "v_readlane_b32 s73, v255, 1\n"
      "s_waitcnt lgkmcnt(4)\n"
      "v_mfma_f32_16x16x32_f16 v[188:191], v[124:127], v[216:219], v[188:191]\n"
      "v_readlane_b32 s74, v255, 2\n"
      "v_mfma_f32_16x16x32_f16 v[192:195], v[92:95], v[216:219], v[192:195]\n"
      "v_readlane_b32 s75, v255, 3\n"
      "v_mfma_f32_16x16x32_f16 v[188:191], v[128:131], v[220:223], v[188:191]\n"
      "v_readlane_b32 s76, v255, 4\n"
      "v_mfma_f32_16x16x32_f16 v[192:195], v[96:99], v[220:223], v[192:195]\n"
      "ds_read2_b32 v[178:179], v30 offset0:144 offset1:145\n"
      "ds_read_b64_tr_b16 v[228:229], v35 offset:544\n"
      "ds_read_b64_tr_b16 v[230:231], v35 offset:528\n"
      "v_readlane_b32 s77, v255, 5\n"
      "s_waitcnt lgkmcnt(3)\n"
      "v_mfma_f32_16x16x32_f16 v[188:191], v[132:135], v[224:227], v[188:191]\n"
      "v_readlane_b32 s78, v255, 6\n"
      "v_mfma_f32_16x16x32_f16 v[192:195], v[100:103], v[224:227], v[192:195]\n"
      "v_readlane_b32 s79, v255, 7\n"
      "v_mfma_f32_16x16x32_f16 v[188:191], v[136:139], v[212:215], v[188:191]\n"
      "ds_read_b128 v[216:219], v33 offset:1600\n"
      "s_movk_i32 s59, 0x7fff\n"
      "s_waitcnt vmcnt(1)\n"
      "s_nop 0\n"
      ".Lcsgh%=:\n"
      "v_cmp_ne_u32 s[64:65], s15, v69\n"
      "v_cmp_ne_u32 vcc, s15, v71\n"
      "s_nop 0\n"
      "s_or_b64 vcc, vcc, s[64:65]\n"
      "s_cbranch_scc1 .Lcsgsh%=\n"
      ".Lcsgoh%=:\n"
      "v_mov_b32 v42, v8\n"
      "v_mfma_f32_16x16x32_f16 v[192:195], v[104:107], v[212:215], v[192:195]\n"
      "v_and_b32 v26, 0xffff0000, v68\n"
      "v_lshlrev_b32 v28, 16, v68\n"
      "v_mfma_f32_16x16x32_f16 v[196:199], v[72:75], v[212:215], v[196:199]\n"
      "ds_read_b128 v[220:223], v33 offset:1664\n"
      "v_rcp_f32 v26, v26\n"
      "v_and_b32 v27, 0xffff0000, v70\n"
      "v_mfma_f32_16x16x32_f16 v[204:207], v[44:47], v[212:215], 0\n"
      "v_lshlrev_b32 v29, 16, v70\n"
      "v_rcp_f32 v27, v27\n"
      "s_waitcnt lgkmcnt(1)\n"
      "v_mfma_f32_16x16x32_f16 v[192:195], v[228:231], v[140:143], v[192:195]\n"
      "ds_read_b128 v[224:227], v33 offset:1728\n"
      "v_cvt_pk_bf16_f32 v24, v188, v190\n"
      "v_cvt_pk_bf16_f32 v25, v189, v191\n"
      "ds_write2st64_b32 v39, v24, v25 offset0:2 offset1:10\n"
      "v_mfma_f32_16x16x32_f16 v[196:199], v[228:231], v[144:147], v[196:199]\n"
      "v_mul_f32 v28, -v28, v26\n"
      "v_mul_f32 v29, -v29, v27\n"
      "v_mfma_f32_16x16x32_f16 v[200:203], v[228:231], v[148:151], v[200:203]\n"
      "ds_read_b64_tr_b16 v[232:233], v35 offset:3640\n"
      "ds_read_b64_tr_b16 v[234:235], v35 offset:3624\n"
      "v_lshlrev_b32 v180, 16, v56\n"
      "s_waitcnt vmcnt(0)\n"
      "v_lshlrev_b32 v181, 16, v60\n"
      "v_mfma_f32_16x16x32_f16 v[192:195], v[108:111], v[216:219], v[192:195]\n"
      "v_and_b32 v182, 0xffff0000, v56\n"
      "v_and_b32 v183, 0xffff0000, v60\n"
      "v_mfma_f32_16x16x32_f16 v[196:199], v[76:79], v[216:219], v[196:199]\n"
      "v_fma_f32 v180, v180, v26, v28\n"
      "v_fma_f32 v181, v181, v27, v29\n"
      "s_setprio 0\n"
      "s_waitcnt lgkmcnt(2)\n"
      "v_mfma_f32_16x16x32_f16 v[192:195], v[112:115], v[220:223], v[192:195]\n"
      "ds_read_b128 v[212:215], v33 offset:1792\n"
      "v_fma_f32 v182, v182, v26, v28\n"
      "v_fma_f32 v183, v183, v27, v29\n"
      "v_mfma_f32_16x16x32_f16 v[196:199], v[80:83], v[220:223], v[196:199]\n"
      "v_cvt_pk_f16_f32 v180, v180, v181\n"
      "v_cvt_pk_f16_f32 v182, v182, v183\n"
      "ds_write2st64_b32 v32, v180, v182 offset0:8 offset1:27\n"
      "v_mfma_f32_16x16x32_f16 v[192:195], v[116:119], v[224:227], v[192:195]\n"
      "ds_read_b128 v[216:219], v33 offset:1856\n"
      "v_lshlrev_b32 v184, 16, v57\n"
      "v_lshlrev_b32 v185, 16, v61\n"
      "v_mfma_f32_16x16x32_f16 v[196:199], v[84:87], v[224:227], v[196:199]\n"
      "v_and_b32 v186, 0xffff0000, v57\n"
      "v_and_b32 v187, 0xffff0000, v61\n"
      "s_waitcnt lgkmcnt(2)\n"
      "v_mfma_f32_16x16x32_f16 v[192:195], v[232:235], v[152:155], v[192:195]\n"
      "ds_read_b128 v[220:223], v33 offset:1920\n"
      "v_fma_f32 v184, v184, v26, v28\n"
      "v_fma_f32 v185, v185, v27, v29\n"
      "v_mfma_f32_16x16x32_f16 v[196:199], v[232:235], v[156:159], v[196:199]\n"
      "v_fma_f32 v186, v186, v26, v28\n"
      "v_fma_f32 v187, v187, v27, v29\n"
      "s_cmp_lg_u32 s33, 0\n"
      "s_cbranch_scc1 .Lcg%=\n"
      "buffer_load_dwordx2 v[252:253], v9, s[16:19], s43 offen sc1\n"
      ".Lcg%=:\n"
      "v_mfma_f32_16x16x32_f16 v[200:203], v[232:235], v[160:163], v[200:203]\n"
      "ds_read_b128 v[224:227], v33 offset:1984\n"
      "v_cvt_pk_f16_f32 v184, v184, v185\n"
      "v_cvt_pk_f16_f32 v186, v186, v187\n"
      "ds_write2st64_b32 v32, v184, v186 offset0:46 offset1:65\n"
      "v_mfma_f32_16x16x32_f16 v[192:195], v[120:123], v[212:215], v[192:195]\n"
      "v_lshlrev_b32 v180, 16, v58\n"
      "v_lshlrev_b32 v181, 16, v62\n"
      "v_mfma_f32_16x16x32_f16 v[196:199], v[88:91], v[212:215], v[196:199]\n"
      "ds_read_b128 v[212:215], v33 offset:2048\n"
      "v_and_b32 v182, 0xffff0000, v58\n"
      "v_and_b32 v183, 0xffff0000, v62\n"
      "s_waitcnt lgkmcnt(3)\n"
      "v_mfma_f32_16x16x32_f16 v[192:195], v[124:127], v[216:219], v[192:195]\n"
      "v_fma_f32 v180, v180, v26, v28\n"
      "v_fma_f32 v181, v181, v27, v29\n"
      "v_mfma_f32_16x16x32_f16 v[196:199], v[92:95], v[216:219], v[196:199]\n"
      "v_fma_f32 v182, v182, v26, v28\n"
      "v_fma_f32 v183, v183, v27, v29\n"
      "v_mfma_f32_16x16x32_f16 v[192:195], v[128:131], v[220:223], v[192:195]\n"
      "v_cvt_pk_f16_f32 v180, v180, v181\n"
      "v_cvt_pk_f16_f32 v182, v182, v183\n"
      "ds_write2st64_b32 v32, v180, v182 offset0:84 offset1:103\n"
      "v_mfma_f32_16x16x32_f16 v[196:199], v[96:99], v[220:223], v[196:199]\n"
      "ds_read_b64_tr_b16 v[228:229], v36 offset:24\n"
      "ds_read_b64_tr_b16 v[230:231], v36 offset:2088\n"
      "v_lshlrev_b32 v184, 16, v59\n"
      "v_lshlrev_b32 v185, 16, v63\n"
      "s_waitcnt lgkmcnt(3)\n"
      "v_mfma_f32_16x16x32_f16 v[192:195], v[132:135], v[224:227], v[192:195]\n"
      "v_and_b32 v186, 0xffff0000, v59\n"
      "v_and_b32 v187, 0xffff0000, v63\n"
      "v_mfma_f32_16x16x32_f16 v[196:199], v[100:103], v[224:227], v[196:199]\n"
      "v_fma_f32 v184, v184, v26, v28\n"
      "v_fma_f32 v185, v185, v27, v29\n"
      "v_mfma_f32_16x16x32_f16 v[192:195], v[136:139], v[212:215], v[192:195]\n"
      "v_fma_f32 v186, v186, v26, v28\n"
      "v_fma_f32 v187, v187, v27, v29\n"
      "v_mfma_f32_16x16x32_f16 v[196:199], v[104:107], v[212:215], v[196:199]\n"
      "ds_read_b64_tr_b16 v[232:233], v36 offset:536\n"
      "ds_read_b64_tr_b16 v[234:235], v36 offset:2600\n"
      "v_cvt_pk_f16_f32 v184, v184, v185\n"
      "v_cvt_pk_f16_f32 v186, v186, v187\n"
      "ds_write2st64_b32 v32, v184, v186 offset0:122 offset1:141\n"
      "v_mfma_f32_16x16x32_f16 v[200:203], v[72:75], v[212:215], v[200:203]\n"
      "v_permlane32_swap_b32 v56, v60\n"
      "v_permlane32_swap_b32 v57, v61\n"
      "v_mfma_f32_16x16x32_f16 v[208:211], v[44:47], v[212:215], 0\n"
      "v_permlane32_swap_b32 v58, v62\n"
      "v_permlane32_swap_b32 v59, v63\n"
      "s_waitcnt lgkmcnt(3)\n"
      "v_mfma_f32_16x16x32_f16 v[196:199], v[228:231], v[164:167], v[196:199]\n"
      "v_cvt_pk_bf16_f32 v24, v192, v194\n"
      "v_cvt_pk_bf16_f32 v25, v193, v195\n"
      "ds_write2st64_b32 v39, v24, v25 offset0:3 offset1:11\n"
      "v_mfma_f32_16x16x32_f16 v[200:203], v[228:231], v[168:171], v[200:203]\n"
      "v_mfma_f32_16x16x32_f16 v[204:207], v[228:231], v[172:175], v[204:207]\n"
      "v_mfma_f32_16x16x32_f16 v[208:211], v[228:231], v[176:179], v[208:211]\n"
      "ds_read_b64_tr_b16 v[228:229], v35 offset:1056\n"
      "ds_read_b64_tr_b16 v[230:231], v35 offset:1040\n"
      "s_cmp_eq_u32 s33, 0\n"
      "s_cbranch_scc1 .Lch%=\n"
      ".Lcho%=:\n"
      "s_waitcnt lgkmcnt(2)\n"
      "s_barrier\n"
      "s_setprio 3\n"
      "v_mfma_f32_16x16x32_f16 v[200:203], v[232:235], v[164:167], v[200:203]\n"
      "ds_read_b64_tr_b16 v[64:65], v40 offset:0\n"
      "ds_read_b64_tr_b16 v[66:67], v40 offset:35072\n"
      "v_lshlrev_b32 v68, 16, v48\n"
      "v_and_b32 v69, 0xffff0000, v48\n"
      "v_mfma_f32_16x16x32_f16 v[204:207], v[232:235], v[168:171], v[204:207]\n"
      "v_lshlrev_b32 v70, 16, v49\n"
      "v_and_b32 v71, 0xffff0000, v49\n"
      "ds_read_b128 v[236:239], v34 offset:2048\n"
      "ds_read_b128 v[240:243], v34 offset:2064\n"
      "ds_read_b32 v12, v14 offset:256\n"
      "v_mfma_f32_16x16x32_f16 v[208:211], v[232:235], v[172:175], v[208:211]\n"
      "ds_read_b128 v[216:219], v33 offset:2112\n"
      "ds_read_b64_tr_b16 v[232:233], v35 offset:4152\n"
      "ds_read_b64_tr_b16 v[234:235], v35 offset:4136\n"
      "v_lshlrev_b32 v26, 16, v50\n"
      "v_and_b32 v27, 0xffff0000, v50\n"
      "s_waitcnt lgkmcnt(6)\n"
      "v_mfma_f32_16x16x32_f16 v[196:199], v[228:231], v[140:143], v[196:199]\n"
      "v_lshlrev_b32 v28, 16, v51\n"
      "v_and_b32 v29, 0xffff0000, v51\n"
      "v_mfma_f32_16x16x32_f16 v[200:203], v[228:231], v[144:147], v[200:203]\n"
      "ds_read_b128 v[220:223], v33 offset:2176\n"
      "v_lshlrev_b32 v48, 16, v64\n"
      "v_and_b32 v64, 0xffff0000, v64\n"
      "v_mfma_f32_16x16x32_f16 v[204:207], v[228:231], v[148:151], v[204:207]\n"
      "v_fmac_f32 v68, s72, v48\n"
      "v_fmac_f32 v69, s73, v64\n"
      "s_waitcnt lgkmcnt(1)\n"
      "v_mfma_f32_16x16x32_f16 v[196:199], v[232:235], v[152:155], v[196:199]\n"
      "ds_read_b128 v[224:227], v33 offset:2240\n"
      "v_lshlrev_b32 v49, 16, v65\n"
      "v_and_b32 v65, 0xffff0000, v65\n"
      "v_mfma_f32_16x16x32_f16 v[200:203], v[232:235], v[156:159], v[200:203]\n"
      "v_fmac_f32 v70, s74, v49\n"
      "v_fmac_f32 v71, s75, v65\n"
      "v_mfma_f32_16x16x32_f16 v[204:207], v[232:235], v[160:163], v[204:207]\n"
      "ds_read_b128 v[212:215], v33 offset:2304\n"
      "v_lshlrev_b32 v50, 16, v66\n"
      "v_and_b32 v66, 0xffff0000, v66\n"
      "v_mfma_f32_16x16x32_f16 v[196:199], v[108:111], v[216:219], v[196:199]\n"
      "v_fmac_f32 v26, s76, v50\n"
      "v_fmac_f32 v27, s77, v66\n"
      "v_mfma_f32_16x16x32_f16 v[200:203], v[76:79], v[216:219], v[200:203]\n"
      "ds_read_b64_tr_b16 v[228:229], v35 offset:1568\n"
      "ds_read_b64_tr_b16 v[230:231], v35 offset:1552\n"
      "v_lshlrev_b32 v51, 16, v67\n"
      "v_and_b32 v67, 0xffff0000, v67\n"
      "s_waitcnt lgkmcnt(3)\n"
      "v_mfma_f32_16x16x32_f16 v[196:199], v[112:115], v[220:223], v[196:199]\n"
      "v_fmac_f32 v28, s78, v51\n"
      "v_fmac_f32 v29, s79, v67\n"
      "v_mfma_f32_16x16x32_f16 v[200:203], v[80:83], v[220:223], v[200:203]\n"
      "v_cvt_pk_bf16_f32 v48, v68, v69\n"
      "v_cvt_pk_bf16_f32 v49, v70, v71\n"
      "v_mfma_f32_16x16x32_f16 v[196:199], v[116:119], v[224:227], v[196:199]\n"
      "ds_read_b128 v[216:219], v33 offset:2368\n"
      "v_cvt_pk_bf16_f32 v50, v26, v27\n"
      "v_cvt_pk_bf16_f32 v51, v28, v29\n"
      "s_setprio 2\n"
      "v_mfma_f32_16x16x32_f16 v[200:203], v[84:87], v[224:227], v[200:203]\n"
      "v_lshlrev_b32 v13, 24, v12\n"
      "v_and_or_b32 v13, v13, s58, v42\n"
      "s_waitcnt lgkmcnt(1)\n"
      "v_mfma_f32_16x16x32_f16 v[196:199], v[120:123], v[212:215], v[196:199]\n"
      "ds_read_b128 v[220:223], v33 offset:2432\n"
      "s_cmp_lg_u32 s13, 0\n"
      "s_cbranch_scc1 .Lcs00%=\n"
      "buffer_store_dwordx4 v[48:51], v13, s[24:27], s54 offen\n"
      ".Lcs00%=:\n"
      "v_alignbit_b32 v245, v238, v237, 16\n"
      "v_alignbit_b32 v246, v239, v238, 16\n"
      "v_mfma_f32_16x16x32_f16 v[200:203], v[88:91], v[212:215], v[200:203]\n"
      "v_alignbit_b32 v247, v240, v239, 16\n"
      "v_alignbit_b32 v248, v241, v240, 16\n"
      "v_mfma_f32_16x16x32_f16 v[200:203], v[228:231], v[140:143], v[200:203]\n"
      "ds_read_b128 v[224:227], v33 offset:2496\n"
      "s_cmp_lg_u32 s13, 1\n"
      "s_cbranch_scc1 .Lcs01%=\n"
      "buffer_store_dwordx4 v[48:51], v13, s[24:27], s54 offen\n"
      ".Lcs01%=:\n"
      "v_alignbit_b32 v249, v242, v241, 16\n"
      "v_pk_add_f16 v16, v245, v246\n"
      "v_mfma_f32_16x16x32_f16 v[204:207], v[228:231], v[144:147], v[204:207]\n"
      "v_pk_add_f16 v17, v246, v247\n"
      "v_pk_add_f16 v18, v247, v248\n"
      "v_mfma_f32_16x16x32_f16 v[208:211], v[228:231], v[148:151], v[208:211]\n"
      "ds_read_b128 v[212:215], v33 offset:2560\n"
      "s_cmp_lg_u32 s13, 2\n"
      "s_cbranch_scc1 .Lcs02%=\n"
      "buffer_store_dwordx4 v[48:51], v13, s[24:27], s54 offen\n"
      ".Lcs02%=:\n"
      "v_pk_add_f16 v19, v248, v249\n"
      "v_pk_add_f16 v20, v16, v238\n"
      "s_waitcnt lgkmcnt(1)\n"
      "v_mfma_f32_16x16x32_f16 v[196:199], v[124:127], v[216:219], v[196:199]\n"
      "v_pk_add_f16 v21, v17, v239\n"
      "v_pk_add_f16 v22, v18, v240\n"
      "v_mfma_f32_16x16x32_f16 v[200:203], v[92:95], v[216:219], v[200:203]\n"
      "s_cmp_lg_u32 s13, 3\n"
      "s_cbranch_scc1 .Lcs03%=\n"
      "buffer_store_dwordx4 v[48:51], v13, s[24:27], s54 offen\n"
      ".Lcs03%=:\n"
      "v_pk_add_f16 v23, v19, v241\n"
      "ds_write_b128 v37, v[20:23] offset:2048\n"
      "v_pk_add_f16 v16, v237, v239\n"
      "v_mfma_f32_16x16x32_f16 v[196:199], v[128:131], v[220:223], v[196:199]\n"
      "ds_read_b64_tr_b16 v[232:233], v35 offset:4664\n"
      "ds_read_b64_tr_b16 v[234:235], v35 offset:4648\n"
      "ds_read_b64_tr_b16 v[64:65], v40 offset:512\n"
      "ds_read_b64_tr_b16 v[66:67], v40 offset:35584\n"
      "v_lshlrev_b32 v68, 16, v52\n"
      "v_and_b32 v69, 0xffff0000, v52\n"
      "v_mfma_f32_16x16x32_f16 v[200:203], v[96:99], v[220:223], v[200:203]\n"
      "v_lshlrev_b32 v70, 16, v53\n"
      "v_and_b32 v71, 0xffff0000, v53\n"
      "v_mfma_f32_16x16x32_f16 v[196:199], v[132:135], v[224:227], v[196:199]\n"
      "v_lshlrev_b32 v26, 16, v54\n"
      "v_and_b32 v27, 0xffff0000, v54\n"
      "v_mfma_f32_16x16x32_f16 v[200:203], v[100:103], v[224:227], v[200:203]\n"
      "ds_read_b128 v[216:219], v33 offset:2624\n"
      "v_lshlrev_b32 v28, 16, v55\n"
      "v_and_b32 v29, 0xffff0000, v55\n"
      "s_waitcnt lgkmcnt(1)\n"
      "v_mfma_f32_16x16x32_f16 v[196:199], v[136:139], v[212:215], v[196:199]\n"
      "v_lshlrev_b32 v52, 16, v64\n"
      "v_and_b32 v64, 0xffff0000, v64\n"
      "v_mfma_f32_16x16x32_f16 v[200:203], v[104:107], v[212:215], v[200:203]\n"
      "ds_read_b128 v[220:223], v33 offset:2688\n"
      "v_fmac_f32 v68, s72, v52\n"
      "v_fmac_f32 v69, s73, v64\n"
      "s_setprio 1\n"
      "v_mfma_f32_16x16x32_f16 v[204:207], v[72:75], v[212:215], v[204:207]\n"
      "v_lshlrev_b32 v53, 16, v65\n"
      "v_and_b32 v65, 0xffff0000, v65\n"
      "v_mfma_f32_16x16x32_f16 v[200:203], v[232:235], v[152:155], v[200:203]\n"
      "ds_read_b128 v[224:227], v33 offset:2752\n"
      "v_fmac_f32 v70, s74, v53\n"
      "v_fmac_f32 v71, s75, v65\n"
      "v_mfma_f32_16x16x32_f16 v[204:207], v[232:235], v[156:159], v[204:207]\n"
      "v_cvt_pk_bf16_f32 v24, v196, v198\n"
      "v_cvt_pk_bf16_f32 v25, v197, v199\n"
      "ds_write2st64_b32 v39, v24, v25 offset0:4 offset1:12\n"
      "v_mfma_f32_16x16x32_f16 v[208:211], v[232:235], v[160:163], v[208:211]\n"
      "ds_read_b128 v[212:215], v33 offset:2816\n"
      "v_lshlrev_b32 v54, 16, v66\n"
      "v_and_b32 v66, 0xffff0000, v66\n"
      "s_waitcnt lgkmcnt(3)\n"
      "v_mfma_f32_16x16x32_f16 v[200:203], v[108:111], v[216:219], v[200:203]\n"
      "v_fmac_f32 v26, s76, v54\n"
      "v_fmac_f32 v27, s77, v66\n"
      "v_mfma_f32_16x16x32_f16 v[204:207], v[76:79], v[216:219], v[204:207]\n"
      "ds_read_b128 v[216:219], v33 offset:2880\n"
      "v_lshlrev_b32 v55, 16, v67\n"
      "v_and_b32 v67, 0xffff0000, v67\n"
      "v_mfma_f32_16x16x32_f16 v[200:203], v[112:115], v[220:223], v[200:203]\n"
      "v_fmac_f32 v28, s78, v55\n"
      "v_fmac_f32 v29, s79, v67\n"
      "v_mfma_f32_16x16x32_f16 v[204:207], v[80:83], v[220:223], v[204:207]\n"
      "ds_read_b128 v[220:223], v33 offset:2944\n"
      "v_cvt_pk_bf16_f32 v52, v68, v69\n"
      "v_cvt_pk_bf16_f32 v53, v70, v71\n"
      "s_waitcnt lgkmcnt(2)\n"
      "v_mfma_f32_16x16x32_f16 v[200:203], v[116:119], v[224:227], v[200:203]\n"
      "v_cvt_pk_bf16_f32 v54, v26, v27\n"
      "v_cvt_pk_bf16_f32 v55, v28, v29\n"
      "v_mfma_f32_16x16x32_f16 v[204:207], v[84:87], v[224:227], v[204:207]\n"
      "ds_read_b128 v[224:227], v33 offset:3008\n"
      "v_lshlrev_b32 v13, 16, v12\n"
      "v_and_or_b32 v13, v13, s58, v42\n"
      "v_mfma_f32_16x16x32_f16 v[200:203], v[120:123], v[212:215], v[200:203]\n"
      "s_cmp_lg_u32 s13, 0\n"
      "s_cbranch_scc1 .Lcs10%=\n"
      "buffer_store_dwordx4 v[52:55], v13, s[24:27], s55 offen\n"
      ".Lcs10%=:\n"
      "v_pk_add_f16 v17, v238, v240\n"
      "v_pk_add_f16 v18, v239, v241\n"
      "s_setprio 0\n"
      "v_mfma_f32_16x16x32_f16 v[204:207], v[88:91], v[212:215], v[204:207]\n"
      "ds_read_b128 v[212:215], v33 offset:3072\n"
      "v_pk_add_f16 v19, v240, v242\n"
      "v_pk_add_f16 v20, v20, v16\n"
      "s_waitcnt lgkmcnt(1)\n"
      "v_mfma_f32_16x16x32_f16 v[200:203], v[124:127], v[216:219], v[200:203]\n"
      "s_cmp_lg_u32 s13, 1\n"
      "s_cbranch_scc1 .Lcs11%=\n"
      "buffer_store_dwordx4 v[52:55], v13, s[24:27], s55 offen\n"
      ".Lcs11%=:\n"
      "v_pk_add_f16 v21, v21, v17\n"
      "v_pk_add_f16 v22, v22, v18\n"
      "v_mfma_f32_16x16x32_f16 v[204:207], v[92:95], v[216:219], v[204:207]\n"
      "v_pk_add_f16 v23, v23, v19\n"
      "ds_write_b128 v37, v[20:23] offset:5152\n"
      "v_mfma_f32_16x16x32_f16 v[200:203], v[128:131], v[220:223], v[200:203]\n"
      "ds_read_b64_tr_b16 v[228:229], v36 offset:1048\n"
      "ds_read_b64_tr_b16 v[230:231], v36 offset:3112\n"
      "s_cmp_lg_u32 s13, 2\n"
      "s_cbranch_scc1 .Lcs12%=\n"
      "buffer_store_dwordx4 v[52:55], v13, s[24:27], s55 offen\n"
      ".Lcs12%=:\n"
      "v_mfma_f32_16x16x32_f16 v[204:207], v[96:99], v[220:223], v[204:207]\n"
      "v_mfma_f32_16x16x32_f16 v[200:203], v[132:135], v[224:227], v[200:203]\n"
      "ds_read_b128 v[216:219], v33 offset:3136\n"
      "s_cmp_lg_u32 s13, 3\n"
      "s_cbranch_scc1 .Lcs13%=\n"
      "buffer_store_dwordx4 v[52:55], v13, s[24:27], s55 offen\n"
      ".Lcs13%=:\n"
      "v_mfma_f32_16x16x32_f16 v[204:207], v[100:103], v[224:227], v[204:207]\n"
      "s_waitcnt lgkmcnt(1)\n"
      "v_mfma_f32_16x16x32_f16 v[200:203], v[136:139], v[212:215], v[200:203]\n"
      "ds_read_b128 v[220:223], v33 offset:3200\n"
      "v_mfma_f32_16x16x32_f16 v[204:207], v[104:107], v[212:215], v[204:207]\n"
      "v_mfma_f32_16x16x32_f16 v[208:211], v[72:75], v[212:215], v[208:211]\n"
      "ds_read_b128 v[224:227], v33 offset:3264\n"
      "v_mfma_f32_16x16x32_f16 v[204:207], v[228:231], v[164:167], v[204:207]\n"
      "v_mfma_f32_16x16x32_f16 v[208:211], v[228:231], v[168:171], v[208:211]\n"
      "ds_read_b64_tr_b16 v[232:233], v36 offset:1560\n"
      "ds_read_b64_tr_b16 v[234:235], v36 offset:3624\n"
      "v_cvt_pk_bf16_f32 v24, v200, v202\n"
      "v_cvt_pk_bf16_f32 v25, v201, v203\n"
      "ds_write2st64_b32 v39, v24, v25 offset0:5 offset1:13\n"
      "s_waitcnt lgkmcnt(5)\n"
      "v_mfma_f32_16x16x32_f16 v[204:207], v[108:111], v[216:219], v[204:207]\n"
      "ds_read_b128 v[212:215], v33 offset:3328\n"
      "v_mfma_f32_16x16x32_f16 v[208:211], v[76:79], v[216:219], v[208:211]\n"
      "s_waitcnt lgkmcnt(5)\n"
      "v_mfma_f32_16x16x32_f16 v[204:207], v[112:115], v[220:223], v[204:207]\n"
      "ds_read_b128 v[216:219], v33 offset:3392\n"
      "s_waitcnt lgkmcnt(2)\n"
      "s_barrier\n"
      "s_setprio 3\n"
      "v_mfma_f32_16x16x32_f16 v[208:211], v[80:83], v[220:223], v[208:211]\n"
      "ds_read_b64_tr_b16 v[64:65], v40 offset:1024\n"
      "ds_read_b64_tr_b16 v[66:67], v40 offset:36096\n"
      "v_lshlrev_b32 v68, 16, v56\n"
      "v_and_b32 v69, 0xffff0000, v56\n"
      "v_mfma_f32_16x16x32_f16 v[204:207], v[116:119], v[224:227], v[204:207]\n"
      "ds_read_b128 v[220:223], v33 offset:3456\n"
      "v_lshlrev_b32 v70, 16, v57\n"
      "v_and_b32 v71, 0xffff0000, v57\n"
      "v_mfma_f32_16x16x32_f16 v[208:211], v[84:87], v[224:227], v[208:211]\n"
      "v_lshlrev_b32 v26, 16, v58\n"
      "v_and_b32 v27, 0xffff0000, v58\n"
      "v_mfma_f32_16x16x32_f16 v[208:211], v[232:235], v[164:167], v[208:211]\n"
      "ds_read_b64_tr_b16 v[228:229], v35 offset:2080\n"
      "ds_read_b64_tr_b16 v[230:231], v35 offset:2064\n"
      "v_lshlrev_b32 v28, 16, v59\n"
      "v_and_b32 v29, 0xffff0000, v59\n"
      "s_waitcnt lgkmcnt(3)\n"
      "v_mfma_f32_16x16x32_f16 v[204:207], v[120:123], v[212:215], v[204:207]\n"
      "v_lshlrev_b32 v56, 16, v64\n"
      "v_and_b32 v64, 0xffff0000, v64\n"
      "v_mfma_f32_16x16x32_f16 v[208:211], v[88:91], v[212:215], v[208:211]\n"
      "ds_read_b128 v[224:227], v33 offset:3520\n"
      "v_fmac_f32 v68, s72, v56\n"
      "v_fmac_f32 v69, s73, v64\n"
      "v_mfma_f32_16x16x32_f16 v[204:207], v[124:127], v[216:219], v[204:207]\n"
      "v_lshlrev_b32 v57, 16, v65\n"
      "v_and_b32 v65, 0xffff0000, v65\n"
      "v_mfma_f32_16x16x32_f16 v[208:211], v[92:95], v[216:219], v[208:211]\n"
      "ds_read_b128 v[212:215], v33 offset:3584\n"
      "v_fmac_f32 v70, s74, v57\n"
      "v_fmac_f32 v71, s75, v65\n"
      "s_setprio 2\n"
      "s_waitcnt lgkmcnt(2)\n"
      "v_mfma_f32_16x16x32_f16 v[204:207], v[128:131], v[220:223], v[204:207]\n"
      "v_lshlrev_b32 v58, 16, v66\n"
      "v_and_b32 v66, 0xffff0000, v66\n"
      "v_mfma_f32_16x16x32_f16 v[208:211], v[96:99], v[220:223], v[208:211]\n"
      "ds_read_b128 v[216:219], v33 offset:3648\n"
      "v_fmac_f32 v26, s76, v58\n"
      "v_fmac_f32 v27, s77, v66\n"
      "v_mfma_f32_16x16x32_f16 v[204:207], v[228:231], v[140:143], v[204:207]\n"
      "ds_read_b64_tr_b16 v[232:233], v35 offset:5176\n"
      "ds_read_b64_tr_b16 v[234:235], v35 offset:5160\n"
      "v_lshlrev_b32 v59, 16, v67\n"
      "v_and_b32 v67, 0xffff0000, v67\n"
      "v_mfma_f32_16x16x32_f16 v[208:211], v[228:231], v[144:147], v[208:211]\n"
      "v_fmac_f32 v28, s78, v59\n"
      "v_fmac_f32 v29, s79, v67\n"
      "s_waitcnt lgkmcnt(3)\n"
      "v_mfma_f32_16x16x32_f16 v[204:207], v[132:135], v[224:227], v[204:207]\n"
      "ds_read_b128 v[220:223], v33 offset:3712\n"
      "v_cvt_pk_bf16_f32 v56, v68, v69\n"
      "v_cvt_pk_bf16_f32 v57, v70, v71\n"
      "v_mfma_f32_16x16x32_f16 v[208:211], v[100:103], v[224:227], v[208:211]\n"
      "ds_read_b128 v[224:227], v33 offset:3776\n"
      "v_cvt_pk_bf16_f32 v58, v26, v27\n"
      "v_cvt_pk_bf16_f32 v59, v28, v29\n"
      "v_mfma_f32_16x16x32_f16 v[204:207], v[136:139], v[212:215], v[204:207]\n"
      "v_lshlrev_b32 v13, 8, v12\n"
      "v_and_or_b32 v13, v13, s58, v42\n"
      "s_setprio 1\n"
      "v_mfma_f32_16x16x32_f16 v[208:211], v[104:107], v[212:215], v[208:211]\n"
      "ds_read_b64_tr_b16 v[228:229], v35 offset:2592\n"
      "ds_read_b64_tr_b16 v[230:231], v35 offset:2576\n"
      "ds_read_b128 v[212:215], v33 offset:3840\n"
      "s_cmp_lg_u32 s13, 0\n"
      "s_cbranch_scc1 .Lcs20%=\n"
      "buffer_store_dwordx4 v[56:59], v13, s[24:27], s56 offen\n"
      ".Lcs20%=:\n"
      "v_lshlrev_b32 v68, 16, v60\n"
      "v_and_b32 v69, 0xffff0000, v60\n"
      "s_waitcnt lgkmcnt(3)\n"
      "v_mfma_f32_16x16x32_f16 v[208:211], v[108:111], v[216:219], v[208:211]\n"
      "ds_read_b128 v[216:219], v33 offset:3904\n"
      "v_lshlrev_b32 v70, 16, v61\n"
      "v_and_b32 v71, 0xffff0000, v61\n"
      "v_mfma_f32_16x16x32_f16 v[204:207], v[232:235], v[152:155], v[204:207]\n"
      "s_cmp_lg_u32 s13, 1\n"
      "s_cbranch_scc1 .Lcs21%=\n"
      "buffer_store_dwordx4 v[56:59], v13, s[24:27], s56 offen\n"
      ".Lcs21%=:\n"
      "v_lshlrev_b32 v26, 16, v62\n"
      "v_and_b32 v27, 0xffff0000, v62\n"
      "v_mfma_f32_16x16x32_f16 v[208:211], v[232:235], v[156:159], v[208:211]\n"
      "ds_read_b64_tr_b16 v[232:233], v35 offset:5688\n"
      "ds_read_b64_tr_b16 v[234:235], v35 offset:5672\n"
      "v_lshlrev_b32 v28, 16, v63\n"
      "v_and_b32 v29, 0xffff0000, v63\n"
      "v_mfma_f32_16x16x32_f16 v[208:211], v[112:115], v[220:223], v[208:211]\n"
      "ds_read_b128 v[220:223], v33 offset:3968\n"
      "s_cmp_lg_u32 s13, 2\n"
      "s_cbranch_scc1 .Lcs22%=\n"
      "buffer_store_dwordx4 v[56:59], v13, s[24:27], s56 offen\n"
      ".Lcs22%=:\n"
      "s_setprio 0\n"
      "v_mfma_f32_16x16x32_f16 v[208:211], v[116:119], v[224:227], v[208:211]\n"
      "ds_read_b128 v[224:227], v33 offset:4032\n"
      "s_waitcnt lgkmcnt(3)\n"
      "v_mfma_f32_16x16x32_f16 v[208:211], v[120:123], v[212:215], v[208:211]\n"
      "ds_read_b128 v[212:215], v33 offset:4096\n"
      "v_cvt_pk_bf16_f32 v24, v204, v206\n"
      "v_cvt_pk_bf16_f32 v25, v205, v207\n"
      "ds_write2st64_b32 v39, v24, v25 offset0:6 offset1:14\n"
      "s_cmp_lg_u32 s13, 3\n"
      "s_cbranch_scc1 .Lcs23%=\n"
      "buffer_store_dwordx4 v[56:59], v13, s[24:27], s56 offen\n"
      ".Lcs23%=:\n"
      "v_mfma_f32_16x16x32_f16 v[208:211], v[228:231], v[140:143], v[208:211]\n"
      "v_mfma_f32_16x16x32_f16 v[208:211], v[124:127], v[216:219], v[208:211]\n"
      "s_waitcnt lgkmcnt(2)\n"
      "v_mfma_f32_16x16x32_f16 v[208:211], v[128:131], v[220:223], v[208:211]\n"
      "v_mfma_f32_16x16x32_f16 v[208:211], v[232:235], v[152:155], v[208:211]\n"
      "v_mfma_f32_16x16x32_f16 v[208:211], v[132:135], v[224:227], v[208:211]\n"
      "s_waitcnt lgkmcnt(1)\n"
      "v_mfma_f32_16x16x32_f16 v[208:211], v[136:139], v[212:215], v[208:211]\n"
      "s_nop 7\n"
      "v_cvt_pk_bf16_f32 v24, v208, v210\n"
      "v_cvt_pk_bf16_f32 v25, v209, v211\n"
      "ds_write2st64_b32 v39, v24, v25 offset0:7 offset1:15\n"
      "s_waitcnt lgkmcnt(0)\n"
      "s_barrier\n"
      "ds_read_b64_tr_b16 v[64:65], v40 offset:1536\n"
      "ds_read_b64_tr_b16 v[66:67], v40 offset:36608\n"
      "s_cmp_lg_u32 s33, 0\n"
      "s_cbranch_scc1 .Lcb%=\n"
      "ds_read_u16_d16_hi v252, v8 offset:56384\n"
      "ds_read_u16_d16_hi v253, v8 offset:65152\n"
      "s_waitcnt lgkmcnt(0)\n"
      "v_pk_mul_f32 v[252:253], v[252:253], v[254:255]\n"
      "v_cvt_pk_bf16_f32 v252, v252, v253\n"
      "buffer_atomic_pk_add_bf16 v252, v251, s[24:27], s41 offen\n"
      ".Lcb%=:\n"
      "s_waitcnt lgkmcnt(1)\n"
      "v_lshlrev_b32 v60, 16, v64\n"
      "v_and_b32 v64, 0xffff0000, v64\n"
      "v_fmac_f32 v68, s72, v60\n"
      "v_fmac_f32 v69, s73, v64\n"
      "v_lshlrev_b32 v61, 16, v65\n"
      "v_and_b32 v65, 0xffff0000, v65\n"
      "v_fmac_f32 v70, s74, v61\n"
      "v_fmac_f32 v71, s75, v65\n"
      "s_waitcnt lgkmcnt(0)\n"
      "v_lshlrev_b32 v62, 16, v66\n"
      "v_and_b32 v66, 0xffff0000, v66\n"
      "v_fmac_f32 v26, s76, v62\n"
      "v_fmac_f32 v27, s77, v66\n"
      "v_lshlrev_b32 v63, 16, v67\n"
      "v_and_b32 v67, 0xffff0000, v67\n"
      "v_fmac_f32 v28, s78, v63\n"
      "v_fmac_f32 v29, s79, v67\n"
      "v_cvt_pk_bf16_f32 v60, v68, v69\n"
      "v_cvt_pk_bf16_f32 v61, v70, v71\n"
      "v_cvt_pk_bf16_f32 v62, v26, v27\n"
      "v_cvt_pk_bf16_f32 v63, v28, v29\n"
      "v_and_or_b32 v13, v12, s58, v42\n"
      "buffer_store_dwordx4 v[60:63], v13, s[24:27], s57 offen\n"
      ".LEx%=:\ns_endpgm\n"
      ".Lcsgsk%=:\n"
      "s_sleep 4\n"
      "s_sub_u32 s59, s59, 1\n"
      "s_cbranch_scc1 .Lcsgok%=\n"
      "buffer_load_dwordx4 v[64:67], v42, s[16:19], s62 offen sc1\n"
      "s_waitcnt vmcnt(0)\n"
      "s_branch .Lcsgk%=\n"
      ".Lcsgsh%=:\n"
      "s_sleep 4\n"
      "s_sub_u32 s59, s59, 1\n"
      "s_cbranch_scc1 .Lcsgoh%=\n"
      "buffer_load_dwordx4 v[68:71], v42, s[16:19], s63 offen sc1\n"
      "s_waitcnt vmcnt(0)\n"
      "s_branch .Lcsgh%=\n"
      ".Lch%=:\n"
      "s_movk_i32 s59, 0x7fff\n"
      ".Lchl%=:\n"
      "s_waitcnt vmcnt(0)\n"
      "v_cmp_ne_u32 vcc, s15, v253\n"
      "v_not_b32 v255, v252\n"
      "s_sub_u32 s59, s59, 1\n"
      "s_cbranch_scc1 .Lchk%=\n"
      "s_cbranch_vccz .Lchk%=\n"
      "s_sleep 1\n"
      "buffer_load_dwordx2 v[252:253], v9, s[16:19], s43 offen sc1\n"
      "s_branch .Lchl%=\n"
      ".Lchk%=:\n"
      "v_and_b32 v31, 0x1ff, v255\n"
      "v_bfe_u32 v8, v255, 9, 2\n"
      "v_lshl_add_u32 v31, v31, 2, v8\n"
      "v_mov_b32 v8, 64\n"
      "v_add_u32 v31, 0x27700, v31\n"
      "ds_write_b8 v31, v8 \n"
      "v_and_b32 v31, 1, v255\n"
      "v_and_b32 v8, 0x7fe, v255\n"
      "v_lshl_add_u32 v8, v31, 11, v8\n"
      "v_mul_u32_u24 v31, 0x1120, v10\n"
      "v_add_u32 v8, v8, v31\n"
      "v_lshlrev_b32 v31, 1, v10\n"
      "v_add_u32 v31, 0x27600, v31\n"
      "v_and_b32 v255, 0x7ff, v255\n"
      "v_lshl_add_u32 v251, v255, 11, v10\n"
      "v_and_b32 v252, 32, v43\n"
      "v_lshl_or_b32 v251, v252, 25, v251\n"
      "ds_read_b64 v[254:255], v31 \n"
      "v_mov_b32 v252, 0\n"
      "v_mov_b32 v253, 0\n"
      "s_waitcnt lgkmcnt(0)\n"
      "s_branch .Lcho%=\n"
      ".LG%=:\n"
      "s_setprio 3\n"
      "s_mul_i32 s35, s48, 14\n"
      "s_add_u32 s35, s35, s49\n"
      "s_and_b32 s55, s33, 3\n"
      "s_mul_i32 s50, s55, 1835008\n"
      "s_lshr_b32 s51, s35, 1\n"
      "s_lshl_b32 s51, s51, 7\n"
      "s_add_u32 s50, s50, s51\n"
      "v_lshrrev_b32 v37, 3, v43\n"
      "v_and_b32 v38, 7, v43\n"
      "v_mul_u32_u24 v37, 0xe000, v37\n"
      "v_lshlrev_b32 v38, 4, v38\n"
      "v_add3_u32 v32, v37, v38, s50\n"
      "s_cmp_lt_u32 s34, 112\n"
      "s_cselect_b32 s26, 0x700000, 0\n"
      "s_mov_b32 s27, 0x20000\n"
      "s_bitcmp1_b32 s49, 0\n"
      "s_waitcnt lgkmcnt(0)\n"
      "s_load_dword s15, s[88:89], 0x0\n"
      "s_cselect_b32 s24, s82, s80\n"
      "s_cselect_b32 s25, s83, s81\n"
      "s_and_b32 s25, s25, 0xffff\n"
      "s_mov_b32 s16, s88\n"
      "s_and_b32 s17, s89, 0xffff\n"
      "s_mov_b32 s18, 0x6e0000\n"
      "s_mov_b32 s19, 0x20000\n"
      "s_mov_b32 s36, s86\n"
      "s_and_b32 s37, s87, 0xffff\n"
      "s_sleep 26\n"
      "s_mov_b32 s60, 0\n"
      "buffer_load_dwordx4 v[64:67], v32, s[24:27], s60 offen nt\n"
      "s_sleep 2\n"
      "s_mov_b32 s60, 7168\n"
      "buffer_load_dwordx4 v[68:71], v32, s[24:27], s60 offen nt\n"
      "s_sleep 2\n"
      "s_mov_b32 s60, 14336\n"
      "buffer_load_dwordx4 v[72:75], v32, s[24:27], s60 offen nt\n"
      "s_sleep 2\n"
      "s_mov_b32 s60, 21504\n"
      "buffer_load_dwordx4 v[76:79], v32, s[24:27], s60 offen nt\n"
      "s_sleep 2\n"
      "s_mov_b32 s60, 28672\n"
      "buffer_load_dwordx4 v[80:83], v32, s[24:27], s60 offen nt\n"
      "s_sleep 2\n"
      "s_mov_b32 s60, 35840\n"
      "buffer_load_dwordx4 v[84:87], v32, s[24:27], s60 offen nt\n"
      "s_sleep 2\n"
      "s_mov_b32 s60, 43008\n"
      "buffer_load_dwordx4 v[88:91], v32, s[24:27], s60 offen nt\n"
      "s_sleep 2\n"
      "s_mov_b32 s60, 50176\n"
      "buffer_load_dwordx4 v[92:95], v32, s[24:27], s60 offen nt\n"
      "s_sleep 2\n"
      "s_mov_b32 s60, 458752\n"
      "buffer_load_dwordx4 v[96:99], v32, s[24:27], s60 offen nt\n"
      "s_sleep 2\n"
      "s_waitcnt lgkmcnt(0)\n"
      "s_add_u32 s15, s15, 0x9e3779b1\n"
      "v_mov_b32 v47, s15\n"
      "s_cmp_ge_u32 s34, 112\n"
      "s_cbranch_scc1 .LT%=\n"
      "v_mov_b32 v48, 0x22680\n"
      "ds_read_b32 v48, v48 \n"
      "s_waitcnt lgkmcnt(0)\n"
      "v_readfirstlane_b32 s12, v48\n"
      "s_mov_b32 s62, 0x05040100\n"
      "s_mov_b32 s63, 0x07060302\n"
      "s_lshl_b32 s42, s34, 3\n"
      "s_add_u32 s42, s42, 2048\n"
      "s_lshl_b32 s43, s48, 8\n"
      "s_and_b32 s50, s49, 1\n"
      "s_lshl_b32 s50, s50, 7\n"
      "s_add_u32 s43, s43, s50\n"
      "s_add_u32 s43, s43, 65536\n"
      "s_mov_b32 s20, s64\n"
      "s_and_b32 s21, s65, 0xffff\n"
      "s_mov_b32 s22, 0x400000\n"
      "s_mov_b32 s23, 0x20000\n"
      "v_lshrrev_b32 v39, 4, v43\n"
      "v_and_b32 v38, 15, v43\n"
      "v_lshlrev_b32 v38, 4, v38\n"
      "v_bfe_u32 v33, v43, 2, 2\n"
      "v_and_b32 v37, 1, v43\n"
      "v_lshl_add_u32 v33, v33, 1, v37\n"
      "v_lshlrev_b32 v33, 3, v33\n"
      "v_and_b32 v37, 2, v43\n"
      "v_lshl_add_u32 v37, v39, 2, v37\n"
      "s_lshl_b32 s50, s55, 9\n"
      "v_lshl_add_u32 v36, v37, 3, s50\n"
      "s_lshl_b32 s51, s55, 4\n"
      "v_lshrrev_b32 v40, 5, v43\n"
      "v_lshl_add_u32 v40, v40, 2, s51\n"
      "s_lshl_b32 s51, s55, 2\n"
      "s_add_u32 s51, s51, 140800\n"
      "v_mov_b32 v41, s51\n"
      "v_mov_b32 v42, 0x22600\n"
      "v_lshl_add_u32 v37, v43, 3, s50\n"
      "v_add_u32 v35, 0x22700, v37\n"
      "s_lshl_b32 s50, s55, 11\n"
      "s_add_u32 s50, s50, 141056\n"
      "v_lshl_add_u32 v34, v39, 9, v38\n"
      "v_add_u32 v34, s50, v34\n"
      "s_lshr_b32 s45, s35, 1\n"
      "s_lshl_b32 s45, s45, 16\n"
      "s_and_b32 s50, s35, 1\n"
      "s_lshl_b32 s50, s50, 11\n"
      "s_add_u32 s45, s45, s50\n"
      "s_add_u32 s45, s45, 524288\n"
      "s_mov_b32 s56, 0x3f80\n"
      "s_mov_b32 s57, 0x3f800000\n"
      "s_movk_i32 s61, 0x7ff\n"
      "s_mov_b32 s58, 131072\n"
      "s_mov_b32 s54, 0\n"
      "s_mov_b64 s[66:67], 0\n"
      "s_movk_i32 s70, 427\n"
      "s_waitcnt vmcnt(8)\n"
      "s_nop 0\n"
      "s_mov_b32 s60, 465920\n"
      "buffer_load_dwordx4 v[100:103], v32, s[24:27], s60 offen nt\n"
      "s_waitcnt vmcnt(8)\n"
      "s_nop 0\n"
      "s_mov_b32 s60, 473088\n"
      "buffer_load_dwordx4 v[104:107], v32, s[24:27], s60 offen nt\n"
      "v_perm_b32 v192, v68, v64, s62\n"
      "v_perm_b32 v196, v68, v64, s63\n"
      "v_perm_b32 v200, v69, v65, s62\n"
      "v_perm_b32 v204, v69, v65, s63\n"
      "v_perm_b32 v208, v70, v66, s62\n"
      "v_perm_b32 v212, v70, v66, s63\n"
      "v_perm_b32 v216, v71, v67, s62\n"
      "v_perm_b32 v220, v71, v67, s63\n"
      "s_waitcnt vmcnt(8)\n"
      "s_nop 0\n"
      "s_mov_b32 s60, 480256\n"
      "buffer_load_dwordx4 v[108:111], v32, s[24:27], s60 offen nt\n"
      "s_waitcnt vmcnt(8)\n"
      "s_nop 0\n"
      "s_mov_b32 s60, 487424\n"
      "buffer_load_dwordx4 v[112:115], v32, s[24:27], s60 offen nt\n"
      "v_perm_b32 v193, v76, v72, s62\n"
      "v_perm_b32 v197, v76, v72, s63\n"
      "v_perm_b32 v201, v77, v73, s62\n"
      "v_perm_b32 v205, v77, v73, s63\n"
      "v_perm_b32 v209, v78, v74, s62\n"
      "v_perm_b32 v213, v78, v74, s63\n"
      "v_perm_b32 v217, v79, v75, s62\n"
      "v_perm_b32 v221, v79, v75, s63\n"
      "s_waitcnt vmcnt(8)\n"
      "s_nop 0\n"
      "s_mov_b32 s60, 494592\n"
      "buffer_load_dwordx4 v[116:119], v32, s[24:27], s60 offen nt\n"
      "s_waitcnt vmcnt(8)\n"
      "s_nop 0\n"
      "s_mov_b32 s60, 501760\n"
      "buffer_load_dwordx4 v[120:123], v32, s[24:27], s60 offen nt\n"
      "v_perm_b32 v194, v84, v80, s62\n"
      "v_perm_b32 v198, v84, v80, s63\n"
      "v_perm_b32 v202, v85, v81, s62\n"
      "v_perm_b32 v206, v85, v81, s63\n"
      "v_perm_b32 v210, v86, v82, s62\n"
      "v_perm_b32 v214, v86, v82, s63\n"
      "v_perm_b32 v218, v87, v83, s62\n"
      "v_perm_b32 v222, v87, v83, s63\n"
      "s_waitcnt vmcnt(8)\n"
      "s_nop 0\n"
      "s_mov_b32 s60, 508928\n"
      "buffer_load_dwordx4 v[124:127], v32, s[24:27], s60 offen nt\n"
      "s_waitcnt vmcnt(8)\n"
      "s_nop 0\n"
      "s_mov_b32 s60, 917504\n"
      "buffer_load_dwordx4 v[128:131], v32, s[24:27], s60 offen nt\n"
      "v_perm_b32 v195, v92, v88, s62\n"
      "v_perm_b32 v199, v92, v88, s63\n"
      "v_perm_b32 v203, v93, v89, s62\n"
      "v_perm_b32 v207, v93, v89, s63\n"
      "v_perm_b32 v211, v94, v90, s62\n"
      "v_perm_b32 v215, v94, v90, s63\n"
      "v_perm_b32 v219, v95, v91, s62\n"
      "v_perm_b32 v223, v95, v91, s63\n"
      "s_waitcnt vmcnt(8)\n"
      "s_nop 0\n"
      "s_mov_b32 s60, 924672\n"
      "buffer_load_dwordx4 v[132:135], v32, s[24:27], s60 offen nt\n"
      "s_waitcnt vmcnt(8)\n"
      "s_nop 0\n"
      "s_mov_b32 s60, 931840\n"
      "buffer_load_dwordx4 v[136:139], v32, s[24:27], s60 offen nt\n"
      "v_perm_b32 v64, v100, v96, s62\n"
      "v_perm_b32 v68, v100, v96, s63\n"
      "v_perm_b32 v72, v101, v97, s62\n"
      "v_perm_b32 v76, v101, v97, s63\n"
      "v_perm_b32 v80, v102, v98, s62\n"
      "v_perm_b32 v84, v102, v98, s63\n"
      "v_perm_b32 v88, v103, v99, s62\n"
      "v_perm_b32 v92, v103, v99, s63\n"
      "s_waitcnt vmcnt(8)\n"
      "s_nop 0\n"
      "s_mov_b32 s60, 939008\n"
      "buffer_load_dwordx4 v[140:143], v32, s[24:27], s60 offen nt\n"
      "s_waitcnt vmcnt(8)\n"
      "s_nop 0\n"
      "s_mov_b32 s60, 946176\n"
      "buffer_load_dwordx4 v[144:147], v32, s[24:27], s60 offen nt\n"
      "v_perm_b32 v65, v108, v104, s62\n"
      "v_perm_b32 v69, v108, v104, s63\n"
      "v_perm_b32 v73, v109, v105, s62\n"
      "v_perm_b32 v77, v109, v105, s63\n"
      "v_perm_b32 v81, v110, v106, s62\n"
      "v_perm_b32 v85, v110, v106, s63\n"
      "v_perm_b32 v89, v111, v107, s62\n"
      "v_perm_b32 v93, v111, v107, s63\n"
      "s_waitcnt vmcnt(8)\n"
      "s_nop 0\n"
      "s_mov_b32 s60, 953344\n"
      "buffer_load_dwordx4 v[148:151], v32, s[24:27], s60 offen nt\n"
      "s_waitcnt vmcnt(8)\n"
      "s_nop 0\n"
      "s_mov_b32 s60, 960512\n"
      "buffer_load_dwordx4 v[152:155], v32, s[24:27], s60 offen nt\n"
      "v_perm_b32 v66, v116, v112, s62\n"
      "v_perm_b32 v70, v116, v112, s63\n"
      "v_perm_b32 v74, v117, v113, s62\n"
      "v_perm_b32 v78, v117, v113, s63\n"
      "v_perm_b32 v82, v118, v114, s62\n"
      "v_perm_b32 v86, v118, v114, s63\n"
      "v_perm_b32 v90, v119, v115, s62\n"
      "v_perm_b32 v94, v119, v115, s63\n"
      "s_waitcnt vmcnt(8)\n"
      "s_nop 0\n"
      "s_mov_b32 s60, 967680\n"
      "buffer_load_dwordx4 v[156:159], v32, s[24:27], s60 offen nt\n"
      "s_waitcnt vmcnt(8)\n"
      "s_nop 0\n"
      "s_mov_b32 s60, 1376256\n"
      "buffer_load_dwordx4 v[160:163], v32, s[24:27], s60 offen nt\n"
      "v_perm_b32 v67, v124, v120, s62\n"
      "v_perm_b32 v71, v124, v120, s63\n"
      "v_perm_b32 v75, v125, v121, s62\n"
      "v_perm_b32 v79, v125, v121, s63\n"
      "v_perm_b32 v83, v126, v122, s62\n"
      "v_perm_b32 v87, v126, v122, s63\n"
      "v_perm_b32 v91, v127, v123, s62\n"
      "v_perm_b32 v95, v127, v123, s63\n"
      "s_waitcnt vmcnt(8)\n"
      "s_nop 0\n"
      "s_mov_b32 s60, 1383424\n"
      "buffer_load_dwordx4 v[164:167], v32, s[24:27], s60 offen nt\n"
      "s_waitcnt vmcnt(8)\n"
      "s_nop 0\n"
      "s_mov_b32 s60, 1390592\n"
      "buffer_load_dwordx4 v[168:171], v32, s[24:27], s60 offen nt\n"
      "v_perm_b32 v96, v132, v128, s62\n"
      "v_perm_b32 v100, v132, v128, s63\n"
      "v_perm_b32 v104, v133, v129, s62\n"
      "v_perm_b32 v108, v133, v129, s63\n"
      "v_perm_b32 v112, v134, v130, s62\n"
      "v_perm_b32 v116, v134, v130, s63\n"
      "v_perm_b32 v120, v135, v131, s62\n"
      "v_perm_b32 v124, v135, v131, s63\n"
      "s_waitcnt vmcnt(8)\n"
      "s_nop 0\n"
      "s_mov_b32 s60, 1397760\n"
      "buffer_load_dwordx4 v[172:175], v32, s[24:27], s60 offen nt\n"
      "s_waitcnt vmcnt(8)\n"
      "s_nop 0\n"
      "s_mov_b32 s60, 1404928\n"
      "buffer_load_dwordx4 v[176:179], v32, s[24:27], s60 offen nt\n"
      "v_perm_b32 v97, v140, v136, s62\n"
      "v_perm_b32 v101, v140, v136, s63\n"
      "v_perm_b32 v105, v141, v137, s62\n"
      "v_perm_b32 v109, v141, v137, s63\n"
      "v_perm_b32 v113, v142, v138, s62\n"
      "v_perm_b32 v117, v142, v138, s63\n"
      "v_perm_b32 v121, v143, v139, s62\n"
      "v_perm_b32 v125, v143, v139, s63\n"
      "s_waitcnt vmcnt(8)\n"
      "s_nop 0\n"
      "s_mov_b32 s60, 1412096\n"
      "buffer_load_dwordx4 v[180:183], v32, s[24:27], s60 offen nt\n"
      "s_waitcnt vmcnt(8)\n"
      "s_nop 0\n"
      "s_mov_b32 s60, 1419264\n"
      "buffer_load_dwordx4 v[184:187], v32, s[24:27], s60 offen nt\n"
      "v_perm_b32 v98, v148, v144, s62\n"
      "v_perm_b32 v102, v148, v144, s63\n"
      "v_perm_b32 v106, v149, v145, s62\n"
      "v_perm_b32 v110, v149, v145, s63\n"
      "v_perm_b32 v114, v150, v146, s62\n"
      "v_perm_b32 v118, v150, v146, s63\n"
      "v_perm_b32 v122, v151, v147, s62\n"
      "v_perm_b32 v126, v151, v147, s63\n"
      "s_waitcnt vmcnt(8)\n"
      "s_nop 0\n"
      "s_mov_b32 s60, 1426432\n"
      "buffer_load_dwordx4 v[188:191], v32, s[24:27], s60 offen nt\n"
      "ds_write_b32 v41, v47 offset:80\n"
      "s_waitcnt vmcnt(8)\n"
      "s_nop 0\n"
      "v_perm_b32 v99, v156, v152, s62\n"
      "v_perm_b32 v103, v156, v152, s63\n"
      "v_perm_b32 v107, v157, v153, s62\n"
      "v_perm_b32 v111, v157, v153, s63\n"
      "v_perm_b32 v115, v158, v154, s62\n"
      "v_perm_b32 v119, v158, v154, s63\n"
      "v_perm_b32 v123, v159, v155, s62\n"
      "v_perm_b32 v127, v159, v155, s63\n"
      "s_waitcnt vmcnt(7)\n"
      "s_nop 0\n"
      "s_memrealtime s[68:69]\n"
      "s_waitcnt vmcnt(6)\n"
      "s_nop 0\n"
      "s_waitcnt lgkmcnt(0)\n"
      "s_sub_u32 s68, s68, s12\n"
      "s_cmp_lt_u32 s68, s70\n"
      "s_cbranch_scc1 .Lgn25%=\n"
      "buffer_load_dwordx2 v[8:9], v33, s[16:19], s43 offen sc1\n"
      "s_mov_b32 s70, -1\n"
      ".Lgn25%=:\n"
      "s_memrealtime s[68:69]\n"
      "v_perm_b32 v128, v164, v160, s62\n"
      "v_perm_b32 v132, v164, v160, s63\n"
      "v_perm_b32 v136, v165, v161, s62\n"
      "v_perm_b32 v140, v165, v161, s63\n"
      "v_perm_b32 v144, v166, v162, s62\n"
      "v_perm_b32 v148, v166, v162, s63\n"
      "v_perm_b32 v152, v167, v163, s62\n"
      "v_perm_b32 v156, v167, v163, s63\n"
      "s_waitcnt vmcnt(5)\n"
      "s_nop 0\n"
      "s_waitcnt lgkmcnt(0)\n"
      "s_sub_u32 s68, s68, s12\n"
      "s_cmp_lt_u32 s68, s70\n"
      "s_cbranch_scc1 .Lgn26%=\n"
      "buffer_load_dwordx2 v[8:9], v33, s[16:19], s43 offen sc1\n"
      "s_mov_b32 s70, -1\n"
      ".Lgn26%=:\n"
      "s_memrealtime s[68:69]\n"
      "s_waitcnt vmcnt(4)\n"
      "s_nop 0\n"
      "s_waitcnt lgkmcnt(0)\n"
      "s_sub_u32 s68, s68, s12\n"
      "s_cmp_lt_u32 s68, s70\n"
      "s_cbranch_scc1 .Lgn27%=\n"
      "buffer_load_dwordx2 v[8:9], v33, s[16:19], s43 offen sc1\n"
      "s_mov_b32 s70, -1\n"
      ".Lgn27%=:\n"
      "s_memrealtime s[68:69]\n"
      "v_perm_b32 v129, v172, v168, s62\n"
      "v_perm_b32 v133, v172, v168, s63\n"
      "v_perm_b32 v137, v173, v169, s62\n"
      "v_perm_b32 v141, v173, v169, s63\n"
      "v_perm_b32 v145, v174, v170, s62\n"
      "v_perm_b32 v149, v174, v170, s63\n"
      "v_perm_b32 v153, v175, v171, s62\n"
      "v_perm_b32 v157, v175, v171, s63\n"
      "s_waitcnt vmcnt(3)\n"
      "s_nop 0\n"
      "s_waitcnt lgkmcnt(0)\n"
      "s_sub_u32 s68, s68, s12\n"
      "s_cmp_lt_u32 s68, s70\n"
      "s_cbranch_scc1 .Lgn28%=\n"
      "buffer_load_dwordx2 v[8:9], v33, s[16:19], s43 offen sc1\n"
      "s_mov_b32 s70, -1\n"
      ".Lgn28%=:\n"
      "s_memrealtime s[68:69]\n"
      "s_waitcnt vmcnt(2)\n"
      "s_nop 0\n"
      "s_waitcnt lgkmcnt(0)\n"
      "s_sub_u32 s68, s68, s12\n"
      "s_cmp_lt_u32 s68, s70\n"
      "s_cbranch_scc1 .Lgn29%=\n"
      "buffer_load_dwordx2 v[8:9], v33, s[16:19], s43 offen sc1\n"
      "s_mov_b32 s70, -1\n"
      ".Lgn29%=:\n"
      "s_memrealtime s[68:69]\n"
      "v_perm_b32 v130, v180, v176, s62\n"
      "v_perm_b32 v134, v180, v176, s63\n"
      "v_perm_b32 v138, v181, v177, s62\n"
      "v_perm_b32 v142, v181, v177, s63\n"
      "v_perm_b32 v146, v182, v178, s62\n"
      "v_perm_b32 v150, v182, v178, s63\n"
      "v_perm_b32 v154, v183, v179, s62\n"
      "v_perm_b32 v158, v183, v179, s63\n"
      "s_waitcnt vmcnt(1)\n"
      "s_nop 0\n"
      "s_waitcnt lgkmcnt(0)\n"
      "s_sub_u32 s68, s68, s12\n"
      "s_cmp_lt_u32 s68, s70\n"
      "s_cbranch_scc1 .Lgn30%=\n"
      "buffer_load_dwordx2 v[8:9], v33, s[16:19], s43 offen sc1\n"
      "s_mov_b32 s70, -1\n"
      ".Lgn30%=:\n"
      "s_memrealtime s[68:69]\n"
      "s_waitcnt vmcnt(0)\n"
      "s_nop 0\n"
      "s_waitcnt lgkmcnt(0)\n"
      "s_sub_u32 s68, s68, s12\n"
      "s_cmp_lt_u32 s68, s70\n"
      "s_cbranch_scc1 .Lgn31%=\n"
      "buffer_load_dwordx2 v[8:9], v33, s[16:19], s43 offen sc1\n"
      "s_mov_b32 s70, -1\n"
      ".Lgn31%=:\n"
      "v_perm_b32 v131, v188, v184, s62\n"
      "v_perm_b32 v135, v188, v184, s63\n"
      "v_perm_b32 v139, v189, v185, s62\n"
      "v_perm_b32 v143, v189, v185, s63\n"
      "v_perm_b32 v147, v190, v186, s62\n"
      "v_perm_b32 v151, v190, v186, s63\n"
      "v_perm_b32 v155, v191, v187, s62\n"
      "v_perm_b32 v159, v191, v187, s63\n"
      "s_movk_i32 s59, 0x3fff\n"
      "s_cmp_eq_u32 s70, -1\n"
      "s_cbranch_scc1 .Lghc%=\n"
      ".Lgpc3_%=:\n"
      "s_memrealtime s[68:69]\n"
      "s_waitcnt lgkmcnt(0)\n"
      "s_sub_u32 s68, s68, s12\n"
      "s_cmp_ge_u32 s68, 435\n"
      "s_cbranch_scc1 .Lgpd3_%=\n"
      "s_sleep 1\n"
      "s_branch .Lgpc3_%=\n"
      ".Lgpd3_%=:\n"
      ".Lgh%=:\n"
      "buffer_load_dwordx2 v[8:9], v33, s[16:19], s43 offen sc1\n"
      ".Lghc%=:\n"
      "s_waitcnt vmcnt(0)\n"
      "v_cmp_ne_u32 vcc, s15, v9\n"
      "v_bitop3_b32 v10, v8, s61, v8 bitop3:0x0c\n"
      "s_sub_u32 s59, s59, 1\n"
      "s_cbranch_vccz .Lgho%=\n"
      "s_sleep 2\n"
      "s_cbranch_scc0 .Lgh%=\n"
      ".Lgho%=:\n"
      "v_lshrrev_b32 v45, 9, v10\n"
      "v_lshl_add_u32 v11, v10, 11, v36\n"
      "v_mul_u32_u24 v45, 0xf000, v45\n"
      "v_lshl_add_u32 v45, v10, 3, v45\n"
      "buffer_load_dwordx2 v[12:13], off, s[16:19], s43 offset:64 sc1\n"
      "buffer_load_dwordx2 v[62:63], v45, s[16:19], s58 offen sc1\n"
      "buffer_load_dwordx4 v[16:19], v11, s[20:23], 0 offen offset:0\n"
      "buffer_load_dwordx4 v[20:23], v11, s[20:23], 0 offen offset:128\n"
      "buffer_load_dwordx4 v[24:27], v11, s[20:23], 0 offen offset:256\n"
      "buffer_load_dwordx4 v[28:31], v11, s[20:23], 0 offen offset:384\n"
      "ds_write_b32 v41, v47 offset:64\n"
      ".Lghx%=:\n"
      "s_movk_i32 s59, 0x7fff\n"
      "s_waitcnt vmcnt(4)\n"
      "s_nop 0\n"
      ".Lgs%=:\n"
      "v_cmp_ne_u32 vcc, s15, v63\n"
      "v_lshlrev_b32 v10, 16, v62\n"
      "v_and_b32 v44, 0xffff0000, v62\n"
      "s_cbranch_vccnz .Lgss%=\n"
      ".Lgso%=:\n"
      "v_rcp_f32 v44, v44\n"
      "v_xor_b32 v10, 0x80000000, v10\n"
      "v_readfirstlane_b32 s68, v13\n"
      "v_readfirstlane_b32 s69, v12\n"
      "s_cmp_eq_u32 s68, s15\n"
      "s_cselect_b32 s12, s69, s12\n"
      "v_mov_b32 v12, s12\n"
      "ds_write_b32 v42, v12 offset:128\n"
      "s_waitcnt vmcnt(3)\n"
      "v_dot2_f32_bf16 v168, v16, s56, v10\n"
      "v_dot2_f32_bf16 v169, v16, s57, v10\n"
      "v_dot2_f32_bf16 v170, v17, s56, v10\n"
      "v_dot2_f32_bf16 v171, v17, s57, v10\n"
      "v_dot2_f32_bf16 v172, v18, s56, v10\n"
      "v_dot2_f32_bf16 v173, v18, s57, v10\n"
      "v_dot2_f32_bf16 v174, v19, s56, v10\n"
      "v_dot2_f32_bf16 v175, v19, s57, v10\n"
      "v_cvt_pk_bf16_f32 v16, v168, v169\n"
      "v_cvt_pk_bf16_f32 v17, v170, v171\n"
      "v_cvt_pk_bf16_f32 v18, v172, v173\n"
      "v_cvt_pk_bf16_f32 v19, v174, v175\n"
      "ds_bpermute_b32 v44, v40, v44\n"
      "s_waitcnt vmcnt(2)\n"
      "v_dot2_f32_bf16 v176, v20, s56, v10\n"
      "v_dot2_f32_bf16 v177, v20, s57, v10\n"
      "v_dot2_f32_bf16 v178, v21, s56, v10\n"
      "v_dot2_f32_bf16 v179, v21, s57, v10\n"
      "v_dot2_f32_bf16 v180, v22, s56, v10\n"
      "v_dot2_f32_bf16 v181, v22, s57, v10\n"
      "v_dot2_f32_bf16 v182, v23, s56, v10\n"
      "v_dot2_f32_bf16 v183, v23, s57, v10\n"
      "v_cvt_pk_bf16_f32 v20, v176, v177\n"
      "v_cvt_pk_bf16_f32 v21, v178, v179\n"
      "v_cvt_pk_bf16_f32 v22, v180, v181\n"
      "v_cvt_pk_bf16_f32 v23, v182, v183\n"
      "s_waitcnt vmcnt(1)\n"
      "v_dot2_f32_bf16 v168, v24, s56, v10\n"
      "v_dot2_f32_bf16 v169, v24, s57, v10\n"
      "v_dot2_f32_bf16 v170, v25, s56, v10\n"
      "v_dot2_f32_bf16 v171, v25, s57, v10\n"
      "v_dot2_f32_bf16 v172, v26, s56, v10\n"
      "v_dot2_f32_bf16 v173, v26, s57, v10\n"
      "v_dot2_f32_bf16 v174, v27, s56, v10\n"
      "v_dot2_f32_bf16 v175, v27, s57, v10\n"
      "v_cvt_pk_bf16_f32 v24, v168, v169\n"
      "v_cvt_pk_bf16_f32 v25, v170, v171\n"
      "v_cvt_pk_bf16_f32 v26, v172, v173\n"
      "v_cvt_pk_bf16_f32 v27, v174, v175\n"
      "s_waitcnt vmcnt(0)\n"
      "v_dot2_f32_bf16 v176, v28, s56, v10\n"
      "v_dot2_f32_bf16 v177, v28, s57, v10\n"
      "v_dot2_f32_bf16 v178, v29, s56, v10\n"
      "v_dot2_f32_bf16 v179, v29, s57, v10\n"
      "v_dot2_f32_bf16 v180, v30, s56, v10\n"
      "v_dot2_f32_bf16 v181, v30, s57, v10\n"
      "v_dot2_f32_bf16 v182, v31, s56, v10\n"
      "v_dot2_f32_bf16 v183, v31, s57, v10\n"
      "v_cvt_pk_bf16_f32 v28, v176, v177\n"
      "v_cvt_pk_bf16_f32 v29, v178, v179\n"
      "v_cvt_pk_bf16_f32 v30, v180, v181\n"
      "v_cvt_pk_bf16_f32 v31, v182, v183\n"
      "v_mfma_f32_16x16x32_bf16 v[224:227], v[16:19], v[192:195], 0\n"
      "v_mfma_f32_16x16x32_bf16 v[228:231], v[16:19], v[196:199], 0\n"
      "v_mfma_f32_16x16x32_bf16 v[232:235], v[16:19], v[200:203], 0\n"
      "v_mfma_f32_16x16x32_bf16 v[236:239], v[16:19], v[204:207], 0\n"
      "v_mfma_f32_16x16x32_bf16 v[240:243], v[16:19], v[208:211], 0\n"
      "v_mfma_f32_16x16x32_bf16 v[244:247], v[16:19], v[212:215], 0\n"
      "v_mfma_f32_16x16x32_bf16 v[248:251], v[16:19], v[216:219], 0\n"
      "v_mfma_f32_16x16x32_bf16 v[252:255], v[16:19], v[220:223], 0\n"
      "v_mfma_f32_16x16x32_bf16 v[224:227], v[20:23], v[64:67], v[224:227]\n"
      "v_mfma_f32_16x16x32_bf16 v[228:231], v[20:23], v[68:71], v[228:231]\n"
      "v_mfma_f32_16x16x32_bf16 v[232:235], v[20:23], v[72:75], v[232:235]\n"
      "v_mfma_f32_16x16x32_bf16 v[236:239], v[20:23], v[76:79], v[236:239]\n"
      "v_mfma_f32_16x16x32_bf16 v[240:243], v[20:23], v[80:83], v[240:243]\n"
      "v_mfma_f32_16x16x32_bf16 v[244:247], v[20:23], v[84:87], v[244:247]\n"
      "v_mfma_f32_16x16x32_bf16 v[248:251], v[20:23], v[88:91], v[248:251]\n"
      "v_mfma_f32_16x16x32_bf16 v[252:255], v[20:23], v[92:95], v[252:255]\n"
      "v_mfma_f32_16x16x32_bf16 v[224:227], v[24:27], v[96:99], v[224:227]\n"
      "v_mfma_f32_16x16x32_bf16 v[228:231], v[24:27], v[100:103], v[228:231]\n"
      "v_mfma_f32_16x16x32_bf16 v[232:235], v[24:27], v[104:107], v[232:235]\n"
      "v_mfma_f32_16x16x32_bf16 v[236:239], v[24:27], v[108:111], v[236:239]\n"
      "v_mfma_f32_16x16x32_bf16 v[240:243], v[24:27], v[112:115], v[240:243]\n"
      "v_mfma_f32_16x16x32_bf16 v[244:247], v[24:27], v[116:119], v[244:247]\n"
      "v_mfma_f32_16x16x32_bf16 v[248:251], v[24:27], v[120:123], v[248:251]\n"
      "v_mfma_f32_16x16x32_bf16 v[252:255], v[24:27], v[124:127], v[252:255]\n"
      "v_mfma_f32_16x16x32_bf16 v[224:227], v[28:31], v[128:131], v[224:227]\n"
      "v_mfma_f32_16x16x32_bf16 v[228:231], v[28:31], v[132:135], v[228:231]\n"
      "v_mfma_f32_16x16x32_bf16 v[232:235], v[28:31], v[136:139], v[232:235]\n"
      "v_mfma_f32_16x16x32_bf16 v[236:239], v[28:31], v[140:143], v[236:239]\n"
      "s_nop 4\n"
      "v_add_f32_dpp v160, v226, v224 row_ror:8 row_mask:0xf bank_mask:0x3\n"
      "v_add_f32_dpp v164, v227, v225 row_ror:8 row_mask:0xf bank_mask:0x3\n"
      "v_mfma_f32_16x16x32_bf16 v[240:243], v[28:31], v[144:147], v[240:243]\n"
      "v_add_f32_dpp v161, v230, v228 row_ror:8 row_mask:0xf bank_mask:0x3\n"
      "v_add_f32_dpp v165, v231, v229 row_ror:8 row_mask:0xf bank_mask:0x3\n"
      "v_mfma_f32_16x16x32_bf16 v[244:247], v[28:31], v[148:151], v[244:247]\n"
      "v_add_f32_dpp v162, v234, v232 row_ror:8 row_mask:0xf bank_mask:0x3\n"
      "v_add_f32_dpp v166, v235, v233 row_ror:8 row_mask:0xf bank_mask:0x3\n"
      "v_mfma_f32_16x16x32_bf16 v[248:251], v[28:31], v[152:155], v[248:251]\n"
      "v_add_f32_dpp v163, v238, v236 row_ror:8 row_mask:0xf bank_mask:0x3\n"
      "v_add_f32_dpp v167, v239, v237 row_ror:8 row_mask:0xf bank_mask:0x3\n"
      "v_mfma_f32_16x16x32_bf16 v[252:255], v[28:31], v[156:159], v[252:255]\n"
      "v_add_f32_dpp v160, v240, v242 row_ror:8 row_mask:0xf bank_mask:0xc\n"
      "v_add_f32_dpp v164, v241, v243 row_ror:8 row_mask:0xf bank_mask:0xc\n"
      "v_add_f32_dpp v161, v244, v246 row_ror:8 row_mask:0xf bank_mask:0xc\n"
      "v_add_f32_dpp v165, v245, v247 row_ror:8 row_mask:0xf bank_mask:0xc\n"
      "s_nop 0\n"
      "v_add_f32_dpp v162, v248, v250 row_ror:8 row_mask:0xf bank_mask:0xc\n"
      "v_add_f32_dpp v166, v249, v251 row_ror:8 row_mask:0xf bank_mask:0xc\n"
      "s_nop 0\n"
      "v_add_f32_dpp v163, v252, v254 row_ror:8 row_mask:0xf bank_mask:0xc\n"
      "v_add_f32_dpp v167, v253, v255 row_ror:8 row_mask:0xf bank_mask:0xc\n"
      "ds_write_b128 v34, v[160:163] \n"
      "ds_write_b128 v34, v[164:167] offset:256\n"
      "ds_write_b32 v41, v47 \n"
      "s_movk_i32 s59, 0x7fff\n"
      ".Lgw%=:\n"
      "ds_read_b128 v[72:75], v42 \n"
      "ds_read_b64 v[64:65], v35 offset:0\n"
      "ds_read_b64 v[66:67], v35 offset:2048\n"
      "ds_read_b64 v[68:69], v35 offset:4096\n"
      "ds_read_b64 v[70:71], v35 offset:6144\n"
      "s_sub_u32 s59, s59, 1\n"
      "s_cbranch_scc1 .Lgwo%=\n"
      "s_waitcnt lgkmcnt(4)\n"
      "v_xor_b32 v72, s15, v72\n"
      "v_bitop3_b32 v72, s15, v73, v72 bitop3:0xbe\n"
      "v_bitop3_b32 v72, s15, v74, v72 bitop3:0xbe\n"
      "v_bitop3_b32 v72, s15, v75, v72 bitop3:0xbe\n"
      "v_cmp_ne_u32 vcc, 0, v72\n"
      "s_waitcnt lgkmcnt(0)\n"
      "s_nop 0\n"
      "s_cbranch_vccnz .Lgw%=\n"
      ".Lgwo%=:\n"
      "s_waitcnt lgkmcnt(0)\n"
      "s_bitcmp1_b32 s49, 0\n"
      "v_pk_add_f32 v[64:65], v[64:65], v[66:67]\n"
      "v_pk_add_f32 v[68:69], v[68:69], v[70:71]\n"
      "v_pk_add_f32 v[64:65], v[64:65], v[68:69]\n"
      "v_mul_f32 v64, v64, v44\n"
      "v_mul_f32 v65, v65, v44\n"
      "v_cvt_pk_bf16_f32 v46, v64, v65\n"
      "s_cbranch_scc1 .Lgu%=\n"
      "v_lshlrev_b32 v64, 16, v46\n"
      "v_and_b32 v65, 0xffff0000, v46\n"
      "v_mul_f32 v66, 0xbfb8aa3b, v64\n"
      "v_mul_f32 v67, 0xbfb8aa3b, v65\n"
      "v_exp_f32 v66, v66\n"
      "v_exp_f32 v67, v67\n"
      "v_add_f32 v66, 1.0, v66\n"
      "v_add_f32 v67, 1.0, v67\n"
      "v_rcp_f32 v66, v66\n"
      "v_rcp_f32 v67, v67\n"
      "v_mul_f32 v64, v64, v66\n"
      "v_mul_f32 v65, v65, v67\n"
      "v_cvt_pk_bf16_f32 v46, v64, v65\n"
      ".Lgu%=:\n"
      "buffer_store_dwordx2 v[46:47], v37, s[16:19], s45 offen sc1\n"
      "s_cmp_lg_u32 s33, 4\n"
      "s_cbranch_scc1 .Lga%=\n"
      "v_mov_b32 v64, s15\n"
      "v_mov_b32 v65, s15\n"
      "buffer_store_dwordx2 v[64:65], off, s[16:19], s42 sc1\n"
      ".Lga%=:\n"
      "s_cmp_eq_u32 s33, 7\n"
      "s_mov_b32 s24, s36\n"
      "s_mov_b32 s25, s37\n"
      "s_mov_b32 s26, 0x400000\n"
      "s_cbranch_scc1 .LZ%=\n"
      "s_endpgm\n"
      ".Lgss%=:\n"
      "s_sleep 1\n"
      "s_sub_u32 s59, s59, 1\n"
      "s_cbranch_scc1 .Lgso%=\n"
      "buffer_load_dwordx2 v[62:63], v45, s[16:19], s58 offen sc1\n"
      "s_waitcnt vmcnt(0)\n"
      "s_branch .Lgs%=\n"
      ".LD2%=:\n"
      "s_setprio 0\n"
      "s_mul_i32 s35, s48, 14\n"
      "s_add_u32 s35, s35, s49\n"
      "s_lshr_b32 s52, s35, 2\n"
      "s_and_b32 s54, s35, 3\n"
      "v_lshlrev_b32 v42, 4, v49\n"
      "s_lshl_b32 s50, s52, 18\n"
      "s_lshl_b32 s51, s54, 9\n"
      "s_add_u32 s50, s50, s51\n"
      "s_lshl_b32 s51, s33, 7\n"
      "s_add_u32 s50, s50, s51\n"
      "v_lshl_add_u32 v32, v50, 14, v42\n"
      "v_add_u32 v32, s50, v32\n"
      "v_lshrrev_b32 v41, 1, v49\n"
      "v_and_b32 v43, 1, v41\n"
      "v_mul_u32_u24 v36, 0x120, v41\n"
      "v_lshlrev_b32 v43, 4, v43\n"
      "v_sub_u32 v36, v36, v43\n"
      "v_and_b32 v43, 1, v49\n"
      "v_lshl_add_u32 v36, v43, 7, v36\n"
      "v_lshrrev_b32 v41, 4, v48\n"
      "v_lshl_add_u32 v36, v41, 4, v36\n"
      "v_and_b32 v43, 8, v48\n"
      "v_add_u32 v36, v36, v43\n"
      "s_lshr_b32 s53, s33, 1\n"
      "s_mul_i32 s50, s53, 1152\n"
      "s_and_b32 s55, s33, 1\n"
      "s_lshl_b32 s51, s55, 6\n"
      "s_add_u32 s50, s50, s51\n"
      "s_add_u32 s50, s50, 149504\n"
      "v_add_u32 v36, s50, v36\n"
      "v_and_b32 v43, 1, v41\n"
      "v_mul_u32_u24 v37, 0x120, v41\n"
      "v_lshlrev_b32 v43, 4, v43\n"
      "v_sub_u32 v37, v37, v43\n"
      "v_and_b32 v43, 15, v48\n"
      "v_lshl_add_u32 v37, v43, 4, v37\n"
      "v_add_u32 v37, 0x24800, v37\n"
      "v_lshlrev_b32 v35, 4, v48\n"
      "s_lshl_b32 s46, s52, 17\n"
      "s_lshl_b32 s50, s53, 16\n"
      "s_add_u32 s46, s46, s50\n"
      "s_lshl_b32 s50, s55, 10\n"
      "s_add_u32 s46, s46, s50\n"
      "s_add_u32 s46, s46, 524288\n"
      "v_and_b32 v41, 3, v50\n"
      "v_lshrrev_b32 v43, 5, v48\n"
      "v_lshl_add_u32 v41, v41, 1, v43\n"
      "v_mul_u32_u24 v38, 0x1c00, v41\n"
      "v_add_u32 v38, v38, v42\n"
      "s_lshl_b32 s50, s54, 2\n"
      "s_add_u32 s50, s50, s33\n"
      "s_mul_i32 s47, s50, 57344\n"
      "s_lshl_b32 s50, s52, 8\n"
      "s_add_u32 s47, s47, s50\n"
      "s_add_u32 s47, s47, 4194304\n"
      "s_mov_b32 s62, 0x05040100\n"
      "s_mov_b32 s63, 0x07060302\n"
      "s_lshl_b32 s50, s33, 2\n"
      "s_add_u32 s50, s50, 140864\n"
      "v_mov_b32 v41, s50\n"
      "v_mov_b32 v42, 0x22640\n"
      "s_movk_i32 s59, 0x3fff\n"
      ".Ldf%=:\n"
      "s_sleep 1\n"
      "ds_read_b128 v[240:243], v42\n"
      "ds_read_b32 v43, v41 offset:16\n"
      "s_sub_u32 s59, s59, 1\n"
      "s_cbranch_scc1 .Ldfo%=\n"
      "s_waitcnt lgkmcnt(0)\n"
      "v_xor_b32 v240, s15, v240\n"
      "v_bitop3_b32 v240, s15, v241, v240 bitop3:0xbe\n"
      "v_bitop3_b32 v240, s15, v242, v240 bitop3:0xbe\n"
      "v_bitop3_b32 v240, s15, v243, v240 bitop3:0xbe\n"
      "v_bitop3_b32 v240, s15, v43, v240 bitop3:0xbe\n"
      "v_cmp_ne_u32 vcc, 0, v240\n"
      "s_nop 1\n"
      "s_cbranch_vccnz .Ldf%=\n"
      ".Ldfo%=:\n"
      "s_mov_b32 s60, 0\n"
      "buffer_load_dwordx4 v[128:131], v32, s[28:31], s60 offen nt\n"
      "s_mov_b32 s60, 2048\n"
      "buffer_load_dwordx4 v[132:135], v32, s[28:31], s60 offen nt\n"
      "s_mov_b32 s60, 4096\n"
      "buffer_load_dwordx4 v[136:139], v32, s[28:31], s60 offen nt\n"
      "s_mov_b32 s60, 6144\n"
      "buffer_load_dwordx4 v[140:143], v32, s[28:31], s60 offen nt\n"
      "s_mov_b32 s60, 8192\n"
      "buffer_load_dwordx4 v[144:147], v32, s[28:31], s60 offen nt\n"
      "s_mov_b32 s60, 10240\n"
      "buffer_load_dwordx4 v[148:151], v32, s[28:31], s60 offen nt\n"
      "s_mov_b32 s60, 12288\n"
      "buffer_load_dwordx4 v[152:155], v32, s[28:31], s60 offen nt\n"
      "s_mov_b32 s60, 14336\n"
      "buffer_load_dwordx4 v[156:159], v32, s[28:31], s60 offen nt\n"
      "s_waitcnt vmcnt(7)\n"
      "s_nop 0\n"
      "s_mov_b32 s60, 131072\n"
      "buffer_load_dwordx4 v[160:163], v32, s[28:31], s60 offen nt\n"
      "s_waitcnt vmcnt(7)\n"
      "s_nop 0\n"
      "s_mov_b32 s60, 133120\n"
      "buffer_load_dwordx4 v[164:167], v32, s[28:31], s60 offen nt\n"
      "v_perm_b32 v64, v132, v128, s62\n"
      "v_perm_b32 v68, v132, v128, s63\n"
      "v_perm_b32 v72, v133, v129, s62\n"
      "v_perm_b32 v76, v133, v129, s63\n"
      "v_perm_b32 v80, v134, v130, s62\n"
      "v_perm_b32 v84, v134, v130, s63\n"
      "v_perm_b32 v88, v135, v131, s62\n"
      "v_perm_b32 v92, v135, v131, s63\n"
      "s_waitcnt vmcnt(7)\n"
      "s_nop 0\n"
      "s_mov_b32 s60, 135168\n"
      "buffer_load_dwordx4 v[168:171], v32, s[28:31], s60 offen nt\n"
      "s_waitcnt vmcnt(7)\n"
      "s_nop 0\n"
      "s_mov_b32 s60, 137216\n"
      "buffer_load_dwordx4 v[172:175], v32, s[28:31], s60 offen nt\n"
      "v_perm_b32 v65, v140, v136, s62\n"
      "v_perm_b32 v69, v140, v136, s63\n"
      "v_perm_b32 v73, v141, v137, s62\n"
      "v_perm_b32 v77, v141, v137, s63\n"
      "v_perm_b32 v81, v142, v138, s62\n"
      "v_perm_b32 v85, v142, v138, s63\n"
      "v_perm_b32 v89, v143, v139, s62\n"
      "v_perm_b32 v93, v143, v139, s63\n"
      "s_waitcnt vmcnt(7)\n"
      "s_nop 0\n"
      "s_mov_b32 s60, 139264\n"
      "buffer_load_dwordx4 v[176:179], v32, s[28:31], s60 offen nt\n"
      "s_waitcnt vmcnt(7)\n"
      "s_nop 0\n"
      "s_mov_b32 s60, 141312\n"
      "buffer_load_dwordx4 v[180:183], v32, s[28:31], s60 offen nt\n"
      "v_perm_b32 v66, v148, v144, s62\n"
      "v_perm_b32 v70, v148, v144, s63\n"
      "v_perm_b32 v74, v149, v145, s62\n"
      "v_perm_b32 v78, v149, v145, s63\n"
      "v_perm_b32 v82, v150, v146, s62\n"
      "v_perm_b32 v86, v150, v146, s63\n"
      "v_perm_b32 v90, v151, v147, s62\n"
      "v_perm_b32 v94, v151, v147, s63\n"
      "s_waitcnt vmcnt(7)\n"
      "s_nop 0\n"
      "s_mov_b32 s60, 143360\n"
      "buffer_load_dwordx4 v[184:187], v32, s[28:31], s60 offen nt\n"
      "s_waitcnt vmcnt(7)\n"
      "s_nop 0\n"
      "s_mov_b32 s60, 145408\n"
      "buffer_load_dwordx4 v[188:191], v32, s[28:31], s60 offen nt\n"
      "v_perm_b32 v67, v156, v152, s62\n"
      "v_perm_b32 v71, v156, v152, s63\n"
      "v_perm_b32 v75, v157, v153, s62\n"
      "v_perm_b32 v79, v157, v153, s63\n"
      "v_perm_b32 v83, v158, v154, s62\n"
      "v_perm_b32 v87, v158, v154, s63\n"
      "v_perm_b32 v91, v159, v155, s62\n"
      "v_perm_b32 v95, v159, v155, s63\n"
      "s_waitcnt vmcnt(7)\n"
      "s_nop 0\n"
      "s_movk_i32 s59, 0x3fff\n"
      "v_add_u32 v41, 0xffffffc0, v41\n"
      ".Lde%=:\n"
      "ds_read_b32 v43, v41\n"
      "s_sub_u32 s59, s59, 1\n"
      "s_cbranch_scc1 .Ldeo%=\n"
      "s_waitcnt lgkmcnt(0)\n"
      "v_cmp_ne_u32 vcc, s15, v43\n"
      "s_nop 1\n"
      "s_cbranch_vccz .Ldeo%=\n"
      "s_sleep 2\n"
      "s_branch .Lde%=\n"
      ".Ldeo%=:\n"
      "s_cmp_lg_u32 s59, 0x3ffe\n"
      "s_cbranch_scc0 .Ldep%=\n"
      "s_sleep 6\n"
      ".Ldep%=:\n"
      "ds_read_b32 v43, v42 offset:64\n"
      "s_waitcnt lgkmcnt(0)\n"
      "v_readfirstlane_b32 s12, v43\n"
      "s_nop 0\n"
      ".Ldpc1_%=:\n"
      "s_memrealtime s[64:65]\n"
      "s_waitcnt lgkmcnt(0)\n"
      "s_sub_u32 s64, s64, s12\n"
      "s_cmp_ge_u32 s64, 640\n"
      "s_cbranch_scc1 .Ldpd1_%=\n"
      "s_sleep 1\n"
      "s_branch .Ldpc1_%=\n"
      ".Ldpd1_%=:\n"
      "buffer_load_dwordx4 v[224:227], v35, s[16:19], s46 offen sc1\n"
      "buffer_load_dwordx4 v[228:231], v35, s[16:19], s46 offen offset:2048 sc1\n"
      "s_waitcnt vmcnt(8)\n"
      "s_nop 0\n"
      "v_perm_b32 v96, v164, v160, s62\n"
      "v_perm_b32 v100, v164, v160, s63\n"
      "v_perm_b32 v104, v165, v161, s62\n"
      "v_perm_b32 v108, v165, v161, s63\n"
      "v_perm_b32 v112, v166, v162, s62\n"
      "v_perm_b32 v116, v166, v162, s63\n"
      "v_perm_b32 v120, v167, v163, s62\n"
      "v_perm_b32 v124, v167, v163, s63\n"
      "s_waitcnt vmcnt(7)\n"
      "s_nop 0\n"
      "s_waitcnt vmcnt(6)\n"
      "s_nop 0\n"
      "v_perm_b32 v97, v172, v168, s62\n"
      "v_perm_b32 v101, v172, v168, s63\n"
      "v_perm_b32 v105, v173, v169, s62\n"
      "v_perm_b32 v109, v173, v169, s63\n"
      "v_perm_b32 v113, v174, v170, s62\n"
      "v_perm_b32 v117, v174, v170, s63\n"
      "v_perm_b32 v121, v175, v171, s62\n"
      "v_perm_b32 v125, v175, v171, s63\n"
      "s_waitcnt vmcnt(5)\n"
      "s_nop 0\n"
      "s_waitcnt vmcnt(4)\n"
      "s_nop 0\n"
      "v_perm_b32 v98, v180, v176, s62\n"
      "v_perm_b32 v102, v180, v176, s63\n"
      "v_perm_b32 v106, v181, v177, s62\n"
      "v_perm_b32 v110, v181, v177, s63\n"
      "v_perm_b32 v114, v182, v178, s62\n"
      "v_perm_b32 v118, v182, v178, s63\n"
      "v_perm_b32 v122, v183, v179, s62\n"
      "v_perm_b32 v126, v183, v179, s63\n"
      "s_waitcnt vmcnt(3)\n"
      "s_nop 0\n"
      "s_waitcnt vmcnt(2)\n"
      "s_nop 0\n"
      "v_perm_b32 v99, v188, v184, s62\n"
      "v_perm_b32 v103, v188, v184, s63\n"
      "v_perm_b32 v107, v189, v185, s62\n"
      "v_perm_b32 v111, v189, v185, s63\n"
      "v_perm_b32 v115, v190, v186, s62\n"
      "v_perm_b32 v119, v190, v186, s63\n"
      "v_perm_b32 v123, v191, v187, s62\n"
      "v_perm_b32 v127, v191, v187, s63\n"
      "v_mov_b32 v137, s15\n"
      "v_mov_b32 v139, s15\n"
      "v_mov_b32 v141, s15\n"
      "v_mov_b32 v143, s15\n"
      "s_movk_i32 s59, 0x7fff\n"
      "s_waitcnt vmcnt(0)\n"
      "s_nop 0\n"
      ".Ldg%=:\n"
      "v_xor_b32 v232, s15, v225\n"
      "v_bitop3_b32 v232, s15, v227, v232 bitop3:0xbe\n"
      "v_bitop3_b32 v232, s15, v229, v232 bitop3:0xbe\n"
      "v_bitop3_b32 v232, s15, v231, v232 bitop3:0xbe\n"
      "v_cmp_ne_u32 vcc, 0, v232\n"
      "v_lshlrev_b32 v232, 16, v224\n"
      "v_and_b32 v233, 0xffff0000, v224\n"
      "v_lshlrev_b32 v234, 16, v226\n"
      "v_and_b32 v235, 0xffff0000, v226\n"
      "s_cbranch_vccnz .Ldgs%=\n"
      ".Ldgo%=:\n"
      "v_lshlrev_b32 v236, 16, v228\n"
      "v_and_b32 v237, 0xffff0000, v228\n"
      "v_lshlrev_b32 v238, 16, v230\n"
      "v_and_b32 v239, 0xffff0000, v230\n"
      "v_pk_mul_f32 v[232:233], v[232:233], v[236:237]\n"
      "v_pk_mul_f32 v[234:235], v[234:235], v[238:239]\n"
      "v_cvt_pk_bf16_f32 v232, v232, v233\n"
      "v_cvt_pk_bf16_f32 v233, v234, v235\n"
      "ds_write_b64 v36, v[232:233] \n"
      "ds_write_b32 v41, v137 offset:96\n"
      "ds_read_b128 v[240:243], v42 offset:32\n"
      "ds_read_b128 v[224:227], v37 \n"
      "ds_read_b128 v[228:231], v37 offset:1152\n"
      "s_movk_i32 s59, 0x7fff\n"
      ".Ldv%=:\n"
      "s_waitcnt lgkmcnt(2)\n"
      "v_xor_b32 v240, s15, v240\n"
      "v_bitop3_b32 v240, s15, v241, v240 bitop3:0xbe\n"
      "v_bitop3_b32 v240, s15, v242, v240 bitop3:0xbe\n"
      "v_bitop3_b32 v240, s15, v243, v240 bitop3:0xbe\n"
      "v_cmp_ne_u32 vcc, 0, v240\n"
      "s_nop 1\n"
      "s_cbranch_vccnz .Ldvs%=\n"
      ".Ldvo%=:\n"
      "s_waitcnt lgkmcnt(1)\n"
      "v_mfma_f32_16x16x32_bf16 v[192:195], v[64:67], v[224:227], 0\n"
      "s_waitcnt lgkmcnt(0)\n"
      "v_mfma_f32_16x16x32_bf16 v[192:195], v[96:99], v[228:231], v[192:195]\n"
      "v_mfma_f32_16x16x32_bf16 v[196:199], v[68:71], v[224:227], 0\n"
      "v_mfma_f32_16x16x32_bf16 v[196:199], v[100:103], v[228:231], v[196:199]\n"
      "v_mfma_f32_16x16x32_bf16 v[200:203], v[72:75], v[224:227], 0\n"
      "s_nop 4\n"
      "v_permlane32_swap_b32 v192, v193\n"
      "v_permlane32_swap_b32 v194, v195\n"
      "v_mfma_f32_16x16x32_bf16 v[200:203], v[104:107], v[228:231], v[200:203]\n"
      "v_mfma_f32_16x16x32_bf16 v[204:207], v[76:79], v[224:227], 0\n"
      "v_permlane32_swap_b32 v196, v197\n"
      "v_permlane32_swap_b32 v198, v199\n"
      "v_mfma_f32_16x16x32_bf16 v[204:207], v[108:111], v[228:231], v[204:207]\n"
      "v_add_f32_dpp v128, v193, v192 row_ror:8 row_mask:0xf bank_mask:0x3\n"
      "s_nop 1\n"
      "v_add_f32_dpp v128, v194, v195 row_ror:8 row_mask:0xf bank_mask:0xc\n"
      "v_mfma_f32_16x16x32_bf16 v[208:211], v[80:83], v[224:227], 0\n"
      "v_permlane32_swap_b32 v200, v201\n"
      "v_permlane32_swap_b32 v202, v203\n"
      "v_mfma_f32_16x16x32_bf16 v[208:211], v[112:115], v[228:231], v[208:211]\n"
      "v_add_f32_dpp v129, v197, v196 row_ror:8 row_mask:0xf bank_mask:0x3\n"
      "s_nop 1\n"
      "v_add_f32_dpp v129, v198, v199 row_ror:8 row_mask:0xf bank_mask:0xc\n"
      "v_mfma_f32_16x16x32_bf16 v[212:215], v[84:87], v[224:227], 0\n"
      "v_permlane32_swap_b32 v204, v205\n"
      "v_permlane32_swap_b32 v206, v207\n"
      "v_mfma_f32_16x16x32_bf16 v[212:215], v[116:119], v[228:231], v[212:215]\n"
      "v_add_f32_dpp v130, v201, v200 row_ror:8 row_mask:0xf bank_mask:0x3\n"
      "s_nop 1\n"
      "v_add_f32_dpp v130, v202, v203 row_ror:8 row_mask:0xf bank_mask:0xc\n"
      "v_mfma_f32_16x16x32_bf16 v[216:219], v[88:91], v[224:227], 0\n"
      "v_permlane32_swap_b32 v208, v209\n"
      "v_permlane32_swap_b32 v210, v211\n"
      "v_mfma_f32_16x16x32_bf16 v[216:219], v[120:123], v[228:231], v[216:219]\n"
      "v_add_f32_dpp v131, v205, v204 row_ror:8 row_mask:0xf bank_mask:0x3\n"
      "s_nop 1\n"
      "v_add_f32_dpp v131, v206, v207 row_ror:8 row_mask:0xf bank_mask:0xc\n"
      "v_mfma_f32_16x16x32_bf16 v[220:223], v[92:95], v[224:227], 0\n"
      "v_permlane32_swap_b32 v212, v213\n"
      "v_permlane32_swap_b32 v214, v215\n"
      "v_mfma_f32_16x16x32_bf16 v[220:223], v[124:127], v[228:231], v[220:223]\n"
      "v_add_f32_dpp v132, v209, v208 row_ror:8 row_mask:0xf bank_mask:0x3\n"
      "s_nop 1\n"
      "v_add_f32_dpp v132, v210, v211 row_ror:8 row_mask:0xf bank_mask:0xc\n"
      "v_cvt_pk_bf16_f32 v136, v128, v129\n"
      "v_cvt_pk_bf16_f32 v138, v130, v131\n"
      "buffer_store_dwordx4 v[136:139], v38, s[16:19], s47 offen offset:0 sc1\n"
      "v_permlane32_swap_b32 v216, v217\n"
      "v_permlane32_swap_b32 v218, v219\n"
      "v_add_f32_dpp v133, v213, v212 row_ror:8 row_mask:0xf bank_mask:0x3\n"
      "s_nop 1\n"
      "v_add_f32_dpp v133, v214, v215 row_ror:8 row_mask:0xf bank_mask:0xc\n"
      "v_permlane32_swap_b32 v220, v221\n"
      "v_permlane32_swap_b32 v222, v223\n"
      "v_add_f32_dpp v134, v217, v216 row_ror:8 row_mask:0xf bank_mask:0x3\n"
      "s_nop 1\n"
      "v_add_f32_dpp v134, v218, v219 row_ror:8 row_mask:0xf bank_mask:0xc\n"
      "v_add_f32_dpp v135, v221, v220 row_ror:8 row_mask:0xf bank_mask:0x3\n"
      "s_nop 1\n"
      "v_add_f32_dpp v135, v222, v223 row_ror:8 row_mask:0xf bank_mask:0xc\n"
      "v_cvt_pk_bf16_f32 v140, v132, v133\n"
      "v_cvt_pk_bf16_f32 v142, v134, v135\n"
      "buffer_store_dwordx4 v[140:143], v38, s[16:19], s47 offen offset:128 sc1\n"
      "s_endpgm\n"
      ".LZ%=:\n"
      "s_setprio 0\n"
      "v_and_b32 v48, 63, v0\n"
      "v_and_b32 v49, 7, v0\n"
      "v_lshlrev_b32 v108, 3, v49\n"
      "s_lshl4_add_u32 s35, s48, s49\n"
      "s_lshl_b32 s41, s35, 4\n"
      "s_and_b32 s43, s41, 0xffffff80\n"
      "s_add_u32 s43, s43, 67584\n"
      "buffer_load_dwordx2 v[104:105], v108, s[16:19], s43 offen sc1\n"
      "s_mul_i32 s44, s35, 7168\n"
      "s_add_u32 s44, s44, 4194304\n"
      "s_add_u32 s67, s44, 0x1000\n"
      "v_lshrrev_b32 v109, 5, v48\n"
      "v_and_b32 v110, 8, v48\n"
      "v_lshl_add_u32 v109, v109, 2, v110\n"
      "v_and_b32 v113, 16, v48\n"
      "s_movk_i32 s59, 0x7fff\n"
      "s_waitcnt vmcnt(0)\n"
      "s_movk_i32 s61, 0x7ff\n"
      ".Ldzh%=:\n"
      "v_cmp_ne_u32 vcc, s15, v105\n"
      "v_bitop3_b32 v110, v104, s61, v104 bitop3:0x0c\n"
      "v_not_b32 v112, v104\n"
      "s_cbranch_vccnz .Ldzs%=\n"
      ".Ldzo%=:\n"
      "v_lshl_add_u32 v110, v110, 11, v109\n"
      "v_lshlrev_b32 v108, 4, v48\n"
      "buffer_load_dword v111, v110, s[20:23], s41 offen\n"
      "v_lshl_or_b32 v113, v113, 26, v110\n"
      "v_ashrrev_i32 v112, 31, v112\n"
      "v_or_b32 v112, 0x80000000, v112\n"
      "v_xor_b32 v112, v112, v104\n"
      "v_and_b32 v112, 0xffff0000, v112\n"
      "v_mul_f32 v112, 0xbfb8aa3b, v112\n"
      "v_exp_f32 v112, v112\n"
      "s_nop 0\n"
      "v_add_f32 v112, 1.0, v112\n"
      "v_rcp_f32 v112, v112\n"
      "s_nop 0\n"
      "v_cvt_pk_bf16_f32 v112, v112, v112\n"
      "v_and_b32 v112, 0xffff0000, v112\n"
      ".Ldpc2_%=:\n"
      "s_memrealtime s[64:65]\n"
      "s_waitcnt lgkmcnt(0)\n"
      "s_sub_u32 s64, s64, s12\n"
      "s_cmp_ge_u32 s64, 825\n"
      "s_cbranch_scc1 .Ldpd2_%=\n"
      "s_sleep 1\n"
      "s_branch .Ldpc2_%=\n"
      ".Ldpd2_%=:\n"
      "buffer_load_dwordx4 v[64:67], v108, s[16:19], s44 offen offset:0 sc1\n"
      "buffer_load_dwordx4 v[68:71], v108, s[16:19], s44 offen offset:1024 sc1\n"
      "buffer_load_dwordx4 v[72:75], v108, s[16:19], s44 offen offset:2048 sc1\n"
      "buffer_load_dwordx4 v[76:79], v108, s[16:19], s44 offen offset:3072 sc1\n"
      "buffer_load_dwordx4 v[80:83], v108, s[16:19], s67 offen offset:0 sc1\n"
      "buffer_load_dwordx4 v[84:87], v108, s[16:19], s67 offen offset:1024 sc1\n"
      "buffer_load_dwordx4 v[88:91], v108, s[16:19], s67 offen offset:2048 sc1\n"
      "s_movk_i32 s59, 0x7fff\n"
      "s_waitcnt vmcnt(0)\n"
      "s_nop 0\n"
      "v_lshlrev_b32 v106, 16, v111\n"
      "v_and_b32 v107, 0xffff0000, v111\n"
      ".Ldq%=:\n"
      "v_xor_b32 v96, s15, v65\n"
      "v_xor_b32 v97, s15, v67\n"
      "v_bitop3_b32 v96, s15, v69, v96 bitop3:0xbe\n"
      "v_bitop3_b32 v97, s15, v71, v97 bitop3:0xbe\n"
      "v_bitop3_b32 v96, s15, v73, v96 bitop3:0xbe\n"
      "v_bitop3_b32 v97, s15, v75, v97 bitop3:0xbe\n"
      "v_bitop3_b32 v96, s15, v77, v96 bitop3:0xbe\n"
      "v_bitop3_b32 v97, s15, v79, v97 bitop3:0xbe\n"
      "v_bitop3_b32 v96, s15, v81, v96 bitop3:0xbe\n"
      "v_bitop3_b32 v97, s15, v83, v97 bitop3:0xbe\n"
      "v_bitop3_b32 v96, s15, v85, v96 bitop3:0xbe\n"
      "v_bitop3_b32 v97, s15, v87, v97 bitop3:0xbe\n"
      "v_bitop3_b32 v96, s15, v89, v96 bitop3:0xbe\n"
      "v_bitop3_b32 v97, s15, v91, v97 bitop3:0xbe\n"
      "v_or_b32 v96, v96, v97\n"
      "v_cmp_ne_u32 vcc, 0, v96\n"
      "v_lshlrev_b32 v96, 16, v64\n"
      "v_and_b32 v97, 0xffff0000, v64\n"
      "v_lshlrev_b32 v98, 16, v66\n"
      "v_and_b32 v99, 0xffff0000, v66\n"
      "s_cbranch_vccnz .Ldqs%=\n"
      ".Ldqo%=:\n"
      "v_lshlrev_b32 v100, 16, v68\n"
      "v_and_b32 v101, 0xffff0000, v68\n"
      "v_lshlrev_b32 v102, 16, v70\n"
      "v_and_b32 v103, 0xffff0000, v70\n"
      "v_add_f32 v96, v96, v100\n"
      "v_add_f32 v97, v97, v101\n"
      "v_add_f32 v98, v98, v102\n"
      "v_add_f32 v99, v99, v103\n"
      "v_lshlrev_b32 v100, 16, v72\n"
      "v_and_b32 v101, 0xffff0000, v72\n"
      "v_lshlrev_b32 v102, 16, v74\n"
      "v_and_b32 v103, 0xffff0000, v74\n"
      "v_add_f32 v96, v96, v100\n"
      "v_add_f32 v97, v97, v101\n"
      "v_add_f32 v98, v98, v102\n"
      "v_add_f32 v99, v99, v103\n"
      "v_lshlrev_b32 v100, 16, v76\n"
      "v_and_b32 v101, 0xffff0000, v76\n"
      "v_lshlrev_b32 v102, 16, v78\n"
      "v_and_b32 v103, 0xffff0000, v78\n"
      "v_add_f32 v96, v96, v100\n"
      "v_add_f32 v97, v97, v101\n"
      "v_add_f32 v98, v98, v102\n"
      "v_add_f32 v99, v99, v103\n"
      "v_lshlrev_b32 v100, 16, v80\n"
      "v_and_b32 v101, 0xffff0000, v80\n"
      "v_lshlrev_b32 v102, 16, v82\n"
      "v_and_b32 v103, 0xffff0000, v82\n"
      "v_add_f32 v96, v96, v100\n"
      "v_add_f32 v97, v97, v101\n"
      "v_add_f32 v98, v98, v102\n"
      "v_add_f32 v99, v99, v103\n"
      "v_lshlrev_b32 v100, 16, v84\n"
      "v_and_b32 v101, 0xffff0000, v84\n"
      "v_lshlrev_b32 v102, 16, v86\n"
      "v_and_b32 v103, 0xffff0000, v86\n"
      "v_add_f32 v96, v96, v100\n"
      "v_add_f32 v97, v97, v101\n"
      "v_add_f32 v98, v98, v102\n"
      "v_add_f32 v99, v99, v103\n"
      "v_lshlrev_b32 v100, 16, v88\n"
      "v_and_b32 v101, 0xffff0000, v88\n"
      "v_lshlrev_b32 v102, 16, v90\n"
      "v_and_b32 v103, 0xffff0000, v90\n"
      "v_add_f32 v96, v96, v100\n"
      "v_add_f32 v97, v97, v101\n"
      "v_add_f32 v98, v98, v102\n"
      "v_add_f32 v99, v99, v103\n"
      "s_nop 0\n"
      "v_permlane32_swap_b32 v96, v98\n"
      "v_permlane32_swap_b32 v97, v99\n"
      "v_add_f32 v96, v96, v98\n"
      "v_add_f32 v97, v97, v99\n"
      "v_mov_b32 v98, v96\n"
      "v_mov_b32 v99, v97\n"
      "s_nop 0\n"
      "v_permlane16_swap_b32 v96, v98\n"
      "v_permlane16_swap_b32 v97, v99\n"
      "v_add_f32 v96, v96, v98\n"
      "v_add_f32 v97, v97, v99\n"
      "v_cvt_pk_bf16_f32 v96, v96, v97\n"
      "v_and_b32 v97, 0xffff0000, v96\n"
      "v_lshlrev_b32 v96, 16, v96\n"
      "v_mul_f32 v97, v97, v112\n"
      "v_mul_f32 v96, v96, v112\n"
      "v_cvt_pk_bf16_f32 v96, v96, v97\n"
      "v_and_b32 v97, 0xffff0000, v96\n"
      "v_lshlrev_b32 v96, 16, v96\n"
      "v_add_f32 v97, v97, v107\n"
      "v_add_f32 v96, v96, v106\n"
      "v_cvt_pk_bf16_f32 v96, v96, v97\n"
      "buffer_atomic_pk_add_bf16 v96, v113, s[24:27], s41 offen\n"
      "s_endpgm\n"
      ".Ldgs%=:\n"
      "s_sleep 1\n"
      "s_sub_u32 s59, s59, 1\n"
      "s_cbranch_scc1 .Ldgo%=\n"
      "buffer_load_dwordx4 v[224:227], v35, s[16:19], s46 offen sc1\n"
      "buffer_load_dwordx4 v[228:231], v35, s[16:19], s46 offen offset:2048 sc1\n"
      "s_waitcnt vmcnt(0)\n"
      "s_branch .Ldg%=\n"
      ".Ldvs%=:\n"
      "s_sub_u32 s59, s59, 1\n"
      "s_cbranch_scc1 .Ldvo%=\n"
      "s_waitcnt lgkmcnt(0)\n"
      "ds_read_b128 v[240:243], v42 offset:32\n"
      "ds_read_b128 v[224:227], v37 \n"
      "ds_read_b128 v[228:231], v37 offset:1152\n"
      "s_waitcnt lgkmcnt(0)\n"
      "s_branch .Ldv%=\n"
      ".Ldzs%=:\n"
      "s_sleep 2\n"
      "s_sub_u32 s59, s59, 1\n"
      "s_cbranch_scc1 .Ldzo%=\n"
      "buffer_load_dwordx2 v[104:105], v108, s[16:19], s43 offen sc1\n"
      "s_waitcnt vmcnt(0)\n"
      "s_branch .Ldzh%=\n"
      ".Ldqs%=:\n"
      "s_sleep 1\n"
      "s_sub_u32 s59, s59, 1\n"
      "s_cbranch_scc1 .Ldqo%=\n"
      "v_cmp_ne_u32 vcc, s15, v65\n"
      "v_cmp_ne_u32 s[64:65], s15, v67\n"
      "s_nop 0\n"
      "s_or_b64 exec, vcc, s[64:65]\n"
      "s_cbranch_scc0 .Ldqk0%=\n"
      "buffer_load_dwordx4 v[64:67], v108, s[16:19], s44 offen offset:0 sc1\n"
      ".Ldqk0%=:\n"
      "s_mov_b64 exec, -1\n"
      "v_cmp_ne_u32 vcc, s15, v69\n"
      "v_cmp_ne_u32 s[64:65], s15, v71\n"
      "s_nop 0\n"
      "s_or_b64 exec, vcc, s[64:65]\n"
      "s_cbranch_scc0 .Ldqk1%=\n"
      "buffer_load_dwordx4 v[68:71], v108, s[16:19], s44 offen offset:1024 sc1\n"
      ".Ldqk1%=:\n"
      "s_mov_b64 exec, -1\n"
      "v_cmp_ne_u32 vcc, s15, v73\n"
      "v_cmp_ne_u32 s[64:65], s15, v75\n"
      "s_nop 0\n"
      "s_or_b64 exec, vcc, s[64:65]\n"
      "s_cbranch_scc0 .Ldqk2%=\n"
      "buffer_load_dwordx4 v[72:75], v108, s[16:19], s44 offen offset:2048 sc1\n"
      ".Ldqk2%=:\n"
      "s_mov_b64 exec, -1\n"
      "v_cmp_ne_u32 vcc, s15, v77\n"
      "v_cmp_ne_u32 s[64:65], s15, v79\n"
      "s_nop 0\n"
      "s_or_b64 exec, vcc, s[64:65]\n"
      "s_cbranch_scc0 .Ldqk3%=\n"
      "buffer_load_dwordx4 v[76:79], v108, s[16:19], s44 offen offset:3072 sc1\n"
      ".Ldqk3%=:\n"
      "s_mov_b64 exec, -1\n"
      "v_cmp_ne_u32 vcc, s15, v81\n"
      "v_cmp_ne_u32 s[64:65], s15, v83\n"
      "s_nop 0\n"
      "s_or_b64 exec, vcc, s[64:65]\n"
      "s_cbranch_scc0 .Ldqk4%=\n"
      "buffer_load_dwordx4 v[80:83], v108, s[16:19], s67 offen offset:0 sc1\n"
      ".Ldqk4%=:\n"
      "s_mov_b64 exec, -1\n"
      "v_cmp_ne_u32 vcc, s15, v85\n"
      "v_cmp_ne_u32 s[64:65], s15, v87\n"
      "s_nop 0\n"
      "s_or_b64 exec, vcc, s[64:65]\n"
      "s_cbranch_scc0 .Ldqk5%=\n"
      "buffer_load_dwordx4 v[84:87], v108, s[16:19], s67 offen offset:1024 sc1\n"
      ".Ldqk5%=:\n"
      "s_mov_b64 exec, -1\n"
      "v_cmp_ne_u32 vcc, s15, v89\n"
      "v_cmp_ne_u32 s[64:65], s15, v91\n"
      "s_nop 0\n"
      "s_or_b64 exec, vcc, s[64:65]\n"
      "s_cbranch_scc0 .Ldqk6%=\n"
      "buffer_load_dwordx4 v[88:91], v108, s[16:19], s67 offen offset:2048 sc1\n"
      ".Ldqk6%=:\n"
      "s_mov_b64 exec, -1\n"
      "s_waitcnt vmcnt(0)\n"
      "s_branch .Ldq%=\n"
      ".LT%=:\n"
      "v_mov_b32 v48, 0x22680\n"
      "ds_read_b32 v48, v48 \n"
      "s_waitcnt lgkmcnt(0)\n"
      "v_readfirstlane_b32 s12, v48\n"
      "v_mov_b32 v48, v43\n"
      "v_and_b32 v49, 7, v43\n"
      "v_lshrrev_b32 v50, 3, v43\n"
      "s_lshl_b32 s42, s34, 3\n"
      "s_add_u32 s42, s42, 2048\n"
      "s_lshl_b32 s43, s48, 8\n"
      "s_and_b32 s50, s49, 1\n"
      "s_lshl_b32 s50, s50, 7\n"
      "s_add_u32 s43, s43, s50\n"
      "s_add_u32 s43, s43, 65536\n"
      "s_mov_b32 s20, s64\n"
      "s_and_b32 s21, s65, 0xffff\n"
      "s_mov_b32 s22, 0x400000\n"
      "s_mov_b32 s23, 0x20000\n"
      "s_mov_b32 s28, s84\n"
      "s_and_b32 s29, s85, 0xffff\n"
      "s_mov_b32 s30, 0x700000\n"
      "s_mov_b32 s31, 0x20000\n"
      "s_sleep 0\n"
      ".LS%=:\n"
      "v_lshlrev_b32 v112, 4, v0\n"
      "v_lshlrev_b32 v113, 2, v0\n"
      "v_sub_u32 v113, 0xfff, v113\n"
      "v_lshlrev_b32 v115, 2, v48\n"
      "v_mov_b32 v116, 0\n"
      "ds_write_b32 v115, v116 offset:27136\n"
      "ds_write_b32 v116, v116 offset:26400\n"
      "v_add_u32 v115, 0x140, v115\n"
      "v_mov_b32 v114, v115\n"
      "s_lshl_b32 s50, s33, 2\n"
      "s_add_u32 s50, s50, 256\n"
      "v_writelane_b32 v114, s50, 63\n"
      "s_lshl_b32 s50, s33, 5\n"
      "v_lshl_add_u32 v117, v49, 2, s50\n"
      "v_lshlrev_b32 v118, 5, v50\n"
      "s_and_b32 s50, s49, 1\n"
      "s_lshl_b32 s50, s50, 10\n"
      "v_lshl_add_u32 v119, v50, 7, s50\n"
      "s_lshr_b32 s51, s33, 2\n"
      "s_mul_i32 s51, s51, 61440\n"
      "s_add_u32 s51, s51, 393216\n"
      "s_mov_b32 s52, 0x80008000\n"
      "s_mov_b32 s53, 0xffff0000\n"
      "s_movk_i32 s59, 0x7fff\n"
      ".Lt_pc4_%=:\n"
      "s_memrealtime s[64:65]\n"
      "s_waitcnt lgkmcnt(0)\n"
      "s_sub_u32 s64, s64, s12\n"
      "s_cmp_ge_u32 s64, 300\n"
      "s_cbranch_scc1 .Lt_pd4_%=\n"
      "s_sleep 1\n"
      "s_branch .Lt_pc4_%=\n"
      ".Lt_pd4_%=:\n"
      ".Lt_p%=:\n"
      "buffer_load_dwordx4 v[72:75], v112, s[16:19], s51 offen sc1\n"
      "s_waitcnt vmcnt(0)\n"
      "v_cmp_ne_u32 vcc, s15, v73\n"
      "v_cmp_ne_u32 s[54:55], s15, v75\n"
      "v_pk_ashrrev_i16 v81, 15, v72 op_sel_hi:[0,1]\n"
      "v_pk_ashrrev_i16 v82, 15, v74 op_sel_hi:[0,1]\n"
      "s_or_b64 vcc, vcc, s[54:55]\n"
      "s_cbranch_scc1 .Lt_ps%=\n"
      ".Lt_po%=:\n"
      "v_bitop3_b32 v81, v72, v81, s52 bitop3:0x1e\n"
      "v_bitop3_b32 v82, v74, v82, s52 bitop3:0x1e\n"
      "v_lshl_or_b32 v76, v81, 16, v113\n"
      "v_and_or_b32 v77, v81, s53, v113\n"
      "v_lshl_or_b32 v78, v82, 16, v113\n"
      "v_and_or_b32 v79, v82, s53, v113\n"
      "v_add_u32 v77, -1, v77\n"
      "v_add_u32 v78, -2, v78\n"
      "v_add_u32 v79, -3, v79\n"
      "v_max3_u32 v80, v76, v77, v78\n"
      "v_max_u32 v80, v80, v79\n"
      "v_mov_b32 v104, v80\n"
      "v_med3_u32 v105, v76, v77, v78\n"
      "v_max_u32_dpp v80, v80, v80 row_shr:1 row_mask:0xf bank_mask:0xf bound_ctrl:1\n"
      "v_max3_u32 v83, v76, v77, v78\n"
      "v_min3_u32 v106, v76, v77, v78\n"
      "v_max_u32_dpp v80, v80, v80 row_shr:2 row_mask:0xf bank_mask:0xf bound_ctrl:1\n"
      "v_med3_u32 v106, v105, v106, v79\n"
      "v_med3_u32 v105, v83, v105, v79\n"
      "v_max_u32_dpp v80, v80, v80 row_shr:4 row_mask:0xf bank_mask:0xf bound_ctrl:1\n"
      "v_mov_b32 v107, 0x100\n"
      "s_nop 0\n"
      "v_max_u32_dpp v80, v80, v80 row_shr:8 row_mask:0xf bank_mask:0xf bound_ctrl:1\n"
      "s_nop 1\n"
      "v_max_u32_dpp v80, v80, v80 row_bcast:15 row_mask:0xa bank_mask:0xf\n"
      "s_nop 1\n"
      "v_max_u32_dpp v80, v80, v80 row_bcast:31 row_mask:0xc bank_mask:0xf\n"
      "ds_write_b32 v114, v80 offset:26112\n"
      "s_waitcnt lgkmcnt(0)\n"
      "s_barrier\n"
      "ds_read_b128 v[92:95], v116 offset:26368\n"
      "ds_read_b128 v[96:99], v116 offset:26384\n"
      "s_waitcnt lgkmcnt(1)\n"
      "v_min3_u32 v81, v92, v93, v94\n"
      "s_waitcnt lgkmcnt(0)\n"
      "v_min3_u32 v82, v96, v97, v98\n"
      "v_min3_u32 v81, v81, v95, v99\n"
      "v_min_u32 v81, v81, v82\n"
      "v_cmp_ge_u32 s[62:63], v105, v81\n"
      "v_cmp_ge_u32 vcc, v106, v81\n"
      "v_cmp_ge_u32 s[60:61], v104, v81\n"
      "v_cndmask_b32 v82, 1, 2, s[62:63]\n"
      "v_cndmask_b32 v82, v82, v107, vcc\n"
      "s_mov_b64 exec, s[60:61]\n"
      "ds_add_rtn_u32 v83, v116, v82 offset:26400\n"
      "s_waitcnt lgkmcnt(0)\n"
      "v_min_u32 v83, 0x50, v83\n"
      "v_lshlrev_b32 v83, 2, v83\n"
      "ds_write_b32 v83, v104 offset:27136\n"
      "s_mov_b64 exec, s[62:63]\n"
      "ds_write_b32 v83, v105 offset:27140\n"
      "s_mov_b64 exec, -1\n"
      "s_waitcnt lgkmcnt(0)\n"
      "s_barrier\n"
      "s_memrealtime s[56:57]\n"
      "ds_read_b32 v81, v116 offset:26400\n"
      ".Lt_r%=:\n"
      "ds_read_b32 v100, v117 offset:27136\n"
      "ds_read_b128 v[92:95], v118 offset:27136\n"
      "ds_read_b128 v[96:99], v118 offset:27152\n"
      "v_mov_b32 v101, s15\n"
      "v_mov_b32 v89, 0\n"
      "s_waitcnt lgkmcnt(3)\n"
      "v_cmp_lt_u32 vcc, 64, v81\n"
      "s_movk_i32 s50, 0x7ff\n"
      "s_cbranch_vccnz .Lt_m%=\n"
      "s_waitcnt lgkmcnt(1)\n"
      "v_cmp_gt_u32 s[60:61], v92, v100\n"
      "v_cmp_gt_u32 s[62:63], v93, v100\n"
      "v_cmp_gt_u32 s[64:65], v94, v100\n"
      "v_cmp_gt_u32 s[66:67], v95, v100\n"
      "s_waitcnt lgkmcnt(0)\n"
      "v_cmp_gt_u32 s[68:69], v96, v100\n"
      "v_cmp_gt_u32 s[70:71], v97, v100\n"
      "v_cmp_gt_u32 s[72:73], v98, v100\n"
      "v_cmp_gt_u32 s[74:75], v99, v100\n"
      "v_bitop3_b32 v81, v100, s50, v100 bitop3:0x0c\n"
      "v_addc_co_u32 v89, vcc, 0, v89, s[60:61]\n"
      "v_addc_co_u32 v89, vcc, 0, v89, s[62:63]\n"
      "v_addc_co_u32 v89, vcc, 0, v89, s[64:65]\n"
      "v_addc_co_u32 v89, vcc, 0, v89, s[66:67]\n"
      "v_addc_co_u32 v89, vcc, 0, v89, s[68:69]\n"
      "v_addc_co_u32 v89, vcc, 0, v89, s[70:71]\n"
      "v_addc_co_u32 v89, vcc, 0, v89, s[72:73]\n"
      "v_addc_co_u32 v89, vcc, 0, v89, s[74:75]\n"
      "v_lshl_add_u32 v82, v81, 11, v119\n"
      "v_lshrrev_b32 v86, 9, v81\n"
      "v_mul_u32_u24 v86, 0xf000, v86\n"
      "v_lshl_add_u32 v81, v81, 3, v86\n"
      "v_add_u32_dpp v89, v89, v89 row_ror:8 row_mask:0xf bank_mask:0xf\n"
      "v_mov_b32 v83, v89\n"
      "s_nop 1\n"
      "v_permlane16_swap_b32 v89, v83\n"
      "v_add_u32 v89, v89, v83\n"
      "v_mov_b32 v83, v89\n"
      "s_nop 1\n"
      "v_permlane32_swap_b32 v89, v83\n"
      "v_add_u32 v89, v89, v83\n"
      "v_cmp_gt_u32 vcc, 8, v89\n"
      "v_lshlrev_b32 v89, 3, v89\n"
      "s_mov_b32 s51, 131072\n"
      "s_cbranch_vccz .Lt_n%=\n"
      "s_and_b64 exec, vcc, 0xff\n"
      "ds_write_b32 v89, v100 offset:26752\n"
      "buffer_load_dword v84, v81, s[16:19], s51 offen sc1\n"
      "s_mov_b64 exec, vcc\n"
      "buffer_load_dword v85, v82, s[20:23], 0 offen\n"
      "s_mov_b64 exec, -1\n"
      ".Lt_n%=:\n"
      "v_lshlrev_b32 v89, 3, v49\n"
      "s_waitcnt lgkmcnt(0)\n"
      "s_barrier\n"
      "s_sub_u32 s12, s56, 390\n"
      "s_movk_i32 s57, 64\n"
      "s_cmp_lg_u32 s33, 0\n"
      "s_cbranch_scc1 .Lt_a%=\n"
      "ds_read_b32 v100, v89 offset:26752\n"
      "v_writelane_b32 v89, s57, 8\n"
      "s_waitcnt lgkmcnt(0)\n"
      "v_writelane_b32 v100, s12, 8\n"
      "s_mov_b64 exec, 0x1ff\n"
      "buffer_store_dwordx2 v[100:101], v89, s[16:19], s43 offen sc1\n"
      "s_mov_b64 exec, -1\n"
      "s_nop 1\n"
      "v_mov_b32 v96, s15\n"
      "v_mov_b32 v97, s15\n"
      "buffer_store_dwordx2 v[96:97], off, s[16:19], s42 sc1\n"
      ".Lt_a%=:\n"
      "s_cmp_lg_u32 s33, 1\n"
      "s_cbranch_scc1 .Lt_c%=\n"
      "ds_read_b32 v100, v89 offset:26752\n"
      "v_lshlrev_b32 v81, 3, v50\n"
      "ds_read_b32 v81, v81 offset:26752\n"
      "v_mov_b32 v92, 0\n"
      "v_mov_b32 v93, 0\n"
      "v_mov_b32 v94, 0\n"
      "v_mov_b32 v95, 0\n"
      "s_and_b32 s51, s43, 0x7ff\n"
      "v_lshlrev_b32 v82, 4, v49\n"
      "s_waitcnt lgkmcnt(0)\n"
      "v_bitop3_b32 v81, v81, s50, v81 bitop3:0x0c\n"
      "v_lshl_add_u32 v81, v81, 11, v82\n"
      "buffer_store_dwordx4 v[92:95], v81, s[24:27], s51 offen sc1\n"
      "s_waitcnt vmcnt(0)\n"
      "s_mov_b64 exec, 0xff\n"
      "buffer_store_dwordx2 v[100:101], v89, s[16:19], s43 offen offset:2048 sc1\n"
      "s_mov_b64 exec, -1\n"
      "s_nop 1\n"
      ".Lt_c%=:\n"
      "s_cmp_lg_u32 s33, 3\n"
      "s_cbranch_scc1 .Lt_z%=\n"
      "s_sleep 55\n"
      "s_branch .LZ%=\n"
      ".Lt_z%=:\n"
      "s_cmp_lg_u32 s34, 127\n"
      "s_cbranch_scc1 .Lt_e%=\n"
      "s_cmp_lg_u32 s33, 0\n"
      "s_cbranch_scc1 .Lt_e%=\n"
      "s_movk_i32 s51, 2048\n"
      "s_movk_i32 s59, 0x7fff\n"
      ".Lt_k%=:\n"
      "s_sleep 16\n"
      "buffer_load_dwordx4 v[92:95], v112, s[16:19], s51 offen sc1\n"
      "buffer_load_dwordx4 v[96:99], v112, s[16:19], s51 offen offset:1024 sc1\n"
      "s_sub_u32 s59, s59, 1\n"
      "s_cbranch_scc1 .Lt_kb%=\n"
      "s_waitcnt vmcnt(0)\n"
      "v_xor_b32 v86, s15, v93\n"
      "v_bitop3_b32 v86, s15, v95, v86 bitop3:0xbe\n"
      "v_bitop3_b32 v86, s15, v97, v86 bitop3:0xbe\n"
      "v_bitop3_b32 v86, s15, v99, v86 bitop3:0xbe\n"
      "v_cmp_ne_u32 vcc, 0, v86\n"
      "s_nop 1\n"
      "s_cbranch_vccnz .Lt_k%=\n"
      ".Lt_kb%=:\n"
      "buffer_store_dword v101, off, s[16:19], 0 sc1\n"
      ".Lt_e%=:\n"
      "s_waitcnt vmcnt(0)\n"
      "s_endpgm\n"
      ".Lt_ps%=:\n"
      "s_sleep 1\n"
      "s_sub_u32 s59, s59, 1\n"
      "s_cbranch_scc0 .Lt_p%=\n"
      "s_branch .Lt_po%=\n"
      ".Lt_m%=:\n"
      "s_waitcnt lgkmcnt(0)\n"
      "s_barrier\n"
      "s_mov_b32 s59, 0\n"
      "v_mov_b32 v90, 0\n"
      ".Lt_ml%=:\n"
      "s_nop 1\n"
      "v_max3_u32 v80, v76, v77, v78\n"
      "v_max_u32 v80, v80, v79\n"
      "s_nop 1\n"
      "v_max_u32_dpp v80, v80, v80 row_shr:1 row_mask:0xf bank_mask:0xf bound_ctrl:1\n"
      "s_nop 1\n"
      "v_max_u32_dpp v80, v80, v80 row_shr:2 row_mask:0xf bank_mask:0xf bound_ctrl:1\n"
      "s_nop 1\n"
      "v_max_u32_dpp v80, v80, v80 row_shr:4 row_mask:0xf bank_mask:0xf bound_ctrl:1\n"
      "s_nop 1\n"
      "v_max_u32_dpp v80, v80, v80 row_shr:8 row_mask:0xf bank_mask:0xf bound_ctrl:1\n"
      "s_nop 1\n"
      "v_max_u32_dpp v80, v80, v80 row_bcast:15 row_mask:0xa bank_mask:0xf\n"
      "s_nop 1\n"
      "v_max_u32_dpp v80, v80, v80 row_bcast:31 row_mask:0xc bank_mask:0xf\n"
      "s_nop 0\n"
      "v_readlane_b32 s50, v80, 63\n"
      "s_mov_b32 m0, s59\n"
      "s_nop 0\n"
      "v_cmp_ne_u32 vcc, s50, v76\n"
      "s_nop 1\n"
      "v_cndmask_b32 v76, 0, v76, vcc\n"
      "v_cmp_ne_u32 vcc, s50, v77\n"
      "s_nop 1\n"
      "v_cndmask_b32 v77, 0, v77, vcc\n"
      "v_cmp_ne_u32 vcc, s50, v78\n"
      "s_nop 1\n"
      "v_cndmask_b32 v78, 0, v78, vcc\n"
      "v_cmp_ne_u32 vcc, s50, v79\n"
      "s_nop 1\n"
      "v_cndmask_b32 v79, 0, v79, vcc\n"
      "v_writelane_b32 v90, s50, m0\n"
      "s_add_u32 s59, s59, 1\n"
      "s_cmp_lt_u32 s59, 8\n"
      "s_cbranch_scc1 .Lt_ml%=\n"
      "s_mov_b64 exec, 0xff\n"
      "ds_write_b32 v117, v90 offset:27136\n"
      "s_mov_b64 exec, -1\n"
      "v_mov_b32 v81, 0\n"
      "s_waitcnt lgkmcnt(0)\n"
      "s_barrier\n"
      "s_branch .Lt_r%=\n"
      ".LEnd%=:\n"
      : "={s[80:95]}"(sts), "={s[96:99]}"(st2)
      : [ka] "s"(__builtin_amdgcn_kernarg_segment_ptr()), [bid] "s"(blockIdx.x), "{v0}"(threadIdx.x), "s"((u32)(uintptr_t)sm)
      : "v8", "v9", "v10", "v11", "v12", "v13", "v14", "v15", "v16", "v17", "v18", "v19", "v20", "v21", "v22", "v23", "v24", "v25", "v26", "v27",
        "v28", "v29", "v30", "v31", "v32", "v33", "v34", "v35", "v36", "v37", "v38", "v39", "v40", "v41", "v42", "v43", "v44", "v45", "v46", "v47",
        "v48", "v49", "v50", "v51", "v52", "v53", "v54", "v55", "v56", "v57", "v58", "v59", "v60", "v61", "v62", "v63", "v64", "v65", "v66", "v67",
        "v68", "v69", "v70", "v71", "v72", "v73", "v74", "v75", "v76", "v77", "v78", "v79", "v80", "v81", "v82", "v83", "v84", "v85", "v86", "v87",
        "v88", "v89", "v90", "v91", "v92", "v93", "v94", "v95", "v96", "v97", "v98", "v99", "v100", "v101", "v102", "v103", "v104", "v105", "v106", "v107",
        "v108", "v109", "v110", "v111", "v112", "v113", "v114", "v115", "v116", "v117", "v118", "v119", "v120", "v121", "v122", "v123", "v124", "v125", "v126", "v127",
        "v128", "v129", "v130", "v131", "v132", "v133", "v134", "v135", "v136", "v137", "v138", "v139", "v140", "v141", "v142", "v143", "v144", "v145", "v146", "v147",
        "v148", "v149", "v150", "v151", "v152", "v153", "v154", "v155", "v156", "v157", "v158", "v159", "v160", "v161", "v162", "v163", "v164", "v165", "v166", "v167",
        "v168", "v169", "v170", "v171", "v172", "v173", "v174", "v175", "v176", "v177", "v178", "v179", "v180", "v181", "v182", "v183", "v184", "v185", "v186", "v187",
        "v188", "v189", "v190", "v191", "v192", "v193", "v194", "v195", "v196", "v197", "v198", "v199", "v200", "v201", "v202", "v203", "v204", "v205", "v206", "v207",
        "v208", "v209", "v210", "v211", "v212", "v213", "v214", "v215", "v216", "v217", "v218", "v219", "v220", "v221", "v222", "v223", "v224", "v225", "v226", "v227",
        "v228", "v229", "v230", "v231", "v232", "v233", "v234", "v235", "v236", "v237", "v238", "v239", "v240", "v241", "v242", "v243", "v244", "v245", "v246", "v247",
        "v248", "v249", "v250", "v251", "v252", "v253", "v254", "v255", "s12", "s13", "s14", "s15", "s16", "s17", "s18", "s19", "s20", "s21", "s22", "s23",
        "s24", "s25", "s26", "s27", "s28", "s29", "s30", "s31", "s33", "s34", "s35", "s36", "s37", "s38", "s39", "s40", "s41", "s42", "s43", "s44",
        "s45", "s46", "s47", "s48", "s49", "s50", "s51", "s52", "s53", "s54", "s55", "s56", "s57", "s58", "s59", "s60", "s61", "s62", "s63", "s64",
        "s65", "s66", "s67", "s68", "s69", "s70", "s71", "s72", "s73", "s74", "s75", "s76", "s77", "s78", "s79",
        "vcc", "scc", "memory");
  const int b = blockIdx.x;
  GETST();
  STAMP(11);
  PEND();
}

extern "C" void kernel_launch(void* const* d_in, const int* in_sizes, int n_in, void* d_out, int out_size,
                              void* d_ws, size_t ws_size, hipStream_t stream) {
  const u16* const* in = (const u16* const*)d_in;
  fused<<<256, 512, 0, stream>>>(in[0], in[1], in[2], in[3], in[4], in[5], in[8], in[6], in[9], in[10], in[11],
                                 (u16*)d_out, (char*)d_ws);
}
```

```cpp
#include <hip/hip_runtime.h>
#include <stdint.h>

typedef unsigned short u16;
typedef unsigned int u32;
typedef unsigned long long u64;
typedef __attribute__((ext_vector_type(4))) u32 u32x4;
typedef __attribute__((ext_vector_type(16))) u32 u32x16;
#define AS3 __attribute__((address_space(3)))

#define STAMP(I)
#define PEND()
#define GETST()
constexpr int SPIN_MAX = 1 << 16;
constexpr int UNUSED = 0;

__global__ __launch_bounds__(512, 2) void fused(const u16* __restrict__ x, const u16* __restrict__ lar,
    const u16* __restrict__ aim, const u16* __restrict__ cre, const u16* __restrict__ cim,
    const u16* __restrict__ ldt, const u16* __restrict__ wr, const u16* __restrict__ dsk,
    const u16* __restrict__ wg, const u16* __restrict__ wu, const u16* __restrict__ wd,
    u16* __restrict__ out, char* ws) {
  __shared__ __attribute__((aligned(16))) char smem_[163840];
  AS3 char* sm = (AS3 char*)smem_;
  u64 ptm = 0, pc0 = 0, pt0 = 0, pte = 0, st_[16] = {};
  u32x16 sts;
  u32x4 st2;
  asm volatile(
      "s_memrealtime s[12:13]\n"
      "s_load_dwordx16 s[64:79], %[ka], 0x0\n"
      "s_load_dwordx8 s[80:87], %[ka], 0x40\n"
      "s_load_dwordx2 s[88:89], %[ka], 0x60\n"
      "v_readfirstlane_b32 s33, v0\n"
      "s_getpc_b64 s[50:51]\n"
      ".Lpc%=:\n"
      "s_mov_b32 s34, %[bid]\n"
      "v_and_b32 v43, 63, v0\n"
      "v_mov_b32 v15, 0\n"
      "s_lshr_b32 s33, s33, 6\n"
      "s_and_b32 s52, s34, -8\n"
      "s_add_u32 s52, s52, s33\n"
      "s_min_u32 s52, s52, (.LEnd%=-.Lpc%=)>>7\n"
      "s_lshl_b32 s52, s52, 7\n"
      "s_and_b32 s50, s50, 0xffffff80\n"
      "s_add_u32 s50, s50, s52\n"
      "s_addc_u32 s51, s51, 0\n"
      "global_load_dword v11, v15, s[50:51] \n"
      "s_and_b32 s48, s34, 7\n"
      "s_lshr_b32 s49, s34, 3\n"
      "s_and_b32 s55, s49, 15\n"
      "s_lshl4_add_u32 s35, s48, s55\n"
      "s_cmp_lt_u32 s34, 128\n"
      "s_cbranch_scc1 .LH%=\n"
      "s_lshl3_add_u32 s50, s35, s33\n"
      "s_lshr_b32 s51, s34, 5\n"
      "s_andn2_b32 s51, 4, s51\n"
      "s_add_u32 s50, s50, s51\n"
      "s_lshl_b32 s51, s50, 8\n"
      "s_lshl_b32 s50, s50, 1\n"
      "v_lshl_add_u32 v48, v43, 2, s51\n"
      "v_mov_b32 v49, s50\n"
      "s_waitcnt lgkmcnt(0)\n"
      "global_load_dword v16, v48, s[66:67] nt\n"
      "global_load_dword v17, v48, s[68:69] nt\n"
      "global_load_ushort v12, v49, s[74:75] \n"
      "global_load_dword v18, v48, s[70:71] nt\n"
      "global_load_dword v19, v48, s[72:73] nt\n"
      "global_load_ushort v13, v49, s[78:79] \n"
      "s_load_dword s15, s[88:89], 0x0\n"
      "s_setprio 3\n"
      "s_branch .LC%=\n"
      ".LH%=:\n"
      "s_cmp_ge_u32 s33, 4\n"
      "s_cbranch_scc1 .LG%=\n"
      ".LD%=:\n"
      "v_and_b32 v52, 48, v43\n"
      "v_and_b32 v53, 3, v43\n"
      "v_lshl_add_u32 v53, v53, 2, v52\n"
      "v_and_b32 v54, 12, v43\n"
      "v_lshlrev_b32 v53, 2, v53\n"
      "v_lshl_add_u32 v52, v54, 9, v53\n"
      "s_and_b32 s53, s34, 127\n"
      "s_and_b32 s52, s33, 3\n"
      "s_lshl2_add_u32 s14, s53, s52\n"
      "s_lshl_b32 s51, s14, 13\n"
      "v_lshl_add_u32 v53, v54, 6, v53\n"
      "s_waitcnt lgkmcnt(0)\n"
      "s_add_u32 s52, s64, s51\n"
      "s_addc_u32 s53, s65, 0\n"
      "global_load_dwordx4 v[224:227], v53, s[76:77] offset:0\n"
      "global_load_dwordx4 v[228:231], v53, s[76:77] offset:1024\n"
      "global_load_dwordx4 v[192:195], v52, s[52:53] offset:0\n"
      "global_load_dwordx4 v[196:199], v52, s[52:53] offset:256\n"
      "global_load_dwordx4 v[200:203], v52, s[52:53] offset:512\n"
      "global_load_dwordx4 v[204:207], v52, s[52:53] offset:768\n"
      "global_load_dwordx4 v[208:211], v52, s[52:53] offset:1024\n"
      "global_load_dwordx4 v[212:215], v52, s[52:53] offset:1280\n"
      "global_load_dwordx4 v[216:219], v52, s[52:53] offset:1536\n"
      "global_load_dwordx4 v[220:223], v52, s[52:53] offset:1792\n"
      "s_load_dword s15, s[88:89], 0x0\n"
      "s_setprio 3\n"
      "s_cmp_lg_u32 s33, 0\n"
      "s_cbranch_scc1 .Ltb%=\n"
      "v_mov_b32 v48, 0x22680\n"
      "v_mov_b32 v49, s12\n"
      "ds_write_b32 v48, v49 \n"
      ".Ltb%=:\n"
      "s_mov_b32 s16, s88\n"
      "s_and_b32 s17, s89, 0xffff\n"
      "s_mov_b32 s18, 0x6e0000\n"
      "s_mov_b32 s19, 0x20000\n"
      "s_mov_b32 s20, s64\n"
      "s_and_b32 s21, s65, 0xffff\n"
      "s_mov_b32 s22, 0x400000\n"
      "s_mov_b32 s23, 0x20000\n"
      "s_mov_b32 s28, s84\n"
      "s_and_b32 s29, s85, 0xffff\n"
      "s_mov_b32 s30, 0x700000\n"
      "s_mov_b32 s31, 0x20000\n"
      "s_mov_b32 s24, s86\n"
      "s_and_b32 s25, s87, 0xffff\n"
      "s_mov_b32 s26, 0x400000\n"
      "s_mov_b32 s27, 0x20000\n"
      "s_lshl_b32 s42, s34, 3\n"
      "s_add_u32 s42, s42, 2048\n"
      "s_lshl_b32 s43, s48, 8\n"
      "s_and_b32 s50, s49, 1\n"
      "s_lshl_b32 s50, s50, 7\n"
      "s_add_u32 s43, s43, s50\n"
      "s_add_u32 s43, s43, 65536\n"
      "v_mov_b32 v20, 0x3f803f80\n"
      "v_mov_b32 v21, 0x3f803f80\n"
      "v_mov_b32 v22, 0x3f803f80\n"
      "v_mov_b32 v23, 0x3f803f80\n"
      "v_mov_b32 v44, 0x7f000000\n"
      "v_mov_b32 v25, 0\n"
      "s_lshr_b32 s70, s14, 7\n"
      "s_mul_i32 s70, s70, 61440\n"
      "s_lshl_b32 s68, s14, 5\n"
      "s_add_u32 s68, s68, s70\n"
      "s_add_u32 s68, s68, 131072\n"
      "s_lshr_b32 s70, s14, 8\n"
      "s_mul_i32 s70, s70, 61440\n"
      "s_lshl4_add_u32 s69, s14, s70\n"
      "s_add_u32 s69, s69, 393216\n"
      "v_writelane_b32 v44, s68, 0\n"
      "s_add_u32 s68, s68, 8\n"
      "v_writelane_b32 v44, s68, 20\n"
      "s_add_u32 s68, s68, 8\n"
      "v_writelane_b32 v44, s68, 40\n"
      "s_add_u32 s68, s68, 8\n"
      "v_writelane_b32 v44, s68, 60\n"
      "s_add_u32 s68, s68, 8\n"
      "v_writelane_b32 v44, s69, 1\n"
      "s_add_u32 s69, s69, 8\n"
      "v_writelane_b32 v44, s69, 2\n"
      "s_waitcnt lgkmcnt(0)\n"
      "s_add_u32 s15, s15, 0x9e3779b1\n"
      "v_mov_b32 v47, s15\n"
      "s_waitcnt vmcnt(9)\n"
      "v_mfma_f32_16x16x32_bf16 v[16:19], v[20:23], v[224:227], 0\n"
      "s_waitcnt vmcnt(8)\n"
      "v_mfma_f32_16x16x32_bf16 v[16:19], v[20:23], v[228:231], v[16:19]\n"
      "s_waitcnt vmcnt(7)\n"
      "v_mfma_f32_16x16x32_bf16 v[232:235], v[192:195], v[224:227], 0\n"
      "v_mfma_f32_16x16x32_bf16 v[252:255], v[192:195], v[192:195], 0\n"
      "v_mfma_f32_16x16x32_bf16 v[248:251], v[192:195], v[20:23], 0\n"
      "s_waitcnt vmcnt(6)\n"
      "v_mfma_f32_16x16x32_bf16 v[236:239], v[196:199], v[224:227], 0\n"
      "v_mfma_f32_16x16x32_bf16 v[252:255], v[196:199], v[196:199], v[252:255]\n"
      "v_mfma_f32_16x16x32_bf16 v[248:251], v[196:199], v[20:23], v[248:251]\n"
      "s_waitcnt vmcnt(5)\n"
      "v_mfma_f32_16x16x32_bf16 v[240:243], v[200:203], v[224:227], 0\n"
      "v_mfma_f32_16x16x32_bf16 v[252:255], v[200:203], v[200:203], v[252:255]\n"
      "v_mfma_f32_16x16x32_bf16 v[248:251], v[200:203], v[20:23], v[248:251]\n"
      "s_waitcnt vmcnt(4)\n"
      "v_mfma_f32_16x16x32_bf16 v[244:247], v[204:207], v[224:227], 0\n"
      "v_mfma_f32_16x16x32_bf16 v[252:255], v[204:207], v[204:207], v[252:255]\n"
      "v_mfma_f32_16x16x32_bf16 v[248:251], v[204:207], v[20:23], v[248:251]\n"
      "s_waitcnt vmcnt(3)\n"
      "v_mfma_f32_16x16x32_bf16 v[232:235], v[208:211], v[228:231], v[232:235]\n"
      "v_mfma_f32_16x16x32_bf16 v[252:255], v[208:211], v[208:211], v[252:255]\n"
      "v_mfma_f32_16x16x32_bf16 v[248:251], v[208:211], v[20:23], v[248:251]\n"
      "v_add_f32_dpp v16, v16, v16 quad_perm:[1,0,3,2] row_mask:0xf bank_mask:0xf\n"
      "s_waitcnt vmcnt(2)\n"
      "v_mfma_f32_16x16x32_bf16 v[236:239], v[212:215], v[228:231], v[236:239]\n"
      "v_mfma_f32_16x16x32_bf16 v[252:255], v[212:215], v[212:215], v[252:255]\n"
      "v_mfma_f32_16x16x32_bf16 v[248:251], v[212:215], v[20:23], v[248:251]\n"
      "v_add_f32_dpp v16, v16, v16 quad_perm:[2,3,0,1] row_mask:0xf bank_mask:0xf\n"
      "v_add_f32_dpp v25, v232, v25 quad_perm:[0,0,0,0] row_mask:0xf bank_mask:0x1\n"
      "s_nop 1\n"
      "v_add_f32_dpp v25, v233, v25 quad_perm:[1,1,1,1] row_mask:0xf bank_mask:0x1\n"
      "s_nop 1\n"
      "v_add_f32_dpp v25, v234, v25 quad_perm:[2,2,2,2] row_mask:0xf bank_mask:0x1\n"
      "s_nop 1\n"
      "v_add_f32_dpp v25, v235, v25 quad_perm:[3,3,3,3] row_mask:0xf bank_mask:0x1\n"
      "s_waitcnt vmcnt(1)\n"
      "v_mfma_f32_16x16x32_bf16 v[240:243], v[216:219], v[228:231], v[240:243]\n"
      "v_mfma_f32_16x16x32_bf16 v[252:255], v[216:219], v[216:219], v[252:255]\n"
      "v_mfma_f32_16x16x32_bf16 v[248:251], v[216:219], v[20:23], v[248:251]\n"
      "v_add_f32_dpp v16, v16, v16 row_ror:4 row_mask:0xf bank_mask:0xf\n"
      "v_add_f32_dpp v25, v236, v25 quad_perm:[0,0,0,0] row_mask:0xf bank_mask:0x2\n"
      "s_nop 1\n"
      "v_add_f32_dpp v25, v237, v25 quad_perm:[1,1,1,1] row_mask:0xf bank_mask:0x2\n"
      "s_nop 1\n"
      "v_add_f32_dpp v25, v238, v25 quad_perm:[2,2,2,2] row_mask:0xf bank_mask:0x2\n"
      "s_nop 1\n"
      "v_add_f32_dpp v25, v239, v25 quad_perm:[3,3,3,3] row_mask:0xf bank_mask:0x2\n"
      "s_waitcnt vmcnt(0)\n"
      "v_mfma_f32_16x16x32_bf16 v[244:247], v[220:223], v[228:231], v[244:247]\n"
      "v_mfma_f32_16x16x32_bf16 v[252:255], v[220:223], v[220:223], v[252:255]\n"
      "v_mfma_f32_16x16x32_bf16 v[248:251], v[220:223], v[20:23], v[248:251]\n"
      "v_add_f32_dpp v16, v16, v16 row_ror:8 row_mask:0xf bank_mask:0xf\n"
      "v_add_f32_dpp v25, v240, v25 quad_perm:[0,0,0,0] row_mask:0xf bank_mask:0x4\n"
      "s_nop 1\n"
      "v_add_f32_dpp v25, v241, v25 quad_perm:[1,1,1,1] row_mask:0xf bank_mask:0x4\n"
      "s_nop 1\n"
      "v_add_f32_dpp v25, v242, v25 quad_perm:[2,2,2,2] row_mask:0xf bank_mask:0x4\n"
      "s_nop 1\n"
      "v_add_f32_dpp v25, v243, v25 quad_perm:[3,3,3,3] row_mask:0xf bank_mask:0x4\n"
      "s_nop 1\n"
      "v_add_f32_dpp v25, v244, v25 quad_perm:[0,0,0,0] row_mask:0xf bank_mask:0x8\n"
      "v_mov_b32_dpp v24, v252 quad_perm:[0,0,0,0] row_mask:0xf bank_mask:0xf\n"
      "v_mov_b32_dpp v27, v254 quad_perm:[2,2,2,2] row_mask:0xf bank_mask:0xf\n"
      "v_add_f32_dpp v25, v245, v25 quad_perm:[1,1,1,1] row_mask:0xf bank_mask:0x8\n"
      "v_add_f32 v248, v248, v249\n"
      "v_add_f32 v250, v250, v251\n"
      "v_add_f32_dpp v24, v253, v24 quad_perm:[1,1,1,1] row_mask:0xf bank_mask:0xf\n"
      "v_add_f32_dpp v27, v255, v27 quad_perm:[3,3,3,3] row_mask:0xf bank_mask:0xf\n"
      "v_add_f32_dpp v25, v246, v25 quad_perm:[2,2,2,2] row_mask:0xf bank_mask:0x8\n"
      "v_add_f32 v248, v248, v250\n"
      "v_add_f32 v24, v24, v27\n"
      "v_add_f32_dpp v25, v247, v25 quad_perm:[3,3,3,3] row_mask:0xf bank_mask:0x8\n"
      "v_mul_f32 v248, 0x3a800000, v248\n"
      "v_mul_f32 v24, 0x3a800000, v24\n"
      "v_add_f32_dpp v25, v25, v25 row_ror:4 row_mask:0xf bank_mask:0xf\n"
      "v_fma_f32 v24, -v248, v248, v24\n"
      "v_max_f32 v24, 0, v24\n"
      "v_add_f32_dpp v25, v25, v25 row_ror:8 row_mask:0xf bank_mask:0xf\n"
      "v_cvt_pk_bf16_f32 v248, v248, v24\n"
      "v_and_b32 v24, 0xffff0000, v248\n"
      "v_add_f32 v24, 0x3727c5ac, v24\n"
      "v_lshlrev_b32 v26, 16, v248\n"
      "v_cvt_pk_bf16_f32 v24, v24, v24\n"
      "v_fma_f32 v25, -v26, v16, v25\n"
      "v_and_b32 v24, 0xffff0000, v24\n"
      "v_sqrt_f32 v24, v24\n"
      "s_nop 0\n"
      "v_cvt_pk_bf16_f32 v46, v26, v24\n"
      "v_and_b32 v24, 0xffff0000, v46\n"
      "v_rcp_f32 v24, v24\n"
      "s_nop 0\n"
      "v_mul_f32 v25, v25, v24\n"
      "v_cvt_pk_bf16_f32 v25, v25, v25\n"
      "s_nop 0\n"
      "v_readlane_b32 s68, v25, 0\n"
      "v_readlane_b32 s69, v25, 20\n"
      "v_readlane_b32 s70, v25, 40\n"
      "v_readlane_b32 s71, v25, 60\n"
      "s_pack_ll_b32_b16 s68, s68, s69\n"
      "s_pack_ll_b32_b16 s70, s70, s71\n"
      "v_writelane_b32 v46, s68, 1\n"
      "v_writelane_b32 v46, s70, 2\n"
      "buffer_store_dwordx2 v[46:47], v44, s[16:19], 0 offen sc1\n"
      "v_mov_b32 v48, v43\n"
      "v_and_b32 v49, 7, v43\n"
      "v_lshrrev_b32 v50, 3, v43\n"
      "s_cmp_ge_u32 s34, 112\n"
      "s_cbranch_scc1 .LS%=\n"
      "s_branch .LD2%=\n"
      ".LC%=:\n"
      "s_mov_b32 s16, s88\n"
      "s_and_b32 s17, s89, 0xffff\n"
      "s_mov_b32 s18, 0x6e0000\n"
      "s_mov_b32 s19, 0x20000\n"
      "s_mov_b32 s20, s64\n"
      "s_and_b32 s21, s65, 0xffff\n"
      "s_mov_b32 s22, 0x400000\n"
      "s_mov_b32 s23, 0x20000\n"
      "s_mul_i32 s39, s33, 6528\n"
      "s_mul_i32 s40, s33, 8768\n"
      "s_add_u32 s52, s40, 52224\n"
      "s_add_u32 s53, s52, 4160\n"
      "s_lshl_b32 s42, s34, 3\n"
      "s_add_u32 s42, s42, 2048\n"
      "v_and_b32 v48, 31, v43\n"
      "v_lshrrev_b32 v49, 5, v43\n"
      "v_lshlrev_b32 v55, 4, v49\n"
      "v_lshrrev_b32 v52, 2, v48\n"
      "v_mul_u32_u24 v52, 0x110, v52\n"
      "v_add3_u32 v20, v52, v55, s52\n"
      "v_and_b32 v52, 3, v43\n"
      "v_mul_u32_u24 v52, 0x110, v52\n"
      "s_add_u32 s50, s52, 2176\n"
      "v_add3_u32 v178, v52, v55, s50\n"
      "v_lshl_add_u32 v52, v43, 2, s50\n"
      "v_mov_b32 v53, 0x3c00\n"
      "ds_write_b32 v52, v53 \n"
      "ds_write_b32 v52, v53 offset:3264\n"
      "v_and_b32 v52, 12, v43\n"
      "s_mov_b32 s50, 0x6600\n"
      "s_mov_b32 s51, 0x6600\n"
      "v_cndmask_b32 v52, v48, v52, s[50:51]\n"
      "s_mov_b32 s50, 0xff000000\n"
      "s_mov_b32 s51, 0xff000000\n"
      "v_cndmask_b32 v52, v52, 9, s[50:51]\n"
      "v_mul_u32_u24 v52, 0x110, v52\n"
      "v_add3_u32 v21, v52, v55, s39\n"
      "v_lshl_add_u32 v52, v43, 2, s39\n"
      "v_mov_b32 v53, 0x3c00\n"
      "ds_write_b32 v52, v53 \n"
      "s_mov_b32 s24, s86\n"
      "s_and_b32 s25, s87, 0xffff\n"
      "s_mov_b32 s26, 0x400000\n"
      "s_mov_b32 s27, 0x20000\n"
      "s_mul_i32 s54, s33, 4864\n"
      "s_add_u32 s54, s54, 122368\n"
      "s_lshl_b32 s41, s35, 4\n"
      "s_and_b32 s43, s41, 0xffffff80\n"
      "s_add_u32 s43, s43, 67584\n"
      "v_and_b32 v50, 15, v43\n"
      "v_lshrrev_b32 v51, 4, v43\n"
      "v_and_b32 v53, 1, v43\n"
      "s_and_b32 s58, s33, 3\n"
      "s_lshl4_add_u32 s50, s33, s55\n"
      "s_lshl_b32 s50, s50, 15\n"
      "s_and_b32 s51, s55, 8\n"
      "s_lshl_b32 s51, s51, 4\n"
      "s_sub_u32 s50, s50, s51\n"
      "v_bfe_u32 v52, v43, 1, 4\n"
      "v_lshl_add_u32 v52, v52, 4, v53\n"
      "v_lshlrev_b32 v52, 7, v52\n"
      "v_add_u32 v15, s50, v52\n"
      "s_bitcmp1_b32 s55, 0\n"
      "s_cselect_b32 s56, s82, s80\n"
      "s_cselect_b32 s57, s83, s81\n"
      "s_lshr_b32 s50, s56, 7\n"
      "s_sub_u32 s50, s48, s50\n"
      "s_add_u32 s50, s50, 3\n"
      "s_and_b32 s50, s50, 7\n"
      "s_min_u32 s50, s50, 6\n"
      "s_mul_i32 s51, s48, 7\n"
      "s_add_u32 s50, s50, s51\n"
      "s_lshl_b32 s50, s50, 7\n"
      "s_lshr_b32 s51, s55, 1\n"
      "s_mul_i32 s51, s51, 24\n"
      "s_lshl_b32 s58, s58, 8\n"
      "s_add_u32 s51, s51, s58\n"
      "s_add_u32 s51, s51, 64\n"
      "s_mul_i32 s51, s51, 7168\n"
      "s_add_u32 s50, s50, s51\n"
      "v_min_u32 v57, 23, v43\n"
      "v_mul_u32_u24 v57, 0x1c00, v57\n"
      "v_add_u32 v57, s50, v57\n"
      "v_lshl_add_u32 v52, v48, 1, v49\n"
      "v_lshlrev_b32 v54, 3, v52\n"
      "s_add_u32 s50, s53, 570\n"
      "v_sub_u32 v22, s50, v54\n"
      "s_lshl_b32 s50, s33, 17\n"
      "v_lshl_add_u32 v8, v52, 11, s50\n"
      "v_mov_b32 v52, 0x63e\n"
      "v_mov_b32 v54, 0x6de\n"
      "s_mov_b32 s50, 0x55555555\n"
      "s_mov_b32 s51, 0x55555555\n"
      "v_cndmask_b32 v24, v52, v54, s[50:51]\n"
      "v_mov_b32 v52, 0x4de\n"
      "v_mov_b32 v54, 0x59e\n"
      "v_cndmask_b32 v52, v52, v54, s[50:51]\n"
      "s_mov_b32 s58, 0\n"
      "s_mov_b32 s59, -1\n"
      "v_cndmask_b32 v24, v24, v52, s[58:59]\n"
      "v_add_u32 v52, -1, v48\n"
      "v_and_b32 v52, -2, v52\n"
      "v_sub_u32 v24, v24, v52\n"
      "v_add_u32 v24, s53, v24\n"
      "v_add_u32 v23, 32, v24\n"
      "s_add_u32 s60, s53, 2048\n"
      "v_writelane_b32 v23, s60, 0\n"
      "v_writelane_b32 v23, s60, 32\n"
      "v_mul_u32_u24 v52, 0xc1, v53\n"
      "v_sub_u32 v52, v52, v48\n"
      "s_add_u32 s60, s53, 1150\n"
      "v_add_u32 v52, s60, v52\n"
      "v_add_u32 v25, 0xffffffc0, v24\n"
      "v_cndmask_b32 v25, v25, v52, s[58:59]\n"
      "v_and_b32 v52, -2, v43\n"
      "v_add_u32 v52, 58, v52\n"
      "v_mov_b32 v54, 0x810\n"
      "s_mov_b32 s60, 0xfffff000\n"
      "s_mov_b32 s61, 0xffffffff\n"
      "v_cndmask_b32 v52, v52, v54, s[60:61]\n"
      "s_movk_i32 s60, 1408\n"
      "s_movk_i32 s61, 1664\n"
      "v_writelane_b32 v52, s60, 8\n"
      "v_writelane_b32 v52, s60, 9\n"
      "v_writelane_b32 v52, s61, 10\n"
      "v_writelane_b32 v52, s61, 11\n"
      "v_mul_u32_u24 v54, 0x83e, v53\n"
      "v_add3_u32 v26, v52, v54, s53\n"
      "v_lshlrev_b32 v52, 1, v50\n"
      "v_add_u32 v52, v52, v54\n"
      "s_add_u32 s60, s53, 2140\n"
      "v_sub_u32 v52, s60, v52\n"
      "v_lshl_add_u32 v29, v51, 4, v52\n"
      "v_lshl_add_u32 v30, v51, 3, v52\n"
      "v_add_u32 v30, 0x440, v30\n"
      "v_bfe_u32 v52, v43, 2, 3\n"
      "v_max_u32 v54, 5, v52\n"
      "v_mul_u32_u24 v52, 0x60, v52\n"
      "v_lshl_add_u32 v52, v54, 6, v52\n"
      "v_add_u32 v52, 0x2a0, v52\n"
      "s_mov_b32 s60, 0xf\n"
      "s_mov_b32 s61, 0xf\n"
      "v_cndmask_b32 v52, v52, v55, s[60:61]\n"
      "v_mul_u32_u24 v54, 0x830, v49\n"
      "v_add3_u32 v27, v52, v54, s53\n"
      "v_and_b32 v55, 3, v43\n"
      "v_lshl_add_u32 v27, v55, 4, v27\n"
      "s_add_u32 s60, s39, 3104\n"
      "s_add_u32 s61, s52, 2080\n"
      "v_mov_b32 v52, s39\n"
      "v_mov_b32 v54, s60\n"
      "s_mov_b32 s50, 0xff00ff00\n"
      "s_mov_b32 s51, 0xff00ff00\n"
      "v_cndmask_b32 v28, v52, v54, s[50:51]\n"
      "v_mov_b32 v52, s52\n"
      "v_mov_b32 v54, s61\n"
      "v_cndmask_b32 v52, v52, v54, s[50:51]\n"
      "s_mov_b32 s50, 0xffff0000\n"
      "s_mov_b32 s51, 0xffff0000\n"
      "v_cndmask_b32 v28, v28, v52, s[50:51]\n"
      "v_and_b32 v52, 7, v43\n"
      "v_lshl_add_u32 v28, v52, 2, v28\n"
      "v_lshlrev_b32 v9, 3, v52\n"
      "v_bfe_u32 v10, v43, 3, 2\n"
      "v_lshlrev_b32 v10, 2, v10\n"
      "v_lshl_add_u32 v58, v49, 8, v48\n"
      "s_lshl_b32 s50, s33, 5\n"
      "v_add_u32 v58, s50, v58\n"
      "s_mov_b32 s50, 122912\n"
      "v_lshl_add_u32 v32, v58, 2, s50\n"
      "v_lshlrev_b32 v41, 12, v58\n"
      "v_mul_u32_u24 v42, 0xf000, v49\n"
      "v_lshl_add_u32 v42, v58, 4, v42\n"
      "v_lshl_add_u32 v52, v50, 1, v51\n"
      "v_lshl_add_u32 v33, v52, 4, s54\n"
      "s_add_u32 s50, s54, 528\n"
      "v_lshl_add_u32 v34, v43, 4, s50\n"
      "v_lshl_add_u32 v35, v43, 3, s39\n"
      "v_lshl_add_u32 v36, v43, 3, s52\n"
      "s_add_u32 s50, s39, 32\n"
      "v_lshl_add_u32 v37, v43, 4, s50\n"
      "s_add_u32 s50, s52, 32\n"
      "v_lshl_add_u32 v38, v43, 4, s50\n"
      "v_lshl_add_u32 v52, v50, 2, v51\n"
      "v_lshl_add_u32 v39, v52, 2, s53\n"
      "v_bfe_u32 v52, v43, 2, 2\n"
      "v_mul_u32_u24 v52, 0x2240, v52\n"
      "v_lshl_add_u32 v52, v49, 11, v52\n"
      "v_lshl_add_u32 v52, v55, 3, v52\n"
      "v_and_b32 v54, 16, v43\n"
      "v_lshl_add_u32 v52, v54, 1, v52\n"
      "s_lshl_b32 s50, s33, 6\n"
      "s_add_u32 s50, s50, 56384\n"
      "v_add_u32 v40, s50, v52\n"
      "v_mov_b32 v54, 0\n"
      "v_mov_b32 v55, 0\n"
      "v_lshl_add_u32 v52, v43, 3, s54\n"
      "ds_write_b64 v52, v[54:55] offset:32\n"
      "s_mov_b32 s50, 161536\n"
      "v_lshl_add_u32 v52, v0, 2, s50\n"
      "ds_write_b32 v52, v54 \n"
      "v_mov_b32 v52, 0xffff\n"
      "v_mov_b32 v53, 0xffff0000\n"
      "s_mov_b32 s50, 0x0\n"
      "s_mov_b32 s51, 0x18000018\n"
      "v_cndmask_b32 v44, 0, v52, s[50:51]\n"
      "s_mov_b32 s60, 0x0\n"
      "s_mov_b32 s61, 0x30000030\n"
      "v_cndmask_b32 v54, 0, v53, s[60:61]\n"
      "v_or_b32 v44, v44, v54\n"
      "s_mov_b32 s50, 0x0\n"
      "s_mov_b32 s51, 0x60000060\n"
      "v_cndmask_b32 v45, 0, v52, s[50:51]\n"
      "s_mov_b32 s60, 0x0\n"
      "s_mov_b32 s61, 0xc00000c0\n"
      "v_cndmask_b32 v54, 0, v53, s[60:61]\n"
      "v_or_b32 v45, v45, v54\n"
      "s_mov_b32 s50, 0x10000\n"
      "s_mov_b32 s51, 0x80000180\n"
      "v_cndmask_b32 v46, 0, v52, s[50:51]\n"
      "s_mov_b32 s60, 0x30000\n"
      "s_mov_b32 s61, 0x300\n"
      "v_cndmask_b32 v54, 0, v53, s[60:61]\n"
      "v_or_b32 v46, v46, v54\n"
      "s_mov_b32 s50, 0x60000\n"
      "s_mov_b32 s51, 0x600\n"
      "v_cndmask_b32 v47, 0, v52, s[50:51]\n"
      "s_mov_b32 s60, 0xc0000\n"
      "s_mov_b32 s61, 0xc00\n"
      "v_cndmask_b32 v54, 0, v53, s[60:61]\n"
      "v_or_b32 v47, v47, v54\n"
      ".Lsc%=:\n"
      "s_waitcnt vmcnt(0)\n"
      "s_mov_b32 s50, 0x3fb8aa3b\n"
      "v_mov_b32 v14, v57\n"
      "v_lshlrev_b32 v48, 16, v12\n"
      "v_and_b32 v52, 0x7fff7fff, v18\n"
      "v_and_b32 v53, 0x7fff7fff, v19\n"
      "v_mul_f32 v49, s50, v48\n"
      "v_pk_max_u16 v52, v52, v53\n"
      "v_fma_f32 v50, v48, s50, -v49\n"
      "v_rndne_f32 v51, v49\n"
      "v_lshlrev_b32 v53, 16, v52\n"
      "v_fmamk_f32 v50, v48, 0x32a5705f, v50\n"
      "v_sub_f32 v49, v49, v51\n"
      "v_max_u32 v52, v52, v53\n"
      "v_add_f32 v49, v49, v50\n"
      "v_cvt_i32_f32 v51, v51\n"
      "v_max_u32_dpp v52, v52, v52 row_shr:1 row_mask:0xf bank_mask:0xf bound_ctrl:1\n"
      "v_exp_f32 v49, v49\n"
      "v_lshlrev_b32 v54, 16, v13\n"
      "v_max_u32_dpp v52, v52, v52 row_shr:2 row_mask:0xf bank_mask:0xf bound_ctrl:1\n"
      "v_ldexp_f32 v49, v49, v51\n"
      "v_add_f32 v54, 1.0, v54\n"
      "v_max_u32_dpp v52, v52, v52 row_shr:4 row_mask:0xf bank_mask:0xf bound_ctrl:1\n"
      "v_cvt_pk_bf16_f32 v49, v49, v49\n"
      "s_waitcnt lgkmcnt(0)\n"
      "v_max_u32_dpp v52, v52, v52 row_shr:8 row_mask:0xf bank_mask:0xf bound_ctrl:1\n"
      "v_and_b32 v49, 0xffff0000, v49\n"
      "v_and_b32 v55, 0x7fffffff, v54\n"
      "v_max_u32_dpp v52, v52, v52 row_bcast:15 row_mask:0xa bank_mask:0xf\n"
      "v_mul_f32 v50, 0xbfb8aa3b, v49\n"
      "v_mul_f32 v51, 0xbe22f983, v49\n"
      "v_max_u32_dpp v52, v52, v52 row_bcast:31 row_mask:0xc bank_mask:0xf\n"
      "v_readfirstlane_b32 s36, v50\n"
      "v_readlane_b32 s50, v52, 63\n"
      "v_readfirstlane_b32 s37, v51\n"
      "v_mul_f32 v55, 0x3b800000, v55\n"
      "v_mul_f32 v52, s50, v49\n"
      "s_add_u32 s15, s15, 0x9e3779b1\n"
      "v_max_f32 v52, v52, v55\n"
      "v_frexp_exp_i32_f32 v52, v52\n"
      "s_nop 0\n"
      "v_readfirstlane_b32 s50, v52\n"
      "s_nop 0\n"
      "s_sub_i32 s50, 7, s50\n"
      "s_max_i32 s50, s50, -107\n"
      "s_min_i32 s50, s50, 93\n"
      "v_ldexp_f32 v49, v49, s50\n"
      "s_sub_i32 s51, s50, 1\n"
      "v_ldexp_f32 v54, v54, s51\n"
      "s_sub_i32 s14, 128, s50\n"
      "s_lshl_b32 s14, s14, 23\n"
      "v_readfirstlane_b32 s38, v49\n"
      "s_mov_b64 s[60:61], 1\n"
      "v_cndmask_b32 v31, 0, v54, s[60:61]\n"
      "s_lshl_b32 s50, s33, 2\n"
      "s_add_u32 s50, s50, 161280\n"
      "v_mov_b32 v50, s50\n"
      "v_mov_b32 v51, s14\n"
      "ds_write_b32 v50, v51 \n"
      "s_mov_b32 s60, s41\n"
      "s_add_u32 s61, s41, 0x200000\n"
      "s_mov_b32 s62, 131072\n"
      "s_mov_b32 s63, 262144\n"
      "s_mov_b32 m0, s40\n"
      "s_add_u32 s13, s40, 52224\n"
      "s_add_u32 s12, s39, 272\n"
      "v_lshlrev_b32 v72, 16, v16\n"
      "v_and_b32 v73, 0xffff0000, v16\n"
      "v_lshlrev_b32 v74, 16, v17\n"
      "v_and_b32 v75, 0xffff0000, v17\n"
      "s_mov_b32 s64, 0x3fb8aa3b\n"
      "s_mov_b32 s66, 0x43800000\n"
      "s_mov_b32 s68, 0x42000000\n"
      "s_mov_b32 s70, s37\n"
      "v_pk_mul_f32 v[72:73], s[64:65], v[72:73] op_sel_hi:[0,1]\n"
      "v_pk_mul_f32 v[74:75], s[70:71], v[74:75] op_sel_hi:[0,1]\n"
      "v_exp_f32 v72, v72\n"
      "v_exp_f32 v73, v73\n"
      "v_pk_mul_f32 v[78:79], s[66:67], v[74:75] op_sel_hi:[0,1]\n"
      "v_pk_mul_f32 v[80:81], s[68:69], v[74:75] op_sel_hi:[0,1]\n"
      "v_fract_f32 v76, v74\n"
      "v_fract_f32 v77, v75\n"
      "v_cvt_pk_bf16_f32 v72, v72, v73\n"
      "v_cos_f32 v90, v76\n"
      "v_cos_f32 v91, v77\n"
      "v_fract_f32 v78, v78\n"
      "v_fract_f32 v79, v79\n"
      "v_and_b32 v73, 0xffff0000, v72\n"
      "v_lshlrev_b32 v72, 16, v72\n"
      "v_sin_f32 v92, v76\n"
      "v_sin_f32 v93, v77\n"
      "v_pk_mul_f32 v[82:83], s[36:37], v[72:73] op_sel_hi:[0,1]\n"
      "v_fract_f32 v80, v80\n"
      "v_fract_f32 v81, v81\n"
      "v_exp_f32 v84, v82\n"
      "v_exp_f32 v85, v83\n"
      "v_pk_mul_f32 v[86:87], s[66:67], v[82:83] op_sel_hi:[0,1]\n"
      "v_cos_f32 v94, v78\n"
      "v_cos_f32 v95, v79\n"
      "v_pk_mul_f32 v[88:89], s[68:69], v[82:83] op_sel_hi:[0,1]\n"
      "v_sin_f32 v96, v78\n"
      "v_sin_f32 v97, v79\n"
      "v_pk_mul_f32 v[90:91], v[90:91], v[84:85]\n"
      "v_exp_f32 v86, v86\n"
      "v_exp_f32 v87, v87\n"
      "v_pk_mul_f32 v[92:93], v[92:93], v[84:85]\n"
      "s_cmp_ge_u32 s33, 4\n"
      "s_cbranch_scc1 .LcR%=\n"
      "buffer_load_dword v11, v15, s[20:23], s41 offen\n"
      "global_load_dword v11, v14, s[56:57]\n"
      ".LcR%=:\n"
      "v_pk_mul_f32 v[82:83], v[92:93], v[92:93]\n"
      "v_cos_f32 v202, v80\n"
      "v_pk_add_f32 v[84:85], v[90:91], v[90:91]\n"
      "v_cos_f32 v150, v81\n"
      "v_pk_fma_f32 v[98:99], v[90:91], v[90:91], v[82:83] neg_lo:[0,0,1] neg_hi:[0,0,1]\n"
      "v_pk_mul_f32 v[94:95], v[94:95], v[86:87]\n"
      "v_pk_mul_f32 v[100:101], v[84:85], v[92:93]\n"
      "v_pk_mul_f32 v[96:97], v[96:97], v[86:87]\n"
      "v_cvt_pk_f16_f32 v208, v90, v92\n"
      "v_sin_f32 v203, v80\n"
      "v_cvt_pk_f16_f32 v156, v91, v93\n"
      "v_sin_f32 v151, v81\n"
      "v_pk_mul_f32 v[82:83], v[100:101], v[100:101]\n"
      "v_exp_f32 v88, v88\n"
      "v_pk_add_f32 v[84:85], v[98:99], v[98:99]\n"
      "v_exp_f32 v89, v89\n"
      "v_pk_fma_f32 v[102:103], v[98:99], v[98:99], v[82:83] neg_lo:[0,0,1] neg_hi:[0,0,1]\n"
      "v_pk_mul_f32 v[114:115], v[96:97], v[96:97]\n"
      "v_pk_mul_f32 v[104:105], v[84:85], v[100:101]\n"
      "v_pk_add_f32 v[116:117], v[94:95], v[94:95]\n"
      "v_cvt_pk_f16_f32 v209, v98, v100\n"
      "v_pk_fma_f32 v[106:107], v[94:95], v[94:95], v[114:115] neg_lo:[0,0,1] neg_hi:[0,0,1]\n"
      "v_cvt_pk_f16_f32 v157, v99, v101\n"
      "v_pk_mul_f32 v[108:109], v[116:117], v[96:97]\n"
      "v_pk_mul_f32 v[82:83], v[104:105], v[104:105]\n"
      "v_cvt_pk_f16_f32 v212, v94, v96\n"
      "v_pk_add_f32 v[84:85], v[102:103], v[102:103]\n"
      "v_cvt_pk_f16_f32 v160, v95, v97\n"
      "v_pk_fma_f32 v[90:91], v[102:103], v[102:103], v[82:83] neg_lo:[0,0,1] neg_hi:[0,0,1]\n"
      "v_pk_mul_f32 v[202:203], v[202:203], v[88:89] op_sel:[0,0] op_sel_hi:[1,0]\n"
      "v_pk_mul_f32 v[92:93], v[84:85], v[104:105]\n"
      "v_pk_mul_f32 v[150:151], v[150:151], v[88:89] op_sel:[0,1] op_sel_hi:[1,1]\n"
      "v_cvt_pk_f16_f32 v210, v102, v104\n"
      "v_cvt_pk_f16_f32 v213, v106, v108\n"
      "v_cvt_pk_f16_f32 v158, v103, v105\n"
      "v_cvt_pk_f16_f32 v161, v107, v109\n"
      "v_pk_mul_f32 v[82:83], v[92:93], v[92:93]\n"
      "v_pk_mul_f32 v[114:115], v[108:109], v[108:109]\n"
      "v_pk_add_f32 v[84:85], v[90:91], v[90:91]\n"
      "v_pk_add_f32 v[116:117], v[106:107], v[106:107]\n"
      "v_pk_fma_f32 v[72:73], v[90:91], v[90:91], v[82:83] neg_lo:[0,0,1] neg_hi:[0,0,1]\n"
      "v_pk_fma_f32 v[94:95], v[106:107], v[106:107], v[114:115] neg_lo:[0,0,1] neg_hi:[0,0,1]\n"
      "v_pk_mul_f32 v[74:75], v[84:85], v[92:93]\n"
      "v_pk_mul_f32 v[96:97], v[116:117], v[108:109]\n"
      "v_cvt_pk_f16_f32 v205, v90, v92\n"
      "v_pk_mul_f16 v214, v212, v213 op_sel_hi:[0,1]\n"
      "v_cvt_pk_f16_f32 v153, v91, v93\n"
      "v_pk_mul_f16 v162, v160, v161 op_sel_hi:[0,1]\n"
      "v_pk_mul_f16 v211, v209, v210 op_sel_hi:[0,1]\n"
      "v_pk_fma_f16 v214, v212, v213, v214 op_sel:[1,1,0] op_sel_hi:[1,0,1] neg_lo:[0,1,0]\n"
      "v_pk_mul_f16 v159, v157, v158 op_sel_hi:[0,1]\n"
      "v_pk_fma_f16 v162, v160, v161, v162 op_sel:[1,1,0] op_sel_hi:[1,0,1] neg_lo:[0,1,0]\n"
      "v_pk_fma_f16 v211, v209, v210, v211 op_sel:[1,1,0] op_sel_hi:[1,0,1] neg_lo:[0,1,0]\n"
      "v_cvt_pk_f16_f32 v215, v94, v96\n"
      "v_pk_fma_f16 v159, v157, v158, v159 op_sel:[1,1,0] op_sel_hi:[1,0,1] neg_lo:[0,1,0]\n"
      "v_cvt_pk_f16_f32 v163, v95, v97\n"
      "v_cvt_pk_f16_f32 v206, v72, v74\n"
      "v_lshlrev_b32 v198, 16, v18\n"
      "v_cvt_pk_f16_f32 v154, v73, v75\n"
      "v_lshlrev_b32 v199, 16, v19\n"
      "v_pk_mul_f16 v207, v205, v206 op_sel_hi:[0,1]\n"
      "v_pk_mul_f32 v[198:199], s[38:39], v[198:199] op_sel_hi:[0,1]\n"
      "v_pk_mul_f16 v155, v153, v154 op_sel_hi:[0,1]\n"
      "v_and_b32 v146, 0xffff0000, v18\n"
      "v_pk_fma_f16 v207, v205, v206, v207 op_sel:[1,1,0] op_sel_hi:[1,0,1] neg_lo:[0,1,0]\n"
      "v_and_b32 v147, 0xffff0000, v19\n"
      "v_pk_fma_f16 v155, v153, v154, v155 op_sel:[1,1,0] op_sel_hi:[1,0,1] neg_lo:[0,1,0]\n"
      "v_pk_mul_f32 v[146:147], s[38:39], v[146:147] op_sel_hi:[0,1]\n"
      "v_cvt_pk_f16_f32 v240, v198, v199\n"
      "v_cvt_pk_f16_f32 v241, v146, v147\n"
      "v_pk_mul_f32 v[200:201], v[198:199], v[202:203] op_sel_hi:[1,0]\n"
      "v_pk_mul_f32 v[148:149], v[146:147], v[150:151] op_sel_hi:[1,0]\n"
      "ds_write_addtid_b32 v240 offset:52224\n"
      "ds_write_addtid_b32 v241 offset:55488\n"
      "v_pk_fma_f32 v[200:201], v[198:199], v[202:203], v[200:201] op_sel:[1,1,0] op_sel_hi:[0,1,1] neg_hi:[0,1,0]\n"
      "v_pk_fma_f32 v[148:149], v[146:147], v[150:151], v[148:149] op_sel:[1,1,0] op_sel_hi:[0,1,1] neg_hi:[0,1,0]\n"
      "ds_write_addtid_b32 v205 offset:54672\n"
      "v_cvt_pk_f16_f32 v242, v200, v201\n"
      "v_cvt_pk_f16_f32 v243, v148, v149\n"
      "v_pk_mul_f32 v[198:199], v[200:201], v[202:203] op_sel_hi:[1,0]\n"
      "v_pk_mul_f32 v[146:147], v[148:149], v[150:151] op_sel_hi:[1,0]\n"
      "ds_write_addtid_b32 v242 offset:52496\n"
      "ds_write_addtid_b32 v243 offset:55760\n"
      "v_pk_fma_f32 v[198:199], v[200:201], v[202:203], v[198:199] op_sel:[1,1,0] op_sel_hi:[0,1,1] neg_hi:[0,1,0]\n"
      "v_pk_fma_f32 v[146:147], v[148:149], v[150:151], v[146:147] op_sel:[1,1,0] op_sel_hi:[0,1,1] neg_hi:[0,1,0]\n"
      "ds_write_addtid_b32 v153 offset:57936\n"
      "v_cvt_pk_f16_f32 v240, v198, v199\n"
      "v_cvt_pk_f16_f32 v241, v146, v147\n"
      "v_pk_mul_f32 v[200:201], v[198:199], v[202:203] op_sel_hi:[1,0]\n"
      "v_pk_mul_f32 v[148:149], v[146:147], v[150:151] op_sel_hi:[1,0]\n"
      "ds_write_addtid_b32 v240 offset:52768\n"
      "ds_write_addtid_b32 v241 offset:56032\n"
      "v_pk_fma_f32 v[200:201], v[198:199], v[202:203], v[200:201] op_sel:[1,1,0] op_sel_hi:[0,1,1] neg_hi:[0,1,0]\n"
      "v_pk_fma_f32 v[148:149], v[146:147], v[150:151], v[148:149] op_sel:[1,1,0] op_sel_hi:[0,1,1] neg_hi:[0,1,0]\n"
      "ds_write_addtid_b32 v206 offset:54944\n"
      "v_cvt_pk_f16_f32 v242, v200, v201\n"
      "v_cvt_pk_f16_f32 v243, v148, v149\n"
      "v_pk_mul_f32 v[198:199], v[200:201], v[202:203] op_sel_hi:[1,0]\n"
      "v_pk_mul_f32 v[146:147], v[148:149], v[150:151] op_sel_hi:[1,0]\n"
      "ds_write_addtid_b32 v242 offset:53040\n"
      "ds_write_addtid_b32 v243 offset:56304\n"
      "v_pk_fma_f32 v[198:199], v[200:201], v[202:203], v[198:199] op_sel:[1,1,0] op_sel_hi:[0,1,1] neg_hi:[0,1,0]\n"
      "v_pk_fma_f32 v[146:147], v[148:149], v[150:151], v[146:147] op_sel:[1,1,0] op_sel_hi:[0,1,1] neg_hi:[0,1,0]\n"
      "ds_write_addtid_b32 v154 offset:58208\n"
      "v_cvt_pk_f16_f32 v240, v198, v199\n"
      "v_cvt_pk_f16_f32 v241, v146, v147\n"
      "v_pk_mul_f32 v[200:201], v[198:199], v[202:203] op_sel_hi:[1,0]\n"
      "v_pk_mul_f32 v[148:149], v[146:147], v[150:151] op_sel_hi:[1,0]\n"
      "ds_write_addtid_b32 v240 offset:53312\n"
      "ds_write_addtid_b32 v241 offset:56576\n"
      "v_pk_fma_f32 v[200:201], v[198:199], v[202:203], v[200:201] op_sel:[1,1,0] op_sel_hi:[0,1,1] neg_hi:[0,1,0]\n"
      "v_pk_fma_f32 v[148:149], v[146:147], v[150:151], v[148:149] op_sel:[1,1,0] op_sel_hi:[0,1,1] neg_hi:[0,1,0]\n"
      "ds_write_addtid_b32 v207 offset:55216\n"
      "v_cvt_pk_f16_f32 v242, v200, v201\n"
      "v_cvt_pk_f16_f32 v243, v148, v149\n"
      "v_pk_mul_f32 v[198:199], v[200:201], v[202:203] op_sel_hi:[1,0]\n"
      "v_pk_mul_f32 v[146:147], v[148:149], v[150:151] op_sel_hi:[1,0]\n"
      "ds_write_addtid_b32 v242 offset:53584\n"
      "ds_write_addtid_b32 v243 offset:56848\n"
      "v_pk_fma_f32 v[198:199], v[200:201], v[202:203], v[198:199] op_sel:[1,1,0] op_sel_hi:[0,1,1] neg_hi:[0,1,0]\n"
      "v_pk_fma_f32 v[146:147], v[148:149], v[150:151], v[146:147] op_sel:[1,1,0] op_sel_hi:[0,1,1] neg_hi:[0,1,0]\n"
      "ds_write_addtid_b32 v155 offset:58480\n"
      "v_cvt_pk_f16_f32 v240, v198, v199\n"
      "v_cvt_pk_f16_f32 v241, v146, v147\n"
      "v_pk_mul_f32 v[200:201], v[198:199], v[202:203] op_sel_hi:[1,0]\n"
      "v_pk_mul_f32 v[148:149], v[146:147], v[150:151] op_sel_hi:[1,0]\n"
      "ds_write_addtid_b32 v240 offset:53856\n"
      "ds_write_addtid_b32 v241 offset:57120\n"
      "v_pk_fma_f32 v[200:201], v[198:199], v[202:203], v[200:201] op_sel:[1,1,0] op_sel_hi:[0,1,1] neg_hi:[0,1,0]\n"
      "v_pk_fma_f32 v[148:149], v[146:147], v[150:151], v[148:149] op_sel:[1,1,0] op_sel_hi:[0,1,1] neg_hi:[0,1,0]\n"
      "v_cvt_pk_f16_f32 v242, v200, v201\n"
      "v_cvt_pk_f16_f32 v243, v148, v149\n"
      "ds_write_addtid_b32 v242 offset:54128\n"
      "ds_write_addtid_b32 v243 offset:57392\n"
      "ds_read_b128 v[104:107], v20 offset:0\n"
      "ds_read_b128 v[108:111], v178 offset:0\n"
      "ds_read_b128 v[112:115], v20 offset:32\n"
      "ds_read_b128 v[116:119], v178 offset:32\n"
      "s_mov_b32 m0, s39\n"
      "s_setprio 2\n"
      "v_pk_mul_f16 v240, v209, v208 op_sel_hi:[0,1]\n"
      "v_pk_mul_f16 v241, v210, v208 op_sel_hi:[0,1]\n"
      "v_pk_mul_f16 v242, v211, v208 op_sel_hi:[0,1]\n"
      "ds_write_addtid_b32 v208 offset:272\n"
      "v_pk_fma_f16 v240, v209, v208, v240 op_sel:[1,1,0] op_sel_hi:[1,0,1] neg_lo:[0,1,0]\n"
      "ds_write_addtid_b32 v209 offset:544\n"
      "v_pk_fma_f16 v241, v210, v208, v241 op_sel:[1,1,0] op_sel_hi:[1,0,1] neg_lo:[0,1,0]\n"
      "ds_write_addtid_b32 v240 offset:816\n"
      "v_pk_fma_f16 v242, v211, v208, v242 op_sel:[1,1,0] op_sel_hi:[1,0,1] neg_lo:[0,1,0]\n"
      "ds_write_addtid_b32 v241 offset:1360\n"
      "ds_write_addtid_b32 v242 offset:1904\n"
      "v_pk_mul_f16 v243, v212, v209 op_sel_hi:[0,1]\n"
      "v_pk_mul_f16 v244, v212, v210 op_sel_hi:[0,1]\n"
      "v_pk_mul_f16 v245, v212, v211 op_sel_hi:[0,1]\n"
      "ds_write_addtid_b32 v210 offset:1088\n"
      "v_pk_fma_f16 v243, v212, v209, v243 op_sel:[1,1,0] op_sel_hi:[1,0,1] neg_lo:[0,1,0]\n"
      "ds_write_addtid_b32 v211 offset:1632\n"
      "v_pk_fma_f16 v244, v212, v210, v244 op_sel:[1,1,0] op_sel_hi:[1,0,1] neg_lo:[0,1,0]\n"
      "ds_write_addtid_b32 v243 offset:2992\n"
      "v_pk_fma_f16 v245, v212, v211, v245 op_sel:[1,1,0] op_sel_hi:[1,0,1] neg_lo:[0,1,0]\n"
      "ds_write_addtid_b32 v244 offset:3264\n"
      "ds_write_addtid_b32 v245 offset:4080\n"
      "v_pk_mul_f16 v246, v213, v210 op_sel_hi:[0,1]\n"
      "v_pk_mul_f16 v247, v214, v210 op_sel_hi:[0,1]\n"
      "v_pk_mul_f16 v248, v215, v215 op_sel_hi:[0,1]\n"
      "ds_write_addtid_b32 v212 offset:2176\n"
      "v_pk_fma_f16 v246, v213, v210, v246 op_sel:[1,1,0] op_sel_hi:[1,0,1] neg_lo:[0,1,0]\n"
      "ds_write_addtid_b32 v213 offset:5440\n"
      "v_pk_fma_f16 v247, v214, v210, v247 op_sel:[1,1,0] op_sel_hi:[1,0,1] neg_lo:[0,1,0]\n"
      "ds_write_addtid_b32 v246 offset:5984\n"
      "v_pk_fma_f16 v248, v215, v215, v248 op_sel:[1,1,0] op_sel_hi:[1,0,1] neg_lo:[0,1,0]\n"
      "ds_write_addtid_b32 v247 offset:6256\n"
      "ds_write_addtid_b32 v248 offset:2448\n"
      "v_pk_mul_f16 v249, v215, v212 op_sel_hi:[0,1]\n"
      "v_pk_mul_f16 v250, v215, v213 op_sel_hi:[0,1]\n"
      "v_pk_mul_f16 v251, v215, v214 op_sel_hi:[0,1]\n"
      "ds_write_addtid_b32 v214 offset:5712\n"
      "v_pk_fma_f16 v249, v215, v212, v249 op_sel:[1,1,0] op_sel_hi:[1,0,1] neg_lo:[0,1,0]\n"
      "ds_write_addtid_b32 v215 offset:4352\n"
      "v_pk_fma_f16 v250, v215, v213, v250 op_sel:[1,1,0] op_sel_hi:[1,0,1] neg_lo:[0,1,0]\n"
      "ds_write_addtid_b32 v249 offset:4624\n"
      "v_pk_fma_f16 v251, v215, v214, v251 op_sel:[1,1,0] op_sel_hi:[1,0,1] neg_lo:[0,1,0]\n"
      "ds_write_addtid_b32 v250 offset:4896\n"
      "ds_write_addtid_b32 v251 offset:5168\n"
      "ds_read_b128 v[72:75], v21 offset:0\n"
      "ds_read_b128 v[76:79], v21 offset:32\n"
      "ds_read_b128 v[80:83], v21 offset:64\n"
      "ds_read_b128 v[84:87], v21 offset:96\n"
      "ds_read_b128 v[88:91], v21 offset:128\n"
      "ds_read_b128 v[92:95], v21 offset:160\n"
      "ds_read_b128 v[96:99], v21 offset:192\n"
      "ds_read_b128 v[100:103], v21 offset:224\n"
      "ds_read_b128 v[120:123], v20 offset:64\n"
      "ds_read_b128 v[124:127], v178 offset:64\n"
      "s_waitcnt lgkmcnt(15)\n"
      "v_pk_mul_f16 v164, v104, v108 op_sel_hi:[0,1] neg_hi:[0,1]\n"
      "v_pk_mul_f16 v165, v105, v109 op_sel_hi:[0,1] neg_hi:[0,1]\n"
      "v_pk_mul_f16 v166, v106, v110 op_sel_hi:[0,1] neg_hi:[0,1]\n"
      "v_pk_mul_f16 v167, v107, v111 op_sel_hi:[0,1] neg_hi:[0,1]\n"
      "v_pk_fma_f16 v164, v104, v108, v164 op_sel:[1,1,0] op_sel_hi:[1,0,1]\n"
      "v_pk_fma_f16 v165, v105, v109, v165 op_sel:[1,1,0] op_sel_hi:[1,0,1]\n"
      "v_pk_fma_f16 v166, v106, v110, v166 op_sel:[1,1,0] op_sel_hi:[1,0,1]\n"
      "v_pk_fma_f16 v167, v107, v111, v167 op_sel:[1,1,0] op_sel_hi:[1,0,1]\n"
      "s_waitcnt lgkmcnt(9)\n"
      "s_nop 0\n"
      "v_mfma_f32_32x32x16_f16 v[180:195], v[72:75], v[164:167], 0\n"
      "v_pk_mul_f16 v240, v157, v156 op_sel_hi:[0,1]\n"
      "v_pk_mul_f16 v241, v158, v156 op_sel_hi:[0,1]\n"
      "v_pk_mul_f16 v242, v159, v156 op_sel_hi:[0,1]\n"
      "v_pk_mul_f16 v168, v112, v116 op_sel_hi:[0,1] neg_hi:[0,1]\n"
      "v_pk_mul_f16 v169, v113, v117 op_sel_hi:[0,1] neg_hi:[0,1]\n"
      "v_pk_mul_f16 v170, v114, v118 op_sel_hi:[0,1] neg_hi:[0,1]\n"
      "v_pk_mul_f16 v171, v115, v119 op_sel_hi:[0,1] neg_hi:[0,1]\n"
      "ds_write_addtid_b32 v156 offset:272\n"
      "v_pk_fma_f16 v240, v157, v156, v240 op_sel:[1,1,0] op_sel_hi:[1,0,1] neg_lo:[0,1,0]\n"
      "ds_write_addtid_b32 v157 offset:544\n"
      "v_pk_fma_f16 v168, v112, v116, v168 op_sel:[1,1,0] op_sel_hi:[1,0,1]\n"
      "v_pk_fma_f16 v169, v113, v117, v169 op_sel:[1,1,0] op_sel_hi:[1,0,1]\n"
      "v_pk_fma_f16 v170, v114, v118, v170 op_sel:[1,1,0] op_sel_hi:[1,0,1]\n"
      "v_pk_fma_f16 v171, v115, v119, v171 op_sel:[1,1,0] op_sel_hi:[1,0,1]\n"
      "ds_read_b128 v[104:107], v20 offset:96\n"
      "ds_read_b128 v[108:111], v178 offset:96\n"
      "s_waitcnt lgkmcnt(12)\n"
      "v_mfma_f32_32x32x16_f16 v[180:195], v[76:79], v[168:171], v[180:195]\n"
      "v_pk_fma_f16 v241, v158, v156, v241 op_sel:[1,1,0] op_sel_hi:[1,0,1] neg_lo:[0,1,0]\n"
      "ds_write_addtid_b32 v240 offset:816\n"
      "v_pk_fma_f16 v242, v159, v156, v242 op_sel:[1,1,0] op_sel_hi:[1,0,1] neg_lo:[0,1,0]\n"
      "s_waitcnt lgkmcnt(5)\n"
      "v_pk_mul_f16 v164, v120, v124 op_sel_hi:[0,1] neg_hi:[0,1]\n"
      "v_pk_mul_f16 v165, v121, v125 op_sel_hi:[0,1] neg_hi:[0,1]\n"
      "v_pk_mul_f16 v166, v122, v126 op_sel_hi:[0,1] neg_hi:[0,1]\n"
      "v_pk_mul_f16 v167, v123, v127 op_sel_hi:[0,1] neg_hi:[0,1]\n"
      "ds_write_addtid_b32 v241 offset:1360\n"
      "ds_write_addtid_b32 v242 offset:1904\n"
      "v_pk_mul_f16 v243, v160, v157 op_sel_hi:[0,1]\n"
      "v_pk_fma_f16 v164, v120, v124, v164 op_sel:[1,1,0] op_sel_hi:[1,0,1]\n"
      "v_pk_fma_f16 v165, v121, v125, v165 op_sel:[1,1,0] op_sel_hi:[1,0,1]\n"
      "v_pk_fma_f16 v166, v122, v126, v166 op_sel:[1,1,0] op_sel_hi:[1,0,1]\n"
      "v_pk_fma_f16 v167, v123, v127, v167 op_sel:[1,1,0] op_sel_hi:[1,0,1]\n"
      "ds_read_b128 v[112:115], v20 offset:128\n"
      "ds_read_b128 v[116:119], v178 offset:128\n"
      "v_mfma_f32_32x32x16_f16 v[180:195], v[80:83], v[164:167], v[180:195]\n"
      "v_pk_mul_f16 v244, v160, v158 op_sel_hi:[0,1]\n"
      "v_pk_mul_f16 v245, v160, v159 op_sel_hi:[0,1]\n"
      "ds_write_addtid_b32 v158 offset:1088\n"
      "s_waitcnt lgkmcnt(6)\n"
      "v_pk_mul_f16 v168, v104, v108 op_sel_hi:[0,1] neg_hi:[0,1]\n"
      "v_pk_mul_f16 v169, v105, v109 op_sel_hi:[0,1] neg_hi:[0,1]\n"
      "v_pk_mul_f16 v170, v106, v110 op_sel_hi:[0,1] neg_hi:[0,1]\n"
      "v_pk_mul_f16 v171, v107, v111 op_sel_hi:[0,1] neg_hi:[0,1]\n"
      "v_pk_fma_f16 v243, v160, v157, v243 op_sel:[1,1,0] op_sel_hi:[1,0,1] neg_lo:[0,1,0]\n"
      "ds_write_addtid_b32 v159 offset:1632\n"
      "v_pk_fma_f16 v244, v160, v158, v244 op_sel:[1,1,0] op_sel_hi:[1,0,1] neg_lo:[0,1,0]\n"
      "v_pk_fma_f16 v168, v104, v108, v168 op_sel:[1,1,0] op_sel_hi:[1,0,1]\n"
      "v_pk_fma_f16 v169, v105, v109, v169 op_sel:[1,1,0] op_sel_hi:[1,0,1]\n"
      "v_pk_fma_f16 v170, v106, v110, v170 op_sel:[1,1,0] op_sel_hi:[1,0,1]\n"
      "v_pk_fma_f16 v171, v107, v111, v171 op_sel:[1,1,0] op_sel_hi:[1,0,1]\n"
      "ds_read_b128 v[120:123], v20 offset:160\n"
      "ds_read_b128 v[124:127], v178 offset:160\n"
      "buffer_load_dwordx4 v[48:51], v41, s[20:23], s60 offen nt\n"
      "v_mfma_f32_32x32x16_f16 v[180:195], v[84:87], v[168:171], v[180:195]\n"
      "ds_write_addtid_b32 v243 offset:2992\n"
      "v_pk_fma_f16 v245, v160, v159, v245 op_sel:[1,1,0] op_sel_hi:[1,0,1] neg_lo:[0,1,0]\n"
      "ds_write_addtid_b32 v244 offset:3264\n"
      "s_waitcnt lgkmcnt(6)\n"
      "v_pk_mul_f16 v164, v112, v116 op_sel_hi:[0,1] neg_hi:[0,1]\n"
      "v_pk_mul_f16 v165, v113, v117 op_sel_hi:[0,1] neg_hi:[0,1]\n"
      "v_pk_mul_f16 v166, v114, v118 op_sel_hi:[0,1] neg_hi:[0,1]\n"
      "v_pk_mul_f16 v167, v115, v119 op_sel_hi:[0,1] neg_hi:[0,1]\n"
      "ds_write_addtid_b32 v245 offset:4080\n"
      "v_pk_mul_f16 v246, v161, v158 op_sel_hi:[0,1]\n"
      "v_pk_mul_f16 v247, v162, v158 op_sel_hi:[0,1]\n"
      "v_pk_fma_f16 v164, v112, v116, v164 op_sel:[1,1,0] op_sel_hi:[1,0,1]\n"
      "v_pk_fma_f16 v165, v113, v117, v165 op_sel:[1,1,0] op_sel_hi:[1,0,1]\n"
      "v_pk_fma_f16 v166, v114, v118, v166 op_sel:[1,1,0] op_sel_hi:[1,0,1]\n"
      "v_pk_fma_f16 v167, v115, v119, v167 op_sel:[1,1,0] op_sel_hi:[1,0,1]\n"
      "ds_read_b128 v[104:107], v20 offset:192\n"
      "ds_read_b128 v[108:111], v178 offset:192\n"
      "v_mfma_f32_32x32x16_f16 v[180:195], v[88:91], v[164:167], v[180:195]\n"
      "v_pk_mul_f16 v248, v163, v163 op_sel_hi:[0,1]\n"
      "ds_write_addtid_b32 v160 offset:2176\n"
      "v_pk_fma_f16 v246, v161, v158, v246 op_sel:[1,1,0] op_sel_hi:[1,0,1] neg_lo:[0,1,0]\n"
      "s_waitcnt lgkmcnt(6)\n"
      "v_pk_mul_f16 v168, v120, v124 op_sel_hi:[0,1] neg_hi:[0,1]\n"
      "v_pk_mul_f16 v169, v121, v125 op_sel_hi:[0,1] neg_hi:[0,1]\n"
      "v_pk_mul_f16 v170, v122, v126 op_sel_hi:[0,1] neg_hi:[0,1]\n"
      "v_pk_mul_f16 v171, v123, v127 op_sel_hi:[0,1] neg_hi:[0,1]\n"
      "ds_write_addtid_b32 v161 offset:5440\n"
      "v_pk_fma_f16 v247, v162, v158, v247 op_sel:[1,1,0] op_sel_hi:[1,0,1] neg_lo:[0,1,0]\n"
      "ds_write_addtid_b32 v246 offset:5984\n"
      "v_pk_fma_f16 v168, v120, v124, v168 op_sel:[1,1,0] op_sel_hi:[1,0,1]\n"
      "v_pk_fma_f16 v169, v121, v125, v169 op_sel:[1,1,0] op_sel_hi:[1,0,1]\n"
      "v_pk_fma_f16 v170, v122, v126, v170 op_sel:[1,1,0] op_sel_hi:[1,0,1]\n"
      "v_pk_fma_f16 v171, v123, v127, v171 op_sel:[1,1,0] op_sel_hi:[1,0,1]\n"
      "ds_read_b128 v[112:115], v20 offset:224\n"
      "ds_read_b128 v[116:119], v178 offset:224\n"
      "v_mfma_f32_32x32x16_f16 v[180:195], v[92:95], v[168:171], v[180:195]\n"
      "v_pk_fma_f16 v248, v163, v163, v248 op_sel:[1,1,0] op_sel_hi:[1,0,1] neg_lo:[0,1,0]\n"
      "ds_write_addtid_b32 v247 offset:6256\n"
      "ds_write_addtid_b32 v248 offset:2448\n"
      "s_waitcnt lgkmcnt(7)\n"
      "v_pk_mul_f16 v164, v104, v108 op_sel_hi:[0,1] neg_hi:[0,1]\n"
      "v_pk_mul_f16 v165, v105, v109 op_sel_hi:[0,1] neg_hi:[0,1]\n"
      "v_pk_mul_f16 v166, v106, v110 op_sel_hi:[0,1] neg_hi:[0,1]\n"
      "v_pk_mul_f16 v167, v107, v111 op_sel_hi:[0,1] neg_hi:[0,1]\n"
      "v_pk_mul_f16 v249, v163, v160 op_sel_hi:[0,1]\n"
      "v_pk_mul_f16 v250, v163, v161 op_sel_hi:[0,1]\n"
      "v_pk_mul_f16 v251, v163, v162 op_sel_hi:[0,1]\n"
      "v_pk_fma_f16 v164, v104, v108, v164 op_sel:[1,1,0] op_sel_hi:[1,0,1]\n"
      "v_pk_fma_f16 v165, v105, v109, v165 op_sel:[1,1,0] op_sel_hi:[1,0,1]\n"
      "v_pk_fma_f16 v166, v106, v110, v166 op_sel:[1,1,0] op_sel_hi:[1,0,1]\n"
      "v_pk_fma_f16 v167, v107, v111, v167 op_sel:[1,1,0] op_sel_hi:[1,0,1]\n"
      "ds_read_b128 v[120:123], v20 offset:3264\n"
      "ds_read_b128 v[124:127], v178 offset:3264\n"
      "v_mfma_f32_32x32x16_f16 v[180:195], v[96:99], v[164:167], v[180:195]\n"
      "ds_write_addtid_b32 v162 offset:5712\n"
      "v_pk_fma_f16 v249, v163, v160, v249 op_sel:[1,1,0] op_sel_hi:[1,0,1] neg_lo:[0,1,0]\n"
      "ds_write_addtid_b32 v163 offset:4352\n"
      "s_waitcnt lgkmcnt(6)\n"
      "v_pk_mul_f16 v168, v112, v116 op_sel_hi:[0,1] neg_hi:[0,1]\n"
      "v_pk_mul_f16 v169, v113, v117 op_sel_hi:[0,1] neg_hi:[0,1]\n"
      "v_pk_mul_f16 v170, v114, v118 op_sel_hi:[0,1] neg_hi:[0,1]\n"
      "v_pk_mul_f16 v171, v115, v119 op_sel_hi:[0,1] neg_hi:[0,1]\n"
      "v_pk_fma_f16 v250, v163, v161, v250 op_sel:[1,1,0] op_sel_hi:[1,0,1] neg_lo:[0,1,0]\n"
      "ds_write_addtid_b32 v249 offset:4624\n"
      "v_pk_fma_f16 v251, v163, v162, v251 op_sel:[1,1,0] op_sel_hi:[1,0,1] neg_lo:[0,1,0]\n"
      "v_pk_fma_f16 v168, v112, v116, v168 op_sel:[1,1,0] op_sel_hi:[1,0,1]\n"
      "v_pk_fma_f16 v169, v113, v117, v169 op_sel:[1,1,0] op_sel_hi:[1,0,1]\n"
      "v_pk_fma_f16 v170, v114, v118, v170 op_sel:[1,1,0] op_sel_hi:[1,0,1]\n"
      "v_pk_fma_f16 v171, v115, v119, v171 op_sel:[1,1,0] op_sel_hi:[1,0,1]\n"
      "ds_read_b128 v[104:107], v20 offset:3296\n"
      "ds_read_b128 v[108:111], v178 offset:3296\n"
      "v_mfma_f32_32x32x16_f16 v[180:195], v[100:103], v[168:171], v[180:195]\n"
      "ds_write_addtid_b32 v250 offset:4896\n"
      "ds_write_addtid_b32 v251 offset:5168\n"
      "s_waitcnt lgkmcnt(7)\n"
      "v_pk_mul_f16 v164, v120, v124 op_sel_hi:[0,1] neg_hi:[0,1]\n"
      "v_pk_mul_f16 v165, v121, v125 op_sel_hi:[0,1] neg_hi:[0,1]\n"
      "v_pk_mul_f16 v166, v122, v126 op_sel_hi:[0,1] neg_hi:[0,1]\n"
      "v_pk_mul_f16 v167, v123, v127 op_sel_hi:[0,1] neg_hi:[0,1]\n"
      "ds_read_b128 v[72:75], v21 offset:0\n"
      "ds_read_b128 v[76:79], v21 offset:32\n"
      "ds_read_b128 v[80:83], v21 offset:64\n"
      "ds_read_b128 v[84:87], v21 offset:96\n"
      "ds_read_b128 v[88:91], v21 offset:128\n"
      "ds_read_b128 v[92:95], v21 offset:160\n"
      "ds_read_b128 v[96:99], v21 offset:192\n"
      "ds_read_b128 v[100:103], v21 offset:224\n"
      "v_pk_fma_f16 v164, v120, v124, v164 op_sel:[1,1,0] op_sel_hi:[1,0,1]\n"
      "v_pk_fma_f16 v165, v121, v125, v165 op_sel:[1,1,0] op_sel_hi:[1,0,1]\n"
      "v_pk_fma_f16 v166, v122, v126, v166 op_sel:[1,1,0] op_sel_hi:[1,0,1]\n"
      "v_pk_fma_f16 v167, v123, v127, v167 op_sel:[1,1,0] op_sel_hi:[1,0,1]\n"
      "ds_read_b128 v[112:115], v20 offset:3328\n"
      "ds_read_b128 v[116:119], v178 offset:3328\n"
      "s_setprio 1\n"
      "s_waitcnt lgkmcnt(9)\n"
      "v_mfma_f32_32x32x16_f16 v[180:195], v[72:75], v[164:167], v[180:195]\n"
      "v_pk_mul_f16 v168, v104, v108 op_sel_hi:[0,1] neg_hi:[0,1]\n"
      "v_pk_mul_f16 v169, v105, v109 op_sel_hi:[0,1] neg_hi:[0,1]\n"
      "v_pk_mul_f16 v170, v106, v110 op_sel_hi:[0,1] neg_hi:[0,1]\n"
      "v_pk_mul_f16 v171, v107, v111 op_sel_hi:[0,1] neg_hi:[0,1]\n"
      "v_pk_fma_f16 v168, v104, v108, v168 op_sel:[1,1,0] op_sel_hi:[1,0,1]\n"
      "v_pk_fma_f16 v169, v105, v109, v169 op_sel:[1,1,0] op_sel_hi:[1,0,1]\n"
      "v_pk_fma_f16 v170, v106, v110, v170 op_sel:[1,1,0] op_sel_hi:[1,0,1]\n"
      "v_pk_fma_f16 v171, v107, v111, v171 op_sel:[1,1,0] op_sel_hi:[1,0,1]\n"
      "ds_read_b128 v[120:123], v20 offset:3360\n"
      "ds_read_b128 v[124:127], v178 offset:3360\n"
      "buffer_load_dwordx4 v[52:55], v41, s[20:23], s60 offen offset:2048 nt\n"
      "s_waitcnt lgkmcnt(10)\n"
      "v_mfma_f32_32x32x16_f16 v[180:195], v[76:79], v[168:171], v[180:195]\n"
      "s_waitcnt lgkmcnt(2)\n"
      "v_pk_mul_f16 v164, v112, v116 op_sel_hi:[0,1] neg_hi:[0,1]\n"
      "v_pk_mul_f16 v165, v113, v117 op_sel_hi:[0,1] neg_hi:[0,1]\n"
      "v_pk_mul_f16 v166, v114, v118 op_sel_hi:[0,1] neg_hi:[0,1]\n"
      "v_pk_mul_f16 v167, v115, v119 op_sel_hi:[0,1] neg_hi:[0,1]\n"
      "v_pk_fma_f16 v164, v112, v116, v164 op_sel:[1,1,0] op_sel_hi:[1,0,1]\n"
      "v_pk_fma_f16 v165, v113, v117, v165 op_sel:[1,1,0] op_sel_hi:[1,0,1]\n"
      "v_pk_fma_f16 v166, v114, v118, v166 op_sel:[1,1,0] op_sel_hi:[1,0,1]\n"
      "v_pk_fma_f16 v167, v115, v119, v167 op_sel:[1,1,0] op_sel_hi:[1,0,1]\n"
      "ds_read_b128 v[104:107], v20 offset:3392\n"
      "ds_read_b128 v[108:111], v178 offset:3392\n"
      "v_mfma_f32_32x32x16_f16 v[180:195], v[80:83], v[164:167], v[180:195]\n"
      "s_waitcnt lgkmcnt(2)\n"
      "v_pk_mul_f16 v168, v120, v124 op_sel_hi:[0,1] neg_hi:[0,1]\n"
      "v_pk_mul_f16 v169, v121, v125 op_sel_hi:[0,1] neg_hi:[0,1]\n"
      "v_pk_mul_f16 v170, v122, v126 op_sel_hi:[0,1] neg_hi:[0,1]\n"
      "v_pk_mul_f16 v171, v123, v127 op_sel_hi:[0,1] neg_hi:[0,1]\n"
      "v_pk_fma_f16 v168, v120, v124, v168 op_sel:[1,1,0] op_sel_hi:[1,0,1]\n"
      "v_pk_fma_f16 v169, v121, v125, v169 op_sel:[1,1,0] op_sel_hi:[1,0,1]\n"
      "v_pk_fma_f16 v170, v122, v126, v170 op_sel:[1,1,0] op_sel_hi:[1,0,1]\n"
      "v_pk_fma_f16 v171, v123, v127, v171 op_sel:[1,1,0] op_sel_hi:[1,0,1]\n"
      "ds_read_b128 v[112:115], v20 offset:3424\n"
      "ds_read_b128 v[116:119], v178 offset:3424\n"
      "v_mfma_f32_32x32x16_f16 v[180:195], v[84:87], v[168:171], v[180:195]\n"
      "s_waitcnt lgkmcnt(2)\n"
      "v_pk_mul_f16 v164, v104, v108 op_sel_hi:[0,1] neg_hi:[0,1]\n"
      "v_pk_mul_f16 v165, v105, v109 op_sel_hi:[0,1] neg_hi:[0,1]\n"
      "v_pk_mul_f16 v166, v106, v110 op_sel_hi:[0,1] neg_hi:[0,1]\n"
      "v_pk_mul_f16 v167, v107, v111 op_sel_hi:[0,1] neg_hi:[0,1]\n"
      "v_pk_fma_f16 v164, v104, v108, v164 op_sel:[1,1,0] op_sel_hi:[1,0,1]\n"
      "v_pk_fma_f16 v165, v105, v109, v165 op_sel:[1,1,0] op_sel_hi:[1,0,1]\n"
      "v_pk_fma_f16 v166, v106, v110, v166 op_sel:[1,1,0] op_sel_hi:[1,0,1]\n"
      "v_pk_fma_f16 v167, v107, v111, v167 op_sel:[1,1,0] op_sel_hi:[1,0,1]\n"
      "ds_read_b128 v[120:123], v20 offset:3456\n"
      "ds_read_b128 v[124:127], v178 offset:3456\n"
      "v_mfma_f32_32x32x16_f16 v[180:195], v[88:91], v[164:167], v[180:195]\n"
      "s_waitcnt lgkmcnt(2)\n"
      "v_pk_mul_f16 v168, v112, v116 op_sel_hi:[0,1] neg_hi:[0,1]\n"
      "v_pk_mul_f16 v169, v113, v117 op_sel_hi:[0,1] neg_hi:[0,1]\n"
      "v_pk_mul_f16 v170, v114, v118 op_sel_hi:[0,1] neg_hi:[0,1]\n"
      "v_pk_mul_f16 v171, v115, v119 op_sel_hi:[0,1] neg_hi:[0,1]\n"
      "v_pk_fma_f16 v168, v112, v116, v168 op_sel:[1,1,0] op_sel_hi:[1,0,1]\n"
      "v_pk_fma_f16 v169, v113, v117, v169 op_sel:[1,1,0] op_sel_hi:[1,0,1]\n"
      "v_pk_fma_f16 v170, v114, v118, v170 op_sel:[1,1,0] op_sel_hi:[1,0,1]\n"
      "v_pk_fma_f16 v171, v115, v119, v171 op_sel:[1,1,0] op_sel_hi:[1,0,1]\n"
      "ds_read_b128 v[104:107], v20 offset:3488\n"
      "ds_read_b128 v[108:111], v178 offset:3488\n"
      "v_mov_b32 v140, 0\n"
      "v_mov_b32 v141, 0\n"
      "v_mov_b32 v142, 0\n"
      "v_mov_b32 v143, 0\n"
      "ds_write_b128 v27, v[140:143] \n"
      "ds_write_b32 v28, v140 \n"
      "v_mfma_f32_32x32x16_f16 v[180:195], v[92:95], v[168:171], v[180:195]\n"
      "s_waitcnt lgkmcnt(2)\n"
      "v_pk_mul_f16 v164, v120, v124 op_sel_hi:[0,1] neg_hi:[0,1]\n"
      "v_pk_mul_f16 v165, v121, v125 op_sel_hi:[0,1] neg_hi:[0,1]\n"
      "v_pk_mul_f16 v166, v122, v126 op_sel_hi:[0,1] neg_hi:[0,1]\n"
      "v_pk_mul_f16 v167, v123, v127 op_sel_hi:[0,1] neg_hi:[0,1]\n"
      "v_pk_fma_f16 v164, v120, v124, v164 op_sel:[1,1,0] op_sel_hi:[1,0,1]\n"
      "v_pk_fma_f16 v165, v121, v125, v165 op_sel:[1,1,0] op_sel_hi:[1,0,1]\n"
      "v_pk_fma_f16 v166, v122, v126, v166 op_sel:[1,1,0] op_sel_hi:[1,0,1]\n"
      "v_pk_fma_f16 v167, v123, v127, v167 op_sel:[1,1,0] op_sel_hi:[1,0,1]\n"
      "s_nop 1\n"
      "v_mfma_f32_32x32x16_f16 v[180:195], v[96:99], v[164:167], v[180:195]\n"
      "s_waitcnt lgkmcnt(0)\n"
      "v_pk_mul_f16 v168, v104, v108 op_sel_hi:[0,1] neg_hi:[0,1]\n"
      "v_pk_mul_f16 v169, v105, v109 op_sel_hi:[0,1] neg_hi:[0,1]\n"
      "v_pk_mul_f16 v170, v106, v110 op_sel_hi:[0,1] neg_hi:[0,1]\n"
      "v_pk_mul_f16 v171, v107, v111 op_sel_hi:[0,1] neg_hi:[0,1]\n"
      "v_pk_fma_f16 v168, v104, v108, v168 op_sel:[1,1,0] op_sel_hi:[1,0,1]\n"
      "v_pk_fma_f16 v169, v105, v109, v169 op_sel:[1,1,0] op_sel_hi:[1,0,1]\n"
      "v_pk_fma_f16 v170, v106, v110, v170 op_sel:[1,1,0] op_sel_hi:[1,0,1]\n"
      "v_pk_fma_f16 v171, v107, v111, v171 op_sel:[1,1,0] op_sel_hi:[1,0,1]\n"
      "s_nop 1\n"
      "v_mfma_f32_32x32x16_f16 v[180:195], v[100:103], v[168:171], v[180:195]\n"
      "s_cmp_lt_u32 s33, 4\n"
      "s_cbranch_scc1 .Lct2%=\n"
      "buffer_load_dword v11, v15, s[20:23], s60 offen\n"
      ".Lct2%=:\n"
      "s_nop 10\n"
      "v_add_f32 v180, v180, v31\n"
      "buffer_load_dwordx4 v[64:67], v42, s[16:19], s62 offen sc1\n"
      "v_cvt_pk_f16_f32 v222, v187, v186\n"
      "v_cvt_pk_f16_f32 v223, v185, v184\n"
      "v_cvt_pk_f16_f32 v225, v186, v185\n"
      "v_cvt_pk_f16_f32 v226, v188, v189\n"
      "ds_write_b64 v22, v[222:223] offset:2110\n"
      "v_cvt_pk_f16_f32 v228, v192, v192\n"
      "ds_write_b16 v22, v222 \n"
      "v_cvt_pk_f16_f32 v220, v183, v182\n"
      "ds_write_b32 v22, v225 offset:2\n"
      "v_cvt_pk_f16_f32 v221, v181, v180\n"
      "ds_write_b16_d16_hi v22, v223 offset:6\n"
      "v_cvt_pk_f16_f32 v224, v182, v181\n"
      "v_readlane_b32 s64, v226, 0\n"
      "v_readlane_b32 s65, v226, 32\n"
      "v_readlane_b32 s66, v228, 0\n"
      "ds_write_b64 v22, v[220:221] offset:510\n"
      "v_cvt_pk_f16_f32 v227, v190, v191\n"
      "ds_write_b16 v22, v220 offset:2620\n"
      "v_mov_b32 v229, s65\n"
      "ds_write_b32 v22, v224 offset:2622\n"
      "v_writelane_b32 v229, s64, 8\n"
      "v_writelane_b32 v229, s64, 9\n"
      "ds_write_b16_d16_hi v22, v221 offset:2626\n"
      "v_writelane_b32 v229, s66, 10\n"
      "v_writelane_b32 v229, s66, 11\n"
      "s_pack_ll_b32_b16 s64, s64, s64\n"
      "ds_write_b16 v26, v229 \n"
      "v_add_u32 v230, 0x240, v29\n"
      "s_setprio 0\n"
      "v_and_b32 v44, s64, v44\n"
      "v_and_b32 v45, s64, v45\n"
      "v_and_b32 v46, s64, v46\n"
      "v_and_b32 v47, s64, v47\n"
      "ds_write_b16 v23, v226 \n"
      "ds_write_b16 v23, v226 offset:2110\n"
      "ds_write_b16_d16_hi v24, v226 \n"
      "ds_write_b16_d16_hi v24, v226 offset:2110\n"
      "ds_write_b16 v25, v227 offset:32\n"
      "ds_write_b16 v25, v227 offset:2142\n"
      "ds_write_b16_d16_hi v25, v227 \n"
      "ds_write_b16_d16_hi v25, v227 offset:2110\n"
      "ds_read2_b32 v[108:109], v230 offset0:0 offset1:1\n"
      "ds_read2_b32 v[110:111], v230 offset0:2 offset1:3\n"
      "ds_read2_b32 v[76:77], v29 offset0:16 offset1:17\n"
      "ds_read2_b32 v[78:79], v29 offset0:18 offset1:19\n"
      "ds_read2_b32 v[112:113], v230 offset0:16 offset1:17\n"
      "ds_read2_b32 v[114:115], v230 offset0:18 offset1:19\n"
      "ds_read2_b32 v[80:81], v29 offset0:32 offset1:33\n"
      "ds_read2_b32 v[82:83], v29 offset0:34 offset1:35\n"
      "ds_read2_b32 v[116:117], v230 offset0:32 offset1:33\n"
      "ds_read2_b32 v[118:119], v230 offset0:34 offset1:35\n"
      "ds_read2_b32 v[84:85], v29 offset0:48 offset1:49\n"
      "ds_read2_b32 v[86:87], v29 offset0:50 offset1:51\n"
      "s_movk_i32 s59, 0x7fff\n"
      "s_waitcnt vmcnt(0)\n"
      "s_nop 0\n"
      ".Lcsgk%=:\n"
      "v_cmp_ne_u32 s[64:65], s15, v65\n"
      "v_cmp_ne_u32 vcc, s15, v67\n"
      "s_nop 0\n"
      "s_or_b64 vcc, vcc, s[64:65]\n"
      "s_cbranch_scc1 .Lcsgsk%=\n"
      ".Lcsgok%=:\n"
      "v_and_b32 v196, 0xffff0000, v64\n"
      "v_lshlrev_b32 v198, 16, v64\n"
      "v_rcp_f32 v196, v196\n"
      "v_and_b32 v197, 0xffff0000, v66\n"
      "v_lshlrev_b32 v199, 16, v66\n"
      "v_rcp_f32 v197, v197\n"
      "v_mul_f32 v198, -v198, v196\n"
      "v_mul_f32 v199, -v199, v197\n"
      "ds_read2_b32 v[120:121], v230 offset0:48 offset1:49\n"
      "ds_read2_b32 v[122:123], v230 offset0:50 offset1:51\n"
      "v_lshlrev_b32 v204, 16, v48\n"
      "v_lshlrev_b32 v205, 16, v52\n"
      "v_and_b32 v206, 0xffff0000, v48\n"
      "v_and_b32 v207, 0xffff0000, v52\n"
      "v_pk_fma_f32 v[204:205], v[204:205], v[196:197], v[198:199]\n"
      "v_pk_fma_f32 v[206:207], v[206:207], v[196:197], v[198:199]\n"
      "v_cvt_pk_f16_f32 v204, v204, v205\n"
      "v_cvt_pk_f16_f32 v206, v206, v207\n"
      "ds_read2_b32 v[88:89], v29 offset0:64 offset1:65\n"
      "ds_read2_b32 v[90:91], v29 offset0:66 offset1:67\n"
      "ds_write2st64_b32 v32, v204, v206 offset0:0 offset1:19\n"
      "v_lshlrev_b32 v208, 16, v49\n"
      "v_lshlrev_b32 v209, 16, v53\n"
      "v_and_b32 v210, 0xffff0000, v49\n"
      "v_and_b32 v211, 0xffff0000, v53\n"
      "v_pk_fma_f32 v[208:209], v[208:209], v[196:197], v[198:199]\n"
      "v_pk_fma_f32 v[210:211], v[210:211], v[196:197], v[198:199]\n"
      "v_cvt_pk_f16_f32 v208, v208, v209\n"
      "ds_read2_b32 v[124:125], v230 offset0:64 offset1:65\n"
      "ds_read2_b32 v[126:127], v230 offset0:66 offset1:67\n"
      "v_cvt_pk_f16_f32 v210, v210, v211\n"
      "ds_write2st64_b32 v32, v208, v210 offset0:38 offset1:57\n"
      "v_lshlrev_b32 v204, 16, v50\n"
      "v_lshlrev_b32 v205, 16, v54\n"
      "v_and_b32 v206, 0xffff0000, v50\n"
      "v_and_b32 v207, 0xffff0000, v54\n"
      "v_pk_fma_f32 v[204:205], v[204:205], v[196:197], v[198:199]\n"
      "v_pk_fma_f32 v[206:207], v[206:207], v[196:197], v[198:199]\n"
      "ds_read2_b32 v[92:93], v29 offset0:80 offset1:81\n"
      "ds_read2_b32 v[94:95], v29 offset0:82 offset1:83\n"
      "v_cvt_pk_f16_f32 v204, v204, v205\n"
      "v_cvt_pk_f16_f32 v206, v206, v207\n"
      "ds_write2st64_b32 v32, v204, v206 offset0:76 offset1:95\n"
      "v_lshlrev_b32 v208, 16, v51\n"
      "v_lshlrev_b32 v209, 16, v55\n"
      "v_and_b32 v210, 0xffff0000, v51\n"
      "v_and_b32 v211, 0xffff0000, v55\n"
      "v_pk_fma_f32 v[208:209], v[208:209], v[196:197], v[198:199]\n"
      "ds_read2_b32 v[128:129], v230 offset0:80 offset1:81\n"
      "ds_read2_b32 v[130:131], v230 offset0:82 offset1:83\n"
      "v_pk_fma_f32 v[210:211], v[210:211], v[196:197], v[198:199]\n"
      "v_cvt_pk_f16_f32 v208, v208, v209\n"
      "v_cvt_pk_f16_f32 v210, v210, v211\n"
      "ds_write2st64_b32 v32, v208, v210 offset0:114 offset1:133\n"
      "s_mov_b32 s54, s41\n"
      "s_add_u32 s55, s41, 0x100000\n"
      "s_add_u32 s56, s41, 0x200000\n"
      "s_add_u32 s57, s41, 0x300000\n"
      "ds_read2_b32 v[96:97], v29 offset0:96 offset1:97\n"
      "ds_read2_b32 v[98:99], v29 offset0:98 offset1:99\n"
      "s_mov_b32 s58, 0x40000000\n"
      "v_lshrrev_b32 v14, 9, v8\n"
      "v_add_u32 v14, 0x27600, v14\n"
      "s_lshr_b32 s13, s33, 1\n"
      "ds_read2_b32 v[132:133], v230 offset0:96 offset1:97\n"
      "ds_read2_b32 v[134:135], v230 offset0:98 offset1:99\n"
      "ds_read2_b32 v[100:101], v29 offset0:112 offset1:113\n"
      "ds_read2_b32 v[102:103], v29 offset0:114 offset1:115\n"
      "ds_read2_b32 v[136:137], v230 offset0:112 offset1:113\n"
      "ds_read2_b32 v[138:139], v230 offset0:114 offset1:115\n"
      "ds_read2_b32 v[104:105], v29 offset0:128 offset1:129\n"
      "ds_read2_b32 v[106:107], v29 offset0:130 offset1:131\n"
      "ds_read2_b32 v[72:73], v29 offset0:0 offset1:1\n"
      "ds_read2_b32 v[74:75], v29 offset0:2 offset1:3\n"
      "s_waitcnt lgkmcnt(12)\n"
      ".Lccv%=:\n"
      "s_barrier\n"
      "s_setprio 3\n"
      "ds_read_b128 v[216:219], v33 offset:64\n"
      "ds_read_b128 v[220:223], v33 offset:128\n"
      "ds_read_b128 v[224:227], v33 offset:192\n"
      "ds_read_b128 v[212:215], v33 offset:256\n"
      "v_permlane32_swap_b32 v48, v52\n"
      "v_permlane32_swap_b32 v49, v53\n"
      "v_permlane32_swap_b32 v50, v54\n"
      "v_permlane32_swap_b32 v51, v55\n"
      "s_cmp_lg_u32 s33, 0\n"
      "s_cbranch_scc1 .Lca%=\n"
      "v_mov_b32 v16, s15\n"
      "v_mov_b32 v17, s15\n"
      "buffer_store_dwordx2 v[16:17], off, s[16:19], s42 sc1\n"
      ".Lca%=:\n"
      "s_waitcnt lgkmcnt(2)\n"
      "v_mfma_f32_16x16x32_f16 v[180:183], v[108:111], v[216:219], 0\n"
      "v_lshlrev_b32 v29, 2, v43\n"
      "v_add_u32 v29, 0x27600, v29\n"
      "ds_read_b32 v255, v29 \n"
      "ds_read_b128 v[236:239], v34 offset:0\n"
      "ds_read_b128 v[240:243], v34 offset:16\n"
      "v_mfma_f32_16x16x32_f16 v[184:187], v[76:79], v[216:219], 0\n"
      "ds_read2_b32 v[140:141], v30 offset0:16 offset1:17\n"
      "ds_read_b128 v[216:219], v33 offset:320\n"
      "v_mfma_f32_16x16x32_f16 v[180:183], v[112:115], v[220:223], v[180:183]\n"
      "v_mfma_f32_16x16x32_f16 v[184:187], v[80:83], v[220:223], v[184:187]\n"
      "ds_read2_b32 v[142:143], v30 offset0:64 offset1:65\n"
      "ds_read_b128 v[220:223], v33 offset:384\n"
      "s_waitcnt lgkmcnt(7)\n"
      "v_mfma_f32_16x16x32_f16 v[180:183], v[116:119], v[224:227], v[180:183]\n"
      "s_waitcnt lgkmcnt(5)\n"
      "v_alignbit_b32 v245, v238, v237, 16\n"
      "v_alignbit_b32 v246, v239, v238, 16\n"
      "v_mfma_f32_16x16x32_f16 v[184:187], v[84:87], v[224:227], v[184:187]\n"
      "ds_read2_b32 v[144:145], v30 offset0:8 offset1:9\n"
      "ds_read_b128 v[224:227], v33 offset:448\n"
      "s_waitcnt lgkmcnt(4)\n"
      "v_alignbit_b32 v247, v240, v239, 16\n"
      "v_alignbit_b32 v248, v241, v240, 16\n"
      "v_mfma_f32_16x16x32_f16 v[180:183], v[120:123], v[212:215], v[180:183]\n"
      "v_alignbit_b32 v249, v242, v241, 16\n"
      "v_pk_add_f16 v16, v245, v246\n"
      "v_mfma_f32_16x16x32_f16 v[184:187], v[88:91], v[212:215], v[184:187]\n"
      "ds_read2_b32 v[146:147], v30 offset0:56 offset1:57\n"
      "ds_read_b128 v[212:215], v33 offset:512\n"
      "v_pk_add_f16 v17, v246, v247\n"
      "v_pk_add_f16 v18, v247, v248\n"
      "v_mfma_f32_16x16x32_f16 v[180:183], v[124:127], v[216:219], v[180:183]\n"
      "v_pk_add_f16 v19, v248, v249\n"
      "v_pk_add_f16 v20, v16, v238\n"
      "v_mfma_f32_16x16x32_f16 v[184:187], v[92:95], v[216:219], v[184:187]\n"
      "ds_read2_b32 v[148:149], v30 offset0:0 offset1:1\n"
      "v_pk_add_f16 v21, v17, v239\n"
      "v_pk_add_f16 v22, v18, v240\n"
      "s_waitcnt lgkmcnt(3)\n"
      "v_mfma_f32_16x16x32_f16 v[180:183], v[128:131], v[220:223], v[180:183]\n"
      "v_pk_add_f16 v23, v19, v241\n"
      "ds_write_b128 v37, v[20:23] offset:0\n"
      "v_pk_add_f16 v16, v237, v239\n"
      "v_mfma_f32_16x16x32_f16 v[184:187], v[96:99], v[220:223], v[184:187]\n"
      "ds_read2_b32 v[150:151], v30 offset0:48 offset1:49\n"
      "ds_read_b128 v[216:219], v33 offset:576\n"
      "v_pk_add_f16 v17, v238, v240\n"
      "v_pk_add_f16 v18, v239, v241\n"
      "v_mfma_f32_16x16x32_f16 v[180:183], v[132:135], v[224:227], v[180:183]\n"
      "v_pk_add_f16 v19, v240, v242\n"
      "v_pk_add_f16 v20, v20, v16\n"
      "v_mfma_f32_16x16x32_f16 v[184:187], v[100:103], v[224:227], v[184:187]\n"
      "ds_read2_b32 v[152:153], v30 offset0:40 offset1:41\n"
      "ds_read_b128 v[220:223], v33 offset:640\n"
      "v_pk_add_f16 v21, v21, v17\n"
      "v_pk_add_f16 v22, v22, v18\n"
      "s_waitcnt lgkmcnt(4)\n"
      "v_mfma_f32_16x16x32_f16 v[180:183], v[136:139], v[212:215], v[180:183]\n"
      "v_pk_add_f16 v23, v23, v19\n"
      "ds_write_b128 v37, v[20:23] offset:3104\n"
      "v_alignbit_b32 v244, v237, v236, 16\n"
      "v_mfma_f32_16x16x32_f16 v[184:187], v[104:107], v[212:215], v[184:187]\n"
      "ds_read2_b32 v[154:155], v30 offset0:88 offset1:89\n"
      "ds_read_b128 v[224:227], v33 offset:704\n"
      "v_alignbit_b32 v250, v243, v242, 16\n"
      "v_pk_add_f16 v16, v244, v247\n"
      "v_mfma_f32_16x16x32_f16 v[188:191], v[72:75], v[212:215], 0\n"
      "v_pk_add_f16 v17, v245, v248\n"
      "v_pk_add_f16 v18, v246, v249\n"
      "v_mfma_f32_16x16x32_f16 v[196:199], v[44:47], v[212:215], 0\n"
      "ds_read2_b32 v[156:157], v30 offset0:32 offset1:33\n"
      "ds_read_b128 v[212:215], v33 offset:768\n"
      "v_pk_add_f16 v19, v247, v250\n"
      "v_pk_add_f16 v20, v20, v16\n"
      "s_waitcnt lgkmcnt(4)\n"
      "v_mfma_f32_16x16x32_f16 v[184:187], v[108:111], v[216:219], v[184:187]\n"
      "v_cvt_pk_bf16_f32 v24, v180, v182\n"
      "v_cvt_pk_bf16_f32 v25, v181, v183\n"
      "ds_write2st64_b32 v39, v24, v25 offset0:0 offset1:8\n"
      "v_mfma_f32_16x16x32_f16 v[188:191], v[76:79], v[216:219], v[188:191]\n"
      "ds_read2_b32 v[158:159], v30 offset0:80 offset1:81\n"
      "ds_read_b128 v[216:219], v33 offset:832\n"
      "v_pk_add_f16 v21, v21, v17\n"
      "v_pk_add_f16 v22, v22, v18\n"
      "v_mfma_f32_16x16x32_f16 v[184:187], v[112:115], v[220:223], v[184:187]\n"
      "buffer_load_dwordx4 v[56:59], v41, s[20:23], s61 offen nt\n"
      "buffer_load_dwordx4 v[68:71], v42, s[16:19], s63 offen sc1\n"
      "v_pk_add_f16 v23, v23, v19\n"
      "ds_write_b128 v38, v[20:23] offset:0\n"
      "v_pk_add_f16 v16, v236, v240\n"
      "v_mfma_f32_16x16x32_f16 v[188:191], v[80:83], v[220:223], v[188:191]\n"
      "ds_read2_b32 v[160:161], v30 offset0:24 offset1:25\n"
      "ds_read_b128 v[220:223], v33 offset:896\n"
      "v_pk_add_f16 v17, v237, v241\n"
      "v_pk_add_f16 v18, v238, v242\n"
      "s_waitcnt lgkmcnt(3)\n"
      "v_mfma_f32_16x16x32_f16 v[184:187], v[116:119], v[224:227], v[184:187]\n"
      "v_pk_add_f16 v19, v239, v243\n"
      "v_pk_add_f16 v20, v20, v16\n"
      "s_setprio 2\n"
      "v_mfma_f32_16x16x32_f16 v[188:191], v[84:87], v[224:227], v[188:191]\n"
      "ds_read2_b32 v[162:163], v30 offset0:72 offset1:73\n"
      "ds_read_b128 v[224:227], v33 offset:960\n"
      "v_pk_add_f16 v21, v21, v17\n"
      "v_pk_add_f16 v22, v22, v18\n"
      "ds_read_b128 v[236:239], v34 offset:1024\n"
      "ds_read_b128 v[240:243], v34 offset:1040\n"
      "v_mfma_f32_16x16x32_f16 v[184:187], v[120:123], v[212:215], v[184:187]\n"
      "v_pk_add_f16 v23, v23, v19\n"
      "ds_write_b128 v38, v[20:23] offset:2080\n"
      "v_mfma_f32_16x16x32_f16 v[188:191], v[88:91], v[212:215], v[188:191]\n"
      "ds_read2_b32 v[164:165], v30 offset0:128 offset1:129\n"
      "ds_read_b128 v[212:215], v33 offset:1024\n"
      "v_mfma_f32_16x16x32_f16 v[184:187], v[124:127], v[216:219], v[184:187]\n"
      "v_mfma_f32_16x16x32_f16 v[188:191], v[92:95], v[216:219], v[188:191]\n"
      "s_waitcnt lgkmcnt(4)\n"
      "v_alignbit_b32 v245, v238, v237, 16\n"
      "v_alignbit_b32 v246, v239, v238, 16\n"
      "v_mfma_f32_16x16x32_f16 v[184:187], v[128:131], v[220:223], v[184:187]\n"
      "s_waitcnt lgkmcnt(3)\n"
      "v_alignbit_b32 v247, v240, v239, 16\n"
      "v_alignbit_b32 v248, v241, v240, 16\n"
      "v_mfma_f32_16x16x32_f16 v[188:191], v[96:99], v[220:223], v[188:191]\n"
      "ds_read2_b32 v[166:167], v30 offset0:168 offset1:169\n"
      "ds_read_b64_tr_b16 v[228:229], v35 offset:32\n"
      "ds_read_b64_tr_b16 v[230:231], v35 offset:16\n"
      "v_alignbit_b32 v249, v242, v241, 16\n"
      "v_pk_add_f16 v16, v245, v246\n"
      "v_mfma_f32_16x16x32_f16 v[184:187], v[132:135], v[224:227], v[184:187]\n"
      "v_pk_add_f16 v17, v246, v247\n"
      "v_pk_add_f16 v18, v247, v248\n"
      "v_mfma_f32_16x16x32_f16 v[188:191], v[100:103], v[224:227], v[188:191]\n"
      "v_pk_add_f16 v19, v248, v249\n"
      "v_pk_add_f16 v20, v16, v238\n"
      "s_waitcnt lgkmcnt(3)\n"
      "v_mfma_f32_16x16x32_f16 v[184:187], v[136:139], v[212:215], v[184:187]\n"
      "ds_read_b128 v[216:219], v33 offset:1088\n"
      "buffer_load_dwordx4 v[60:63], v41, s[20:23], s61 offen offset:2048 nt\n"
      "v_pk_add_f16 v21, v17, v239\n"
      "v_pk_add_f16 v22, v18, v240\n"
      "v_mfma_f32_16x16x32_f16 v[188:191], v[104:107], v[212:215], v[188:191]\n"
      "ds_read2_b32 v[168:169], v30 offset0:120 offset1:121\n"
      "v_pk_add_f16 v23, v19, v241\n"
      "ds_write_b128 v37, v[20:23] offset:1024\n"
      "v_pk_add_f16 v16, v237, v239\n"
      "v_mfma_f32_16x16x32_f16 v[192:195], v[72:75], v[212:215], 0\n"
      "ds_read_b128 v[220:223], v33 offset:1152\n"
      "v_pk_add_f16 v17, v238, v240\n"
      "v_pk_add_f16 v18, v239, v241\n"
      "v_mfma_f32_16x16x32_f16 v[200:203], v[44:47], v[212:215], 0\n"
      "v_pk_add_f16 v19, v240, v242\n"
      "v_pk_add_f16 v20, v20, v16\n"
      "s_waitcnt lgkmcnt(3)\n"
      "v_mfma_f32_16x16x32_f16 v[188:191], v[228:231], v[140:143], v[188:191]\n"
      "ds_read_b128 v[224:227], v33 offset:1216\n"
      "v_cvt_pk_bf16_f32 v24, v184, v186\n"
      "v_cvt_pk_bf16_f32 v25, v185, v187\n"
      "ds_write2st64_b32 v39, v24, v25 offset0:1 offset1:9\n"
      "v_mfma_f32_16x16x32_f16 v[192:195], v[228:231], v[144:147], v[192:195]\n"
      "ds_read2_b32 v[170:171], v30 offset0:160 offset1:161\n"
      "v_pk_add_f16 v21, v21, v17\n"
      "v_pk_add_f16 v22, v22, v18\n"
      "v_mfma_f32_16x16x32_f16 v[196:199], v[228:231], v[148:151], v[196:199]\n"
      "ds_read_b64_tr_b16 v[232:233], v35 offset:3128\n"
      "ds_read_b64_tr_b16 v[234:235], v35 offset:3112\n"
      "v_pk_add_f16 v23, v23, v19\n"
      "ds_write_b128 v37, v[20:23] offset:4128\n"
      "v_alignbit_b32 v244, v237, v236, 16\n"
      "v_mfma_f32_16x16x32_f16 v[188:191], v[108:111], v[216:219], v[188:191]\n"
      "v_alignbit_b32 v250, v243, v242, 16\n"
      "v_pk_add_f16 v16, v244, v247\n"
      "v_mfma_f32_16x16x32_f16 v[192:195], v[76:79], v[216:219], v[192:195]\n"
      "v_pk_add_f16 v17, v245, v248\n"
      "v_pk_add_f16 v18, v246, v249\n"
      "s_waitcnt lgkmcnt(3)\n"
      "v_mfma_f32_16x16x32_f16 v[188:191], v[112:115], v[220:223], v[188:191]\n"
      "ds_read2_b32 v[172:173], v30 offset0:112 offset1:113\n"
      "ds_read_b128 v[212:215], v33 offset:1280\n"
      "v_pk_add_f16 v19, v247, v250\n"
      "v_pk_add_f16 v20, v20, v16\n"
      "v_mfma_f32_16x16x32_f16 v[192:195], v[80:83], v[220:223], v[192:195]\n"
      "v_pk_add_f16 v21, v21, v17\n"
      "v_pk_add_f16 v22, v22, v18\n"
      "v_mfma_f32_16x16x32_f16 v[188:191], v[116:119], v[224:227], v[188:191]\n"
      "ds_read_b128 v[216:219], v33 offset:1344\n"
      "v_pk_add_f16 v23, v23, v19\n"
      "ds_write_b128 v38, v[20:23] offset:1024\n"
      "v_pk_add_f16 v16, v236, v240\n"
      "v_mfma_f32_16x16x32_f16 v[192:195], v[84:87], v[224:227], v[192:195]\n"
      "v_pk_add_f16 v17, v237, v241\n"
      "v_pk_add_f16 v18, v238, v242\n"
      "s_setprio 1\n"
      "s_waitcnt lgkmcnt(2)\n"
      "v_mfma_f32_16x16x32_f16 v[188:191], v[232:235], v[152:155], v[188:191]\n"
      "ds_read2_b32 v[174:175], v30 offset0:152 offset1:153\n"
      "ds_read_b128 v[220:223], v33 offset:1408\n"
      "v_pk_add_f16 v19, v239, v243\n"
      "v_pk_add_f16 v20, v20, v16\n"
      "v_mfma_f32_16x16x32_f16 v[192:195], v[232:235], v[156:159], v[192:195]\n"
      "v_pk_add_f16 v21, v21, v17\n"
      "v_pk_add_f16 v22, v22, v18\n"
      "v_mfma_f32_16x16x32_f16 v[196:199], v[232:235], v[160:163], v[196:199]\n"
      "ds_read_b128 v[224:227], v33 offset:1472\n"
      "v_pk_add_f16 v23, v23, v19\n"
      "ds_write_b128 v38, v[20:23] offset:3104\n"
      "v_mfma_f32_16x16x32_f16 v[188:191], v[120:123], v[212:215], v[188:191]\n"
      "v_readlane_b32 s72, v255, 0\n"
      "v_mfma_f32_16x16x32_f16 v[192:195], v[88:91], v[212:215], v[192:195]\n"
      "ds_read2_b32 v[176:177], v30 offset0:104 offset1:105\n"
      "ds_read_b128 v[212:215], v33 offset:1536\n"
      "v_readlane_b32 s73, v255, 1\n"
      "s_waitcnt lgkmcnt(4)\n"
      "v_mfma_f32_16x16x32_f16 v[188:191], v[124:127], v[216:219], v[188:191]\n"
      "v_readlane_b32 s74, v255, 2\n"
      "v_mfma_f32_16x16x32_f16 v[192:195], v[92:95], v[216:219], v[192:195]\n"
      "v_readlane_b32 s75, v255, 3\n"
      "v_mfma_f32_16x16x32_f16 v[188:191], v[128:131], v[220:223], v[188:191]\n"
      "v_readlane_b32 s76, v255, 4\n"
      "v_mfma_f32_16x16x32_f16 v[192:195], v[96:99], v[220:223], v[192:195]\n"
      "ds_read2_b32 v[178:179], v30 offset0:144 offset1:145\n"
      "ds_read_b64_tr_b16 v[228:229], v35 offset:544\n"
      "ds_read_b64_tr_b16 v[230:231], v35 offset:528\n"
      "v_readlane_b32 s77, v255, 5\n"
      "s_waitcnt lgkmcnt(3)\n"
      "v_mfma_f32_16x16x32_f16 v[188:191], v[132:135], v[224:227], v[188:191]\n"
      "v_readlane_b32 s78, v255, 6\n"
      "v_mfma_f32_16x16x32_f16 v[192:195], v[100:103], v[224:227], v[192:195]\n"
      "v_readlane_b32 s79, v255, 7\n"
      "v_mfma_f32_16x16x32_f16 v[188:191], v[136:139], v[212:215], v[188:191]\n"
      "ds_read_b128 v[216:219], v33 offset:1600\n"
      "s_movk_i32 s59, 0x7fff\n"
      "s_waitcnt vmcnt(1)\n"
      "s_nop 0\n"
      ".Lcsgh%=:\n"
      "v_cmp_ne_u32 s[64:65], s15, v69\n"
      "v_cmp_ne_u32 vcc, s15, v71\n"
      "s_nop 0\n"
      "s_or_b64 vcc, vcc, s[64:65]\n"
      "s_cbranch_scc1 .Lcsgsh%=\n"
      ".Lcsgoh%=:\n"
      "v_mov_b32 v42, v8\n"
      "v_mfma_f32_16x16x32_f16 v[192:195], v[104:107], v[212:215], v[192:195]\n"
      "v_and_b32 v26, 0xffff0000, v68\n"
      "v_lshlrev_b32 v28, 16, v68\n"
      "v_mfma_f32_16x16x32_f16 v[196:199], v[72:75], v[212:215], v[196:199]\n"
      "ds_read_b128 v[220:223], v33 offset:1664\n"
      "v_rcp_f32 v26, v26\n"
      "v_and_b32 v27, 0xffff0000, v70\n"
      "v_mfma_f32_16x16x32_f16 v[204:207], v[44:47], v[212:215], 0\n"
      "v_lshlrev_b32 v29, 16, v70\n"
      "v_rcp_f32 v27, v27\n"
      "s_waitcnt lgkmcnt(1)\n"
      "v_mfma_f32_16x16x32_f16 v[192:195], v[228:231], v[140:143], v[192:195]\n"
      "ds_read_b128 v[224:227], v33 offset:1728\n"
      "v_cvt_pk_bf16_f32 v24, v188, v190\n"
      "v_cvt_pk_bf16_f32 v25, v189, v191\n"
      "ds_write2st64_b32 v39, v24, v25 offset0:2 offset1:10\n"
      "v_mfma_f32_16x16x32_f16 v[196:199], v[228:231], v[144:147], v[196:199]\n"
      "v_mul_f32 v28, -v28, v26\n"
      "v_mul_f32 v29, -v29, v27\n"
      "v_mfma_f32_16x16x32_f16 v[200:203], v[228:231], v[148:151], v[200:203]\n"
      "ds_read_b64_tr_b16 v[232:233], v35 offset:3640\n"
      "ds_read_b64_tr_b16 v[234:235], v35 offset:3624\n"
      "v_lshlrev_b32 v180, 16, v56\n"
      "s_waitcnt vmcnt(0)\n"
      "v_lshlrev_b32 v181, 16, v60\n"
      "v_mfma_f32_16x16x32_f16 v[192:195], v[108:111], v[216:219], v[192:195]\n"
      "v_and_b32 v182, 0xffff0000, v56\n"
      "v_and_b32 v183, 0xffff0000, v60\n"
      "v_mfma_f32_16x16x32_f16 v[196:199], v[76:79], v[216:219], v[196:199]\n"
      "v_fma_f32 v180, v180, v26, v28\n"
      "v_fma_f32 v181, v181, v27, v29\n"
      "s_setprio 0\n"
      "s_waitcnt lgkmcnt(2)\n"
      "v_mfma_f32_16x16x32_f16 v[192:195], v[112:115], v[220:223], v[192:195]\n"
      "ds_read_b128 v[212:215], v33 offset:1792\n"
      "v_fma_f32 v182, v182, v26, v28\n"
      "v_fma_f32 v183, v183, v27, v29\n"
      "v_mfma_f32_16x16x32_f16 v[196:199], v[80:83], v[220:223], v[196:199]\n"
      "v_cvt_pk_f16_f32 v180, v180, v181\n"
      "v_cvt_pk_f16_f32 v182, v182, v183\n"
      "ds_write2st64_b32 v32, v180, v182 offset0:8 offset1:27\n"
      "v_mfma_f32_16x16x32_f16 v[192:195], v[116:119], v[224:227], v[192:195]\n"
      "ds_read_b128 v[216:219], v33 offset:1856\n"
      "v_lshlrev_b32 v184, 16, v57\n"
      "v_lshlrev_b32 v185, 16, v61\n"
      "v_mfma_f32_16x16x32_f16 v[196:199], v[84:87], v[224:227], v[196:199]\n"
      "v_and_b32 v186, 0xffff0000, v57\n"
      "v_and_b32 v187, 0xffff0000, v61\n"
      "s_waitcnt lgkmcnt(2)\n"
      "v_mfma_f32_16x16x32_f16 v[192:195], v[232:235], v[152:155], v[192:195]\n"
      "ds_read_b128 v[220:223], v33 offset:1920\n"
      "v_fma_f32 v184, v184, v26, v28\n"
      "v_fma_f32 v185, v185, v27, v29\n"
      "v_mfma_f32_16x16x32_f16 v[196:199], v[232:235], v[156:159], v[196:199]\n"
      "v_fma_f32 v186, v186, v26, v28\n"
      "v_fma_f32 v187, v187, v27, v29\n"
      "s_cmp_lg_u32 s33, 0\n"
      "s_cbranch_scc1 .Lcg%=\n"
      "buffer_load_dwordx2 v[252:253], v9, s[16:19], s43 offen sc1\n"
      ".Lcg%=:\n"
      "v_mfma_f32_16x16x32_f16 v[200:203], v[232:235], v[160:163], v[200:203]\n"
      "ds_read_b128 v[224:227], v33 offset:1984\n"
      "v_cvt_pk_f16_f32 v184, v184, v185\n"
      "v_cvt_pk_f16_f32 v186, v186, v187\n"
      "ds_write2st64_b32 v32, v184, v186 offset0:46 offset1:65\n"
      "v_mfma_f32_16x16x32_f16 v[192:195], v[120:123], v[212:215], v[192:195]\n"
      "v_lshlrev_b32 v180, 16, v58\n"
      "v_lshlrev_b32 v181, 16, v62\n"
      "v_mfma_f32_16x16x32_f16 v[196:199], v[88:91], v[212:215], v[196:199]\n"
      "ds_read_b128 v[212:215], v33 offset:2048\n"
      "v_and_b32 v182, 0xffff0000, v58\n"
      "v_and_b32 v183, 0xffff0000, v62\n"
      "s_waitcnt lgkmcnt(3)\n"
      "v_mfma_f32_16x16x32_f16 v[192:195], v[124:127], v[216:219], v[192:195]\n"
      "v_fma_f32 v180, v180, v26, v28\n"
      "v_fma_f32 v181, v181, v27, v29\n"
      "v_mfma_f32_16x16x32_f16 v[196:199], v[92:95], v[216:219], v[196:199]\n"
      "v_fma_f32 v182, v182, v26, v28\n"
      "v_fma_f32 v183, v183, v27, v29\n"
      "v_mfma_f32_16x16x32_f16 v[192:195], v[128:131], v[220:223], v[192:195]\n"
      "v_cvt_pk_f16_f32 v180, v180, v181\n"
      "v_cvt_pk_f16_f32 v182, v182, v183\n"
      "ds_write2st64_b32 v32, v180, v182 offset0:84 offset1:103\n"
      "v_mfma_f32_16x16x32_f16 v[196:199], v[96:99], v[220:223], v[196:199]\n"
      "ds_read_b64_tr_b16 v[228:229], v36 offset:24\n"
      "ds_read_b64_tr_b16 v[230:231], v36 offset:2088\n"
      "v_lshlrev_b32 v184, 16, v59\n"
      "v_lshlrev_b32 v185, 16, v63\n"
      "s_waitcnt lgkmcnt(3)\n"
      "v_mfma_f32_16x16x32_f16 v[192:195], v[132:135], v[224:227], v[192:195]\n"
      "v_and_b32 v186, 0xffff0000, v59\n"
      "v_and_b32 v187, 0xffff0000, v63\n"
      "v_mfma_f32_16x16x32_f16 v[196:199], v[100:103], v[224:227], v[196:199]\n"
      "v_fma_f32 v184, v184, v26, v28\n"
      "v_fma_f32 v185, v185, v27, v29\n"
      "v_mfma_f32_16x16x32_f16 v[192:195], v[136:139], v[212:215], v[192:195]\n"
      "v_fma_f32 v186, v186, v26, v28\n"
      "v_fma_f32 v187, v187, v27, v29\n"
      "v_mfma_f32_16x16x32_f16 v[196:199], v[104:107], v[212:215], v[196:199]\n"
      "ds_read_b64_tr_b16 v[232:233], v36 offset:536\n"
      "ds_read_b64_tr_b16 v[234:235], v36 offset:2600\n"
      "v_cvt_pk_f16_f32 v184, v184, v185\n"
      "v_cvt_pk_f16_f32 v186, v186, v187\n"
      "ds_write2st64_b32 v32, v184, v186 offset0:122 offset1:141\n"
      "v_mfma_f32_16x16x32_f16 v[200:203], v[72:75], v[212:215], v[200:203]\n"
      "v_permlane32_swap_b32 v56, v60\n"
      "v_permlane32_swap_b32 v57, v61\n"
      "v_mfma_f32_16x16x32_f16 v[208:211], v[44:47], v[212:215], 0\n"
      "v_permlane32_swap_b32 v58, v62\n"
      "v_permlane32_swap_b32 v59, v63\n"
      "s_waitcnt lgkmcnt(3)\n"
      "v_mfma_f32_16x16x32_f16 v[196:199], v[228:231], v[164:167], v[196:199]\n"
      "v_cvt_pk_bf16_f32 v24, v192, v194\n"
      "v_cvt_pk_bf16_f32 v25, v193, v195\n"
      "ds_write2st64_b32 v39, v24, v25 offset0:3 offset1:11\n"
      "v_mfma_f32_16x16x32_f16 v[200:203], v[228:231], v[168:171], v[200:203]\n"
      "v_mfma_f32_16x16x32_f16 v[204:207], v[228:231], v[172:175], v[204:207]\n"
      "v_mfma_f32_16x16x32_f16 v[208:211], v[228:231], v[176:179], v[208:211]\n"
      "ds_read_b64_tr_b16 v[228:229], v35 offset:1056\n"
      "ds_read_b64_tr_b16 v[230:231], v35 offset:1040\n"
      "s_cmp_eq_u32 s33, 0\n"
      "s_cbranch_scc1 .Lch%=\n"
      ".Lcho%=:\n"
      "s_waitcnt lgkmcnt(2)\n"
      "s_barrier\n"
      "s_setprio 3\n"
      "v_mfma_f32_16x16x32_f16 v[200:203], v[232:235], v[164:167], v[200:203]\n"
      "ds_read_b64_tr_b16 v[64:65], v40 offset:0\n"
      "ds_read_b64_tr_b16 v[66:67], v40 offset:35072\n"
      "v_lshlrev_b32 v68, 16, v48\n"
      "v_and_b32 v69, 0xffff0000, v48\n"
      "v_mfma_f32_16x16x32_f16 v[204:207], v[232:235], v[168:171], v[204:207]\n"
      "v_lshlrev_b32 v70, 16, v49\n"
      "v_and_b32 v71, 0xffff0000, v49\n"
      "ds_read_b128 v[236:239], v34 offset:2048\n"
      "ds_read_b128 v[240:243], v34 offset:2064\n"
      "ds_read_b32 v12, v14 offset:256\n"
      "v_mfma_f32_16x16x32_f16 v[208:211], v[232:235], v[172:175], v[208:211]\n"
      "ds_read_b128 v[216:219], v33 offset:2112\n"
      "ds_read_b64_tr_b16 v[232:233], v35 offset:4152\n"
      "ds_read_b64_tr_b16 v[234:235], v35 offset:4136\n"
      "v_lshlrev_b32 v26, 16, v50\n"
      "v_and_b32 v27, 0xffff0000, v50\n"
      "s_waitcnt lgkmcnt(6)\n"
      "v_mfma_f32_16x16x32_f16 v[196:199], v[228:231], v[140:143], v[196:199]\n"
      "v_lshlrev_b32 v28, 16, v51\n"
      "v_and_b32 v29, 0xffff0000, v51\n"
      "v_mfma_f32_16x16x32_f16 v[200:203], v[228:231], v[144:147], v[200:203]\n"
      "ds_read_b128 v[220:223], v33 offset:2176\n"
      "v_lshlrev_b32 v48, 16, v64\n"
      "v_and_b32 v64, 0xffff0000, v64\n"
      "v_mfma_f32_16x16x32_f16 v[204:207], v[228:231], v[148:151], v[204:207]\n"
      "v_fmac_f32 v68, s72, v48\n"
      "v_fmac_f32 v69, s73, v64\n"
      "s_waitcnt lgkmcnt(1)\n"
      "v_mfma_f32_16x16x32_f16 v[196:199], v[232:235], v[152:155], v[196:199]\n"
      "ds_read_b128 v[224:227], v33 offset:2240\n"
      "v_lshlrev_b32 v49, 16, v65\n"
      "v_and_b32 v65, 0xffff0000, v65\n"
      "v_mfma_f32_16x16x32_f16 v[200:203], v[232:235], v[156:159], v[200:203]\n"
      "v_fmac_f32 v70, s74, v49\n"
      "v_fmac_f32 v71, s75, v65\n"
      "v_mfma_f32_16x16x32_f16 v[204:207], v[232:235], v[160:163], v[204:207]\n"
      "ds_read_b128 v[212:215], v33 offset:2304\n"
      "v_lshlrev_b32 v50, 16, v66\n"
      "v_and_b32 v66, 0xffff0000, v66\n"
      "v_mfma_f32_16x16x32_f16 v[196:199], v[108:111], v[216:219], v[196:199]\n"
      "v_fmac_f32 v26, s76, v50\n"
      "v_fmac_f32 v27, s77, v66\n"
      "v_mfma_f32_16x16x32_f16 v[200:203], v[76:79], v[216:219], v[200:203]\n"
      "ds_read_b64_tr_b16 v[228:229], v35 offset:1568\n"
      "ds_read_b64_tr_b16 v[230:231], v35 offset:1552\n"
      "v_lshlrev_b32 v51, 16, v67\n"
      "v_and_b32 v67, 0xffff0000, v67\n"
      "s_waitcnt lgkmcnt(3)\n"
      "v_mfma_f32_16x16x32_f16 v[196:199], v[112:115], v[220:223], v[196:199]\n"
      "v_fmac_f32 v28, s78, v51\n"
      "v_fmac_f32 v29, s79, v67\n"
      "v_mfma_f32_16x16x32_f16 v[200:203], v[80:83], v[220:223], v[200:203]\n"
      "v_cvt_pk_bf16_f32 v48, v68, v69\n"
      "v_cvt_pk_bf16_f32 v49, v70, v71\n"
      "v_mfma_f32_16x16x32_f16 v[196:199], v[116:119], v[224:227], v[196:199]\n"
      "ds_read_b128 v[216:219], v33 offset:2368\n"
      "v_cvt_pk_bf16_f32 v50, v26, v27\n"
      "v_cvt_pk_bf16_f32 v51, v28, v29\n"
      "s_setprio 2\n"
      "v_mfma_f32_16x16x32_f16 v[200:203], v[84:87], v[224:227], v[200:203]\n"
      "v_lshlrev_b32 v13, 24, v12\n"
      "v_and_or_b32 v13, v13, s58, v42\n"
      "s_waitcnt lgkmcnt(1)\n"
      "v_mfma_f32_16x16x32_f16 v[196:199], v[120:123], v[212:215], v[196:199]\n"
      "ds_read_b128 v[220:223], v33 offset:2432\n"
      "s_cmp_lg_u32 s13, 0\n"
      "s_cbranch_scc1 .Lcs00%=\n"
      "buffer_store_dwordx4 v[48:51], v13, s[24:27], s54 offen\n"
      ".Lcs00%=:\n"
      "v_alignbit_b32 v245, v238, v237, 16\n"
      "v_alignbit_b32 v246, v239, v238, 16\n"
      "v_mfma_f32_16x16x32_f16 v[200:203], v[88:91], v[212:215], v[200:203]\n"
      "v_alignbit_b32 v247, v240, v239, 16\n"
      "v_alignbit_b32 v248, v241, v240, 16\n"
      "v_mfma_f32_16x16x32_f16 v[200:203], v[228:231], v[140:143], v[200:203]\n"
      "ds_read_b128 v[224:227], v33 offset:2496\n"
      "s_cmp_lg_u32 s13, 1\n"
      "s_cbranch_scc1 .Lcs01%=\n"
      "buffer_store_dwordx4 v[48:51], v13, s[24:27], s54 offen\n"
      ".Lcs01%=:\n"
      "v_alignbit_b32 v249, v242, v241, 16\n"
      "v_pk_add_f16 v16, v245, v246\n"
      "v_mfma_f32_16x16x32_f16 v[204:207], v[228:231], v[144:147], v[204:207]\n"
      "v_pk_add_f16 v17, v246, v247\n"
      "v_pk_add_f16 v18, v247, v248\n"
      "v_mfma_f32_16x16x32_f16 v[208:211], v[228:231], v[148:151], v[208:211]\n"
      "ds_read_b128 v[212:215], v33 offset:2560\n"
      "s_cmp_lg_u32 s13, 2\n"
      "s_cbranch_scc1 .Lcs02%=\n"
      "buffer_store_dwordx4 v[48:51], v13, s[24:27], s54 offen\n"
      ".Lcs02%=:\n"
      "v_pk_add_f16 v19, v248, v249\n"
      "v_pk_add_f16 v20, v16, v238\n"
      "s_waitcnt lgkmcnt(1)\n"
      "v_mfma_f32_16x16x32_f16 v[196:199], v[124:127], v[216:219], v[196:199]\n"
      "v_pk_add_f16 v21, v17, v239\n"
      "v_pk_add_f16 v22, v18, v240\n"
      "v_mfma_f32_16x16x32_f16 v[200:203], v[92:95], v[216:219], v[200:203]\n"
      "s_cmp_lg_u32 s13, 3\n"
      "s_cbranch_scc1 .Lcs03%=\n"
      "buffer_store_dwordx4 v[48:51], v13, s[24:27], s54 offen\n"
      ".Lcs03%=:\n"
      "v_pk_add_f16 v23, v19, v241\n"
      "ds_write_b128 v37, v[20:23] offset:2048\n"
      "v_pk_add_f16 v16, v237, v239\n"
      "v_mfma_f32_16x16x32_f16 v[196:199], v[128:131], v[220:223], v[196:199]\n"
      "ds_read_b64_tr_b16 v[232:233], v35 offset:4664\n"
      "ds_read_b64_tr_b16 v[234:235], v35 offset:4648\n"
      "ds_read_b64_tr_b16 v[64:65], v40 offset:512\n"
      "ds_read_b64_tr_b16 v[66:67], v40 offset:35584\n"
      "v_lshlrev_b32 v68, 16, v52\n"
      "v_and_b32 v69, 0xffff0000, v52\n"
      "v_mfma_f32_16x16x32_f16 v[200:203], v[96:99], v[220:223], v[200:203]\n"
      "v_lshlrev_b32 v70, 16, v53\n"
      "v_and_b32 v71, 0xffff0000, v53\n"
      "v_mfma_f32_16x16x32_f16 v[196:199], v[132:135], v[224:227], v[196:199]\n"
      "v_lshlrev_b32 v26, 16, v54\n"
      "v_and_b32 v27, 0xffff0000, v54\n"
      "v_mfma_f32_16x16x32_f16 v[200:203], v[100:103], v[224:227], v[200:203]\n"
      "ds_read_b128 v[216:219], v33 offset:2624\n"
      "v_lshlrev_b32 v28, 16, v55\n"
      "v_and_b32 v29, 0xffff0000, v55\n"
      "s_waitcnt lgkmcnt(1)\n"
      "v_mfma_f32_16x16x32_f16 v[196:199], v[136:139], v[212:215], v[196:199]\n"
      "v_lshlrev_b32 v52, 16, v64\n"
      "v_and_b32 v64, 0xffff0000, v64\n"
      "v_mfma_f32_16x16x32_f16 v[200:203], v[104:107], v[212:215], v[200:203]\n"
      "ds_read_b128 v[220:223], v33 offset:2688\n"
      "v_fmac_f32 v68, s72, v52\n"
      "v_fmac_f32 v69, s73, v64\n"
      "s_setprio 1\n"
      "v_mfma_f32_16x16x32_f16 v[204:207], v[72:75], v[212:215], v[204:207]\n"
      "v_lshlrev_b32 v53, 16, v65\n"
      "v_and_b32 v65, 0xffff0000, v65\n"
      "v_mfma_f32_16x16x32_f16 v[200:203], v[232:235], v[152:155], v[200:203]\n"
      "ds_read_b128 v[224:227], v33 offset:2752\n"
      "v_fmac_f32 v70, s74, v53\n"
      "v_fmac_f32 v71, s75, v65\n"
      "v_mfma_f32_16x16x32_f16 v[204:207], v[232:235], v[156:159], v[204:207]\n"
      "v_cvt_pk_bf16_f32 v24, v196, v198\n"
      "v_cvt_pk_bf16_f32 v25, v197, v199\n"
      "ds_write2st64_b32 v39, v24, v25 offset0:4 offset1:12\n"
      "v_mfma_f32_16x16x32_f16 v[208:211], v[232:235], v[160:163], v[208:211]\n"
      "ds_read_b128 v[212:215], v33 offset:2816\n"
      "v_lshlrev_b32 v54, 16, v66\n"
      "v_and_b32 v66, 0xffff0000, v66\n"
      "s_waitcnt lgkmcnt(3)\n"
      "v_mfma_f32_16x16x32_f16 v[200:203], v[108:111], v[216:219], v[200:203]\n"
      "v_fmac_f32 v26, s76, v54\n"
      "v_fmac_f32 v27, s77, v66\n"
      "v_mfma_f32_16x16x32_f16 v[204:207], v[76:79], v[216:219], v[204:207]\n"
      "ds_read_b128 v[216:219], v33 offset:2880\n"
      "v_lshlrev_b32 v55, 16, v67\n"
      "v_and_b32 v67, 0xffff0000, v67\n"
      "v_mfma_f32_16x16x32_f16 v[200:203], v[112:115], v[220:223], v[200:203]\n"
      "v_fmac_f32 v28, s78, v55\n"
      "v_fmac_f32 v29, s79, v67\n"
      "v_mfma_f32_16x16x32_f16 v[204:207], v[80:83], v[220:223], v[204:207]\n"
      "ds_read_b128 v[220:223], v33 offset:2944\n"
      "v_cvt_pk_bf16_f32 v52, v68, v69\n"
      "v_cvt_pk_bf16_f32 v53, v70, v71\n"
      "s_waitcnt lgkmcnt(2)\n"
      "v_mfma_f32_16x16x32_f16 v[200:203], v[116:119], v[224:227], v[200:203]\n"
      "v_cvt_pk_bf16_f32 v54, v26, v27\n"
      "v_cvt_pk_bf16_f32 v55, v28, v29\n"
      "v_mfma_f32_16x16x32_f16 v[204:207], v[84:87], v[224:227], v[204:207]\n"
      "ds_read_b128 v[224:227], v33 offset:3008\n"
      "v_lshlrev_b32 v13, 16, v12\n"
      "v_and_or_b32 v13, v13, s58, v42\n"
      "v_mfma_f32_16x16x32_f16 v[200:203], v[120:123], v[212:215], v[200:203]\n"
      "s_cmp_lg_u32 s13, 0\n"
      "s_cbranch_scc1 .Lcs10%=\n"
      "buffer_store_dwordx4 v[52:55], v13, s[24:27], s55 offen\n"
      ".Lcs10%=:\n"
      "v_pk_add_f16 v17, v238, v240\n"
      "v_pk_add_f16 v18, v239, v241\n"
      "s_setprio 0\n"
      "v_mfma_f32_16x16x32_f16 v[204:207], v[88:91], v[212:215], v[204:207]\n"
      "ds_read_b128 v[212:215], v33 offset:3072\n"
      "v_pk_add_f16 v19, v240, v242\n"
      "v_pk_add_f16 v20, v20, v16\n"
      "s_waitcnt lgkmcnt(1)\n"
      "v_mfma_f32_16x16x32_f16 v[200:203], v[124:127], v[216:219], v[200:203]\n"
      "s_cmp_lg_u32 s13, 1\n"
      "s_cbranch_scc1 .Lcs11%=\n"
      "buffer_store_dwordx4 v[52:55], v13, s[24:27], s55 offen\n"
      ".Lcs11%=:\n"
      "v_pk_add_f16 v21, v21, v17\n"
      "v_pk_add_f16 v22, v22, v18\n"
      "v_mfma_f32_16x16x32_f16 v[204:207], v[92:95], v[216:219], v[204:207]\n"
      "v_pk_add_f16 v23, v23, v19\n"
      "ds_write_b128 v37, v[20:23] offset:5152\n"
      "v_mfma_f32_16x16x32_f16 v[200:203], v[128:131], v[220:223], v[200:203]\n"
      "ds_read_b64_tr_b16 v[228:229], v36 offset:1048\n"
      "ds_read_b64_tr_b16 v[230:231], v36 offset:3112\n"
      "s_cmp_lg_u32 s13, 2\n"
      "s_cbranch_scc1 .Lcs12%=\n"
      "buffer_store_dwordx4 v[52:55], v13, s[24:27], s55 offen\n"
      ".Lcs12%=:\n"
      "v_mfma_f32_16x16x32_f16 v[204:207], v[96:99], v[220:223], v[204:207]\n"
      "v_mfma_f32_16x16x32_f16 v[200:203], v[132:135], v[224:227], v[200:203]\n"
      "ds_read_b128 v[216:219], v33 offset:3136\n"
      "s_cmp_lg_u32 s13, 3\n"
      "s_cbranch_scc1 .Lcs13%=\n"
      "buffer_store_dwordx4 v[52:55], v13, s[24:27], s55 offen\n"
      ".Lcs13%=:\n"
      "v_mfma_f32_16x16x32_f16 v[204:207], v[100:103], v[224:227], v[204:207]\n"
      "s_waitcnt lgkmcnt(1)\n"
      "v_mfma_f32_16x16x32_f16 v[200:203], v[136:139], v[212:215], v[200:203]\n"
      "ds_read_b128 v[220:223], v33 offset:3200\n"
      "v_mfma_f32_16x16x32_f16 v[204:207], v[104:107], v[212:215], v[204:207]\n"
      "v_mfma_f32_16x16x32_f16 v[208:211], v[72:75], v[212:215], v[208:211]\n"
      "ds_read_b128 v[224:227], v33 offset:3264\n"
      "v_mfma_f32_16x16x32_f16 v[204:207], v[228:231], v[164:167], v[204:207]\n"
      "v_mfma_f32_16x16x32_f16 v[208:211], v[228:231], v[168:171], v[208:211]\n"
      "ds_read_b64_tr_b16 v[232:233], v36 offset:1560\n"
      "ds_read_b64_tr_b16 v[234:235], v36 offset:3624\n"
      "v_cvt_pk_bf16_f32 v24, v200, v202\n"
      "v_cvt_pk_bf16_f32 v25, v201, v203\n"
      "ds_write2st64_b32 v39, v24, v25 offset0:5 offset1:13\n"
      "s_waitcnt lgkmcnt(5)\n"
      "v_mfma_f32_16x16x32_f16 v[204:207], v[108:111], v[216:219], v[204:207]\n"
      "ds_read_b128 v[212:215], v33 offset:3328\n"
      "v_mfma_f32_16x16x32_f16 v[208:211], v[76:79], v[216:219], v[208:211]\n"
      "s_waitcnt lgkmcnt(5)\n"
      "v_mfma_f32_16x16x32_f16 v[204:207], v[112:115], v[220:223], v[204:207]\n"
      "ds_read_b128 v[216:219], v33 offset:3392\n"
      "s_waitcnt lgkmcnt(2)\n"
      "s_barrier\n"
      "s_setprio 3\n"
      "v_mfma_f32_16x16x32_f16 v[208:211], v[80:83], v[220:223], v[208:211]\n"
      "ds_read_b64_tr_b16 v[64:65], v40 offset:1024\n"
      "ds_read_b64_tr_b16 v[66:67], v40 offset:36096\n"
      "v_lshlrev_b32 v68, 16, v56\n"
      "v_and_b32 v69, 0xffff0000, v56\n"
      "v_mfma_f32_16x16x32_f16 v[204:207], v[116:119], v[224:227], v[204:207]\n"
      "ds_read_b128 v[220:223], v33 offset:3456\n"
      "v_lshlrev_b32 v70, 16, v57\n"
      "v_and_b32 v71, 0xffff0000, v57\n"
      "v_mfma_f32_16x16x32_f16 v[208:211], v[84:87], v[224:227], v[208:211]\n"
      "v_lshlrev_b32 v26, 16, v58\n"
      "v_and_b32 v27, 0xffff0000, v58\n"
      "v_mfma_f32_16x16x32_f16 v[208:211], v[232:235], v[164:167], v[208:211]\n"
      "ds_read_b64_tr_b16 v[228:229], v35 offset:2080\n"
      "ds_read_b64_tr_b16 v[230:231], v35 offset:2064\n"
      "v_lshlrev_b32 v28, 16, v59\n"
      "v_and_b32 v29, 0xffff0000, v59\n"
      "s_waitcnt lgkmcnt(3)\n"
      "v_mfma_f32_16x16x32_f16 v[204:207], v[120:123], v[212:215], v[204:207]\n"
      "v_lshlrev_b32 v56, 16, v64\n"
      "v_and_b32 v64, 0xffff0000, v64\n"
      "v_mfma_f32_16x16x32_f16 v[208:211], v[88:91], v[212:215], v[208:211]\n"
      "ds_read_b128 v[224:227], v33 offset:3520\n"
      "v_fmac_f32 v68, s72, v56\n"
      "v_fmac_f32 v69, s73, v64\n"
      "v_mfma_f32_16x16x32_f16 v[204:207], v[124:127], v[216:219], v[204:207]\n"
      "v_lshlrev_b32 v57, 16, v65\n"
      "v_and_b32 v65, 0xffff0000, v65\n"
      "v_mfma_f32_16x16x32_f16 v[208:211], v[92:95], v[216:219], v[208:211]\n"
      "ds_read_b128 v[212:215], v33 offset:3584\n"
      "v_fmac_f32 v70, s74, v57\n"
      "v_fmac_f32 v71, s75, v65\n"
      "s_setprio 2\n"
      "s_waitcnt lgkmcnt(2)\n"
      "v_mfma_f32_16x16x32_f16 v[204:207], v[128:131], v[220:223], v[204:207]\n"
      "v_lshlrev_b32 v58, 16, v66\n"
      "v_and_b32 v66, 0xffff0000, v66\n"
      "v_mfma_f32_16x16x32_f16 v[208:211], v[96:99], v[220:223], v[208:211]\n"
      "ds_read_b128 v[216:219], v33 offset:3648\n"
      "v_fmac_f32 v26, s76, v58\n"
      "v_fmac_f32 v27, s77, v66\n"
      "v_mfma_f32_16x16x32_f16 v[204:207], v[228:231], v[140:143], v[204:207]\n"
      "ds_read_b64_tr_b16 v[232:233], v35 offset:5176\n"
      "ds_read_b64_tr_b16 v[234:235], v35 offset:5160\n"
      "v_lshlrev_b32 v59, 16, v67\n"
      "v_and_b32 v67, 0xffff0000, v67\n"
      "v_mfma_f32_16x16x32_f16 v[208:211], v[228:231], v[144:147], v[208:211]\n"
      "v_fmac_f32 v28, s78, v59\n"
      "v_fmac_f32 v29, s79, v67\n"
      "s_waitcnt lgkmcnt(3)\n"
      "v_mfma_f32_16x16x32_f16 v[204:207], v[132:135], v[224:227], v[204:207]\n"
      "ds_read_b128 v[220:223], v33 offset:3712\n"
      "v_cvt_pk_bf16_f32 v56, v68, v69\n"
      "v_cvt_pk_bf16_f32 v57, v70, v71\n"
      "v_mfma_f32_16x16x32_f16 v[208:211], v[100:103], v[224:227], v[208:211]\n"
      "ds_read_b128 v[224:227], v33 offset:3776\n"
      "v_cvt_pk_bf16_f32 v58, v26, v27\n"
      "v_cvt_pk_bf16_f32 v59, v28, v29\n"
      "v_mfma_f32_16x16x32_f16 v[204:207], v[136:139], v[212:215], v[204:207]\n"
      "v_lshlrev_b32 v13, 8, v12\n"
      "v_and_or_b32 v13, v13, s58, v42\n"
      "s_setprio 1\n"
      "v_mfma_f32_16x16x32_f16 v[208:211], v[104:107], v[212:215], v[208:211]\n"
      "ds_read_b64_tr_b16 v[228:229], v35 offset:2592\n"
      "ds_read_b64_tr_b16 v[230:231], v35 offset:2576\n"
      "ds_read_b128 v[212:215], v33 offset:3840\n"
      "s_cmp_lg_u32 s13, 0\n"
      "s_cbranch_scc1 .Lcs20%=\n"
      "buffer_store_dwordx4 v[56:59], v13, s[24:27], s56 offen\n"
      ".Lcs20%=:\n"
      "v_lshlrev_b32 v68, 16, v60\n"
      "v_and_b32 v69, 0xffff0000, v60\n"
      "s_waitcnt lgkmcnt(3)\n"
      "v_mfma_f32_16x16x32_f16 v[208:211], v[108:111], v[216:219], v[208:211]\n"
      "ds_read_b128 v[216:219], v33 offset:3904\n"
      "v_lshlrev_b32 v70, 16, v61\n"
      "v_and_b32 v71, 0xffff0000, v61\n"
      "v_mfma_f32_16x16x32_f16 v[204:207], v[232:235], v[152:155], v[204:207]\n"
      "s_cmp_lg_u32 s13, 1\n"
      "s_cbranch_scc1 .Lcs21%=\n"
      "buffer_store_dwordx4 v[56:59], v13, s[24:27], s56 offen\n"
      ".Lcs21%=:\n"
      "v_lshlrev_b32 v26, 16, v62\n"
      "v_and_b32 v27, 0xffff0000, v62\n"
      "v_mfma_f32_16x16x32_f16 v[208:211], v[232:235], v[156:159], v[208:211]\n"
      "ds_read_b64_tr_b16 v[232:233], v35 offset:5688\n"
      "ds_read_b64_tr_b16 v[234:235], v35 offset:5672\n"
      "v_lshlrev_b32 v28, 16, v63\n"
      "v_and_b32 v29, 0xffff0000, v63\n"
      "v_mfma_f32_16x16x32_f16 v[208:211], v[112:115], v[220:223], v[208:211]\n"
      "ds_read_b128 v[220:223], v33 offset:3968\n"
      "s_cmp_lg_u32 s13, 2\n"
      "s_cbranch_scc1 .Lcs22%=\n"
      "buffer_store_dwordx4 v[56:59], v13, s[24:27], s56 offen\n"
      ".Lcs22%=:\n"
      "s_setprio 0\n"
      "v_mfma_f32_16x16x32_f16 v[208:211], v[116:119], v[224:227], v[208:211]\n"
      "ds_read_b128 v[224:227], v33 offset:4032\n"
      "s_waitcnt lgkmcnt(3)\n"
      "v_mfma_f32_16x16x32_f16 v[208:211], v[120:123], v[212:215], v[208:211]\n"
      "ds_read_b128 v[212:215], v33 offset:4096\n"
      "v_cvt_pk_bf16_f32 v24, v204, v206\n"
      "v_cvt_pk_bf16_f32 v25, v205, v207\n"
      "ds_write2st64_b32 v39, v24, v25 offset0:6 offset1:14\n"
      "s_cmp_lg_u32 s13, 3\n"
      "s_cbranch_scc1 .Lcs23%=\n"
      "buffer_store_dwordx4 v[56:59], v13, s[24:27], s56 offen\n"
      ".Lcs23%=:\n"
      "v_mfma_f32_16x16x32_f16 v[208:211], v[228:231], v[140:143], v[208:211]\n"
      "v_mfma_f32_16x16x32_f16 v[208:211], v[124:127], v[216:219], v[208:211]\n"
      "s_waitcnt lgkmcnt(2)\n"
      "v_mfma_f32_16x16x32_f16 v[208:211], v[128:131], v[220:223], v[208:211]\n"
      "v_mfma_f32_16x16x32_f16 v[208:211], v[232:235], v[152:155], v[208:211]\n"
      "v_mfma_f32_16x16x32_f16 v[208:211], v[132:135], v[224:227], v[208:211]\n"
      "s_waitcnt lgkmcnt(1)\n"
      "v_mfma_f32_16x16x32_f16 v[208:211], v[136:139], v[212:215], v[208:211]\n"
      "s_nop 7\n"
      "v_cvt_pk_bf16_f32 v24, v208, v210\n"
      "v_cvt_pk_bf16_f32 v25, v209, v211\n"
      "ds_write2st64_b32 v39, v24, v25 offset0:7 offset1:15\n"
      "s_waitcnt lgkmcnt(0)\n"
      "s_barrier\n"
      "ds_read_b64_tr_b16 v[64:65], v40 offset:1536\n"
      "ds_read_b64_tr_b16 v[66:67], v40 offset:36608\n"
      "s_cmp_lg_u32 s33, 0\n"
      "s_cbranch_scc1 .Lcb%=\n"
      "ds_read_u16_d16_hi v252, v8 offset:56384\n"
      "ds_read_u16_d16_hi v253, v8 offset:65152\n"
      "s_waitcnt lgkmcnt(0)\n"
      "v_pk_mul_f32 v[252:253], v[252:253], v[254:255]\n"
      "v_cvt_pk_bf16_f32 v252, v252, v253\n"
      "buffer_atomic_pk_add_bf16 v252, v251, s[24:27], s41 offen\n"
      ".Lcb%=:\n"
      "s_waitcnt lgkmcnt(1)\n"
      "v_lshlrev_b32 v60, 16, v64\n"
      "v_and_b32 v64, 0xffff0000, v64\n"
      "v_fmac_f32 v68, s72, v60\n"
      "v_fmac_f32 v69, s73, v64\n"
      "v_lshlrev_b32 v61, 16, v65\n"
      "v_and_b32 v65, 0xffff0000, v65\n"
      "v_fmac_f32 v70, s74, v61\n"
      "v_fmac_f32 v71, s75, v65\n"
      "s_waitcnt lgkmcnt(0)\n"
      "v_lshlrev_b32 v62, 16, v66\n"
      "v_and_b32 v66, 0xffff0000, v66\n"
      "v_fmac_f32 v26, s76, v62\n"
      "v_fmac_f32 v27, s77, v66\n"
      "v_lshlrev_b32 v63, 16, v67\n"
      "v_and_b32 v67, 0xffff0000, v67\n"
      "v_fmac_f32 v28, s78, v63\n"
      "v_fmac_f32 v29, s79, v67\n"
      "v_cvt_pk_bf16_f32 v60, v68, v69\n"
      "v_cvt_pk_bf16_f32 v61, v70, v71\n"
      "v_cvt_pk_bf16_f32 v62, v26, v27\n"
      "v_cvt_pk_bf16_f32 v63, v28, v29\n"
      "v_and_or_b32 v13, v12, s58, v42\n"
      "buffer_store_dwordx4 v[60:63], v13, s[24:27], s57 offen\n"
      ".LEx%=:\ns_endpgm\n"
      ".Lcsgsk%=:\n"
      "s_sleep 4\n"
      "s_sub_u32 s59, s59, 1\n"
      "s_cbranch_scc1 .Lcsgok%=\n"
      "buffer_load_dwordx4 v[64:67], v42, s[16:19], s62 offen sc1\n"
      "s_waitcnt vmcnt(0)\n"
      "s_branch .Lcsgk%=\n"
      ".Lcsgsh%=:\n"
      "s_sleep 4\n"
      "s_sub_u32 s59, s59, 1\n"
      "s_cbranch_scc1 .Lcsgoh%=\n"
      "buffer_load_dwordx4 v[68:71], v42, s[16:19], s63 offen sc1\n"
      "s_waitcnt vmcnt(0)\n"
      "s_branch .Lcsgh%=\n"
      ".Lch%=:\n"
      "s_movk_i32 s59, 0x7fff\n"
      ".Lchl%=:\n"
      "s_waitcnt vmcnt(0)\n"
      "v_cmp_ne_u32 vcc, s15, v253\n"
      "v_not_b32 v255, v252\n"
      "s_sub_u32 s59, s59, 1\n"
      "s_cbranch_scc1 .Lchk%=\n"
      "s_cbranch_vccz .Lchk%=\n"
      "s_sleep 1\n"
      "buffer_load_dwordx2 v[252:253], v9, s[16:19], s43 offen sc1\n"
      "s_branch .Lchl%=\n"
      ".Lchk%=:\n"
      "v_and_b32 v31, 0x1ff, v255\n"
      "v_bfe_u32 v8, v255, 9, 2\n"
      "v_lshl_add_u32 v31, v31, 2, v8\n"
      "v_mov_b32 v8, 64\n"
      "v_add_u32 v31, 0x27700, v31\n"
      "ds_write_b8 v31, v8 \n"
      "v_and_b32 v31, 1, v255\n"
      "v_and_b32 v8, 0x7fe, v255\n"
      "v_lshl_add_u32 v8, v31, 11, v8\n"
      "v_mul_u32_u24 v31, 0x1120, v10\n"
      "v_add_u32 v8, v8, v31\n"
      "v_lshlrev_b32 v31, 1, v10\n"
      "v_add_u32 v31, 0x27600, v31\n"
      "v_and_b32 v255, 0x7ff, v255\n"
      "v_lshl_add_u32 v251, v255, 11, v10\n"
      "v_and_b32 v252, 32, v43\n"
      "v_lshl_or_b32 v251, v252, 25, v251\n"
      "ds_read_b64 v[254:255], v31 \n"
      "v_mov_b32 v252, 0\n"
      "v_mov_b32 v253, 0\n"
      "s_waitcnt lgkmcnt(0)\n"
      "s_branch .Lcho%=\n"
      ".LG%=:\n"
      "s_setprio 3\n"
      "s_mul_i32 s35, s48, 14\n"
      "s_add_u32 s35, s35, s49\n"
      "s_and_b32 s55, s33, 3\n"
      "s_mul_i32 s50, s55, 1835008\n"
      "s_lshr_b32 s51, s35, 1\n"
      "s_lshl_b32 s51, s51, 7\n"
      "s_add_u32 s50, s50, s51\n"
      "v_lshrrev_b32 v37, 3, v43\n"
      "v_and_b32 v38, 7, v43\n"
      "v_mul_u32_u24 v37, 0xe000, v37\n"
      "v_lshlrev_b32 v38, 4, v38\n"
      "v_add3_u32 v32, v37, v38, s50\n"
      "s_cmp_lt_u32 s34, 112\n"
      "s_cselect_b32 s26, 0x700000, 0\n"
      "s_mov_b32 s27, 0x20000\n"
      "s_bitcmp1_b32 s49, 0\n"
      "s_waitcnt lgkmcnt(0)\n"
      "s_load_dword s15, s[88:89], 0x0\n"
      "s_cselect_b32 s24, s82, s80\n"
      "s_cselect_b32 s25, s83, s81\n"
      "s_and_b32 s25, s25, 0xffff\n"
      "s_mov_b32 s16, s88\n"
      "s_and_b32 s17, s89, 0xffff\n"
      "s_mov_b32 s18, 0x6e0000\n"
      "s_mov_b32 s19, 0x20000\n"
      "s_mov_b32 s36, s86\n"
      "s_and_b32 s37, s87, 0xffff\n"
      "s_sleep 26\n"
      "s_mov_b32 s60, 0\n"
      "buffer_load_dwordx4 v[64:67], v32, s[24:27], s60 offen nt\n"
      "s_sleep 2\n"
      "s_mov_b32 s60, 7168\n"
      "buffer_load_dwordx4 v[68:71], v32, s[24:27], s60 offen nt\n"
      "s_sleep 2\n"
      "s_mov_b32 s60, 14336\n"
      "buffer_load_dwordx4 v[72:75], v32, s[24:27], s60 offen nt\n"
      "s_sleep 2\n"
      "s_mov_b32 s60, 21504\n"
      "buffer_load_dwordx4 v[76:79], v32, s[24:27], s60 offen nt\n"
      "s_sleep 2\n"
      "s_mov_b32 s60, 28672\n"
      "buffer_load_dwordx4 v[80:83], v32, s[24:27], s60 offen nt\n"
      "s_sleep 2\n"
      "s_mov_b32 s60, 35840\n"
      "buffer_load_dwordx4 v[84:87], v32, s[24:27], s60 offen nt\n"
      "s_sleep 2\n"
      "s_mov_b32 s60, 43008\n"
      "buffer_load_dwordx4 v[88:91], v32, s[24:27], s60 offen nt\n"
      "s_sleep 2\n"
      "s_mov_b32 s60, 50176\n"
      "buffer_load_dwordx4 v[92:95], v32, s[24:27], s60 offen nt\n"
      "s_sleep 2\n"
      "s_mov_b32 s60, 458752\n"
      "buffer_load_dwordx4 v[96:99], v32, s[24:27], s60 offen nt\n"
      "s_sleep 2\n"
      "s_waitcnt lgkmcnt(0)\n"
      "s_add_u32 s15, s15, 0x9e3779b1\n"
      "v_mov_b32 v47, s15\n"
      "s_cmp_ge_u32 s34, 112\n"
      "s_cbranch_scc1 .LT%=\n"
      "v_mov_b32 v48, 0x22680\n"
      "ds_read_b32 v48, v48 \n"
      "s_waitcnt lgkmcnt(0)\n"
      "v_readfirstlane_b32 s12, v48\n"
      "s_mov_b32 s62, 0x05040100\n"
      "s_mov_b32 s63, 0x07060302\n"
      "s_lshl_b32 s42, s34, 3\n"
      "s_add_u32 s42, s42, 2048\n"
      "s_lshl_b32 s43, s48, 8\n"
      "s_and_b32 s50, s49, 1\n"
      "s_lshl_b32 s50, s50, 7\n"
      "s_add_u32 s43, s43, s50\n"
      "s_add_u32 s43, s43, 65536\n"
      "s_mov_b32 s20, s64\n"
      "s_and_b32 s21, s65, 0xffff\n"
      "s_mov_b32 s22, 0x400000\n"
      "s_mov_b32 s23, 0x20000\n"
      "v_lshrrev_b32 v39, 4, v43\n"
      "v_and_b32 v38, 15, v43\n"
      "v_lshlrev_b32 v38, 4, v38\n"
      "v_bfe_u32 v33, v43, 2, 2\n"
      "v_and_b32 v37, 1, v43\n"
      "v_lshl_add_u32 v33, v33, 1, v37\n"
      "v_lshlrev_b32 v33, 3, v33\n"
      "v_and_b32 v37, 2, v43\n"
      "v_lshl_add_u32 v37, v39, 2, v37\n"
      "s_lshl_b32 s50, s55, 9\n"
      "v_lshl_add_u32 v36, v37, 3, s50\n"
      "s_lshl_b32 s51, s55, 4\n"
      "v_lshrrev_b32 v40, 5, v43\n"
      "v_lshl_add_u32 v40, v40, 2, s51\n"
      "s_lshl_b32 s51, s55, 2\n"
      "s_add_u32 s51, s51, 140800\n"
      "v_mov_b32 v41, s51\n"
      "v_mov_b32 v42, 0x22600\n"
      "v_lshl_add_u32 v37, v43, 3, s50\n"
      "v_add_u32 v35, 0x22700, v37\n"
      "s_lshl_b32 s50, s55, 11\n"
      "s_add_u32 s50, s50, 141056\n"
      "v_lshl_add_u32 v34, v39, 9, v38\n"
      "v_add_u32 v34, s50, v34\n"
      "s_lshr_b32 s45, s35, 1\n"
      "s_lshl_b32 s45, s45, 16\n"
      "s_and_b32 s50, s35, 1\n"
      "s_lshl_b32 s50, s50, 11\n"
      "s_add_u32 s45, s45, s50\n"
      "s_add_u32 s45, s45, 524288\n"
      "s_mov_b32 s56, 0x3f80\n"
      "s_mov_b32 s57, 0x3f800000\n"
      "s_movk_i32 s61, 0x7ff\n"
      "s_mov_b32 s58, 131072\n"
      "s_mov_b32 s54, 0\n"
      "s_mov_b64 s[66:67], 0\n"
      "s_movk_i32 s70, 427\n"
      "s_waitcnt vmcnt(8)\n"
      "s_nop 0\n"
      "s_mov_b32 s60, 465920\n"
      "buffer_load_dwordx4 v[100:103], v32, s[24:27], s60 offen nt\n"
      "s_waitcnt vmcnt(8)\n"
      "s_nop 0\n"
      "s_mov_b32 s60, 473088\n"
      "buffer_load_dwordx4 v[104:107], v32, s[24:27], s60 offen nt\n"
      "v_perm_b32 v192, v68, v64, s62\n"
      "v_perm_b32 v196, v68, v64, s63\n"
      "v_perm_b32 v200, v69, v65, s62\n"
      "v_perm_b32 v204, v69, v65, s63\n"
      "v_perm_b32 v208, v70, v66, s62\n"
      "v_perm_b32 v212, v70, v66, s63\n"
      "v_perm_b32 v216, v71, v67, s62\n"
      "v_perm_b32 v220, v71, v67, s63\n"
      "s_waitcnt vmcnt(8)\n"
      "s_nop 0\n"
      "s_mov_b32 s60, 480256\n"
      "buffer_load_dwordx4 v[108:111], v32, s[24:27], s60 offen nt\n"
      "s_waitcnt vmcnt(8)\n"
      "s_nop 0\n"
      "s_mov_b32 s60, 487424\n"
      "buffer_load_dwordx4 v[112:115], v32, s[24:27], s60 offen nt\n"
      "v_perm_b32 v193, v76, v72, s62\n"
      "v_perm_b32 v197, v76, v72, s63\n"
      "v_perm_b32 v201, v77, v73, s62\n"
      "v_perm_b32 v205, v77, v73, s63\n"
      "v_perm_b32 v209, v78, v74, s62\n"
      "v_perm_b32 v213, v78, v74, s63\n"
      "v_perm_b32 v217, v79, v75, s62\n"
      "v_perm_b32 v221, v79, v75, s63\n"
      "s_waitcnt vmcnt(8)\n"
      "s_nop 0\n"
      "s_mov_b32 s60, 494592\n"
      "buffer_load_dwordx4 v[116:119], v32, s[24:27], s60 offen nt\n"
      "s_waitcnt vmcnt(8)\n"
      "s_nop 0\n"
      "s_mov_b32 s60, 501760\n"
      "buffer_load_dwordx4 v[120:123], v32, s[24:27], s60 offen nt\n"
      "v_perm_b32 v194, v84, v80, s62\n"
      "v_perm_b32 v198, v84, v80, s63\n"
      "v_perm_b32 v202, v85, v81, s62\n"
      "v_perm_b32 v206, v85, v81, s63\n"
      "v_perm_b32 v210, v86, v82, s62\n"
      "v_perm_b32 v214, v86, v82, s63\n"
      "v_perm_b32 v218, v87, v83, s62\n"
      "v_perm_b32 v222, v87, v83, s63\n"
      "s_waitcnt vmcnt(8)\n"
      "s_nop 0\n"
      "s_mov_b32 s60, 508928\n"
      "buffer_load_dwordx4 v[124:127], v32, s[24:27], s60 offen nt\n"
      "s_waitcnt vmcnt(8)\n"
      "s_nop 0\n"
      "s_mov_b32 s60, 917504\n"
      "buffer_load_dwordx4 v[128:131], v32, s[24:27], s60 offen nt\n"
      "v_perm_b32 v195, v92, v88, s62\n"
      "v_perm_b32 v199, v92, v88, s63\n"
      "v_perm_b32 v203, v93, v89, s62\n"
      "v_perm_b32 v207, v93, v89, s63\n"
      "v_perm_b32 v211, v94, v90, s62\n"
      "v_perm_b32 v215, v94, v90, s63\n"
      "v_perm_b32 v219, v95, v91, s62\n"
      "v_perm_b32 v223, v95, v91, s63\n"
      "s_waitcnt vmcnt(8)\n"
      "s_nop 0\n"
      "s_mov_b32 s60, 924672\n"
      "buffer_load_dwordx4 v[132:135], v32, s[24:27], s60 offen nt\n"
      "s_waitcnt vmcnt(8)\n"
      "s_nop 0\n"
      "s_mov_b32 s60, 931840\n"
      "buffer_load_dwordx4 v[136:139], v32, s[24:27], s60 offen nt\n"
      "v_perm_b32 v64, v100, v96, s62\n"
      "v_perm_b32 v68, v100, v96, s63\n"
      "v_perm_b32 v72, v101, v97, s62\n"
      "v_perm_b32 v76, v101, v97, s63\n"
      "v_perm_b32 v80, v102, v98, s62\n"
      "v_perm_b32 v84, v102, v98, s63\n"
      "v_perm_b32 v88, v103, v99, s62\n"
      "v_perm_b32 v92, v103, v99, s63\n"
      "s_waitcnt vmcnt(8)\n"
      "s_nop 0\n"
      "s_mov_b32 s60, 939008\n"
      "buffer_load_dwordx4 v[140:143], v32, s[24:27], s60 offen nt\n"
      "s_waitcnt vmcnt(8)\n"
      "s_nop 0\n"
      "s_mov_b32 s60, 946176\n"
      "buffer_load_dwordx4 v[144:147], v32, s[24:27], s60 offen nt\n"
      "v_perm_b32 v65, v108, v104, s62\n"
      "v_perm_b32 v69, v108, v104, s63\n"
      "v_perm_b32 v73, v109, v105, s62\n"
      "v_perm_b32 v77, v109, v105, s63\n"
      "v_perm_b32 v81, v110, v106, s62\n"
      "v_perm_b32 v85, v110, v106, s63\n"
      "v_perm_b32 v89, v111, v107, s62\n"
      "v_perm_b32 v93, v111, v107, s63\n"
      "s_waitcnt vmcnt(8)\n"
      "s_nop 0\n"
      "s_mov_b32 s60, 953344\n"
      "buffer_load_dwordx4 v[148:151], v32, s[24:27], s60 offen nt\n"
      "s_waitcnt vmcnt(8)\n"
      "s_nop 0\n"
      "s_mov_b32 s60, 960512\n"
      "buffer_load_dwordx4 v[152:155], v32, s[24:27], s60 offen nt\n"
      "v_perm_b32 v66, v116, v112, s62\n"
      "v_perm_b32 v70, v116, v112, s63\n"
      "v_perm_b32 v74, v117, v113, s62\n"
      "v_perm_b32 v78, v117, v113, s63\n"
      "v_perm_b32 v82, v118, v114, s62\n"
      "v_perm_b32 v86, v118, v114, s63\n"
      "v_perm_b32 v90, v119, v115, s62\n"
      "v_perm_b32 v94, v119, v115, s63\n"
      "s_waitcnt vmcnt(8)\n"
      "s_nop 0\n"
      "s_mov_b32 s60, 967680\n"
      "buffer_load_dwordx4 v[156:159], v32, s[24:27], s60 offen nt\n"
      "s_waitcnt vmcnt(8)\n"
      "s_nop 0\n"
      "s_mov_b32 s60, 1376256\n"
      "buffer_load_dwordx4 v[160:163], v32, s[24:27], s60 offen nt\n"
      "v_perm_b32 v67, v124, v120, s62\n"
      "v_perm_b32 v71, v124, v120, s63\n"
      "v_perm_b32 v75, v125, v121, s62\n"
      "v_perm_b32 v79, v125, v121, s63\n"
      "v_perm_b32 v83, v126, v122, s62\n"
      "v_perm_b32 v87, v126, v122, s63\n"
      "v_perm_b32 v91, v127, v123, s62\n"
      "v_perm_b32 v95, v127, v123, s63\n"
      "s_waitcnt vmcnt(8)\n"
      "s_nop 0\n"
      "s_mov_b32 s60, 1383424\n"
      "buffer_load_dwordx4 v[164:167], v32, s[24:27], s60 offen nt\n"
      "s_waitcnt vmcnt(8)\n"
      "s_nop 0\n"
      "s_mov_b32 s60, 1390592\n"
      "buffer_load_dwordx4 v[168:171], v32, s[24:27], s60 offen nt\n"
      "v_perm_b32 v96, v132, v128, s62\n"
      "v_perm_b32 v100, v132, v128, s63\n"
      "v_perm_b32 v104, v133, v129, s62\n"
      "v_perm_b32 v108, v133, v129, s63\n"
      "v_perm_b32 v112, v134, v130, s62\n"
      "v_perm_b32 v116, v134, v130, s63\n"
      "v_perm_b32 v120, v135, v131, s62\n"
      "v_perm_b32 v124, v135, v131, s63\n"
      "s_waitcnt vmcnt(8)\n"
      "s_nop 0\n"
      "s_mov_b32 s60, 1397760\n"
      "buffer_load_dwordx4 v[172:175], v32, s[24:27], s60 offen nt\n"
      "s_waitcnt vmcnt(8)\n"
      "s_nop 0\n"
      "s_mov_b32 s60, 1404928\n"
      "buffer_load_dwordx4 v[176:179], v32, s[24:27], s60 offen nt\n"
      "v_perm_b32 v97, v140, v136, s62\n"
      "v_perm_b32 v101, v140, v136, s63\n"
      "v_perm_b32 v105, v141, v137, s62\n"
      "v_perm_b32 v109, v141, v137, s63\n"
      "v_perm_b32 v113, v142, v138, s62\n"
      "v_perm_b32 v117, v142, v138, s63\n"
      "v_perm_b32 v121, v143, v139, s62\n"
      "v_perm_b32 v125, v143, v139, s63\n"
      "s_waitcnt vmcnt(8)\n"
      "s_nop 0\n"
      "s_mov_b32 s60, 1412096\n"
      "buffer_load_dwordx4 v[180:183], v32, s[24:27], s60 offen nt\n"
      "s_waitcnt vmcnt(8)\n"
      "s_nop 0\n"
      "s_mov_b32 s60, 1419264\n"
      "buffer_load_dwordx4 v[184:187], v32, s[24:27], s60 offen nt\n"
      "v_perm_b32 v98, v148, v144, s62\n"
      "v_perm_b32 v102, v148, v144, s63\n"
      "v_perm_b32 v106, v149, v145, s62\n"
      "v_perm_b32 v110, v149, v145, s63\n"
      "v_perm_b32 v114, v150, v146, s62\n"
      "v_perm_b32 v118, v150, v146, s63\n"
      "v_perm_b32 v122, v151, v147, s62\n"
      "v_perm_b32 v126, v151, v147, s63\n"
      "s_waitcnt vmcnt(8)\n"
      "s_nop 0\n"
      "s_mov_b32 s60, 1426432\n"
      "buffer_load_dwordx4 v[188:191], v32, s[24:27], s60 offen nt\n"
      "ds_write_b32 v41, v47 offset:80\n"
      "s_waitcnt vmcnt(8)\n"
      "s_nop 0\n"
      "v_perm_b32 v99, v156, v152, s62\n"
      "v_perm_b32 v103, v156, v152, s63\n"
      "v_perm_b32 v107, v157, v153, s62\n"
      "v_perm_b32 v111, v157, v153, s63\n"
      "v_perm_b32 v115, v158, v154, s62\n"
      "v_perm_b32 v119, v158, v154, s63\n"
      "v_perm_b32 v123, v159, v155, s62\n"
      "v_perm_b32 v127, v159, v155, s63\n"
      "s_waitcnt vmcnt(7)\n"
      "s_nop 0\n"
      "s_memrealtime s[68:69]\n"
      "s_waitcnt vmcnt(6)\n"
      "s_nop 0\n"
      "s_waitcnt lgkmcnt(0)\n"
      "s_sub_u32 s68, s68, s12\n"
      "s_cmp_lt_u32 s68, s70\n"
      "s_cbranch_scc1 .Lgn25%=\n"
      "buffer_load_dwordx2 v[8:9], v33, s[16:19], s43 offen sc1\n"
      "s_mov_b32 s70, -1\n"
      ".Lgn25%=:\n"
      "s_memrealtime s[68:69]\n"
      "v_perm_b32 v128, v164, v160, s62\n"
      "v_perm_b32 v132, v164, v160, s63\n"
      "v_perm_b32 v136, v165, v161, s62\n"
      "v_perm_b32 v140, v165, v161, s63\n"
      "v_perm_b32 v144, v166, v162, s62\n"
      "v_perm_b32 v148, v166, v162, s63\n"
      "v_perm_b32 v152, v167, v163, s62\n"
      "v_perm_b32 v156, v167, v163, s63\n"
      "s_waitcnt vmcnt(5)\n"
      "s_nop 0\n"
      "s_waitcnt lgkmcnt(0)\n"
      "s_sub_u32 s68, s68, s12\n"
      "s_cmp_lt_u32 s68, s70\n"
      "s_cbranch_scc1 .Lgn26%=\n"
      "buffer_load_dwordx2 v[8:9], v33, s[16:19], s43 offen sc1\n"
      "s_mov_b32 s70, -1\n"
      ".Lgn26%=:\n"
      "s_memrealtime s[68:69]\n"
      "s_waitcnt vmcnt(4)\n"
      "s_nop 0\n"
      "s_waitcnt lgkmcnt(0)\n"
      "s_sub_u32 s68, s68, s12\n"
      "s_cmp_lt_u32 s68, s70\n"
      "s_cbranch_scc1 .Lgn27%=\n"
      "buffer_load_dwordx2 v[8:9], v33, s[16:19], s43 offen sc1\n"
      "s_mov_b32 s70, -1\n"
      ".Lgn27%=:\n"
      "s_memrealtime s[68:69]\n"
      "v_perm_b32 v129, v172, v168, s62\n"
      "v_perm_b32 v133, v172, v168, s63\n"
      "v_perm_b32 v137, v173, v169, s62\n"
      "v_perm_b32 v141, v173, v169, s63\n"
      "v_perm_b32 v145, v174, v170, s62\n"
      "v_perm_b32 v149, v174, v170, s63\n"
      "v_perm_b32 v153, v175, v171, s62\n"
      "v_perm_b32 v157, v175, v171, s63\n"
      "s_waitcnt vmcnt(3)\n"
      "s_nop 0\n"
      "s_waitcnt lgkmcnt(0)\n"
      "s_sub_u32 s68, s68, s12\n"
      "s_cmp_lt_u32 s68, s70\n"
      "s_cbranch_scc1 .Lgn28%=\n"
      "buffer_load_dwordx2 v[8:9], v33, s[16:19], s43 offen sc1\n"
      "s_mov_b32 s70, -1\n"
      ".Lgn28%=:\n"
      "s_memrealtime s[68:69]\n"
      "s_waitcnt vmcnt(2)\n"
      "s_nop 0\n"
      "s_waitcnt lgkmcnt(0)\n"
      "s_sub_u32 s68, s68, s12\n"
      "s_cmp_lt_u32 s68, s70\n"
      "s_cbranch_scc1 .Lgn29%=\n"
      "buffer_load_dwordx2 v[8:9], v33, s[16:19], s43 offen sc1\n"
      "s_mov_b32 s70, -1\n"
      ".Lgn29%=:\n"
      "s_memrealtime s[68:69]\n"
      "v_perm_b32 v130, v180, v176, s62\n"
      "v_perm_b32 v134, v180, v176, s63\n"
      "v_perm_b32 v138, v181, v177, s62\n"
      "v_perm_b32 v142, v181, v177, s63\n"
      "v_perm_b32 v146, v182, v178, s62\n"
      "v_perm_b32 v150, v182, v178, s63\n"
      "v_perm_b32 v154, v183, v179, s62\n"
      "v_perm_b32 v158, v183, v179, s63\n"
      "s_waitcnt vmcnt(1)\n"
      "s_nop 0\n"
      "s_waitcnt lgkmcnt(0)\n"
      "s_sub_u32 s68, s68, s12\n"
      "s_cmp_lt_u32 s68, s70\n"
      "s_cbranch_scc1 .Lgn30%=\n"
      "buffer_load_dwordx2 v[8:9], v33, s[16:19], s43 offen sc1\n"
      "s_mov_b32 s70, -1\n"
      ".Lgn30%=:\n"
      "s_memrealtime s[68:69]\n"
      "s_waitcnt vmcnt(0)\n"
      "s_nop 0\n"
      "s_waitcnt lgkmcnt(0)\n"
      "s_sub_u32 s68, s68, s12\n"
      "s_cmp_lt_u32 s68, s70\n"
      "s_cbranch_scc1 .Lgn31%=\n"
      "buffer_load_dwordx2 v[8:9], v33, s[16:19], s43 offen sc1\n"
      "s_mov_b32 s70, -1\n"
      ".Lgn31%=:\n"
      "v_perm_b32 v131, v188, v184, s62\n"
      "v_perm_b32 v135, v188, v184, s63\n"
      "v_perm_b32 v139, v189, v185, s62\n"
      "v_perm_b32 v143, v189, v185, s63\n"
      "v_perm_b32 v147, v190, v186, s62\n"
      "v_perm_b32 v151, v190, v186, s63\n"
      "v_perm_b32 v155, v191, v187, s62\n"
      "v_perm_b32 v159, v191, v187, s63\n"
      "s_movk_i32 s59, 0x3fff\n"
      "s_cmp_eq_u32 s70, -1\n"
      "s_cbranch_scc1 .Lghc%=\n"
      ".Lgpc3_%=:\n"
      "s_memrealtime s[68:69]\n"
      "s_waitcnt lgkmcnt(0)\n"
      "s_sub_u32 s68, s68, s12\n"
      "s_cmp_ge_u32 s68, 435\n"
      "s_cbranch_scc1 .Lgpd3_%=\n"
      "s_sleep 1\n"
      "s_branch .Lgpc3_%=\n"
      ".Lgpd3_%=:\n"
      ".Lgh%=:\n"
      "buffer_load_dwordx2 v[8:9], v33, s[16:19], s43 offen sc1\n"
      ".Lghc%=:\n"
      "s_waitcnt vmcnt(0)\n"
      "v_cmp_ne_u32 vcc, s15, v9\n"
      "v_bitop3_b32 v10, v8, s61, v8 bitop3:0x0c\n"
      "s_sub_u32 s59, s59, 1\n"
      "s_cbranch_vccz .Lgho%=\n"
      "s_sleep 2\n"
      "s_cbranch_scc0 .Lgh%=\n"
      ".Lgho%=:\n"
      "v_lshrrev_b32 v45, 9, v10\n"
      "v_lshl_add_u32 v11, v10, 11, v36\n"
      "v_mul_u32_u24 v45, 0xf000, v45\n"
      "v_lshl_add_u32 v45, v10, 3, v45\n"
      "buffer_load_dwordx2 v[12:13], off, s[16:19], s43 offset:64 sc1\n"
      "buffer_load_dwordx2 v[62:63], v45, s[16:19], s58 offen sc1\n"
      "buffer_load_dwordx4 v[16:19], v11, s[20:23], 0 offen offset:0\n"
      "buffer_load_dwordx4 v[20:23], v11, s[20:23], 0 offen offset:128\n"
      "buffer_load_dwordx4 v[24:27], v11, s[20:23], 0 offen offset:256\n"
      "buffer_load_dwordx4 v[28:31], v11, s[20:23], 0 offen offset:384\n"
      "ds_write_b32 v41, v47 offset:64\n"
      ".Lghx%=:\n"
      "s_movk_i32 s59, 0x7fff\n"
      "s_waitcnt vmcnt(4)\n"
      "s_nop 0\n"
      ".Lgs%=:\n"
      "v_cmp_ne_u32 vcc, s15, v63\n"
      "v_lshlrev_b32 v10, 16, v62\n"
      "v_and_b32 v44, 0xffff0000, v62\n"
      "s_cbranch_vccnz .Lgss%=\n"
      ".Lgso%=:\n"
      "v_rcp_f32 v44, v44\n"
      "v_xor_b32 v10, 0x80000000, v10\n"
      "v_readfirstlane_b32 s68, v13\n"
      "v_readfirstlane_b32 s69, v12\n"
      "s_cmp_eq_u32 s68, s15\n"
      "s_cselect_b32 s12, s69, s12\n"
      "v_mov_b32 v12, s12\n"
      "ds_write_b32 v42, v12 offset:128\n"
      "s_waitcnt vmcnt(3)\n"
      "v_dot2_f32_bf16 v168, v16, s56, v10\n"
      "v_dot2_f32_bf16 v169, v16, s57, v10\n"
      "v_dot2_f32_bf16 v170, v17, s56, v10\n"
      "v_dot2_f32_bf16 v171, v17, s57, v10\n"
      "v_dot2_f32_bf16 v172, v18, s56, v10\n"
      "v_dot2_f32_bf16 v173, v18, s57, v10\n"
      "v_dot2_f32_bf16 v174, v19, s56, v10\n"
      "v_dot2_f32_bf16 v175, v19, s57, v10\n"
      "v_cvt_pk_bf16_f32 v16, v168, v169\n"
      "v_cvt_pk_bf16_f32 v17, v170, v171\n"
      "v_cvt_pk_bf16_f32 v18, v172, v173\n"
      "v_cvt_pk_bf16_f32 v19, v174, v175\n"
      "ds_bpermute_b32 v44, v40, v44\n"
      "s_waitcnt vmcnt(2)\n"
      "v_dot2_f32_bf16 v176, v20, s56, v10\n"
      "v_dot2_f32_bf16 v177, v20, s57, v10\n"
      "v_dot2_f32_bf16 v178, v21, s56, v10\n"
      "v_dot2_f32_bf16 v179, v21, s57, v10\n"
      "v_dot2_f32_bf16 v180, v22, s56, v10\n"
      "v_dot2_f32_bf16 v181, v22, s57, v10\n"
      "v_dot2_f32_bf16 v182, v23, s56, v10\n"
      "v_dot2_f32_bf16 v183, v23, s57, v10\n"
      "v_cvt_pk_bf16_f32 v20, v176, v177\n"
      "v_cvt_pk_bf16_f32 v21, v178, v179\n"
      "v_cvt_pk_bf16_f32 v22, v180, v181\n"
      "v_cvt_pk_bf16_f32 v23, v182, v183\n"
      "s_waitcnt vmcnt(1)\n"
      "v_dot2_f32_bf16 v168, v24, s56, v10\n"
      "v_dot2_f32_bf16 v169, v24, s57, v10\n"
      "v_dot2_f32_bf16 v170, v25, s56, v10\n"
      "v_dot2_f32_bf16 v171, v25, s57, v10\n"
      "v_dot2_f32_bf16 v172, v26, s56, v10\n"
      "v_dot2_f32_bf16 v173, v26, s57, v10\n"
      "v_dot2_f32_bf16 v174, v27, s56, v10\n"
      "v_dot2_f32_bf16 v175, v27, s57, v10\n"
      "v_cvt_pk_bf16_f32 v24, v168, v169\n"
      "v_cvt_pk_bf16_f32 v25, v170, v171\n"
      "v_cvt_pk_bf16_f32 v26, v172, v173\n"
      "v_cvt_pk_bf16_f32 v27, v174, v175\n"
      "s_waitcnt vmcnt(0)\n"
      "v_dot2_f32_bf16 v176, v28, s56, v10\n"
      "v_dot2_f32_bf16 v177, v28, s57, v10\n"
      "v_dot2_f32_bf16 v178, v29, s56, v10\n"
      "v_dot2_f32_bf16 v179, v29, s57, v10\n"
      "v_dot2_f32_bf16 v180, v30, s56, v10\n"
      "v_dot2_f32_bf16 v181, v30, s57, v10\n"
      "v_dot2_f32_bf16 v182, v31, s56, v10\n"
      "v_dot2_f32_bf16 v183, v31, s57, v10\n"
      "v_cvt_pk_bf16_f32 v28, v176, v177\n"
      "v_cvt_pk_bf16_f32 v29, v178, v179\n"
      "v_cvt_pk_bf16_f32 v30, v180, v181\n"
      "v_cvt_pk_bf16_f32 v31, v182, v183\n"
      "v_mfma_f32_16x16x32_bf16 v[224:227], v[16:19], v[192:195], 0\n"
      "v_mfma_f32_16x16x32_bf16 v[228:231], v[16:19], v[196:199], 0\n"
      "v_mfma_f32_16x16x32_bf16 v[232:235], v[16:19], v[200:203], 0\n"
      "v_mfma_f32_16x16x32_bf16 v[236:239], v[16:19], v[204:207], 0\n"
      "v_mfma_f32_16x16x32_bf16 v[240:243], v[16:19], v[208:211], 0\n"
      "v_mfma_f32_16x16x32_bf16 v[244:247], v[16:19], v[212:215], 0\n"
      "v_mfma_f32_16x16x32_bf16 v[248:251], v[16:19], v[216:219], 0\n"
      "v_mfma_f32_16x16x32_bf16 v[252:255], v[16:19], v[220:223], 0\n"
      "v_mfma_f32_16x16x32_bf16 v[224:227], v[20:23], v[64:67], v[224:227]\n"
      "v_mfma_f32_16x16x32_bf16 v[228:231], v[20:23], v[68:71], v[228:231]\n"
      "v_mfma_f32_16x16x32_bf16 v[232:235], v[20:23], v[72:75], v[232:235]\n"
      "v_mfma_f32_16x16x32_bf16 v[236:239], v[20:23], v[76:79], v[236:239]\n"
      "v_mfma_f32_16x16x32_bf16 v[240:243], v[20:23], v[80:83], v[240:243]\n"
      "v_mfma_f32_16x16x32_bf16 v[244:247], v[20:23], v[84:87], v[244:247]\n"
      "v_mfma_f32_16x16x32_bf16 v[248:251], v[20:23], v[88:91], v[248:251]\n"
      "v_mfma_f32_16x16x32_bf16 v[252:255], v[20:23], v[92:95], v[252:255]\n"
      "v_mfma_f32_16x16x32_bf16 v[224:227], v[24:27], v[96:99], v[224:227]\n"
      "v_mfma_f32_16x16x32_bf16 v[228:231], v[24:27], v[100:103], v[228:231]\n"
      "v_mfma_f32_16x16x32_bf16 v[232:235], v[24:27], v[104:107], v[232:235]\n"
      "v_mfma_f32_16x16x32_bf16 v[236:239], v[24:27], v[108:111], v[236:239]\n"
      "v_mfma_f32_16x16x32_bf16 v[240:243], v[24:27], v[112:115], v[240:243]\n"
      "v_mfma_f32_16x16x32_bf16 v[244:247], v[24:27], v[116:119], v[244:247]\n"
      "v_mfma_f32_16x16x32_bf16 v[248:251], v[24:27], v[120:123], v[248:251]\n"
      "v_mfma_f32_16x16x32_bf16 v[252:255], v[24:27], v[124:127], v[252:255]\n"
      "v_mfma_f32_16x16x32_bf16 v[224:227], v[28:31], v[128:131], v[224:227]\n"
      "v_mfma_f32_16x16x32_bf16 v[228:231], v[28:31], v[132:135], v[228:231]\n"
      "v_mfma_f32_16x16x32_bf16 v[232:235], v[28:31], v[136:139], v[232:235]\n"
      "v_mfma_f32_16x16x32_bf16 v[236:239], v[28:31], v[140:143], v[236:239]\n"
      "s_nop 4\n"
      "v_add_f32_dpp v160, v226, v224 row_ror:8 row_mask:0xf bank_mask:0x3\n"
      "v_add_f32_dpp v164, v227, v225 row_ror:8 row_mask:0xf bank_mask:0x3\n"
      "v_mfma_f32_16x16x32_bf16 v[240:243], v[28:31], v[144:147], v[240:243]\n"
      "v_add_f32_dpp v161, v230, v228 row_ror:8 row_mask:0xf bank_mask:0x3\n"
      "v_add_f32_dpp v165, v231, v229 row_ror:8 row_mask:0xf bank_mask:0x3\n"
      "v_mfma_f32_16x16x32_bf16 v[244:247], v[28:31], v[148:151], v[244:247]\n"
      "v_add_f32_dpp v162, v234, v232 row_ror:8 row_mask:0xf bank_mask:0x3\n"
      "v_add_f32_dpp v166, v235, v233 row_ror:8 row_mask:0xf bank_mask:0x3\n"
      "v_mfma_f32_16x16x32_bf16 v[248:251], v[28:31], v[152:155], v[248:251]\n"
      "v_add_f32_dpp v163, v238, v236 row_ror:8 row_mask:0xf bank_mask:0x3\n"
      "v_add_f32_dpp v167, v239, v237 row_ror:8 row_mask:0xf bank_mask:0x3\n"
      "v_mfma_f32_16x16x32_bf16 v[252:255], v[28:31], v[156:159], v[252:255]\n"
      "v_add_f32_dpp v160, v240, v242 row_ror:8 row_mask:0xf bank_mask:0xc\n"
      "v_add_f32_dpp v164, v241, v243 row_ror:8 row_mask:0xf bank_mask:0xc\n"
      "v_add_f32_dpp v161, v244, v246 row_ror:8 row_mask:0xf bank_mask:0xc\n"
      "v_add_f32_dpp v165, v245, v247 row_ror:8 row_mask:0xf bank_mask:0xc\n"
      "s_nop 0\n"
      "v_add_f32_dpp v162, v248, v250 row_ror:8 row_mask:0xf bank_mask:0xc\n"
      "v_add_f32_dpp v166, v249, v251 row_ror:8 row_mask:0xf bank_mask:0xc\n"
      "s_nop 0\n"
      "v_add_f32_dpp v163, v252, v254 row_ror:8 row_mask:0xf bank_mask:0xc\n"
      "v_add_f32_dpp v167, v253, v255 row_ror:8 row_mask:0xf bank_mask:0xc\n"
      "ds_write_b128 v34, v[160:163] \n"
      "ds_write_b128 v34, v[164:167] offset:256\n"
      "ds_write_b32 v41, v47 \n"
      "s_movk_i32 s59, 0x7fff\n"
      ".Lgw%=:\n"
      "ds_read_b128 v[72:75], v42 \n"
      "ds_read_b64 v[64:65], v35 offset:0\n"
      "ds_read_b64 v[66:67], v35 offset:2048\n"
      "ds_read_b64 v[68:69], v35 offset:4096\n"
      "ds_read_b64 v[70:71], v35 offset:6144\n"
      "s_sub_u32 s59, s59, 1\n"
      "s_cbranch_scc1 .Lgwo%=\n"
      "s_waitcnt lgkmcnt(4)\n"
      "v_xor_b32 v72, s15, v72\n"
      "v_bitop3_b32 v72, s15, v73, v72 bitop3:0xbe\n"
      "v_bitop3_b32 v72, s15, v74, v72 bitop3:0xbe\n"
      "v_bitop3_b32 v72, s15, v75, v72 bitop3:0xbe\n"
      "v_cmp_ne_u32 vcc, 0, v72\n"
      "s_waitcnt lgkmcnt(0)\n"
      "s_nop 0\n"
      "s_cbranch_vccnz .Lgw%=\n"
      ".Lgwo%=:\n"
      "s_waitcnt lgkmcnt(0)\n"
      "s_bitcmp1_b32 s49, 0\n"
      "v_pk_add_f32 v[64:65], v[64:65], v[66:67]\n"
      "v_pk_add_f32 v[68:69], v[68:69], v[70:71]\n"
      "v_pk_add_f32 v[64:65], v[64:65], v[68:69]\n"
      "v_mul_f32 v64, v64, v44\n"
      "v_mul_f32 v65, v65, v44\n"
      "v_cvt_pk_bf16_f32 v46, v64, v65\n"
      "s_cbranch_scc1 .Lgu%=\n"
      "v_lshlrev_b32 v64, 16, v46\n"
      "v_and_b32 v65, 0xffff0000, v46\n"
      "v_mul_f32 v66, 0xbfb8aa3b, v64\n"
      "v_mul_f32 v67, 0xbfb8aa3b, v65\n"
      "v_exp_f32 v66, v66\n"
      "v_exp_f32 v67, v67\n"
      "v_add_f32 v66, 1.0, v66\n"
      "v_add_f32 v67, 1.0, v67\n"
      "v_rcp_f32 v66, v66\n"
      "v_rcp_f32 v67, v67\n"
      "v_mul_f32 v64, v64, v66\n"
      "v_mul_f32 v65, v65, v67\n"
      "v_cvt_pk_bf16_f32 v46, v64, v65\n"
      ".Lgu%=:\n"
      "buffer_store_dwordx2 v[46:47], v37, s[16:19], s45 offen sc1\n"
      "s_cmp_lg_u32 s33, 4\n"
      "s_cbranch_scc1 .Lga%=\n"
      "v_mov_b32 v64, s15\n"
      "v_mov_b32 v65, s15\n"
      "buffer_store_dwordx2 v[64:65], off, s[16:19], s42 sc1\n"
      ".Lga%=:\n"
      "s_cmp_eq_u32 s33, 7\n"
      "s_mov_b32 s24, s36\n"
      "s_mov_b32 s25, s37\n"
      "s_mov_b32 s26, 0x400000\n"
      "s_cbranch_scc1 .LZ%=\n"
      "s_endpgm\n"
      ".Lgss%=:\n"
      "s_sleep 1\n"
      "s_sub_u32 s59, s59, 1\n"
      "s_cbranch_scc1 .Lgso%=\n"
      "buffer_load_dwordx2 v[62:63], v45, s[16:19], s58 offen sc1\n"
      "s_waitcnt vmcnt(0)\n"
      "s_branch .Lgs%=\n"
      ".LD2%=:\n"
      "s_setprio 0\n"
      "s_mul_i32 s35, s48, 14\n"
      "s_add_u32 s35, s35, s49\n"
      "s_lshr_b32 s52, s35, 2\n"
      "s_and_b32 s54, s35, 3\n"
      "v_lshlrev_b32 v42, 4, v49\n"
      "s_lshl_b32 s50, s52, 18\n"
      "s_lshl_b32 s51, s54, 9\n"
      "s_add_u32 s50, s50, s51\n"
      "s_lshl_b32 s51, s33, 7\n"
      "s_add_u32 s50, s50, s51\n"
      "v_lshl_add_u32 v32, v50, 14, v42\n"
      "v_add_u32 v32, s50, v32\n"
      "v_lshrrev_b32 v41, 1, v49\n"
      "v_and_b32 v43, 1, v41\n"
      "v_mul_u32_u24 v36, 0x120, v41\n"
      "v_lshlrev_b32 v43, 4, v43\n"
      "v_sub_u32 v36, v36, v43\n"
      "v_and_b32 v43, 1, v49\n"
      "v_lshl_add_u32 v36, v43, 7, v36\n"
      "v_lshrrev_b32 v41, 4, v48\n"
      "v_lshl_add_u32 v36, v41, 4, v36\n"
      "v_and_b32 v43, 8, v48\n"
      "v_add_u32 v36, v36, v43\n"
      "s_lshr_b32 s53, s33, 1\n"
      "s_mul_i32 s50, s53, 1152\n"
      "s_and_b32 s55, s33, 1\n"
      "s_lshl_b32 s51, s55, 6\n"
      "s_add_u32 s50, s50, s51\n"
      "s_add_u32 s50, s50, 149504\n"
      "v_add_u32 v36, s50, v36\n"
      "v_and_b32 v43, 1, v41\n"
      "v_mul_u32_u24 v37, 0x120, v41\n"
      "v_lshlrev_b32 v43, 4, v43\n"
      "v_sub_u32 v37, v37, v43\n"
      "v_and_b32 v43, 15, v48\n"
      "v_lshl_add_u32 v37, v43, 4, v37\n"
      "v_add_u32 v37, 0x24800, v37\n"
      "v_lshlrev_b32 v35, 4, v48\n"
      "s_lshl_b32 s46, s52, 17\n"
      "s_lshl_b32 s50, s53, 16\n"
      "s_add_u32 s46, s46, s50\n"
      "s_lshl_b32 s50, s55, 10\n"
      "s_add_u32 s46, s46, s50\n"
      "s_add_u32 s46, s46, 524288\n"
      "v_and_b32 v41, 3, v50\n"
      "v_lshrrev_b32 v43, 5, v48\n"
      "v_lshl_add_u32 v41, v41, 1, v43\n"
      "v_mul_u32_u24 v38, 0x1c00, v41\n"
      "v_add_u32 v38, v38, v42\n"
      "s_lshl_b32 s50, s54, 2\n"
      "s_add_u32 s50, s50, s33\n"
      "s_mul_i32 s47, s50, 57344\n"
      "s_lshl_b32 s50, s52, 8\n"
      "s_add_u32 s47, s47, s50\n"
      "s_add_u32 s47, s47, 4194304\n"
      "s_mov_b32 s62, 0x05040100\n"
      "s_mov_b32 s63, 0x07060302\n"
      "s_lshl_b32 s50, s33, 2\n"
      "s_add_u32 s50, s50, 140864\n"
      "v_mov_b32 v41, s50\n"
      "v_mov_b32 v42, 0x22640\n"
      "s_movk_i32 s59, 0x3fff\n"
      ".Ldf%=:\n"
      "s_sleep 1\n"
      "ds_read_b128 v[240:243], v42\n"
      "ds_read_b32 v43, v41 offset:16\n"
      "s_sub_u32 s59, s59, 1\n"
      "s_cbranch_scc1 .Ldfo%=\n"
      "s_waitcnt lgkmcnt(0)\n"
      "v_xor_b32 v240, s15, v240\n"
      "v_bitop3_b32 v240, s15, v241, v240 bitop3:0xbe\n"
      "v_bitop3_b32 v240, s15, v242, v240 bitop3:0xbe\n"
      "v_bitop3_b32 v240, s15, v243, v240 bitop3:0xbe\n"
      "v_bitop3_b32 v240, s15, v43, v240 bitop3:0xbe\n"
      "v_cmp_ne_u32 vcc, 0, v240\n"
      "s_nop 1\n"
      "s_cbranch_vccnz .Ldf%=\n"
      ".Ldfo%=:\n"
      "s_mov_b32 s60, 0\n"
      "buffer_load_dwordx4 v[128:131], v32, s[28:31], s60 offen nt\n"
      "s_mov_b32 s60, 2048\n"
      "buffer_load_dwordx4 v[132:135], v32, s[28:31], s60 offen nt\n"
      "s_mov_b32 s60, 4096\n"
      "buffer_load_dwordx4 v[136:139], v32, s[28:31], s60 offen nt\n"
      "s_mov_b32 s60, 6144\n"
      "buffer_load_dwordx4 v[140:143], v32, s[28:31], s60 offen nt\n"
      "s_mov_b32 s60, 8192\n"
      "buffer_load_dwordx4 v[144:147], v32, s[28:31], s60 offen nt\n"
      "s_mov_b32 s60, 10240\n"
      "buffer_load_dwordx4 v[148:151], v32, s[28:31], s60 offen nt\n"
      "s_mov_b32 s60, 12288\n"
      "buffer_load_dwordx4 v[152:155], v32, s[28:31], s60 offen nt\n"
      "s_mov_b32 s60, 14336\n"
      "buffer_load_dwordx4 v[156:159], v32, s[28:31], s60 offen nt\n"
      "s_waitcnt vmcnt(7)\n"
      "s_nop 0\n"
      "s_mov_b32 s60, 131072\n"
      "buffer_load_dwordx4 v[160:163], v32, s[28:31], s60 offen nt\n"
      "s_waitcnt vmcnt(7)\n"
      "s_nop 0\n"
      "s_mov_b32 s60, 133120\n"
      "buffer_load_dwordx4 v[164:167], v32, s[28:31], s60 offen nt\n"
      "v_perm_b32 v64, v132, v128, s62\n"
      "v_perm_b32 v68, v132, v128, s63\n"
      "v_perm_b32 v72, v133, v129, s62\n"
      "v_perm_b32 v76, v133, v129, s63\n"
      "v_perm_b32 v80, v134, v130, s62\n"
      "v_perm_b32 v84, v134, v130, s63\n"
      "v_perm_b32 v88, v135, v131, s62\n"
      "v_perm_b32 v92, v135, v131, s63\n"
      "s_waitcnt vmcnt(7)\n"
      "s_nop 0\n"
      "s_mov_b32 s60, 135168\n"
      "buffer_load_dwordx4 v[168:171], v32, s[28:31], s60 offen nt\n"
      "s_waitcnt vmcnt(7)\n"
      "s_nop 0\n"
      "s_mov_b32 s60, 137216\n"
      "buffer_load_dwordx4 v[172:175], v32, s[28:31], s60 offen nt\n"
      "v_perm_b32 v65, v140, v136, s62\n"
      "v_perm_b32 v69, v140, v136, s63\n"
      "v_perm_b32 v73, v141, v137, s62\n"
      "v_perm_b32 v77, v141, v137, s63\n"
      "v_perm_b32 v81, v142, v138, s62\n"
      "v_perm_b32 v85, v142, v138, s63\n"
      "v_perm_b32 v89, v143, v139, s62\n"
      "v_perm_b32 v93, v143, v139, s63\n"
      "s_waitcnt vmcnt(7)\n"
      "s_nop 0\n"
      "s_mov_b32 s60, 139264\n"
      "buffer_load_dwordx4 v[176:179], v32, s[28:31], s60 offen nt\n"
      "s_waitcnt vmcnt(7)\n"
      "s_nop 0\n"
      "s_mov_b32 s60, 141312\n"
      "buffer_load_dwordx4 v[180:183], v32, s[28:31], s60 offen nt\n"
      "v_perm_b32 v66, v148, v144, s62\n"
      "v_perm_b32 v70, v148, v144, s63\n"
      "v_perm_b32 v74, v149, v145, s62\n"
      "v_perm_b32 v78, v149, v145, s63\n"
      "v_perm_b32 v82, v150, v146, s62\n"
      "v_perm_b32 v86, v150, v146, s63\n"
      "v_perm_b32 v90, v151, v147, s62\n"
      "v_perm_b32 v94, v151, v147, s63\n"
      "s_waitcnt vmcnt(7)\n"
      "s_nop 0\n"
      "s_mov_b32 s60, 143360\n"
      "buffer_load_dwordx4 v[184:187], v32, s[28:31], s60 offen nt\n"
      "s_waitcnt vmcnt(7)\n"
      "s_nop 0\n"
      "s_mov_b32 s60, 145408\n"
      "buffer_load_dwordx4 v[188:191], v32, s[28:31], s60 offen nt\n"
      "v_perm_b32 v67, v156, v152, s62\n"
      "v_perm_b32 v71, v156, v152, s63\n"
      "v_perm_b32 v75, v157, v153, s62\n"
      "v_perm_b32 v79, v157, v153, s63\n"
      "v_perm_b32 v83, v158, v154, s62\n"
      "v_perm_b32 v87, v158, v154, s63\n"
      "v_perm_b32 v91, v159, v155, s62\n"
      "v_perm_b32 v95, v159, v155, s63\n"
      "s_waitcnt vmcnt(7)\n"
      "s_nop 0\n"
      "s_movk_i32 s59, 0x3fff\n"
      "v_add_u32 v41, 0xffffffc0, v41\n"
      ".Lde%=:\n"
      "ds_read_b32 v43, v41\n"
      "s_sub_u32 s59, s59, 1\n"
      "s_cbranch_scc1 .Ldeo%=\n"
      "s_waitcnt lgkmcnt(0)\n"
      "v_cmp_ne_u32 vcc, s15, v43\n"
      "s_nop 1\n"
      "s_cbranch_vccz .Ldeo%=\n"
      "s_sleep 2\n"
      "s_branch .Lde%=\n"
      ".Ldeo%=:\n"
      "s_cmp_lg_u32 s59, 0x3ffe\n"
      "s_cbranch_scc0 .Ldep%=\n"
      "s_sleep 6\n"
      ".Ldep%=:\n"
      "ds_read_b32 v43, v42 offset:64\n"
      "s_waitcnt lgkmcnt(0)\n"
      "v_readfirstlane_b32 s12, v43\n"
      "s_nop 0\n"
      ".Ldpc1_%=:\n"
      "s_memrealtime s[64:65]\n"
      "s_waitcnt lgkmcnt(0)\n"
      "s_sub_u32 s64, s64, s12\n"
      "s_cmp_ge_u32 s64, 640\n"
      "s_cbranch_scc1 .Ldpd1_%=\n"
      "s_sleep 1\n"
      "s_branch .Ldpc1_%=\n"
      ".Ldpd1_%=:\n"
      "buffer_load_dwordx4 v[224:227], v35, s[16:19], s46 offen sc1\n"
      "buffer_load_dwordx4 v[228:231], v35, s[16:19], s46 offen offset:2048 sc1\n"
      "s_waitcnt vmcnt(8)\n"
      "s_nop 0\n"
      "v_perm_b32 v96, v164, v160, s62\n"
      "v_perm_b32 v100, v164, v160, s63\n"
      "v_perm_b32 v104, v165, v161, s62\n"
      "v_perm_b32 v108, v165, v161, s63\n"
      "v_perm_b32 v112, v166, v162, s62\n"
      "v_perm_b32 v116, v166, v162, s63\n"
      "v_perm_b32 v120, v167, v163, s62\n"
      "v_perm_b32 v124, v167, v163, s63\n"
      "s_waitcnt vmcnt(7)\n"
      "s_nop 0\n"
      "s_waitcnt vmcnt(6)\n"
      "s_nop 0\n"
      "v_perm_b32 v97, v172, v168, s62\n"
      "v_perm_b32 v101, v172, v168, s63\n"
      "v_perm_b32 v105, v173, v169, s62\n"
      "v_perm_b32 v109, v173, v169, s63\n"
      "v_perm_b32 v113, v174, v170, s62\n"
      "v_perm_b32 v117, v174, v170, s63\n"
      "v_perm_b32 v121, v175, v171, s62\n"
      "v_perm_b32 v125, v175, v171, s63\n"
      "s_waitcnt vmcnt(5)\n"
      "s_nop 0\n"
      "s_waitcnt vmcnt(4)\n"
      "s_nop 0\n"
      "v_perm_b32 v98, v180, v176, s62\n"
      "v_perm_b32 v102, v180, v176, s63\n"
      "v_perm_b32 v106, v181, v177, s62\n"
      "v_perm_b32 v110, v181, v177, s63\n"
      "v_perm_b32 v114, v182, v178, s62\n"
      "v_perm_b32 v118, v182, v178, s63\n"
      "v_perm_b32 v122, v183, v179, s62\n"
      "v_perm_b32 v126, v183, v179, s63\n"
      "s_waitcnt vmcnt(3)\n"
      "s_nop 0\n"
      "s_waitcnt vmcnt(2)\n"
      "s_nop 0\n"
      "v_perm_b32 v99, v188, v184, s62\n"
      "v_perm_b32 v103, v188, v184, s63\n"
      "v_perm_b32 v107, v189, v185, s62\n"
      "v_perm_b32 v111, v189, v185, s63\n"
      "v_perm_b32 v115, v190, v186, s62\n"
      "v_perm_b32 v119, v190, v186, s63\n"
      "v_perm_b32 v123, v191, v187, s62\n"
      "v_perm_b32 v127, v191, v187, s63\n"
      "v_mov_b32 v137, s15\n"
      "v_mov_b32 v139, s15\n"
      "v_mov_b32 v141, s15\n"
      "v_mov_b32 v143, s15\n"
      "s_movk_i32 s59, 0x7fff\n"
      "s_waitcnt vmcnt(0)\n"
      "s_nop 0\n"
      ".Ldg%=:\n"
      "v_xor_b32 v232, s15, v225\n"
      "v_bitop3_b32 v232, s15, v227, v232 bitop3:0xbe\n"
      "v_bitop3_b32 v232, s15, v229, v232 bitop3:0xbe\n"
      "v_bitop3_b32 v232, s15, v231, v232 bitop3:0xbe\n"
      "v_cmp_ne_u32 vcc, 0, v232\n"
      "v_lshlrev_b32 v232, 16, v224\n"
      "v_and_b32 v233, 0xffff0000, v224\n"
      "v_lshlrev_b32 v234, 16, v226\n"
      "v_and_b32 v235, 0xffff0000, v226\n"
      "s_cbranch_vccnz .Ldgs%=\n"
      ".Ldgo%=:\n"
      "v_lshlrev_b32 v236, 16, v228\n"
      "v_and_b32 v237, 0xffff0000, v228\n"
      "v_lshlrev_b32 v238, 16, v230\n"
      "v_and_b32 v239, 0xffff0000, v230\n"
      "v_pk_mul_f32 v[232:233], v[232:233], v[236:237]\n"
      "v_pk_mul_f32 v[234:235], v[234:235], v[238:239]\n"
      "v_cvt_pk_bf16_f32 v232, v232, v233\n"
      "v_cvt_pk_bf16_f32 v233, v234, v235\n"
      "ds_write_b64 v36, v[232:233] \n"
      "ds_write_b32 v41, v137 offset:96\n"
      "ds_read_b128 v[240:243], v42 offset:32\n"
      "ds_read_b128 v[224:227], v37 \n"
      "ds_read_b128 v[228:231], v37 offset:1152\n"
      "s_movk_i32 s59, 0x7fff\n"
      ".Ldv%=:\n"
      "s_waitcnt lgkmcnt(2)\n"
      "v_xor_b32 v240, s15, v240\n"
      "v_bitop3_b32 v240, s15, v241, v240 bitop3:0xbe\n"
      "v_bitop3_b32 v240, s15, v242, v240 bitop3:0xbe\n"
      "v_bitop3_b32 v240, s15, v243, v240 bitop3:0xbe\n"
      "v_cmp_ne_u32 vcc, 0, v240\n"
      "s_nop 1\n"
      "s_cbranch_vccnz .Ldvs%=\n"
      ".Ldvo%=:\n"
      "s_waitcnt lgkmcnt(1)\n"
      "v_mfma_f32_16x16x32_bf16 v[192:195], v[64:67], v[224:227], 0\n"
      "s_waitcnt lgkmcnt(0)\n"
      "v_mfma_f32_16x16x32_bf16 v[192:195], v[96:99], v[228:231], v[192:195]\n"
      "v_mfma_f32_16x16x32_bf16 v[196:199], v[68:71], v[224:227], 0\n"
      "v_mfma_f32_16x16x32_bf16 v[196:199], v[100:103], v[228:231], v[196:199]\n"
      "v_mfma_f32_16x16x32_bf16 v[200:203], v[72:75], v[224:227], 0\n"
      "s_nop 4\n"
      "v_permlane32_swap_b32 v192, v193\n"
      "v_permlane32_swap_b32 v194, v195\n"
      "v_mfma_f32_16x16x32_bf16 v[200:203], v[104:107], v[228:231], v[200:203]\n"
      "v_mfma_f32_16x16x32_bf16 v[204:207], v[76:79], v[224:227], 0\n"
      "v_permlane32_swap_b32 v196, v197\n"
      "v_permlane32_swap_b32 v198, v199\n"
      "v_mfma_f32_16x16x32_bf16 v[204:207], v[108:111], v[228:231], v[204:207]\n"
      "v_add_f32_dpp v128, v193, v192 row_ror:8 row_mask:0xf bank_mask:0x3\n"
      "s_nop 1\n"
      "v_add_f32_dpp v128, v194, v195 row_ror:8 row_mask:0xf bank_mask:0xc\n"
      "v_mfma_f32_16x16x32_bf16 v[208:211], v[80:83], v[224:227], 0\n"
      "v_permlane32_swap_b32 v200, v201\n"
      "v_permlane32_swap_b32 v202, v203\n"
      "v_mfma_f32_16x16x32_bf16 v[208:211], v[112:115], v[228:231], v[208:211]\n"
      "v_add_f32_dpp v129, v197, v196 row_ror:8 row_mask:0xf bank_mask:0x3\n"
      "s_nop 1\n"
      "v_add_f32_dpp v129, v198, v199 row_ror:8 row_mask:0xf bank_mask:0xc\n"
      "v_mfma_f32_16x16x32_bf16 v[212:215], v[84:87], v[224:227], 0\n"
      "v_permlane32_swap_b32 v204, v205\n"
      "v_permlane32_swap_b32 v206, v207\n"
      "v_mfma_f32_16x16x32_bf16 v[212:215], v[116:119], v[228:231], v[212:215]\n"
      "v_add_f32_dpp v130, v201, v200 row_ror:8 row_mask:0xf bank_mask:0x3\n"
      "s_nop 1\n"
      "v_add_f32_dpp v130, v202, v203 row_ror:8 row_mask:0xf bank_mask:0xc\n"
      "v_mfma_f32_16x16x32_bf16 v[216:219], v[88:91], v[224:227], 0\n"
      "v_permlane32_swap_b32 v208, v209\n"
      "v_permlane32_swap_b32 v210, v211\n"
      "v_mfma_f32_16x16x32_bf16 v[216:219], v[120:123], v[228:231], v[216:219]\n"
      "v_add_f32_dpp v131, v205, v204 row_ror:8 row_mask:0xf bank_mask:0x3\n"
      "s_nop 1\n"
      "v_add_f32_dpp v131, v206, v207 row_ror:8 row_mask:0xf bank_mask:0xc\n"
      "v_mfma_f32_16x16x32_bf16 v[220:223], v[92:95], v[224:227], 0\n"
      "v_permlane32_swap_b32 v212, v213\n"
      "v_permlane32_swap_b32 v214, v215\n"
      "v_mfma_f32_16x16x32_bf16 v[220:223], v[124:127], v[228:231], v[220:223]\n"
      "v_add_f32_dpp v132, v209, v208 row_ror:8 row_mask:0xf bank_mask:0x3\n"
      "s_nop 1\n"
      "v_add_f32_dpp v132, v210, v211 row_ror:8 row_mask:0xf bank_mask:0xc\n"
      "v_cvt_pk_bf16_f32 v136, v128, v129\n"
      "v_cvt_pk_bf16_f32 v138, v130, v131\n"
      "buffer_store_dwordx4 v[136:139], v38, s[16:19], s47 offen offset:0 sc1\n"
      "v_permlane32_swap_b32 v216, v217\n"
      "v_permlane32_swap_b32 v218, v219\n"
      "v_add_f32_dpp v133, v213, v212 row_ror:8 row_mask:0xf bank_mask:0x3\n"
      "s_nop 1\n"
      "v_add_f32_dpp v133, v214, v215 row_ror:8 row_mask:0xf bank_mask:0xc\n"
      "v_permlane32_swap_b32 v220, v221\n"
      "v_permlane32_swap_b32 v222, v223\n"
      "v_add_f32_dpp v134, v217, v216 row_ror:8 row_mask:0xf bank_mask:0x3\n"
      "s_nop 1\n"
      "v_add_f32_dpp v134, v218, v219 row_ror:8 row_mask:0xf bank_mask:0xc\n"
      "v_add_f32_dpp v135, v221, v220 row_ror:8 row_mask:0xf bank_mask:0x3\n"
      "s_nop 1\n"
      "v_add_f32_dpp v135, v222, v223 row_ror:8 row_mask:0xf bank_mask:0xc\n"
      "v_cvt_pk_bf16_f32 v140, v132, v133\n"
      "v_cvt_pk_bf16_f32 v142, v134, v135\n"
      "buffer_store_dwordx4 v[140:143], v38, s[16:19], s47 offen offset:128 sc1\n"
      "s_endpgm\n"
      ".LZ%=:\n"
      "s_setprio 0\n"
      "v_and_b32 v48, 63, v0\n"
      "v_and_b32 v49, 7, v0\n"
      "v_lshlrev_b32 v108, 3, v49\n"
      "s_lshl4_add_u32 s35, s48, s49\n"
      "s_lshl_b32 s41, s35, 4\n"
      "s_and_b32 s43, s41, 0xffffff80\n"
      "s_add_u32 s43, s43, 67584\n"
      "buffer_load_dwordx2 v[104:105], v108, s[16:19], s43 offen sc1\n"
      "s_mul_i32 s44, s35, 7168\n"
      "s_add_u32 s44, s44, 4194304\n"
      "s_add_u32 s67, s44, 0x1000\n"
      "v_lshrrev_b32 v109, 5, v48\n"
      "v_and_b32 v110, 8, v48\n"
      "v_lshl_add_u32 v109, v109, 2, v110\n"
      "v_and_b32 v113, 16, v48\n"
      "s_movk_i32 s59, 0x7fff\n"
      "s_waitcnt vmcnt(0)\n"
      "s_movk_i32 s61, 0x7ff\n"
      ".Ldzh%=:\n"
      "v_cmp_ne_u32 vcc, s15, v105\n"
      "v_bitop3_b32 v110, v104, s61, v104 bitop3:0x0c\n"
      "v_not_b32 v112, v104\n"
      "s_cbranch_vccnz .Ldzs%=\n"
      ".Ldzo%=:\n"
      "v_lshl_add_u32 v110, v110, 11, v109\n"
      "v_lshlrev_b32 v108, 4, v48\n"
      "buffer_load_dword v111, v110, s[20:23], s41 offen\n"
      "v_lshl_or_b32 v113, v113, 26, v110\n"
      "v_ashrrev_i32 v112, 31, v112\n"
      "v_or_b32 v112, 0x80000000, v112\n"
      "v_xor_b32 v112, v112, v104\n"
      "v_and_b32 v112, 0xffff0000, v112\n"
      "v_mul_f32 v112, 0xbfb8aa3b, v112\n"
      "v_exp_f32 v112, v112\n"
      "s_nop 0\n"
      "v_add_f32 v112, 1.0, v112\n"
      "v_rcp_f32 v112, v112\n"
      "s_nop 0\n"
      "v_cvt_pk_bf16_f32 v112, v112, v112\n"
      "v_and_b32 v112, 0xffff0000, v112\n"
      ".Ldpc2_%=:\n"
      "s_memrealtime s[64:65]\n"
      "s_waitcnt lgkmcnt(0)\n"
      "s_sub_u32 s64, s64, s12\n"
      "s_cmp_ge_u32 s64, 820\n"
      "s_cbranch_scc1 .Ldpd2_%=\n"
      "s_sleep 1\n"
      "s_branch .Ldpc2_%=\n"
      ".Ldpd2_%=:\n"
      "buffer_load_dwordx4 v[64:67], v108, s[16:19], s44 offen offset:0 sc1\n"
      "buffer_load_dwordx4 v[68:71], v108, s[16:19], s44 offen offset:1024 sc1\n"
      "buffer_load_dwordx4 v[72:75], v108, s[16:19], s44 offen offset:2048 sc1\n"
      "buffer_load_dwordx4 v[76:79], v108, s[16:19], s44 offen offset:3072 sc1\n"
      "buffer_load_dwordx4 v[80:83], v108, s[16:19], s67 offen offset:0 sc1\n"
      "buffer_load_dwordx4 v[84:87], v108, s[16:19], s67 offen offset:1024 sc1\n"
      "buffer_load_dwordx4 v[88:91], v108, s[16:19], s67 offen offset:2048 sc1\n"
      "s_movk_i32 s59, 0x7fff\n"
      "s_mov_b32 s56, 0x3f80\n"
      "s_mov_b32 s57, 0x3f800000\n"
      "s_waitcnt vmcnt(7)\n"
      "v_lshlrev_b32 v106, 16, v111\n"
      "v_and_b32 v107, 0xffff0000, v111\n"
      ".Ldq%=:\n"
      "s_waitcnt vmcnt(6)\n"
      "v_xor_b32 v100, s15, v65\n"
      "v_xor_b32 v101, s15, v67\n"
      "v_lshlrev_b32 v96, 16, v64\n"
      "v_and_b32 v97, 0xffff0000, v64\n"
      "v_lshlrev_b32 v98, 16, v66\n"
      "v_and_b32 v99, 0xffff0000, v66\n"
      "s_waitcnt vmcnt(5)\n"
      "v_dot2_f32_bf16 v96, v68, s56, v96\n"
      "v_dot2_f32_bf16 v97, v68, s57, v97\n"
      "v_dot2_f32_bf16 v98, v70, s56, v98\n"
      "v_dot2_f32_bf16 v99, v70, s57, v99\n"
      "v_bitop3_b32 v100, s15, v69, v100 bitop3:0xbe\n"
      "v_bitop3_b32 v101, s15, v71, v101 bitop3:0xbe\n"
      "s_waitcnt vmcnt(4)\n"
      "v_dot2_f32_bf16 v96, v72, s56, v96\n"
      "v_dot2_f32_bf16 v97, v72, s57, v97\n"
      "v_dot2_f32_bf16 v98, v74, s56, v98\n"
      "v_dot2_f32_bf16 v99, v74, s57, v99\n"
      "v_bitop3_b32 v100, s15, v73, v100 bitop3:0xbe\n"
      "v_bitop3_b32 v101, s15, v75, v101 bitop3:0xbe\n"
      "s_waitcnt vmcnt(3)\n"
      "v_dot2_f32_bf16 v96, v76, s56, v96\n"
      "v_dot2_f32_bf16 v97, v76, s57, v97\n"
      "v_dot2_f32_bf16 v98, v78, s56, v98\n"
      "v_dot2_f32_bf16 v99, v78, s57, v99\n"
      "v_bitop3_b32 v100, s15, v77, v100 bitop3:0xbe\n"
      "v_bitop3_b32 v101, s15, v79, v101 bitop3:0xbe\n"
      "s_waitcnt vmcnt(2)\n"
      "v_dot2_f32_bf16 v96, v80, s56, v96\n"
      "v_dot2_f32_bf16 v97, v80, s57, v97\n"
      "v_dot2_f32_bf16 v98, v82, s56, v98\n"
      "v_dot2_f32_bf16 v99, v82, s57, v99\n"
      "v_bitop3_b32 v100, s15, v81, v100 bitop3:0xbe\n"
      "v_bitop3_b32 v101, s15, v83, v101 bitop3:0xbe\n"
      "s_waitcnt vmcnt(1)\n"
      "v_dot2_f32_bf16 v96, v84, s56, v96\n"
      "v_dot2_f32_bf16 v97, v84, s57, v97\n"
      "v_dot2_f32_bf16 v98, v86, s56, v98\n"
      "v_dot2_f32_bf16 v99, v86, s57, v99\n"
      "v_bitop3_b32 v100, s15, v85, v100 bitop3:0xbe\n"
      "v_bitop3_b32 v101, s15, v87, v101 bitop3:0xbe\n"
      "s_waitcnt vmcnt(0)\n"
      "v_dot2_f32_bf16 v96, v88, s56, v96\n"
      "v_dot2_f32_bf16 v97, v88, s57, v97\n"
      "v_dot2_f32_bf16 v98, v90, s56, v98\n"
      "v_dot2_f32_bf16 v99, v90, s57, v99\n"
      "v_bitop3_b32 v100, s15, v89, v100 bitop3:0xbe\n"
      "v_bitop3_b32 v101, s15, v91, v101 bitop3:0xbe\n"
      "v_or_b32 v100, v100, v101\n"
      "v_cmp_ne_u32 vcc, 0, v100\n"
      "v_permlane32_swap_b32 v96, v98\n"
      "v_permlane32_swap_b32 v97, v99\n"
      "s_cbranch_vccnz .Ldqs%=\n"
      ".Ldqo%=:\n"
      "v_add_f32 v96, v96, v98\n"
      "v_add_f32 v97, v97, v99\n"
      "v_mov_b32 v98, v96\n"
      "v_mov_b32 v99, v97\n"
      "s_nop 0\n"
      "v_permlane16_swap_b32 v96, v98\n"
      "v_permlane16_swap_b32 v97, v99\n"
      "v_add_f32 v96, v96, v98\n"
      "v_add_f32 v97, v97, v99\n"
      "v_cvt_pk_bf16_f32 v96, v96, v97\n"
      "v_and_b32 v97, 0xffff0000, v96\n"
      "v_lshlrev_b32 v96, 16, v96\n"
      "v_mul_f32 v97, v97, v112\n"
      "v_mul_f32 v96, v96, v112\n"
      "v_cvt_pk_bf16_f32 v96, v96, v97\n"
      "v_and_b32 v97, 0xffff0000, v96\n"
      "v_lshlrev_b32 v96, 16, v96\n"
      "v_add_f32 v97, v97, v107\n"
      "v_add_f32 v96, v96, v106\n"
      "v_cvt_pk_bf16_f32 v96, v96, v97\n"
      "buffer_atomic_pk_add_bf16 v96, v113, s[24:27], s41 offen\n"
      "s_endpgm\n"
      ".Ldgs%=:\n"
      "s_sleep 1\n"
      "s_sub_u32 s59, s59, 1\n"
      "s_cbranch_scc1 .Ldgo%=\n"
      "buffer_load_dwordx4 v[224:227], v35, s[16:19], s46 offen sc1\n"
      "buffer_load_dwordx4 v[228:231], v35, s[16:19], s46 offen offset:2048 sc1\n"
      "s_waitcnt vmcnt(0)\n"
      "s_branch .Ldg%=\n"
      ".Ldvs%=:\n"
      "s_sub_u32 s59, s59, 1\n"
      "s_cbranch_scc1 .Ldvo%=\n"
      "s_waitcnt lgkmcnt(0)\n"
      "ds_read_b128 v[240:243], v42 offset:32\n"
      "ds_read_b128 v[224:227], v37 \n"
      "ds_read_b128 v[228:231], v37 offset:1152\n"
      "s_waitcnt lgkmcnt(0)\n"
      "s_branch .Ldv%=\n"
      ".Ldzs%=:\n"
      "s_sleep 2\n"
      "s_sub_u32 s59, s59, 1\n"
      "s_cbranch_scc1 .Ldzo%=\n"
      "buffer_load_dwordx2 v[104:105], v108, s[16:19], s43 offen sc1\n"
      "s_waitcnt vmcnt(0)\n"
      "s_branch .Ldzh%=\n"
      ".Ldqs%=:\n"
      "s_sleep 1\n"
      "s_sub_u32 s59, s59, 1\n"
      "s_cbranch_scc1 .Ldqo%=\n"
      "v_cmp_ne_u32 vcc, s15, v65\n"
      "v_cmp_ne_u32 s[64:65], s15, v67\n"
      "s_nop 0\n"
      "s_or_b64 exec, vcc, s[64:65]\n"
      "s_cbranch_scc0 .Ldqk0%=\n"
      "buffer_load_dwordx4 v[64:67], v108, s[16:19], s44 offen offset:0 sc1\n"
      ".Ldqk0%=:\n"
      "s_mov_b64 exec, -1\n"
      "v_cmp_ne_u32 vcc, s15, v69\n"
      "v_cmp_ne_u32 s[64:65], s15, v71\n"
      "s_nop 0\n"
      "s_or_b64 exec, vcc, s[64:65]\n"
      "s_cbranch_scc0 .Ldqk1%=\n"
      "buffer_load_dwordx4 v[68:71], v108, s[16:19], s44 offen offset:1024 sc1\n"
      ".Ldqk1%=:\n"
      "s_mov_b64 exec, -1\n"
      "v_cmp_ne_u32 vcc, s15, v73\n"
      "v_cmp_ne_u32 s[64:65], s15, v75\n"
      "s_nop 0\n"
      "s_or_b64 exec, vcc, s[64:65]\n"
      "s_cbranch_scc0 .Ldqk2%=\n"
      "buffer_load_dwordx4 v[72:75], v108, s[16:19], s44 offen offset:2048 sc1\n"
      ".Ldqk2%=:\n"
      "s_mov_b64 exec, -1\n"
      "v_cmp_ne_u32 vcc, s15, v77\n"
      "v_cmp_ne_u32 s[64:65], s15, v79\n"
      "s_nop 0\n"
      "s_or_b64 exec, vcc, s[64:65]\n"
      "s_cbranch_scc0 .Ldqk3%=\n"
      "buffer_load_dwordx4 v[76:79], v108, s[16:19], s44 offen offset:3072 sc1\n"
      ".Ldqk3%=:\n"
      "s_mov_b64 exec, -1\n"
      "v_cmp_ne_u32 vcc, s15, v81\n"
      "v_cmp_ne_u32 s[64:65], s15, v83\n"
      "s_nop 0\n"
      "s_or_b64 exec, vcc, s[64:65]\n"
      "s_cbranch_scc0 .Ldqk4%=\n"
      "buffer_load_dwordx4 v[80:83], v108, s[16:19], s67 offen offset:0 sc1\n"
      ".Ldqk4%=:\n"
      "s_mov_b64 exec, -1\n"
      "v_cmp_ne_u32 vcc, s15, v85\n"
      "v_cmp_ne_u32 s[64:65], s15, v87\n"
      "s_nop 0\n"
      "s_or_b64 exec, vcc, s[64:65]\n"
      "s_cbranch_scc0 .Ldqk5%=\n"
      "buffer_load_dwordx4 v[84:87], v108, s[16:19], s67 offen offset:1024 sc1\n"
      ".Ldqk5%=:\n"
      "s_mov_b64 exec, -1\n"
      "v_cmp_ne_u32 vcc, s15, v89\n"
      "v_cmp_ne_u32 s[64:65], s15, v91\n"
      "s_nop 0\n"
      "s_or_b64 exec, vcc, s[64:65]\n"
      "s_cbranch_scc0 .Ldqk6%=\n"
      "buffer_load_dwordx4 v[88:91], v108, s[16:19], s67 offen offset:2048 sc1\n"
      ".Ldqk6%=:\n"
      "s_mov_b64 exec, -1\n"
      "s_waitcnt vmcnt(0)\n"
      "s_branch .Ldq%=\n"
      ".LT%=:\n"
      "v_mov_b32 v48, 0x22680\n"
      "ds_read_b32 v48, v48 \n"
      "s_waitcnt lgkmcnt(0)\n"
      "v_readfirstlane_b32 s12, v48\n"
      "v_mov_b32 v48, v43\n"
      "v_and_b32 v49, 7, v43\n"
      "v_lshrrev_b32 v50, 3, v43\n"
      "s_lshl_b32 s42, s34, 3\n"
      "s_add_u32 s42, s42, 2048\n"
      "s_lshl_b32 s43, s48, 8\n"
      "s_and_b32 s50, s49, 1\n"
      "s_lshl_b32 s50, s50, 7\n"
      "s_add_u32 s43, s43, s50\n"
      "s_add_u32 s43, s43, 65536\n"
      "s_mov_b32 s20, s64\n"
      "s_and_b32 s21, s65, 0xffff\n"
      "s_mov_b32 s22, 0x400000\n"
      "s_mov_b32 s23, 0x20000\n"
      "s_mov_b32 s28, s84\n"
      "s_and_b32 s29, s85, 0xffff\n"
      "s_mov_b32 s30, 0x700000\n"
      "s_mov_b32 s31, 0x20000\n"
      "s_sleep 0\n"
      ".LS%=:\n"
      "v_lshlrev_b32 v112, 4, v0\n"
      "v_lshlrev_b32 v113, 2, v0\n"
      "v_sub_u32 v113, 0xfff, v113\n"
      "v_lshlrev_b32 v115, 2, v48\n"
      "v_mov_b32 v116, 0\n"
      "ds_write_b32 v115, v116 offset:27136\n"
      "ds_write_b32 v116, v116 offset:26400\n"
      "v_add_u32 v115, 0x140, v115\n"
      "v_mov_b32 v114, v115\n"
      "s_lshl_b32 s50, s33, 2\n"
      "s_add_u32 s50, s50, 256\n"
      "v_writelane_b32 v114, s50, 63\n"
      "s_lshl_b32 s50, s33, 5\n"
      "v_lshl_add_u32 v117, v49, 2, s50\n"
      "v_lshlrev_b32 v118, 5, v50\n"
      "s_and_b32 s50, s49, 1\n"
      "s_lshl_b32 s50, s50, 10\n"
      "v_lshl_add_u32 v119, v50, 7, s50\n"
      "s_lshr_b32 s51, s33, 2\n"
      "s_mul_i32 s51, s51, 61440\n"
      "s_add_u32 s51, s51, 393216\n"
      "s_mov_b32 s52, 0x80008000\n"
      "s_mov_b32 s53, 0xffff0000\n"
      "s_movk_i32 s59, 0x7fff\n"
      ".Lt_pc4_%=:\n"
      "s_memrealtime s[64:65]\n"
      "s_waitcnt lgkmcnt(0)\n"
      "s_sub_u32 s64, s64, s12\n"
      "s_cmp_ge_u32 s64, 300\n"
      "s_cbranch_scc1 .Lt_pd4_%=\n"
      "s_sleep 1\n"
      "s_branch .Lt_pc4_%=\n"
      ".Lt_pd4_%=:\n"
      ".Lt_p%=:\n"
      "buffer_load_dwordx4 v[72:75], v112, s[16:19], s51 offen sc1\n"
      "s_waitcnt vmcnt(0)\n"
      "v_cmp_ne_u32 vcc, s15, v73\n"
      "v_cmp_ne_u32 s[54:55], s15, v75\n"
      "v_pk_ashrrev_i16 v81, 15, v72 op_sel_hi:[0,1]\n"
      "v_pk_ashrrev_i16 v82, 15, v74 op_sel_hi:[0,1]\n"
      "s_or_b64 vcc, vcc, s[54:55]\n"
      "s_cbranch_scc1 .Lt_ps%=\n"
      ".Lt_po%=:\n"
      "v_bitop3_b32 v81, v72, v81, s52 bitop3:0x1e\n"
      "v_bitop3_b32 v82, v74, v82, s52 bitop3:0x1e\n"
      "v_lshl_or_b32 v76, v81, 16, v113\n"
      "v_and_or_b32 v77, v81, s53, v113\n"
      "v_lshl_or_b32 v78, v82, 16, v113\n"
      "v_and_or_b32 v79, v82, s53, v113\n"
      "v_add_u32 v77, -1, v77\n"
      "v_add_u32 v78, -2, v78\n"
      "v_add_u32 v79, -3, v79\n"
      "v_max3_u32 v80, v76, v77, v78\n"
      "v_max_u32 v80, v80, v79\n"
      "v_mov_b32 v104, v80\n"
      "v_med3_u32 v105, v76, v77, v78\n"
      "v_max_u32_dpp v80, v80, v80 row_shr:1 row_mask:0xf bank_mask:0xf bound_ctrl:1\n"
      "v_max3_u32 v83, v76, v77, v78\n"
      "v_min3_u32 v106, v76, v77, v78\n"
      "v_max_u32_dpp v80, v80, v80 row_shr:2 row_mask:0xf bank_mask:0xf bound_ctrl:1\n"
      "v_med3_u32 v106, v105, v106, v79\n"
      "v_med3_u32 v105, v83, v105, v79\n"
      "v_max_u32_dpp v80, v80, v80 row_shr:4 row_mask:0xf bank_mask:0xf bound_ctrl:1\n"
      "v_mov_b32 v107, 0x100\n"
      "s_nop 0\n"
      "v_max_u32_dpp v80, v80, v80 row_shr:8 row_mask:0xf bank_mask:0xf bound_ctrl:1\n"
      "s_nop 1\n"
      "v_max_u32_dpp v80, v80, v80 row_bcast:15 row_mask:0xa bank_mask:0xf\n"
      "s_nop 1\n"
      "v_max_u32_dpp v80, v80, v80 row_bcast:31 row_mask:0xc bank_mask:0xf\n"
      "ds_write_b32 v114, v80 offset:26112\n"
      "s_waitcnt lgkmcnt(0)\n"
      "s_barrier\n"
      "ds_read_b128 v[92:95], v116 offset:26368\n"
      "ds_read_b128 v[96:99], v116 offset:26384\n"
      "s_waitcnt lgkmcnt(1)\n"
      "v_min3_u32 v81, v92, v93, v94\n"
      "s_waitcnt lgkmcnt(0)\n"
      "v_min3_u32 v82, v96, v97, v98\n"
      "v_min3_u32 v81, v81, v95, v99\n"
      "v_min_u32 v81, v81, v82\n"
      "v_cmp_ge_u32 s[62:63], v105, v81\n"
      "v_cmp_ge_u32 vcc, v106, v81\n"
      "v_cmp_ge_u32 s[60:61], v104, v81\n"
      "v_cndmask_b32 v82, 1, 2, s[62:63]\n"
      "v_cndmask_b32 v82, v82, v107, vcc\n"
      "s_mov_b64 exec, s[60:61]\n"
      "ds_add_rtn_u32 v83, v116, v82 offset:26400\n"
      "s_waitcnt lgkmcnt(0)\n"
      "v_min_u32 v83, 0x50, v83\n"
      "v_lshlrev_b32 v83, 2, v83\n"
      "ds_write_b32 v83, v104 offset:27136\n"
      "s_mov_b64 exec, s[62:63]\n"
      "ds_write_b32 v83, v105 offset:27140\n"
      "s_mov_b64 exec, -1\n"
      "s_waitcnt lgkmcnt(0)\n"
      "s_barrier\n"
      "s_memrealtime s[56:57]\n"
      "ds_read_b32 v81, v116 offset:26400\n"
      ".Lt_r%=:\n"
      "ds_read_b32 v100, v117 offset:27136\n"
      "ds_read_b128 v[92:95], v118 offset:27136\n"
      "ds_read_b128 v[96:99], v118 offset:27152\n"
      "v_mov_b32 v101, s15\n"
      "v_mov_b32 v89, 0\n"
      "s_waitcnt lgkmcnt(3)\n"
      "v_cmp_lt_u32 vcc, 64, v81\n"
      "s_movk_i32 s50, 0x7ff\n"
      "s_cbranch_vccnz .Lt_m%=\n"
      "s_waitcnt lgkmcnt(1)\n"
      "v_cmp_gt_u32 s[60:61], v92, v100\n"
      "v_cmp_gt_u32 s[62:63], v93, v100\n"
      "v_cmp_gt_u32 s[64:65], v94, v100\n"
      "v_cmp_gt_u32 s[66:67], v95, v100\n"
      "s_waitcnt lgkmcnt(0)\n"
      "v_cmp_gt_u32 s[68:69], v96, v100\n"
      "v_cmp_gt_u32 s[70:71], v97, v100\n"
      "v_cmp_gt_u32 s[72:73], v98, v100\n"
      "v_cmp_gt_u32 s[74:75], v99, v100\n"
      "v_bitop3_b32 v81, v100, s50, v100 bitop3:0x0c\n"
      "v_addc_co_u32 v89, vcc, 0, v89, s[60:61]\n"
      "v_addc_co_u32 v89, vcc, 0, v89, s[62:63]\n"
      "v_addc_co_u32 v89, vcc, 0, v89, s[64:65]\n"
      "v_addc_co_u32 v89, vcc, 0, v89, s[66:67]\n"
      "v_addc_co_u32 v89, vcc, 0, v89, s[68:69]\n"
      "v_addc_co_u32 v89, vcc, 0, v89, s[70:71]\n"
      "v_addc_co_u32 v89, vcc, 0, v89, s[72:73]\n"
      "v_addc_co_u32 v89, vcc, 0, v89, s[74:75]\n"
      "v_lshl_add_u32 v82, v81, 11, v119\n"
      "v_lshrrev_b32 v86, 9, v81\n"
      "v_mul_u32_u24 v86, 0xf000, v86\n"
      "v_lshl_add_u32 v81, v81, 3, v86\n"
      "v_add_u32_dpp v89, v89, v89 row_ror:8 row_mask:0xf bank_mask:0xf\n"
      "v_mov_b32 v83, v89\n"
      "s_nop 1\n"
      "v_permlane16_swap_b32 v89, v83\n"
      "v_add_u32 v89, v89, v83\n"
      "v_mov_b32 v83, v89\n"
      "s_nop 1\n"
      "v_permlane32_swap_b32 v89, v83\n"
      "v_add_u32 v89, v89, v83\n"
      "v_cmp_gt_u32 vcc, 8, v89\n"
      "v_lshlrev_b32 v89, 3, v89\n"
      "s_mov_b32 s51, 131072\n"
      "s_cbranch_vccz .Lt_n%=\n"
      "s_and_b64 exec, vcc, 0xff\n"
      "ds_write_b32 v89, v100 offset:26752\n"
      "buffer_load_dword v84, v81, s[16:19], s51 offen sc1\n"
      "s_mov_b64 exec, vcc\n"
      "buffer_load_dword v85, v82, s[20:23], 0 offen\n"
      "s_mov_b64 exec, -1\n"
      ".Lt_n%=:\n"
      "v_lshlrev_b32 v89, 3, v49\n"
      "s_waitcnt lgkmcnt(0)\n"
      "s_barrier\n"
      "s_sub_u32 s12, s56, 390\n"
      "s_movk_i32 s57, 64\n"
      "s_cmp_lg_u32 s33, 0\n"
      "s_cbranch_scc1 .Lt_a%=\n"
      "ds_read_b32 v100, v89 offset:26752\n"
      "v_writelane_b32 v89, s57, 8\n"
      "s_waitcnt lgkmcnt(0)\n"
      "v_writelane_b32 v100, s12, 8\n"
      "s_mov_b64 exec, 0x1ff\n"
      "buffer_store_dwordx2 v[100:101], v89, s[16:19], s43 offen sc1\n"
      "s_mov_b64 exec, -1\n"
      "s_nop 1\n"
      "v_mov_b32 v96, s15\n"
      "v_mov_b32 v97, s15\n"
      "buffer_store_dwordx2 v[96:97], off, s[16:19], s42 sc1\n"
      ".Lt_a%=:\n"
      "s_cmp_lg_u32 s33, 1\n"
      "s_cbranch_scc1 .Lt_c%=\n"
      "ds_read_b32 v100, v89 offset:26752\n"
      "v_lshlrev_b32 v81, 3, v50\n"
      "ds_read_b32 v81, v81 offset:26752\n"
      "v_mov_b32 v92, 0\n"
      "v_mov_b32 v93, 0\n"
      "v_mov_b32 v94, 0\n"
      "v_mov_b32 v95, 0\n"
      "s_and_b32 s51, s43, 0x7ff\n"
      "v_lshlrev_b32 v82, 4, v49\n"
      "s_waitcnt lgkmcnt(0)\n"
      "v_bitop3_b32 v81, v81, s50, v81 bitop3:0x0c\n"
      "v_lshl_add_u32 v81, v81, 11, v82\n"
      "buffer_store_dwordx4 v[92:95], v81, s[24:27], s51 offen sc1\n"
      "s_waitcnt vmcnt(0)\n"
      "s_mov_b64 exec, 0xff\n"
      "buffer_store_dwordx2 v[100:101], v89, s[16:19], s43 offen offset:2048 sc1\n"
      "s_mov_b64 exec, -1\n"
      "s_nop 1\n"
      ".Lt_c%=:\n"
      "s_cmp_lg_u32 s33, 3\n"
      "s_cbranch_scc1 .Lt_z%=\n"
      "s_sleep 55\n"
      "s_branch .LZ%=\n"
      ".Lt_z%=:\n"
      "s_cmp_lg_u32 s34, 127\n"
      "s_cbranch_scc1 .Lt_e%=\n"
      "s_cmp_lg_u32 s33, 0\n"
      "s_cbranch_scc1 .Lt_e%=\n"
      "s_movk_i32 s51, 2048\n"
      "s_movk_i32 s59, 0x7fff\n"
      ".Lt_k%=:\n"
      "s_sleep 16\n"
      "buffer_load_dwordx4 v[92:95], v112, s[16:19], s51 offen sc1\n"
      "buffer_load_dwordx4 v[96:99], v112, s[16:19], s51 offen offset:1024 sc1\n"
      "s_sub_u32 s59, s59, 1\n"
      "s_cbranch_scc1 .Lt_kb%=\n"
      "s_waitcnt vmcnt(0)\n"
      "v_xor_b32 v86, s15, v93\n"
      "v_bitop3_b32 v86, s15, v95, v86 bitop3:0xbe\n"
      "v_bitop3_b32 v86, s15, v97, v86 bitop3:0xbe\n"
      "v_bitop3_b32 v86, s15, v99, v86 bitop3:0xbe\n"
      "v_cmp_ne_u32 vcc, 0, v86\n"
      "s_nop 1\n"
      "s_cbranch_vccnz .Lt_k%=\n"
      ".Lt_kb%=:\n"
      "buffer_store_dword v101, off, s[16:19], 0 sc1\n"
      ".Lt_e%=:\n"
      "s_waitcnt vmcnt(0)\n"
      "s_endpgm\n"
      ".Lt_ps%=:\n"
      "s_sleep 1\n"
      "s_sub_u32 s59, s59, 1\n"
      "s_cbranch_scc0 .Lt_p%=\n"
      "s_branch .Lt_po%=\n"
      ".Lt_m%=:\n"
      "s_waitcnt lgkmcnt(0)\n"
      "s_barrier\n"
      "s_mov_b32 s59, 0\n"
      "v_mov_b32 v90, 0\n"
      ".Lt_ml%=:\n"
      "s_nop 1\n"
      "v_max3_u32 v80, v76, v77, v78\n"
      "v_max_u32 v80, v80, v79\n"
      "s_nop 1\n"
      "v_max_u32_dpp v80, v80, v80 row_shr:1 row_mask:0xf bank_mask:0xf bound_ctrl:1\n"
      "s_nop 1\n"
      "v_max_u32_dpp v80, v80, v80 row_shr:2 row_mask:0xf bank_mask:0xf bound_ctrl:1\n"
      "s_nop 1\n"
      "v_max_u32_dpp v80, v80, v80 row_shr:4 row_mask:0xf bank_mask:0xf bound_ctrl:1\n"
      "s_nop 1\n"
      "v_max_u32_dpp v80, v80, v80 row_shr:8 row_mask:0xf bank_mask:0xf bound_ctrl:1\n"
      "s_nop 1\n"
      "v_max_u32_dpp v80, v80, v80 row_bcast:15 row_mask:0xa bank_mask:0xf\n"
      "s_nop 1\n"
      "v_max_u32_dpp v80, v80, v80 row_bcast:31 row_mask:0xc bank_mask:0xf\n"
      "s_nop 0\n"
      "v_readlane_b32 s50, v80, 63\n"
      "s_mov_b32 m0, s59\n"
      "s_nop 0\n"
      "v_cmp_ne_u32 vcc, s50, v76\n"
      "s_nop 1\n"
      "v_cndmask_b32 v76, 0, v76, vcc\n"
      "v_cmp_ne_u32 vcc, s50, v77\n"
      "s_nop 1\n"
      "v_cndmask_b32 v77, 0, v77, vcc\n"
      "v_cmp_ne_u32 vcc, s50, v78\n"
      "s_nop 1\n"
      "v_cndmask_b32 v78, 0, v78, vcc\n"
      "v_cmp_ne_u32 vcc, s50, v79\n"
      "s_nop 1\n"
      "v_cndmask_b32 v79, 0, v79, vcc\n"
      "v_writelane_b32 v90, s50, m0\n"
      "s_add_u32 s59, s59, 1\n"
      "s_cmp_lt_u32 s59, 8\n"
      "s_cbranch_scc1 .Lt_ml%=\n"
      "s_mov_b64 exec, 0xff\n"
      "ds_write_b32 v117, v90 offset:27136\n"
      "s_mov_b64 exec, -1\n"
      "v_mov_b32 v81, 0\n"
      "s_waitcnt lgkmcnt(0)\n"
      "s_barrier\n"
      "s_branch .Lt_r%=\n"
      ".LEnd%=:\n"
      : "={s[80:95]}"(sts), "={s[96:99]}"(st2)
      : [ka] "s"(__builtin_amdgcn_kernarg_segment_ptr()), [bid] "s"(blockIdx.x), "{v0}"(threadIdx.x), "s"((u32)(uintptr_t)sm)
      : "v8", "v9", "v10", "v11", "v12", "v13", "v14", "v15", "v16", "v17", "v18", "v19", "v20", "v21", "v22", "v23", "v24", "v25", "v26", "v27",
        "v28", "v29", "v30", "v31", "v32", "v33", "v34", "v35", "v36", "v37", "v38", "v39", "v40", "v41", "v42", "v43", "v44", "v45", "v46", "v47",
        "v48", "v49", "v50", "v51", "v52", "v53", "v54", "v55", "v56", "v57", "v58", "v59", "v60", "v61", "v62", "v63", "v64", "v65", "v66", "v67",
        "v68", "v69", "v70", "v71", "v72", "v73", "v74", "v75", "v76", "v77", "v78", "v79", "v80", "v81", "v82", "v83", "v84", "v85", "v86", "v87",
        "v88", "v89", "v90", "v91", "v92", "v93", "v94", "v95", "v96", "v97", "v98", "v99", "v100", "v101", "v102", "v103", "v104", "v105", "v106", "v107",
        "v108", "v109", "v110", "v111", "v112", "v113", "v114", "v115", "v116", "v117", "v118", "v119", "v120", "v121", "v122", "v123", "v124", "v125", "v126", "v127",
        "v128", "v129", "v130", "v131", "v132", "v133", "v134", "v135", "v136", "v137", "v138", "v139", "v140", "v141", "v142", "v143", "v144", "v145", "v146", "v147",
        "v148", "v149", "v150", "v151", "v152", "v153", "v154", "v155", "v156", "v157", "v158", "v159", "v160", "v161", "v162", "v163", "v164", "v165", "v166", "v167",
        "v168", "v169", "v170", "v171", "v172", "v173", "v174", "v175", "v176", "v177", "v178", "v179", "v180", "v181", "v182", "v183", "v184", "v185", "v186", "v187",
        "v188", "v189", "v190", "v191", "v192", "v193", "v194", "v195", "v196", "v197", "v198", "v199", "v200", "v201", "v202", "v203", "v204", "v205", "v206", "v207",
        "v208", "v209", "v210", "v211", "v212", "v213", "v214", "v215", "v216", "v217", "v218", "v219", "v220", "v221", "v222", "v223", "v224", "v225", "v226", "v227",
        "v228", "v229", "v230", "v231", "v232", "v233", "v234", "v235", "v236", "v237", "v238", "v239", "v240", "v241", "v242", "v243", "v244", "v245", "v246", "v247",
        "v248", "v249", "v250", "v251", "v252", "v253", "v254", "v255", "s12", "s13", "s14", "s15", "s16", "s17", "s18", "s19", "s20", "s21", "s22", "s23",
        "s24", "s25", "s26", "s27", "s28", "s29", "s30", "s31", "s33", "s34", "s35", "s36", "s37", "s38", "s39", "s40", "s41", "s42", "s43", "s44",
        "s45", "s46", "s47", "s48", "s49", "s50", "s51", "s52", "s53", "s54", "s55", "s56", "s57", "s58", "s59", "s60", "s61", "s62", "s63", "s64",
        "s65", "s66", "s67", "s68", "s69", "s70", "s71", "s72", "s73", "s74", "s75", "s76", "s77", "s78", "s79",
        "vcc", "scc", "memory");
  const int b = blockIdx.x;
  GETST();
  STAMP(11);
  PEND();
}

extern "C" void kernel_launch(void* const* d_in, const int* in_sizes, int n_in, void* d_out, int out_size,
                              void* d_ws, size_t ws_size, hipStream_t stream) {
  const u16* const* in = (const u16* const*)d_in;
  fused<<<256, 512, 0, stream>>>(in[0], in[1], in[2], in[3], in[4], in[5], in[8], in[6], in[9], in[10], in[11],
                                 (u16*)d_out, (char*)d_ws);
}
```

```cpp
#include <hip/hip_runtime.h>
#include <stdint.h>

typedef unsigned short u16;
typedef unsigned int u32;
typedef unsigned long long u64;
typedef __attribute__((ext_vector_type(4))) u32 u32x4;
typedef __attribute__((ext_vector_type(16))) u32 u32x16;
#define AS3 __attribute__((address_space(3)))

#define STAMP(I)
#define PEND()
#define GETST()
constexpr int SPIN_MAX = 1 << 16;
constexpr int UNUSED = 0;

__global__ __launch_bounds__(512, 2) void fused(const u16* __restrict__ x, const u16* __restrict__ lar,
    const u16* __restrict__ aim, const u16* __restrict__ cre, const u16* __restrict__ cim,
    const u16* __restrict__ ldt, const u16* __restrict__ wr, const u16* __restrict__ dsk,
    const u16* __restrict__ wg, const u16* __restrict__ wu, const u16* __restrict__ wd,
    u16* __restrict__ out, char* ws) {
  __shared__ __attribute__((aligned(16))) char smem_[163840];
  AS3 char* sm = (AS3 char*)smem_;
  u64 ptm = 0, pc0 = 0, pt0 = 0, pte = 0, st_[16] = {};
  u32x16 sts;
  u32x4 st2;
  asm volatile(
      "s_memrealtime s[12:13]\n"
      "s_load_dwordx16 s[64:79], %[ka], 0x0\n"
      "s_load_dwordx8 s[80:87], %[ka], 0x40\n"
      "s_load_dwordx2 s[88:89], %[ka], 0x60\n"
      "v_readfirstlane_b32 s33, v0\n"
      "s_getpc_b64 s[50:51]\n"
      ".Lpc%=:\n"
      "s_mov_b32 s34, %[bid]\n"
      "v_and_b32 v43, 63, v0\n"
      "v_mov_b32 v15, 0\n"
      "s_lshr_b32 s33, s33, 6\n"
      "s_and_b32 s52, s34, -8\n"
      "s_add_u32 s52, s52, s33\n"
      "s_min_u32 s52, s52, (.LEnd%=-.Lpc%=)>>7\n"
      "s_lshl_b32 s52, s52, 7\n"
      "s_and_b32 s50, s50, 0xffffff80\n"
      "s_add_u32 s50, s50, s52\n"
      "s_addc_u32 s51, s51, 0\n"
      "global_load_dword v11, v15, s[50:51] \n"
      "s_and_b32 s48, s34, 7\n"
      "s_lshr_b32 s49, s34, 3\n"
      "s_and_b32 s55, s49, 15\n"
      "s_lshl4_add_u32 s35, s48, s55\n"
      "s_cmp_lt_u32 s34, 128\n"
      "s_cbranch_scc1 .LH%=\n"
      "s_lshl3_add_u32 s50, s35, s33\n"
      "s_lshr_b32 s51, s34, 5\n"
      "s_andn2_b32 s51, 4, s51\n"
      "s_add_u32 s50, s50, s51\n"
      "s_lshl_b32 s51, s50, 8\n"
      "s_lshl_b32 s50, s50, 1\n"
      "v_lshl_add_u32 v48, v43, 2, s51\n"
      "v_mov_b32 v49, s50\n"
      "s_waitcnt lgkmcnt(0)\n"
      "global_load_dword v16, v48, s[66:67] nt\n"
      "global_load_dword v17, v48, s[68:69] nt\n"
      "global_load_ushort v12, v49, s[74:75] \n"
      "global_load_dword v18, v48, s[70:71] nt\n"
      "global_load_dword v19, v48, s[72:73] nt\n"
      "global_load_ushort v13, v49, s[78:79] \n"
      "s_load_dword s15, s[88:89], 0x0\n"
      "s_setprio 3\n"
      "s_branch .LC%=\n"
      ".LH%=:\n"
      "s_cmp_ge_u32 s33, 4\n"
      "s_cbranch_scc1 .LG%=\n"
      ".LD%=:\n"
      "v_and_b32 v52, 48, v43\n"
      "v_and_b32 v53, 3, v43\n"
      "v_lshl_add_u32 v53, v53, 2, v52\n"
      "v_and_b32 v54, 12, v43\n"
      "v_lshlrev_b32 v53, 2, v53\n"
      "v_lshl_add_u32 v52, v54, 9, v53\n"
      "s_and_b32 s53, s34, 127\n"
      "s_and_b32 s52, s33, 3\n"
      "s_lshl2_add_u32 s14, s53, s52\n"
      "s_lshl_b32 s51, s14, 13\n"
      "v_lshl_add_u32 v53, v54, 6, v53\n"
      "s_waitcnt lgkmcnt(0)\n"
      "s_add_u32 s52, s64, s51\n"
      "s_addc_u32 s53, s65, 0\n"
      "global_load_dwordx4 v[224:227], v53, s[76:77] offset:0\n"
      "global_load_dwordx4 v[228:231], v53, s[76:77] offset:1024\n"
      "global_load_dwordx4 v[192:195], v52, s[52:53] offset:0\n"
      "global_load_dwordx4 v[196:199], v52, s[52:53] offset:256\n"
      "global_load_dwordx4 v[200:203], v52, s[52:53] offset:512\n"
      "global_load_dwordx4 v[204:207], v52, s[52:53] offset:768\n"
      "global_load_dwordx4 v[208:211], v52, s[52:53] offset:1024\n"
      "global_load_dwordx4 v[212:215], v52, s[52:53] offset:1280\n"
      "global_load_dwordx4 v[216:219], v52, s[52:53] offset:1536\n"
      "global_load_dwordx4 v[220:223], v52, s[52:53] offset:1792\n"
      "s_load_dword s15, s[88:89], 0x0\n"
      "s_setprio 3\n"
      "s_cmp_lg_u32 s33, 0\n"
      "s_cbranch_scc1 .Ltb%=\n"
      "v_mov_b32 v48, 0x22680\n"
      "v_mov_b32 v49, s12\n"
      "ds_write_b32 v48, v49 \n"
      "v_and_b32 v50, 31, v43\n"
      "v_lshlrev_b32 v50, 2, v50\n"
      "v_add_u32 v50, 0x22600, v50\n"
      "ds_write_b32 v50, v15 \n"
      "s_waitcnt lgkmcnt(0)\n"
      ".Ltb%=:\n"
      "s_barrier\n"
      "s_mov_b32 s16, s88\n"
      "s_and_b32 s17, s89, 0xffff\n"
      "s_mov_b32 s18, 0x6e0000\n"
      "s_mov_b32 s19, 0x20000\n"
      "s_mov_b32 s20, s64\n"
      "s_and_b32 s21, s65, 0xffff\n"
      "s_mov_b32 s22, 0x400000\n"
      "s_mov_b32 s23, 0x20000\n"
      "s_mov_b32 s28, s84\n"
      "s_and_b32 s29, s85, 0xffff\n"
      "s_mov_b32 s30, 0x700000\n"
      "s_mov_b32 s31, 0x20000\n"
      "s_mov_b32 s24, s86\n"
      "s_and_b32 s25, s87, 0xffff\n"
      "s_mov_b32 s26, 0x400000\n"
      "s_mov_b32 s27, 0x20000\n"
      "s_lshl_b32 s42, s34, 3\n"
      "s_add_u32 s42, s42, 2048\n"
      "s_lshl_b32 s43, s48, 8\n"
      "s_and_b32 s50, s49, 1\n"
      "s_lshl_b32 s50, s50, 7\n"
      "s_add_u32 s43, s43, s50\n"
      "s_add_u32 s43, s43, 65536\n"
      "v_mov_b32 v20, 0x3f803f80\n"
      "v_mov_b32 v21, 0x3f803f80\n"
      "v_mov_b32 v22, 0x3f803f80\n"
      "v_mov_b32 v23, 0x3f803f80\n"
      "v_mov_b32 v44, 0x7f000000\n"
      "v_mov_b32 v25, 0\n"
      "s_lshr_b32 s70, s14, 7\n"
      "s_mul_i32 s70, s70, 61440\n"
      "s_lshl_b32 s68, s14, 5\n"
      "s_add_u32 s68, s68, s70\n"
      "s_add_u32 s68, s68, 131072\n"
      "s_lshr_b32 s70, s14, 8\n"
      "s_mul_i32 s70, s70, 61440\n"
      "s_lshl4_add_u32 s69, s14, s70\n"
      "s_add_u32 s69, s69, 393216\n"
      "v_writelane_b32 v44, s68, 0\n"
      "s_add_u32 s68, s68, 8\n"
      "v_writelane_b32 v44, s68, 20\n"
      "s_add_u32 s68, s68, 8\n"
      "v_writelane_b32 v44, s68, 40\n"
      "s_add_u32 s68, s68, 8\n"
      "v_writelane_b32 v44, s68, 60\n"
      "s_add_u32 s68, s68, 8\n"
      "v_writelane_b32 v44, s69, 1\n"
      "s_add_u32 s69, s69, 8\n"
      "v_writelane_b32 v44, s69, 2\n"
      "s_waitcnt lgkmcnt(0)\n"
      "s_add_u32 s15, s15, 0x9e3779b1\n"
      "v_mov_b32 v47, s15\n"
      "s_waitcnt vmcnt(9)\n"
      "v_mfma_f32_16x16x32_bf16 v[16:19], v[20:23], v[224:227], 0\n"
      "s_waitcnt vmcnt(8)\n"
      "v_mfma_f32_16x16x32_bf16 v[16:19], v[20:23], v[228:231], v[16:19]\n"
      "s_waitcnt vmcnt(7)\n"
      "v_mfma_f32_16x16x32_bf16 v[232:235], v[192:195], v[224:227], 0\n"
      "v_mfma_f32_16x16x32_bf16 v[252:255], v[192:195], v[192:195], 0\n"
      "v_mfma_f32_16x16x32_bf16 v[248:251], v[192:195], v[20:23], 0\n"
      "s_waitcnt vmcnt(6)\n"
      "v_mfma_f32_16x16x32_bf16 v[236:239], v[196:199], v[224:227], 0\n"
      "v_mfma_f32_16x16x32_bf16 v[252:255], v[196:199], v[196:199], v[252:255]\n"
      "v_mfma_f32_16x16x32_bf16 v[248:251], v[196:199], v[20:23], v[248:251]\n"
      "s_waitcnt vmcnt(5)\n"
      "v_mfma_f32_16x16x32_bf16 v[240:243], v[200:203], v[224:227], 0\n"
      "v_mfma_f32_16x16x32_bf16 v[252:255], v[200:203], v[200:203], v[252:255]\n"
      "v_mfma_f32_16x16x32_bf16 v[248:251], v[200:203], v[20:23], v[248:251]\n"
      "s_waitcnt vmcnt(4)\n"
      "v_mfma_f32_16x16x32_bf16 v[244:247], v[204:207], v[224:227], 0\n"
      "v_mfma_f32_16x16x32_bf16 v[252:255], v[204:207], v[204:207], v[252:255]\n"
      "v_mfma_f32_16x16x32_bf16 v[248:251], v[204:207], v[20:23], v[248:251]\n"
      "s_waitcnt vmcnt(3)\n"
      "v_mfma_f32_16x16x32_bf16 v[232:235], v[208:211], v[228:231], v[232:235]\n"
      "v_mfma_f32_16x16x32_bf16 v[252:255], v[208:211], v[208:211], v[252:255]\n"
      "v_mfma_f32_16x16x32_bf16 v[248:251], v[208:211], v[20:23], v[248:251]\n"
      "v_add_f32_dpp v16, v16, v16 quad_perm:[1,0,3,2] row_mask:0xf bank_mask:0xf\n"
      "s_waitcnt vmcnt(2)\n"
      "v_mfma_f32_16x16x32_bf16 v[236:239], v[212:215], v[228:231], v[236:239]\n"
      "v_mfma_f32_16x16x32_bf16 v[252:255], v[212:215], v[212:215], v[252:255]\n"
      "v_mfma_f32_16x16x32_bf16 v[248:251], v[212:215], v[20:23], v[248:251]\n"
      "v_add_f32_dpp v16, v16, v16 quad_perm:[2,3,0,1] row_mask:0xf bank_mask:0xf\n"
      "v_add_f32_dpp v25, v232, v25 quad_perm:[0,0,0,0] row_mask:0xf bank_mask:0x1\n"
      "s_nop 1\n"
      "v_add_f32_dpp v25, v233, v25 quad_perm:[1,1,1,1] row_mask:0xf bank_mask:0x1\n"
      "s_nop 1\n"
      "v_add_f32_dpp v25, v234, v25 quad_perm:[2,2,2,2] row_mask:0xf bank_mask:0x1\n"
      "s_nop 1\n"
      "v_add_f32_dpp v25, v235, v25 quad_perm:[3,3,3,3] row_mask:0xf bank_mask:0x1\n"
      "s_waitcnt vmcnt(1)\n"
      "v_mfma_f32_16x16x32_bf16 v[240:243], v[216:219], v[228:231], v[240:243]\n"
      "v_mfma_f32_16x16x32_bf16 v[252:255], v[216:219], v[216:219], v[252:255]\n"
      "v_mfma_f32_16x16x32_bf16 v[248:251], v[216:219], v[20:23], v[248:251]\n"
      "v_add_f32_dpp v16, v16, v16 row_ror:4 row_mask:0xf bank_mask:0xf\n"
      "v_add_f32_dpp v25, v236, v25 quad_perm:[0,0,0,0] row_mask:0xf bank_mask:0x2\n"
      "s_nop 1\n"
      "v_add_f32_dpp v25, v237, v25 quad_perm:[1,1,1,1] row_mask:0xf bank_mask:0x2\n"
      "s_nop 1\n"
      "v_add_f32_dpp v25, v238, v25 quad_perm:[2,2,2,2] row_mask:0xf bank_mask:0x2\n"
      "s_nop 1\n"
      "v_add_f32_dpp v25, v239, v25 quad_perm:[3,3,3,3] row_mask:0xf bank_mask:0x2\n"
      "s_waitcnt vmcnt(0)\n"
      "v_mfma_f32_16x16x32_bf16 v[244:247], v[220:223], v[228:231], v[244:247]\n"
      "v_mfma_f32_16x16x32_bf16 v[252:255], v[220:223], v[220:223], v[252:255]\n"
      "v_mfma_f32_16x16x32_bf16 v[248:251], v[220:223], v[20:23], v[248:251]\n"
      "v_add_f32_dpp v16, v16, v16 row_ror:8 row_mask:0xf bank_mask:0xf\n"
      "v_add_f32_dpp v25, v240, v25 quad_perm:[0,0,0,0] row_mask:0xf bank_mask:0x4\n"
      "s_nop 1\n"
      "v_add_f32_dpp v25, v241, v25 quad_perm:[1,1,1,1] row_mask:0xf bank_mask:0x4\n"
      "s_nop 1\n"
      "v_add_f32_dpp v25, v242, v25 quad_perm:[2,2,2,2] row_mask:0xf bank_mask:0x4\n"
      "s_nop 1\n"
      "v_add_f32_dpp v25, v243, v25 quad_perm:[3,3,3,3] row_mask:0xf bank_mask:0x4\n"
      "s_nop 1\n"
      "v_add_f32_dpp v25, v244, v25 quad_perm:[0,0,0,0] row_mask:0xf bank_mask:0x8\n"
      "v_mov_b32_dpp v24, v252 quad_perm:[0,0,0,0] row_mask:0xf bank_mask:0xf\n"
      "v_mov_b32_dpp v27, v254 quad_perm:[2,2,2,2] row_mask:0xf bank_mask:0xf\n"
      "v_add_f32_dpp v25, v245, v25 quad_perm:[1,1,1,1] row_mask:0xf bank_mask:0x8\n"
      "v_add_f32 v248, v248, v249\n"
      "v_add_f32 v250, v250, v251\n"
      "v_add_f32_dpp v24, v253, v24 quad_perm:[1,1,1,1] row_mask:0xf bank_mask:0xf\n"
      "v_add_f32_dpp v27, v255, v27 quad_perm:[3,3,3,3] row_mask:0xf bank_mask:0xf\n"
      "v_add_f32_dpp v25, v246, v25 quad_perm:[2,2,2,2] row_mask:0xf bank_mask:0x8\n"
      "v_add_f32 v248, v248, v250\n"
      "v_add_f32 v24, v24, v27\n"
      "v_add_f32_dpp v25, v247, v25 quad_perm:[3,3,3,3] row_mask:0xf bank_mask:0x8\n"
      "v_mul_f32 v248, 0x3a800000, v248\n"
      "v_mul_f32 v24, 0x3a800000, v24\n"
      "v_add_f32_dpp v25, v25, v25 row_ror:4 row_mask:0xf bank_mask:0xf\n"
      "v_fma_f32 v24, -v248, v248, v24\n"
      "v_max_f32 v24, 0, v24\n"
      "v_add_f32_dpp v25, v25, v25 row_ror:8 row_mask:0xf bank_mask:0xf\n"
      "v_cvt_pk_bf16_f32 v248, v248, v24\n"
      "v_and_b32 v24, 0xffff0000, v248\n"
      "v_add_f32 v24, 0x3727c5ac, v24\n"
      "v_lshlrev_b32 v26, 16, v248\n"
      "v_cvt_pk_bf16_f32 v24, v24, v24\n"
      "v_fma_f32 v25, -v26, v16, v25\n"
      "v_and_b32 v24, 0xffff0000, v24\n"
      "v_sqrt_f32 v24, v24\n"
      "s_nop 0\n"
      "v_cvt_pk_bf16_f32 v46, v26, v24\n"
      "v_and_b32 v24, 0xffff0000, v46\n"
      "v_rcp_f32 v24, v24\n"
      "s_nop 0\n"
      "v_mul_f32 v25, v25, v24\n"
      "v_cvt_pk_bf16_f32 v25, v25, v25\n"
      "s_nop 0\n"
      "v_readlane_b32 s68, v25, 0\n"
      "v_readlane_b32 s69, v25, 20\n"
      "v_readlane_b32 s70, v25, 40\n"
      "v_readlane_b32 s71, v25, 60\n"
      "s_pack_ll_b32_b16 s68, s68, s69\n"
      "s_pack_ll_b32_b16 s70, s70, s71\n"
      "v_writelane_b32 v46, s68, 1\n"
      "v_writelane_b32 v46, s70, 2\n"
      "buffer_store_dwordx2 v[46:47], v44, s[16:19], 0 offen sc1\n"
      "v_mov_b32 v48, v43\n"
      "v_and_b32 v49, 7, v43\n"
      "v_lshrrev_b32 v50, 3, v43\n"
      "s_cmp_ge_u32 s34, 112\n"
      "s_cbranch_scc1 .LS%=\n"
      "s_branch .LD2%=\n"
      ".LC%=:\n"
      "s_mov_b32 s16, s88\n"
      "s_and_b32 s17, s89, 0xffff\n"
      "s_mov_b32 s18, 0x6e0000\n"
      "s_mov_b32 s19, 0x20000\n"
      "s_mov_b32 s20, s64\n"
      "s_and_b32 s21, s65, 0xffff\n"
      "s_mov_b32 s22, 0x400000\n"
      "s_mov_b32 s23, 0x20000\n"
      "s_mul_i32 s39, s33, 6528\n"
      "s_mul_i32 s40, s33, 8768\n"
      "s_add_u32 s52, s40, 52224\n"
      "s_add_u32 s53, s52, 4160\n"
      "s_lshl_b32 s42, s34, 3\n"
      "s_add_u32 s42, s42, 2048\n"
      "v_and_b32 v48, 31, v43\n"
      "v_lshrrev_b32 v49, 5, v43\n"
      "v_lshlrev_b32 v55, 4, v49\n"
      "v_lshrrev_b32 v52, 2, v48\n"
      "v_mul_u32_u24 v52, 0x110, v52\n"
      "v_add3_u32 v20, v52, v55, s52\n"
      "v_and_b32 v52, 3, v43\n"
      "v_mul_u32_u24 v52, 0x110, v52\n"
      "s_add_u32 s50, s52, 2176\n"
      "v_add3_u32 v178, v52, v55, s50\n"
      "v_lshl_add_u32 v52, v43, 2, s50\n"
      "v_mov_b32 v53, 0x3c00\n"
      "ds_write_b32 v52, v53 \n"
      "ds_write_b32 v52, v53 offset:3264\n"
      "v_and_b32 v52, 12, v43\n"
      "s_mov_b32 s50, 0x6600\n"
      "s_mov_b32 s51, 0x6600\n"
      "v_cndmask_b32 v52, v48, v52, s[50:51]\n"
      "s_mov_b32 s50, 0xff000000\n"
      "s_mov_b32 s51, 0xff000000\n"
      "v_cndmask_b32 v52, v52, 9, s[50:51]\n"
      "v_mul_u32_u24 v52, 0x110, v52\n"
      "v_add3_u32 v21, v52, v55, s39\n"
      "v_lshl_add_u32 v52, v43, 2, s39\n"
      "v_mov_b32 v53, 0x3c00\n"
      "ds_write_b32 v52, v53 \n"
      "s_mov_b32 s24, s86\n"
      "s_and_b32 s25, s87, 0xffff\n"
      "s_mov_b32 s26, 0x400000\n"
      "s_mov_b32 s27, 0x20000\n"
      "s_mul_i32 s54, s33, 4864\n"
      "s_add_u32 s54, s54, 122368\n"
      "s_lshl_b32 s41, s35, 4\n"
      "s_and_b32 s43, s41, 0xffffff80\n"
      "s_add_u32 s43, s43, 67584\n"
      "v_and_b32 v50, 15, v43\n"
      "v_lshrrev_b32 v51, 4, v43\n"
      "v_and_b32 v53, 1, v43\n"
      "s_and_b32 s58, s33, 3\n"
      "s_lshl4_add_u32 s50, s33, s55\n"
      "s_lshl_b32 s50, s50, 15\n"
      "s_and_b32 s51, s55, 8\n"
      "s_lshl_b32 s51, s51, 4\n"
      "s_sub_u32 s50, s50, s51\n"
      "v_bfe_u32 v52, v43, 1, 4\n"
      "v_lshl_add_u32 v52, v52, 4, v53\n"
      "v_lshlrev_b32 v52, 7, v52\n"
      "v_add_u32 v15, s50, v52\n"
      "s_bitcmp1_b32 s55, 0\n"
      "s_cselect_b32 s56, s82, s80\n"
      "s_cselect_b32 s57, s83, s81\n"
      "s_lshr_b32 s50, s56, 7\n"
      "s_sub_u32 s50, s48, s50\n"
      "s_add_u32 s50, s50, 3\n"
      "s_and_b32 s50, s50, 7\n"
      "s_min_u32 s50, s50, 6\n"
      "s_mul_i32 s51, s48, 7\n"
      "s_add_u32 s50, s50, s51\n"
      "s_lshl_b32 s50, s50, 7\n"
      "s_lshr_b32 s51, s55, 1\n"
      "s_mul_i32 s51, s51, 24\n"
      "s_lshl_b32 s58, s58, 8\n"
      "s_add_u32 s51, s51, s58\n"
      "s_add_u32 s51, s51, 64\n"
      "s_mul_i32 s51, s51, 7168\n"
      "s_add_u32 s50, s50, s51\n"
      "v_min_u32 v57, 23, v43\n"
      "v_mul_u32_u24 v57, 0x1c00, v57\n"
      "v_add_u32 v57, s50, v57\n"
      "v_lshl_add_u32 v52, v48, 1, v49\n"
      "v_lshlrev_b32 v54, 3, v52\n"
      "s_add_u32 s50, s53, 570\n"
      "v_sub_u32 v22, s50, v54\n"
      "s_lshl_b32 s50, s33, 17\n"
      "v_lshl_add_u32 v8, v52, 11, s50\n"
      "v_mov_b32 v52, 0x63e\n"
      "v_mov_b32 v54, 0x6de\n"
      "s_mov_b32 s50, 0x55555555\n"
      "s_mov_b32 s51, 0x55555555\n"
      "v_cndmask_b32 v24, v52, v54, s[50:51]\n"
      "v_mov_b32 v52, 0x4de\n"
      "v_mov_b32 v54, 0x59e\n"
      "v_cndmask_b32 v52, v52, v54, s[50:51]\n"
      "s_mov_b32 s58, 0\n"
      "s_mov_b32 s59, -1\n"
      "v_cndmask_b32 v24, v24, v52, s[58:59]\n"
      "v_add_u32 v52, -1, v48\n"
      "v_and_b32 v52, -2, v52\n"
      "v_sub_u32 v24, v24, v52\n"
      "v_add_u32 v24, s53, v24\n"
      "v_add_u32 v23, 32, v24\n"
      "s_add_u32 s60, s53, 2048\n"
      "v_writelane_b32 v23, s60, 0\n"
      "v_writelane_b32 v23, s60, 32\n"
      "v_mul_u32_u24 v52, 0xc1, v53\n"
      "v_sub_u32 v52, v52, v48\n"
      "s_add_u32 s60, s53, 1150\n"
      "v_add_u32 v52, s60, v52\n"
      "v_add_u32 v25, 0xffffffc0, v24\n"
      "v_cndmask_b32 v25, v25, v52, s[58:59]\n"
      "v_and_b32 v52, -2, v43\n"
      "v_add_u32 v52, 58, v52\n"
      "v_mov_b32 v54, 0x810\n"
      "s_mov_b32 s60, 0xfffff000\n"
      "s_mov_b32 s61, 0xffffffff\n"
      "v_cndmask_b32 v52, v52, v54, s[60:61]\n"
      "s_movk_i32 s60, 1408\n"
      "s_movk_i32 s61, 1664\n"
      "v_writelane_b32 v52, s60, 8\n"
      "v_writelane_b32 v52, s60, 9\n"
      "v_writelane_b32 v52, s61, 10\n"
      "v_writelane_b32 v52, s61, 11\n"
      "v_mul_u32_u24 v54, 0x83e, v53\n"
      "v_add3_u32 v26, v52, v54, s53\n"
      "v_lshlrev_b32 v52, 1, v50\n"
      "v_add_u32 v52, v52, v54\n"
      "s_add_u32 s60, s53, 2140\n"
      "v_sub_u32 v52, s60, v52\n"
      "v_lshl_add_u32 v29, v51, 4, v52\n"
      "v_lshl_add_u32 v30, v51, 3, v52\n"
      "v_add_u32 v30, 0x440, v30\n"
      "v_bfe_u32 v52, v43, 2, 3\n"
      "v_max_u32 v54, 5, v52\n"
      "v_mul_u32_u24 v52, 0x60, v52\n"
      "v_lshl_add_u32 v52, v54, 6, v52\n"
      "v_add_u32 v52, 0x2a0, v52\n"
      "s_mov_b32 s60, 0xf\n"
      "s_mov_b32 s61, 0xf\n"
      "v_cndmask_b32 v52, v52, v55, s[60:61]\n"
      "v_mul_u32_u24 v54, 0x830, v49\n"
      "v_add3_u32 v27, v52, v54, s53\n"
      "v_and_b32 v55, 3, v43\n"
      "v_lshl_add_u32 v27, v55, 4, v27\n"
      "s_add_u32 s60, s39, 3104\n"
      "s_add_u32 s61, s52, 2080\n"
      "v_mov_b32 v52, s39\n"
      "v_mov_b32 v54, s60\n"
      "s_mov_b32 s50, 0xff00ff00\n"
      "s_mov_b32 s51, 0xff00ff00\n"
      "v_cndmask_b32 v28, v52, v54, s[50:51]\n"
      "v_mov_b32 v52, s52\n"
      "v_mov_b32 v54, s61\n"
      "v_cndmask_b32 v52, v52, v54, s[50:51]\n"
      "s_mov_b32 s50, 0xffff0000\n"
      "s_mov_b32 s51, 0xffff0000\n"
      "v_cndmask_b32 v28, v28, v52, s[50:51]\n"
      "v_and_b32 v52, 7, v43\n"
      "v_lshl_add_u32 v28, v52, 2, v28\n"
      "v_lshlrev_b32 v9, 3, v52\n"
      "v_bfe_u32 v10, v43, 3, 2\n"
      "v_lshlrev_b32 v10, 2, v10\n"
      "v_lshl_add_u32 v58, v49, 8, v48\n"
      "s_lshl_b32 s50, s33, 5\n"
      "v_add_u32 v58, s50, v58\n"
      "s_mov_b32 s50, 122912\n"
      "v_lshl_add_u32 v32, v58, 2, s50\n"
      "v_lshlrev_b32 v41, 12, v58\n"
      "v_mul_u32_u24 v42, 0xf000, v49\n"
      "v_lshl_add_u32 v42, v58, 4, v42\n"
      "v_lshl_add_u32 v52, v50, 1, v51\n"
      "v_lshl_add_u32 v33, v52, 4, s54\n"
      "s_add_u32 s50, s54, 528\n"
      "v_lshl_add_u32 v34, v43, 4, s50\n"
      "v_lshl_add_u32 v35, v43, 3, s39\n"
      "v_lshl_add_u32 v36, v43, 3, s52\n"
      "s_add_u32 s50, s39, 32\n"
      "v_lshl_add_u32 v37, v43, 4, s50\n"
      "s_add_u32 s50, s52, 32\n"
      "v_lshl_add_u32 v38, v43, 4, s50\n"
      "v_lshl_add_u32 v52, v50, 2, v51\n"
      "v_lshl_add_u32 v39, v52, 2, s53\n"
      "v_bfe_u32 v52, v43, 2, 2\n"
      "v_mul_u32_u24 v52, 0x2240, v52\n"
      "v_lshl_add_u32 v52, v49, 11, v52\n"
      "v_lshl_add_u32 v52, v55, 3, v52\n"
      "v_and_b32 v54, 16, v43\n"
      "v_lshl_add_u32 v52, v54, 1, v52\n"
      "s_lshl_b32 s50, s33, 6\n"
      "s_add_u32 s50, s50, 56384\n"
      "v_add_u32 v40, s50, v52\n"
      "v_mov_b32 v54, 0\n"
      "v_mov_b32 v55, 0\n"
      "v_lshl_add_u32 v52, v43, 3, s54\n"
      "ds_write_b64 v52, v[54:55] offset:32\n"
      "s_mov_b32 s50, 161536\n"
      "v_lshl_add_u32 v52, v0, 2, s50\n"
      "ds_write_b32 v52, v54 \n"
      "v_mov_b32 v52, 0xffff\n"
      "v_mov_b32 v53, 0xffff0000\n"
      "s_mov_b32 s50, 0x0\n"
      "s_mov_b32 s51, 0x18000018\n"
      "v_cndmask_b32 v44, 0, v52, s[50:51]\n"
      "s_mov_b32 s60, 0x0\n"
      "s_mov_b32 s61, 0x30000030\n"
      "v_cndmask_b32 v54, 0, v53, s[60:61]\n"
      "v_or_b32 v44, v44, v54\n"
      "s_mov_b32 s50, 0x0\n"
      "s_mov_b32 s51, 0x60000060\n"
      "v_cndmask_b32 v45, 0, v52, s[50:51]\n"
      "s_mov_b32 s60, 0x0\n"
      "s_mov_b32 s61, 0xc00000c0\n"
      "v_cndmask_b32 v54, 0, v53, s[60:61]\n"
      "v_or_b32 v45, v45, v54\n"
      "s_mov_b32 s50, 0x10000\n"
      "s_mov_b32 s51, 0x80000180\n"
      "v_cndmask_b32 v46, 0, v52, s[50:51]\n"
      "s_mov_b32 s60, 0x30000\n"
      "s_mov_b32 s61, 0x300\n"
      "v_cndmask_b32 v54, 0, v53, s[60:61]\n"
      "v_or_b32 v46, v46, v54\n"
      "s_mov_b32 s50, 0x60000\n"
      "s_mov_b32 s51, 0x600\n"
      "v_cndmask_b32 v47, 0, v52, s[50:51]\n"
      "s_mov_b32 s60, 0xc0000\n"
      "s_mov_b32 s61, 0xc00\n"
      "v_cndmask_b32 v54, 0, v53, s[60:61]\n"
      "v_or_b32 v47, v47, v54\n"
      ".Lsc%=:\n"
      "s_waitcnt vmcnt(0)\n"
      "s_mov_b32 s50, 0x3fb8aa3b\n"
      "v_mov_b32 v14, v57\n"
      "v_lshlrev_b32 v48, 16, v12\n"
      "v_and_b32 v52, 0x7fff7fff, v18\n"
      "v_and_b32 v53, 0x7fff7fff, v19\n"
      "v_mul_f32 v49, s50, v48\n"
      "v_pk_max_u16 v52, v52, v53\n"
      "v_fma_f32 v50, v48, s50, -v49\n"
      "v_rndne_f32 v51, v49\n"
      "v_lshlrev_b32 v53, 16, v52\n"
      "v_fmamk_f32 v50, v48, 0x32a5705f, v50\n"
      "v_sub_f32 v49, v49, v51\n"
      "v_max_u32 v52, v52, v53\n"
      "v_add_f32 v49, v49, v50\n"
      "v_cvt_i32_f32 v51, v51\n"
      "v_max_u32_dpp v52, v52, v52 row_shr:1 row_mask:0xf bank_mask:0xf bound_ctrl:1\n"
      "v_exp_f32 v49, v49\n"
      "v_lshlrev_b32 v54, 16, v13\n"
      "v_max_u32_dpp v52, v52, v52 row_shr:2 row_mask:0xf bank_mask:0xf bound_ctrl:1\n"
      "v_ldexp_f32 v49, v49, v51\n"
      "v_add_f32 v54, 1.0, v54\n"
      "v_max_u32_dpp v52, v52, v52 row_shr:4 row_mask:0xf bank_mask:0xf bound_ctrl:1\n"
      "v_cvt_pk_bf16_f32 v49, v49, v49\n"
      "s_waitcnt lgkmcnt(0)\n"
      "v_max_u32_dpp v52, v52, v52 row_shr:8 row_mask:0xf bank_mask:0xf bound_ctrl:1\n"
      "v_and_b32 v49, 0xffff0000, v49\n"
      "v_and_b32 v55, 0x7fffffff, v54\n"
      "v_max_u32_dpp v52, v52, v52 row_bcast:15 row_mask:0xa bank_mask:0xf\n"
      "v_mul_f32 v50, 0xbfb8aa3b, v49\n"
      "v_mul_f32 v51, 0xbe22f983, v49\n"
      "v_max_u32_dpp v52, v52, v52 row_bcast:31 row_mask:0xc bank_mask:0xf\n"
      "v_readfirstlane_b32 s36, v50\n"
      "v_readlane_b32 s50, v52, 63\n"
      "v_readfirstlane_b32 s37, v51\n"
      "v_mul_f32 v55, 0x3b800000, v55\n"
      "v_mul_f32 v52, s50, v49\n"
      "s_add_u32 s15, s15, 0x9e3779b1\n"
      "v_max_f32 v52, v52, v55\n"
      "v_frexp_exp_i32_f32 v52, v52\n"
      "s_nop 0\n"
      "v_readfirstlane_b32 s50, v52\n"
      "s_nop 0\n"
      "s_sub_i32 s50, 7, s50\n"
      "s_max_i32 s50, s50, -107\n"
      "s_min_i32 s50, s50, 93\n"
      "v_ldexp_f32 v49, v49, s50\n"
      "s_sub_i32 s51, s50, 1\n"
      "v_ldexp_f32 v54, v54, s51\n"
      "s_sub_i32 s14, 128, s50\n"
      "s_lshl_b32 s14, s14, 23\n"
      "v_readfirstlane_b32 s38, v49\n"
      "s_mov_b64 s[60:61], 1\n"
      "v_cndmask_b32 v31, 0, v54, s[60:61]\n"
      "s_lshl_b32 s50, s33, 2\n"
      "s_add_u32 s50, s50, 161280\n"
      "v_mov_b32 v50, s50\n"
      "v_mov_b32 v51, s14\n"
      "ds_write_b32 v50, v51 \n"
      "s_mov_b32 s60, s41\n"
      "s_add_u32 s61, s41, 0x200000\n"
      "s_mov_b32 s62, 131072\n"
      "s_mov_b32 s63, 262144\n"
      "s_mov_b32 m0, s40\n"
      "s_add_u32 s13, s40, 52224\n"
      "s_add_u32 s12, s39, 272\n"
      "v_lshlrev_b32 v72, 16, v16\n"
      "v_and_b32 v73, 0xffff0000, v16\n"
      "v_lshlrev_b32 v74, 16, v17\n"
      "v_and_b32 v75, 0xffff0000, v17\n"
      "s_mov_b32 s64, 0x3fb8aa3b\n"
      "s_mov_b32 s66, 0x43800000\n"
      "s_mov_b32 s68, 0x42000000\n"
      "s_mov_b32 s70, s37\n"
      "v_pk_mul_f32 v[72:73], s[64:65], v[72:73] op_sel_hi:[0,1]\n"
      "v_pk_mul_f32 v[74:75], s[70:71], v[74:75] op_sel_hi:[0,1]\n"
      "v_exp_f32 v72, v72\n"
      "v_exp_f32 v73, v73\n"
      "v_pk_mul_f32 v[78:79], s[66:67], v[74:75] op_sel_hi:[0,1]\n"
      "v_pk_mul_f32 v[80:81], s[68:69], v[74:75] op_sel_hi:[0,1]\n"
      "v_fract_f32 v76, v74\n"
      "v_fract_f32 v77, v75\n"
      "v_cvt_pk_bf16_f32 v72, v72, v73\n"
      "v_cos_f32 v90, v76\n"
      "v_cos_f32 v91, v77\n"
      "v_fract_f32 v78, v78\n"
      "v_fract_f32 v79, v79\n"
      "v_and_b32 v73, 0xffff0000, v72\n"
      "v_lshlrev_b32 v72, 16, v72\n"
      "v_sin_f32 v92, v76\n"
      "v_sin_f32 v93, v77\n"
      "v_pk_mul_f32 v[82:83], s[36:37], v[72:73] op_sel_hi:[0,1]\n"
      "v_fract_f32 v80, v80\n"
      "v_fract_f32 v81, v81\n"
      "v_exp_f32 v84, v82\n"
      "v_exp_f32 v85, v83\n"
      "v_pk_mul_f32 v[86:87], s[66:67], v[82:83] op_sel_hi:[0,1]\n"
      "v_cos_f32 v94, v78\n"
      "v_cos_f32 v95, v79\n"
      "v_pk_mul_f32 v[88:89], s[68:69], v[82:83] op_sel_hi:[0,1]\n"
      "v_sin_f32 v96, v78\n"
      "v_sin_f32 v97, v79\n"
      "v_pk_mul_f32 v[90:91], v[90:91], v[84:85]\n"
      "v_exp_f32 v86, v86\n"
      "v_exp_f32 v87, v87\n"
      "v_pk_mul_f32 v[92:93], v[92:93], v[84:85]\n"
      "s_cmp_ge_u32 s33, 4\n"
      "s_cbranch_scc1 .LcR%=\n"
      "buffer_load_dword v11, v15, s[20:23], s41 offen\n"
      "global_load_dword v11, v14, s[56:57]\n"
      ".LcR%=:\n"
      "v_pk_mul_f32 v[82:83], v[92:93], v[92:93]\n"
      "v_cos_f32 v202, v80\n"
      "v_pk_add_f32 v[84:85], v[90:91], v[90:91]\n"
      "v_cos_f32 v150, v81\n"
      "v_pk_fma_f32 v[98:99], v[90:91], v[90:91], v[82:83] neg_lo:[0,0,1] neg_hi:[0,0,1]\n"
      "v_pk_mul_f32 v[94:95], v[94:95], v[86:87]\n"
      "v_pk_mul_f32 v[100:101], v[84:85], v[92:93]\n"
      "v_pk_mul_f32 v[96:97], v[96:97], v[86:87]\n"
      "v_cvt_pk_f16_f32 v208, v90, v92\n"
      "v_sin_f32 v203, v80\n"
      "v_cvt_pk_f16_f32 v156, v91, v93\n"
      "v_sin_f32 v151, v81\n"
      "v_pk_mul_f32 v[82:83], v[100:101], v[100:101]\n"
      "v_exp_f32 v88, v88\n"
      "v_pk_add_f32 v[84:85], v[98:99], v[98:99]\n"
      "v_exp_f32 v89, v89\n"
      "v_pk_fma_f32 v[102:103], v[98:99], v[98:99], v[82:83] neg_lo:[0,0,1] neg_hi:[0,0,1]\n"
      "v_pk_mul_f32 v[114:115], v[96:97], v[96:97]\n"
      "v_pk_mul_f32 v[104:105], v[84:85], v[100:101]\n"
      "v_pk_add_f32 v[116:117], v[94:95], v[94:95]\n"
      "v_cvt_pk_f16_f32 v209, v98, v100\n"
      "v_pk_fma_f32 v[106:107], v[94:95], v[94:95], v[114:115] neg_lo:[0,0,1] neg_hi:[0,0,1]\n"
      "v_cvt_pk_f16_f32 v157, v99, v101\n"
      "v_pk_mul_f32 v[108:109], v[116:117], v[96:97]\n"
      "v_pk_mul_f32 v[82:83], v[104:105], v[104:105]\n"
      "v_cvt_pk_f16_f32 v212, v94, v96\n"
      "v_pk_add_f32 v[84:85], v[102:103], v[102:103]\n"
      "v_cvt_pk_f16_f32 v160, v95, v97\n"
      "v_pk_fma_f32 v[90:91], v[102:103], v[102:103], v[82:83] neg_lo:[0,0,1] neg_hi:[0,0,1]\n"
      "v_pk_mul_f32 v[202:203], v[202:203], v[88:89] op_sel:[0,0] op_sel_hi:[1,0]\n"
      "v_pk_mul_f32 v[92:93], v[84:85], v[104:105]\n"
      "v_pk_mul_f32 v[150:151], v[150:151], v[88:89] op_sel:[0,1] op_sel_hi:[1,1]\n"
      "v_cvt_pk_f16_f32 v210, v102, v104\n"
      "v_cvt_pk_f16_f32 v213, v106, v108\n"
      "v_cvt_pk_f16_f32 v158, v103, v105\n"
      "v_cvt_pk_f16_f32 v161, v107, v109\n"
      "v_pk_mul_f32 v[82:83], v[92:93], v[92:93]\n"
      "v_pk_mul_f32 v[114:115], v[108:109], v[108:109]\n"
      "v_pk_add_f32 v[84:85], v[90:91], v[90:91]\n"
      "v_pk_add_f32 v[116:117], v[106:107], v[106:107]\n"
      "v_pk_fma_f32 v[72:73], v[90:91], v[90:91], v[82:83] neg_lo:[0,0,1] neg_hi:[0,0,1]\n"
      "v_pk_fma_f32 v[94:95], v[106:107], v[106:107], v[114:115] neg_lo:[0,0,1] neg_hi:[0,0,1]\n"
      "v_pk_mul_f32 v[74:75], v[84:85], v[92:93]\n"
      "v_pk_mul_f32 v[96:97], v[116:117], v[108:109]\n"
      "v_cvt_pk_f16_f32 v205, v90, v92\n"
      "v_pk_mul_f16 v214, v212, v213 op_sel_hi:[0,1]\n"
      "v_cvt_pk_f16_f32 v153, v91, v93\n"
      "v_pk_mul_f16 v162, v160, v161 op_sel_hi:[0,1]\n"
      "v_pk_mul_f16 v211, v209, v210 op_sel_hi:[0,1]\n"
      "v_pk_fma_f16 v214, v212, v213, v214 op_sel:[1,1,0] op_sel_hi:[1,0,1] neg_lo:[0,1,0]\n"
      "v_pk_mul_f16 v159, v157, v158 op_sel_hi:[0,1]\n"
      "v_pk_fma_f16 v162, v160, v161, v162 op_sel:[1,1,0] op_sel_hi:[1,0,1] neg_lo:[0,1,0]\n"
      "v_pk_fma_f16 v211, v209, v210, v211 op_sel:[1,1,0] op_sel_hi:[1,0,1] neg_lo:[0,1,0]\n"
      "v_cvt_pk_f16_f32 v215, v94, v96\n"
      "v_pk_fma_f16 v159, v157, v158, v159 op_sel:[1,1,0] op_sel_hi:[1,0,1] neg_lo:[0,1,0]\n"
      "v_cvt_pk_f16_f32 v163, v95, v97\n"
      "v_cvt_pk_f16_f32 v206, v72, v74\n"
      "v_lshlrev_b32 v198, 16, v18\n"
      "v_cvt_pk_f16_f32 v154, v73, v75\n"
      "v_lshlrev_b32 v199, 16, v19\n"
      "v_pk_mul_f16 v207, v205, v206 op_sel_hi:[0,1]\n"
      "v_pk_mul_f32 v[198:199], s[38:39], v[198:199] op_sel_hi:[0,1]\n"
      "v_pk_mul_f16 v155, v153, v154 op_sel_hi:[0,1]\n"
      "v_and_b32 v146, 0xffff0000, v18\n"
      "v_pk_fma_f16 v207, v205, v206, v207 op_sel:[1,1,0] op_sel_hi:[1,0,1] neg_lo:[0,1,0]\n"
      "v_and_b32 v147, 0xffff0000, v19\n"
      "v_pk_fma_f16 v155, v153, v154, v155 op_sel:[1,1,0] op_sel_hi:[1,0,1] neg_lo:[0,1,0]\n"
      "v_pk_mul_f32 v[146:147], s[38:39], v[146:147] op_sel_hi:[0,1]\n"
      "v_cvt_pk_f16_f32 v240, v198, v199\n"
      "v_cvt_pk_f16_f32 v241, v146, v147\n"
      "v_pk_mul_f32 v[200:201], v[198:199], v[202:203] op_sel_hi:[1,0]\n"
      "v_pk_mul_f32 v[148:149], v[146:147], v[150:151] op_sel_hi:[1,0]\n"
      "ds_write_addtid_b32 v240 offset:52224\n"
      "ds_write_addtid_b32 v241 offset:55488\n"
      "v_pk_fma_f32 v[200:201], v[198:199], v[202:203], v[200:201] op_sel:[1,1,0] op_sel_hi:[0,1,1] neg_hi:[0,1,0]\n"
      "v_pk_fma_f32 v[148:149], v[146:147], v[150:151], v[148:149] op_sel:[1,1,0] op_sel_hi:[0,1,1] neg_hi:[0,1,0]\n"
      "ds_write_addtid_b32 v205 offset:54672\n"
      "v_cvt_pk_f16_f32 v242, v200, v201\n"
      "v_cvt_pk_f16_f32 v243, v148, v149\n"
      "v_pk_mul_f32 v[198:199], v[200:201], v[202:203] op_sel_hi:[1,0]\n"
      "v_pk_mul_f32 v[146:147], v[148:149], v[150:151] op_sel_hi:[1,0]\n"
      "ds_write_addtid_b32 v242 offset:52496\n"
      "ds_write_addtid_b32 v243 offset:55760\n"
      "v_pk_fma_f32 v[198:199], v[200:201], v[202:203], v[198:199] op_sel:[1,1,0] op_sel_hi:[0,1,1] neg_hi:[0,1,0]\n"
      "v_pk_fma_f32 v[146:147], v[148:149], v[150:151], v[146:147] op_sel:[1,1,0] op_sel_hi:[0,1,1] neg_hi:[0,1,0]\n"
      "ds_write_addtid_b32 v153 offset:57936\n"
      "v_cvt_pk_f16_f32 v240, v198, v199\n"
      "v_cvt_pk_f16_f32 v241, v146, v147\n"
      "v_pk_mul_f32 v[200:201], v[198:199], v[202:203] op_sel_hi:[1,0]\n"
      "v_pk_mul_f32 v[148:149], v[146:147], v[150:151] op_sel_hi:[1,0]\n"
      "ds_write_addtid_b32 v240 offset:52768\n"
      "ds_write_addtid_b32 v241 offset:56032\n"
      "v_pk_fma_f32 v[200:201], v[198:199], v[202:203], v[200:201] op_sel:[1,1,0] op_sel_hi:[0,1,1] neg_hi:[0,1,0]\n"
      "v_pk_fma_f32 v[148:149], v[146:147], v[150:151], v[148:149] op_sel:[1,1,0] op_sel_hi:[0,1,1] neg_hi:[0,1,0]\n"
      "ds_write_addtid_b32 v206 offset:54944\n"
      "v_cvt_pk_f16_f32 v242, v200, v201\n"
      "v_cvt_pk_f16_f32 v243, v148, v149\n"
      "v_pk_mul_f32 v[198:199], v[200:201], v[202:203] op_sel_hi:[1,0]\n"
      "v_pk_mul_f32 v[146:147], v[148:149], v[150:151] op_sel_hi:[1,0]\n"
      "ds_write_addtid_b32 v242 offset:53040\n"
      "ds_write_addtid_b32 v243 offset:56304\n"
      "v_pk_fma_f32 v[198:199], v[200:201], v[202:203], v[198:199] op_sel:[1,1,0] op_sel_hi:[0,1,1] neg_hi:[0,1,0]\n"
      "v_pk_fma_f32 v[146:147], v[148:149], v[150:151], v[146:147] op_sel:[1,1,0] op_sel_hi:[0,1,1] neg_hi:[0,1,0]\n"
      "ds_write_addtid_b32 v154 offset:58208\n"
      "v_cvt_pk_f16_f32 v240, v198, v199\n"
      "v_cvt_pk_f16_f32 v241, v146, v147\n"
      "v_pk_mul_f32 v[200:201], v[198:199], v[202:203] op_sel_hi:[1,0]\n"
      "v_pk_mul_f32 v[148:149], v[146:147], v[150:151] op_sel_hi:[1,0]\n"
      "ds_write_addtid_b32 v240 offset:53312\n"
      "ds_write_addtid_b32 v241 offset:56576\n"
      "v_pk_fma_f32 v[200:201], v[198:199], v[202:203], v[200:201] op_sel:[1,1,0] op_sel_hi:[0,1,1] neg_hi:[0,1,0]\n"
      "v_pk_fma_f32 v[148:149], v[146:147], v[150:151], v[148:149] op_sel:[1,1,0] op_sel_hi:[0,1,1] neg_hi:[0,1,0]\n"
      "ds_write_addtid_b32 v207 offset:55216\n"
      "v_cvt_pk_f16_f32 v242, v200, v201\n"
      "v_cvt_pk_f16_f32 v243, v148, v149\n"
      "v_pk_mul_f32 v[198:199], v[200:201], v[202:203] op_sel_hi:[1,0]\n"
      "v_pk_mul_f32 v[146:147], v[148:149], v[150:151] op_sel_hi:[1,0]\n"
      "ds_write_addtid_b32 v242 offset:53584\n"
      "ds_write_addtid_b32 v243 offset:56848\n"
      "v_pk_fma_f32 v[198:199], v[200:201], v[202:203], v[198:199] op_sel:[1,1,0] op_sel_hi:[0,1,1] neg_hi:[0,1,0]\n"
      "v_pk_fma_f32 v[146:147], v[148:149], v[150:151], v[146:147] op_sel:[1,1,0] op_sel_hi:[0,1,1] neg_hi:[0,1,0]\n"
      "ds_write_addtid_b32 v155 offset:58480\n"
      "v_cvt_pk_f16_f32 v240, v198, v199\n"
      "v_cvt_pk_f16_f32 v241, v146, v147\n"
      "v_pk_mul_f32 v[200:201], v[198:199], v[202:203] op_sel_hi:[1,0]\n"
      "v_pk_mul_f32 v[148:149], v[146:147], v[150:151] op_sel_hi:[1,0]\n"
      "ds_write_addtid_b32 v240 offset:53856\n"
      "ds_write_addtid_b32 v241 offset:57120\n"
      "v_pk_fma_f32 v[200:201], v[198:199], v[202:203], v[200:201] op_sel:[1,1,0] op_sel_hi:[0,1,1] neg_hi:[0,1,0]\n"
      "v_pk_fma_f32 v[148:149], v[146:147], v[150:151], v[148:149] op_sel:[1,1,0] op_sel_hi:[0,1,1] neg_hi:[0,1,0]\n"
      "v_cvt_pk_f16_f32 v242, v200, v201\n"
      "v_cvt_pk_f16_f32 v243, v148, v149\n"
      "ds_write_addtid_b32 v242 offset:54128\n"
      "ds_write_addtid_b32 v243 offset:57392\n"
      "ds_read_b128 v[104:107], v20 offset:0\n"
      "ds_read_b128 v[108:111], v178 offset:0\n"
      "ds_read_b128 v[112:115], v20 offset:32\n"
      "ds_read_b128 v[116:119], v178 offset:32\n"
      "s_mov_b32 m0, s39\n"
      "s_setprio 2\n"
      "v_pk_mul_f16 v240, v209, v208 op_sel_hi:[0,1]\n"
      "v_pk_mul_f16 v241, v210, v208 op_sel_hi:[0,1]\n"
      "v_pk_mul_f16 v242, v211, v208 op_sel_hi:[0,1]\n"
      "ds_write_addtid_b32 v208 offset:272\n"
      "v_pk_fma_f16 v240, v209, v208, v240 op_sel:[1,1,0] op_sel_hi:[1,0,1] neg_lo:[0,1,0]\n"
      "ds_write_addtid_b32 v209 offset:544\n"
      "v_pk_fma_f16 v241, v210, v208, v241 op_sel:[1,1,0] op_sel_hi:[1,0,1] neg_lo:[0,1,0]\n"
      "ds_write_addtid_b32 v240 offset:816\n"
      "v_pk_fma_f16 v242, v211, v208, v242 op_sel:[1,1,0] op_sel_hi:[1,0,1] neg_lo:[0,1,0]\n"
      "ds_write_addtid_b32 v241 offset:1360\n"
      "ds_write_addtid_b32 v242 offset:1904\n"
      "v_pk_mul_f16 v243, v212, v209 op_sel_hi:[0,1]\n"
      "v_pk_mul_f16 v244, v212, v210 op_sel_hi:[0,1]\n"
      "v_pk_mul_f16 v245, v212, v211 op_sel_hi:[0,1]\n"
      "ds_write_addtid_b32 v210 offset:1088\n"
      "v_pk_fma_f16 v243, v212, v209, v243 op_sel:[1,1,0] op_sel_hi:[1,0,1] neg_lo:[0,1,0]\n"
      "ds_write_addtid_b32 v211 offset:1632\n"
      "v_pk_fma_f16 v244, v212, v210, v244 op_sel:[1,1,0] op_sel_hi:[1,0,1] neg_lo:[0,1,0]\n"
      "ds_write_addtid_b32 v243 offset:2992\n"
      "v_pk_fma_f16 v245, v212, v211, v245 op_sel:[1,1,0] op_sel_hi:[1,0,1] neg_lo:[0,1,0]\n"
      "ds_write_addtid_b32 v244 offset:3264\n"
      "ds_write_addtid_b32 v245 offset:4080\n"
      "v_pk_mul_f16 v246, v213, v210 op_sel_hi:[0,1]\n"
      "v_pk_mul_f16 v247, v214, v210 op_sel_hi:[0,1]\n"
      "v_pk_mul_f16 v248, v215, v215 op_sel_hi:[0,1]\n"
      "ds_write_addtid_b32 v212 offset:2176\n"
      "v_pk_fma_f16 v246, v213, v210, v246 op_sel:[1,1,0] op_sel_hi:[1,0,1] neg_lo:[0,1,0]\n"
      "ds_write_addtid_b32 v213 offset:5440\n"
      "v_pk_fma_f16 v247, v214, v210, v247 op_sel:[1,1,0] op_sel_hi:[1,0,1] neg_lo:[0,1,0]\n"
      "ds_write_addtid_b32 v246 offset:5984\n"
      "v_pk_fma_f16 v248, v215, v215, v248 op_sel:[1,1,0] op_sel_hi:[1,0,1] neg_lo:[0,1,0]\n"
      "ds_write_addtid_b32 v247 offset:6256\n"
      "ds_write_addtid_b32 v248 offset:2448\n"
      "v_pk_mul_f16 v249, v215, v212 op_sel_hi:[0,1]\n"
      "v_pk_mul_f16 v250, v215, v213 op_sel_hi:[0,1]\n"
      "v_pk_mul_f16 v251, v215, v214 op_sel_hi:[0,1]\n"
      "ds_write_addtid_b32 v214 offset:5712\n"
      "v_pk_fma_f16 v249, v215, v212, v249 op_sel:[1,1,0] op_sel_hi:[1,0,1] neg_lo:[0,1,0]\n"
      "ds_write_addtid_b32 v215 offset:4352\n"
      "v_pk_fma_f16 v250, v215, v213, v250 op_sel:[1,1,0] op_sel_hi:[1,0,1] neg_lo:[0,1,0]\n"
      "ds_write_addtid_b32 v249 offset:4624\n"
      "v_pk_fma_f16 v251, v215, v214, v251 op_sel:[1,1,0] op_sel_hi:[1,0,1] neg_lo:[0,1,0]\n"
      "ds_write_addtid_b32 v250 offset:4896\n"
      "ds_write_addtid_b32 v251 offset:5168\n"
      "ds_read_b128 v[72:75], v21 offset:0\n"
      "ds_read_b128 v[76:79], v21 offset:32\n"
      "ds_read_b128 v[80:83], v21 offset:64\n"
      "ds_read_b128 v[84:87], v21 offset:96\n"
      "ds_read_b128 v[88:91], v21 offset:128\n"
      "ds_read_b128 v[92:95], v21 offset:160\n"
      "ds_read_b128 v[96:99], v21 offset:192\n"
      "ds_read_b128 v[100:103], v21 offset:224\n"
      "ds_read_b128 v[120:123], v20 offset:64\n"
      "ds_read_b128 v[124:127], v178 offset:64\n"
      "s_waitcnt lgkmcnt(15)\n"
      "v_pk_mul_f16 v164, v104, v108 op_sel_hi:[0,1] neg_hi:[0,1]\n"
      "v_pk_mul_f16 v165, v105, v109 op_sel_hi:[0,1] neg_hi:[0,1]\n"
      "v_pk_mul_f16 v166, v106, v110 op_sel_hi:[0,1] neg_hi:[0,1]\n"
      "v_pk_mul_f16 v167, v107, v111 op_sel_hi:[0,1] neg_hi:[0,1]\n"
      "v_pk_fma_f16 v164, v104, v108, v164 op_sel:[1,1,0] op_sel_hi:[1,0,1]\n"
      "v_pk_fma_f16 v165, v105, v109, v165 op_sel:[1,1,0] op_sel_hi:[1,0,1]\n"
      "v_pk_fma_f16 v166, v106, v110, v166 op_sel:[1,1,0] op_sel_hi:[1,0,1]\n"
      "v_pk_fma_f16 v167, v107, v111, v167 op_sel:[1,1,0] op_sel_hi:[1,0,1]\n"
      "s_waitcnt lgkmcnt(9)\n"
      "s_nop 0\n"
      "v_mfma_f32_32x32x16_f16 v[180:195], v[72:75], v[164:167], 0\n"
      "v_pk_mul_f16 v240, v157, v156 op_sel_hi:[0,1]\n"
      "v_pk_mul_f16 v241, v158, v156 op_sel_hi:[0,1]\n"
      "v_pk_mul_f16 v242, v159, v156 op_sel_hi:[0,1]\n"
      "v_pk_mul_f16 v168, v112, v116 op_sel_hi:[0,1] neg_hi:[0,1]\n"
      "v_pk_mul_f16 v169, v113, v117 op_sel_hi:[0,1] neg_hi:[0,1]\n"
      "v_pk_mul_f16 v170, v114, v118 op_sel_hi:[0,1] neg_hi:[0,1]\n"
      "v_pk_mul_f16 v171, v115, v119 op_sel_hi:[0,1] neg_hi:[0,1]\n"
      "ds_write_addtid_b32 v156 offset:272\n"
      "v_pk_fma_f16 v240, v157, v156, v240 op_sel:[1,1,0] op_sel_hi:[1,0,1] neg_lo:[0,1,0]\n"
      "ds_write_addtid_b32 v157 offset:544\n"
      "v_pk_fma_f16 v168, v112, v116, v168 op_sel:[1,1,0] op_sel_hi:[1,0,1]\n"
      "v_pk_fma_f16 v169, v113, v117, v169 op_sel:[1,1,0] op_sel_hi:[1,0,1]\n"
      "v_pk_fma_f16 v170, v114, v118, v170 op_sel:[1,1,0] op_sel_hi:[1,0,1]\n"
      "v_pk_fma_f16 v171, v115, v119, v171 op_sel:[1,1,0] op_sel_hi:[1,0,1]\n"
      "ds_read_b128 v[104:107], v20 offset:96\n"
      "ds_read_b128 v[108:111], v178 offset:96\n"
      "s_waitcnt lgkmcnt(12)\n"
      "v_mfma_f32_32x32x16_f16 v[180:195], v[76:79], v[168:171], v[180:195]\n"
      "v_pk_fma_f16 v241, v158, v156, v241 op_sel:[1,1,0] op_sel_hi:[1,0,1] neg_lo:[0,1,0]\n"
      "ds_write_addtid_b32 v240 offset:816\n"
      "v_pk_fma_f16 v242, v159, v156, v242 op_sel:[1,1,0] op_sel_hi:[1,0,1] neg_lo:[0,1,0]\n"
      "s_waitcnt lgkmcnt(5)\n"
      "v_pk_mul_f16 v164, v120, v124 op_sel_hi:[0,1] neg_hi:[0,1]\n"
      "v_pk_mul_f16 v165, v121, v125 op_sel_hi:[0,1] neg_hi:[0,1]\n"
      "v_pk_mul_f16 v166, v122, v126 op_sel_hi:[0,1] neg_hi:[0,1]\n"
      "v_pk_mul_f16 v167, v123, v127 op_sel_hi:[0,1] neg_hi:[0,1]\n"
      "ds_write_addtid_b32 v241 offset:1360\n"
      "ds_write_addtid_b32 v242 offset:1904\n"
      "v_pk_mul_f16 v243, v160, v157 op_sel_hi:[0,1]\n"
      "v_pk_fma_f16 v164, v120, v124, v164 op_sel:[1,1,0] op_sel_hi:[1,0,1]\n"
      "v_pk_fma_f16 v165, v121, v125, v165 op_sel:[1,1,0] op_sel_hi:[1,0,1]\n"
      "v_pk_fma_f16 v166, v122, v126, v166 op_sel:[1,1,0] op_sel_hi:[1,0,1]\n"
      "v_pk_fma_f16 v167, v123, v127, v167 op_sel:[1,1,0] op_sel_hi:[1,0,1]\n"
      "ds_read_b128 v[112:115], v20 offset:128\n"
      "ds_read_b128 v[116:119], v178 offset:128\n"
      "v_mfma_f32_32x32x16_f16 v[180:195], v[80:83], v[164:167], v[180:195]\n"
      "v_pk_mul_f16 v244, v160, v158 op_sel_hi:[0,1]\n"
      "v_pk_mul_f16 v245, v160, v159 op_sel_hi:[0,1]\n"
      "ds_write_addtid_b32 v158 offset:1088\n"
      "s_waitcnt lgkmcnt(6)\n"
      "v_pk_mul_f16 v168, v104, v108 op_sel_hi:[0,1] neg_hi:[0,1]\n"
      "v_pk_mul_f16 v169, v105, v109 op_sel_hi:[0,1] neg_hi:[0,1]\n"
      "v_pk_mul_f16 v170, v106, v110 op_sel_hi:[0,1] neg_hi:[0,1]\n"
      "v_pk_mul_f16 v171, v107, v111 op_sel_hi:[0,1] neg_hi:[0,1]\n"
      "v_pk_fma_f16 v243, v160, v157, v243 op_sel:[1,1,0] op_sel_hi:[1,0,1] neg_lo:[0,1,0]\n"
      "ds_write_addtid_b32 v159 offset:1632\n"
      "v_pk_fma_f16 v244, v160, v158, v244 op_sel:[1,1,0] op_sel_hi:[1,0,1] neg_lo:[0,1,0]\n"
      "v_pk_fma_f16 v168, v104, v108, v168 op_sel:[1,1,0] op_sel_hi:[1,0,1]\n"
      "v_pk_fma_f16 v169, v105, v109, v169 op_sel:[1,1,0] op_sel_hi:[1,0,1]\n"
      "v_pk_fma_f16 v170, v106, v110, v170 op_sel:[1,1,0] op_sel_hi:[1,0,1]\n"
      "v_pk_fma_f16 v171, v107, v111, v171 op_sel:[1,1,0] op_sel_hi:[1,0,1]\n"
      "ds_read_b128 v[120:123], v20 offset:160\n"
      "ds_read_b128 v[124:127], v178 offset:160\n"
      "buffer_load_dwordx4 v[48:51], v41, s[20:23], s60 offen nt\n"
      "v_mfma_f32_32x32x16_f16 v[180:195], v[84:87], v[168:171], v[180:195]\n"
      "ds_write_addtid_b32 v243 offset:2992\n"
      "v_pk_fma_f16 v245, v160, v159, v245 op_sel:[1,1,0] op_sel_hi:[1,0,1] neg_lo:[0,1,0]\n"
      "ds_write_addtid_b32 v244 offset:3264\n"
      "s_waitcnt lgkmcnt(6)\n"
      "v_pk_mul_f16 v164, v112, v116 op_sel_hi:[0,1] neg_hi:[0,1]\n"
      "v_pk_mul_f16 v165, v113, v117 op_sel_hi:[0,1] neg_hi:[0,1]\n"
      "v_pk_mul_f16 v166, v114, v118 op_sel_hi:[0,1] neg_hi:[0,1]\n"
      "v_pk_mul_f16 v167, v115, v119 op_sel_hi:[0,1] neg_hi:[0,1]\n"
      "ds_write_addtid_b32 v245 offset:4080\n"
      "v_pk_mul_f16 v246, v161, v158 op_sel_hi:[0,1]\n"
      "v_pk_mul_f16 v247, v162, v158 op_sel_hi:[0,1]\n"
      "v_pk_fma_f16 v164, v112, v116, v164 op_sel:[1,1,0] op_sel_hi:[1,0,1]\n"
      "v_pk_fma_f16 v165, v113, v117, v165 op_sel:[1,1,0] op_sel_hi:[1,0,1]\n"
      "v_pk_fma_f16 v166, v114, v118, v166 op_sel:[1,1,0] op_sel_hi:[1,0,1]\n"
      "v_pk_fma_f16 v167, v115, v119, v167 op_sel:[1,1,0] op_sel_hi:[1,0,1]\n"
      "ds_read_b128 v[104:107], v20 offset:192\n"
      "ds_read_b128 v[108:111], v178 offset:192\n"
      "v_mfma_f32_32x32x16_f16 v[180:195], v[88:91], v[164:167], v[180:195]\n"
      "v_pk_mul_f16 v248, v163, v163 op_sel_hi:[0,1]\n"
      "ds_write_addtid_b32 v160 offset:2176\n"
      "v_pk_fma_f16 v246, v161, v158, v246 op_sel:[1,1,0] op_sel_hi:[1,0,1] neg_lo:[0,1,0]\n"
      "s_waitcnt lgkmcnt(6)\n"
      "v_pk_mul_f16 v168, v120, v124 op_sel_hi:[0,1] neg_hi:[0,1]\n"
      "v_pk_mul_f16 v169, v121, v125 op_sel_hi:[0,1] neg_hi:[0,1]\n"
      "v_pk_mul_f16 v170, v122, v126 op_sel_hi:[0,1] neg_hi:[0,1]\n"
      "v_pk_mul_f16 v171, v123, v127 op_sel_hi:[0,1] neg_hi:[0,1]\n"
      "ds_write_addtid_b32 v161 offset:5440\n"
      "v_pk_fma_f16 v247, v162, v158, v247 op_sel:[1,1,0] op_sel_hi:[1,0,1] neg_lo:[0,1,0]\n"
      "ds_write_addtid_b32 v246 offset:5984\n"
      "v_pk_fma_f16 v168, v120, v124, v168 op_sel:[1,1,0] op_sel_hi:[1,0,1]\n"
      "v_pk_fma_f16 v169, v121, v125, v169 op_sel:[1,1,0] op_sel_hi:[1,0,1]\n"
      "v_pk_fma_f16 v170, v122, v126, v170 op_sel:[1,1,0] op_sel_hi:[1,0,1]\n"
      "v_pk_fma_f16 v171, v123, v127, v171 op_sel:[1,1,0] op_sel_hi:[1,0,1]\n"
      "ds_read_b128 v[112:115], v20 offset:224\n"
      "ds_read_b128 v[116:119], v178 offset:224\n"
      "v_mfma_f32_32x32x16_f16 v[180:195], v[92:95], v[168:171], v[180:195]\n"
      "v_pk_fma_f16 v248, v163, v163, v248 op_sel:[1,1,0] op_sel_hi:[1,0,1] neg_lo:[0,1,0]\n"
      "ds_write_addtid_b32 v247 offset:6256\n"
      "ds_write_addtid_b32 v248 offset:2448\n"
      "s_waitcnt lgkmcnt(7)\n"
      "v_pk_mul_f16 v164, v104, v108 op_sel_hi:[0,1] neg_hi:[0,1]\n"
      "v_pk_mul_f16 v165, v105, v109 op_sel_hi:[0,1] neg_hi:[0,1]\n"
      "v_pk_mul_f16 v166, v106, v110 op_sel_hi:[0,1] neg_hi:[0,1]\n"
      "v_pk_mul_f16 v167, v107, v111 op_sel_hi:[0,1] neg_hi:[0,1]\n"
      "v_pk_mul_f16 v249, v163, v160 op_sel_hi:[0,1]\n"
      "v_pk_mul_f16 v250, v163, v161 op_sel_hi:[0,1]\n"
      "v_pk_mul_f16 v251, v163, v162 op_sel_hi:[0,1]\n"
      "v_pk_fma_f16 v164, v104, v108, v164 op_sel:[1,1,0] op_sel_hi:[1,0,1]\n"
      "v_pk_fma_f16 v165, v105, v109, v165 op_sel:[1,1,0] op_sel_hi:[1,0,1]\n"
      "v_pk_fma_f16 v166, v106, v110, v166 op_sel:[1,1,0] op_sel_hi:[1,0,1]\n"
      "v_pk_fma_f16 v167, v107, v111, v167 op_sel:[1,1,0] op_sel_hi:[1,0,1]\n"
      "ds_read_b128 v[120:123], v20 offset:3264\n"
      "ds_read_b128 v[124:127], v178 offset:3264\n"
      "v_mfma_f32_32x32x16_f16 v[180:195], v[96:99], v[164:167], v[180:195]\n"
      "ds_write_addtid_b32 v162 offset:5712\n"
      "v_pk_fma_f16 v249, v163, v160, v249 op_sel:[1,1,0] op_sel_hi:[1,0,1] neg_lo:[0,1,0]\n"
      "ds_write_addtid_b32 v163 offset:4352\n"
      "s_waitcnt lgkmcnt(6)\n"
      "v_pk_mul_f16 v168, v112, v116 op_sel_hi:[0,1] neg_hi:[0,1]\n"
      "v_pk_mul_f16 v169, v113, v117 op_sel_hi:[0,1] neg_hi:[0,1]\n"
      "v_pk_mul_f16 v170, v114, v118 op_sel_hi:[0,1] neg_hi:[0,1]\n"
      "v_pk_mul_f16 v171, v115, v119 op_sel_hi:[0,1] neg_hi:[0,1]\n"
      "v_pk_fma_f16 v250, v163, v161, v250 op_sel:[1,1,0] op_sel_hi:[1,0,1] neg_lo:[0,1,0]\n"
      "ds_write_addtid_b32 v249 offset:4624\n"
      "v_pk_fma_f16 v251, v163, v162, v251 op_sel:[1,1,0] op_sel_hi:[1,0,1] neg_lo:[0,1,0]\n"
      "v_pk_fma_f16 v168, v112, v116, v168 op_sel:[1,1,0] op_sel_hi:[1,0,1]\n"
      "v_pk_fma_f16 v169, v113, v117, v169 op_sel:[1,1,0] op_sel_hi:[1,0,1]\n"
      "v_pk_fma_f16 v170, v114, v118, v170 op_sel:[1,1,0] op_sel_hi:[1,0,1]\n"
      "v_pk_fma_f16 v171, v115, v119, v171 op_sel:[1,1,0] op_sel_hi:[1,0,1]\n"
      "ds_read_b128 v[104:107], v20 offset:3296\n"
      "ds_read_b128 v[108:111], v178 offset:3296\n"
      "v_mfma_f32_32x32x16_f16 v[180:195], v[100:103], v[168:171], v[180:195]\n"
      "ds_write_addtid_b32 v250 offset:4896\n"
      "ds_write_addtid_b32 v251 offset:5168\n"
      "s_waitcnt lgkmcnt(7)\n"
      "v_pk_mul_f16 v164, v120, v124 op_sel_hi:[0,1] neg_hi:[0,1]\n"
      "v_pk_mul_f16 v165, v121, v125 op_sel_hi:[0,1] neg_hi:[0,1]\n"
      "v_pk_mul_f16 v166, v122, v126 op_sel_hi:[0,1] neg_hi:[0,1]\n"
      "v_pk_mul_f16 v167, v123, v127 op_sel_hi:[0,1] neg_hi:[0,1]\n"
      "ds_read_b128 v[72:75], v21 offset:0\n"
      "ds_read_b128 v[76:79], v21 offset:32\n"
      "ds_read_b128 v[80:83], v21 offset:64\n"
      "ds_read_b128 v[84:87], v21 offset:96\n"
      "ds_read_b128 v[88:91], v21 offset:128\n"
      "ds_read_b128 v[92:95], v21 offset:160\n"
      "ds_read_b128 v[96:99], v21 offset:192\n"
      "ds_read_b128 v[100:103], v21 offset:224\n"
      "v_pk_fma_f16 v164, v120, v124, v164 op_sel:[1,1,0] op_sel_hi:[1,0,1]\n"
      "v_pk_fma_f16 v165, v121, v125, v165 op_sel:[1,1,0] op_sel_hi:[1,0,1]\n"
      "v_pk_fma_f16 v166, v122, v126, v166 op_sel:[1,1,0] op_sel_hi:[1,0,1]\n"
      "v_pk_fma_f16 v167, v123, v127, v167 op_sel:[1,1,0] op_sel_hi:[1,0,1]\n"
      "ds_read_b128 v[112:115], v20 offset:3328\n"
      "ds_read_b128 v[116:119], v178 offset:3328\n"
      "s_setprio 1\n"
      "s_waitcnt lgkmcnt(9)\n"
      "v_mfma_f32_32x32x16_f16 v[180:195], v[72:75], v[164:167], v[180:195]\n"
      "v_pk_mul_f16 v168, v104, v108 op_sel_hi:[0,1] neg_hi:[0,1]\n"
      "v_pk_mul_f16 v169, v105, v109 op_sel_hi:[0,1] neg_hi:[0,1]\n"
      "v_pk_mul_f16 v170, v106, v110 op_sel_hi:[0,1] neg_hi:[0,1]\n"
      "v_pk_mul_f16 v171, v107, v111 op_sel_hi:[0,1] neg_hi:[0,1]\n"
      "v_pk_fma_f16 v168, v104, v108, v168 op_sel:[1,1,0] op_sel_hi:[1,0,1]\n"
      "v_pk_fma_f16 v169, v105, v109, v169 op_sel:[1,1,0] op_sel_hi:[1,0,1]\n"
      "v_pk_fma_f16 v170, v106, v110, v170 op_sel:[1,1,0] op_sel_hi:[1,0,1]\n"
      "v_pk_fma_f16 v171, v107, v111, v171 op_sel:[1,1,0] op_sel_hi:[1,0,1]\n"
      "ds_read_b128 v[120:123], v20 offset:3360\n"
      "ds_read_b128 v[124:127], v178 offset:3360\n"
      "buffer_load_dwordx4 v[52:55], v41, s[20:23], s60 offen offset:2048 nt\n"
      "s_waitcnt lgkmcnt(10)\n"
      "v_mfma_f32_32x32x16_f16 v[180:195], v[76:79], v[168:171], v[180:195]\n"
      "s_waitcnt lgkmcnt(2)\n"
      "v_pk_mul_f16 v164, v112, v116 op_sel_hi:[0,1] neg_hi:[0,1]\n"
      "v_pk_mul_f16 v165, v113, v117 op_sel_hi:[0,1] neg_hi:[0,1]\n"
      "v_pk_mul_f16 v166, v114, v118 op_sel_hi:[0,1] neg_hi:[0,1]\n"
      "v_pk_mul_f16 v167, v115, v119 op_sel_hi:[0,1] neg_hi:[0,1]\n"
      "v_pk_fma_f16 v164, v112, v116, v164 op_sel:[1,1,0] op_sel_hi:[1,0,1]\n"
      "v_pk_fma_f16 v165, v113, v117, v165 op_sel:[1,1,0] op_sel_hi:[1,0,1]\n"
      "v_pk_fma_f16 v166, v114, v118, v166 op_sel:[1,1,0] op_sel_hi:[1,0,1]\n"
      "v_pk_fma_f16 v167, v115, v119, v167 op_sel:[1,1,0] op_sel_hi:[1,0,1]\n"
      "ds_read_b128 v[104:107], v20 offset:3392\n"
      "ds_read_b128 v[108:111], v178 offset:3392\n"
      "v_mfma_f32_32x32x16_f16 v[180:195], v[80:83], v[164:167], v[180:195]\n"
      "s_waitcnt lgkmcnt(2)\n"
      "v_pk_mul_f16 v168, v120, v124 op_sel_hi:[0,1] neg_hi:[0,1]\n"
      "v_pk_mul_f16 v169, v121, v125 op_sel_hi:[0,1] neg_hi:[0,1]\n"
      "v_pk_mul_f16 v170, v122, v126 op_sel_hi:[0,1] neg_hi:[0,1]\n"
      "v_pk_mul_f16 v171, v123, v127 op_sel_hi:[0,1] neg_hi:[0,1]\n"
      "v_pk_fma_f16 v168, v120, v124, v168 op_sel:[1,1,0] op_sel_hi:[1,0,1]\n"
      "v_pk_fma_f16 v169, v121, v125, v169 op_sel:[1,1,0] op_sel_hi:[1,0,1]\n"
      "v_pk_fma_f16 v170, v122, v126, v170 op_sel:[1,1,0] op_sel_hi:[1,0,1]\n"
      "v_pk_fma_f16 v171, v123, v127, v171 op_sel:[1,1,0] op_sel_hi:[1,0,1]\n"
      "ds_read_b128 v[112:115], v20 offset:3424\n"
      "ds_read_b128 v[116:119], v178 offset:3424\n"
      "v_mfma_f32_32x32x16_f16 v[180:195], v[84:87], v[168:171], v[180:195]\n"
      "s_waitcnt lgkmcnt(2)\n"
      "v_pk_mul_f16 v164, v104, v108 op_sel_hi:[0,1] neg_hi:[0,1]\n"
      "v_pk_mul_f16 v165, v105, v109 op_sel_hi:[0,1] neg_hi:[0,1]\n"
      "v_pk_mul_f16 v166, v106, v110 op_sel_hi:[0,1] neg_hi:[0,1]\n"
      "v_pk_mul_f16 v167, v107, v111 op_sel_hi:[0,1] neg_hi:[0,1]\n"
      "v_pk_fma_f16 v164, v104, v108, v164 op_sel:[1,1,0] op_sel_hi:[1,0,1]\n"
      "v_pk_fma_f16 v165, v105, v109, v165 op_sel:[1,1,0] op_sel_hi:[1,0,1]\n"
      "v_pk_fma_f16 v166, v106, v110, v166 op_sel:[1,1,0] op_sel_hi:[1,0,1]\n"
      "v_pk_fma_f16 v167, v107, v111, v167 op_sel:[1,1,0] op_sel_hi:[1,0,1]\n"
      "ds_read_b128 v[120:123], v20 offset:3456\n"
      "ds_read_b128 v[124:127], v178 offset:3456\n"
      "v_mfma_f32_32x32x16_f16 v[180:195], v[88:91], v[164:167], v[180:195]\n"
      "s_waitcnt lgkmcnt(2)\n"
      "v_pk_mul_f16 v168, v112, v116 op_sel_hi:[0,1] neg_hi:[0,1]\n"
      "v_pk_mul_f16 v169, v113, v117 op_sel_hi:[0,1] neg_hi:[0,1]\n"
      "v_pk_mul_f16 v170, v114, v118 op_sel_hi:[0,1] neg_hi:[0,1]\n"
      "v_pk_mul_f16 v171, v115, v119 op_sel_hi:[0,1] neg_hi:[0,1]\n"
      "v_pk_fma_f16 v168, v112, v116, v168 op_sel:[1,1,0] op_sel_hi:[1,0,1]\n"
      "v_pk_fma_f16 v169, v113, v117, v169 op_sel:[1,1,0] op_sel_hi:[1,0,1]\n"
      "v_pk_fma_f16 v170, v114, v118, v170 op_sel:[1,1,0] op_sel_hi:[1,0,1]\n"
      "v_pk_fma_f16 v171, v115, v119, v171 op_sel:[1,1,0] op_sel_hi:[1,0,1]\n"
      "ds_read_b128 v[104:107], v20 offset:3488\n"
      "ds_read_b128 v[108:111], v178 offset:3488\n"
      "v_mov_b32 v140, 0\n"
      "v_mov_b32 v141, 0\n"
      "v_mov_b32 v142, 0\n"
      "v_mov_b32 v143, 0\n"
      "ds_write_b128 v27, v[140:143] \n"
      "ds_write_b32 v28, v140 \n"
      "v_mfma_f32_32x32x16_f16 v[180:195], v[92:95], v[168:171], v[180:195]\n"
      "s_waitcnt lgkmcnt(2)\n"
      "v_pk_mul_f16 v164, v120, v124 op_sel_hi:[0,1] neg_hi:[0,1]\n"
      "v_pk_mul_f16 v165, v121, v125 op_sel_hi:[0,1] neg_hi:[0,1]\n"
      "v_pk_mul_f16 v166, v122, v126 op_sel_hi:[0,1] neg_hi:[0,1]\n"
      "v_pk_mul_f16 v167, v123, v127 op_sel_hi:[0,1] neg_hi:[0,1]\n"
      "v_pk_fma_f16 v164, v120, v124, v164 op_sel:[1,1,0] op_sel_hi:[1,0,1]\n"
      "v_pk_fma_f16 v165, v121, v125, v165 op_sel:[1,1,0] op_sel_hi:[1,0,1]\n"
      "v_pk_fma_f16 v166, v122, v126, v166 op_sel:[1,1,0] op_sel_hi:[1,0,1]\n"
      "v_pk_fma_f16 v167, v123, v127, v167 op_sel:[1,1,0] op_sel_hi:[1,0,1]\n"
      "s_nop 1\n"
      "v_mfma_f32_32x32x16_f16 v[180:195], v[96:99], v[164:167], v[180:195]\n"
      "s_waitcnt lgkmcnt(0)\n"
      "v_pk_mul_f16 v168, v104, v108 op_sel_hi:[0,1] neg_hi:[0,1]\n"
      "v_pk_mul_f16 v169, v105, v109 op_sel_hi:[0,1] neg_hi:[0,1]\n"
      "v_pk_mul_f16 v170, v106, v110 op_sel_hi:[0,1] neg_hi:[0,1]\n"
      "v_pk_mul_f16 v171, v107, v111 op_sel_hi:[0,1] neg_hi:[0,1]\n"
      "v_pk_fma_f16 v168, v104, v108, v168 op_sel:[1,1,0] op_sel_hi:[1,0,1]\n"
      "v_pk_fma_f16 v169, v105, v109, v169 op_sel:[1,1,0] op_sel_hi:[1,0,1]\n"
      "v_pk_fma_f16 v170, v106, v110, v170 op_sel:[1,1,0] op_sel_hi:[1,0,1]\n"
      "v_pk_fma_f16 v171, v107, v111, v171 op_sel:[1,1,0] op_sel_hi:[1,0,1]\n"
      "s_nop 1\n"
      "v_mfma_f32_32x32x16_f16 v[180:195], v[100:103], v[168:171], v[180:195]\n"
      "s_cmp_lt_u32 s33, 4\n"
      "s_cbranch_scc1 .Lct2%=\n"
      "buffer_load_dword v11, v15, s[20:23], s60 offen\n"
      ".Lct2%=:\n"
      "s_nop 10\n"
      "v_add_f32 v180, v180, v31\n"
      "buffer_load_dwordx4 v[64:67], v42, s[16:19], s62 offen sc1\n"
      "v_cvt_pk_f16_f32 v222, v187, v186\n"
      "v_cvt_pk_f16_f32 v223, v185, v184\n"
      "v_cvt_pk_f16_f32 v225, v186, v185\n"
      "v_cvt_pk_f16_f32 v226, v188, v189\n"
      "ds_write_b64 v22, v[222:223] offset:2110\n"
      "v_cvt_pk_f16_f32 v228, v192, v192\n"
      "ds_write_b16 v22, v222 \n"
      "v_cvt_pk_f16_f32 v220, v183, v182\n"
      "ds_write_b32 v22, v225 offset:2\n"
      "v_cvt_pk_f16_f32 v221, v181, v180\n"
      "ds_write_b16_d16_hi v22, v223 offset:6\n"
      "v_cvt_pk_f16_f32 v224, v182, v181\n"
      "v_readlane_b32 s64, v226, 0\n"
      "v_readlane_b32 s65, v226, 32\n"
      "v_readlane_b32 s66, v228, 0\n"
      "ds_write_b64 v22, v[220:221] offset:510\n"
      "v_cvt_pk_f16_f32 v227, v190, v191\n"
      "ds_write_b16 v22, v220 offset:2620\n"
      "v_mov_b32 v229, s65\n"
      "ds_write_b32 v22, v224 offset:2622\n"
      "v_writelane_b32 v229, s64, 8\n"
      "v_writelane_b32 v229, s64, 9\n"
      "ds_write_b16_d16_hi v22, v221 offset:2626\n"
      "v_writelane_b32 v229, s66, 10\n"
      "v_writelane_b32 v229, s66, 11\n"
      "s_pack_ll_b32_b16 s64, s64, s64\n"
      "ds_write_b16 v26, v229 \n"
      "v_add_u32 v230, 0x240, v29\n"
      "s_setprio 0\n"
      "v_and_b32 v44, s64, v44\n"
      "v_and_b32 v45, s64, v45\n"
      "v_and_b32 v46, s64, v46\n"
      "v_and_b32 v47, s64, v47\n"
      "ds_write_b16 v23, v226 \n"
      "ds_write_b16 v23, v226 offset:2110\n"
      "ds_write_b16_d16_hi v24, v226 \n"
      "ds_write_b16_d16_hi v24, v226 offset:2110\n"
      "ds_write_b16 v25, v227 offset:32\n"
      "ds_write_b16 v25, v227 offset:2142\n"
      "ds_write_b16_d16_hi v25, v227 \n"
      "ds_write_b16_d16_hi v25, v227 offset:2110\n"
      "ds_read2_b32 v[108:109], v230 offset0:0 offset1:1\n"
      "ds_read2_b32 v[110:111], v230 offset0:2 offset1:3\n"
      "ds_read2_b32 v[76:77], v29 offset0:16 offset1:17\n"
      "ds_read2_b32 v[78:79], v29 offset0:18 offset1:19\n"
      "ds_read2_b32 v[112:113], v230 offset0:16 offset1:17\n"
      "ds_read2_b32 v[114:115], v230 offset0:18 offset1:19\n"
      "ds_read2_b32 v[80:81], v29 offset0:32 offset1:33\n"
      "ds_read2_b32 v[82:83], v29 offset0:34 offset1:35\n"
      "ds_read2_b32 v[116:117], v230 offset0:32 offset1:33\n"
      "ds_read2_b32 v[118:119], v230 offset0:34 offset1:35\n"
      "ds_read2_b32 v[84:85], v29 offset0:48 offset1:49\n"
      "ds_read2_b32 v[86:87], v29 offset0:50 offset1:51\n"
      "s_movk_i32 s59, 0x7fff\n"
      "s_waitcnt vmcnt(0)\n"
      "s_nop 0\n"
      ".Lcsgk%=:\n"
      "v_cmp_ne_u32 s[64:65], s15, v65\n"
      "v_cmp_ne_u32 vcc, s15, v67\n"
      "s_nop 0\n"
      "s_or_b64 vcc, vcc, s[64:65]\n"
      "s_cbranch_scc1 .Lcsgsk%=\n"
      ".Lcsgok%=:\n"
      "v_and_b32 v196, 0xffff0000, v64\n"
      "v_lshlrev_b32 v198, 16, v64\n"
      "v_rcp_f32 v196, v196\n"
      "v_and_b32 v197, 0xffff0000, v66\n"
      "v_lshlrev_b32 v199, 16, v66\n"
      "v_rcp_f32 v197, v197\n"
      "v_mul_f32 v198, -v198, v196\n"
      "v_mul_f32 v199, -v199, v197\n"
      "ds_read2_b32 v[120:121], v230 offset0:48 offset1:49\n"
      "ds_read2_b32 v[122:123], v230 offset0:50 offset1:51\n"
      "v_lshlrev_b32 v204, 16, v48\n"
      "v_lshlrev_b32 v205, 16, v52\n"
      "v_and_b32 v206, 0xffff0000, v48\n"
      "v_and_b32 v207, 0xffff0000, v52\n"
      "v_pk_fma_f32 v[204:205], v[204:205], v[196:197], v[198:199]\n"
      "v_pk_fma_f32 v[206:207], v[206:207], v[196:197], v[198:199]\n"
      "v_cvt_pk_f16_f32 v204, v204, v205\n"
      "v_cvt_pk_f16_f32 v206, v206, v207\n"
      "ds_read2_b32 v[88:89], v29 offset0:64 offset1:65\n"
      "ds_read2_b32 v[90:91], v29 offset0:66 offset1:67\n"
      "ds_write2st64_b32 v32, v204, v206 offset0:0 offset1:19\n"
      "v_lshlrev_b32 v208, 16, v49\n"
      "v_lshlrev_b32 v209, 16, v53\n"
      "v_and_b32 v210, 0xffff0000, v49\n"
      "v_and_b32 v211, 0xffff0000, v53\n"
      "v_pk_fma_f32 v[208:209], v[208:209], v[196:197], v[198:199]\n"
      "v_pk_fma_f32 v[210:211], v[210:211], v[196:197], v[198:199]\n"
      "v_cvt_pk_f16_f32 v208, v208, v209\n"
      "ds_read2_b32 v[124:125], v230 offset0:64 offset1:65\n"
      "ds_read2_b32 v[126:127], v230 offset0:66 offset1:67\n"
      "v_cvt_pk_f16_f32 v210, v210, v211\n"
      "ds_write2st64_b32 v32, v208, v210 offset0:38 offset1:57\n"
      "v_lshlrev_b32 v204, 16, v50\n"
      "v_lshlrev_b32 v205, 16, v54\n"
      "v_and_b32 v206, 0xffff0000, v50\n"
      "v_and_b32 v207, 0xffff0000, v54\n"
      "v_pk_fma_f32 v[204:205], v[204:205], v[196:197], v[198:199]\n"
      "v_pk_fma_f32 v[206:207], v[206:207], v[196:197], v[198:199]\n"
      "ds_read2_b32 v[92:93], v29 offset0:80 offset1:81\n"
      "ds_read2_b32 v[94:95], v29 offset0:82 offset1:83\n"
      "v_cvt_pk_f16_f32 v204, v204, v205\n"
      "v_cvt_pk_f16_f32 v206, v206, v207\n"
      "ds_write2st64_b32 v32, v204, v206 offset0:76 offset1:95\n"
      "v_lshlrev_b32 v208, 16, v51\n"
      "v_lshlrev_b32 v209, 16, v55\n"
      "v_and_b32 v210, 0xffff0000, v51\n"
      "v_and_b32 v211, 0xffff0000, v55\n"
      "v_pk_fma_f32 v[208:209], v[208:209], v[196:197], v[198:199]\n"
      "ds_read2_b32 v[128:129], v230 offset0:80 offset1:81\n"
      "ds_read2_b32 v[130:131], v230 offset0:82 offset1:83\n"
      "v_pk_fma_f32 v[210:211], v[210:211], v[196:197], v[198:199]\n"
      "v_cvt_pk_f16_f32 v208, v208, v209\n"
      "v_cvt_pk_f16_f32 v210, v210, v211\n"
      "ds_write2st64_b32 v32, v208, v210 offset0:114 offset1:133\n"
      "s_mov_b32 s54, s41\n"
      "s_add_u32 s55, s41, 0x100000\n"
      "s_add_u32 s56, s41, 0x200000\n"
      "s_add_u32 s57, s41, 0x300000\n"
      "ds_read2_b32 v[96:97], v29 offset0:96 offset1:97\n"
      "ds_read2_b32 v[98:99], v29 offset0:98 offset1:99\n"
      "s_mov_b32 s58, 0x40000000\n"
      "v_lshrrev_b32 v14, 9, v8\n"
      "v_add_u32 v14, 0x27600, v14\n"
      "s_lshr_b32 s13, s33, 1\n"
      "ds_read2_b32 v[132:133], v230 offset0:96 offset1:97\n"
      "ds_read2_b32 v[134:135], v230 offset0:98 offset1:99\n"
      "ds_read2_b32 v[100:101], v29 offset0:112 offset1:113\n"
      "ds_read2_b32 v[102:103], v29 offset0:114 offset1:115\n"
      "ds_read2_b32 v[136:137], v230 offset0:112 offset1:113\n"
      "ds_read2_b32 v[138:139], v230 offset0:114 offset1:115\n"
      "ds_read2_b32 v[104:105], v29 offset0:128 offset1:129\n"
      "ds_read2_b32 v[106:107], v29 offset0:130 offset1:131\n"
      "ds_read2_b32 v[72:73], v29 offset0:0 offset1:1\n"
      "ds_read2_b32 v[74:75], v29 offset0:2 offset1:3\n"
      "s_waitcnt lgkmcnt(12)\n"
      ".Lccv%=:\n"
      "s_barrier\n"
      "s_setprio 3\n"
      "ds_read_b128 v[216:219], v33 offset:64\n"
      "ds_read_b128 v[220:223], v33 offset:128\n"
      "ds_read_b128 v[224:227], v33 offset:192\n"
      "ds_read_b128 v[212:215], v33 offset:256\n"
      "v_permlane32_swap_b32 v48, v52\n"
      "v_permlane32_swap_b32 v49, v53\n"
      "v_permlane32_swap_b32 v50, v54\n"
      "v_permlane32_swap_b32 v51, v55\n"
      "s_cmp_lg_u32 s33, 0\n"
      "s_cbranch_scc1 .Lca%=\n"
      "v_mov_b32 v16, s15\n"
      "v_mov_b32 v17, s15\n"
      "buffer_store_dwordx2 v[16:17], off, s[16:19], s42 sc1\n"
      ".Lca%=:\n"
      "s_waitcnt lgkmcnt(2)\n"
      "v_mfma_f32_16x16x32_f16 v[180:183], v[108:111], v[216:219], 0\n"
      "v_lshlrev_b32 v29, 2, v43\n"
      "v_add_u32 v29, 0x27600, v29\n"
      "ds_read_b32 v255, v29 \n"
      "ds_read_b128 v[236:239], v34 offset:0\n"
      "ds_read_b128 v[240:243], v34 offset:16\n"
      "v_mfma_f32_16x16x32_f16 v[184:187], v[76:79], v[216:219], 0\n"
      "ds_read2_b32 v[140:141], v30 offset0:16 offset1:17\n"
      "ds_read_b128 v[216:219], v33 offset:320\n"
      "v_mfma_f32_16x16x32_f16 v[180:183], v[112:115], v[220:223], v[180:183]\n"
      "v_mfma_f32_16x16x32_f16 v[184:187], v[80:83], v[220:223], v[184:187]\n"
      "ds_read2_b32 v[142:143], v30 offset0:64 offset1:65\n"
      "ds_read_b128 v[220:223], v33 offset:384\n"
      "s_waitcnt lgkmcnt(7)\n"
      "v_mfma_f32_16x16x32_f16 v[180:183], v[116:119], v[224:227], v[180:183]\n"
      "s_waitcnt lgkmcnt(5)\n"
      "v_alignbit_b32 v245, v238, v237, 16\n"
      "v_alignbit_b32 v246, v239, v238, 16\n"
      "v_mfma_f32_16x16x32_f16 v[184:187], v[84:87], v[224:227], v[184:187]\n"
      "ds_read2_b32 v[144:145], v30 offset0:8 offset1:9\n"
      "ds_read_b128 v[224:227], v33 offset:448\n"
      "s_waitcnt lgkmcnt(4)\n"
      "v_alignbit_b32 v247, v240, v239, 16\n"
      "v_alignbit_b32 v248, v241, v240, 16\n"
      "v_mfma_f32_16x16x32_f16 v[180:183], v[120:123], v[212:215], v[180:183]\n"
      "v_alignbit_b32 v249, v242, v241, 16\n"
      "v_pk_add_f16 v16, v245, v246\n"
      "v_mfma_f32_16x16x32_f16 v[184:187], v[88:91], v[212:215], v[184:187]\n"
      "ds_read2_b32 v[146:147], v30 offset0:56 offset1:57\n"
      "ds_read_b128 v[212:215], v33 offset:512\n"
      "v_pk_add_f16 v17, v246, v247\n"
      "v_pk_add_f16 v18, v247, v248\n"
      "v_mfma_f32_16x16x32_f16 v[180:183], v[124:127], v[216:219], v[180:183]\n"
      "v_pk_add_f16 v19, v248, v249\n"
      "v_pk_add_f16 v20, v16, v238\n"
      "v_mfma_f32_16x16x32_f16 v[184:187], v[92:95], v[216:219], v[184:187]\n"
      "ds_read2_b32 v[148:149], v30 offset0:0 offset1:1\n"
      "v_pk_add_f16 v21, v17, v239\n"
      "v_pk_add_f16 v22, v18, v240\n"
      "s_waitcnt lgkmcnt(3)\n"
      "v_mfma_f32_16x16x32_f16 v[180:183], v[128:131], v[220:223], v[180:183]\n"
      "v_pk_add_f16 v23, v19, v241\n"
      "ds_write_b128 v37, v[20:23] offset:0\n"
      "v_pk_add_f16 v16, v237, v239\n"
      "v_mfma_f32_16x16x32_f16 v[184:187], v[96:99], v[220:223], v[184:187]\n"
      "ds_read2_b32 v[150:151], v30 offset0:48 offset1:49\n"
      "ds_read_b128 v[216:219], v33 offset:576\n"
      "v_pk_add_f16 v17, v238, v240\n"
      "v_pk_add_f16 v18, v239, v241\n"
      "v_mfma_f32_16x16x32_f16 v[180:183], v[132:135], v[224:227], v[180:183]\n"
      "v_pk_add_f16 v19, v240, v242\n"
      "v_pk_add_f16 v20, v20, v16\n"
      "v_mfma_f32_16x16x32_f16 v[184:187], v[100:103], v[224:227], v[184:187]\n"
      "ds_read2_b32 v[152:153], v30 offset0:40 offset1:41\n"
      "ds_read_b128 v[220:223], v33 offset:640\n"
      "v_pk_add_f16 v21, v21, v17\n"
      "v_pk_add_f16 v22, v22, v18\n"
      "s_waitcnt lgkmcnt(4)\n"
      "v_mfma_f32_16x16x32_f16 v[180:183], v[136:139], v[212:215], v[180:183]\n"
      "v_pk_add_f16 v23, v23, v19\n"
      "ds_write_b128 v37, v[20:23] offset:3104\n"
      "v_alignbit_b32 v244, v237, v236, 16\n"
      "v_mfma_f32_16x16x32_f16 v[184:187], v[104:107], v[212:215], v[184:187]\n"
      "ds_read2_b32 v[154:155], v30 offset0:88 offset1:89\n"
      "ds_read_b128 v[224:227], v33 offset:704\n"
      "v_alignbit_b32 v250, v243, v242, 16\n"
      "v_pk_add_f16 v16, v244, v247\n"
      "v_mfma_f32_16x16x32_f16 v[188:191], v[72:75], v[212:215], 0\n"
      "v_pk_add_f16 v17, v245, v248\n"
      "v_pk_add_f16 v18, v246, v249\n"
      "v_mfma_f32_16x16x32_f16 v[196:199], v[44:47], v[212:215], 0\n"
      "ds_read2_b32 v[156:157], v30 offset0:32 offset1:33\n"
      "ds_read_b128 v[212:215], v33 offset:768\n"
      "v_pk_add_f16 v19, v247, v250\n"
      "v_pk_add_f16 v20, v20, v16\n"
      "s_waitcnt lgkmcnt(4)\n"
      "v_mfma_f32_16x16x32_f16 v[184:187], v[108:111], v[216:219], v[184:187]\n"
      "v_cvt_pk_bf16_f32 v24, v180, v182\n"
      "v_cvt_pk_bf16_f32 v25, v181, v183\n"
      "ds_write2st64_b32 v39, v24, v25 offset0:0 offset1:8\n"
      "v_mfma_f32_16x16x32_f16 v[188:191], v[76:79], v[216:219], v[188:191]\n"
      "ds_read2_b32 v[158:159], v30 offset0:80 offset1:81\n"
      "ds_read_b128 v[216:219], v33 offset:832\n"
      "v_pk_add_f16 v21, v21, v17\n"
      "v_pk_add_f16 v22, v22, v18\n"
      "v_mfma_f32_16x16x32_f16 v[184:187], v[112:115], v[220:223], v[184:187]\n"
      "buffer_load_dwordx4 v[56:59], v41, s[20:23], s61 offen nt\n"
      "buffer_load_dwordx4 v[68:71], v42, s[16:19], s63 offen sc1\n"
      "v_pk_add_f16 v23, v23, v19\n"
      "ds_write_b128 v38, v[20:23] offset:0\n"
      "v_pk_add_f16 v16, v236, v240\n"
      "v_mfma_f32_16x16x32_f16 v[188:191], v[80:83], v[220:223], v[188:191]\n"
      "ds_read2_b32 v[160:161], v30 offset0:24 offset1:25\n"
      "ds_read_b128 v[220:223], v33 offset:896\n"
      "v_pk_add_f16 v17, v237, v241\n"
      "v_pk_add_f16 v18, v238, v242\n"
      "s_waitcnt lgkmcnt(3)\n"
      "v_mfma_f32_16x16x32_f16 v[184:187], v[116:119], v[224:227], v[184:187]\n"
      "v_pk_add_f16 v19, v239, v243\n"
      "v_pk_add_f16 v20, v20, v16\n"
      "s_setprio 2\n"
      "v_mfma_f32_16x16x32_f16 v[188:191], v[84:87], v[224:227], v[188:191]\n"
      "ds_read2_b32 v[162:163], v30 offset0:72 offset1:73\n"
      "ds_read_b128 v[224:227], v33 offset:960\n"
      "v_pk_add_f16 v21, v21, v17\n"
      "v_pk_add_f16 v22, v22, v18\n"
      "ds_read_b128 v[236:239], v34 offset:1024\n"
      "ds_read_b128 v[240:243], v34 offset:1040\n"
      "v_mfma_f32_16x16x32_f16 v[184:187], v[120:123], v[212:215], v[184:187]\n"
      "v_pk_add_f16 v23, v23, v19\n"
      "ds_write_b128 v38, v[20:23] offset:2080\n"
      "v_mfma_f32_16x16x32_f16 v[188:191], v[88:91], v[212:215], v[188:191]\n"
      "ds_read2_b32 v[164:165], v30 offset0:128 offset1:129\n"
      "ds_read_b128 v[212:215], v33 offset:1024\n"
      "v_mfma_f32_16x16x32_f16 v[184:187], v[124:127], v[216:219], v[184:187]\n"
      "v_mfma_f32_16x16x32_f16 v[188:191], v[92:95], v[216:219], v[188:191]\n"
      "s_waitcnt lgkmcnt(4)\n"
      "v_alignbit_b32 v245, v238, v237, 16\n"
      "v_alignbit_b32 v246, v239, v238, 16\n"
      "v_mfma_f32_16x16x32_f16 v[184:187], v[128:131], v[220:223], v[184:187]\n"
      "s_waitcnt lgkmcnt(3)\n"
      "v_alignbit_b32 v247, v240, v239, 16\n"
      "v_alignbit_b32 v248, v241, v240, 16\n"
      "v_mfma_f32_16x16x32_f16 v[188:191], v[96:99], v[220:223], v[188:191]\n"
      "ds_read2_b32 v[166:167], v30 offset0:168 offset1:169\n"
      "ds_read_b64_tr_b16 v[228:229], v35 offset:32\n"
      "ds_read_b64_tr_b16 v[230:231], v35 offset:16\n"
      "v_alignbit_b32 v249, v242, v241, 16\n"
      "v_pk_add_f16 v16, v245, v246\n"
      "v_mfma_f32_16x16x32_f16 v[184:187], v[132:135], v[224:227], v[184:187]\n"
      "v_pk_add_f16 v17, v246, v247\n"
      "v_pk_add_f16 v18, v247, v248\n"
      "v_mfma_f32_16x16x32_f16 v[188:191], v[100:103], v[224:227], v[188:191]\n"
      "v_pk_add_f16 v19, v248, v249\n"
      "v_pk_add_f16 v20, v16, v238\n"
      "s_waitcnt lgkmcnt(3)\n"
      "v_mfma_f32_16x16x32_f16 v[184:187], v[136:139], v[212:215], v[184:187]\n"
      "ds_read_b128 v[216:219], v33 offset:1088\n"
      "buffer_load_dwordx4 v[60:63], v41, s[20:23], s61 offen offset:2048 nt\n"
      "v_pk_add_f16 v21, v17, v239\n"
      "v_pk_add_f16 v22, v18, v240\n"
      "v_mfma_f32_16x16x32_f16 v[188:191], v[104:107], v[212:215], v[188:191]\n"
      "ds_read2_b32 v[168:169], v30 offset0:120 offset1:121\n"
      "v_pk_add_f16 v23, v19, v241\n"
      "ds_write_b128 v37, v[20:23] offset:1024\n"
      "v_pk_add_f16 v16, v237, v239\n"
      "v_mfma_f32_16x16x32_f16 v[192:195], v[72:75], v[212:215], 0\n"
      "ds_read_b128 v[220:223], v33 offset:1152\n"
      "v_pk_add_f16 v17, v238, v240\n"
      "v_pk_add_f16 v18, v239, v241\n"
      "v_mfma_f32_16x16x32_f16 v[200:203], v[44:47], v[212:215], 0\n"
      "v_pk_add_f16 v19, v240, v242\n"
      "v_pk_add_f16 v20, v20, v16\n"
      "s_waitcnt lgkmcnt(3)\n"
      "v_mfma_f32_16x16x32_f16 v[188:191], v[228:231], v[140:143], v[188:191]\n"
      "ds_read_b128 v[224:227], v33 offset:1216\n"
      "v_cvt_pk_bf16_f32 v24, v184, v186\n"
      "v_cvt_pk_bf16_f32 v25, v185, v187\n"
      "ds_write2st64_b32 v39, v24, v25 offset0:1 offset1:9\n"
      "v_mfma_f32_16x16x32_f16 v[192:195], v[228:231], v[144:147], v[192:195]\n"
      "ds_read2_b32 v[170:171], v30 offset0:160 offset1:161\n"
      "v_pk_add_f16 v21, v21, v17\n"
      "v_pk_add_f16 v22, v22, v18\n"
      "v_mfma_f32_16x16x32_f16 v[196:199], v[228:231], v[148:151], v[196:199]\n"
      "ds_read_b64_tr_b16 v[232:233], v35 offset:3128\n"
      "ds_read_b64_tr_b16 v[234:235], v35 offset:3112\n"
      "v_pk_add_f16 v23, v23, v19\n"
      "ds_write_b128 v37, v[20:23] offset:4128\n"
      "v_alignbit_b32 v244, v237, v236, 16\n"
      "v_mfma_f32_16x16x32_f16 v[188:191], v[108:111], v[216:219], v[188:191]\n"
      "v_alignbit_b32 v250, v243, v242, 16\n"
      "v_pk_add_f16 v16, v244, v247\n"
      "v_mfma_f32_16x16x32_f16 v[192:195], v[76:79], v[216:219], v[192:195]\n"
      "v_pk_add_f16 v17, v245, v248\n"
      "v_pk_add_f16 v18, v246, v249\n"
      "s_waitcnt lgkmcnt(3)\n"
      "v_mfma_f32_16x16x32_f16 v[188:191], v[112:115], v[220:223], v[188:191]\n"
      "ds_read2_b32 v[172:173], v30 offset0:112 offset1:113\n"
      "ds_read_b128 v[212:215], v33 offset:1280\n"
      "v_pk_add_f16 v19, v247, v250\n"
      "v_pk_add_f16 v20, v20, v16\n"
      "v_mfma_f32_16x16x32_f16 v[192:195], v[80:83], v[220:223], v[192:195]\n"
      "v_pk_add_f16 v21, v21, v17\n"
      "v_pk_add_f16 v22, v22, v18\n"
      "v_mfma_f32_16x16x32_f16 v[188:191], v[116:119], v[224:227], v[188:191]\n"
      "ds_read_b128 v[216:219], v33 offset:1344\n"
      "v_pk_add_f16 v23, v23, v19\n"
      "ds_write_b128 v38, v[20:23] offset:1024\n"
      "v_pk_add_f16 v16, v236, v240\n"
      "v_mfma_f32_16x16x32_f16 v[192:195], v[84:87], v[224:227], v[192:195]\n"
      "v_pk_add_f16 v17, v237, v241\n"
      "v_pk_add_f16 v18, v238, v242\n"
      "s_setprio 1\n"
      "s_waitcnt lgkmcnt(2)\n"
      "v_mfma_f32_16x16x32_f16 v[188:191], v[232:235], v[152:155], v[188:191]\n"
      "ds_read2_b32 v[174:175], v30 offset0:152 offset1:153\n"
      "ds_read_b128 v[220:223], v33 offset:1408\n"
      "v_pk_add_f16 v19, v239, v243\n"
      "v_pk_add_f16 v20, v20, v16\n"
      "v_mfma_f32_16x16x32_f16 v[192:195], v[232:235], v[156:159], v[192:195]\n"
      "v_pk_add_f16 v21, v21, v17\n"
      "v_pk_add_f16 v22, v22, v18\n"
      "v_mfma_f32_16x16x32_f16 v[196:199], v[232:235], v[160:163], v[196:199]\n"
      "ds_read_b128 v[224:227], v33 offset:1472\n"
      "v_pk_add_f16 v23, v23, v19\n"
      "ds_write_b128 v38, v[20:23] offset:3104\n"
      "v_mfma_f32_16x16x32_f16 v[188:191], v[120:123], v[212:215], v[188:191]\n"
      "v_readlane_b32 s72, v255, 0\n"
      "v_mfma_f32_16x16x32_f16 v[192:195], v[88:91], v[212:215], v[192:195]\n"
      "ds_read2_b32 v[176:177], v30 offset0:104 offset1:105\n"
      "ds_read_b128 v[212:215], v33 offset:1536\n"
      "v_readlane_b32 s73, v255, 1\n"
      "s_waitcnt lgkmcnt(4)\n"
      "v_mfma_f32_16x16x32_f16 v[188:191], v[124:127], v[216:219], v[188:191]\n"
      "v_readlane_b32 s74, v255, 2\n"
      "v_mfma_f32_16x16x32_f16 v[192:195], v[92:95], v[216:219], v[192:195]\n"
      "v_readlane_b32 s75, v255, 3\n"
      "v_mfma_f32_16x16x32_f16 v[188:191], v[128:131], v[220:223], v[188:191]\n"
      "v_readlane_b32 s76, v255, 4\n"
      "v_mfma_f32_16x16x32_f16 v[192:195], v[96:99], v[220:223], v[192:195]\n"
      "ds_read2_b32 v[178:179], v30 offset0:144 offset1:145\n"
      "ds_read_b64_tr_b16 v[228:229], v35 offset:544\n"
      "ds_read_b64_tr_b16 v[230:231], v35 offset:528\n"
      "v_readlane_b32 s77, v255, 5\n"
      "s_waitcnt lgkmcnt(3)\n"
      "v_mfma_f32_16x16x32_f16 v[188:191], v[132:135], v[224:227], v[188:191]\n"
      "v_readlane_b32 s78, v255, 6\n"
      "v_mfma_f32_16x16x32_f16 v[192:195], v[100:103], v[224:227], v[192:195]\n"
      "v_readlane_b32 s79, v255, 7\n"
      "v_mfma_f32_16x16x32_f16 v[188:191], v[136:139], v[212:215], v[188:191]\n"
      "ds_read_b128 v[216:219], v33 offset:1600\n"
      "s_movk_i32 s59, 0x7fff\n"
      "s_waitcnt vmcnt(1)\n"
      "s_nop 0\n"
      ".Lcsgh%=:\n"
      "v_cmp_ne_u32 s[64:65], s15, v69\n"
      "v_cmp_ne_u32 vcc, s15, v71\n"
      "s_nop 0\n"
      "s_or_b64 vcc, vcc, s[64:65]\n"
      "s_cbranch_scc1 .Lcsgsh%=\n"
      ".Lcsgoh%=:\n"
      "v_mov_b32 v42, v8\n"
      "v_mfma_f32_16x16x32_f16 v[192:195], v[104:107], v[212:215], v[192:195]\n"
      "v_and_b32 v26, 0xffff0000, v68\n"
      "v_lshlrev_b32 v28, 16, v68\n"
      "v_mfma_f32_16x16x32_f16 v[196:199], v[72:75], v[212:215], v[196:199]\n"
      "ds_read_b128 v[220:223], v33 offset:1664\n"
      "v_rcp_f32 v26, v26\n"
      "v_and_b32 v27, 0xffff0000, v70\n"
      "v_mfma_f32_16x16x32_f16 v[204:207], v[44:47], v[212:215], 0\n"
      "v_lshlrev_b32 v29, 16, v70\n"
      "v_rcp_f32 v27, v27\n"
      "s_waitcnt lgkmcnt(1)\n"
      "v_mfma_f32_16x16x32_f16 v[192:195], v[228:231], v[140:143], v[192:195]\n"
      "ds_read_b128 v[224:227], v33 offset:1728\n"
      "v_cvt_pk_bf16_f32 v24, v188, v190\n"
      "v_cvt_pk_bf16_f32 v25, v189, v191\n"
      "ds_write2st64_b32 v39, v24, v25 offset0:2 offset1:10\n"
      "v_mfma_f32_16x16x32_f16 v[196:199], v[228:231], v[144:147], v[196:199]\n"
      "v_mul_f32 v28, -v28, v26\n"
      "v_mul_f32 v29, -v29, v27\n"
      "v_mfma_f32_16x16x32_f16 v[200:203], v[228:231], v[148:151], v[200:203]\n"
      "ds_read_b64_tr_b16 v[232:233], v35 offset:3640\n"
      "ds_read_b64_tr_b16 v[234:235], v35 offset:3624\n"
      "v_lshlrev_b32 v180, 16, v56\n"
      "s_waitcnt vmcnt(0)\n"
      "v_lshlrev_b32 v181, 16, v60\n"
      "v_mfma_f32_16x16x32_f16 v[192:195], v[108:111], v[216:219], v[192:195]\n"
      "v_and_b32 v182, 0xffff0000, v56\n"
      "v_and_b32 v183, 0xffff0000, v60\n"
      "v_mfma_f32_16x16x32_f16 v[196:199], v[76:79], v[216:219], v[196:199]\n"
      "v_fma_f32 v180, v180, v26, v28\n"
      "v_fma_f32 v181, v181, v27, v29\n"
      "s_setprio 0\n"
      "s_waitcnt lgkmcnt(2)\n"
      "v_mfma_f32_16x16x32_f16 v[192:195], v[112:115], v[220:223], v[192:195]\n"
      "ds_read_b128 v[212:215], v33 offset:1792\n"
      "v_fma_f32 v182, v182, v26, v28\n"
      "v_fma_f32 v183, v183, v27, v29\n"
      "v_mfma_f32_16x16x32_f16 v[196:199], v[80:83], v[220:223], v[196:199]\n"
      "v_cvt_pk_f16_f32 v180, v180, v181\n"
      "v_cvt_pk_f16_f32 v182, v182, v183\n"
      "ds_write2st64_b32 v32, v180, v182 offset0:8 offset1:27\n"
      "v_mfma_f32_16x16x32_f16 v[192:195], v[116:119], v[224:227], v[192:195]\n"
      "ds_read_b128 v[216:219], v33 offset:1856\n"
      "v_lshlrev_b32 v184, 16, v57\n"
      "v_lshlrev_b32 v185, 16, v61\n"
      "v_mfma_f32_16x16x32_f16 v[196:199], v[84:87], v[224:227], v[196:199]\n"
      "v_and_b32 v186, 0xffff0000, v57\n"
      "v_and_b32 v187, 0xffff0000, v61\n"
      "s_waitcnt lgkmcnt(2)\n"
      "v_mfma_f32_16x16x32_f16 v[192:195], v[232:235], v[152:155], v[192:195]\n"
      "ds_read_b128 v[220:223], v33 offset:1920\n"
      "v_fma_f32 v184, v184, v26, v28\n"
      "v_fma_f32 v185, v185, v27, v29\n"
      "v_mfma_f32_16x16x32_f16 v[196:199], v[232:235], v[156:159], v[196:199]\n"
      "v_fma_f32 v186, v186, v26, v28\n"
      "v_fma_f32 v187, v187, v27, v29\n"
      "s_cmp_lg_u32 s33, 0\n"
      "s_cbranch_scc1 .Lcg%=\n"
      "buffer_load_dwordx2 v[252:253], v9, s[16:19], s43 offen sc1\n"
      ".Lcg%=:\n"
      "v_mfma_f32_16x16x32_f16 v[200:203], v[232:235], v[160:163], v[200:203]\n"
      "ds_read_b128 v[224:227], v33 offset:1984\n"
      "v_cvt_pk_f16_f32 v184, v184, v185\n"
      "v_cvt_pk_f16_f32 v186, v186, v187\n"
      "ds_write2st64_b32 v32, v184, v186 offset0:46 offset1:65\n"
      "v_mfma_f32_16x16x32_f16 v[192:195], v[120:123], v[212:215], v[192:195]\n"
      "v_lshlrev_b32 v180, 16, v58\n"
      "v_lshlrev_b32 v181, 16, v62\n"
      "v_mfma_f32_16x16x32_f16 v[196:199], v[88:91], v[212:215], v[196:199]\n"
      "ds_read_b128 v[212:215], v33 offset:2048\n"
      "v_and_b32 v182, 0xffff0000, v58\n"
      "v_and_b32 v183, 0xffff0000, v62\n"
      "s_waitcnt lgkmcnt(3)\n"
      "v_mfma_f32_16x16x32_f16 v[192:195], v[124:127], v[216:219], v[192:195]\n"
      "v_fma_f32 v180, v180, v26, v28\n"
      "v_fma_f32 v181, v181, v27, v29\n"
      "v_mfma_f32_16x16x32_f16 v[196:199], v[92:95], v[216:219], v[196:199]\n"
      "v_fma_f32 v182, v182, v26, v28\n"
      "v_fma_f32 v183, v183, v27, v29\n"
      "v_mfma_f32_16x16x32_f16 v[192:195], v[128:131], v[220:223], v[192:195]\n"
      "v_cvt_pk_f16_f32 v180, v180, v181\n"
      "v_cvt_pk_f16_f32 v182, v182, v183\n"
      "ds_write2st64_b32 v32, v180, v182 offset0:84 offset1:103\n"
      "v_mfma_f32_16x16x32_f16 v[196:199], v[96:99], v[220:223], v[196:199]\n"
      "ds_read_b64_tr_b16 v[228:229], v36 offset:24\n"
      "ds_read_b64_tr_b16 v[230:231], v36 offset:2088\n"
      "v_lshlrev_b32 v184, 16, v59\n"
      "v_lshlrev_b32 v185, 16, v63\n"
      "s_waitcnt lgkmcnt(3)\n"
      "v_mfma_f32_16x16x32_f16 v[192:195], v[132:135], v[224:227], v[192:195]\n"
      "v_and_b32 v186, 0xffff0000, v59\n"
      "v_and_b32 v187, 0xffff0000, v63\n"
      "v_mfma_f32_16x16x32_f16 v[196:199], v[100:103], v[224:227], v[196:199]\n"
      "v_fma_f32 v184, v184, v26, v28\n"
      "v_fma_f32 v185, v185, v27, v29\n"
      "v_mfma_f32_16x16x32_f16 v[192:195], v[136:139], v[212:215], v[192:195]\n"
      "v_fma_f32 v186, v186, v26, v28\n"
      "v_fma_f32 v187, v187, v27, v29\n"
      "v_mfma_f32_16x16x32_f16 v[196:199], v[104:107], v[212:215], v[196:199]\n"
      "ds_read_b64_tr_b16 v[232:233], v36 offset:536\n"
      "ds_read_b64_tr_b16 v[234:235], v36 offset:2600\n"
      "v_cvt_pk_f16_f32 v184, v184, v185\n"
      "v_cvt_pk_f16_f32 v186, v186, v187\n"
      "ds_write2st64_b32 v32, v184, v186 offset0:122 offset1:141\n"
      "v_mfma_f32_16x16x32_f16 v[200:203], v[72:75], v[212:215], v[200:203]\n"
      "v_permlane32_swap_b32 v56, v60\n"
      "v_permlane32_swap_b32 v57, v61\n"
      "v_mfma_f32_16x16x32_f16 v[208:211], v[44:47], v[212:215], 0\n"
      "v_permlane32_swap_b32 v58, v62\n"
      "v_permlane32_swap_b32 v59, v63\n"
      "s_waitcnt lgkmcnt(3)\n"
      "v_mfma_f32_16x16x32_f16 v[196:199], v[228:231], v[164:167], v[196:199]\n"
      "v_cvt_pk_bf16_f32 v24, v192, v194\n"
      "v_cvt_pk_bf16_f32 v25, v193, v195\n"
      "ds_write2st64_b32 v39, v24, v25 offset0:3 offset1:11\n"
      "v_mfma_f32_16x16x32_f16 v[200:203], v[228:231], v[168:171], v[200:203]\n"
      "v_mfma_f32_16x16x32_f16 v[204:207], v[228:231], v[172:175], v[204:207]\n"
      "v_mfma_f32_16x16x32_f16 v[208:211], v[228:231], v[176:179], v[208:211]\n"
      "ds_read_b64_tr_b16 v[228:229], v35 offset:1056\n"
      "ds_read_b64_tr_b16 v[230:231], v35 offset:1040\n"
      "s_cmp_eq_u32 s33, 0\n"
      "s_cbranch_scc1 .Lch%=\n"
      ".Lcho%=:\n"
      "s_waitcnt lgkmcnt(2)\n"
      "s_barrier\n"
      "s_setprio 3\n"
      "v_mfma_f32_16x16x32_f16 v[200:203], v[232:235], v[164:167], v[200:203]\n"
      "ds_read_b64_tr_b16 v[64:65], v40 offset:0\n"
      "ds_read_b64_tr_b16 v[66:67], v40 offset:35072\n"
      "v_lshlrev_b32 v68, 16, v48\n"
      "v_and_b32 v69, 0xffff0000, v48\n"
      "v_mfma_f32_16x16x32_f16 v[204:207], v[232:235], v[168:171], v[204:207]\n"
      "v_lshlrev_b32 v70, 16, v49\n"
      "v_and_b32 v71, 0xffff0000, v49\n"
      "ds_read_b128 v[236:239], v34 offset:2048\n"
      "ds_read_b128 v[240:243], v34 offset:2064\n"
      "ds_read_b32 v12, v14 offset:256\n"
      "v_mfma_f32_16x16x32_f16 v[208:211], v[232:235], v[172:175], v[208:211]\n"
      "ds_read_b128 v[216:219], v33 offset:2112\n"
      "ds_read_b64_tr_b16 v[232:233], v35 offset:4152\n"
      "ds_read_b64_tr_b16 v[234:235], v35 offset:4136\n"
      "v_lshlrev_b32 v26, 16, v50\n"
      "v_and_b32 v27, 0xffff0000, v50\n"
      "s_waitcnt lgkmcnt(6)\n"
      "v_mfma_f32_16x16x32_f16 v[196:199], v[228:231], v[140:143], v[196:199]\n"
      "v_lshlrev_b32 v28, 16, v51\n"
      "v_and_b32 v29, 0xffff0000, v51\n"
      "v_mfma_f32_16x16x32_f16 v[200:203], v[228:231], v[144:147], v[200:203]\n"
      "ds_read_b128 v[220:223], v33 offset:2176\n"
      "v_lshlrev_b32 v48, 16, v64\n"
      "v_and_b32 v64, 0xffff0000, v64\n"
      "v_mfma_f32_16x16x32_f16 v[204:207], v[228:231], v[148:151], v[204:207]\n"
      "v_fmac_f32 v68, s72, v48\n"
      "v_fmac_f32 v69, s73, v64\n"
      "s_waitcnt lgkmcnt(1)\n"
      "v_mfma_f32_16x16x32_f16 v[196:199], v[232:235], v[152:155], v[196:199]\n"
      "ds_read_b128 v[224:227], v33 offset:2240\n"
      "v_lshlrev_b32 v49, 16, v65\n"
      "v_and_b32 v65, 0xffff0000, v65\n"
      "v_mfma_f32_16x16x32_f16 v[200:203], v[232:235], v[156:159], v[200:203]\n"
      "v_fmac_f32 v70, s74, v49\n"
      "v_fmac_f32 v71, s75, v65\n"
      "v_mfma_f32_16x16x32_f16 v[204:207], v[232:235], v[160:163], v[204:207]\n"
      "ds_read_b128 v[212:215], v33 offset:2304\n"
      "v_lshlrev_b32 v50, 16, v66\n"
      "v_and_b32 v66, 0xffff0000, v66\n"
      "v_mfma_f32_16x16x32_f16 v[196:199], v[108:111], v[216:219], v[196:199]\n"
      "v_fmac_f32 v26, s76, v50\n"
      "v_fmac_f32 v27, s77, v66\n"
      "v_mfma_f32_16x16x32_f16 v[200:203], v[76:79], v[216:219], v[200:203]\n"
      "ds_read_b64_tr_b16 v[228:229], v35 offset:1568\n"
      "ds_read_b64_tr_b16 v[230:231], v35 offset:1552\n"
      "v_lshlrev_b32 v51, 16, v67\n"
      "v_and_b32 v67, 0xffff0000, v67\n"
      "s_waitcnt lgkmcnt(3)\n"
      "v_mfma_f32_16x16x32_f16 v[196:199], v[112:115], v[220:223], v[196:199]\n"
      "v_fmac_f32 v28, s78, v51\n"
      "v_fmac_f32 v29, s79, v67\n"
      "v_mfma_f32_16x16x32_f16 v[200:203], v[80:83], v[220:223], v[200:203]\n"
      "v_cvt_pk_bf16_f32 v48, v68, v69\n"
      "v_cvt_pk_bf16_f32 v49, v70, v71\n"
      "v_mfma_f32_16x16x32_f16 v[196:199], v[116:119], v[224:227], v[196:199]\n"
      "ds_read_b128 v[216:219], v33 offset:2368\n"
      "v_cvt_pk_bf16_f32 v50, v26, v27\n"
      "v_cvt_pk_bf16_f32 v51, v28, v29\n"
      "s_setprio 2\n"
      "v_mfma_f32_16x16x32_f16 v[200:203], v[84:87], v[224:227], v[200:203]\n"
      "v_lshlrev_b32 v13, 24, v12\n"
      "v_and_or_b32 v13, v13, s58, v42\n"
      "s_waitcnt lgkmcnt(1)\n"
      "v_mfma_f32_16x16x32_f16 v[196:199], v[120:123], v[212:215], v[196:199]\n"
      "ds_read_b128 v[220:223], v33 offset:2432\n"
      "s_cmp_lg_u32 s13, 0\n"
      "s_cbranch_scc1 .Lcs00%=\n"
      "buffer_store_dwordx4 v[48:51], v13, s[24:27], s54 offen\n"
      ".Lcs00%=:\n"
      "v_alignbit_b32 v245, v238, v237, 16\n"
      "v_alignbit_b32 v246, v239, v238, 16\n"
      "v_mfma_f32_16x16x32_f16 v[200:203], v[88:91], v[212:215], v[200:203]\n"
      "v_alignbit_b32 v247, v240, v239, 16\n"
      "v_alignbit_b32 v248, v241, v240, 16\n"
      "v_mfma_f32_16x16x32_f16 v[200:203], v[228:231], v[140:143], v[200:203]\n"
      "ds_read_b128 v[224:227], v33 offset:2496\n"
      "s_cmp_lg_u32 s13, 1\n"
      "s_cbranch_scc1 .Lcs01%=\n"
      "buffer_store_dwordx4 v[48:51], v13, s[24:27], s54 offen\n"
      ".Lcs01%=:\n"
      "v_alignbit_b32 v249, v242, v241, 16\n"
      "v_pk_add_f16 v16, v245, v246\n"
      "v_mfma_f32_16x16x32_f16 v[204:207], v[228:231], v[144:147], v[204:207]\n"
      "v_pk_add_f16 v17, v246, v247\n"
      "v_pk_add_f16 v18, v247, v248\n"
      "v_mfma_f32_16x16x32_f16 v[208:211], v[228:231], v[148:151], v[208:211]\n"
      "ds_read_b128 v[212:215], v33 offset:2560\n"
      "s_cmp_lg_u32 s13, 2\n"
      "s_cbranch_scc1 .Lcs02%=\n"
      "buffer_store_dwordx4 v[48:51], v13, s[24:27], s54 offen\n"
      ".Lcs02%=:\n"
      "v_pk_add_f16 v19, v248, v249\n"
      "v_pk_add_f16 v20, v16, v238\n"
      "s_waitcnt lgkmcnt(1)\n"
      "v_mfma_f32_16x16x32_f16 v[196:199], v[124:127], v[216:219], v[196:199]\n"
      "v_pk_add_f16 v21, v17, v239\n"
      "v_pk_add_f16 v22, v18, v240\n"
      "v_mfma_f32_16x16x32_f16 v[200:203], v[92:95], v[216:219], v[200:203]\n"
      "s_cmp_lg_u32 s13, 3\n"
      "s_cbranch_scc1 .Lcs03%=\n"
      "buffer_store_dwordx4 v[48:51], v13, s[24:27], s54 offen\n"
      ".Lcs03%=:\n"
      "v_pk_add_f16 v23, v19, v241\n"
      "ds_write_b128 v37, v[20:23] offset:2048\n"
      "v_pk_add_f16 v16, v237, v239\n"
      "v_mfma_f32_16x16x32_f16 v[196:199], v[128:131], v[220:223], v[196:199]\n"
      "ds_read_b64_tr_b16 v[232:233], v35 offset:4664\n"
      "ds_read_b64_tr_b16 v[234:235], v35 offset:4648\n"
      "ds_read_b64_tr_b16 v[64:65], v40 offset:512\n"
      "ds_read_b64_tr_b16 v[66:67], v40 offset:35584\n"
      "v_lshlrev_b32 v68, 16, v52\n"
      "v_and_b32 v69, 0xffff0000, v52\n"
      "v_mfma_f32_16x16x32_f16 v[200:203], v[96:99], v[220:223], v[200:203]\n"
      "v_lshlrev_b32 v70, 16, v53\n"
      "v_and_b32 v71, 0xffff0000, v53\n"
      "v_mfma_f32_16x16x32_f16 v[196:199], v[132:135], v[224:227], v[196:199]\n"
      "v_lshlrev_b32 v26, 16, v54\n"
      "v_and_b32 v27, 0xffff0000, v54\n"
      "v_mfma_f32_16x16x32_f16 v[200:203], v[100:103], v[224:227], v[200:203]\n"
      "ds_read_b128 v[216:219], v33 offset:2624\n"
      "v_lshlrev_b32 v28, 16, v55\n"
      "v_and_b32 v29, 0xffff0000, v55\n"
      "s_waitcnt lgkmcnt(1)\n"
      "v_mfma_f32_16x16x32_f16 v[196:199], v[136:139], v[212:215], v[196:199]\n"
      "v_lshlrev_b32 v52, 16, v64\n"
      "v_and_b32 v64, 0xffff0000, v64\n"
      "v_mfma_f32_16x16x32_f16 v[200:203], v[104:107], v[212:215], v[200:203]\n"
      "ds_read_b128 v[220:223], v33 offset:2688\n"
      "v_fmac_f32 v68, s72, v52\n"
      "v_fmac_f32 v69, s73, v64\n"
      "s_setprio 1\n"
      "v_mfma_f32_16x16x32_f16 v[204:207], v[72:75], v[212:215], v[204:207]\n"
      "v_lshlrev_b32 v53, 16, v65\n"
      "v_and_b32 v65, 0xffff0000, v65\n"
      "v_mfma_f32_16x16x32_f16 v[200:203], v[232:235], v[152:155], v[200:203]\n"
      "ds_read_b128 v[224:227], v33 offset:2752\n"
      "v_fmac_f32 v70, s74, v53\n"
      "v_fmac_f32 v71, s75, v65\n"
      "v_mfma_f32_16x16x32_f16 v[204:207], v[232:235], v[156:159], v[204:207]\n"
      "v_cvt_pk_bf16_f32 v24, v196, v198\n"
      "v_cvt_pk_bf16_f32 v25, v197, v199\n"
      "ds_write2st64_b32 v39, v24, v25 offset0:4 offset1:12\n"
      "v_mfma_f32_16x16x32_f16 v[208:211], v[232:235], v[160:163], v[208:211]\n"
      "ds_read_b128 v[212:215], v33 offset:2816\n"
      "v_lshlrev_b32 v54, 16, v66\n"
      "v_and_b32 v66, 0xffff0000, v66\n"
      "s_waitcnt lgkmcnt(3)\n"
      "v_mfma_f32_16x16x32_f16 v[200:203], v[108:111], v[216:219], v[200:203]\n"
      "v_fmac_f32 v26, s76, v54\n"
      "v_fmac_f32 v27, s77, v66\n"
      "v_mfma_f32_16x16x32_f16 v[204:207], v[76:79], v[216:219], v[204:207]\n"
      "ds_read_b128 v[216:219], v33 offset:2880\n"
      "v_lshlrev_b32 v55, 16, v67\n"
      "v_and_b32 v67, 0xffff0000, v67\n"
      "v_mfma_f32_16x16x32_f16 v[200:203], v[112:115], v[220:223], v[200:203]\n"
      "v_fmac_f32 v28, s78, v55\n"
      "v_fmac_f32 v29, s79, v67\n"
      "v_mfma_f32_16x16x32_f16 v[204:207], v[80:83], v[220:223], v[204:207]\n"
      "ds_read_b128 v[220:223], v33 offset:2944\n"
      "v_cvt_pk_bf16_f32 v52, v68, v69\n"
      "v_cvt_pk_bf16_f32 v53, v70, v71\n"
      "s_waitcnt lgkmcnt(2)\n"
      "v_mfma_f32_16x16x32_f16 v[200:203], v[116:119], v[224:227], v[200:203]\n"
      "v_cvt_pk_bf16_f32 v54, v26, v27\n"
      "v_cvt_pk_bf16_f32 v55, v28, v29\n"
      "v_mfma_f32_16x16x32_f16 v[204:207], v[84:87], v[224:227], v[204:207]\n"
      "ds_read_b128 v[224:227], v33 offset:3008\n"
      "v_lshlrev_b32 v13, 16, v12\n"
      "v_and_or_b32 v13, v13, s58, v42\n"
      "v_mfma_f32_16x16x32_f16 v[200:203], v[120:123], v[212:215], v[200:203]\n"
      "s_cmp_lg_u32 s13, 0\n"
      "s_cbranch_scc1 .Lcs10%=\n"
      "buffer_store_dwordx4 v[52:55], v13, s[24:27], s55 offen\n"
      ".Lcs10%=:\n"
      "v_pk_add_f16 v17, v238, v240\n"
      "v_pk_add_f16 v18, v239, v241\n"
      "s_setprio 0\n"
      "v_mfma_f32_16x16x32_f16 v[204:207], v[88:91], v[212:215], v[204:207]\n"
      "ds_read_b128 v[212:215], v33 offset:3072\n"
      "v_pk_add_f16 v19, v240, v242\n"
      "v_pk_add_f16 v20, v20, v16\n"
      "s_waitcnt lgkmcnt(1)\n"
      "v_mfma_f32_16x16x32_f16 v[200:203], v[124:127], v[216:219], v[200:203]\n"
      "s_cmp_lg_u32 s13, 1\n"
      "s_cbranch_scc1 .Lcs11%=\n"
      "buffer_store_dwordx4 v[52:55], v13, s[24:27], s55 offen\n"
      ".Lcs11%=:\n"
      "v_pk_add_f16 v21, v21, v17\n"
      "v_pk_add_f16 v22, v22, v18\n"
      "v_mfma_f32_16x16x32_f16 v[204:207], v[92:95], v[216:219], v[204:207]\n"
      "v_pk_add_f16 v23, v23, v19\n"
      "ds_write_b128 v37, v[20:23] offset:5152\n"
      "v_mfma_f32_16x16x32_f16 v[200:203], v[128:131], v[220:223], v[200:203]\n"
      "ds_read_b64_tr_b16 v[228:229], v36 offset:1048\n"
      "ds_read_b64_tr_b16 v[230:231], v36 offset:3112\n"
      "s_cmp_lg_u32 s13, 2\n"
      "s_cbranch_scc1 .Lcs12%=\n"
      "buffer_store_dwordx4 v[52:55], v13, s[24:27], s55 offen\n"
      ".Lcs12%=:\n"
      "v_mfma_f32_16x16x32_f16 v[204:207], v[96:99], v[220:223], v[204:207]\n"
      "v_mfma_f32_16x16x32_f16 v[200:203], v[132:135], v[224:227], v[200:203]\n"
      "ds_read_b128 v[216:219], v33 offset:3136\n"
      "s_cmp_lg_u32 s13, 3\n"
      "s_cbranch_scc1 .Lcs13%=\n"
      "buffer_store_dwordx4 v[52:55], v13, s[24:27], s55 offen\n"
      ".Lcs13%=:\n"
      "v_mfma_f32_16x16x32_f16 v[204:207], v[100:103], v[224:227], v[204:207]\n"
      "s_waitcnt lgkmcnt(1)\n"
      "v_mfma_f32_16x16x32_f16 v[200:203], v[136:139], v[212:215], v[200:203]\n"
      "ds_read_b128 v[220:223], v33 offset:3200\n"
      "v_mfma_f32_16x16x32_f16 v[204:207], v[104:107], v[212:215], v[204:207]\n"
      "v_mfma_f32_16x16x32_f16 v[208:211], v[72:75], v[212:215], v[208:211]\n"
      "ds_read_b128 v[224:227], v33 offset:3264\n"
      "v_mfma_f32_16x16x32_f16 v[204:207], v[228:231], v[164:167], v[204:207]\n"
      "v_mfma_f32_16x16x32_f16 v[208:211], v[228:231], v[168:171], v[208:211]\n"
      "ds_read_b64_tr_b16 v[232:233], v36 offset:1560\n"
      "ds_read_b64_tr_b16 v[234:235], v36 offset:3624\n"
      "v_cvt_pk_bf16_f32 v24, v200, v202\n"
      "v_cvt_pk_bf16_f32 v25, v201, v203\n"
      "ds_write2st64_b32 v39, v24, v25 offset0:5 offset1:13\n"
      "s_waitcnt lgkmcnt(5)\n"
      "v_mfma_f32_16x16x32_f16 v[204:207], v[108:111], v[216:219], v[204:207]\n"
      "ds_read_b128 v[212:215], v33 offset:3328\n"
      "v_mfma_f32_16x16x32_f16 v[208:211], v[76:79], v[216:219], v[208:211]\n"
      "s_waitcnt lgkmcnt(5)\n"
      "v_mfma_f32_16x16x32_f16 v[204:207], v[112:115], v[220:223], v[204:207]\n"
      "ds_read_b128 v[216:219], v33 offset:3392\n"
      "s_waitcnt lgkmcnt(2)\n"
      "s_barrier\n"
      "s_setprio 3\n"
      "v_mfma_f32_16x16x32_f16 v[208:211], v[80:83], v[220:223], v[208:211]\n"
      "ds_read_b64_tr_b16 v[64:65], v40 offset:1024\n"
      "ds_read_b64_tr_b16 v[66:67], v40 offset:36096\n"
      "v_lshlrev_b32 v68, 16, v56\n"
      "v_and_b32 v69, 0xffff0000, v56\n"
      "v_mfma_f32_16x16x32_f16 v[204:207], v[116:119], v[224:227], v[204:207]\n"
      "ds_read_b128 v[220:223], v33 offset:3456\n"
      "v_lshlrev_b32 v70, 16, v57\n"
      "v_and_b32 v71, 0xffff0000, v57\n"
      "v_mfma_f32_16x16x32_f16 v[208:211], v[84:87], v[224:227], v[208:211]\n"
      "v_lshlrev_b32 v26, 16, v58\n"
      "v_and_b32 v27, 0xffff0000, v58\n"
      "v_mfma_f32_16x16x32_f16 v[208:211], v[232:235], v[164:167], v[208:211]\n"
      "ds_read_b64_tr_b16 v[228:229], v35 offset:2080\n"
      "ds_read_b64_tr_b16 v[230:231], v35 offset:2064\n"
      "v_lshlrev_b32 v28, 16, v59\n"
      "v_and_b32 v29, 0xffff0000, v59\n"
      "s_waitcnt lgkmcnt(3)\n"
      "v_mfma_f32_16x16x32_f16 v[204:207], v[120:123], v[212:215], v[204:207]\n"
      "v_lshlrev_b32 v56, 16, v64\n"
      "v_and_b32 v64, 0xffff0000, v64\n"
      "v_mfma_f32_16x16x32_f16 v[208:211], v[88:91], v[212:215], v[208:211]\n"
      "ds_read_b128 v[224:227], v33 offset:3520\n"
      "v_fmac_f32 v68, s72, v56\n"
      "v_fmac_f32 v69, s73, v64\n"
      "v_mfma_f32_16x16x32_f16 v[204:207], v[124:127], v[216:219], v[204:207]\n"
      "v_lshlrev_b32 v57, 16, v65\n"
      "v_and_b32 v65, 0xffff0000, v65\n"
      "v_mfma_f32_16x16x32_f16 v[208:211], v[92:95], v[216:219], v[208:211]\n"
      "ds_read_b128 v[212:215], v33 offset:3584\n"
      "v_fmac_f32 v70, s74, v57\n"
      "v_fmac_f32 v71, s75, v65\n"
      "s_setprio 2\n"
      "s_waitcnt lgkmcnt(2)\n"
      "v_mfma_f32_16x16x32_f16 v[204:207], v[128:131], v[220:223], v[204:207]\n"
      "v_lshlrev_b32 v58, 16, v66\n"
      "v_and_b32 v66, 0xffff0000, v66\n"
      "v_mfma_f32_16x16x32_f16 v[208:211], v[96:99], v[220:223], v[208:211]\n"
      "ds_read_b128 v[216:219], v33 offset:3648\n"
      "v_fmac_f32 v26, s76, v58\n"
      "v_fmac_f32 v27, s77, v66\n"
      "v_mfma_f32_16x16x32_f16 v[204:207], v[228:231], v[140:143], v[204:207]\n"
      "ds_read_b64_tr_b16 v[232:233], v35 offset:5176\n"
      "ds_read_b64_tr_b16 v[234:235], v35 offset:5160\n"
      "v_lshlrev_b32 v59, 16, v67\n"
      "v_and_b32 v67, 0xffff0000, v67\n"
      "v_mfma_f32_16x16x32_f16 v[208:211], v[228:231], v[144:147], v[208:211]\n"
      "v_fmac_f32 v28, s78, v59\n"
      "v_fmac_f32 v29, s79, v67\n"
      "s_waitcnt lgkmcnt(3)\n"
      "v_mfma_f32_16x16x32_f16 v[204:207], v[132:135], v[224:227], v[204:207]\n"
      "ds_read_b128 v[220:223], v33 offset:3712\n"
      "v_cvt_pk_bf16_f32 v56, v68, v69\n"
      "v_cvt_pk_bf16_f32 v57, v70, v71\n"
      "v_mfma_f32_16x16x32_f16 v[208:211], v[100:103], v[224:227], v[208:211]\n"
      "ds_read_b128 v[224:227], v33 offset:3776\n"
      "v_cvt_pk_bf16_f32 v58, v26, v27\n"
      "v_cvt_pk_bf16_f32 v59, v28, v29\n"
      "v_mfma_f32_16x16x32_f16 v[204:207], v[136:139], v[212:215], v[204:207]\n"
      "v_lshlrev_b32 v13, 8, v12\n"
      "v_and_or_b32 v13, v13, s58, v42\n"
      "s_setprio 1\n"
      "v_mfma_f32_16x16x32_f16 v[208:211], v[104:107], v[212:215], v[208:211]\n"
      "ds_read_b64_tr_b16 v[228:229], v35 offset:2592\n"
      "ds_read_b64_tr_b16 v[230:231], v35 offset:2576\n"
      "ds_read_b128 v[212:215], v33 offset:3840\n"
      "s_cmp_lg_u32 s13, 0\n"
      "s_cbranch_scc1 .Lcs20%=\n"
      "buffer_store_dwordx4 v[56:59], v13, s[24:27], s56 offen\n"
      ".Lcs20%=:\n"
      "v_lshlrev_b32 v68, 16, v60\n"
      "v_and_b32 v69, 0xffff0000, v60\n"
      "s_waitcnt lgkmcnt(3)\n"
      "v_mfma_f32_16x16x32_f16 v[208:211], v[108:111], v[216:219], v[208:211]\n"
      "ds_read_b128 v[216:219], v33 offset:3904\n"
      "v_lshlrev_b32 v70, 16, v61\n"
      "v_and_b32 v71, 0xffff0000, v61\n"
      "v_mfma_f32_16x16x32_f16 v[204:207], v[232:235], v[152:155], v[204:207]\n"
      "s_cmp_lg_u32 s13, 1\n"
      "s_cbranch_scc1 .Lcs21%=\n"
      "buffer_store_dwordx4 v[56:59], v13, s[24:27], s56 offen\n"
      ".Lcs21%=:\n"
      "v_lshlrev_b32 v26, 16, v62\n"
      "v_and_b32 v27, 0xffff0000, v62\n"
      "v_mfma_f32_16x16x32_f16 v[208:211], v[232:235], v[156:159], v[208:211]\n"
      "ds_read_b64_tr_b16 v[232:233], v35 offset:5688\n"
      "ds_read_b64_tr_b16 v[234:235], v35 offset:5672\n"
      "v_lshlrev_b32 v28, 16, v63\n"
      "v_and_b32 v29, 0xffff0000, v63\n"
      "v_mfma_f32_16x16x32_f16 v[208:211], v[112:115], v[220:223], v[208:211]\n"
      "ds_read_b128 v[220:223], v33 offset:3968\n"
      "s_cmp_lg_u32 s13, 2\n"
      "s_cbranch_scc1 .Lcs22%=\n"
      "buffer_store_dwordx4 v[56:59], v13, s[24:27], s56 offen\n"
      ".Lcs22%=:\n"
      "s_setprio 0\n"
      "v_mfma_f32_16x16x32_f16 v[208:211], v[116:119], v[224:227], v[208:211]\n"
      "ds_read_b128 v[224:227], v33 offset:4032\n"
      "s_waitcnt lgkmcnt(3)\n"
      "v_mfma_f32_16x16x32_f16 v[208:211], v[120:123], v[212:215], v[208:211]\n"
      "ds_read_b128 v[212:215], v33 offset:4096\n"
      "v_cvt_pk_bf16_f32 v24, v204, v206\n"
      "v_cvt_pk_bf16_f32 v25, v205, v207\n"
      "ds_write2st64_b32 v39, v24, v25 offset0:6 offset1:14\n"
      "s_cmp_lg_u32 s13, 3\n"
      "s_cbranch_scc1 .Lcs23%=\n"
      "buffer_store_dwordx4 v[56:59], v13, s[24:27], s56 offen\n"
      ".Lcs23%=:\n"
      "v_mfma_f32_16x16x32_f16 v[208:211], v[228:231], v[140:143], v[208:211]\n"
      "v_mfma_f32_16x16x32_f16 v[208:211], v[124:127], v[216:219], v[208:211]\n"
      "s_waitcnt lgkmcnt(2)\n"
      "v_mfma_f32_16x16x32_f16 v[208:211], v[128:131], v[220:223], v[208:211]\n"
      "v_mfma_f32_16x16x32_f16 v[208:211], v[232:235], v[152:155], v[208:211]\n"
      "v_mfma_f32_16x16x32_f16 v[208:211], v[132:135], v[224:227], v[208:211]\n"
      "s_waitcnt lgkmcnt(1)\n"
      "v_mfma_f32_16x16x32_f16 v[208:211], v[136:139], v[212:215], v[208:211]\n"
      "s_nop 7\n"
      "v_cvt_pk_bf16_f32 v24, v208, v210\n"
      "v_cvt_pk_bf16_f32 v25, v209, v211\n"
      "ds_write2st64_b32 v39, v24, v25 offset0:7 offset1:15\n"
      "s_waitcnt lgkmcnt(0)\n"
      "s_barrier\n"
      "ds_read_b64_tr_b16 v[64:65], v40 offset:1536\n"
      "ds_read_b64_tr_b16 v[66:67], v40 offset:36608\n"
      "s_cmp_lg_u32 s33, 0\n"
      "s_cbranch_scc1 .Lcb%=\n"
      "ds_read_u16_d16_hi v252, v8 offset:56384\n"
      "ds_read_u16_d16_hi v253, v8 offset:65152\n"
      "s_waitcnt lgkmcnt(0)\n"
      "v_pk_mul_f32 v[252:253], v[252:253], v[254:255]\n"
      "v_cvt_pk_bf16_f32 v252, v252, v253\n"
      "buffer_atomic_pk_add_bf16 v252, v251, s[24:27], s41 offen\n"
      ".Lcb%=:\n"
      "s_waitcnt lgkmcnt(1)\n"
      "v_lshlrev_b32 v60, 16, v64\n"
      "v_and_b32 v64, 0xffff0000, v64\n"
      "v_fmac_f32 v68, s72, v60\n"
      "v_fmac_f32 v69, s73, v64\n"
      "v_lshlrev_b32 v61, 16, v65\n"
      "v_and_b32 v65, 0xffff0000, v65\n"
      "v_fmac_f32 v70, s74, v61\n"
      "v_fmac_f32 v71, s75, v65\n"
      "s_waitcnt lgkmcnt(0)\n"
      "v_lshlrev_b32 v62, 16, v66\n"
      "v_and_b32 v66, 0xffff0000, v66\n"
      "v_fmac_f32 v26, s76, v62\n"
      "v_fmac_f32 v27, s77, v66\n"
      "v_lshlrev_b32 v63, 16, v67\n"
      "v_and_b32 v67, 0xffff0000, v67\n"
      "v_fmac_f32 v28, s78, v63\n"
      "v_fmac_f32 v29, s79, v67\n"
      "v_cvt_pk_bf16_f32 v60, v68, v69\n"
      "v_cvt_pk_bf16_f32 v61, v70, v71\n"
      "v_cvt_pk_bf16_f32 v62, v26, v27\n"
      "v_cvt_pk_bf16_f32 v63, v28, v29\n"
      "v_and_or_b32 v13, v12, s58, v42\n"
      "buffer_store_dwordx4 v[60:63], v13, s[24:27], s57 offen\n"
      ".LEx%=:\ns_endpgm\n"
      ".Lcsgsk%=:\n"
      "s_sleep 4\n"
      "s_sub_u32 s59, s59, 1\n"
      "s_cbranch_scc1 .Lcsgok%=\n"
      "buffer_load_dwordx4 v[64:67], v42, s[16:19], s62 offen sc1\n"
      "s_waitcnt vmcnt(0)\n"
      "s_branch .Lcsgk%=\n"
      ".Lcsgsh%=:\n"
      "s_sleep 4\n"
      "s_sub_u32 s59, s59, 1\n"
      "s_cbranch_scc1 .Lcsgoh%=\n"
      "buffer_load_dwordx4 v[68:71], v42, s[16:19], s63 offen sc1\n"
      "s_waitcnt vmcnt(0)\n"
      "s_branch .Lcsgh%=\n"
      ".Lch%=:\n"
      "s_movk_i32 s59, 0x7fff\n"
      ".Lchl%=:\n"
      "s_waitcnt vmcnt(0)\n"
      "v_cmp_ne_u32 vcc, s15, v253\n"
      "v_not_b32 v255, v252\n"
      "s_sub_u32 s59, s59, 1\n"
      "s_cbranch_scc1 .Lchk%=\n"
      "s_cbranch_vccz .Lchk%=\n"
      "s_sleep 1\n"
      "buffer_load_dwordx2 v[252:253], v9, s[16:19], s43 offen sc1\n"
      "s_branch .Lchl%=\n"
      ".Lchk%=:\n"
      "v_and_b32 v31, 0x1ff, v255\n"
      "v_bfe_u32 v8, v255, 9, 2\n"
      "v_lshl_add_u32 v31, v31, 2, v8\n"
      "v_mov_b32 v8, 64\n"
      "v_add_u32 v31, 0x27700, v31\n"
      "ds_write_b8 v31, v8 \n"
      "v_and_b32 v31, 1, v255\n"
      "v_and_b32 v8, 0x7fe, v255\n"
      "v_lshl_add_u32 v8, v31, 11, v8\n"
      "v_mul_u32_u24 v31, 0x1120, v10\n"
      "v_add_u32 v8, v8, v31\n"
      "v_lshlrev_b32 v31, 1, v10\n"
      "v_add_u32 v31, 0x27600, v31\n"
      "v_and_b32 v255, 0x7ff, v255\n"
      "v_lshl_add_u32 v251, v255, 11, v10\n"
      "v_and_b32 v252, 32, v43\n"
      "v_lshl_or_b32 v251, v252, 25, v251\n"
      "ds_read_b64 v[254:255], v31 \n"
      "v_mov_b32 v252, 0\n"
      "v_mov_b32 v253, 0\n"
      "s_waitcnt lgkmcnt(0)\n"
      "s_branch .Lcho%=\n"
      ".LG%=:\n"
      "s_setprio 3\n"
      "s_mul_i32 s35, s48, 14\n"
      "s_add_u32 s35, s35, s49\n"
      "s_and_b32 s55, s33, 3\n"
      "s_mul_i32 s50, s55, 1835008\n"
      "s_lshr_b32 s51, s35, 1\n"
      "s_lshl_b32 s51, s51, 7\n"
      "s_add_u32 s50, s50, s51\n"
      "v_lshrrev_b32 v37, 3, v43\n"
      "v_and_b32 v38, 7, v43\n"
      "v_mul_u32_u24 v37, 0xe000, v37\n"
      "v_lshlrev_b32 v38, 4, v38\n"
      "v_add3_u32 v32, v37, v38, s50\n"
      "s_cmp_lt_u32 s34, 112\n"
      "s_cselect_b32 s26, 0x700000, 0\n"
      "s_mov_b32 s27, 0x20000\n"
      "s_bitcmp1_b32 s49, 0\n"
      "s_waitcnt lgkmcnt(0)\n"
      "s_load_dword s15, s[88:89], 0x0\n"
      "s_cselect_b32 s24, s82, s80\n"
      "s_cselect_b32 s25, s83, s81\n"
      "s_and_b32 s25, s25, 0xffff\n"
      "s_mov_b32 s16, s88\n"
      "s_and_b32 s17, s89, 0xffff\n"
      "s_mov_b32 s18, 0x6e0000\n"
      "s_mov_b32 s19, 0x20000\n"
      "s_mov_b32 s36, s86\n"
      "s_and_b32 s37, s87, 0xffff\n"
      "s_barrier\n"
      "s_sleep 20\n"
      "s_mov_b32 s60, 0\n"
      "buffer_load_dwordx4 v[64:67], v32, s[24:27], s60 offen nt\n"
      "s_sleep 2\n"
      "s_mov_b32 s60, 7168\n"
      "buffer_load_dwordx4 v[68:71], v32, s[24:27], s60 offen nt\n"
      "s_sleep 2\n"
      "s_mov_b32 s60, 14336\n"
      "buffer_load_dwordx4 v[72:75], v32, s[24:27], s60 offen nt\n"
      "s_sleep 2\n"
      "s_mov_b32 s60, 21504\n"
      "buffer_load_dwordx4 v[76:79], v32, s[24:27], s60 offen nt\n"
      "s_sleep 2\n"
      "s_mov_b32 s60, 28672\n"
      "buffer_load_dwordx4 v[80:83], v32, s[24:27], s60 offen nt\n"
      "s_sleep 2\n"
      "s_mov_b32 s60, 35840\n"
      "buffer_load_dwordx4 v[84:87], v32, s[24:27], s60 offen nt\n"
      "s_sleep 2\n"
      "s_mov_b32 s60, 43008\n"
      "buffer_load_dwordx4 v[88:91], v32, s[24:27], s60 offen nt\n"
      "s_sleep 2\n"
      "s_mov_b32 s60, 50176\n"
      "buffer_load_dwordx4 v[92:95], v32, s[24:27], s60 offen nt\n"
      "s_sleep 2\n"
      "s_mov_b32 s60, 458752\n"
      "buffer_load_dwordx4 v[96:99], v32, s[24:27], s60 offen nt\n"
      "s_sleep 2\n"
      "s_waitcnt lgkmcnt(0)\n"
      "s_add_u32 s15, s15, 0x9e3779b1\n"
      "v_mov_b32 v47, s15\n"
      "s_cmp_ge_u32 s34, 112\n"
      "s_cbranch_scc1 .LT%=\n"
      "v_mov_b32 v48, 0x22680\n"
      "ds_read_b32 v48, v48 \n"
      "s_waitcnt lgkmcnt(0)\n"
      "v_readfirstlane_b32 s12, v48\n"
      "s_mov_b32 s62, 0x05040100\n"
      "s_mov_b32 s63, 0x07060302\n"
      "s_lshl_b32 s42, s34, 3\n"
      "s_add_u32 s42, s42, 2048\n"
      "s_lshl_b32 s43, s48, 8\n"
      "s_and_b32 s50, s49, 1\n"
      "s_lshl_b32 s50, s50, 7\n"
      "s_add_u32 s43, s43, s50\n"
      "s_add_u32 s43, s43, 65536\n"
      "s_mov_b32 s20, s64\n"
      "s_and_b32 s21, s65, 0xffff\n"
      "s_mov_b32 s22, 0x400000\n"
      "s_mov_b32 s23, 0x20000\n"
      "v_lshrrev_b32 v39, 4, v43\n"
      "v_and_b32 v38, 15, v43\n"
      "v_lshlrev_b32 v38, 4, v38\n"
      "v_bfe_u32 v33, v43, 2, 2\n"
      "v_and_b32 v37, 1, v43\n"
      "v_lshl_add_u32 v33, v33, 1, v37\n"
      "v_lshlrev_b32 v33, 3, v33\n"
      "v_and_b32 v37, 2, v43\n"
      "v_lshl_add_u32 v37, v39, 2, v37\n"
      "s_lshl_b32 s50, s55, 9\n"
      "v_lshl_add_u32 v36, v37, 3, s50\n"
      "s_lshl_b32 s51, s55, 4\n"
      "v_lshrrev_b32 v40, 5, v43\n"
      "v_lshl_add_u32 v40, v40, 2, s51\n"
      "s_lshl_b32 s51, s55, 2\n"
      "s_add_u32 s51, s51, 140800\n"
      "v_mov_b32 v41, s51\n"
      "v_mov_b32 v42, 0x22600\n"
      "v_lshl_add_u32 v37, v43, 3, s50\n"
      "v_add_u32 v35, 0x22700, v37\n"
      "s_lshl_b32 s50, s55, 11\n"
      "s_add_u32 s50, s50, 141056\n"
      "v_lshl_add_u32 v34, v39, 9, v38\n"
      "v_add_u32 v34, s50, v34\n"
      "s_lshr_b32 s45, s35, 1\n"
      "s_lshl_b32 s45, s45, 16\n"
      "s_and_b32 s50, s35, 1\n"
      "s_lshl_b32 s50, s50, 11\n"
      "s_add_u32 s45, s45, s50\n"
      "s_add_u32 s45, s45, 524288\n"
      "s_mov_b32 s56, 0x3f80\n"
      "s_mov_b32 s57, 0x3f800000\n"
      "s_movk_i32 s61, 0x7ff\n"
      "s_mov_b32 s58, 131072\n"
      "s_mov_b32 s54, 0\n"
      "s_mov_b64 s[66:67], 0\n"
      "s_movk_i32 s70, 427\n"
      "s_waitcnt vmcnt(8)\n"
      "s_nop 0\n"
      "s_mov_b32 s60, 465920\n"
      "buffer_load_dwordx4 v[100:103], v32, s[24:27], s60 offen nt\n"
      "s_waitcnt vmcnt(8)\n"
      "s_nop 0\n"
      "s_mov_b32 s60, 473088\n"
      "buffer_load_dwordx4 v[104:107], v32, s[24:27], s60 offen nt\n"
      "v_perm_b32 v192, v68, v64, s62\n"
      "v_perm_b32 v196, v68, v64, s63\n"
      "v_perm_b32 v200, v69, v65, s62\n"
      "v_perm_b32 v204, v69, v65, s63\n"
      "v_perm_b32 v208, v70, v66, s62\n"
      "v_perm_b32 v212, v70, v66, s63\n"
      "v_perm_b32 v216, v71, v67, s62\n"
      "v_perm_b32 v220, v71, v67, s63\n"
      "s_waitcnt vmcnt(8)\n"
      "s_nop 0\n"
      "s_mov_b32 s60, 480256\n"
      "buffer_load_dwordx4 v[108:111], v32, s[24:27], s60 offen nt\n"
      "s_waitcnt vmcnt(8)\n"
      "s_nop 0\n"
      "s_mov_b32 s60, 487424\n"
      "buffer_load_dwordx4 v[112:115], v32, s[24:27], s60 offen nt\n"
      "v_perm_b32 v193, v76, v72, s62\n"
      "v_perm_b32 v197, v76, v72, s63\n"
      "v_perm_b32 v201, v77, v73, s62\n"
      "v_perm_b32 v205, v77, v73, s63\n"
      "v_perm_b32 v209, v78, v74, s62\n"
      "v_perm_b32 v213, v78, v74, s63\n"
      "v_perm_b32 v217, v79, v75, s62\n"
      "v_perm_b32 v221, v79, v75, s63\n"
      "s_waitcnt vmcnt(8)\n"
      "s_nop 0\n"
      "s_mov_b32 s60, 494592\n"
      "buffer_load_dwordx4 v[116:119], v32, s[24:27], s60 offen nt\n"
      "s_waitcnt vmcnt(8)\n"
      "s_nop 0\n"
      "s_mov_b32 s60, 501760\n"
      "buffer_load_dwordx4 v[120:123], v32, s[24:27], s60 offen nt\n"
      "v_perm_b32 v194, v84, v80, s62\n"
      "v_perm_b32 v198, v84, v80, s63\n"
      "v_perm_b32 v202, v85, v81, s62\n"
      "v_perm_b32 v206, v85, v81, s63\n"
      "v_perm_b32 v210, v86, v82, s62\n"
      "v_perm_b32 v214, v86, v82, s63\n"
      "v_perm_b32 v218, v87, v83, s62\n"
      "v_perm_b32 v222, v87, v83, s63\n"
      "s_waitcnt vmcnt(8)\n"
      "s_nop 0\n"
      "s_mov_b32 s60, 508928\n"
      "buffer_load_dwordx4 v[124:127], v32, s[24:27], s60 offen nt\n"
      "s_waitcnt vmcnt(8)\n"
      "s_nop 0\n"
      "s_mov_b32 s60, 917504\n"
      "buffer_load_dwordx4 v[128:131], v32, s[24:27], s60 offen nt\n"
      "v_perm_b32 v195, v92, v88, s62\n"
      "v_perm_b32 v199, v92, v88, s63\n"
      "v_perm_b32 v203, v93, v89, s62\n"
      "v_perm_b32 v207, v93, v89, s63\n"
      "v_perm_b32 v211, v94, v90, s62\n"
      "v_perm_b32 v215, v94, v90, s63\n"
      "v_perm_b32 v219, v95, v91, s62\n"
      "v_perm_b32 v223, v95, v91, s63\n"
      "s_waitcnt vmcnt(8)\n"
      "s_nop 0\n"
      "s_mov_b32 s60, 924672\n"
      "buffer_load_dwordx4 v[132:135], v32, s[24:27], s60 offen nt\n"
      "s_waitcnt vmcnt(8)\n"
      "s_nop 0\n"
      "s_mov_b32 s60, 931840\n"
      "buffer_load_dwordx4 v[136:139], v32, s[24:27], s60 offen nt\n"
      "v_perm_b32 v64, v100, v96, s62\n"
      "v_perm_b32 v68, v100, v96, s63\n"
      "v_perm_b32 v72, v101, v97, s62\n"
      "v_perm_b32 v76, v101, v97, s63\n"
      "v_perm_b32 v80, v102, v98, s62\n"
      "v_perm_b32 v84, v102, v98, s63\n"
      "v_perm_b32 v88, v103, v99, s62\n"
      "v_perm_b32 v92, v103, v99, s63\n"
      "s_waitcnt vmcnt(8)\n"
      "s_nop 0\n"
      "s_mov_b32 s60, 939008\n"
      "buffer_load_dwordx4 v[140:143], v32, s[24:27], s60 offen nt\n"
      "s_waitcnt vmcnt(8)\n"
      "s_nop 0\n"
      "s_mov_b32 s60, 946176\n"
      "buffer_load_dwordx4 v[144:147], v32, s[24:27], s60 offen nt\n"
      "v_perm_b32 v65, v108, v104, s62\n"
      "v_perm_b32 v69, v108, v104, s63\n"
      "v_perm_b32 v73, v109, v105, s62\n"
      "v_perm_b32 v77, v109, v105, s63\n"
      "v_perm_b32 v81, v110, v106, s62\n"
      "v_perm_b32 v85, v110, v106, s63\n"
      "v_perm_b32 v89, v111, v107, s62\n"
      "v_perm_b32 v93, v111, v107, s63\n"
      "s_waitcnt vmcnt(8)\n"
      "s_nop 0\n"
      "s_mov_b32 s60, 953344\n"
      "buffer_load_dwordx4 v[148:151], v32, s[24:27], s60 offen nt\n"
      "s_waitcnt vmcnt(8)\n"
      "s_nop 0\n"
      "s_mov_b32 s60, 960512\n"
      "buffer_load_dwordx4 v[152:155], v32, s[24:27], s60 offen nt\n"
      "v_perm_b32 v66, v116, v112, s62\n"
      "v_perm_b32 v70, v116, v112, s63\n"
      "v_perm_b32 v74, v117, v113, s62\n"
      "v_perm_b32 v78, v117, v113, s63\n"
      "v_perm_b32 v82, v118, v114, s62\n"
      "v_perm_b32 v86, v118, v114, s63\n"
      "v_perm_b32 v90, v119, v115, s62\n"
      "v_perm_b32 v94, v119, v115, s63\n"
      "s_waitcnt vmcnt(8)\n"
      "s_nop 0\n"
      "s_mov_b32 s60, 967680\n"
      "buffer_load_dwordx4 v[156:159], v32, s[24:27], s60 offen nt\n"
      "s_waitcnt vmcnt(8)\n"
      "s_nop 0\n"
      "s_mov_b32 s60, 1376256\n"
      "buffer_load_dwordx4 v[160:163], v32, s[24:27], s60 offen nt\n"
      "v_perm_b32 v67, v124, v120, s62\n"
      "v_perm_b32 v71, v124, v120, s63\n"
      "v_perm_b32 v75, v125, v121, s62\n"
      "v_perm_b32 v79, v125, v121, s63\n"
      "v_perm_b32 v83, v126, v122, s62\n"
      "v_perm_b32 v87, v126, v122, s63\n"
      "v_perm_b32 v91, v127, v123, s62\n"
      "v_perm_b32 v95, v127, v123, s63\n"
      "s_waitcnt vmcnt(8)\n"
      "s_nop 0\n"
      "s_mov_b32 s60, 1383424\n"
      "buffer_load_dwordx4 v[164:167], v32, s[24:27], s60 offen nt\n"
      "s_waitcnt vmcnt(8)\n"
      "s_nop 0\n"
      "s_mov_b32 s60, 1390592\n"
      "buffer_load_dwordx4 v[168:171], v32, s[24:27], s60 offen nt\n"
      "v_perm_b32 v96, v132, v128, s62\n"
      "v_perm_b32 v100, v132, v128, s63\n"
      "v_perm_b32 v104, v133, v129, s62\n"
      "v_perm_b32 v108, v133, v129, s63\n"
      "v_perm_b32 v112, v134, v130, s62\n"
      "v_perm_b32 v116, v134, v130, s63\n"
      "v_perm_b32 v120, v135, v131, s62\n"
      "v_perm_b32 v124, v135, v131, s63\n"
      "s_waitcnt vmcnt(8)\n"
      "s_nop 0\n"
      "s_mov_b32 s60, 1397760\n"
      "buffer_load_dwordx4 v[172:175], v32, s[24:27], s60 offen nt\n"
      "s_waitcnt vmcnt(8)\n"
      "s_nop 0\n"
      "s_mov_b32 s60, 1404928\n"
      "buffer_load_dwordx4 v[176:179], v32, s[24:27], s60 offen nt\n"
      "v_perm_b32 v97, v140, v136, s62\n"
      "v_perm_b32 v101, v140, v136, s63\n"
      "v_perm_b32 v105, v141, v137, s62\n"
      "v_perm_b32 v109, v141, v137, s63\n"
      "v_perm_b32 v113, v142, v138, s62\n"
      "v_perm_b32 v117, v142, v138, s63\n"
      "v_perm_b32 v121, v143, v139, s62\n"
      "v_perm_b32 v125, v143, v139, s63\n"
      "s_waitcnt vmcnt(8)\n"
      "s_nop 0\n"
      "s_mov_b32 s60, 1412096\n"
      "buffer_load_dwordx4 v[180:183], v32, s[24:27], s60 offen nt\n"
      "s_waitcnt vmcnt(8)\n"
      "s_nop 0\n"
      "s_mov_b32 s60, 1419264\n"
      "buffer_load_dwordx4 v[184:187], v32, s[24:27], s60 offen nt\n"
      "v_perm_b32 v98, v148, v144, s62\n"
      "v_perm_b32 v102, v148, v144, s63\n"
      "v_perm_b32 v106, v149, v145, s62\n"
      "v_perm_b32 v110, v149, v145, s63\n"
      "v_perm_b32 v114, v150, v146, s62\n"
      "v_perm_b32 v118, v150, v146, s63\n"
      "v_perm_b32 v122, v151, v147, s62\n"
      "v_perm_b32 v126, v151, v147, s63\n"
      "s_waitcnt vmcnt(8)\n"
      "s_nop 0\n"
      "s_mov_b32 s60, 1426432\n"
      "buffer_load_dwordx4 v[188:191], v32, s[24:27], s60 offen nt\n"
      "ds_write_b32 v41, v47 offset:80\n"
      "s_waitcnt vmcnt(8)\n"
      "s_nop 0\n"
      "v_perm_b32 v99, v156, v152, s62\n"
      "v_perm_b32 v103, v156, v152, s63\n"
      "v_perm_b32 v107, v157, v153, s62\n"
      "v_perm_b32 v111, v157, v153, s63\n"
      "v_perm_b32 v115, v158, v154, s62\n"
      "v_perm_b32 v119, v158, v154, s63\n"
      "v_perm_b32 v123, v159, v155, s62\n"
      "v_perm_b32 v127, v159, v155, s63\n"
      "s_waitcnt vmcnt(7)\n"
      "s_nop 0\n"
      "s_memrealtime s[68:69]\n"
      "s_waitcnt vmcnt(6)\n"
      "s_nop 0\n"
      "s_waitcnt lgkmcnt(0)\n"
      "s_sub_u32 s68, s68, s12\n"
      "s_cmp_lt_u32 s68, s70\n"
      "s_cbranch_scc1 .Lgn25%=\n"
      "buffer_load_dwordx2 v[8:9], v33, s[16:19], s43 offen sc1\n"
      "s_mov_b32 s70, -1\n"
      ".Lgn25%=:\n"
      "s_memrealtime s[68:69]\n"
      "v_perm_b32 v128, v164, v160, s62\n"
      "v_perm_b32 v132, v164, v160, s63\n"
      "v_perm_b32 v136, v165, v161, s62\n"
      "v_perm_b32 v140, v165, v161, s63\n"
      "v_perm_b32 v144, v166, v162, s62\n"
      "v_perm_b32 v148, v166, v162, s63\n"
      "v_perm_b32 v152, v167, v163, s62\n"
      "v_perm_b32 v156, v167, v163, s63\n"
      "s_waitcnt vmcnt(5)\n"
      "s_nop 0\n"
      "s_waitcnt lgkmcnt(0)\n"
      "s_sub_u32 s68, s68, s12\n"
      "s_cmp_lt_u32 s68, s70\n"
      "s_cbranch_scc1 .Lgn26%=\n"
      "buffer_load_dwordx2 v[8:9], v33, s[16:19], s43 offen sc1\n"
      "s_mov_b32 s70, -1\n"
      ".Lgn26%=:\n"
      "s_memrealtime s[68:69]\n"
      "s_waitcnt vmcnt(4)\n"
      "s_nop 0\n"
      "s_waitcnt lgkmcnt(0)\n"
      "s_sub_u32 s68, s68, s12\n"
      "s_cmp_lt_u32 s68, s70\n"
      "s_cbranch_scc1 .Lgn27%=\n"
      "buffer_load_dwordx2 v[8:9], v33, s[16:19], s43 offen sc1\n"
      "s_mov_b32 s70, -1\n"
      ".Lgn27%=:\n"
      "s_memrealtime s[68:69]\n"
      "v_perm_b32 v129, v172, v168, s62\n"
      "v_perm_b32 v133, v172, v168, s63\n"
      "v_perm_b32 v137, v173, v169, s62\n"
      "v_perm_b32 v141, v173, v169, s63\n"
      "v_perm_b32 v145, v174, v170, s62\n"
      "v_perm_b32 v149, v174, v170, s63\n"
      "v_perm_b32 v153, v175, v171, s62\n"
      "v_perm_b32 v157, v175, v171, s63\n"
      "s_waitcnt vmcnt(3)\n"
      "s_nop 0\n"
      "s_waitcnt lgkmcnt(0)\n"
      "s_sub_u32 s68, s68, s12\n"
      "s_cmp_lt_u32 s68, s70\n"
      "s_cbranch_scc1 .Lgn28%=\n"
      "buffer_load_dwordx2 v[8:9], v33, s[16:19], s43 offen sc1\n"
      "s_mov_b32 s70, -1\n"
      ".Lgn28%=:\n"
      "s_memrealtime s[68:69]\n"
      "s_waitcnt vmcnt(2)\n"
      "s_nop 0\n"
      "s_waitcnt lgkmcnt(0)\n"
      "s_sub_u32 s68, s68, s12\n"
      "s_cmp_lt_u32 s68, s70\n"
      "s_cbranch_scc1 .Lgn29%=\n"
      "buffer_load_dwordx2 v[8:9], v33, s[16:19], s43 offen sc1\n"
      "s_mov_b32 s70, -1\n"
      ".Lgn29%=:\n"
      "s_memrealtime s[68:69]\n"
      "v_perm_b32 v130, v180, v176, s62\n"
      "v_perm_b32 v134, v180, v176, s63\n"
      "v_perm_b32 v138, v181, v177, s62\n"
      "v_perm_b32 v142, v181, v177, s63\n"
      "v_perm_b32 v146, v182, v178, s62\n"
      "v_perm_b32 v150, v182, v178, s63\n"
      "v_perm_b32 v154, v183, v179, s62\n"
      "v_perm_b32 v158, v183, v179, s63\n"
      "s_waitcnt vmcnt(1)\n"
      "s_nop 0\n"
      "s_waitcnt lgkmcnt(0)\n"
      "s_sub_u32 s68, s68, s12\n"
      "s_cmp_lt_u32 s68, s70\n"
      "s_cbranch_scc1 .Lgn30%=\n"
      "buffer_load_dwordx2 v[8:9], v33, s[16:19], s43 offen sc1\n"
      "s_mov_b32 s70, -1\n"
      ".Lgn30%=:\n"
      "s_memrealtime s[68:69]\n"
      "s_waitcnt vmcnt(0)\n"
      "s_nop 0\n"
      "s_waitcnt lgkmcnt(0)\n"
      "s_sub_u32 s68, s68, s12\n"
      "s_cmp_lt_u32 s68, s70\n"
      "s_cbranch_scc1 .Lgn31%=\n"
      "buffer_load_dwordx2 v[8:9], v33, s[16:19], s43 offen sc1\n"
      "s_mov_b32 s70, -1\n"
      ".Lgn31%=:\n"
      "v_perm_b32 v131, v188, v184, s62\n"
      "v_perm_b32 v135, v188, v184, s63\n"
      "v_perm_b32 v139, v189, v185, s62\n"
      "v_perm_b32 v143, v189, v185, s63\n"
      "v_perm_b32 v147, v190, v186, s62\n"
      "v_perm_b32 v151, v190, v186, s63\n"
      "v_perm_b32 v155, v191, v187, s62\n"
      "v_perm_b32 v159, v191, v187, s63\n"
      "s_movk_i32 s59, 0x3fff\n"
      "s_cmp_eq_u32 s70, -1\n"
      "s_cbranch_scc1 .Lghc%=\n"
      ".Lgpc3_%=:\n"
      "s_memrealtime s[68:69]\n"
      "s_waitcnt lgkmcnt(0)\n"
      "s_sub_u32 s68, s68, s12\n"
      "s_cmp_ge_u32 s68, 435\n"
      "s_cbranch_scc1 .Lgpd3_%=\n"
      "s_sleep 1\n"
      "s_branch .Lgpc3_%=\n"
      ".Lgpd3_%=:\n"
      ".Lgh%=:\n"
      "buffer_load_dwordx2 v[8:9], v33, s[16:19], s43 offen sc1\n"
      ".Lghc%=:\n"
      "s_waitcnt vmcnt(0)\n"
      "v_cmp_ne_u32 vcc, s15, v9\n"
      "v_bitop3_b32 v10, v8, s61, v8 bitop3:0x0c\n"
      "s_sub_u32 s59, s59, 1\n"
      "s_cbranch_vccz .Lgho%=\n"
      "s_sleep 2\n"
      "s_cbranch_scc0 .Lgh%=\n"
      ".Lgho%=:\n"
      "v_lshrrev_b32 v45, 9, v10\n"
      "v_lshl_add_u32 v11, v10, 11, v36\n"
      "v_mul_u32_u24 v45, 0xf000, v45\n"
      "v_lshl_add_u32 v45, v10, 3, v45\n"
      "buffer_load_dwordx2 v[12:13], off, s[16:19], s43 offset:64 sc1\n"
      "buffer_load_dwordx2 v[62:63], v45, s[16:19], s58 offen sc1\n"
      "buffer_load_dwordx4 v[16:19], v11, s[20:23], 0 offen offset:0\n"
      "buffer_load_dwordx4 v[20:23], v11, s[20:23], 0 offen offset:128\n"
      "buffer_load_dwordx4 v[24:27], v11, s[20:23], 0 offen offset:256\n"
      "buffer_load_dwordx4 v[28:31], v11, s[20:23], 0 offen offset:384\n"
      "ds_write_b32 v41, v47 offset:64\n"
      ".Lghx%=:\n"
      "s_movk_i32 s59, 0x7fff\n"
      "s_waitcnt vmcnt(4)\n"
      "s_nop 0\n"
      ".Lgs%=:\n"
      "v_cmp_ne_u32 vcc, s15, v63\n"
      "v_lshlrev_b32 v10, 16, v62\n"
      "v_and_b32 v44, 0xffff0000, v62\n"
      "s_cbranch_vccnz .Lgss%=\n"
      ".Lgso%=:\n"
      "v_rcp_f32 v44, v44\n"
      "v_xor_b32 v10, 0x80000000, v10\n"
      "v_readfirstlane_b32 s68, v13\n"
      "v_readfirstlane_b32 s69, v12\n"
      "s_cmp_eq_u32 s68, s15\n"
      "s_cselect_b32 s12, s69, s12\n"
      "v_mov_b32 v12, s12\n"
      "ds_write_b32 v42, v12 offset:128\n"
      "s_waitcnt vmcnt(3)\n"
      "v_dot2_f32_bf16 v168, v16, s56, v10\n"
      "v_dot2_f32_bf16 v169, v16, s57, v10\n"
      "v_dot2_f32_bf16 v170, v17, s56, v10\n"
      "v_dot2_f32_bf16 v171, v17, s57, v10\n"
      "v_dot2_f32_bf16 v172, v18, s56, v10\n"
      "v_dot2_f32_bf16 v173, v18, s57, v10\n"
      "v_dot2_f32_bf16 v174, v19, s56, v10\n"
      "v_dot2_f32_bf16 v175, v19, s57, v10\n"
      "v_cvt_pk_bf16_f32 v16, v168, v169\n"
      "v_cvt_pk_bf16_f32 v17, v170, v171\n"
      "v_cvt_pk_bf16_f32 v18, v172, v173\n"
      "v_cvt_pk_bf16_f32 v19, v174, v175\n"
      "ds_bpermute_b32 v44, v40, v44\n"
      "s_waitcnt vmcnt(2)\n"
      "v_dot2_f32_bf16 v176, v20, s56, v10\n"
      "v_dot2_f32_bf16 v177, v20, s57, v10\n"
      "v_dot2_f32_bf16 v178, v21, s56, v10\n"
      "v_dot2_f32_bf16 v179, v21, s57, v10\n"
      "v_dot2_f32_bf16 v180, v22, s56, v10\n"
      "v_dot2_f32_bf16 v181, v22, s57, v10\n"
      "v_dot2_f32_bf16 v182, v23, s56, v10\n"
      "v_dot2_f32_bf16 v183, v23, s57, v10\n"
      "v_cvt_pk_bf16_f32 v20, v176, v177\n"
      "v_cvt_pk_bf16_f32 v21, v178, v179\n"
      "v_cvt_pk_bf16_f32 v22, v180, v181\n"
      "v_cvt_pk_bf16_f32 v23, v182, v183\n"
      "s_waitcnt vmcnt(1)\n"
      "v_dot2_f32_bf16 v168, v24, s56, v10\n"
      "v_dot2_f32_bf16 v169, v24, s57, v10\n"
      "v_dot2_f32_bf16 v170, v25, s56, v10\n"
      "v_dot2_f32_bf16 v171, v25, s57, v10\n"
      "v_dot2_f32_bf16 v172, v26, s56, v10\n"
      "v_dot2_f32_bf16 v173, v26, s57, v10\n"
      "v_dot2_f32_bf16 v174, v27, s56, v10\n"
      "v_dot2_f32_bf16 v175, v27, s57, v10\n"
      "v_cvt_pk_bf16_f32 v24, v168, v169\n"
      "v_cvt_pk_bf16_f32 v25, v170, v171\n"
      "v_cvt_pk_bf16_f32 v26, v172, v173\n"
      "v_cvt_pk_bf16_f32 v27, v174, v175\n"
      "s_waitcnt vmcnt(0)\n"
      "v_dot2_f32_bf16 v176, v28, s56, v10\n"
      "v_dot2_f32_bf16 v177, v28, s57, v10\n"
      "v_dot2_f32_bf16 v178, v29, s56, v10\n"
      "v_dot2_f32_bf16 v179, v29, s57, v10\n"
      "v_dot2_f32_bf16 v180, v30, s56, v10\n"
      "v_dot2_f32_bf16 v181, v30, s57, v10\n"
      "v_dot2_f32_bf16 v182, v31, s56, v10\n"
      "v_dot2_f32_bf16 v183, v31, s57, v10\n"
      "v_cvt_pk_bf16_f32 v28, v176, v177\n"
      "v_cvt_pk_bf16_f32 v29, v178, v179\n"
      "v_cvt_pk_bf16_f32 v30, v180, v181\n"
      "v_cvt_pk_bf16_f32 v31, v182, v183\n"
      "v_mfma_f32_16x16x32_bf16 v[224:227], v[16:19], v[192:195], 0\n"
      "v_mfma_f32_16x16x32_bf16 v[228:231], v[16:19], v[196:199], 0\n"
      "v_mfma_f32_16x16x32_bf16 v[232:235], v[16:19], v[200:203], 0\n"
      "v_mfma_f32_16x16x32_bf16 v[236:239], v[16:19], v[204:207], 0\n"
      "v_mfma_f32_16x16x32_bf16 v[240:243], v[16:19], v[208:211], 0\n"
      "v_mfma_f32_16x16x32_bf16 v[244:247], v[16:19], v[212:215], 0\n"
      "v_mfma_f32_16x16x32_bf16 v[248:251], v[16:19], v[216:219], 0\n"
      "v_mfma_f32_16x16x32_bf16 v[252:255], v[16:19], v[220:223], 0\n"
      "v_mfma_f32_16x16x32_bf16 v[224:227], v[20:23], v[64:67], v[224:227]\n"
      "v_mfma_f32_16x16x32_bf16 v[228:231], v[20:23], v[68:71], v[228:231]\n"
      "v_mfma_f32_16x16x32_bf16 v[232:235], v[20:23], v[72:75], v[232:235]\n"
      "v_mfma_f32_16x16x32_bf16 v[236:239], v[20:23], v[76:79], v[236:239]\n"
      "v_mfma_f32_16x16x32_bf16 v[240:243], v[20:23], v[80:83], v[240:243]\n"
      "v_mfma_f32_16x16x32_bf16 v[244:247], v[20:23], v[84:87], v[244:247]\n"
      "v_mfma_f32_16x16x32_bf16 v[248:251], v[20:23], v[88:91], v[248:251]\n"
      "v_mfma_f32_16x16x32_bf16 v[252:255], v[20:23], v[92:95], v[252:255]\n"
      "v_mfma_f32_16x16x32_bf16 v[224:227], v[24:27], v[96:99], v[224:227]\n"
      "v_mfma_f32_16x16x32_bf16 v[228:231], v[24:27], v[100:103], v[228:231]\n"
      "v_mfma_f32_16x16x32_bf16 v[232:235], v[24:27], v[104:107], v[232:235]\n"
      "v_mfma_f32_16x16x32_bf16 v[236:239], v[24:27], v[108:111], v[236:239]\n"
      "v_mfma_f32_16x16x32_bf16 v[240:243], v[24:27], v[112:115], v[240:243]\n"
      "v_mfma_f32_16x16x32_bf16 v[244:247], v[24:27], v[116:119], v[244:247]\n"
      "v_mfma_f32_16x16x32_bf16 v[248:251], v[24:27], v[120:123], v[248:251]\n"
      "v_mfma_f32_16x16x32_bf16 v[252:255], v[24:27], v[124:127], v[252:255]\n"
      "v_mfma_f32_16x16x32_bf16 v[224:227], v[28:31], v[128:131], v[224:227]\n"
      "v_mfma_f32_16x16x32_bf16 v[228:231], v[28:31], v[132:135], v[228:231]\n"
      "v_mfma_f32_16x16x32_bf16 v[232:235], v[28:31], v[136:139], v[232:235]\n"
      "v_mfma_f32_16x16x32_bf16 v[236:239], v[28:31], v[140:143], v[236:239]\n"
      "s_nop 4\n"
      "v_add_f32_dpp v160, v226, v224 row_ror:8 row_mask:0xf bank_mask:0x3\n"
      "v_add_f32_dpp v164, v227, v225 row_ror:8 row_mask:0xf bank_mask:0x3\n"
      "v_mfma_f32_16x16x32_bf16 v[240:243], v[28:31], v[144:147], v[240:243]\n"
      "v_add_f32_dpp v161, v230, v228 row_ror:8 row_mask:0xf bank_mask:0x3\n"
      "v_add_f32_dpp v165, v231, v229 row_ror:8 row_mask:0xf bank_mask:0x3\n"
      "v_mfma_f32_16x16x32_bf16 v[244:247], v[28:31], v[148:151], v[244:247]\n"
      "v_add_f32_dpp v162, v234, v232 row_ror:8 row_mask:0xf bank_mask:0x3\n"
      "v_add_f32_dpp v166, v235, v233 row_ror:8 row_mask:0xf bank_mask:0x3\n"
      "v_mfma_f32_16x16x32_bf16 v[248:251], v[28:31], v[152:155], v[248:251]\n"
      "v_add_f32_dpp v163, v238, v236 row_ror:8 row_mask:0xf bank_mask:0x3\n"
      "v_add_f32_dpp v167, v239, v237 row_ror:8 row_mask:0xf bank_mask:0x3\n"
      "v_mfma_f32_16x16x32_bf16 v[252:255], v[28:31], v[156:159], v[252:255]\n"
      "v_add_f32_dpp v160, v240, v242 row_ror:8 row_mask:0xf bank_mask:0xc\n"
      "v_add_f32_dpp v164, v241, v243 row_ror:8 row_mask:0xf bank_mask:0xc\n"
      "v_add_f32_dpp v161, v244, v246 row_ror:8 row_mask:0xf bank_mask:0xc\n"
      "v_add_f32_dpp v165, v245, v247 row_ror:8 row_mask:0xf bank_mask:0xc\n"
      "s_nop 0\n"
      "v_add_f32_dpp v162, v248, v250 row_ror:8 row_mask:0xf bank_mask:0xc\n"
      "v_add_f32_dpp v166, v249, v251 row_ror:8 row_mask:0xf bank_mask:0xc\n"
      "s_nop 0\n"
      "v_add_f32_dpp v163, v252, v254 row_ror:8 row_mask:0xf bank_mask:0xc\n"
      "v_add_f32_dpp v167, v253, v255 row_ror:8 row_mask:0xf bank_mask:0xc\n"
      "ds_write_b128 v34, v[160:163] \n"
      "ds_write_b128 v34, v[164:167] offset:256\n"
      "ds_write_b32 v41, v47 \n"
      "s_movk_i32 s59, 0x7fff\n"
      ".Lgw%=:\n"
      "ds_read_b128 v[72:75], v42 \n"
      "ds_read_b64 v[64:65], v35 offset:0\n"
      "ds_read_b64 v[66:67], v35 offset:2048\n"
      "ds_read_b64 v[68:69], v35 offset:4096\n"
      "ds_read_b64 v[70:71], v35 offset:6144\n"
      "s_sub_u32 s59, s59, 1\n"
      "s_cbranch_scc1 .Lgwo%=\n"
      "s_waitcnt lgkmcnt(4)\n"
      "v_xor_b32 v72, s15, v72\n"
      "v_bitop3_b32 v72, s15, v73, v72 bitop3:0xbe\n"
      "v_bitop3_b32 v72, s15, v74, v72 bitop3:0xbe\n"
      "v_bitop3_b32 v72, s15, v75, v72 bitop3:0xbe\n"
      "v_cmp_ne_u32 vcc, 0, v72\n"
      "s_waitcnt lgkmcnt(0)\n"
      "s_nop 0\n"
      "s_cbranch_vccnz .Lgw%=\n"
      ".Lgwo%=:\n"
      "s_waitcnt lgkmcnt(0)\n"
      "s_bitcmp1_b32 s49, 0\n"
      "v_pk_add_f32 v[64:65], v[64:65], v[66:67]\n"
      "v_pk_add_f32 v[68:69], v[68:69], v[70:71]\n"
      "v_pk_add_f32 v[64:65], v[64:65], v[68:69]\n"
      "v_mul_f32 v64, v64, v44\n"
      "v_mul_f32 v65, v65, v44\n"
      "v_cvt_pk_bf16_f32 v46, v64, v65\n"
      "s_cbranch_scc1 .Lgu%=\n"
      "v_lshlrev_b32 v64, 16, v46\n"
      "v_and_b32 v65, 0xffff0000, v46\n"
      "v_mul_f32 v66, 0xbfb8aa3b, v64\n"
      "v_mul_f32 v67, 0xbfb8aa3b, v65\n"
      "v_exp_f32 v66, v66\n"
      "v_exp_f32 v67, v67\n"
      "v_add_f32 v66, 1.0, v66\n"
      "v_add_f32 v67, 1.0, v67\n"
      "v_rcp_f32 v66, v66\n"
      "v_rcp_f32 v67, v67\n"
      "v_mul_f32 v64, v64, v66\n"
      "v_mul_f32 v65, v65, v67\n"
      "v_cvt_pk_bf16_f32 v46, v64, v65\n"
      ".Lgu%=:\n"
      "buffer_store_dwordx2 v[46:47], v37, s[16:19], s45 offen sc1\n"
      "s_cmp_lg_u32 s33, 4\n"
      "s_cbranch_scc1 .Lga%=\n"
      "v_mov_b32 v64, s15\n"
      "v_mov_b32 v65, s15\n"
      "buffer_store_dwordx2 v[64:65], off, s[16:19], s42 sc1\n"
      ".Lga%=:\n"
      "s_cmp_eq_u32 s33, 7\n"
      "s_mov_b32 s24, s36\n"
      "s_mov_b32 s25, s37\n"
      "s_mov_b32 s26, 0x400000\n"
      "s_cbranch_scc1 .LZ%=\n"
      "s_endpgm\n"
      ".Lgss%=:\n"
      "s_sleep 1\n"
      "s_sub_u32 s59, s59, 1\n"
      "s_cbranch_scc1 .Lgso%=\n"
      "buffer_load_dwordx2 v[62:63], v45, s[16:19], s58 offen sc1\n"
      "s_waitcnt vmcnt(0)\n"
      "s_branch .Lgs%=\n"
      ".LD2%=:\n"
      "s_setprio 0\n"
      "s_mul_i32 s35, s48, 14\n"
      "s_add_u32 s35, s35, s49\n"
      "s_lshr_b32 s52, s35, 2\n"
      "s_and_b32 s54, s35, 3\n"
      "v_lshlrev_b32 v42, 4, v49\n"
      "s_lshl_b32 s50, s52, 18\n"
      "s_lshl_b32 s51, s54, 9\n"
      "s_add_u32 s50, s50, s51\n"
      "s_lshl_b32 s51, s33, 7\n"
      "s_add_u32 s50, s50, s51\n"
      "v_lshl_add_u32 v32, v50, 14, v42\n"
      "v_add_u32 v32, s50, v32\n"
      "v_lshrrev_b32 v41, 1, v49\n"
      "v_and_b32 v43, 1, v41\n"
      "v_mul_u32_u24 v36, 0x120, v41\n"
      "v_lshlrev_b32 v43, 4, v43\n"
      "v_sub_u32 v36, v36, v43\n"
      "v_and_b32 v43, 1, v49\n"
      "v_lshl_add_u32 v36, v43, 7, v36\n"
      "v_lshrrev_b32 v41, 4, v48\n"
      "v_lshl_add_u32 v36, v41, 4, v36\n"
      "v_and_b32 v43, 8, v48\n"
      "v_add_u32 v36, v36, v43\n"
      "s_lshr_b32 s53, s33, 1\n"
      "s_mul_i32 s50, s53, 1152\n"
      "s_and_b32 s55, s33, 1\n"
      "s_lshl_b32 s51, s55, 6\n"
      "s_add_u32 s50, s50, s51\n"
      "s_add_u32 s50, s50, 149504\n"
      "v_add_u32 v36, s50, v36\n"
      "v_and_b32 v43, 1, v41\n"
      "v_mul_u32_u24 v37, 0x120, v41\n"
      "v_lshlrev_b32 v43, 4, v43\n"
      "v_sub_u32 v37, v37, v43\n"
      "v_and_b32 v43, 15, v48\n"
      "v_lshl_add_u32 v37, v43, 4, v37\n"
      "v_add_u32 v37, 0x24800, v37\n"
      "v_lshlrev_b32 v35, 4, v48\n"
      "s_lshl_b32 s46, s52, 17\n"
      "s_lshl_b32 s50, s53, 16\n"
      "s_add_u32 s46, s46, s50\n"
      "s_lshl_b32 s50, s55, 10\n"
      "s_add_u32 s46, s46, s50\n"
      "s_add_u32 s46, s46, 524288\n"
      "v_and_b32 v41, 3, v50\n"
      "v_lshrrev_b32 v43, 5, v48\n"
      "v_lshl_add_u32 v41, v41, 1, v43\n"
      "v_mul_u32_u24 v38, 0x1c00, v41\n"
      "v_add_u32 v38, v38, v42\n"
      "s_lshl_b32 s50, s54, 2\n"
      "s_add_u32 s50, s50, s33\n"
      "s_mul_i32 s47, s50, 57344\n"
      "s_lshl_b32 s50, s52, 8\n"
      "s_add_u32 s47, s47, s50\n"
      "s_add_u32 s47, s47, 4194304\n"
      "s_mov_b32 s62, 0x05040100\n"
      "s_mov_b32 s63, 0x07060302\n"
      "s_lshl_b32 s50, s33, 2\n"
      "s_add_u32 s50, s50, 140864\n"
      "v_mov_b32 v41, s50\n"
      "v_mov_b32 v42, 0x22640\n"
      "s_movk_i32 s59, 0x3fff\n"
      ".Ldf%=:\n"
      "s_sleep 1\n"
      "ds_read_b128 v[240:243], v42\n"
      "ds_read_b32 v43, v41 offset:16\n"
      "s_sub_u32 s59, s59, 1\n"
      "s_cbranch_scc1 .Ldfo%=\n"
      "s_waitcnt lgkmcnt(0)\n"
      "v_xor_b32 v240, s15, v240\n"
      "v_bitop3_b32 v240, s15, v241, v240 bitop3:0xbe\n"
      "v_bitop3_b32 v240, s15, v242, v240 bitop3:0xbe\n"
      "v_bitop3_b32 v240, s15, v243, v240 bitop3:0xbe\n"
      "v_bitop3_b32 v240, s15, v43, v240 bitop3:0xbe\n"
      "v_cmp_ne_u32 vcc, 0, v240\n"
      "s_nop 1\n"
      "s_cbranch_vccnz .Ldf%=\n"
      ".Ldfo%=:\n"
      "s_mov_b32 s60, 0\n"
      "buffer_load_dwordx4 v[128:131], v32, s[28:31], s60 offen nt\n"
      "s_mov_b32 s60, 2048\n"
      "buffer_load_dwordx4 v[132:135], v32, s[28:31], s60 offen nt\n"
      "s_mov_b32 s60, 4096\n"
      "buffer_load_dwordx4 v[136:139], v32, s[28:31], s60 offen nt\n"
      "s_mov_b32 s60, 6144\n"
      "buffer_load_dwordx4 v[140:143], v32, s[28:31], s60 offen nt\n"
      "s_mov_b32 s60, 8192\n"
      "buffer_load_dwordx4 v[144:147], v32, s[28:31], s60 offen nt\n"
      "s_mov_b32 s60, 10240\n"
      "buffer_load_dwordx4 v[148:151], v32, s[28:31], s60 offen nt\n"
      "s_mov_b32 s60, 12288\n"
      "buffer_load_dwordx4 v[152:155], v32, s[28:31], s60 offen nt\n"
      "s_mov_b32 s60, 14336\n"
      "buffer_load_dwordx4 v[156:159], v32, s[28:31], s60 offen nt\n"
      "s_waitcnt vmcnt(7)\n"
      "s_nop 0\n"
      "s_mov_b32 s60, 131072\n"
      "buffer_load_dwordx4 v[160:163], v32, s[28:31], s60 offen nt\n"
      "s_waitcnt vmcnt(7)\n"
      "s_nop 0\n"
      "s_mov_b32 s60, 133120\n"
      "buffer_load_dwordx4 v[164:167], v32, s[28:31], s60 offen nt\n"
      "v_perm_b32 v64, v132, v128, s62\n"
      "v_perm_b32 v68, v132, v128, s63\n"
      "v_perm_b32 v72, v133, v129, s62\n"
      "v_perm_b32 v76, v133, v129, s63\n"
      "v_perm_b32 v80, v134, v130, s62\n"
      "v_perm_b32 v84, v134, v130, s63\n"
      "v_perm_b32 v88, v135, v131, s62\n"
      "v_perm_b32 v92, v135, v131, s63\n"
      "s_waitcnt vmcnt(7)\n"
      "s_nop 0\n"
      "s_mov_b32 s60, 135168\n"
      "buffer_load_dwordx4 v[168:171], v32, s[28:31], s60 offen nt\n"
      "s_waitcnt vmcnt(7)\n"
      "s_nop 0\n"
      "s_mov_b32 s60, 137216\n"
      "buffer_load_dwordx4 v[172:175], v32, s[28:31], s60 offen nt\n"
      "v_perm_b32 v65, v140, v136, s62\n"
      "v_perm_b32 v69, v140, v136, s63\n"
      "v_perm_b32 v73, v141, v137, s62\n"
      "v_perm_b32 v77, v141, v137, s63\n"
      "v_perm_b32 v81, v142, v138, s62\n"
      "v_perm_b32 v85, v142, v138, s63\n"
      "v_perm_b32 v89, v143, v139, s62\n"
      "v_perm_b32 v93, v143, v139, s63\n"
      "s_waitcnt vmcnt(7)\n"
      "s_nop 0\n"
      "s_mov_b32 s60, 139264\n"
      "buffer_load_dwordx4 v[176:179], v32, s[28:31], s60 offen nt\n"
      "s_waitcnt vmcnt(7)\n"
      "s_nop 0\n"
      "s_mov_b32 s60, 141312\n"
      "buffer_load_dwordx4 v[180:183], v32, s[28:31], s60 offen nt\n"
      "v_perm_b32 v66, v148, v144, s62\n"
      "v_perm_b32 v70, v148, v144, s63\n"
      "v_perm_b32 v74, v149, v145, s62\n"
      "v_perm_b32 v78, v149, v145, s63\n"
      "v_perm_b32 v82, v150, v146, s62\n"
      "v_perm_b32 v86, v150, v146, s63\n"
      "v_perm_b32 v90, v151, v147, s62\n"
      "v_perm_b32 v94, v151, v147, s63\n"
      "s_waitcnt vmcnt(7)\n"
      "s_nop 0\n"
      "s_mov_b32 s60, 143360\n"
      "buffer_load_dwordx4 v[184:187], v32, s[28:31], s60 offen nt\n"
      "s_waitcnt vmcnt(7)\n"
      "s_nop 0\n"
      "s_mov_b32 s60, 145408\n"
      "buffer_load_dwordx4 v[188:191], v32, s[28:31], s60 offen nt\n"
      "v_perm_b32 v67, v156, v152, s62\n"
      "v_perm_b32 v71, v156, v152, s63\n"
      "v_perm_b32 v75, v157, v153, s62\n"
      "v_perm_b32 v79, v157, v153, s63\n"
      "v_perm_b32 v83, v158, v154, s62\n"
      "v_perm_b32 v87, v158, v154, s63\n"
      "v_perm_b32 v91, v159, v155, s62\n"
      "v_perm_b32 v95, v159, v155, s63\n"
      "s_waitcnt vmcnt(7)\n"
      "s_nop 0\n"
      "s_movk_i32 s59, 0x3fff\n"
      "v_add_u32 v41, 0xffffffc0, v41\n"
      ".Lde%=:\n"
      "ds_read_b32 v43, v41\n"
      "s_sub_u32 s59, s59, 1\n"
      "s_cbranch_scc1 .Ldeo%=\n"
      "s_waitcnt lgkmcnt(0)\n"
      "v_cmp_ne_u32 vcc, s15, v43\n"
      "s_nop 1\n"
      "s_cbranch_vccz .Ldeo%=\n"
      "s_sleep 2\n"
      "s_branch .Lde%=\n"
      ".Ldeo%=:\n"
      "s_cmp_lg_u32 s59, 0x3ffe\n"
      "s_cbranch_scc0 .Ldep%=\n"
      "s_sleep 6\n"
      ".Ldep%=:\n"
      "ds_read_b32 v43, v42 offset:64\n"
      "s_waitcnt lgkmcnt(0)\n"
      "v_readfirstlane_b32 s12, v43\n"
      "s_nop 0\n"
      ".Ldpc1_%=:\n"
      "s_memrealtime s[64:65]\n"
      "s_waitcnt lgkmcnt(0)\n"
      "s_sub_u32 s64, s64, s12\n"
      "s_cmp_ge_u32 s64, 640\n"
      "s_cbranch_scc1 .Ldpd1_%=\n"
      "s_sleep 1\n"
      "s_branch .Ldpc1_%=\n"
      ".Ldpd1_%=:\n"
      "buffer_load_dwordx4 v[224:227], v35, s[16:19], s46 offen sc1\n"
      "buffer_load_dwordx4 v[228:231], v35, s[16:19], s46 offen offset:2048 sc1\n"
      "s_waitcnt vmcnt(8)\n"
      "s_nop 0\n"
      "v_perm_b32 v96, v164, v160, s62\n"
      "v_perm_b32 v100, v164, v160, s63\n"
      "v_perm_b32 v104, v165, v161, s62\n"
      "v_perm_b32 v108, v165, v161, s63\n"
      "v_perm_b32 v112, v166, v162, s62\n"
      "v_perm_b32 v116, v166, v162, s63\n"
      "v_perm_b32 v120, v167, v163, s62\n"
      "v_perm_b32 v124, v167, v163, s63\n"
      "s_waitcnt vmcnt(7)\n"
      "s_nop 0\n"
      "s_waitcnt vmcnt(6)\n"
      "s_nop 0\n"
      "v_perm_b32 v97, v172, v168, s62\n"
      "v_perm_b32 v101, v172, v168, s63\n"
      "v_perm_b32 v105, v173, v169, s62\n"
      "v_perm_b32 v109, v173, v169, s63\n"
      "v_perm_b32 v113, v174, v170, s62\n"
      "v_perm_b32 v117, v174, v170, s63\n"
      "v_perm_b32 v121, v175, v171, s62\n"
      "v_perm_b32 v125, v175, v171, s63\n"
      "s_waitcnt vmcnt(5)\n"
      "s_nop 0\n"
      "s_waitcnt vmcnt(4)\n"
      "s_nop 0\n"
      "v_perm_b32 v98, v180, v176, s62\n"
      "v_perm_b32 v102, v180, v176, s63\n"
      "v_perm_b32 v106, v181, v177, s62\n"
      "v_perm_b32 v110, v181, v177, s63\n"
      "v_perm_b32 v114, v182, v178, s62\n"
      "v_perm_b32 v118, v182, v178, s63\n"
      "v_perm_b32 v122, v183, v179, s62\n"
      "v_perm_b32 v126, v183, v179, s63\n"
      "s_waitcnt vmcnt(3)\n"
      "s_nop 0\n"
      "s_waitcnt vmcnt(2)\n"
      "s_nop 0\n"
      "v_perm_b32 v99, v188, v184, s62\n"
      "v_perm_b32 v103, v188, v184, s63\n"
      "v_perm_b32 v107, v189, v185, s62\n"
      "v_perm_b32 v111, v189, v185, s63\n"
      "v_perm_b32 v115, v190, v186, s62\n"
      "v_perm_b32 v119, v190, v186, s63\n"
      "v_perm_b32 v123, v191, v187, s62\n"
      "v_perm_b32 v127, v191, v187, s63\n"
      "v_mov_b32 v137, s15\n"
      "v_mov_b32 v139, s15\n"
      "v_mov_b32 v141, s15\n"
      "v_mov_b32 v143, s15\n"
      "s_movk_i32 s59, 0x7fff\n"
      "s_waitcnt vmcnt(0)\n"
      "s_nop 0\n"
      ".Ldg%=:\n"
      "v_xor_b32 v232, s15, v225\n"
      "v_bitop3_b32 v232, s15, v227, v232 bitop3:0xbe\n"
      "v_bitop3_b32 v232, s15, v229, v232 bitop3:0xbe\n"
      "v_bitop3_b32 v232, s15, v231, v232 bitop3:0xbe\n"
      "v_cmp_ne_u32 vcc, 0, v232\n"
      "v_lshlrev_b32 v232, 16, v224\n"
      "v_and_b32 v233, 0xffff0000, v224\n"
      "v_lshlrev_b32 v234, 16, v226\n"
      "v_and_b32 v235, 0xffff0000, v226\n"
      "s_cbranch_vccnz .Ldgs%=\n"
      ".Ldgo%=:\n"
      "v_lshlrev_b32 v236, 16, v228\n"
      "v_and_b32 v237, 0xffff0000, v228\n"
      "v_lshlrev_b32 v238, 16, v230\n"
      "v_and_b32 v239, 0xffff0000, v230\n"
      "v_pk_mul_f32 v[232:233], v[232:233], v[236:237]\n"
      "v_pk_mul_f32 v[234:235], v[234:235], v[238:239]\n"
      "v_cvt_pk_bf16_f32 v232, v232, v233\n"
      "v_cvt_pk_bf16_f32 v233, v234, v235\n"
      "ds_write_b64 v36, v[232:233] \n"
      "ds_write_b32 v41, v137 offset:96\n"
      "ds_read_b128 v[240:243], v42 offset:32\n"
      "ds_read_b128 v[224:227], v37 \n"
      "ds_read_b128 v[228:231], v37 offset:1152\n"
      "s_movk_i32 s59, 0x7fff\n"
      ".Ldv%=:\n"
      "s_waitcnt lgkmcnt(2)\n"
      "v_xor_b32 v240, s15, v240\n"
      "v_bitop3_b32 v240, s15, v241, v240 bitop3:0xbe\n"
      "v_bitop3_b32 v240, s15, v242, v240 bitop3:0xbe\n"
      "v_bitop3_b32 v240, s15, v243, v240 bitop3:0xbe\n"
      "v_cmp_ne_u32 vcc, 0, v240\n"
      "s_nop 1\n"
      "s_cbranch_vccnz .Ldvs%=\n"
      ".Ldvo%=:\n"
      "s_waitcnt lgkmcnt(1)\n"
      "v_mfma_f32_16x16x32_bf16 v[192:195], v[64:67], v[224:227], 0\n"
      "s_waitcnt lgkmcnt(0)\n"
      "v_mfma_f32_16x16x32_bf16 v[192:195], v[96:99], v[228:231], v[192:195]\n"
      "v_mfma_f32_16x16x32_bf16 v[196:199], v[68:71], v[224:227], 0\n"
      "v_mfma_f32_16x16x32_bf16 v[196:199], v[100:103], v[228:231], v[196:199]\n"
      "v_mfma_f32_16x16x32_bf16 v[200:203], v[72:75], v[224:227], 0\n"
      "s_nop 4\n"
      "v_permlane32_swap_b32 v192, v193\n"
      "v_permlane32_swap_b32 v194, v195\n"
      "v_mfma_f32_16x16x32_bf16 v[200:203], v[104:107], v[228:231], v[200:203]\n"
      "v_mfma_f32_16x16x32_bf16 v[204:207], v[76:79], v[224:227], 0\n"
      "v_permlane32_swap_b32 v196, v197\n"
      "v_permlane32_swap_b32 v198, v199\n"
      "v_mfma_f32_16x16x32_bf16 v[204:207], v[108:111], v[228:231], v[204:207]\n"
      "v_add_f32_dpp v128, v193, v192 row_ror:8 row_mask:0xf bank_mask:0x3\n"
      "s_nop 1\n"
      "v_add_f32_dpp v128, v194, v195 row_ror:8 row_mask:0xf bank_mask:0xc\n"
      "v_mfma_f32_16x16x32_bf16 v[208:211], v[80:83], v[224:227], 0\n"
      "v_permlane32_swap_b32 v200, v201\n"
      "v_permlane32_swap_b32 v202, v203\n"
      "v_mfma_f32_16x16x32_bf16 v[208:211], v[112:115], v[228:231], v[208:211]\n"
      "v_add_f32_dpp v129, v197, v196 row_ror:8 row_mask:0xf bank_mask:0x3\n"
      "s_nop 1\n"
      "v_add_f32_dpp v129, v198, v199 row_ror:8 row_mask:0xf bank_mask:0xc\n"
      "v_mfma_f32_16x16x32_bf16 v[212:215], v[84:87], v[224:227], 0\n"
      "v_permlane32_swap_b32 v204, v205\n"
      "v_permlane32_swap_b32 v206, v207\n"
      "v_mfma_f32_16x16x32_bf16 v[212:215], v[116:119], v[228:231], v[212:215]\n"
      "v_add_f32_dpp v130, v201, v200 row_ror:8 row_mask:0xf bank_mask:0x3\n"
      "s_nop 1\n"
      "v_add_f32_dpp v130, v202, v203 row_ror:8 row_mask:0xf bank_mask:0xc\n"
      "v_mfma_f32_16x16x32_bf16 v[216:219], v[88:91], v[224:227], 0\n"
      "v_permlane32_swap_b32 v208, v209\n"
      "v_permlane32_swap_b32 v210, v211\n"
      "v_mfma_f32_16x16x32_bf16 v[216:219], v[120:123], v[228:231], v[216:219]\n"
      "v_add_f32_dpp v131, v205, v204 row_ror:8 row_mask:0xf bank_mask:0x3\n"
      "s_nop 1\n"
      "v_add_f32_dpp v131, v206, v207 row_ror:8 row_mask:0xf bank_mask:0xc\n"
      "v_mfma_f32_16x16x32_bf16 v[220:223], v[92:95], v[224:227], 0\n"
      "v_permlane32_swap_b32 v212, v213\n"
      "v_permlane32_swap_b32 v214, v215\n"
      "v_mfma_f32_16x16x32_bf16 v[220:223], v[124:127], v[228:231], v[220:223]\n"
      "v_add_f32_dpp v132, v209, v208 row_ror:8 row_mask:0xf bank_mask:0x3\n"
      "s_nop 1\n"
      "v_add_f32_dpp v132, v210, v211 row_ror:8 row_mask:0xf bank_mask:0xc\n"
      "v_cvt_pk_bf16_f32 v136, v128, v129\n"
      "v_cvt_pk_bf16_f32 v138, v130, v131\n"
      "buffer_store_dwordx4 v[136:139], v38, s[16:19], s47 offen offset:0 sc1\n"
      "v_permlane32_swap_b32 v216, v217\n"
      "v_permlane32_swap_b32 v218, v219\n"
      "v_add_f32_dpp v133, v213, v212 row_ror:8 row_mask:0xf bank_mask:0x3\n"
      "s_nop 1\n"
      "v_add_f32_dpp v133, v214, v215 row_ror:8 row_mask:0xf bank_mask:0xc\n"
      "v_permlane32_swap_b32 v220, v221\n"
      "v_permlane32_swap_b32 v222, v223\n"
      "v_add_f32_dpp v134, v217, v216 row_ror:8 row_mask:0xf bank_mask:0x3\n"
      "s_nop 1\n"
      "v_add_f32_dpp v134, v218, v219 row_ror:8 row_mask:0xf bank_mask:0xc\n"
      "v_add_f32_dpp v135, v221, v220 row_ror:8 row_mask:0xf bank_mask:0x3\n"
      "s_nop 1\n"
      "v_add_f32_dpp v135, v222, v223 row_ror:8 row_mask:0xf bank_mask:0xc\n"
      "v_cvt_pk_bf16_f32 v140, v132, v133\n"
      "v_cvt_pk_bf16_f32 v142, v134, v135\n"
      "buffer_store_dwordx4 v[140:143], v38, s[16:19], s47 offen offset:128 sc1\n"
      "s_endpgm\n"
      ".LZ%=:\n"
      "s_setprio 0\n"
      "v_and_b32 v48, 63, v0\n"
      "v_and_b32 v49, 7, v0\n"
      "v_lshlrev_b32 v108, 3, v49\n"
      "s_lshl4_add_u32 s35, s48, s49\n"
      "s_lshl_b32 s41, s35, 4\n"
      "s_and_b32 s43, s41, 0xffffff80\n"
      "s_add_u32 s43, s43, 67584\n"
      "buffer_load_dwordx2 v[104:105], v108, s[16:19], s43 offen sc1\n"
      "s_mul_i32 s44, s35, 7168\n"
      "s_add_u32 s44, s44, 4194304\n"
      "s_add_u32 s67, s44, 0x1000\n"
      "v_lshrrev_b32 v109, 5, v48\n"
      "v_and_b32 v110, 8, v48\n"
      "v_lshl_add_u32 v109, v109, 2, v110\n"
      "v_and_b32 v113, 16, v48\n"
      "s_movk_i32 s59, 0x7fff\n"
      "s_waitcnt vmcnt(0)\n"
      "s_movk_i32 s61, 0x7ff\n"
      ".Ldzh%=:\n"
      "v_cmp_ne_u32 vcc, s15, v105\n"
      "v_bitop3_b32 v110, v104, s61, v104 bitop3:0x0c\n"
      "v_not_b32 v112, v104\n"
      "s_cbranch_vccnz .Ldzs%=\n"
      ".Ldzo%=:\n"
      "v_lshl_add_u32 v110, v110, 11, v109\n"
      "v_lshlrev_b32 v108, 4, v48\n"
      "buffer_load_dword v111, v110, s[20:23], s41 offen\n"
      "v_lshl_or_b32 v113, v113, 26, v110\n"
      "v_ashrrev_i32 v112, 31, v112\n"
      "v_or_b32 v112, 0x80000000, v112\n"
      "v_xor_b32 v112, v112, v104\n"
      "v_and_b32 v112, 0xffff0000, v112\n"
      "v_mul_f32 v112, 0xbfb8aa3b, v112\n"
      "v_exp_f32 v112, v112\n"
      "s_nop 0\n"
      "v_add_f32 v112, 1.0, v112\n"
      "v_rcp_f32 v112, v112\n"
      "s_nop 0\n"
      "v_cvt_pk_bf16_f32 v112, v112, v112\n"
      "v_and_b32 v112, 0xffff0000, v112\n"
      ".Ldpc2_%=:\n"
      "s_memrealtime s[64:65]\n"
      "s_waitcnt lgkmcnt(0)\n"
      "s_sub_u32 s64, s64, s12\n"
      "s_cmp_ge_u32 s64, 830\n"
      "s_cbranch_scc1 .Ldpd2_%=\n"
      "s_sleep 1\n"
      "s_branch .Ldpc2_%=\n"
      ".Ldpd2_%=:\n"
      "buffer_load_dwordx4 v[64:67], v108, s[16:19], s44 offen offset:0 sc1\n"
      "buffer_load_dwordx4 v[68:71], v108, s[16:19], s44 offen offset:1024 sc1\n"
      "buffer_load_dwordx4 v[72:75], v108, s[16:19], s44 offen offset:2048 sc1\n"
      "buffer_load_dwordx4 v[76:79], v108, s[16:19], s44 offen offset:3072 sc1\n"
      "buffer_load_dwordx4 v[80:83], v108, s[16:19], s67 offen offset:0 sc1\n"
      "buffer_load_dwordx4 v[84:87], v108, s[16:19], s67 offen offset:1024 sc1\n"
      "buffer_load_dwordx4 v[88:91], v108, s[16:19], s67 offen offset:2048 sc1\n"
      "s_movk_i32 s59, 0x7fff\n"
      "s_mov_b32 s56, 0x3f80\n"
      "s_mov_b32 s57, 0x3f800000\n"
      "s_waitcnt vmcnt(7)\n"
      "v_lshlrev_b32 v106, 16, v111\n"
      "v_and_b32 v107, 0xffff0000, v111\n"
      ".Ldq%=:\n"
      "s_waitcnt vmcnt(6)\n"
      "v_xor_b32 v100, s15, v65\n"
      "v_xor_b32 v101, s15, v67\n"
      "v_lshlrev_b32 v96, 16, v64\n"
      "v_and_b32 v97, 0xffff0000, v64\n"
      "v_lshlrev_b32 v98, 16, v66\n"
      "v_and_b32 v99, 0xffff0000, v66\n"
      "s_waitcnt vmcnt(5)\n"
      "v_dot2_f32_bf16 v96, v68, s56, v96\n"
      "v_dot2_f32_bf16 v97, v68, s57, v97\n"
      "v_dot2_f32_bf16 v98, v70, s56, v98\n"
      "v_dot2_f32_bf16 v99, v70, s57, v99\n"
      "v_bitop3_b32 v100, s15, v69, v100 bitop3:0xbe\n"
      "v_bitop3_b32 v101, s15, v71, v101 bitop3:0xbe\n"
      "s_waitcnt vmcnt(4)\n"
      "v_dot2_f32_bf16 v96, v72, s56, v96\n"
      "v_dot2_f32_bf16 v97, v72, s57, v97\n"
      "v_dot2_f32_bf16 v98, v74, s56, v98\n"
      "v_dot2_f32_bf16 v99, v74, s57, v99\n"
      "v_bitop3_b32 v100, s15, v73, v100 bitop3:0xbe\n"
      "v_bitop3_b32 v101, s15, v75, v101 bitop3:0xbe\n"
      "s_waitcnt vmcnt(3)\n"
      "v_dot2_f32_bf16 v96, v76, s56, v96\n"
      "v_dot2_f32_bf16 v97, v76, s57, v97\n"
      "v_dot2_f32_bf16 v98, v78, s56, v98\n"
      "v_dot2_f32_bf16 v99, v78, s57, v99\n"
      "v_bitop3_b32 v100, s15, v77, v100 bitop3:0xbe\n"
      "v_bitop3_b32 v101, s15, v79, v101 bitop3:0xbe\n"
      "s_waitcnt vmcnt(2)\n"
      "v_dot2_f32_bf16 v96, v80, s56, v96\n"
      "v_dot2_f32_bf16 v97, v80, s57, v97\n"
      "v_dot2_f32_bf16 v98, v82, s56, v98\n"
      "v_dot2_f32_bf16 v99, v82, s57, v99\n"
      "v_bitop3_b32 v100, s15, v81, v100 bitop3:0xbe\n"
      "v_bitop3_b32 v101, s15, v83, v101 bitop3:0xbe\n"
      "s_waitcnt vmcnt(1)\n"
      "v_dot2_f32_bf16 v96, v84, s56, v96\n"
      "v_dot2_f32_bf16 v97, v84, s57, v97\n"
      "v_dot2_f32_bf16 v98, v86, s56, v98\n"
      "v_dot2_f32_bf16 v99, v86, s57, v99\n"
      "v_bitop3_b32 v100, s15, v85, v100 bitop3:0xbe\n"
      "v_bitop3_b32 v101, s15, v87, v101 bitop3:0xbe\n"
      "s_waitcnt vmcnt(0)\n"
      "v_dot2_f32_bf16 v96, v88, s56, v96\n"
      "v_dot2_f32_bf16 v97, v88, s57, v97\n"
      "v_dot2_f32_bf16 v98, v90, s56, v98\n"
      "v_dot2_f32_bf16 v99, v90, s57, v99\n"
      "v_bitop3_b32 v100, s15, v89, v100 bitop3:0xbe\n"
      "v_bitop3_b32 v101, s15, v91, v101 bitop3:0xbe\n"
      "v_or_b32 v100, v100, v101\n"
      "v_cmp_ne_u32 vcc, 0, v100\n"
      "v_permlane32_swap_b32 v96, v98\n"
      "v_permlane32_swap_b32 v97, v99\n"
      "s_cbranch_vccnz .Ldqs%=\n"
      ".Ldqo%=:\n"
      "v_add_f32 v96, v96, v98\n"
      "v_add_f32 v97, v97, v99\n"
      "v_mov_b32 v98, v96\n"
      "v_mov_b32 v99, v97\n"
      "s_nop 0\n"
      "v_permlane16_swap_b32 v96, v98\n"
      "v_permlane16_swap_b32 v97, v99\n"
      "v_add_f32 v96, v96, v98\n"
      "v_add_f32 v97, v97, v99\n"
      "v_cvt_pk_bf16_f32 v96, v96, v97\n"
      "v_and_b32 v97, 0xffff0000, v96\n"
      "v_lshlrev_b32 v96, 16, v96\n"
      "v_mul_f32 v97, v97, v112\n"
      "v_mul_f32 v96, v96, v112\n"
      "v_cvt_pk_bf16_f32 v96, v96, v97\n"
      "v_and_b32 v97, 0xffff0000, v96\n"
      "v_lshlrev_b32 v96, 16, v96\n"
      "v_add_f32 v97, v97, v107\n"
      "v_add_f32 v96, v96, v106\n"
      "v_cvt_pk_bf16_f32 v96, v96, v97\n"
      "buffer_atomic_pk_add_bf16 v96, v113, s[24:27], s41 offen\n"
      "s_endpgm\n"
      ".Ldgs%=:\n"
      "s_sleep 1\n"
      "s_sub_u32 s59, s59, 1\n"
      "s_cbranch_scc1 .Ldgo%=\n"
      "buffer_load_dwordx4 v[224:227], v35, s[16:19], s46 offen sc1\n"
      "buffer_load_dwordx4 v[228:231], v35, s[16:19], s46 offen offset:2048 sc1\n"
      "s_waitcnt vmcnt(0)\n"
      "s_branch .Ldg%=\n"
      ".Ldvs%=:\n"
      "s_sub_u32 s59, s59, 1\n"
      "s_cbranch_scc1 .Ldvo%=\n"
      "s_waitcnt lgkmcnt(0)\n"
      "ds_read_b128 v[240:243], v42 offset:32\n"
      "ds_read_b128 v[224:227], v37 \n"
      "ds_read_b128 v[228:231], v37 offset:1152\n"
      "s_waitcnt lgkmcnt(0)\n"
      "s_branch .Ldv%=\n"
      ".Ldzs%=:\n"
      "s_sleep 2\n"
      "s_sub_u32 s59, s59, 1\n"
      "s_cbranch_scc1 .Ldzo%=\n"
      "buffer_load_dwordx2 v[104:105], v108, s[16:19], s43 offen sc1\n"
      "s_waitcnt vmcnt(0)\n"
      "s_branch .Ldzh%=\n"
      ".Ldqs%=:\n"
      "s_sleep 1\n"
      "s_sub_u32 s59, s59, 1\n"
      "s_cbranch_scc1 .Ldqo%=\n"
      "v_cmp_ne_u32 vcc, s15, v65\n"
      "v_cmp_ne_u32 s[64:65], s15, v67\n"
      "s_nop 0\n"
      "s_or_b64 exec, vcc, s[64:65]\n"
      "s_cbranch_scc0 .Ldqk0%=\n"
      "buffer_load_dwordx4 v[64:67], v108, s[16:19], s44 offen offset:0 sc1\n"
      ".Ldqk0%=:\n"
      "s_mov_b64 exec, -1\n"
      "v_cmp_ne_u32 vcc, s15, v69\n"
      "v_cmp_ne_u32 s[64:65], s15, v71\n"
      "s_nop 0\n"
      "s_or_b64 exec, vcc, s[64:65]\n"
      "s_cbranch_scc0 .Ldqk1%=\n"
      "buffer_load_dwordx4 v[68:71], v108, s[16:19], s44 offen offset:1024 sc1\n"
      ".Ldqk1%=:\n"
      "s_mov_b64 exec, -1\n"
      "v_cmp_ne_u32 vcc, s15, v73\n"
      "v_cmp_ne_u32 s[64:65], s15, v75\n"
      "s_nop 0\n"
      "s_or_b64 exec, vcc, s[64:65]\n"
      "s_cbranch_scc0 .Ldqk2%=\n"
      "buffer_load_dwordx4 v[72:75], v108, s[16:19], s44 offen offset:2048 sc1\n"
      ".Ldqk2%=:\n"
      "s_mov_b64 exec, -1\n"
      "v_cmp_ne_u32 vcc, s15, v77\n"
      "v_cmp_ne_u32 s[64:65], s15, v79\n"
      "s_nop 0\n"
      "s_or_b64 exec, vcc, s[64:65]\n"
      "s_cbranch_scc0 .Ldqk3%=\n"
      "buffer_load_dwordx4 v[76:79], v108, s[16:19], s44 offen offset:3072 sc1\n"
      ".Ldqk3%=:\n"
      "s_mov_b64 exec, -1\n"
      "v_cmp_ne_u32 vcc, s15, v81\n"
      "v_cmp_ne_u32 s[64:65], s15, v83\n"
      "s_nop 0\n"
      "s_or_b64 exec, vcc, s[64:65]\n"
      "s_cbranch_scc0 .Ldqk4%=\n"
      "buffer_load_dwordx4 v[80:83], v108, s[16:19], s67 offen offset:0 sc1\n"
      ".Ldqk4%=:\n"
      "s_mov_b64 exec, -1\n"
      "v_cmp_ne_u32 vcc, s15, v85\n"
      "v_cmp_ne_u32 s[64:65], s15, v87\n"
      "s_nop 0\n"
      "s_or_b64 exec, vcc, s[64:65]\n"
      "s_cbranch_scc0 .Ldqk5%=\n"
      "buffer_load_dwordx4 v[84:87], v108, s[16:19], s67 offen offset:1024 sc1\n"
      ".Ldqk5%=:\n"
      "s_mov_b64 exec, -1\n"
      "v_cmp_ne_u32 vcc, s15, v89\n"
      "v_cmp_ne_u32 s[64:65], s15, v91\n"
      "s_nop 0\n"
      "s_or_b64 exec, vcc, s[64:65]\n"
      "s_cbranch_scc0 .Ldqk6%=\n"
      "buffer_load_dwordx4 v[88:91], v108, s[16:19], s67 offen offset:2048 sc1\n"
      ".Ldqk6%=:\n"
      "s_mov_b64 exec, -1\n"
      "s_waitcnt vmcnt(0)\n"
      "s_branch .Ldq%=\n"
      ".LT%=:\n"
      "v_mov_b32 v48, 0x22680\n"
      "ds_read_b32 v48, v48 \n"
      "s_waitcnt lgkmcnt(0)\n"
      "v_readfirstlane_b32 s12, v48\n"
      "v_mov_b32 v48, v43\n"
      "v_and_b32 v49, 7, v43\n"
      "v_lshrrev_b32 v50, 3, v43\n"
      "s_lshl_b32 s42, s34, 3\n"
      "s_add_u32 s42, s42, 2048\n"
      "s_lshl_b32 s43, s48, 8\n"
      "s_and_b32 s50, s49, 1\n"
      "s_lshl_b32 s50, s50, 7\n"
      "s_add_u32 s43, s43, s50\n"
      "s_add_u32 s43, s43, 65536\n"
      "s_mov_b32 s20, s64\n"
      "s_and_b32 s21, s65, 0xffff\n"
      "s_mov_b32 s22, 0x400000\n"
      "s_mov_b32 s23, 0x20000\n"
      "s_mov_b32 s28, s84\n"
      "s_and_b32 s29, s85, 0xffff\n"
      "s_mov_b32 s30, 0x700000\n"
      "s_mov_b32 s31, 0x20000\n"
      "s_sleep 0\n"
      ".LS%=:\n"
      "v_lshlrev_b32 v112, 4, v0\n"
      "v_lshlrev_b32 v113, 2, v0\n"
      "v_sub_u32 v113, 0xfff, v113\n"
      "v_lshlrev_b32 v115, 2, v48\n"
      "v_mov_b32 v116, 0\n"
      "ds_write_b32 v115, v116 offset:27136\n"
      "ds_write_b32 v116, v116 offset:26400\n"
      "v_add_u32 v115, 0x140, v115\n"
      "v_mov_b32 v114, v115\n"
      "s_lshl_b32 s50, s33, 2\n"
      "s_add_u32 s50, s50, 256\n"
      "v_writelane_b32 v114, s50, 63\n"
      "s_lshl_b32 s50, s33, 5\n"
      "v_lshl_add_u32 v117, v49, 2, s50\n"
      "v_lshlrev_b32 v118, 5, v50\n"
      "s_and_b32 s50, s49, 1\n"
      "s_lshl_b32 s50, s50, 10\n"
      "v_lshl_add_u32 v119, v50, 7, s50\n"
      "s_lshr_b32 s51, s33, 2\n"
      "s_mul_i32 s51, s51, 61440\n"
      "s_add_u32 s51, s51, 393216\n"
      "s_mov_b32 s52, 0x80008000\n"
      "s_mov_b32 s53, 0xffff0000\n"
      "s_movk_i32 s59, 0x7fff\n"
      ".Lt_pc4_%=:\n"
      "s_memrealtime s[64:65]\n"
      "s_waitcnt lgkmcnt(0)\n"
      "s_sub_u32 s64, s64, s12\n"
      "s_cmp_ge_u32 s64, 300\n"
      "s_cbranch_scc1 .Lt_pd4_%=\n"
      "s_sleep 1\n"
      "s_branch .Lt_pc4_%=\n"
      ".Lt_pd4_%=:\n"
      ".Lt_p%=:\n"
      "buffer_load_dwordx4 v[72:75], v112, s[16:19], s51 offen sc1\n"
      "s_waitcnt vmcnt(0)\n"
      "v_cmp_ne_u32 vcc, s15, v73\n"
      "v_cmp_ne_u32 s[54:55], s15, v75\n"
      "v_pk_ashrrev_i16 v81, 15, v72 op_sel_hi:[0,1]\n"
      "v_pk_ashrrev_i16 v82, 15, v74 op_sel_hi:[0,1]\n"
      "s_or_b64 vcc, vcc, s[54:55]\n"
      "s_cbranch_scc1 .Lt_ps%=\n"
      ".Lt_po%=:\n"
      "v_bitop3_b32 v81, v72, v81, s52 bitop3:0x1e\n"
      "v_bitop3_b32 v82, v74, v82, s52 bitop3:0x1e\n"
      "v_lshl_or_b32 v76, v81, 16, v113\n"
      "v_and_or_b32 v77, v81, s53, v113\n"
      "v_lshl_or_b32 v78, v82, 16, v113\n"
      "v_and_or_b32 v79, v82, s53, v113\n"
      "v_add_u32 v77, -1, v77\n"
      "v_add_u32 v78, -2, v78\n"
      "v_add_u32 v79, -3, v79\n"
      "v_max3_u32 v80, v76, v77, v78\n"
      "v_max_u32 v80, v80, v79\n"
      "v_mov_b32 v104, v80\n"
      "v_med3_u32 v105, v76, v77, v78\n"
      "v_max_u32_dpp v80, v80, v80 row_shr:1 row_mask:0xf bank_mask:0xf bound_ctrl:1\n"
      "v_max3_u32 v83, v76, v77, v78\n"
      "v_min3_u32 v106, v76, v77, v78\n"
      "v_max_u32_dpp v80, v80, v80 row_shr:2 row_mask:0xf bank_mask:0xf bound_ctrl:1\n"
      "v_med3_u32 v106, v105, v106, v79\n"
      "v_med3_u32 v105, v83, v105, v79\n"
      "v_max_u32_dpp v80, v80, v80 row_shr:4 row_mask:0xf bank_mask:0xf bound_ctrl:1\n"
      "v_mov_b32 v107, 0x100\n"
      "s_nop 0\n"
      "v_max_u32_dpp v80, v80, v80 row_shr:8 row_mask:0xf bank_mask:0xf bound_ctrl:1\n"
      "s_nop 1\n"
      "v_max_u32_dpp v80, v80, v80 row_bcast:15 row_mask:0xa bank_mask:0xf\n"
      "s_nop 1\n"
      "v_max_u32_dpp v80, v80, v80 row_bcast:31 row_mask:0xc bank_mask:0xf\n"
      "ds_write_b32 v114, v80 offset:26112\n"
      "s_waitcnt lgkmcnt(0)\n"
      "s_barrier\n"
      "ds_read_b128 v[92:95], v116 offset:26368\n"
      "ds_read_b128 v[96:99], v116 offset:26384\n"
      "s_waitcnt lgkmcnt(1)\n"
      "v_min3_u32 v81, v92, v93, v94\n"
      "s_waitcnt lgkmcnt(0)\n"
      "v_min3_u32 v82, v96, v97, v98\n"
      "v_min3_u32 v81, v81, v95, v99\n"
      "v_min_u32 v81, v81, v82\n"
      "v_cmp_ge_u32 s[62:63], v105, v81\n"
      "v_cmp_ge_u32 vcc, v106, v81\n"
      "v_cmp_ge_u32 s[60:61], v104, v81\n"
      "v_cndmask_b32 v82, 1, 2, s[62:63]\n"
      "v_cndmask_b32 v82, v82, v107, vcc\n"
      "s_mov_b64 exec, s[60:61]\n"
      "ds_add_rtn_u32 v83, v116, v82 offset:26400\n"
      "s_waitcnt lgkmcnt(0)\n"
      "v_min_u32 v83, 0x50, v83\n"
      "v_lshlrev_b32 v83, 2, v83\n"
      "ds_write_b32 v83, v104 offset:27136\n"
      "s_mov_b64 exec, s[62:63]\n"
      "ds_write_b32 v83, v105 offset:27140\n"
      "s_mov_b64 exec, -1\n"
      "s_waitcnt lgkmcnt(0)\n"
      "s_barrier\n"
      "s_memrealtime s[56:57]\n"
      "ds_read_b32 v81, v116 offset:26400\n"
      ".Lt_r%=:\n"
      "ds_read_b32 v100, v117 offset:27136\n"
      "ds_read_b128 v[92:95], v118 offset:27136\n"
      "ds_read_b128 v[96:99], v118 offset:27152\n"
      "v_mov_b32 v101, s15\n"
      "v_mov_b32 v89, 0\n"
      "s_waitcnt lgkmcnt(3)\n"
      "v_cmp_lt_u32 vcc, 64, v81\n"
      "s_movk_i32 s50, 0x7ff\n"
      "s_cbranch_vccnz .Lt_m%=\n"
      "s_waitcnt lgkmcnt(1)\n"
      "v_cmp_gt_u32 s[60:61], v92, v100\n"
      "v_cmp_gt_u32 s[62:63], v93, v100\n"
      "v_cmp_gt_u32 s[64:65], v94, v100\n"
      "v_cmp_gt_u32 s[66:67], v95, v100\n"
      "s_waitcnt lgkmcnt(0)\n"
      "v_cmp_gt_u32 s[68:69], v96, v100\n"
      "v_cmp_gt_u32 s[70:71], v97, v100\n"
      "v_cmp_gt_u32 s[72:73], v98, v100\n"
      "v_cmp_gt_u32 s[74:75], v99, v100\n"
      "v_bitop3_b32 v81, v100, s50, v100 bitop3:0x0c\n"
      "v_addc_co_u32 v89, vcc, 0, v89, s[60:61]\n"
      "v_addc_co_u32 v89, vcc, 0, v89, s[62:63]\n"
      "v_addc_co_u32 v89, vcc, 0, v89, s[64:65]\n"
      "v_addc_co_u32 v89, vcc, 0, v89, s[66:67]\n"
      "v_addc_co_u32 v89, vcc, 0, v89, s[68:69]\n"
      "v_addc_co_u32 v89, vcc, 0, v89, s[70:71]\n"
      "v_addc_co_u32 v89, vcc, 0, v89, s[72:73]\n"
      "v_addc_co_u32 v89, vcc, 0, v89, s[74:75]\n"
      "v_lshl_add_u32 v82, v81, 11, v119\n"
      "v_lshrrev_b32 v86, 9, v81\n"
      "v_mul_u32_u24 v86, 0xf000, v86\n"
      "v_lshl_add_u32 v81, v81, 3, v86\n"
      "v_add_u32_dpp v89, v89, v89 row_ror:8 row_mask:0xf bank_mask:0xf\n"
      "v_mov_b32 v83, v89\n"
      "s_nop 1\n"
      "v_permlane16_swap_b32 v89, v83\n"
      "v_add_u32 v89, v89, v83\n"
      "v_mov_b32 v83, v89\n"
      "s_nop 1\n"
      "v_permlane32_swap_b32 v89, v83\n"
      "v_add_u32 v89, v89, v83\n"
      "v_cmp_gt_u32 vcc, 8, v89\n"
      "v_lshlrev_b32 v89, 3, v89\n"
      "s_mov_b32 s51, 131072\n"
      "s_cbranch_vccz .Lt_n%=\n"
      "s_and_b64 exec, vcc, 0xff\n"
      "ds_write_b32 v89, v100 offset:26752\n"
      "buffer_load_dword v84, v81, s[16:19], s51 offen sc1\n"
      "s_mov_b64 exec, vcc\n"
      "buffer_load_dword v85, v82, s[20:23], 0 offen\n"
      "s_mov_b64 exec, -1\n"
      ".Lt_n%=:\n"
      "v_lshlrev_b32 v89, 3, v49\n"
      "s_waitcnt lgkmcnt(0)\n"
      "s_barrier\n"
      "s_sub_u32 s12, s56, 390\n"
      "s_movk_i32 s57, 64\n"
      "s_cmp_lg_u32 s33, 0\n"
      "s_cbranch_scc1 .Lt_a%=\n"
      "ds_read_b32 v100, v89 offset:26752\n"
      "v_writelane_b32 v89, s57, 8\n"
      "s_waitcnt lgkmcnt(0)\n"
      "v_writelane_b32 v100, s12, 8\n"
      "s_mov_b64 exec, 0x1ff\n"
      "buffer_store_dwordx2 v[100:101], v89, s[16:19], s43 offen sc1\n"
      "s_mov_b64 exec, -1\n"
      "s_nop 1\n"
      "v_mov_b32 v96, s15\n"
      "v_mov_b32 v97, s15\n"
      "buffer_store_dwordx2 v[96:97], off, s[16:19], s42 sc1\n"
      ".Lt_a%=:\n"
      "s_cmp_lg_u32 s33, 1\n"
      "s_cbranch_scc1 .Lt_c%=\n"
      "ds_read_b32 v100, v89 offset:26752\n"
      "v_lshlrev_b32 v81, 3, v50\n"
      "ds_read_b32 v81, v81 offset:26752\n"
      "v_mov_b32 v92, 0\n"
      "v_mov_b32 v93, 0\n"
      "v_mov_b32 v94, 0\n"
      "v_mov_b32 v95, 0\n"
      "s_and_b32 s51, s43, 0x7ff\n"
      "v_lshlrev_b32 v82, 4, v49\n"
      "s_waitcnt lgkmcnt(0)\n"
      "v_bitop3_b32 v81, v81, s50, v81 bitop3:0x0c\n"
      "v_lshl_add_u32 v81, v81, 11, v82\n"
      "buffer_store_dwordx4 v[92:95], v81, s[24:27], s51 offen sc1\n"
      "s_waitcnt vmcnt(0)\n"
      "s_mov_b64 exec, 0xff\n"
      "buffer_store_dwordx2 v[100:101], v89, s[16:19], s43 offen offset:2048 sc1\n"
      "s_mov_b64 exec, -1\n"
      "s_nop 1\n"
      ".Lt_c%=:\n"
      "s_cmp_lg_u32 s33, 3\n"
      "s_cbranch_scc1 .Lt_z%=\n"
      "s_sleep 55\n"
      "s_branch .LZ%=\n"
      ".Lt_z%=:\n"
      "s_cmp_lg_u32 s34, 127\n"
      "s_cbranch_scc1 .Lt_e%=\n"
      "s_cmp_lg_u32 s33, 0\n"
      "s_cbranch_scc1 .Lt_e%=\n"
      "s_movk_i32 s51, 2048\n"
      "s_movk_i32 s59, 0x7fff\n"
      ".Lt_k%=:\n"
      "s_sleep 16\n"
      "buffer_load_dwordx4 v[92:95], v112, s[16:19], s51 offen sc1\n"
      "buffer_load_dwordx4 v[96:99], v112, s[16:19], s51 offen offset:1024 sc1\n"
      "s_sub_u32 s59, s59, 1\n"
      "s_cbranch_scc1 .Lt_kb%=\n"
      "s_waitcnt vmcnt(0)\n"
      "v_xor_b32 v86, s15, v93\n"
      "v_bitop3_b32 v86, s15, v95, v86 bitop3:0xbe\n"
      "v_bitop3_b32 v86, s15, v97, v86 bitop3:0xbe\n"
      "v_bitop3_b32 v86, s15, v99, v86 bitop3:0xbe\n"
      "v_cmp_ne_u32 vcc, 0, v86\n"
      "s_nop 1\n"
      "s_cbranch_vccnz .Lt_k%=\n"
      ".Lt_kb%=:\n"
      "buffer_store_dword v101, off, s[16:19], 0 sc1\n"
      ".Lt_e%=:\n"
      "s_waitcnt vmcnt(0)\n"
      "s_endpgm\n"
      ".Lt_ps%=:\n"
      "s_sleep 1\n"
      "s_sub_u32 s59, s59, 1\n"
      "s_cbranch_scc0 .Lt_p%=\n"
      "s_branch .Lt_po%=\n"
      ".Lt_m%=:\n"
      "s_waitcnt lgkmcnt(0)\n"
      "s_barrier\n"
      "s_mov_b32 s59, 0\n"
      "v_mov_b32 v90, 0\n"
      ".Lt_ml%=:\n"
      "s_nop 1\n"
      "v_max3_u32 v80, v76, v77, v78\n"
      "v_max_u32 v80, v80, v79\n"
      "s_nop 1\n"
      "v_max_u32_dpp v80, v80, v80 row_shr:1 row_mask:0xf bank_mask:0xf bound_ctrl:1\n"
      "s_nop 1\n"
      "v_max_u32_dpp v80, v80, v80 row_shr:2 row_mask:0xf bank_mask:0xf bound_ctrl:1\n"
      "s_nop 1\n"
      "v_max_u32_dpp v80, v80, v80 row_shr:4 row_mask:0xf bank_mask:0xf bound_ctrl:1\n"
      "s_nop 1\n"
      "v_max_u32_dpp v80, v80, v80 row_shr:8 row_mask:0xf bank_mask:0xf bound_ctrl:1\n"
      "s_nop 1\n"
      "v_max_u32_dpp v80, v80, v80 row_bcast:15 row_mask:0xa bank_mask:0xf\n"
      "s_nop 1\n"
      "v_max_u32_dpp v80, v80, v80 row_bcast:31 row_mask:0xc bank_mask:0xf\n"
      "s_nop 0\n"
      "v_readlane_b32 s50, v80, 63\n"
      "s_mov_b32 m0, s59\n"
      "s_nop 0\n"
      "v_cmp_ne_u32 vcc, s50, v76\n"
      "s_nop 1\n"
      "v_cndmask_b32 v76, 0, v76, vcc\n"
      "v_cmp_ne_u32 vcc, s50, v77\n"
      "s_nop 1\n"
      "v_cndmask_b32 v77, 0, v77, vcc\n"
      "v_cmp_ne_u32 vcc, s50, v78\n"
      "s_nop 1\n"
      "v_cndmask_b32 v78, 0, v78, vcc\n"
      "v_cmp_ne_u32 vcc, s50, v79\n"
      "s_nop 1\n"
      "v_cndmask_b32 v79, 0, v79, vcc\n"
      "v_writelane_b32 v90, s50, m0\n"
      "s_add_u32 s59, s59, 1\n"
      "s_cmp_lt_u32 s59, 8\n"
      "s_cbranch_scc1 .Lt_ml%=\n"
      "s_mov_b64 exec, 0xff\n"
      "ds_write_b32 v117, v90 offset:27136\n"
      "s_mov_b64 exec, -1\n"
      "v_mov_b32 v81, 0\n"
      "s_waitcnt lgkmcnt(0)\n"
      "s_barrier\n"
      "s_branch .Lt_r%=\n"
      ".LEnd%=:\n"
      : "={s[80:95]}"(sts), "={s[96:99]}"(st2)
      : [ka] "s"(__builtin_amdgcn_kernarg_segment_ptr()), [bid] "s"(blockIdx.x), "{v0}"(threadIdx.x), "s"((u32)(uintptr_t)sm)
      : "v8", "v9", "v10", "v11", "v12", "v13", "v14", "v15", "v16", "v17", "v18", "v19", "v20", "v21", "v22", "v23", "v24", "v25", "v26", "v27",
        "v28", "v29", "v30", "v31", "v32", "v33", "v34", "v35", "v36", "v37", "v38", "v39", "v40", "v41", "v42", "v43", "v44", "v45", "v46", "v47",
        "v48", "v49", "v50", "v51", "v52", "v53", "v54", "v55", "v56", "v57", "v58", "v59", "v60", "v61", "v62", "v63", "v64", "v65", "v66", "v67",
        "v68", "v69", "v70", "v71", "v72", "v73", "v74", "v75", "v76", "v77", "v78", "v79", "v80", "v81", "v82", "v83", "v84", "v85", "v86", "v87",
        "v88", "v89", "v90", "v91", "v92", "v93", "v94", "v95", "v96", "v97", "v98", "v99", "v100", "v101", "v102", "v103", "v104", "v105", "v106", "v107",
        "v108", "v109", "v110", "v111", "v112", "v113", "v114", "v115", "v116", "v117", "v118", "v119", "v120", "v121", "v122", "v123", "v124", "v125", "v126", "v127",
        "v128", "v129", "v130", "v131", "v132", "v133", "v134", "v135", "v136", "v137", "v138", "v139", "v140", "v141", "v142", "v143", "v144", "v145", "v146", "v147",
        "v148", "v149", "v150", "v151", "v152", "v153", "v154", "v155", "v156", "v157", "v158", "v159", "v160", "v161", "v162", "v163", "v164", "v165", "v166", "v167",
        "v168", "v169", "v170", "v171", "v172", "v173", "v174", "v175", "v176", "v177", "v178", "v179", "v180", "v181", "v182", "v183", "v184", "v185", "v186", "v187",
        "v188", "v189", "v190", "v191", "v192", "v193", "v194", "v195", "v196", "v197", "v198", "v199", "v200", "v201", "v202", "v203", "v204", "v205", "v206", "v207",
        "v208", "v209", "v210", "v211", "v212", "v213", "v214", "v215", "v216", "v217", "v218", "v219", "v220", "v221", "v222", "v223", "v224", "v225", "v226", "v227",
        "v228", "v229", "v230", "v231", "v232", "v233", "v234", "v235", "v236", "v237", "v238", "v239", "v240", "v241", "v242", "v243", "v244", "v245", "v246", "v247",
        "v248", "v249", "v250", "v251", "v252", "v253", "v254", "v255", "s12", "s13", "s14", "s15", "s16", "s17", "s18", "s19", "s20", "s21", "s22", "s23",
        "s24", "s25", "s26", "s27", "s28", "s29", "s30", "s31", "s33", "s34", "s35", "s36", "s37", "s38", "s39", "s40", "s41", "s42", "s43", "s44",
        "s45", "s46", "s47", "s48", "s49", "s50", "s51", "s52", "s53", "s54", "s55", "s56", "s57", "s58", "s59", "s60", "s61", "s62", "s63", "s64",
        "s65", "s66", "s67", "s68", "s69", "s70", "s71", "s72", "s73", "s74", "s75", "s76", "s77", "s78", "s79",
        "vcc", "scc", "memory");
  const int b = blockIdx.x;
  GETST();
  STAMP(11);
  PEND();
}

extern "C" void kernel_launch(void* const* d_in, const int* in_sizes, int n_in, void* d_out, int out_size,
                              void* d_ws, size_t ws_size, hipStream_t stream) {
  const u16* const* in = (const u16* const*)d_in;
  fused<<<256, 512, 0, stream>>>(in[0], in[1], in[2], in[3], in[4], in[5], in[8], in[6], in[9], in[10], in[11],
                                 (u16*)d_out, (char*)d_ws);
}
```

```cpp
#include <hip/hip_runtime.h>
#include <stdint.h>

typedef unsigned short u16;
typedef unsigned int u32;
typedef unsigned long long u64;
typedef __attribute__((ext_vector_type(4))) u32 u32x4;
typedef __attribute__((ext_vector_type(16))) u32 u32x16;
#define AS3 __attribute__((address_space(3)))

#define STAMP(I)
#define PEND()
#define GETST()
constexpr int SPIN_MAX = 1 << 16;
constexpr int UNUSED = 0;

__global__ __launch_bounds__(512, 2) void fused(const u16* __restrict__ x, const u16* __restrict__ lar,
    const u16* __restrict__ aim, const u16* __restrict__ cre, const u16* __restrict__ cim,
    const u16* __restrict__ ldt, const u16* __restrict__ wr, const u16* __restrict__ dsk,
    const u16* __restrict__ wg, const u16* __restrict__ wu, const u16* __restrict__ wd,
    u16* __restrict__ out, char* ws) {
  __shared__ __attribute__((aligned(16))) char smem_[163840];
  AS3 char* sm = (AS3 char*)smem_;
  u64 ptm = 0, pc0 = 0, pt0 = 0, pte = 0, st_[16] = {};
  u32x16 sts;
  u32x4 st2;
  asm volatile(
      "s_memrealtime s[12:13]\n"
      "s_load_dwordx16 s[64:79], %[ka], 0x0\n"
      "s_load_dwordx8 s[80:87], %[ka], 0x40\n"
      "s_load_dwordx2 s[88:89], %[ka], 0x60\n"
      "v_readfirstlane_b32 s33, v0\n"
      "s_getpc_b64 s[44:45]\n"
      ".Lpc%=:\n"
      "s_mov_b32 s34, %[bid]\n"
      "v_and_b32 v43, 63, v0\n"
      "v_mov_b32 v15, 0\n"
      "s_lshr_b32 s33, s33, 6\n"
      "s_and_b32 s52, s34, -8\n"
      "s_add_u32 s52, s52, s33\n"
      "s_min_u32 s52, s52, (.LEnd%=-.Lpc%=)>>7\n"
      "s_lshl_b32 s52, s52, 7\n"
      "s_and_b32 s44, s44, 0xffffff80\n"
      "s_add_u32 s44, s44, s52\n"
      "s_addc_u32 s45, s45, 0\n"
      "global_load_dword v11, v15, s[44:45] \n"
      "s_and_b32 s48, s34, 7\n"
      "s_lshr_b32 s49, s34, 3\n"
      "s_and_b32 s55, s49, 15\n"
      "s_lshl4_add_u32 s35, s48, s55\n"
      "s_cmp_lt_u32 s34, 128\n"
      "s_cbranch_scc1 .LH%=\n"
      "s_lshl3_add_u32 s50, s35, s33\n"
      "s_lshr_b32 s51, s34, 5\n"
      "s_andn2_b32 s51, 4, s51\n"
      "s_add_u32 s50, s50, s51\n"
      "s_lshl_b32 s51, s50, 8\n"
      "s_lshl_b32 s50, s50, 1\n"
      "v_lshl_add_u32 v48, v43, 2, s51\n"
      "v_mov_b32 v49, s50\n"
      "s_getpc_b64 s[46:47]\n"
      "s_add_u32 s46, s46, .Ltab%=@rel32@lo+4\n"
      "s_addc_u32 s47, s47, .Ltab%=@rel32@hi+12\n"
      "v_lshlrev_b32 v59, 4, v43\n"
      "s_waitcnt lgkmcnt(0)\n"
      "global_load_dword v16, v48, s[66:67] nt\n"
      "global_load_dword v17, v48, s[68:69] nt\n"
      "global_load_ushort v12, v49, s[74:75] \n"
      "global_load_dword v18, v48, s[70:71] nt\n"
      "global_load_dword v19, v48, s[72:73] nt\n"
      "global_load_ushort v13, v49, s[78:79] \n"
      "global_load_dwordx4 v[64:67], v59, s[46:47] \n"
      "global_load_dwordx4 v[68:71], v59, s[46:47] offset:1024\n"
      "global_load_dwordx4 v[44:47], v59, s[46:47] offset:2048\n"
      "s_load_dword s15, s[88:89], 0x0\n"
      "s_setprio 3\n"
      "s_branch .LC%=\n"
      ".LH%=:\n"
      "s_cmp_ge_u32 s33, 4\n"
      "s_cbranch_scc1 .LG%=\n"
      ".LD%=:\n"
      "v_and_b32 v52, 48, v43\n"
      "v_and_b32 v53, 3, v43\n"
      "v_lshl_add_u32 v53, v53, 2, v52\n"
      "v_and_b32 v54, 12, v43\n"
      "v_lshlrev_b32 v53, 2, v53\n"
      "v_lshl_add_u32 v52, v54, 9, v53\n"
      "s_and_b32 s53, s34, 127\n"
      "s_and_b32 s52, s33, 3\n"
      "s_lshl2_add_u32 s14, s53, s52\n"
      "s_lshl_b32 s51, s14, 13\n"
      "v_lshl_add_u32 v53, v54, 6, v53\n"
      "s_waitcnt lgkmcnt(0)\n"
      "s_add_u32 s52, s64, s51\n"
      "s_addc_u32 s53, s65, 0\n"
      "global_load_dwordx4 v[224:227], v53, s[76:77] offset:0\n"
      "global_load_dwordx4 v[228:231], v53, s[76:77] offset:1024\n"
      "global_load_dwordx4 v[192:195], v52, s[52:53] offset:0\n"
      "global_load_dwordx4 v[196:199], v52, s[52:53] offset:256\n"
      "global_load_dwordx4 v[200:203], v52, s[52:53] offset:512\n"
      "global_load_dwordx4 v[204:207], v52, s[52:53] offset:768\n"
      "global_load_dwordx4 v[208:211], v52, s[52:53] offset:1024\n"
      "global_load_dwordx4 v[212:215], v52, s[52:53] offset:1280\n"
      "global_load_dwordx4 v[216:219], v52, s[52:53] offset:1536\n"
      "global_load_dwordx4 v[220:223], v52, s[52:53] offset:1792\n"
      "s_load_dword s15, s[88:89], 0x0\n"
      "s_setprio 3\n"
      "s_cmp_lg_u32 s33, 0\n"
      "s_cbranch_scc1 .Ltb%=\n"
      "v_mov_b32 v48, 0x22680\n"
      "v_mov_b32 v49, s12\n"
      "ds_write_b32 v48, v49 \n"
      "v_and_b32 v50, 31, v43\n"
      "v_lshlrev_b32 v50, 2, v50\n"
      "v_add_u32 v50, 0x22600, v50\n"
      "ds_write_b32 v50, v15 \n"
      "s_waitcnt lgkmcnt(0)\n"
      ".Ltb%=:\n"
      "s_barrier\n"
      "s_mov_b32 s16, s88\n"
      "s_and_b32 s17, s89, 0xffff\n"
      "s_mov_b32 s18, 0x6e0000\n"
      "s_mov_b32 s19, 0x20000\n"
      "s_mov_b32 s20, s64\n"
      "s_and_b32 s21, s65, 0xffff\n"
      "s_mov_b32 s22, 0x400000\n"
      "s_mov_b32 s23, 0x20000\n"
      "s_mov_b32 s28, s84\n"
      "s_and_b32 s29, s85, 0xffff\n"
      "s_mov_b32 s30, 0x700000\n"
      "s_mov_b32 s31, 0x20000\n"
      "s_mov_b32 s24, s86\n"
      "s_and_b32 s25, s87, 0xffff\n"
      "s_mov_b32 s26, 0x400000\n"
      "s_mov_b32 s27, 0x20000\n"
      "s_lshl_b32 s42, s34, 3\n"
      "s_add_u32 s42, s42, 2048\n"
      "s_lshl_b32 s43, s48, 8\n"
      "s_and_b32 s50, s49, 1\n"
      "s_lshl_b32 s50, s50, 7\n"
      "s_add_u32 s43, s43, s50\n"
      "s_add_u32 s43, s43, 65536\n"
      "v_mov_b32 v20, 0x3f803f80\n"
      "v_mov_b32 v21, 0x3f803f80\n"
      "v_mov_b32 v22, 0x3f803f80\n"
      "v_mov_b32 v23, 0x3f803f80\n"
      "v_mov_b32 v44, 0x7f000000\n"
      "v_mov_b32 v25, 0\n"
      "s_lshr_b32 s70, s14, 7\n"
      "s_mul_i32 s70, s70, 61440\n"
      "s_lshl_b32 s68, s14, 5\n"
      "s_add_u32 s68, s68, s70\n"
      "s_add_u32 s68, s68, 131072\n"
      "s_lshr_b32 s70, s14, 8\n"
      "s_mul_i32 s70, s70, 61440\n"
      "s_lshl4_add_u32 s69, s14, s70\n"
      "s_add_u32 s69, s69, 393216\n"
      "v_writelane_b32 v44, s68, 0\n"
      "s_add_u32 s68, s68, 8\n"
      "v_writelane_b32 v44, s68, 20\n"
      "s_add_u32 s68, s68, 8\n"
      "v_writelane_b32 v44, s68, 40\n"
      "s_add_u32 s68, s68, 8\n"
      "v_writelane_b32 v44, s68, 60\n"
      "s_add_u32 s68, s68, 8\n"
      "v_writelane_b32 v44, s69, 1\n"
      "s_add_u32 s69, s69, 8\n"
      "v_writelane_b32 v44, s69, 2\n"
      "s_waitcnt lgkmcnt(0)\n"
      "s_add_u32 s15, s15, 0x9e3779b1\n"
      "v_mov_b32 v47, s15\n"
      "s_waitcnt vmcnt(9)\n"
      "v_mfma_f32_16x16x32_bf16 v[16:19], v[20:23], v[224:227], 0\n"
      "s_waitcnt vmcnt(8)\n"
      "v_mfma_f32_16x16x32_bf16 v[16:19], v[20:23], v[228:231], v[16:19]\n"
      "s_waitcnt vmcnt(7)\n"
      "v_mfma_f32_16x16x32_bf16 v[232:235], v[192:195], v[224:227], 0\n"
      "v_mfma_f32_16x16x32_bf16 v[252:255], v[192:195], v[192:195], 0\n"
      "v_mfma_f32_16x16x32_bf16 v[248:251], v[192:195], v[20:23], 0\n"
      "s_waitcnt vmcnt(6)\n"
      "v_mfma_f32_16x16x32_bf16 v[236:239], v[196:199], v[224:227], 0\n"
      "v_mfma_f32_16x16x32_bf16 v[252:255], v[196:199], v[196:199], v[252:255]\n"
      "v_mfma_f32_16x16x32_bf16 v[248:251], v[196:199], v[20:23], v[248:251]\n"
      "s_waitcnt vmcnt(5)\n"
      "v_mfma_f32_16x16x32_bf16 v[240:243], v[200:203], v[224:227], 0\n"
      "v_mfma_f32_16x16x32_bf16 v[252:255], v[200:203], v[200:203], v[252:255]\n"
      "v_mfma_f32_16x16x32_bf16 v[248:251], v[200:203], v[20:23], v[248:251]\n"
      "s_waitcnt vmcnt(4)\n"
      "v_mfma_f32_16x16x32_bf16 v[244:247], v[204:207], v[224:227], 0\n"
      "v_mfma_f32_16x16x32_bf16 v[252:255], v[204:207], v[204:207], v[252:255]\n"
      "v_mfma_f32_16x16x32_bf16 v[248:251], v[204:207], v[20:23], v[248:251]\n"
      "s_waitcnt vmcnt(3)\n"
      "v_mfma_f32_16x16x32_bf16 v[232:235], v[208:211], v[228:231], v[232:235]\n"
      "v_mfma_f32_16x16x32_bf16 v[252:255], v[208:211], v[208:211], v[252:255]\n"
      "v_mfma_f32_16x16x32_bf16 v[248:251], v[208:211], v[20:23], v[248:251]\n"
      "v_add_f32_dpp v16, v16, v16 quad_perm:[1,0,3,2] row_mask:0xf bank_mask:0xf\n"
      "s_waitcnt vmcnt(2)\n"
      "v_mfma_f32_16x16x32_bf16 v[236:239], v[212:215], v[228:231], v[236:239]\n"
      "v_mfma_f32_16x16x32_bf16 v[252:255], v[212:215], v[212:215], v[252:255]\n"
      "v_mfma_f32_16x16x32_bf16 v[248:251], v[212:215], v[20:23], v[248:251]\n"
      "v_add_f32_dpp v16, v16, v16 quad_perm:[2,3,0,1] row_mask:0xf bank_mask:0xf\n"
      "v_add_f32_dpp v25, v232, v25 quad_perm:[0,0,0,0] row_mask:0xf bank_mask:0x1\n"
      "s_nop 1\n"
      "v_add_f32_dpp v25, v233, v25 quad_perm:[1,1,1,1] row_mask:0xf bank_mask:0x1\n"
      "s_nop 1\n"
      "v_add_f32_dpp v25, v234, v25 quad_perm:[2,2,2,2] row_mask:0xf bank_mask:0x1\n"
      "s_nop 1\n"
      "v_add_f32_dpp v25, v235, v25 quad_perm:[3,3,3,3] row_mask:0xf bank_mask:0x1\n"
      "s_waitcnt vmcnt(1)\n"
      "v_mfma_f32_16x16x32_bf16 v[240:243], v[216:219], v[228:231], v[240:243]\n"
      "v_mfma_f32_16x16x32_bf16 v[252:255], v[216:219], v[216:219], v[252:255]\n"
      "v_mfma_f32_16x16x32_bf16 v[248:251], v[216:219], v[20:23], v[248:251]\n"
      "v_add_f32_dpp v16, v16, v16 row_ror:4 row_mask:0xf bank_mask:0xf\n"
      "v_add_f32_dpp v25, v236, v25 quad_perm:[0,0,0,0] row_mask:0xf bank_mask:0x2\n"
      "s_nop 1\n"
      "v_add_f32_dpp v25, v237, v25 quad_perm:[1,1,1,1] row_mask:0xf bank_mask:0x2\n"
      "s_nop 1\n"
      "v_add_f32_dpp v25, v238, v25 quad_perm:[2,2,2,2] row_mask:0xf bank_mask:0x2\n"
      "s_nop 1\n"
      "v_add_f32_dpp v25, v239, v25 quad_perm:[3,3,3,3] row_mask:0xf bank_mask:0x2\n"
      "s_waitcnt vmcnt(0)\n"
      "v_mfma_f32_16x16x32_bf16 v[244:247], v[220:223], v[228:231], v[244:247]\n"
      "v_mfma_f32_16x16x32_bf16 v[252:255], v[220:223], v[220:223], v[252:255]\n"
      "v_mfma_f32_16x16x32_bf16 v[248:251], v[220:223], v[20:23], v[248:251]\n"
      "v_add_f32_dpp v16, v16, v16 row_ror:8 row_mask:0xf bank_mask:0xf\n"
      "v_add_f32_dpp v25, v240, v25 quad_perm:[0,0,0,0] row_mask:0xf bank_mask:0x4\n"
      "s_nop 1\n"
      "v_add_f32_dpp v25, v241, v25 quad_perm:[1,1,1,1] row_mask:0xf bank_mask:0x4\n"
      "s_nop 1\n"
      "v_add_f32_dpp v25, v242, v25 quad_perm:[2,2,2,2] row_mask:0xf bank_mask:0x4\n"
      "s_nop 1\n"
      "v_add_f32_dpp v25, v243, v25 quad_perm:[3,3,3,3] row_mask:0xf bank_mask:0x4\n"
      "s_nop 1\n"
      "v_add_f32_dpp v25, v244, v25 quad_perm:[0,0,0,0] row_mask:0xf bank_mask:0x8\n"
      "v_mov_b32_dpp v24, v252 quad_perm:[0,0,0,0] row_mask:0xf bank_mask:0xf\n"
      "v_mov_b32_dpp v27, v254 quad_perm:[2,2,2,2] row_mask:0xf bank_mask:0xf\n"
      "v_add_f32_dpp v25, v245, v25 quad_perm:[1,1,1,1] row_mask:0xf bank_mask:0x8\n"
      "v_add_f32 v248, v248, v249\n"
      "v_add_f32 v250, v250, v251\n"
      "v_add_f32_dpp v24, v253, v24 quad_perm:[1,1,1,1] row_mask:0xf bank_mask:0xf\n"
      "v_add_f32_dpp v27, v255, v27 quad_perm:[3,3,3,3] row_mask:0xf bank_mask:0xf\n"
      "v_add_f32_dpp v25, v246, v25 quad_perm:[2,2,2,2] row_mask:0xf bank_mask:0x8\n"
      "v_add_f32 v248, v248, v250\n"
      "v_add_f32 v24, v24, v27\n"
      "v_add_f32_dpp v25, v247, v25 quad_perm:[3,3,3,3] row_mask:0xf bank_mask:0x8\n"
      "v_mul_f32 v248, 0x3a800000, v248\n"
      "v_mul_f32 v24, 0x3a800000, v24\n"
      "v_add_f32_dpp v25, v25, v25 row_ror:4 row_mask:0xf bank_mask:0xf\n"
      "v_fma_f32 v24, -v248, v248, v24\n"
      "v_max_f32 v24, 0, v24\n"
      "v_add_f32_dpp v25, v25, v25 row_ror:8 row_mask:0xf bank_mask:0xf\n"
      "v_cvt_pk_bf16_f32 v248, v248, v24\n"
      "v_and_b32 v24, 0xffff0000, v248\n"
      "v_add_f32 v24, 0x3727c5ac, v24\n"
      "v_lshlrev_b32 v26, 16, v248\n"
      "v_cvt_pk_bf16_f32 v24, v24, v24\n"
      "v_fma_f32 v25, -v26, v16, v25\n"
      "v_and_b32 v24, 0xffff0000, v24\n"
      "v_sqrt_f32 v24, v24\n"
      "s_nop 0\n"
      "v_cvt_pk_bf16_f32 v46, v26, v24\n"
      "v_and_b32 v24, 0xffff0000, v46\n"
      "v_rcp_f32 v24, v24\n"
      "s_nop 0\n"
      "v_mul_f32 v25, v25, v24\n"
      "v_cvt_pk_bf16_f32 v25, v25, v25\n"
      "s_nop 0\n"
      "v_readlane_b32 s68, v25, 0\n"
      "v_readlane_b32 s69, v25, 20\n"
      "v_readlane_b32 s70, v25, 40\n"
      "v_readlane_b32 s71, v25, 60\n"
      "s_pack_ll_b32_b16 s68, s68, s69\n"
      "s_pack_ll_b32_b16 s70, s70, s71\n"
      "v_writelane_b32 v46, s68, 1\n"
      "v_writelane_b32 v46, s70, 2\n"
      "buffer_store_dwordx2 v[46:47], v44, s[16:19], 0 offen sc1\n"
      "v_mov_b32 v48, v43\n"
      "v_and_b32 v49, 7, v43\n"
      "v_lshrrev_b32 v50, 3, v43\n"
      "s_cmp_ge_u32 s34, 112\n"
      "s_cbranch_scc1 .LS%=\n"
      "s_branch .LD2%=\n"
      ".LC%=:\n"
      "s_mov_b32 s16, s88\n"
      "s_and_b32 s17, s89, 0xffff\n"
      "s_mov_b32 s18, 0x6e0000\n"
      "s_mov_b32 s19, 0x20000\n"
      "s_mov_b32 s20, s64\n"
      "s_and_b32 s21, s65, 0xffff\n"
      "s_mov_b32 s22, 0x400000\n"
      "s_mov_b32 s23, 0x20000\n"
      "s_mov_b32 s24, s86\n"
      "s_and_b32 s25, s87, 0xffff\n"
      "s_mov_b32 s26, 0x400000\n"
      "s_mov_b32 s27, 0x20000\n"
      "s_mul_i32 s39, s33, 6528\n"
      "s_mul_i32 s40, s33, 8768\n"
      "s_add_u32 s52, s40, 52224\n"
      "s_add_u32 s53, s52, 4160\n"
      "s_mul_i32 s54, s33, 4864\n"
      "s_add_u32 s54, s54, 122368\n"
      "s_lshl_b32 s41, s35, 4\n"
      "s_and_b32 s43, s41, 0xffffff80\n"
      "s_add_u32 s43, s43, 67584\n"
      "s_lshl_b32 s42, s34, 3\n"
      "s_add_u32 s42, s42, 2048\n"
      "v_and_b32 v48, 31, v43\n"
      "v_lshrrev_b32 v49, 5, v43\n"
      "v_lshl_add_u32 v52, v43, 2, s52\n"
      "v_mov_b32 v53, 0x3c00\n"
      "ds_write_b32 v52, v53 offset:2176\n"
      "ds_write_b32 v52, v53 offset:5440\n"
      "v_lshl_add_u32 v52, v43, 2, s39\n"
      "ds_write_b32 v52, v53 \n"
      "v_lshl_add_u32 v52, v48, 1, v49\n"
      "s_lshl_b32 s50, s33, 17\n"
      "v_lshl_add_u32 v8, v52, 11, s50\n"
      "v_lshl_add_u32 v58, v49, 8, v48\n"
      "s_lshl_b32 s50, s33, 5\n"
      "v_add_u32 v58, s50, v58\n"
      "s_mov_b32 s50, 122912\n"
      "v_lshl_add_u32 v32, v58, 2, s50\n"
      "v_lshlrev_b32 v41, 12, v58\n"
      "v_mul_u32_u24 v42, 0xf000, v49\n"
      "v_lshl_add_u32 v42, v58, 4, v42\n"
      "s_add_u32 s50, s54, 528\n"
      "v_add_u32 v34, s50, v59\n"
      "v_lshl_add_u32 v35, v43, 3, s39\n"
      "v_lshl_add_u32 v36, v43, 3, s52\n"
      "s_add_u32 s50, s39, 32\n"
      "v_add_u32 v37, s50, v59\n"
      "s_add_u32 s50, s52, 32\n"
      "v_add_u32 v38, s50, v59\n"
      "v_mov_b32 v54, 0\n"
      "v_mov_b32 v55, 0\n"
      "v_lshl_add_u32 v52, v43, 3, s54\n"
      "ds_write_b64 v52, v[54:55] offset:32\n"
      "s_mov_b32 s50, 161536\n"
      "v_lshl_add_u32 v52, v0, 2, s50\n"
      "ds_write_b32 v52, v54 \n"
      "v_and_b32 v9, 7, v43\n"
      "v_lshlrev_b32 v9, 3, v9\n"
      "v_bfe_u32 v10, v43, 3, 2\n"
      "v_lshlrev_b32 v10, 2, v10\n"
      "s_and_b32 s58, s33, 3\n"
      "s_bitcmp1_b32 s55, 0\n"
      "s_cselect_b32 s56, s82, s80\n"
      "s_cselect_b32 s57, s83, s81\n"
      "s_lshr_b32 s50, s56, 7\n"
      "s_sub_u32 s50, s48, s50\n"
      "s_add_u32 s50, s50, 3\n"
      "s_and_b32 s50, s50, 7\n"
      "s_min_u32 s50, s50, 6\n"
      "s_mul_i32 s51, s48, 7\n"
      "s_add_u32 s50, s50, s51\n"
      "s_lshl_b32 s50, s50, 7\n"
      "s_lshr_b32 s51, s55, 1\n"
      "s_mul_i32 s51, s51, 24\n"
      "s_lshl_b32 s58, s58, 8\n"
      "s_add_u32 s51, s51, s58\n"
      "s_add_u32 s51, s51, 64\n"
      "s_mul_i32 s51, s51, 7168\n"
      "s_add_u32 s50, s50, s51\n"
      "v_min_u32 v57, 23, v43\n"
      "v_mul_u32_u24 v57, 0x1c00, v57\n"
      "v_add_u32 v57, s50, v57\n"
      "v_mov_b32 v52, s39\n"
      "v_mov_b32 v53, s52\n"
      "s_mov_b32 s50, 0xffff0000\n"
      "s_mov_b32 s51, 0xffff0000\n"
      "v_cndmask_b32 v52, v52, v53, s[50:51]\n"
      "s_lshl_b32 s50, s33, 6\n"
      "s_add_u32 s50, s50, 56384\n"
      "s_lshl4_add_u32 s58, s33, s55\n"
      "s_lshl_b32 s58, s58, 15\n"
      "s_and_b32 s51, s55, 8\n"
      "s_lshl_b32 s51, s51, 4\n"
      "s_sub_u32 s58, s58, s51\n"
      "s_mov_b32 s44, s50\n"
      "v_mov_b32 v28, v52\n"
      "v_bfe_u32 v52, v43, 1, 4\n"
      "v_and_b32 v53, 1, v43\n"
      "v_lshl_add_u32 v52, v52, 4, v53\n"
      "v_lshl_add_u32 v15, v52, 7, s58\n"
      ".Lsc%=:\n"
      "s_waitcnt vmcnt(3)\n"
      "s_mov_b32 s50, 0x3fb8aa3b\n"
      "v_mov_b32 v14, v57\n"
      "v_lshlrev_b32 v48, 16, v12\n"
      "v_and_b32 v52, 0x7fff7fff, v18\n"
      "v_and_b32 v53, 0x7fff7fff, v19\n"
      "v_mul_f32 v49, s50, v48\n"
      "v_pk_max_u16 v52, v52, v53\n"
      "v_fma_f32 v50, v48, s50, -v49\n"
      "v_rndne_f32 v51, v49\n"
      "v_lshlrev_b32 v53, 16, v52\n"
      "v_fmamk_f32 v50, v48, 0x32a5705f, v50\n"
      "v_sub_f32 v49, v49, v51\n"
      "v_max_u32 v52, v52, v53\n"
      "v_add_f32 v49, v49, v50\n"
      "v_cvt_i32_f32 v51, v51\n"
      "v_max_u32_dpp v52, v52, v52 row_shr:1 row_mask:0xf bank_mask:0xf bound_ctrl:1\n"
      "v_exp_f32 v49, v49\n"
      "v_lshlrev_b32 v54, 16, v13\n"
      "v_max_u32_dpp v52, v52, v52 row_shr:2 row_mask:0xf bank_mask:0xf bound_ctrl:1\n"
      "v_ldexp_f32 v49, v49, v51\n"
      "v_add_f32 v54, 1.0, v54\n"
      "v_max_u32_dpp v52, v52, v52 row_shr:4 row_mask:0xf bank_mask:0xf bound_ctrl:1\n"
      "v_cvt_pk_bf16_f32 v49, v49, v49\n"
      "s_waitcnt lgkmcnt(0)\n"
      "v_max_u32_dpp v52, v52, v52 row_shr:8 row_mask:0xf bank_mask:0xf bound_ctrl:1\n"
      "v_and_b32 v49, 0xffff0000, v49\n"
      "v_and_b32 v55, 0x7fffffff, v54\n"
      "v_max_u32_dpp v52, v52, v52 row_bcast:15 row_mask:0xa bank_mask:0xf\n"
      "v_mul_f32 v50, 0xbfb8aa3b, v49\n"
      "v_mul_f32 v51, 0xbe22f983, v49\n"
      "v_max_u32_dpp v52, v52, v52 row_bcast:31 row_mask:0xc bank_mask:0xf\n"
      "v_readfirstlane_b32 s36, v50\n"
      "v_readlane_b32 s50, v52, 63\n"
      "v_readfirstlane_b32 s37, v51\n"
      "v_mul_f32 v55, 0x3b800000, v55\n"
      "v_mul_f32 v52, s50, v49\n"
      "s_add_u32 s15, s15, 0x9e3779b1\n"
      "v_max_f32 v52, v52, v55\n"
      "v_frexp_exp_i32_f32 v52, v52\n"
      "s_nop 0\n"
      "v_readfirstlane_b32 s50, v52\n"
      "s_nop 0\n"
      "s_sub_i32 s50, 7, s50\n"
      "s_max_i32 s50, s50, -107\n"
      "s_min_i32 s50, s50, 93\n"
      "v_ldexp_f32 v49, v49, s50\n"
      "s_sub_i32 s51, s50, 1\n"
      "v_ldexp_f32 v54, v54, s51\n"
      "s_sub_i32 s14, 128, s50\n"
      "s_lshl_b32 s14, s14, 23\n"
      "v_readfirstlane_b32 s38, v49\n"
      "s_mov_b64 s[60:61], 1\n"
      "v_cndmask_b32 v31, 0, v54, s[60:61]\n"
      "s_lshl_b32 s50, s33, 2\n"
      "s_add_u32 s50, s50, 161280\n"
      "v_mov_b32 v50, s50\n"
      "v_mov_b32 v51, s14\n"
      "ds_write_b32 v50, v51 \n"
      "s_mov_b32 s60, s41\n"
      "s_add_u32 s61, s41, 0x200000\n"
      "s_mov_b32 s62, 131072\n"
      "s_mov_b32 s63, 262144\n"
      "s_mov_b32 m0, s40\n"
      "s_add_u32 s13, s40, 52224\n"
      "s_add_u32 s12, s39, 272\n"
      "v_lshlrev_b32 v72, 16, v16\n"
      "v_and_b32 v73, 0xffff0000, v16\n"
      "v_lshlrev_b32 v74, 16, v17\n"
      "v_and_b32 v75, 0xffff0000, v17\n"
      "s_mov_b32 s64, 0x3fb8aa3b\n"
      "s_mov_b32 s66, 0x43800000\n"
      "s_mov_b32 s68, 0x42000000\n"
      "s_mov_b32 s70, s37\n"
      "v_pk_mul_f32 v[72:73], s[64:65], v[72:73] op_sel_hi:[0,1]\n"
      "v_pk_mul_f32 v[74:75], s[70:71], v[74:75] op_sel_hi:[0,1]\n"
      "v_exp_f32 v72, v72\n"
      "v_exp_f32 v73, v73\n"
      "v_pk_mul_f32 v[78:79], s[66:67], v[74:75] op_sel_hi:[0,1]\n"
      "v_pk_mul_f32 v[80:81], s[68:69], v[74:75] op_sel_hi:[0,1]\n"
      "v_fract_f32 v76, v74\n"
      "v_fract_f32 v77, v75\n"
      "v_cvt_pk_bf16_f32 v72, v72, v73\n"
      "v_cos_f32 v90, v76\n"
      "v_cos_f32 v91, v77\n"
      "v_fract_f32 v78, v78\n"
      "v_fract_f32 v79, v79\n"
      "v_and_b32 v73, 0xffff0000, v72\n"
      "v_lshlrev_b32 v72, 16, v72\n"
      "v_sin_f32 v92, v76\n"
      "v_sin_f32 v93, v77\n"
      "v_pk_mul_f32 v[82:83], s[36:37], v[72:73] op_sel_hi:[0,1]\n"
      "v_fract_f32 v80, v80\n"
      "v_fract_f32 v81, v81\n"
      "v_exp_f32 v84, v82\n"
      "v_exp_f32 v85, v83\n"
      "v_pk_mul_f32 v[86:87], s[66:67], v[82:83] op_sel_hi:[0,1]\n"
      "v_cos_f32 v94, v78\n"
      "v_cos_f32 v95, v79\n"
      "v_pk_mul_f32 v[88:89], s[68:69], v[82:83] op_sel_hi:[0,1]\n"
      "v_sin_f32 v96, v78\n"
      "v_sin_f32 v97, v79\n"
      "v_pk_mul_f32 v[90:91], v[90:91], v[84:85]\n"
      "v_exp_f32 v86, v86\n"
      "v_exp_f32 v87, v87\n"
      "v_pk_mul_f32 v[92:93], v[92:93], v[84:85]\n"
      "v_pk_mul_f32 v[82:83], v[92:93], v[92:93]\n"
      "v_cos_f32 v202, v80\n"
      "v_pk_add_f32 v[84:85], v[90:91], v[90:91]\n"
      "v_cos_f32 v150, v81\n"
      "v_pk_fma_f32 v[98:99], v[90:91], v[90:91], v[82:83] neg_lo:[0,0,1] neg_hi:[0,0,1]\n"
      "v_pk_mul_f32 v[94:95], v[94:95], v[86:87]\n"
      "v_pk_mul_f32 v[100:101], v[84:85], v[92:93]\n"
      "v_pk_mul_f32 v[96:97], v[96:97], v[86:87]\n"
      "v_cvt_pk_f16_f32 v208, v90, v92\n"
      "v_sin_f32 v203, v80\n"
      "v_cvt_pk_f16_f32 v156, v91, v93\n"
      "v_sin_f32 v151, v81\n"
      "v_pk_mul_f32 v[82:83], v[100:101], v[100:101]\n"
      "v_exp_f32 v88, v88\n"
      "v_pk_add_f32 v[84:85], v[98:99], v[98:99]\n"
      "v_exp_f32 v89, v89\n"
      "v_pk_fma_f32 v[102:103], v[98:99], v[98:99], v[82:83] neg_lo:[0,0,1] neg_hi:[0,0,1]\n"
      "v_pk_mul_f32 v[114:115], v[96:97], v[96:97]\n"
      "v_pk_mul_f32 v[104:105], v[84:85], v[100:101]\n"
      "v_pk_add_f32 v[116:117], v[94:95], v[94:95]\n"
      "v_cvt_pk_f16_f32 v209, v98, v100\n"
      "v_pk_fma_f32 v[106:107], v[94:95], v[94:95], v[114:115] neg_lo:[0,0,1] neg_hi:[0,0,1]\n"
      "v_cvt_pk_f16_f32 v157, v99, v101\n"
      "v_pk_mul_f32 v[108:109], v[116:117], v[96:97]\n"
      "v_pk_mul_f32 v[82:83], v[104:105], v[104:105]\n"
      "v_cvt_pk_f16_f32 v212, v94, v96\n"
      "v_pk_add_f32 v[84:85], v[102:103], v[102:103]\n"
      "v_cvt_pk_f16_f32 v160, v95, v97\n"
      "v_pk_fma_f32 v[90:91], v[102:103], v[102:103], v[82:83] neg_lo:[0,0,1] neg_hi:[0,0,1]\n"
      "v_pk_mul_f32 v[202:203], v[202:203], v[88:89] op_sel:[0,0] op_sel_hi:[1,0]\n"
      "s_waitcnt vmcnt(0)\n"
      "s_nop 0\n"
      "s_cmp_ge_u32 s33, 4\n"
      "s_cbranch_scc1 .LcR%=\n"
      "buffer_load_dword v11, v15, s[20:23], s41 offen\n"
      "global_load_dword v11, v14, s[56:57]\n"
      ".LcR%=:\n"
      "v_pk_mul_f32 v[92:93], v[84:85], v[104:105]\n"
      "v_pk_mul_f32 v[150:151], v[150:151], v[88:89] op_sel:[0,1] op_sel_hi:[1,1]\n"
      "v_cvt_pk_f16_f32 v210, v102, v104\n"
      "v_cvt_pk_f16_f32 v213, v106, v108\n"
      "v_cvt_pk_f16_f32 v158, v103, v105\n"
      "v_cvt_pk_f16_f32 v161, v107, v109\n"
      "v_pk_mul_f32 v[82:83], v[92:93], v[92:93]\n"
      "v_pk_mul_f32 v[114:115], v[108:109], v[108:109]\n"
      "v_pk_add_f32 v[84:85], v[90:91], v[90:91]\n"
      "v_pk_add_f32 v[116:117], v[106:107], v[106:107]\n"
      "v_pk_fma_f32 v[72:73], v[90:91], v[90:91], v[82:83] neg_lo:[0,0,1] neg_hi:[0,0,1]\n"
      "v_pk_fma_f32 v[94:95], v[106:107], v[106:107], v[114:115] neg_lo:[0,0,1] neg_hi:[0,0,1]\n"
      "v_pk_mul_f32 v[74:75], v[84:85], v[92:93]\n"
      "v_pk_mul_f32 v[96:97], v[116:117], v[108:109]\n"
      "v_cvt_pk_f16_f32 v205, v90, v92\n"
      "v_pk_mul_f16 v214, v212, v213 op_sel_hi:[0,1]\n"
      "v_cvt_pk_f16_f32 v153, v91, v93\n"
      "v_pk_mul_f16 v162, v160, v161 op_sel_hi:[0,1]\n"
      "v_pk_mul_f16 v211, v209, v210 op_sel_hi:[0,1]\n"
      "v_pk_fma_f16 v214, v212, v213, v214 op_sel:[1,1,0] op_sel_hi:[1,0,1] neg_lo:[0,1,0]\n"
      "v_pk_mul_f16 v159, v157, v158 op_sel_hi:[0,1]\n"
      "v_pk_fma_f16 v162, v160, v161, v162 op_sel:[1,1,0] op_sel_hi:[1,0,1] neg_lo:[0,1,0]\n"
      "v_pk_fma_f16 v211, v209, v210, v211 op_sel:[1,1,0] op_sel_hi:[1,0,1] neg_lo:[0,1,0]\n"
      "v_cvt_pk_f16_f32 v215, v94, v96\n"
      "v_pk_fma_f16 v159, v157, v158, v159 op_sel:[1,1,0] op_sel_hi:[1,0,1] neg_lo:[0,1,0]\n"
      "v_cvt_pk_f16_f32 v163, v95, v97\n"
      "v_cvt_pk_f16_f32 v206, v72, v74\n"
      "v_lshlrev_b32 v198, 16, v18\n"
      "v_cvt_pk_f16_f32 v154, v73, v75\n"
      "v_lshlrev_b32 v199, 16, v19\n"
      "v_pk_mul_f16 v207, v205, v206 op_sel_hi:[0,1]\n"
      "v_pk_mul_f32 v[198:199], s[38:39], v[198:199] op_sel_hi:[0,1]\n"
      "v_pk_mul_f16 v155, v153, v154 op_sel_hi:[0,1]\n"
      "v_and_b32 v146, 0xffff0000, v18\n"
      "v_pk_fma_f16 v207, v205, v206, v207 op_sel:[1,1,0] op_sel_hi:[1,0,1] neg_lo:[0,1,0]\n"
      "v_and_b32 v147, 0xffff0000, v19\n"
      "v_pk_fma_f16 v155, v153, v154, v155 op_sel:[1,1,0] op_sel_hi:[1,0,1] neg_lo:[0,1,0]\n"
      "v_pk_mul_f32 v[146:147], s[38:39], v[146:147] op_sel_hi:[0,1]\n"
      "v_cvt_pk_f16_f32 v240, v198, v199\n"
      "v_cvt_pk_f16_f32 v241, v146, v147\n"
      "v_pk_mul_f32 v[200:201], v[198:199], v[202:203] op_sel_hi:[1,0]\n"
      "v_pk_mul_f32 v[148:149], v[146:147], v[150:151] op_sel_hi:[1,0]\n"
      "ds_write_addtid_b32 v240 offset:52224\n"
      "ds_write_addtid_b32 v241 offset:55488\n"
      "v_pk_fma_f32 v[200:201], v[198:199], v[202:203], v[200:201] op_sel:[1,1,0] op_sel_hi:[0,1,1] neg_hi:[0,1,0]\n"
      "v_pk_fma_f32 v[148:149], v[146:147], v[150:151], v[148:149] op_sel:[1,1,0] op_sel_hi:[0,1,1] neg_hi:[0,1,0]\n"
      "ds_write_addtid_b32 v205 offset:54672\n"
      "v_cvt_pk_f16_f32 v242, v200, v201\n"
      "v_cvt_pk_f16_f32 v243, v148, v149\n"
      "v_pk_mul_f32 v[198:199], v[200:201], v[202:203] op_sel_hi:[1,0]\n"
      "v_pk_mul_f32 v[146:147], v[148:149], v[150:151] op_sel_hi:[1,0]\n"
      "ds_write_addtid_b32 v242 offset:52496\n"
      "ds_write_addtid_b32 v243 offset:55760\n"
      "v_pk_fma_f32 v[198:199], v[200:201], v[202:203], v[198:199] op_sel:[1,1,0] op_sel_hi:[0,1,1] neg_hi:[0,1,0]\n"
      "v_pk_fma_f32 v[146:147], v[148:149], v[150:151], v[146:147] op_sel:[1,1,0] op_sel_hi:[0,1,1] neg_hi:[0,1,0]\n"
      "ds_write_addtid_b32 v153 offset:57936\n"
      "v_cvt_pk_f16_f32 v240, v198, v199\n"
      "v_cvt_pk_f16_f32 v241, v146, v147\n"
      "v_pk_mul_f32 v[200:201], v[198:199], v[202:203] op_sel_hi:[1,0]\n"
      "v_pk_mul_f32 v[148:149], v[146:147], v[150:151] op_sel_hi:[1,0]\n"
      "ds_write_addtid_b32 v240 offset:52768\n"
      "ds_write_addtid_b32 v241 offset:56032\n"
      "v_pk_fma_f32 v[200:201], v[198:199], v[202:203], v[200:201] op_sel:[1,1,0] op_sel_hi:[0,1,1] neg_hi:[0,1,0]\n"
      "v_pk_fma_f32 v[148:149], v[146:147], v[150:151], v[148:149] op_sel:[1,1,0] op_sel_hi:[0,1,1] neg_hi:[0,1,0]\n"
      "ds_write_addtid_b32 v206 offset:54944\n"
      "v_cvt_pk_f16_f32 v242, v200, v201\n"
      "v_cvt_pk_f16_f32 v243, v148, v149\n"
      "v_pk_mul_f32 v[198:199], v[200:201], v[202:203] op_sel_hi:[1,0]\n"
      "v_pk_mul_f32 v[146:147], v[148:149], v[150:151] op_sel_hi:[1,0]\n"
      "ds_write_addtid_b32 v242 offset:53040\n"
      "ds_write_addtid_b32 v243 offset:56304\n"
      "v_pk_fma_f32 v[198:199], v[200:201], v[202:203], v[198:199] op_sel:[1,1,0] op_sel_hi:[0,1,1] neg_hi:[0,1,0]\n"
      "v_pk_fma_f32 v[146:147], v[148:149], v[150:151], v[146:147] op_sel:[1,1,0] op_sel_hi:[0,1,1] neg_hi:[0,1,0]\n"
      "ds_write_addtid_b32 v154 offset:58208\n"
      "v_cvt_pk_f16_f32 v240, v198, v199\n"
      "v_cvt_pk_f16_f32 v241, v146, v147\n"
      "v_pk_mul_f32 v[200:201], v[198:199], v[202:203] op_sel_hi:[1,0]\n"
      "v_pk_mul_f32 v[148:149], v[146:147], v[150:151] op_sel_hi:[1,0]\n"
      "ds_write_addtid_b32 v240 offset:53312\n"
      "ds_write_addtid_b32 v241 offset:56576\n"
      "v_pk_fma_f32 v[200:201], v[198:199], v[202:203], v[200:201] op_sel:[1,1,0] op_sel_hi:[0,1,1] neg_hi:[0,1,0]\n"
      "v_pk_fma_f32 v[148:149], v[146:147], v[150:151], v[148:149] op_sel:[1,1,0] op_sel_hi:[0,1,1] neg_hi:[0,1,0]\n"
      "ds_write_addtid_b32 v207 offset:55216\n"
      "v_add_u32_sdwa v20, s52, v64 dst_sel:DWORD dst_unused:UNUSED_PAD src0_sel:DWORD src1_sel:WORD_0\n"
      "v_add_u32_sdwa v21, s39, v64 dst_sel:DWORD dst_unused:UNUSED_PAD src0_sel:DWORD src1_sel:WORD_1\n"
      "v_add_u32_sdwa v178, s52, v71 dst_sel:DWORD dst_unused:UNUSED_PAD src0_sel:DWORD src1_sel:WORD_0\n"
      "v_add_u32_sdwa v22, s53, v65 dst_sel:DWORD dst_unused:UNUSED_PAD src0_sel:DWORD src1_sel:WORD_0\n"
      "v_cvt_pk_f16_f32 v242, v200, v201\n"
      "v_cvt_pk_f16_f32 v243, v148, v149\n"
      "v_pk_mul_f32 v[198:199], v[200:201], v[202:203] op_sel_hi:[1,0]\n"
      "v_pk_mul_f32 v[146:147], v[148:149], v[150:151] op_sel_hi:[1,0]\n"
      "ds_write_addtid_b32 v242 offset:53584\n"
      "ds_write_addtid_b32 v243 offset:56848\n"
      "v_pk_fma_f32 v[198:199], v[200:201], v[202:203], v[198:199] op_sel:[1,1,0] op_sel_hi:[0,1,1] neg_hi:[0,1,0]\n"
      "v_pk_fma_f32 v[146:147], v[148:149], v[150:151], v[146:147] op_sel:[1,1,0] op_sel_hi:[0,1,1] neg_hi:[0,1,0]\n"
      "ds_write_addtid_b32 v155 offset:58480\n"
      "v_add_u32_sdwa v23, s53, v65 dst_sel:DWORD dst_unused:UNUSED_PAD src0_sel:DWORD src1_sel:WORD_1\n"
      "v_add_u32_sdwa v24, s53, v66 dst_sel:DWORD dst_unused:UNUSED_PAD src0_sel:DWORD src1_sel:WORD_0\n"
      "v_add_u32_sdwa v25, s53, v66 dst_sel:DWORD dst_unused:UNUSED_PAD src0_sel:DWORD src1_sel:WORD_1\n"
      "v_add_u32_sdwa v26, s53, v67 dst_sel:DWORD dst_unused:UNUSED_PAD src0_sel:DWORD src1_sel:WORD_0\n"
      "v_cvt_pk_f16_f32 v240, v198, v199\n"
      "v_cvt_pk_f16_f32 v241, v146, v147\n"
      "v_pk_mul_f32 v[200:201], v[198:199], v[202:203] op_sel_hi:[1,0]\n"
      "v_pk_mul_f32 v[148:149], v[146:147], v[150:151] op_sel_hi:[1,0]\n"
      "ds_write_addtid_b32 v240 offset:53856\n"
      "ds_write_addtid_b32 v241 offset:57120\n"
      "v_pk_fma_f32 v[200:201], v[198:199], v[202:203], v[200:201] op_sel:[1,1,0] op_sel_hi:[0,1,1] neg_hi:[0,1,0]\n"
      "v_pk_fma_f32 v[148:149], v[146:147], v[150:151], v[148:149] op_sel:[1,1,0] op_sel_hi:[0,1,1] neg_hi:[0,1,0]\n"
      "v_add_u32_sdwa v27, s53, v67 dst_sel:DWORD dst_unused:UNUSED_PAD src0_sel:DWORD src1_sel:WORD_1\n"
      "v_add_u32_sdwa v29, s53, v68 dst_sel:DWORD dst_unused:UNUSED_PAD src0_sel:DWORD src1_sel:WORD_0\n"
      "v_add_u32_sdwa v30, s53, v68 dst_sel:DWORD dst_unused:UNUSED_PAD src0_sel:DWORD src1_sel:WORD_1\n"
      "v_add_u32_sdwa v39, s53, v69 dst_sel:DWORD dst_unused:UNUSED_PAD src0_sel:DWORD src1_sel:WORD_0\n"
      "v_cvt_pk_f16_f32 v242, v200, v201\n"
      "v_cvt_pk_f16_f32 v243, v148, v149\n"
      "ds_write_addtid_b32 v242 offset:54128\n"
      "ds_write_addtid_b32 v243 offset:57392\n"
      "v_add_u32_sdwa v33, s54, v69 dst_sel:DWORD dst_unused:UNUSED_PAD src0_sel:DWORD src1_sel:WORD_1\n"
      "v_add_u32_sdwa v40, s44, v70 dst_sel:DWORD dst_unused:UNUSED_PAD src0_sel:DWORD src1_sel:WORD_0\n"
      "v_add_u32_sdwa v28, v28, v70 dst_sel:DWORD dst_unused:UNUSED_PAD src0_sel:DWORD src1_sel:WORD_1\n"
      "ds_read_b128 v[104:107], v20 offset:0\n"
      "ds_read_b128 v[108:111], v178 offset:0\n"
      "ds_read_b128 v[112:115], v20 offset:32\n"
      "ds_read_b128 v[116:119], v178 offset:32\n"
      "s_mov_b32 m0, s39\n"
      "s_setprio 2\n"
      "v_pk_mul_f16 v240, v209, v208 op_sel_hi:[0,1]\n"
      "v_pk_mul_f16 v241, v210, v208 op_sel_hi:[0,1]\n"
      "v_pk_mul_f16 v242, v211, v208 op_sel_hi:[0,1]\n"
      "ds_write_addtid_b32 v208 offset:272\n"
      "v_pk_fma_f16 v240, v209, v208, v240 op_sel:[1,1,0] op_sel_hi:[1,0,1] neg_lo:[0,1,0]\n"
      "ds_write_addtid_b32 v209 offset:544\n"
      "v_pk_fma_f16 v241, v210, v208, v241 op_sel:[1,1,0] op_sel_hi:[1,0,1] neg_lo:[0,1,0]\n"
      "ds_write_addtid_b32 v240 offset:816\n"
      "v_pk_fma_f16 v242, v211, v208, v242 op_sel:[1,1,0] op_sel_hi:[1,0,1] neg_lo:[0,1,0]\n"
      "ds_write_addtid_b32 v241 offset:1360\n"
      "ds_write_addtid_b32 v242 offset:1904\n"
      "v_pk_mul_f16 v243, v212, v209 op_sel_hi:[0,1]\n"
      "v_pk_mul_f16 v244, v212, v210 op_sel_hi:[0,1]\n"
      "v_pk_mul_f16 v245, v212, v211 op_sel_hi:[0,1]\n"
      "ds_write_addtid_b32 v210 offset:1088\n"
      "v_pk_fma_f16 v243, v212, v209, v243 op_sel:[1,1,0] op_sel_hi:[1,0,1] neg_lo:[0,1,0]\n"
      "ds_write_addtid_b32 v211 offset:1632\n"
      "v_pk_fma_f16 v244, v212, v210, v244 op_sel:[1,1,0] op_sel_hi:[1,0,1] neg_lo:[0,1,0]\n"
      "ds_write_addtid_b32 v243 offset:2992\n"
      "v_pk_fma_f16 v245, v212, v211, v245 op_sel:[1,1,0] op_sel_hi:[1,0,1] neg_lo:[0,1,0]\n"
      "ds_write_addtid_b32 v244 offset:3264\n"
      "ds_write_addtid_b32 v245 offset:4080\n"
      "v_pk_mul_f16 v246, v213, v210 op_sel_hi:[0,1]\n"
      "v_pk_mul_f16 v247, v214, v210 op_sel_hi:[0,1]\n"
      "v_pk_mul_f16 v248, v215, v215 op_sel_hi:[0,1]\n"
      "ds_write_addtid_b32 v212 offset:2176\n"
      "v_pk_fma_f16 v246, v213, v210, v246 op_sel:[1,1,0] op_sel_hi:[1,0,1] neg_lo:[0,1,0]\n"
      "ds_write_addtid_b32 v213 offset:5440\n"
      "v_pk_fma_f16 v247, v214, v210, v247 op_sel:[1,1,0] op_sel_hi:[1,0,1] neg_lo:[0,1,0]\n"
      "ds_write_addtid_b32 v246 offset:5984\n"
      "v_pk_fma_f16 v248, v215, v215, v248 op_sel:[1,1,0] op_sel_hi:[1,0,1] neg_lo:[0,1,0]\n"
      "ds_write_addtid_b32 v247 offset:6256\n"
      "ds_write_addtid_b32 v248 offset:2448\n"
      "v_pk_mul_f16 v249, v215, v212 op_sel_hi:[0,1]\n"
      "v_pk_mul_f16 v250, v215, v213 op_sel_hi:[0,1]\n"
      "v_pk_mul_f16 v251, v215, v214 op_sel_hi:[0,1]\n"
      "ds_write_addtid_b32 v214 offset:5712\n"
      "v_pk_fma_f16 v249, v215, v212, v249 op_sel:[1,1,0] op_sel_hi:[1,0,1] neg_lo:[0,1,0]\n"
      "ds_write_addtid_b32 v215 offset:4352\n"
      "v_pk_fma_f16 v250, v215, v213, v250 op_sel:[1,1,0] op_sel_hi:[1,0,1] neg_lo:[0,1,0]\n"
      "ds_write_addtid_b32 v249 offset:4624\n"
      "v_pk_fma_f16 v251, v215, v214, v251 op_sel:[1,1,0] op_sel_hi:[1,0,1] neg_lo:[0,1,0]\n"
      "ds_write_addtid_b32 v250 offset:4896\n"
      "ds_write_addtid_b32 v251 offset:5168\n"
      "ds_read_b128 v[72:75], v21 offset:0\n"
      "ds_read_b128 v[76:79], v21 offset:32\n"
      "ds_read_b128 v[80:83], v21 offset:64\n"
      "ds_read_b128 v[84:87], v21 offset:96\n"
      "ds_read_b128 v[88:91], v21 offset:128\n"
      "ds_read_b128 v[92:95], v21 offset:160\n"
      "ds_read_b128 v[96:99], v21 offset:192\n"
      "ds_read_b128 v[100:103], v21 offset:224\n"
      "ds_read_b128 v[120:123], v20 offset:64\n"
      "ds_read_b128 v[124:127], v178 offset:64\n"
      "s_waitcnt lgkmcnt(15)\n"
      "v_pk_mul_f16 v164, v104, v108 op_sel_hi:[0,1] neg_hi:[0,1]\n"
      "v_pk_mul_f16 v165, v105, v109 op_sel_hi:[0,1] neg_hi:[0,1]\n"
      "v_pk_mul_f16 v166, v106, v110 op_sel_hi:[0,1] neg_hi:[0,1]\n"
      "v_pk_mul_f16 v167, v107, v111 op_sel_hi:[0,1] neg_hi:[0,1]\n"
      "v_pk_fma_f16 v164, v104, v108, v164 op_sel:[1,1,0] op_sel_hi:[1,0,1]\n"
      "v_pk_fma_f16 v165, v105, v109, v165 op_sel:[1,1,0] op_sel_hi:[1,0,1]\n"
      "v_pk_fma_f16 v166, v106, v110, v166 op_sel:[1,1,0] op_sel_hi:[1,0,1]\n"
      "v_pk_fma_f16 v167, v107, v111, v167 op_sel:[1,1,0] op_sel_hi:[1,0,1]\n"
      "s_waitcnt lgkmcnt(9)\n"
      "s_nop 0\n"
      "v_mfma_f32_32x32x16_f16 v[180:195], v[72:75], v[164:167], 0\n"
      "v_pk_mul_f16 v240, v157, v156 op_sel_hi:[0,1]\n"
      "v_pk_mul_f16 v241, v158, v156 op_sel_hi:[0,1]\n"
      "v_pk_mul_f16 v242, v159, v156 op_sel_hi:[0,1]\n"
      "v_pk_mul_f16 v168, v112, v116 op_sel_hi:[0,1] neg_hi:[0,1]\n"
      "v_pk_mul_f16 v169, v113, v117 op_sel_hi:[0,1] neg_hi:[0,1]\n"
      "v_pk_mul_f16 v170, v114, v118 op_sel_hi:[0,1] neg_hi:[0,1]\n"
      "v_pk_mul_f16 v171, v115, v119 op_sel_hi:[0,1] neg_hi:[0,1]\n"
      "ds_write_addtid_b32 v156 offset:272\n"
      "v_pk_fma_f16 v240, v157, v156, v240 op_sel:[1,1,0] op_sel_hi:[1,0,1] neg_lo:[0,1,0]\n"
      "ds_write_addtid_b32 v157 offset:544\n"
      "v_pk_fma_f16 v168, v112, v116, v168 op_sel:[1,1,0] op_sel_hi:[1,0,1]\n"
      "v_pk_fma_f16 v169, v113, v117, v169 op_sel:[1,1,0] op_sel_hi:[1,0,1]\n"
      "v_pk_fma_f16 v170, v114, v118, v170 op_sel:[1,1,0] op_sel_hi:[1,0,1]\n"
      "v_pk_fma_f16 v171, v115, v119, v171 op_sel:[1,1,0] op_sel_hi:[1,0,1]\n"
      "ds_read_b128 v[104:107], v20 offset:96\n"
      "ds_read_b128 v[108:111], v178 offset:96\n"
      "s_waitcnt lgkmcnt(12)\n"
      "v_mfma_f32_32x32x16_f16 v[180:195], v[76:79], v[168:171], v[180:195]\n"
      "v_pk_fma_f16 v241, v158, v156, v241 op_sel:[1,1,0] op_sel_hi:[1,0,1] neg_lo:[0,1,0]\n"
      "ds_write_addtid_b32 v240 offset:816\n"
      "v_pk_fma_f16 v242, v159, v156, v242 op_sel:[1,1,0] op_sel_hi:[1,0,1] neg_lo:[0,1,0]\n"
      "s_waitcnt lgkmcnt(5)\n"
      "v_pk_mul_f16 v164, v120, v124 op_sel_hi:[0,1] neg_hi:[0,1]\n"
      "v_pk_mul_f16 v165, v121, v125 op_sel_hi:[0,1] neg_hi:[0,1]\n"
      "v_pk_mul_f16 v166, v122, v126 op_sel_hi:[0,1] neg_hi:[0,1]\n"
      "v_pk_mul_f16 v167, v123, v127 op_sel_hi:[0,1] neg_hi:[0,1]\n"
      "ds_write_addtid_b32 v241 offset:1360\n"
      "ds_write_addtid_b32 v242 offset:1904\n"
      "v_pk_mul_f16 v243, v160, v157 op_sel_hi:[0,1]\n"
      "v_pk_fma_f16 v164, v120, v124, v164 op_sel:[1,1,0] op_sel_hi:[1,0,1]\n"
      "v_pk_fma_f16 v165, v121, v125, v165 op_sel:[1,1,0] op_sel_hi:[1,0,1]\n"
      "v_pk_fma_f16 v166, v122, v126, v166 op_sel:[1,1,0] op_sel_hi:[1,0,1]\n"
      "v_pk_fma_f16 v167, v123, v127, v167 op_sel:[1,1,0] op_sel_hi:[1,0,1]\n"
      "ds_read_b128 v[112:115], v20 offset:128\n"
      "ds_read_b128 v[116:119], v178 offset:128\n"
      "v_mfma_f32_32x32x16_f16 v[180:195], v[80:83], v[164:167], v[180:195]\n"
      "v_pk_mul_f16 v244, v160, v158 op_sel_hi:[0,1]\n"
      "v_pk_mul_f16 v245, v160, v159 op_sel_hi:[0,1]\n"
      "ds_write_addtid_b32 v158 offset:1088\n"
      "s_waitcnt lgkmcnt(6)\n"
      "v_pk_mul_f16 v168, v104, v108 op_sel_hi:[0,1] neg_hi:[0,1]\n"
      "v_pk_mul_f16 v169, v105, v109 op_sel_hi:[0,1] neg_hi:[0,1]\n"
      "v_pk_mul_f16 v170, v106, v110 op_sel_hi:[0,1] neg_hi:[0,1]\n"
      "v_pk_mul_f16 v171, v107, v111 op_sel_hi:[0,1] neg_hi:[0,1]\n"
      "v_pk_fma_f16 v243, v160, v157, v243 op_sel:[1,1,0] op_sel_hi:[1,0,1] neg_lo:[0,1,0]\n"
      "ds_write_addtid_b32 v159 offset:1632\n"
      "v_pk_fma_f16 v244, v160, v158, v244 op_sel:[1,1,0] op_sel_hi:[1,0,1] neg_lo:[0,1,0]\n"
      "v_pk_fma_f16 v168, v104, v108, v168 op_sel:[1,1,0] op_sel_hi:[1,0,1]\n"
      "v_pk_fma_f16 v169, v105, v109, v169 op_sel:[1,1,0] op_sel_hi:[1,0,1]\n"
      "v_pk_fma_f16 v170, v106, v110, v170 op_sel:[1,1,0] op_sel_hi:[1,0,1]\n"
      "v_pk_fma_f16 v171, v107, v111, v171 op_sel:[1,1,0] op_sel_hi:[1,0,1]\n"
      "ds_read_b128 v[120:123], v20 offset:160\n"
      "ds_read_b128 v[124:127], v178 offset:160\n"
      "buffer_load_dwordx4 v[48:51], v41, s[20:23], s60 offen nt\n"
      "v_mfma_f32_32x32x16_f16 v[180:195], v[84:87], v[168:171], v[180:195]\n"
      "ds_write_addtid_b32 v243 offset:2992\n"
      "v_pk_fma_f16 v245, v160, v159, v245 op_sel:[1,1,0] op_sel_hi:[1,0,1] neg_lo:[0,1,0]\n"
      "ds_write_addtid_b32 v244 offset:3264\n"
      "s_waitcnt lgkmcnt(6)\n"
      "v_pk_mul_f16 v164, v112, v116 op_sel_hi:[0,1] neg_hi:[0,1]\n"
      "v_pk_mul_f16 v165, v113, v117 op_sel_hi:[0,1] neg_hi:[0,1]\n"
      "v_pk_mul_f16 v166, v114, v118 op_sel_hi:[0,1] neg_hi:[0,1]\n"
      "v_pk_mul_f16 v167, v115, v119 op_sel_hi:[0,1] neg_hi:[0,1]\n"
      "ds_write_addtid_b32 v245 offset:4080\n"
      "v_pk_mul_f16 v246, v161, v158 op_sel_hi:[0,1]\n"
      "v_pk_mul_f16 v247, v162, v158 op_sel_hi:[0,1]\n"
      "v_pk_fma_f16 v164, v112, v116, v164 op_sel:[1,1,0] op_sel_hi:[1,0,1]\n"
      "v_pk_fma_f16 v165, v113, v117, v165 op_sel:[1,1,0] op_sel_hi:[1,0,1]\n"
      "v_pk_fma_f16 v166, v114, v118, v166 op_sel:[1,1,0] op_sel_hi:[1,0,1]\n"
      "v_pk_fma_f16 v167, v115, v119, v167 op_sel:[1,1,0] op_sel_hi:[1,0,1]\n"
      "ds_read_b128 v[104:107], v20 offset:192\n"
      "ds_read_b128 v[108:111], v178 offset:192\n"
      "v_mfma_f32_32x32x16_f16 v[180:195], v[88:91], v[164:167], v[180:195]\n"
      "v_pk_mul_f16 v248, v163, v163 op_sel_hi:[0,1]\n"
      "ds_write_addtid_b32 v160 offset:2176\n"
      "v_pk_fma_f16 v246, v161, v158, v246 op_sel:[1,1,0] op_sel_hi:[1,0,1] neg_lo:[0,1,0]\n"
      "s_waitcnt lgkmcnt(6)\n"
      "v_pk_mul_f16 v168, v120, v124 op_sel_hi:[0,1] neg_hi:[0,1]\n"
      "v_pk_mul_f16 v169, v121, v125 op_sel_hi:[0,1] neg_hi:[0,1]\n"
      "v_pk_mul_f16 v170, v122, v126 op_sel_hi:[0,1] neg_hi:[0,1]\n"
      "v_pk_mul_f16 v171, v123, v127 op_sel_hi:[0,1] neg_hi:[0,1]\n"
      "ds_write_addtid_b32 v161 offset:5440\n"
      "v_pk_fma_f16 v247, v162, v158, v247 op_sel:[1,1,0] op_sel_hi:[1,0,1] neg_lo:[0,1,0]\n"
      "ds_write_addtid_b32 v246 offset:5984\n"
      "v_pk_fma_f16 v168, v120, v124, v168 op_sel:[1,1,0] op_sel_hi:[1,0,1]\n"
      "v_pk_fma_f16 v169, v121, v125, v169 op_sel:[1,1,0] op_sel_hi:[1,0,1]\n"
      "v_pk_fma_f16 v170, v122, v126, v170 op_sel:[1,1,0] op_sel_hi:[1,0,1]\n"
      "v_pk_fma_f16 v171, v123, v127, v171 op_sel:[1,1,0] op_sel_hi:[1,0,1]\n"
      "ds_read_b128 v[112:115], v20 offset:224\n"
      "ds_read_b128 v[116:119], v178 offset:224\n"
      "v_mfma_f32_32x32x16_f16 v[180:195], v[92:95], v[168:171], v[180:195]\n"
      "v_pk_fma_f16 v248, v163, v163, v248 op_sel:[1,1,0] op_sel_hi:[1,0,1] neg_lo:[0,1,0]\n"
      "ds_write_addtid_b32 v247 offset:6256\n"
      "ds_write_addtid_b32 v248 offset:2448\n"
      "s_waitcnt lgkmcnt(7)\n"
      "v_pk_mul_f16 v164, v104, v108 op_sel_hi:[0,1] neg_hi:[0,1]\n"
      "v_pk_mul_f16 v165, v105, v109 op_sel_hi:[0,1] neg_hi:[0,1]\n"
      "v_pk_mul_f16 v166, v106, v110 op_sel_hi:[0,1] neg_hi:[0,1]\n"
      "v_pk_mul_f16 v167, v107, v111 op_sel_hi:[0,1] neg_hi:[0,1]\n"
      "v_pk_mul_f16 v249, v163, v160 op_sel_hi:[0,1]\n"
      "v_pk_mul_f16 v250, v163, v161 op_sel_hi:[0,1]\n"
      "v_pk_mul_f16 v251, v163, v162 op_sel_hi:[0,1]\n"
      "v_pk_fma_f16 v164, v104, v108, v164 op_sel:[1,1,0] op_sel_hi:[1,0,1]\n"
      "v_pk_fma_f16 v165, v105, v109, v165 op_sel:[1,1,0] op_sel_hi:[1,0,1]\n"
      "v_pk_fma_f16 v166, v106, v110, v166 op_sel:[1,1,0] op_sel_hi:[1,0,1]\n"
      "v_pk_fma_f16 v167, v107, v111, v167 op_sel:[1,1,0] op_sel_hi:[1,0,1]\n"
      "ds_read_b128 v[120:123], v20 offset:3264\n"
      "ds_read_b128 v[124:127], v178 offset:3264\n"
      "v_mfma_f32_32x32x16_f16 v[180:195], v[96:99], v[164:167], v[180:195]\n"
      "ds_write_addtid_b32 v162 offset:5712\n"
      "v_pk_fma_f16 v249, v163, v160, v249 op_sel:[1,1,0] op_sel_hi:[1,0,1] neg_lo:[0,1,0]\n"
      "ds_write_addtid_b32 v163 offset:4352\n"
      "s_waitcnt lgkmcnt(6)\n"
      "v_pk_mul_f16 v168, v112, v116 op_sel_hi:[0,1] neg_hi:[0,1]\n"
      "v_pk_mul_f16 v169, v113, v117 op_sel_hi:[0,1] neg_hi:[0,1]\n"
      "v_pk_mul_f16 v170, v114, v118 op_sel_hi:[0,1] neg_hi:[0,1]\n"
      "v_pk_mul_f16 v171, v115, v119 op_sel_hi:[0,1] neg_hi:[0,1]\n"
      "v_pk_fma_f16 v250, v163, v161, v250 op_sel:[1,1,0] op_sel_hi:[1,0,1] neg_lo:[0,1,0]\n"
      "ds_write_addtid_b32 v249 offset:4624\n"
      "v_pk_fma_f16 v251, v163, v162, v251 op_sel:[1,1,0] op_sel_hi:[1,0,1] neg_lo:[0,1,0]\n"
      "v_pk_fma_f16 v168, v112, v116, v168 op_sel:[1,1,0] op_sel_hi:[1,0,1]\n"
      "v_pk_fma_f16 v169, v113, v117, v169 op_sel:[1,1,0] op_sel_hi:[1,0,1]\n"
      "v_pk_fma_f16 v170, v114, v118, v170 op_sel:[1,1,0] op_sel_hi:[1,0,1]\n"
      "v_pk_fma_f16 v171, v115, v119, v171 op_sel:[1,1,0] op_sel_hi:[1,0,1]\n"
      "ds_read_b128 v[104:107], v20 offset:3296\n"
      "ds_read_b128 v[108:111], v178 offset:3296\n"
      "v_mfma_f32_32x32x16_f16 v[180:195], v[100:103], v[168:171], v[180:195]\n"
      "ds_write_addtid_b32 v250 offset:4896\n"
      "ds_write_addtid_b32 v251 offset:5168\n"
      "s_waitcnt lgkmcnt(7)\n"
      "v_pk_mul_f16 v164, v120, v124 op_sel_hi:[0,1] neg_hi:[0,1]\n"
      "v_pk_mul_f16 v165, v121, v125 op_sel_hi:[0,1] neg_hi:[0,1]\n"
      "v_pk_mul_f16 v166, v122, v126 op_sel_hi:[0,1] neg_hi:[0,1]\n"
      "v_pk_mul_f16 v167, v123, v127 op_sel_hi:[0,1] neg_hi:[0,1]\n"
      "ds_read_b128 v[72:75], v21 offset:0\n"
      "ds_read_b128 v[76:79], v21 offset:32\n"
      "ds_read_b128 v[80:83], v21 offset:64\n"
      "ds_read_b128 v[84:87], v21 offset:96\n"
      "ds_read_b128 v[88:91], v21 offset:128\n"
      "ds_read_b128 v[92:95], v21 offset:160\n"
      "ds_read_b128 v[96:99], v21 offset:192\n"
      "ds_read_b128 v[100:103], v21 offset:224\n"
      "v_pk_fma_f16 v164, v120, v124, v164 op_sel:[1,1,0] op_sel_hi:[1,0,1]\n"
      "v_pk_fma_f16 v165, v121, v125, v165 op_sel:[1,1,0] op_sel_hi:[1,0,1]\n"
      "v_pk_fma_f16 v166, v122, v126, v166 op_sel:[1,1,0] op_sel_hi:[1,0,1]\n"
      "v_pk_fma_f16 v167, v123, v127, v167 op_sel:[1,1,0] op_sel_hi:[1,0,1]\n"
      "ds_read_b128 v[112:115], v20 offset:3328\n"
      "ds_read_b128 v[116:119], v178 offset:3328\n"
      "s_setprio 1\n"
      "s_waitcnt lgkmcnt(9)\n"
      "v_mfma_f32_32x32x16_f16 v[180:195], v[72:75], v[164:167], v[180:195]\n"
      "v_pk_mul_f16 v168, v104, v108 op_sel_hi:[0,1] neg_hi:[0,1]\n"
      "v_pk_mul_f16 v169, v105, v109 op_sel_hi:[0,1] neg_hi:[0,1]\n"
      "v_pk_mul_f16 v170, v106, v110 op_sel_hi:[0,1] neg_hi:[0,1]\n"
      "v_pk_mul_f16 v171, v107, v111 op_sel_hi:[0,1] neg_hi:[0,1]\n"
      "v_pk_fma_f16 v168, v104, v108, v168 op_sel:[1,1,0] op_sel_hi:[1,0,1]\n"
      "v_pk_fma_f16 v169, v105, v109, v169 op_sel:[1,1,0] op_sel_hi:[1,0,1]\n"
      "v_pk_fma_f16 v170, v106, v110, v170 op_sel:[1,1,0] op_sel_hi:[1,0,1]\n"
      "v_pk_fma_f16 v171, v107, v111, v171 op_sel:[1,1,0] op_sel_hi:[1,0,1]\n"
      "ds_read_b128 v[120:123], v20 offset:3360\n"
      "ds_read_b128 v[124:127], v178 offset:3360\n"
      "buffer_load_dwordx4 v[52:55], v41, s[20:23], s60 offen offset:2048 nt\n"
      "s_waitcnt lgkmcnt(10)\n"
      "v_mfma_f32_32x32x16_f16 v[180:195], v[76:79], v[168:171], v[180:195]\n"
      "s_waitcnt lgkmcnt(2)\n"
      "v_pk_mul_f16 v164, v112, v116 op_sel_hi:[0,1] neg_hi:[0,1]\n"
      "v_pk_mul_f16 v165, v113, v117 op_sel_hi:[0,1] neg_hi:[0,1]\n"
      "v_pk_mul_f16 v166, v114, v118 op_sel_hi:[0,1] neg_hi:[0,1]\n"
      "v_pk_mul_f16 v167, v115, v119 op_sel_hi:[0,1] neg_hi:[0,1]\n"
      "v_pk_fma_f16 v164, v112, v116, v164 op_sel:[1,1,0] op_sel_hi:[1,0,1]\n"
      "v_pk_fma_f16 v165, v113, v117, v165 op_sel:[1,1,0] op_sel_hi:[1,0,1]\n"
      "v_pk_fma_f16 v166, v114, v118, v166 op_sel:[1,1,0] op_sel_hi:[1,0,1]\n"
      "v_pk_fma_f16 v167, v115, v119, v167 op_sel:[1,1,0] op_sel_hi:[1,0,1]\n"
      "ds_read_b128 v[104:107], v20 offset:3392\n"
      "ds_read_b128 v[108:111], v178 offset:3392\n"
      "v_mfma_f32_32x32x16_f16 v[180:195], v[80:83], v[164:167], v[180:195]\n"
      "s_waitcnt lgkmcnt(2)\n"
      "v_pk_mul_f16 v168, v120, v124 op_sel_hi:[0,1] neg_hi:[0,1]\n"
      "v_pk_mul_f16 v169, v121, v125 op_sel_hi:[0,1] neg_hi:[0,1]\n"
      "v_pk_mul_f16 v170, v122, v126 op_sel_hi:[0,1] neg_hi:[0,1]\n"
      "v_pk_mul_f16 v171, v123, v127 op_sel_hi:[0,1] neg_hi:[0,1]\n"
      "v_pk_fma_f16 v168, v120, v124, v168 op_sel:[1,1,0] op_sel_hi:[1,0,1]\n"
      "v_pk_fma_f16 v169, v121, v125, v169 op_sel:[1,1,0] op_sel_hi:[1,0,1]\n"
      "v_pk_fma_f16 v170, v122, v126, v170 op_sel:[1,1,0] op_sel_hi:[1,0,1]\n"
      "v_pk_fma_f16 v171, v123, v127, v171 op_sel:[1,1,0] op_sel_hi:[1,0,1]\n"
      "ds_read_b128 v[112:115], v20 offset:3424\n"
      "ds_read_b128 v[116:119], v178 offset:3424\n"
      "v_mfma_f32_32x32x16_f16 v[180:195], v[84:87], v[168:171], v[180:195]\n"
      "s_waitcnt lgkmcnt(2)\n"
      "v_pk_mul_f16 v164, v104, v108 op_sel_hi:[0,1] neg_hi:[0,1]\n"
      "v_pk_mul_f16 v165, v105, v109 op_sel_hi:[0,1] neg_hi:[0,1]\n"
      "v_pk_mul_f16 v166, v106, v110 op_sel_hi:[0,1] neg_hi:[0,1]\n"
      "v_pk_mul_f16 v167, v107, v111 op_sel_hi:[0,1] neg_hi:[0,1]\n"
      "v_pk_fma_f16 v164, v104, v108, v164 op_sel:[1,1,0] op_sel_hi:[1,0,1]\n"
      "v_pk_fma_f16 v165, v105, v109, v165 op_sel:[1,1,0] op_sel_hi:[1,0,1]\n"
      "v_pk_fma_f16 v166, v106, v110, v166 op_sel:[1,1,0] op_sel_hi:[1,0,1]\n"
      "v_pk_fma_f16 v167, v107, v111, v167 op_sel:[1,1,0] op_sel_hi:[1,0,1]\n"
      "ds_read_b128 v[120:123], v20 offset:3456\n"
      "ds_read_b128 v[124:127], v178 offset:3456\n"
      "v_mfma_f32_32x32x16_f16 v[180:195], v[88:91], v[164:167], v[180:195]\n"
      "s_waitcnt lgkmcnt(2)\n"
      "v_pk_mul_f16 v168, v112, v116 op_sel_hi:[0,1] neg_hi:[0,1]\n"
      "v_pk_mul_f16 v169, v113, v117 op_sel_hi:[0,1] neg_hi:[0,1]\n"
      "v_pk_mul_f16 v170, v114, v118 op_sel_hi:[0,1] neg_hi:[0,1]\n"
      "v_pk_mul_f16 v171, v115, v119 op_sel_hi:[0,1] neg_hi:[0,1]\n"
      "v_pk_fma_f16 v168, v112, v116, v168 op_sel:[1,1,0] op_sel_hi:[1,0,1]\n"
      "v_pk_fma_f16 v169, v113, v117, v169 op_sel:[1,1,0] op_sel_hi:[1,0,1]\n"
      "v_pk_fma_f16 v170, v114, v118, v170 op_sel:[1,1,0] op_sel_hi:[1,0,1]\n"
      "v_pk_fma_f16 v171, v115, v119, v171 op_sel:[1,1,0] op_sel_hi:[1,0,1]\n"
      "ds_read_b128 v[104:107], v20 offset:3488\n"
      "ds_read_b128 v[108:111], v178 offset:3488\n"
      "v_mov_b32 v140, 0\n"
      "v_mov_b32 v141, 0\n"
      "v_mov_b32 v142, 0\n"
      "v_mov_b32 v143, 0\n"
      "ds_write_b128 v27, v[140:143] \n"
      "ds_write_b32 v28, v140 \n"
      "v_mfma_f32_32x32x16_f16 v[180:195], v[92:95], v[168:171], v[180:195]\n"
      "s_waitcnt lgkmcnt(2)\n"
      "v_pk_mul_f16 v164, v120, v124 op_sel_hi:[0,1] neg_hi:[0,1]\n"
      "v_pk_mul_f16 v165, v121, v125 op_sel_hi:[0,1] neg_hi:[0,1]\n"
      "v_pk_mul_f16 v166, v122, v126 op_sel_hi:[0,1] neg_hi:[0,1]\n"
      "v_pk_mul_f16 v167, v123, v127 op_sel_hi:[0,1] neg_hi:[0,1]\n"
      "v_pk_fma_f16 v164, v120, v124, v164 op_sel:[1,1,0] op_sel_hi:[1,0,1]\n"
      "v_pk_fma_f16 v165, v121, v125, v165 op_sel:[1,1,0] op_sel_hi:[1,0,1]\n"
      "v_pk_fma_f16 v166, v122, v126, v166 op_sel:[1,1,0] op_sel_hi:[1,0,1]\n"
      "v_pk_fma_f16 v167, v123, v127, v167 op_sel:[1,1,0] op_sel_hi:[1,0,1]\n"
      "s_nop 1\n"
      "v_mfma_f32_32x32x16_f16 v[180:195], v[96:99], v[164:167], v[180:195]\n"
      "s_waitcnt lgkmcnt(0)\n"
      "v_pk_mul_f16 v168, v104, v108 op_sel_hi:[0,1] neg_hi:[0,1]\n"
      "v_pk_mul_f16 v169, v105, v109 op_sel_hi:[0,1] neg_hi:[0,1]\n"
      "v_pk_mul_f16 v170, v106, v110 op_sel_hi:[0,1] neg_hi:[0,1]\n"
      "v_pk_mul_f16 v171, v107, v111 op_sel_hi:[0,1] neg_hi:[0,1]\n"
      "v_pk_fma_f16 v168, v104, v108, v168 op_sel:[1,1,0] op_sel_hi:[1,0,1]\n"
      "v_pk_fma_f16 v169, v105, v109, v169 op_sel:[1,1,0] op_sel_hi:[1,0,1]\n"
      "v_pk_fma_f16 v170, v106, v110, v170 op_sel:[1,1,0] op_sel_hi:[1,0,1]\n"
      "v_pk_fma_f16 v171, v107, v111, v171 op_sel:[1,1,0] op_sel_hi:[1,0,1]\n"
      "s_nop 1\n"
      "v_mfma_f32_32x32x16_f16 v[180:195], v[100:103], v[168:171], v[180:195]\n"
      "s_cmp_lt_u32 s33, 4\n"
      "s_cbranch_scc1 .Lct2%=\n"
      "buffer_load_dword v11, v15, s[20:23], s60 offen\n"
      ".Lct2%=:\n"
      "s_nop 10\n"
      "v_add_f32 v180, v180, v31\n"
      "buffer_load_dwordx4 v[64:67], v42, s[16:19], s62 offen sc1\n"
      "v_cvt_pk_f16_f32 v222, v187, v186\n"
      "v_cvt_pk_f16_f32 v223, v185, v184\n"
      "v_cvt_pk_f16_f32 v225, v186, v185\n"
      "v_cvt_pk_f16_f32 v226, v188, v189\n"
      "ds_write_b64 v22, v[222:223] offset:2110\n"
      "v_cvt_pk_f16_f32 v228, v192, v192\n"
      "ds_write_b16 v22, v222 \n"
      "v_cvt_pk_f16_f32 v220, v183, v182\n"
      "ds_write_b32 v22, v225 offset:2\n"
      "v_cvt_pk_f16_f32 v221, v181, v180\n"
      "ds_write_b16_d16_hi v22, v223 offset:6\n"
      "v_cvt_pk_f16_f32 v224, v182, v181\n"
      "v_readlane_b32 s64, v226, 0\n"
      "v_readlane_b32 s65, v226, 32\n"
      "v_readlane_b32 s66, v228, 0\n"
      "ds_write_b64 v22, v[220:221] offset:510\n"
      "v_cvt_pk_f16_f32 v227, v190, v191\n"
      "ds_write_b16 v22, v220 offset:2620\n"
      "v_mov_b32 v229, s65\n"
      "ds_write_b32 v22, v224 offset:2622\n"
      "v_writelane_b32 v229, s64, 8\n"
      "v_writelane_b32 v229, s64, 9\n"
      "ds_write_b16_d16_hi v22, v221 offset:2626\n"
      "v_writelane_b32 v229, s66, 10\n"
      "v_writelane_b32 v229, s66, 11\n"
      "s_pack_ll_b32_b16 s64, s64, s64\n"
      "ds_write_b16 v26, v229 \n"
      "v_add_u32 v230, 0x240, v29\n"
      "s_setprio 0\n"
      "v_and_b32 v44, s64, v44\n"
      "v_and_b32 v45, s64, v45\n"
      "v_and_b32 v46, s64, v46\n"
      "v_and_b32 v47, s64, v47\n"
      "ds_write_b16 v23, v226 \n"
      "ds_write_b16 v23, v226 offset:2110\n"
      "ds_write_b16_d16_hi v24, v226 \n"
      "ds_write_b16_d16_hi v24, v226 offset:2110\n"
      "ds_write_b16 v25, v227 offset:32\n"
      "ds_write_b16 v25, v227 offset:2142\n"
      "ds_write_b16_d16_hi v25, v227 \n"
      "ds_write_b16_d16_hi v25, v227 offset:2110\n"
      "ds_read2_b32 v[108:109], v230 offset0:0 offset1:1\n"
      "ds_read2_b32 v[110:111], v230 offset0:2 offset1:3\n"
      "ds_read2_b32 v[76:77], v29 offset0:16 offset1:17\n"
      "ds_read2_b32 v[78:79], v29 offset0:18 offset1:19\n"
      "ds_read2_b32 v[112:113], v230 offset0:16 offset1:17\n"
      "ds_read2_b32 v[114:115], v230 offset0:18 offset1:19\n"
      "ds_read2_b32 v[80:81], v29 offset0:32 offset1:33\n"
      "ds_read2_b32 v[82:83], v29 offset0:34 offset1:35\n"
      "ds_read2_b32 v[116:117], v230 offset0:32 offset1:33\n"
      "ds_read2_b32 v[118:119], v230 offset0:34 offset1:35\n"
      "ds_read2_b32 v[84:85], v29 offset0:48 offset1:49\n"
      "ds_read2_b32 v[86:87], v29 offset0:50 offset1:51\n"
      "s_movk_i32 s59, 0x7fff\n"
      "s_waitcnt vmcnt(0)\n"
      "s_nop 0\n"
      ".Lcsgk%=:\n"
      "v_cmp_ne_u32 s[64:65], s15, v65\n"
      "v_cmp_ne_u32 vcc, s15, v67\n"
      "s_nop 0\n"
      "s_or_b64 vcc, vcc, s[64:65]\n"
      "s_cbranch_scc1 .Lcsgsk%=\n"
      ".Lcsgok%=:\n"
      "v_and_b32 v196, 0xffff0000, v64\n"
      "v_lshlrev_b32 v198, 16, v64\n"
      "v_rcp_f32 v196, v196\n"
      "v_and_b32 v197, 0xffff0000, v66\n"
      "v_lshlrev_b32 v199, 16, v66\n"
      "v_rcp_f32 v197, v197\n"
      "v_mul_f32 v198, -v198, v196\n"
      "v_mul_f32 v199, -v199, v197\n"
      "ds_read2_b32 v[120:121], v230 offset0:48 offset1:49\n"
      "ds_read2_b32 v[122:123], v230 offset0:50 offset1:51\n"
      "v_lshlrev_b32 v204, 16, v48\n"
      "v_lshlrev_b32 v205, 16, v52\n"
      "v_and_b32 v206, 0xffff0000, v48\n"
      "v_and_b32 v207, 0xffff0000, v52\n"
      "v_pk_fma_f32 v[204:205], v[204:205], v[196:197], v[198:199]\n"
      "v_pk_fma_f32 v[206:207], v[206:207], v[196:197], v[198:199]\n"
      "v_cvt_pk_f16_f32 v204, v204, v205\n"
      "v_cvt_pk_f16_f32 v206, v206, v207\n"
      "ds_read2_b32 v[88:89], v29 offset0:64 offset1:65\n"
      "ds_read2_b32 v[90:91], v29 offset0:66 offset1:67\n"
      "ds_write2st64_b32 v32, v204, v206 offset0:0 offset1:19\n"
      "v_lshlrev_b32 v208, 16, v49\n"
      "v_lshlrev_b32 v209, 16, v53\n"
      "v_and_b32 v210, 0xffff0000, v49\n"
      "v_and_b32 v211, 0xffff0000, v53\n"
      "v_pk_fma_f32 v[208:209], v[208:209], v[196:197], v[198:199]\n"
      "v_pk_fma_f32 v[210:211], v[210:211], v[196:197], v[198:199]\n"
      "v_cvt_pk_f16_f32 v208, v208, v209\n"
      "ds_read2_b32 v[124:125], v230 offset0:64 offset1:65\n"
      "ds_read2_b32 v[126:127], v230 offset0:66 offset1:67\n"
      "v_cvt_pk_f16_f32 v210, v210, v211\n"
      "ds_write2st64_b32 v32, v208, v210 offset0:38 offset1:57\n"
      "v_lshlrev_b32 v204, 16, v50\n"
      "v_lshlrev_b32 v205, 16, v54\n"
      "v_and_b32 v206, 0xffff0000, v50\n"
      "v_and_b32 v207, 0xffff0000, v54\n"
      "v_pk_fma_f32 v[204:205], v[204:205], v[196:197], v[198:199]\n"
      "v_pk_fma_f32 v[206:207], v[206:207], v[196:197], v[198:199]\n"
      "ds_read2_b32 v[92:93], v29 offset0:80 offset1:81\n"
      "ds_read2_b32 v[94:95], v29 offset0:82 offset1:83\n"
      "v_cvt_pk_f16_f32 v204, v204, v205\n"
      "v_cvt_pk_f16_f32 v206, v206, v207\n"
      "ds_write2st64_b32 v32, v204, v206 offset0:76 offset1:95\n"
      "v_lshlrev_b32 v208, 16, v51\n"
      "v_lshlrev_b32 v209, 16, v55\n"
      "v_and_b32 v210, 0xffff0000, v51\n"
      "v_and_b32 v211, 0xffff0000, v55\n"
      "v_pk_fma_f32 v[208:209], v[208:209], v[196:197], v[198:199]\n"
      "ds_read2_b32 v[128:129], v230 offset0:80 offset1:81\n"
      "ds_read2_b32 v[130:131], v230 offset0:82 offset1:83\n"
      "v_pk_fma_f32 v[210:211], v[210:211], v[196:197], v[198:199]\n"
      "v_cvt_pk_f16_f32 v208, v208, v209\n"
      "v_cvt_pk_f16_f32 v210, v210, v211\n"
      "ds_write2st64_b32 v32, v208, v210 offset0:114 offset1:133\n"
      "s_mov_b32 s54, s41\n"
      "s_add_u32 s55, s41, 0x100000\n"
      "s_add_u32 s56, s41, 0x200000\n"
      "s_add_u32 s57, s41, 0x300000\n"
      "ds_read2_b32 v[96:97], v29 offset0:96 offset1:97\n"
      "ds_read2_b32 v[98:99], v29 offset0:98 offset1:99\n"
      "s_mov_b32 s58, 0x40000000\n"
      "v_lshrrev_b32 v14, 9, v8\n"
      "v_add_u32 v14, 0x27600, v14\n"
      "s_lshr_b32 s13, s33, 1\n"
      "ds_read2_b32 v[132:133], v230 offset0:96 offset1:97\n"
      "ds_read2_b32 v[134:135], v230 offset0:98 offset1:99\n"
      "ds_read2_b32 v[100:101], v29 offset0:112 offset1:113\n"
      "ds_read2_b32 v[102:103], v29 offset0:114 offset1:115\n"
      "ds_read2_b32 v[136:137], v230 offset0:112 offset1:113\n"
      "ds_read2_b32 v[138:139], v230 offset0:114 offset1:115\n"
      "ds_read2_b32 v[104:105], v29 offset0:128 offset1:129\n"
      "ds_read2_b32 v[106:107], v29 offset0:130 offset1:131\n"
      "ds_read2_b32 v[72:73], v29 offset0:0 offset1:1\n"
      "ds_read2_b32 v[74:75], v29 offset0:2 offset1:3\n"
      "s_waitcnt lgkmcnt(12)\n"
      ".Lccv%=:\n"
      "s_barrier\n"
      "s_setprio 3\n"
      "ds_read_b128 v[216:219], v33 offset:64\n"
      "ds_read_b128 v[220:223], v33 offset:128\n"
      "ds_read_b128 v[224:227], v33 offset:192\n"
      "ds_read_b128 v[212:215], v33 offset:256\n"
      "v_permlane32_swap_b32 v48, v52\n"
      "v_permlane32_swap_b32 v49, v53\n"
      "v_permlane32_swap_b32 v50, v54\n"
      "v_permlane32_swap_b32 v51, v55\n"
      "s_cmp_lg_u32 s33, 0\n"
      "s_cbranch_scc1 .Lca%=\n"
      "v_mov_b32 v16, s15\n"
      "v_mov_b32 v17, s15\n"
      "buffer_store_dwordx2 v[16:17], off, s[16:19], s42 sc1\n"
      ".Lca%=:\n"
      "s_waitcnt lgkmcnt(2)\n"
      "v_mfma_f32_16x16x32_f16 v[180:183], v[108:111], v[216:219], 0\n"
      "v_lshlrev_b32 v29, 2, v43\n"
      "v_add_u32 v29, 0x27600, v29\n"
      "ds_read_b32 v255, v29 \n"
      "ds_read_b128 v[236:239], v34 offset:0\n"
      "ds_read_b128 v[240:243], v34 offset:16\n"
      "v_mfma_f32_16x16x32_f16 v[184:187], v[76:79], v[216:219], 0\n"
      "ds_read2_b32 v[140:141], v30 offset0:16 offset1:17\n"
      "ds_read_b128 v[216:219], v33 offset:320\n"
      "v_mfma_f32_16x16x32_f16 v[180:183], v[112:115], v[220:223], v[180:183]\n"
      "v_mfma_f32_16x16x32_f16 v[184:187], v[80:83], v[220:223], v[184:187]\n"
      "ds_read2_b32 v[142:143], v30 offset0:64 offset1:65\n"
      "ds_read_b128 v[220:223], v33 offset:384\n"
      "s_waitcnt lgkmcnt(7)\n"
      "v_mfma_f32_16x16x32_f16 v[180:183], v[116:119], v[224:227], v[180:183]\n"
      "s_waitcnt lgkmcnt(5)\n"
      "v_alignbit_b32 v245, v238, v237, 16\n"
      "v_alignbit_b32 v246, v239, v238, 16\n"
      "v_mfma_f32_16x16x32_f16 v[184:187], v[84:87], v[224:227], v[184:187]\n"
      "ds_read2_b32 v[144:145], v30 offset0:8 offset1:9\n"
      "ds_read_b128 v[224:227], v33 offset:448\n"
      "s_waitcnt lgkmcnt(4)\n"
      "v_alignbit_b32 v247, v240, v239, 16\n"
      "v_alignbit_b32 v248, v241, v240, 16\n"
      "v_mfma_f32_16x16x32_f16 v[180:183], v[120:123], v[212:215], v[180:183]\n"
      "v_alignbit_b32 v249, v242, v241, 16\n"
      "v_pk_add_f16 v16, v245, v246\n"
      "v_mfma_f32_16x16x32_f16 v[184:187], v[88:91], v[212:215], v[184:187]\n"
      "ds_read2_b32 v[146:147], v30 offset0:56 offset1:57\n"
      "ds_read_b128 v[212:215], v33 offset:512\n"
      "v_pk_add_f16 v17, v246, v247\n"
      "v_pk_add_f16 v18, v247, v248\n"
      "v_mfma_f32_16x16x32_f16 v[180:183], v[124:127], v[216:219], v[180:183]\n"
      "v_pk_add_f16 v19, v248, v249\n"
      "v_pk_add_f16 v20, v16, v238\n"
      "v_mfma_f32_16x16x32_f16 v[184:187], v[92:95], v[216:219], v[184:187]\n"
      "ds_read2_b32 v[148:149], v30 offset0:0 offset1:1\n"
      "v_pk_add_f16 v21, v17, v239\n"
      "v_pk_add_f16 v22, v18, v240\n"
      "s_waitcnt lgkmcnt(3)\n"
      "v_mfma_f32_16x16x32_f16 v[180:183], v[128:131], v[220:223], v[180:183]\n"
      "v_pk_add_f16 v23, v19, v241\n"
      "ds_write_b128 v37, v[20:23] offset:0\n"
      "v_pk_add_f16 v16, v237, v239\n"
      "v_mfma_f32_16x16x32_f16 v[184:187], v[96:99], v[220:223], v[184:187]\n"
      "ds_read2_b32 v[150:151], v30 offset0:48 offset1:49\n"
      "ds_read_b128 v[216:219], v33 offset:576\n"
      "v_pk_add_f16 v17, v238, v240\n"
      "v_pk_add_f16 v18, v239, v241\n"
      "v_mfma_f32_16x16x32_f16 v[180:183], v[132:135], v[224:227], v[180:183]\n"
      "v_pk_add_f16 v19, v240, v242\n"
      "v_pk_add_f16 v20, v20, v16\n"
      "v_mfma_f32_16x16x32_f16 v[184:187], v[100:103], v[224:227], v[184:187]\n"
      "ds_read2_b32 v[152:153], v30 offset0:40 offset1:41\n"
      "ds_read_b128 v[220:223], v33 offset:640\n"
      "v_pk_add_f16 v21, v21, v17\n"
      "v_pk_add_f16 v22, v22, v18\n"
      "s_waitcnt lgkmcnt(4)\n"
      "v_mfma_f32_16x16x32_f16 v[180:183], v[136:139], v[212:215], v[180:183]\n"
      "v_pk_add_f16 v23, v23, v19\n"
      "ds_write_b128 v37, v[20:23] offset:3104\n"
      "v_alignbit_b32 v244, v237, v236, 16\n"
      "v_mfma_f32_16x16x32_f16 v[184:187], v[104:107], v[212:215], v[184:187]\n"
      "ds_read2_b32 v[154:155], v30 offset0:88 offset1:89\n"
      "ds_read_b128 v[224:227], v33 offset:704\n"
      "v_alignbit_b32 v250, v243, v242, 16\n"
      "v_pk_add_f16 v16, v244, v247\n"
      "v_mfma_f32_16x16x32_f16 v[188:191], v[72:75], v[212:215], 0\n"
      "v_pk_add_f16 v17, v245, v248\n"
      "v_pk_add_f16 v18, v246, v249\n"
      "v_mfma_f32_16x16x32_f16 v[196:199], v[44:47], v[212:215], 0\n"
      "ds_read2_b32 v[156:157], v30 offset0:32 offset1:33\n"
      "ds_read_b128 v[212:215], v33 offset:768\n"
      "v_pk_add_f16 v19, v247, v250\n"
      "v_pk_add_f16 v20, v20, v16\n"
      "s_waitcnt lgkmcnt(4)\n"
      "v_mfma_f32_16x16x32_f16 v[184:187], v[108:111], v[216:219], v[184:187]\n"
      "v_cvt_pk_bf16_f32 v24, v180, v182\n"
      "v_cvt_pk_bf16_f32 v25, v181, v183\n"
      "ds_write2st64_b32 v39, v24, v25 offset0:0 offset1:8\n"
      "v_mfma_f32_16x16x32_f16 v[188:191], v[76:79], v[216:219], v[188:191]\n"
      "ds_read2_b32 v[158:159], v30 offset0:80 offset1:81\n"
      "ds_read_b128 v[216:219], v33 offset:832\n"
      "v_pk_add_f16 v21, v21, v17\n"
      "v_pk_add_f16 v22, v22, v18\n"
      "v_mfma_f32_16x16x32_f16 v[184:187], v[112:115], v[220:223], v[184:187]\n"
      "buffer_load_dwordx4 v[56:59], v41, s[20:23], s61 offen nt\n"
      "buffer_load_dwordx4 v[68:71], v42, s[16:19], s63 offen sc1\n"
      "v_pk_add_f16 v23, v23, v19\n"
      "ds_write_b128 v38, v[20:23] offset:0\n"
      "v_pk_add_f16 v16, v236, v240\n"
      "v_mfma_f32_16x16x32_f16 v[188:191], v[80:83], v[220:223], v[188:191]\n"
      "ds_read2_b32 v[160:161], v30 offset0:24 offset1:25\n"
      "ds_read_b128 v[220:223], v33 offset:896\n"
      "v_pk_add_f16 v17, v237, v241\n"
      "v_pk_add_f16 v18, v238, v242\n"
      "s_waitcnt lgkmcnt(3)\n"
      "v_mfma_f32_16x16x32_f16 v[184:187], v[116:119], v[224:227], v[184:187]\n"
      "v_pk_add_f16 v19, v239, v243\n"
      "v_pk_add_f16 v20, v20, v16\n"
      "s_setprio 2\n"
      "v_mfma_f32_16x16x32_f16 v[188:191], v[84:87], v[224:227], v[188:191]\n"
      "ds_read2_b32 v[162:163], v30 offset0:72 offset1:73\n"
      "ds_read_b128 v[224:227], v33 offset:960\n"
      "v_pk_add_f16 v21, v21, v17\n"
      "v_pk_add_f16 v22, v22, v18\n"
      "ds_read_b128 v[236:239], v34 offset:1024\n"
      "ds_read_b128 v[240:243], v34 offset:1040\n"
      "v_mfma_f32_16x16x32_f16 v[184:187], v[120:123], v[212:215], v[184:187]\n"
      "v_pk_add_f16 v23, v23, v19\n"
      "ds_write_b128 v38, v[20:23] offset:2080\n"
      "v_mfma_f32_16x16x32_f16 v[188:191], v[88:91], v[212:215], v[188:191]\n"
      "ds_read2_b32 v[164:165], v30 offset0:128 offset1:129\n"
      "ds_read_b128 v[212:215], v33 offset:1024\n"
      "v_mfma_f32_16x16x32_f16 v[184:187], v[124:127], v[216:219], v[184:187]\n"
      "v_mfma_f32_16x16x32_f16 v[188:191], v[92:95], v[216:219], v[188:191]\n"
      "s_waitcnt lgkmcnt(4)\n"
      "v_alignbit_b32 v245, v238, v237, 16\n"
      "v_alignbit_b32 v246, v239, v238, 16\n"
      "v_mfma_f32_16x16x32_f16 v[184:187], v[128:131], v[220:223], v[184:187]\n"
      "s_waitcnt lgkmcnt(3)\n"
      "v_alignbit_b32 v247, v240, v239, 16\n"
      "v_alignbit_b32 v248, v241, v240, 16\n"
      "v_mfma_f32_16x16x32_f16 v[188:191], v[96:99], v[220:223], v[188:191]\n"
      "ds_read2_b32 v[166:167], v30 offset0:168 offset1:169\n"
      "ds_read_b64_tr_b16 v[228:229], v35 offset:32\n"
      "ds_read_b64_tr_b16 v[230:231], v35 offset:16\n"
      "v_alignbit_b32 v249, v242, v241, 16\n"
      "v_pk_add_f16 v16, v245, v246\n"
      "v_mfma_f32_16x16x32_f16 v[184:187], v[132:135], v[224:227], v[184:187]\n"
      "v_pk_add_f16 v17, v246, v247\n"
      "v_pk_add_f16 v18, v247, v248\n"
      "v_mfma_f32_16x16x32_f16 v[188:191], v[100:103], v[224:227], v[188:191]\n"
      "v_pk_add_f16 v19, v248, v249\n"
      "v_pk_add_f16 v20, v16, v238\n"
      "s_waitcnt lgkmcnt(3)\n"
      "v_mfma_f32_16x16x32_f16 v[184:187], v[136:139], v[212:215], v[184:187]\n"
      "ds_read_b128 v[216:219], v33 offset:1088\n"
      "buffer_load_dwordx4 v[60:63], v41, s[20:23], s61 offen offset:2048 nt\n"
      "v_pk_add_f16 v21, v17, v239\n"
      "v_pk_add_f16 v22, v18, v240\n"
      "v_mfma_f32_16x16x32_f16 v[188:191], v[104:107], v[212:215], v[188:191]\n"
      "ds_read2_b32 v[168:169], v30 offset0:120 offset1:121\n"
      "v_pk_add_f16 v23, v19, v241\n"
      "ds_write_b128 v37, v[20:23] offset:1024\n"
      "v_pk_add_f16 v16, v237, v239\n"
      "v_mfma_f32_16x16x32_f16 v[192:195], v[72:75], v[212:215], 0\n"
      "ds_read_b128 v[220:223], v33 offset:1152\n"
      "v_pk_add_f16 v17, v238, v240\n"
      "v_pk_add_f16 v18, v239, v241\n"
      "v_mfma_f32_16x16x32_f16 v[200:203], v[44:47], v[212:215], 0\n"
      "v_pk_add_f16 v19, v240, v242\n"
      "v_pk_add_f16 v20, v20, v16\n"
      "s_waitcnt lgkmcnt(3)\n"
      "v_mfma_f32_16x16x32_f16 v[188:191], v[228:231], v[140:143], v[188:191]\n"
      "ds_read_b128 v[224:227], v33 offset:1216\n"
      "v_cvt_pk_bf16_f32 v24, v184, v186\n"
      "v_cvt_pk_bf16_f32 v25, v185, v187\n"
      "ds_write2st64_b32 v39, v24, v25 offset0:1 offset1:9\n"
      "v_mfma_f32_16x16x32_f16 v[192:195], v[228:231], v[144:147], v[192:195]\n"
      "ds_read2_b32 v[170:171], v30 offset0:160 offset1:161\n"
      "v_pk_add_f16 v21, v21, v17\n"
      "v_pk_add_f16 v22, v22, v18\n"
      "v_mfma_f32_16x16x32_f16 v[196:199], v[228:231], v[148:151], v[196:199]\n"
      "ds_read_b64_tr_b16 v[232:233], v35 offset:3128\n"
      "ds_read_b64_tr_b16 v[234:235], v35 offset:3112\n"
      "v_pk_add_f16 v23, v23, v19\n"
      "ds_write_b128 v37, v[20:23] offset:4128\n"
      "v_alignbit_b32 v244, v237, v236, 16\n"
      "v_mfma_f32_16x16x32_f16 v[188:191], v[108:111], v[216:219], v[188:191]\n"
      "v_alignbit_b32 v250, v243, v242, 16\n"
      "v_pk_add_f16 v16, v244, v247\n"
      "v_mfma_f32_16x16x32_f16 v[192:195], v[76:79], v[216:219], v[192:195]\n"
      "v_pk_add_f16 v17, v245, v248\n"
      "v_pk_add_f16 v18, v246, v249\n"
      "s_waitcnt lgkmcnt(3)\n"
      "v_mfma_f32_16x16x32_f16 v[188:191], v[112:115], v[220:223], v[188:191]\n"
      "ds_read2_b32 v[172:173], v30 offset0:112 offset1:113\n"
      "ds_read_b128 v[212:215], v33 offset:1280\n"
      "v_pk_add_f16 v19, v247, v250\n"
      "v_pk_add_f16 v20, v20, v16\n"
      "v_mfma_f32_16x16x32_f16 v[192:195], v[80:83], v[220:223], v[192:195]\n"
      "v_pk_add_f16 v21, v21, v17\n"
      "v_pk_add_f16 v22, v22, v18\n"
      "v_mfma_f32_16x16x32_f16 v[188:191], v[116:119], v[224:227], v[188:191]\n"
      "ds_read_b128 v[216:219], v33 offset:1344\n"
      "v_pk_add_f16 v23, v23, v19\n"
      "ds_write_b128 v38, v[20:23] offset:1024\n"
      "v_pk_add_f16 v16, v236, v240\n"
      "v_mfma_f32_16x16x32_f16 v[192:195], v[84:87], v[224:227], v[192:195]\n"
      "v_pk_add_f16 v17, v237, v241\n"
      "v_pk_add_f16 v18, v238, v242\n"
      "s_setprio 1\n"
      "s_waitcnt lgkmcnt(2)\n"
      "v_mfma_f32_16x16x32_f16 v[188:191], v[232:235], v[152:155], v[188:191]\n"
      "ds_read2_b32 v[174:175], v30 offset0:152 offset1:153\n"
      "ds_read_b128 v[220:223], v33 offset:1408\n"
      "v_pk_add_f16 v19, v239, v243\n"
      "v_pk_add_f16 v20, v20, v16\n"
      "v_mfma_f32_16x16x32_f16 v[192:195], v[232:235], v[156:159], v[192:195]\n"
      "v_pk_add_f16 v21, v21, v17\n"
      "v_pk_add_f16 v22, v22, v18\n"
      "v_mfma_f32_16x16x32_f16 v[196:199], v[232:235], v[160:163], v[196:199]\n"
      "ds_read_b128 v[224:227], v33 offset:1472\n"
      "v_pk_add_f16 v23, v23, v19\n"
      "ds_write_b128 v38, v[20:23] offset:3104\n"
      "v_mfma_f32_16x16x32_f16 v[188:191], v[120:123], v[212:215], v[188:191]\n"
      "v_readlane_b32 s72, v255, 0\n"
      "v_mfma_f32_16x16x32_f16 v[192:195], v[88:91], v[212:215], v[192:195]\n"
      "ds_read2_b32 v[176:177], v30 offset0:104 offset1:105\n"
      "ds_read_b128 v[212:215], v33 offset:1536\n"
      "v_readlane_b32 s73, v255, 1\n"
      "s_waitcnt lgkmcnt(4)\n"
      "v_mfma_f32_16x16x32_f16 v[188:191], v[124:127], v[216:219], v[188:191]\n"
      "v_readlane_b32 s74, v255, 2\n"
      "v_mfma_f32_16x16x32_f16 v[192:195], v[92:95], v[216:219], v[192:195]\n"
      "v_readlane_b32 s75, v255, 3\n"
      "v_mfma_f32_16x16x32_f16 v[188:191], v[128:131], v[220:223], v[188:191]\n"
      "v_readlane_b32 s76, v255, 4\n"
      "v_mfma_f32_16x16x32_f16 v[192:195], v[96:99], v[220:223], v[192:195]\n"
      "ds_read2_b32 v[178:179], v30 offset0:144 offset1:145\n"
      "ds_read_b64_tr_b16 v[228:229], v35 offset:544\n"
      "ds_read_b64_tr_b16 v[230:231], v35 offset:528\n"
      "v_readlane_b32 s77, v255, 5\n"
      "s_waitcnt lgkmcnt(3)\n"
      "v_mfma_f32_16x16x32_f16 v[188:191], v[132:135], v[224:227], v[188:191]\n"
      "v_readlane_b32 s78, v255, 6\n"
      "v_mfma_f32_16x16x32_f16 v[192:195], v[100:103], v[224:227], v[192:195]\n"
      "v_readlane_b32 s79, v255, 7\n"
      "v_mfma_f32_16x16x32_f16 v[188:191], v[136:139], v[212:215], v[188:191]\n"
      "ds_read_b128 v[216:219], v33 offset:1600\n"
      "s_movk_i32 s59, 0x7fff\n"
      "s_waitcnt vmcnt(1)\n"
      "s_nop 0\n"
      ".Lcsgh%=:\n"
      "v_cmp_ne_u32 s[64:65], s15, v69\n"
      "v_cmp_ne_u32 vcc, s15, v71\n"
      "s_nop 0\n"
      "s_or_b64 vcc, vcc, s[64:65]\n"
      "s_cbranch_scc1 .Lcsgsh%=\n"
      ".Lcsgoh%=:\n"
      "v_mov_b32 v42, v8\n"
      "v_mfma_f32_16x16x32_f16 v[192:195], v[104:107], v[212:215], v[192:195]\n"
      "v_and_b32 v26, 0xffff0000, v68\n"
      "v_lshlrev_b32 v28, 16, v68\n"
      "v_mfma_f32_16x16x32_f16 v[196:199], v[72:75], v[212:215], v[196:199]\n"
      "ds_read_b128 v[220:223], v33 offset:1664\n"
      "v_rcp_f32 v26, v26\n"
      "v_and_b32 v27, 0xffff0000, v70\n"
      "v_mfma_f32_16x16x32_f16 v[204:207], v[44:47], v[212:215], 0\n"
      "v_lshlrev_b32 v29, 16, v70\n"
      "v_rcp_f32 v27, v27\n"
      "s_waitcnt lgkmcnt(1)\n"
      "v_mfma_f32_16x16x32_f16 v[192:195], v[228:231], v[140:143], v[192:195]\n"
      "ds_read_b128 v[224:227], v33 offset:1728\n"
      "v_cvt_pk_bf16_f32 v24, v188, v190\n"
      "v_cvt_pk_bf16_f32 v25, v189, v191\n"
      "ds_write2st64_b32 v39, v24, v25 offset0:2 offset1:10\n"
      "v_mfma_f32_16x16x32_f16 v[196:199], v[228:231], v[144:147], v[196:199]\n"
      "v_mul_f32 v28, -v28, v26\n"
      "v_mul_f32 v29, -v29, v27\n"
      "v_mfma_f32_16x16x32_f16 v[200:203], v[228:231], v[148:151], v[200:203]\n"
      "ds_read_b64_tr_b16 v[232:233], v35 offset:3640\n"
      "ds_read_b64_tr_b16 v[234:235], v35 offset:3624\n"
      "v_lshlrev_b32 v180, 16, v56\n"
      "s_waitcnt vmcnt(0)\n"
      "v_lshlrev_b32 v181, 16, v60\n"
      "v_mfma_f32_16x16x32_f16 v[192:195], v[108:111], v[216:219], v[192:195]\n"
      "v_and_b32 v182, 0xffff0000, v56\n"
      "v_and_b32 v183, 0xffff0000, v60\n"
      "v_mfma_f32_16x16x32_f16 v[196:199], v[76:79], v[216:219], v[196:199]\n"
      "v_fma_f32 v180, v180, v26, v28\n"
      "v_fma_f32 v181, v181, v27, v29\n"
      "s_setprio 0\n"
      "s_waitcnt lgkmcnt(2)\n"
      "v_mfma_f32_16x16x32_f16 v[192:195], v[112:115], v[220:223], v[192:195]\n"
      "ds_read_b128 v[212:215], v33 offset:1792\n"
      "v_fma_f32 v182, v182, v26, v28\n"
      "v_fma_f32 v183, v183, v27, v29\n"
      "v_mfma_f32_16x16x32_f16 v[196:199], v[80:83], v[220:223], v[196:199]\n"
      "v_cvt_pk_f16_f32 v180, v180, v181\n"
      "v_cvt_pk_f16_f32 v182, v182, v183\n"
      "ds_write2st64_b32 v32, v180, v182 offset0:8 offset1:27\n"
      "v_mfma_f32_16x16x32_f16 v[192:195], v[116:119], v[224:227], v[192:195]\n"
      "ds_read_b128 v[216:219], v33 offset:1856\n"
      "v_lshlrev_b32 v184, 16, v57\n"
      "v_lshlrev_b32 v185, 16, v61\n"
      "v_mfma_f32_16x16x32_f16 v[196:199], v[84:87], v[224:227], v[196:199]\n"
      "v_and_b32 v186, 0xffff0000, v57\n"
      "v_and_b32 v187, 0xffff0000, v61\n"
      "s_waitcnt lgkmcnt(2)\n"
      "v_mfma_f32_16x16x32_f16 v[192:195], v[232:235], v[152:155], v[192:195]\n"
      "ds_read_b128 v[220:223], v33 offset:1920\n"
      "v_fma_f32 v184, v184, v26, v28\n"
      "v_fma_f32 v185, v185, v27, v29\n"
      "v_mfma_f32_16x16x32_f16 v[196:199], v[232:235], v[156:159], v[196:199]\n"
      "v_fma_f32 v186, v186, v26, v28\n"
      "v_fma_f32 v187, v187, v27, v29\n"
      "s_cmp_lg_u32 s33, 0\n"
      "s_cbranch_scc1 .Lcg%=\n"
      "buffer_load_dwordx2 v[252:253], v9, s[16:19], s43 offen sc1\n"
      ".Lcg%=:\n"
      "v_mfma_f32_16x16x32_f16 v[200:203], v[232:235], v[160:163], v[200:203]\n"
      "ds_read_b128 v[224:227], v33 offset:1984\n"
      "v_cvt_pk_f16_f32 v184, v184, v185\n"
      "v_cvt_pk_f16_f32 v186, v186, v187\n"
      "ds_write2st64_b32 v32, v184, v186 offset0:46 offset1:65\n"
      "v_mfma_f32_16x16x32_f16 v[192:195], v[120:123], v[212:215], v[192:195]\n"
      "v_lshlrev_b32 v180, 16, v58\n"
      "v_lshlrev_b32 v181, 16, v62\n"
      "v_mfma_f32_16x16x32_f16 v[196:199], v[88:91], v[212:215], v[196:199]\n"
      "ds_read_b128 v[212:215], v33 offset:2048\n"
      "v_and_b32 v182, 0xffff0000, v58\n"
      "v_and_b32 v183, 0xffff0000, v62\n"
      "s_waitcnt lgkmcnt(3)\n"
      "v_mfma_f32_16x16x32_f16 v[192:195], v[124:127], v[216:219], v[192:195]\n"
      "v_fma_f32 v180, v180, v26, v28\n"
      "v_fma_f32 v181, v181, v27, v29\n"
      "v_mfma_f32_16x16x32_f16 v[196:199], v[92:95], v[216:219], v[196:199]\n"
      "v_fma_f32 v182, v182, v26, v28\n"
      "v_fma_f32 v183, v183, v27, v29\n"
      "v_mfma_f32_16x16x32_f16 v[192:195], v[128:131], v[220:223], v[192:195]\n"
      "v_cvt_pk_f16_f32 v180, v180, v181\n"
      "v_cvt_pk_f16_f32 v182, v182, v183\n"
      "ds_write2st64_b32 v32, v180, v182 offset0:84 offset1:103\n"
      "v_mfma_f32_16x16x32_f16 v[196:199], v[96:99], v[220:223], v[196:199]\n"
      "ds_read_b64_tr_b16 v[228:229], v36 offset:24\n"
      "ds_read_b64_tr_b16 v[230:231], v36 offset:2088\n"
      "v_lshlrev_b32 v184, 16, v59\n"
      "v_lshlrev_b32 v185, 16, v63\n"
      "s_waitcnt lgkmcnt(3)\n"
      "v_mfma_f32_16x16x32_f16 v[192:195], v[132:135], v[224:227], v[192:195]\n"
      "v_and_b32 v186, 0xffff0000, v59\n"
      "v_and_b32 v187, 0xffff0000, v63\n"
      "v_mfma_f32_16x16x32_f16 v[196:199], v[100:103], v[224:227], v[196:199]\n"
      "v_fma_f32 v184, v184, v26, v28\n"
      "v_fma_f32 v185, v185, v27, v29\n"
      "v_mfma_f32_16x16x32_f16 v[192:195], v[136:139], v[212:215], v[192:195]\n"
      "v_fma_f32 v186, v186, v26, v28\n"
      "v_fma_f32 v187, v187, v27, v29\n"
      "v_mfma_f32_16x16x32_f16 v[196:199], v[104:107], v[212:215], v[196:199]\n"
      "ds_read_b64_tr_b16 v[232:233], v36 offset:536\n"
      "ds_read_b64_tr_b16 v[234:235], v36 offset:2600\n"
      "v_cvt_pk_f16_f32 v184, v184, v185\n"
      "v_cvt_pk_f16_f32 v186, v186, v187\n"
      "ds_write2st64_b32 v32, v184, v186 offset0:122 offset1:141\n"
      "v_mfma_f32_16x16x32_f16 v[200:203], v[72:75], v[212:215], v[200:203]\n"
      "v_permlane32_swap_b32 v56, v60\n"
      "v_permlane32_swap_b32 v57, v61\n"
      "v_mfma_f32_16x16x32_f16 v[208:211], v[44:47], v[212:215], 0\n"
      "v_permlane32_swap_b32 v58, v62\n"
      "v_permlane32_swap_b32 v59, v63\n"
      "s_waitcnt lgkmcnt(3)\n"
      "v_mfma_f32_16x16x32_f16 v[196:199], v[228:231], v[164:167], v[196:199]\n"
      "v_cvt_pk_bf16_f32 v24, v192, v194\n"
      "v_cvt_pk_bf16_f32 v25, v193, v195\n"
      "ds_write2st64_b32 v39, v24, v25 offset0:3 offset1:11\n"
      "v_mfma_f32_16x16x32_f16 v[200:203], v[228:231], v[168:171], v[200:203]\n"
      "v_mfma_f32_16x16x32_f16 v[204:207], v[228:231], v[172:175], v[204:207]\n"
      "v_mfma_f32_16x16x32_f16 v[208:211], v[228:231], v[176:179], v[208:211]\n"
      "ds_read_b64_tr_b16 v[228:229], v35 offset:1056\n"
      "ds_read_b64_tr_b16 v[230:231], v35 offset:1040\n"
      "s_cmp_eq_u32 s33, 0\n"
      "s_cbranch_scc1 .Lch%=\n"
      ".Lcho%=:\n"
      "s_waitcnt lgkmcnt(2)\n"
      "s_barrier\n"
      "s_setprio 3\n"
      "v_mfma_f32_16x16x32_f16 v[200:203], v[232:235], v[164:167], v[200:203]\n"
      "ds_read_b64_tr_b16 v[64:65], v40 offset:0\n"
      "ds_read_b64_tr_b16 v[66:67], v40 offset:35072\n"
      "v_lshlrev_b32 v68, 16, v48\n"
      "v_and_b32 v69, 0xffff0000, v48\n"
      "v_mfma_f32_16x16x32_f16 v[204:207], v[232:235], v[168:171], v[204:207]\n"
      "v_lshlrev_b32 v70, 16, v49\n"
      "v_and_b32 v71, 0xffff0000, v49\n"
      "ds_read_b128 v[236:239], v34 offset:2048\n"
      "ds_read_b128 v[240:243], v34 offset:2064\n"
      "ds_read_b32 v12, v14 offset:256\n"
      "v_mfma_f32_16x16x32_f16 v[208:211], v[232:235], v[172:175], v[208:211]\n"
      "ds_read_b128 v[216:219], v33 offset:2112\n"
      "ds_read_b64_tr_b16 v[232:233], v35 offset:4152\n"
      "ds_read_b64_tr_b16 v[234:235], v35 offset:4136\n"
      "v_lshlrev_b32 v26, 16, v50\n"
      "v_and_b32 v27, 0xffff0000, v50\n"
      "s_waitcnt lgkmcnt(6)\n"
      "v_mfma_f32_16x16x32_f16 v[196:199], v[228:231], v[140:143], v[196:199]\n"
      "v_lshlrev_b32 v28, 16, v51\n"
      "v_and_b32 v29, 0xffff0000, v51\n"
      "v_mfma_f32_16x16x32_f16 v[200:203], v[228:231], v[144:147], v[200:203]\n"
      "ds_read_b128 v[220:223], v33 offset:2176\n"
      "v_lshlrev_b32 v48, 16, v64\n"
      "v_and_b32 v64, 0xffff0000, v64\n"
      "v_mfma_f32_16x16x32_f16 v[204:207], v[228:231], v[148:151], v[204:207]\n"
      "v_fmac_f32 v68, s72, v48\n"
      "v_fmac_f32 v69, s73, v64\n"
      "s_waitcnt lgkmcnt(1)\n"
      "v_mfma_f32_16x16x32_f16 v[196:199], v[232:235], v[152:155], v[196:199]\n"
      "ds_read_b128 v[224:227], v33 offset:2240\n"
      "v_lshlrev_b32 v49, 16, v65\n"
      "v_and_b32 v65, 0xffff0000, v65\n"
      "v_mfma_f32_16x16x32_f16 v[200:203], v[232:235], v[156:159], v[200:203]\n"
      "v_fmac_f32 v70, s74, v49\n"
      "v_fmac_f32 v71, s75, v65\n"
      "v_mfma_f32_16x16x32_f16 v[204:207], v[232:235], v[160:163], v[204:207]\n"
      "ds_read_b128 v[212:215], v33 offset:2304\n"
      "v_lshlrev_b32 v50, 16, v66\n"
      "v_and_b32 v66, 0xffff0000, v66\n"
      "v_mfma_f32_16x16x32_f16 v[196:199], v[108:111], v[216:219], v[196:199]\n"
      "v_fmac_f32 v26, s76, v50\n"
      "v_fmac_f32 v27, s77, v66\n"
      "v_mfma_f32_16x16x32_f16 v[200:203], v[76:79], v[216:219], v[200:203]\n"
      "ds_read_b64_tr_b16 v[228:229], v35 offset:1568\n"
      "ds_read_b64_tr_b16 v[230:231], v35 offset:1552\n"
      "v_lshlrev_b32 v51, 16, v67\n"
      "v_and_b32 v67, 0xffff0000, v67\n"
      "s_waitcnt lgkmcnt(3)\n"
      "v_mfma_f32_16x16x32_f16 v[196:199], v[112:115], v[220:223], v[196:199]\n"
      "v_fmac_f32 v28, s78, v51\n"
      "v_fmac_f32 v29, s79, v67\n"
      "v_mfma_f32_16x16x32_f16 v[200:203], v[80:83], v[220:223], v[200:203]\n"
      "v_cvt_pk_bf16_f32 v48, v68, v69\n"
      "v_cvt_pk_bf16_f32 v49, v70, v71\n"
      "v_mfma_f32_16x16x32_f16 v[196:199], v[116:119], v[224:227], v[196:199]\n"
      "ds_read_b128 v[216:219], v33 offset:2368\n"
      "v_cvt_pk_bf16_f32 v50, v26, v27\n"
      "v_cvt_pk_bf16_f32 v51, v28, v29\n"
      "s_setprio 2\n"
      "v_mfma_f32_16x16x32_f16 v[200:203], v[84:87], v[224:227], v[200:203]\n"
      "v_lshlrev_b32 v13, 24, v12\n"
      "v_and_or_b32 v13, v13, s58, v42\n"
      "s_waitcnt lgkmcnt(1)\n"
      "v_mfma_f32_16x16x32_f16 v[196:199], v[120:123], v[212:215], v[196:199]\n"
      "ds_read_b128 v[220:223], v33 offset:2432\n"
      "s_cmp_lg_u32 s13, 0\n"
      "s_cbranch_scc1 .Lcs00%=\n"
      "buffer_store_dwordx4 v[48:51], v13, s[24:27], s54 offen\n"
      ".Lcs00%=:\n"
      "v_alignbit_b32 v245, v238, v237, 16\n"
      "v_alignbit_b32 v246, v239, v238, 16\n"
      "v_mfma_f32_16x16x32_f16 v[200:203], v[88:91], v[212:215], v[200:203]\n"
      "v_alignbit_b32 v247, v240, v239, 16\n"
      "v_alignbit_b32 v248, v241, v240, 16\n"
      "v_mfma_f32_16x16x32_f16 v[200:203], v[228:231], v[140:143], v[200:203]\n"
      "ds_read_b128 v[224:227], v33 offset:2496\n"
      "s_cmp_lg_u32 s13, 1\n"
      "s_cbranch_scc1 .Lcs01%=\n"
      "buffer_store_dwordx4 v[48:51], v13, s[24:27], s54 offen\n"
      ".Lcs01%=:\n"
      "v_alignbit_b32 v249, v242, v241, 16\n"
      "v_pk_add_f16 v16, v245, v246\n"
      "v_mfma_f32_16x16x32_f16 v[204:207], v[228:231], v[144:147], v[204:207]\n"
      "v_pk_add_f16 v17, v246, v247\n"
      "v_pk_add_f16 v18, v247, v248\n"
      "v_mfma_f32_16x16x32_f16 v[208:211], v[228:231], v[148:151], v[208:211]\n"
      "ds_read_b128 v[212:215], v33 offset:2560\n"
      "s_cmp_lg_u32 s13, 2\n"
      "s_cbranch_scc1 .Lcs02%=\n"
      "buffer_store_dwordx4 v[48:51], v13, s[24:27], s54 offen\n"
      ".Lcs02%=:\n"
      "v_pk_add_f16 v19, v248, v249\n"
      "v_pk_add_f16 v20, v16, v238\n"
      "s_waitcnt lgkmcnt(1)\n"
      "v_mfma_f32_16x16x32_f16 v[196:199], v[124:127], v[216:219], v[196:199]\n"
      "v_pk_add_f16 v21, v17, v239\n"
      "v_pk_add_f16 v22, v18, v240\n"
      "v_mfma_f32_16x16x32_f16 v[200:203], v[92:95], v[216:219], v[200:203]\n"
      "s_cmp_lg_u32 s13, 3\n"
      "s_cbranch_scc1 .Lcs03%=\n"
      "buffer_store_dwordx4 v[48:51], v13, s[24:27], s54 offen\n"
      ".Lcs03%=:\n"
      "v_pk_add_f16 v23, v19, v241\n"
      "ds_write_b128 v37, v[20:23] offset:2048\n"
      "v_pk_add_f16 v16, v237, v239\n"
      "v_mfma_f32_16x16x32_f16 v[196:199], v[128:131], v[220:223], v[196:199]\n"
      "ds_read_b64_tr_b16 v[232:233], v35 offset:4664\n"
      "ds_read_b64_tr_b16 v[234:235], v35 offset:4648\n"
      "ds_read_b64_tr_b16 v[64:65], v40 offset:512\n"
      "ds_read_b64_tr_b16 v[66:67], v40 offset:35584\n"
      "v_lshlrev_b32 v68, 16, v52\n"
      "v_and_b32 v69, 0xffff0000, v52\n"
      "v_mfma_f32_16x16x32_f16 v[200:203], v[96:99], v[220:223], v[200:203]\n"
      "v_lshlrev_b32 v70, 16, v53\n"
      "v_and_b32 v71, 0xffff0000, v53\n"
      "v_mfma_f32_16x16x32_f16 v[196:199], v[132:135], v[224:227], v[196:199]\n"
      "v_lshlrev_b32 v26, 16, v54\n"
      "v_and_b32 v27, 0xffff0000, v54\n"
      "v_mfma_f32_16x16x32_f16 v[200:203], v[100:103], v[224:227], v[200:203]\n"
      "ds_read_b128 v[216:219], v33 offset:2624\n"
      "v_lshlrev_b32 v28, 16, v55\n"
      "v_and_b32 v29, 0xffff0000, v55\n"
      "s_waitcnt lgkmcnt(1)\n"
      "v_mfma_f32_16x16x32_f16 v[196:199], v[136:139], v[212:215], v[196:199]\n"
      "v_lshlrev_b32 v52, 16, v64\n"
      "v_and_b32 v64, 0xffff0000, v64\n"
      "v_mfma_f32_16x16x32_f16 v[200:203], v[104:107], v[212:215], v[200:203]\n"
      "ds_read_b128 v[220:223], v33 offset:2688\n"
      "v_fmac_f32 v68, s72, v52\n"
      "v_fmac_f32 v69, s73, v64\n"
      "s_setprio 1\n"
      "v_mfma_f32_16x16x32_f16 v[204:207], v[72:75], v[212:215], v[204:207]\n"
      "v_lshlrev_b32 v53, 16, v65\n"
      "v_and_b32 v65, 0xffff0000, v65\n"
      "v_mfma_f32_16x16x32_f16 v[200:203], v[232:235], v[152:155], v[200:203]\n"
      "ds_read_b128 v[224:227], v33 offset:2752\n"
      "v_fmac_f32 v70, s74, v53\n"
      "v_fmac_f32 v71, s75, v65\n"
      "v_mfma_f32_16x16x32_f16 v[204:207], v[232:235], v[156:159], v[204:207]\n"
      "v_cvt_pk_bf16_f32 v24, v196, v198\n"
      "v_cvt_pk_bf16_f32 v25, v197, v199\n"
      "ds_write2st64_b32 v39, v24, v25 offset0:4 offset1:12\n"
      "v_mfma_f32_16x16x32_f16 v[208:211], v[232:235], v[160:163], v[208:211]\n"
      "ds_read_b128 v[212:215], v33 offset:2816\n"
      "v_lshlrev_b32 v54, 16, v66\n"
      "v_and_b32 v66, 0xffff0000, v66\n"
      "s_waitcnt lgkmcnt(3)\n"
      "v_mfma_f32_16x16x32_f16 v[200:203], v[108:111], v[216:219], v[200:203]\n"
      "v_fmac_f32 v26, s76, v54\n"
      "v_fmac_f32 v27, s77, v66\n"
      "v_mfma_f32_16x16x32_f16 v[204:207], v[76:79], v[216:219], v[204:207]\n"
      "ds_read_b128 v[216:219], v33 offset:2880\n"
      "v_lshlrev_b32 v55, 16, v67\n"
      "v_and_b32 v67, 0xffff0000, v67\n"
      "v_mfma_f32_16x16x32_f16 v[200:203], v[112:115], v[220:223], v[200:203]\n"
      "v_fmac_f32 v28, s78, v55\n"
      "v_fmac_f32 v29, s79, v67\n"
      "v_mfma_f32_16x16x32_f16 v[204:207], v[80:83], v[220:223], v[204:207]\n"
      "ds_read_b128 v[220:223], v33 offset:2944\n"
      "v_cvt_pk_bf16_f32 v52, v68, v69\n"
      "v_cvt_pk_bf16_f32 v53, v70, v71\n"
      "s_waitcnt lgkmcnt(2)\n"
      "v_mfma_f32_16x16x32_f16 v[200:203], v[116:119], v[224:227], v[200:203]\n"
      "v_cvt_pk_bf16_f32 v54, v26, v27\n"
      "v_cvt_pk_bf16_f32 v55, v28, v29\n"
      "v_mfma_f32_16x16x32_f16 v[204:207], v[84:87], v[224:227], v[204:207]\n"
      "ds_read_b128 v[224:227], v33 offset:3008\n"
      "v_lshlrev_b32 v13, 16, v12\n"
      "v_and_or_b32 v13, v13, s58, v42\n"
      "v_mfma_f32_16x16x32_f16 v[200:203], v[120:123], v[212:215], v[200:203]\n"
      "s_cmp_lg_u32 s13, 0\n"
      "s_cbranch_scc1 .Lcs10%=\n"
      "buffer_store_dwordx4 v[52:55], v13, s[24:27], s55 offen\n"
      ".Lcs10%=:\n"
      "v_pk_add_f16 v17, v238, v240\n"
      "v_pk_add_f16 v18, v239, v241\n"
      "s_setprio 0\n"
      "v_mfma_f32_16x16x32_f16 v[204:207], v[88:91], v[212:215], v[204:207]\n"
      "ds_read_b128 v[212:215], v33 offset:3072\n"
      "v_pk_add_f16 v19, v240, v242\n"
      "v_pk_add_f16 v20, v20, v16\n"
      "s_waitcnt lgkmcnt(1)\n"
      "v_mfma_f32_16x16x32_f16 v[200:203], v[124:127], v[216:219], v[200:203]\n"
      "s_cmp_lg_u32 s13, 1\n"
      "s_cbranch_scc1 .Lcs11%=\n"
      "buffer_store_dwordx4 v[52:55], v13, s[24:27], s55 offen\n"
      ".Lcs11%=:\n"
      "v_pk_add_f16 v21, v21, v17\n"
      "v_pk_add_f16 v22, v22, v18\n"
      "v_mfma_f32_16x16x32_f16 v[204:207], v[92:95], v[216:219], v[204:207]\n"
      "v_pk_add_f16 v23, v23, v19\n"
      "ds_write_b128 v37, v[20:23] offset:5152\n"
      "v_mfma_f32_16x16x32_f16 v[200:203], v[128:131], v[220:223], v[200:203]\n"
      "ds_read_b64_tr_b16 v[228:229], v36 offset:1048\n"
      "ds_read_b64_tr_b16 v[230:231], v36 offset:3112\n"
      "s_cmp_lg_u32 s13, 2\n"
      "s_cbranch_scc1 .Lcs12%=\n"
      "buffer_store_dwordx4 v[52:55], v13, s[24:27], s55 offen\n"
      ".Lcs12%=:\n"
      "v_mfma_f32_16x16x32_f16 v[204:207], v[96:99], v[220:223], v[204:207]\n"
      "v_mfma_f32_16x16x32_f16 v[200:203], v[132:135], v[224:227], v[200:203]\n"
      "ds_read_b128 v[216:219], v33 offset:3136\n"
      "s_cmp_lg_u32 s13, 3\n"
      "s_cbranch_scc1 .Lcs13%=\n"
      "buffer_store_dwordx4 v[52:55], v13, s[24:27], s55 offen\n"
      ".Lcs13%=:\n"
      "v_mfma_f32_16x16x32_f16 v[204:207], v[100:103], v[224:227], v[204:207]\n"
      "s_waitcnt lgkmcnt(1)\n"
      "v_mfma_f32_16x16x32_f16 v[200:203], v[136:139], v[212:215], v[200:203]\n"
      "ds_read_b128 v[220:223], v33 offset:3200\n"
      "v_mfma_f32_16x16x32_f16 v[204:207], v[104:107], v[212:215], v[204:207]\n"
      "v_mfma_f32_16x16x32_f16 v[208:211], v[72:75], v[212:215], v[208:211]\n"
      "ds_read_b128 v[224:227], v33 offset:3264\n"
      "v_mfma_f32_16x16x32_f16 v[204:207], v[228:231], v[164:167], v[204:207]\n"
      "v_mfma_f32_16x16x32_f16 v[208:211], v[228:231], v[168:171], v[208:211]\n"
      "ds_read_b64_tr_b16 v[232:233], v36 offset:1560\n"
      "ds_read_b64_tr_b16 v[234:235], v36 offset:3624\n"
      "v_cvt_pk_bf16_f32 v24, v200, v202\n"
      "v_cvt_pk_bf16_f32 v25, v201, v203\n"
      "ds_write2st64_b32 v39, v24, v25 offset0:5 offset1:13\n"
      "s_waitcnt lgkmcnt(5)\n"
      "v_mfma_f32_16x16x32_f16 v[204:207], v[108:111], v[216:219], v[204:207]\n"
      "ds_read_b128 v[212:215], v33 offset:3328\n"
      "v_mfma_f32_16x16x32_f16 v[208:211], v[76:79], v[216:219], v[208:211]\n"
      "s_waitcnt lgkmcnt(5)\n"
      "v_mfma_f32_16x16x32_f16 v[204:207], v[112:115], v[220:223], v[204:207]\n"
      "ds_read_b128 v[216:219], v33 offset:3392\n"
      "s_waitcnt lgkmcnt(2)\n"
      "s_barrier\n"
      "s_setprio 3\n"
      "v_mfma_f32_16x16x32_f16 v[208:211], v[80:83], v[220:223], v[208:211]\n"
      "ds_read_b64_tr_b16 v[64:65], v40 offset:1024\n"
      "ds_read_b64_tr_b16 v[66:67], v40 offset:36096\n"
      "v_lshlrev_b32 v68, 16, v56\n"
      "v_and_b32 v69, 0xffff0000, v56\n"
      "v_mfma_f32_16x16x32_f16 v[204:207], v[116:119], v[224:227], v[204:207]\n"
      "ds_read_b128 v[220:223], v33 offset:3456\n"
      "v_lshlrev_b32 v70, 16, v57\n"
      "v_and_b32 v71, 0xffff0000, v57\n"
      "v_mfma_f32_16x16x32_f16 v[208:211], v[84:87], v[224:227], v[208:211]\n"
      "v_lshlrev_b32 v26, 16, v58\n"
      "v_and_b32 v27, 0xffff0000, v58\n"
      "v_mfma_f32_16x16x32_f16 v[208:211], v[232:235], v[164:167], v[208:211]\n"
      "ds_read_b64_tr_b16 v[228:229], v35 offset:2080\n"
      "ds_read_b64_tr_b16 v[230:231], v35 offset:2064\n"
      "v_lshlrev_b32 v28, 16, v59\n"
      "v_and_b32 v29, 0xffff0000, v59\n"
      "s_waitcnt lgkmcnt(3)\n"
      "v_mfma_f32_16x16x32_f16 v[204:207], v[120:123], v[212:215], v[204:207]\n"
      "v_lshlrev_b32 v56, 16, v64\n"
      "v_and_b32 v64, 0xffff0000, v64\n"
      "v_mfma_f32_16x16x32_f16 v[208:211], v[88:91], v[212:215], v[208:211]\n"
      "ds_read_b128 v[224:227], v33 offset:3520\n"
      "v_fmac_f32 v68, s72, v56\n"
      "v_fmac_f32 v69, s73, v64\n"
      "v_mfma_f32_16x16x32_f16 v[204:207], v[124:127], v[216:219], v[204:207]\n"
      "v_lshlrev_b32 v57, 16, v65\n"
      "v_and_b32 v65, 0xffff0000, v65\n"
      "v_mfma_f32_16x16x32_f16 v[208:211], v[92:95], v[216:219], v[208:211]\n"
      "ds_read_b128 v[212:215], v33 offset:3584\n"
      "v_fmac_f32 v70, s74, v57\n"
      "v_fmac_f32 v71, s75, v65\n"
      "s_setprio 2\n"
      "s_waitcnt lgkmcnt(2)\n"
      "v_mfma_f32_16x16x32_f16 v[204:207], v[128:131], v[220:223], v[204:207]\n"
      "v_lshlrev_b32 v58, 16, v66\n"
      "v_and_b32 v66, 0xffff0000, v66\n"
      "v_mfma_f32_16x16x32_f16 v[208:211], v[96:99], v[220:223], v[208:211]\n"
      "ds_read_b128 v[216:219], v33 offset:3648\n"
      "v_fmac_f32 v26, s76, v58\n"
      "v_fmac_f32 v27, s77, v66\n"
      "v_mfma_f32_16x16x32_f16 v[204:207], v[228:231], v[140:143], v[204:207]\n"
      "ds_read_b64_tr_b16 v[232:233], v35 offset:5176\n"
      "ds_read_b64_tr_b16 v[234:235], v35 offset:5160\n"
      "v_lshlrev_b32 v59, 16, v67\n"
      "v_and_b32 v67, 0xffff0000, v67\n"
      "v_mfma_f32_16x16x32_f16 v[208:211], v[228:231], v[144:147], v[208:211]\n"
      "v_fmac_f32 v28, s78, v59\n"
      "v_fmac_f32 v29, s79, v67\n"
      "s_waitcnt lgkmcnt(3)\n"
      "v_mfma_f32_16x16x32_f16 v[204:207], v[132:135], v[224:227], v[204:207]\n"
      "ds_read_b128 v[220:223], v33 offset:3712\n"
      "v_cvt_pk_bf16_f32 v56, v68, v69\n"
      "v_cvt_pk_bf16_f32 v57, v70, v71\n"
      "v_mfma_f32_16x16x32_f16 v[208:211], v[100:103], v[224:227], v[208:211]\n"
      "ds_read_b128 v[224:227], v33 offset:3776\n"
      "v_cvt_pk_bf16_f32 v58, v26, v27\n"
      "v_cvt_pk_bf16_f32 v59, v28, v29\n"
      "v_mfma_f32_16x16x32_f16 v[204:207], v[136:139], v[212:215], v[204:207]\n"
      "v_lshlrev_b32 v13, 8, v12\n"
      "v_and_or_b32 v13, v13, s58, v42\n"
      "s_setprio 1\n"
      "v_mfma_f32_16x16x32_f16 v[208:211], v[104:107], v[212:215], v[208:211]\n"
      "ds_read_b64_tr_b16 v[228:229], v35 offset:2592\n"
      "ds_read_b64_tr_b16 v[230:231], v35 offset:2576\n"
      "ds_read_b128 v[212:215], v33 offset:3840\n"
      "s_cmp_lg_u32 s13, 0\n"
      "s_cbranch_scc1 .Lcs20%=\n"
      "buffer_store_dwordx4 v[56:59], v13, s[24:27], s56 offen\n"
      ".Lcs20%=:\n"
      "v_lshlrev_b32 v68, 16, v60\n"
      "v_and_b32 v69, 0xffff0000, v60\n"
      "s_waitcnt lgkmcnt(3)\n"
      "v_mfma_f32_16x16x32_f16 v[208:211], v[108:111], v[216:219], v[208:211]\n"
      "ds_read_b128 v[216:219], v33 offset:3904\n"
      "v_lshlrev_b32 v70, 16, v61\n"
      "v_and_b32 v71, 0xffff0000, v61\n"
      "v_mfma_f32_16x16x32_f16 v[204:207], v[232:235], v[152:155], v[204:207]\n"
      "s_cmp_lg_u32 s13, 1\n"
      "s_cbranch_scc1 .Lcs21%=\n"
      "buffer_store_dwordx4 v[56:59], v13, s[24:27], s56 offen\n"
      ".Lcs21%=:\n"
      "v_lshlrev_b32 v26, 16, v62\n"
      "v_and_b32 v27, 0xffff0000, v62\n"
      "v_mfma_f32_16x16x32_f16 v[208:211], v[232:235], v[156:159], v[208:211]\n"
      "ds_read_b64_tr_b16 v[232:233], v35 offset:5688\n"
      "ds_read_b64_tr_b16 v[234:235], v35 offset:5672\n"
      "v_lshlrev_b32 v28, 16, v63\n"
      "v_and_b32 v29, 0xffff0000, v63\n"
      "v_mfma_f32_16x16x32_f16 v[208:211], v[112:115], v[220:223], v[208:211]\n"
      "ds_read_b128 v[220:223], v33 offset:3968\n"
      "s_cmp_lg_u32 s13, 2\n"
      "s_cbranch_scc1 .Lcs22%=\n"
      "buffer_store_dwordx4 v[56:59], v13, s[24:27], s56 offen\n"
      ".Lcs22%=:\n"
      "s_setprio 0\n"
      "v_mfma_f32_16x16x32_f16 v[208:211], v[116:119], v[224:227], v[208:211]\n"
      "ds_read_b128 v[224:227], v33 offset:4032\n"
      "s_waitcnt lgkmcnt(3)\n"
      "v_mfma_f32_16x16x32_f16 v[208:211], v[120:123], v[212:215], v[208:211]\n"
      "ds_read_b128 v[212:215], v33 offset:4096\n"
      "v_cvt_pk_bf16_f32 v24, v204, v206\n"
      "v_cvt_pk_bf16_f32 v25, v205, v207\n"
      "ds_write2st64_b32 v39, v24, v25 offset0:6 offset1:14\n"
      "s_cmp_lg_u32 s13, 3\n"
      "s_cbranch_scc1 .Lcs23%=\n"
      "buffer_store_dwordx4 v[56:59], v13, s[24:27], s56 offen\n"
      ".Lcs23%=:\n"
      "v_mfma_f32_16x16x32_f16 v[208:211], v[228:231], v[140:143], v[208:211]\n"
      "v_mfma_f32_16x16x32_f16 v[208:211], v[124:127], v[216:219], v[208:211]\n"
      "s_waitcnt lgkmcnt(2)\n"
      "v_mfma_f32_16x16x32_f16 v[208:211], v[128:131], v[220:223], v[208:211]\n"
      "v_mfma_f32_16x16x32_f16 v[208:211], v[232:235], v[152:155], v[208:211]\n"
      "v_mfma_f32_16x16x32_f16 v[208:211], v[132:135], v[224:227], v[208:211]\n"
      "s_waitcnt lgkmcnt(1)\n"
      "v_mfma_f32_16x16x32_f16 v[208:211], v[136:139], v[212:215], v[208:211]\n"
      "s_nop 7\n"
      "v_cvt_pk_bf16_f32 v24, v208, v210\n"
      "v_cvt_pk_bf16_f32 v25, v209, v211\n"
      "ds_write2st64_b32 v39, v24, v25 offset0:7 offset1:15\n"
      "s_waitcnt lgkmcnt(0)\n"
      "s_barrier\n"
      "ds_read_b64_tr_b16 v[64:65], v40 offset:1536\n"
      "ds_read_b64_tr_b16 v[66:67], v40 offset:36608\n"
      "s_cmp_lg_u32 s33, 0\n"
      "s_cbranch_scc1 .Lcb%=\n"
      "ds_read_u16_d16_hi v252, v8 offset:56384\n"
      "ds_read_u16_d16_hi v253, v8 offset:65152\n"
      "s_waitcnt lgkmcnt(0)\n"
      "v_pk_mul_f32 v[252:253], v[252:253], v[254:255]\n"
      "v_cvt_pk_bf16_f32 v252, v252, v253\n"
      "buffer_atomic_pk_add_bf16 v252, v251, s[24:27], s41 offen\n"
      ".Lcb%=:\n"
      "s_waitcnt lgkmcnt(1)\n"
      "v_lshlrev_b32 v60, 16, v64\n"
      "v_and_b32 v64, 0xffff0000, v64\n"
      "v_fmac_f32 v68, s72, v60\n"
      "v_fmac_f32 v69, s73, v64\n"
      "v_lshlrev_b32 v61, 16, v65\n"
      "v_and_b32 v65, 0xffff0000, v65\n"
      "v_fmac_f32 v70, s74, v61\n"
      "v_fmac_f32 v71, s75, v65\n"
      "s_waitcnt lgkmcnt(0)\n"
      "v_lshlrev_b32 v62, 16, v66\n"
      "v_and_b32 v66, 0xffff0000, v66\n"
      "v_fmac_f32 v26, s76, v62\n"
      "v_fmac_f32 v27, s77, v66\n"
      "v_lshlrev_b32 v63, 16, v67\n"
      "v_and_b32 v67, 0xffff0000, v67\n"
      "v_fmac_f32 v28, s78, v63\n"
      "v_fmac_f32 v29, s79, v67\n"
      "v_cvt_pk_bf16_f32 v60, v68, v69\n"
      "v_cvt_pk_bf16_f32 v61, v70, v71\n"
      "v_cvt_pk_bf16_f32 v62, v26, v27\n"
      "v_cvt_pk_bf16_f32 v63, v28, v29\n"
      "v_and_or_b32 v13, v12, s58, v42\n"
      "buffer_store_dwordx4 v[60:63], v13, s[24:27], s57 offen\n"
      ".LEx%=:\ns_endpgm\n"
      ".Lcsgsk%=:\n"
      "s_sleep 4\n"
      "s_sub_u32 s59, s59, 1\n"
      "s_cbranch_scc1 .Lcsgok%=\n"
      "buffer_load_dwordx4 v[64:67], v42, s[16:19], s62 offen sc1\n"
      "s_waitcnt vmcnt(0)\n"
      "s_branch .Lcsgk%=\n"
      ".Lcsgsh%=:\n"
      "s_sleep 4\n"
      "s_sub_u32 s59, s59, 1\n"
      "s_cbranch_scc1 .Lcsgoh%=\n"
      "buffer_load_dwordx4 v[68:71], v42, s[16:19], s63 offen sc1\n"
      "s_waitcnt vmcnt(0)\n"
      "s_branch .Lcsgh%=\n"
      ".Lch%=:\n"
      "s_movk_i32 s59, 0x7fff\n"
      ".Lchl%=:\n"
      "s_waitcnt vmcnt(0)\n"
      "v_cmp_ne_u32 vcc, s15, v253\n"
      "v_not_b32 v255, v252\n"
      "s_sub_u32 s59, s59, 1\n"
      "s_cbranch_scc1 .Lchk%=\n"
      "s_cbranch_vccz .Lchk%=\n"
      "s_sleep 1\n"
      "buffer_load_dwordx2 v[252:253], v9, s[16:19], s43 offen sc1\n"
      "s_branch .Lchl%=\n"
      ".Lchk%=:\n"
      "v_and_b32 v31, 0x1ff, v255\n"
      "v_bfe_u32 v8, v255, 9, 2\n"
      "v_lshl_add_u32 v31, v31, 2, v8\n"
      "v_mov_b32 v8, 64\n"
      "v_add_u32 v31, 0x27700, v31\n"
      "ds_write_b8 v31, v8 \n"
      "v_and_b32 v31, 1, v255\n"
      "v_and_b32 v8, 0x7fe, v255\n"
      "v_lshl_add_u32 v8, v31, 11, v8\n"
      "v_mul_u32_u24 v31, 0x1120, v10\n"
      "v_add_u32 v8, v8, v31\n"
      "v_lshlrev_b32 v31, 1, v10\n"
      "v_add_u32 v31, 0x27600, v31\n"
      "v_and_b32 v255, 0x7ff, v255\n"
      "v_lshl_add_u32 v251, v255, 11, v10\n"
      "v_and_b32 v252, 32, v43\n"
      "v_lshl_or_b32 v251, v252, 25, v251\n"
      "ds_read_b64 v[254:255], v31 \n"
      "v_mov_b32 v252, 0\n"
      "v_mov_b32 v253, 0\n"
      "s_waitcnt lgkmcnt(0)\n"
      "s_branch .Lcho%=\n"
      ".LG%=:\n"
      "s_setprio 3\n"
      "s_mul_i32 s35, s48, 14\n"
      "s_add_u32 s35, s35, s49\n"
      "s_and_b32 s55, s33, 3\n"
      "s_mul_i32 s50, s55, 1835008\n"
      "s_lshr_b32 s51, s35, 1\n"
      "s_lshl_b32 s51, s51, 7\n"
      "s_add_u32 s50, s50, s51\n"
      "v_lshrrev_b32 v37, 3, v43\n"
      "v_and_b32 v38, 7, v43\n"
      "v_mul_u32_u24 v37, 0xe000, v37\n"
      "v_lshlrev_b32 v38, 4, v38\n"
      "v_add3_u32 v32, v37, v38, s50\n"
      "s_cmp_lt_u32 s34, 112\n"
      "s_cselect_b32 s26, 0x700000, 0\n"
      "s_mov_b32 s27, 0x20000\n"
      "s_bitcmp1_b32 s49, 0\n"
      "s_waitcnt lgkmcnt(0)\n"
      "s_load_dword s15, s[88:89], 0x0\n"
      "s_cselect_b32 s24, s82, s80\n"
      "s_cselect_b32 s25, s83, s81\n"
      "s_and_b32 s25, s25, 0xffff\n"
      "s_mov_b32 s16, s88\n"
      "s_and_b32 s17, s89, 0xffff\n"
      "s_mov_b32 s18, 0x6e0000\n"
      "s_mov_b32 s19, 0x20000\n"
      "s_mov_b32 s36, s86\n"
      "s_and_b32 s37, s87, 0xffff\n"
      "s_barrier\n"
      "s_sleep 20\n"
      "s_mov_b32 s60, 0\n"
      "buffer_load_dwordx4 v[64:67], v32, s[24:27], s60 offen nt\n"
      "s_sleep 2\n"
      "s_mov_b32 s60, 7168\n"
      "buffer_load_dwordx4 v[68:71], v32, s[24:27], s60 offen nt\n"
      "s_sleep 2\n"
      "s_mov_b32 s60, 14336\n"
      "buffer_load_dwordx4 v[72:75], v32, s[24:27], s60 offen nt\n"
      "s_sleep 2\n"
      "s_mov_b32 s60, 21504\n"
      "buffer_load_dwordx4 v[76:79], v32, s[24:27], s60 offen nt\n"
      "s_sleep 2\n"
      "s_mov_b32 s60, 28672\n"
      "buffer_load_dwordx4 v[80:83], v32, s[24:27], s60 offen nt\n"
      "s_sleep 2\n"
      "s_mov_b32 s60, 35840\n"
      "buffer_load_dwordx4 v[84:87], v32, s[24:27], s60 offen nt\n"
      "s_sleep 2\n"
      "s_mov_b32 s60, 43008\n"
      "buffer_load_dwordx4 v[88:91], v32, s[24:27], s60 offen nt\n"
      "s_sleep 2\n"
      "s_mov_b32 s60, 50176\n"
      "buffer_load_dwordx4 v[92:95], v32, s[24:27], s60 offen nt\n"
      "s_sleep 2\n"
      "s_mov_b32 s60, 458752\n"
      "buffer_load_dwordx4 v[96:99], v32, s[24:27], s60 offen nt\n"
      "s_sleep 2\n"
      "s_waitcnt lgkmcnt(0)\n"
      "s_add_u32 s15, s15, 0x9e3779b1\n"
      "v_mov_b32 v47, s15\n"
      "s_cmp_ge_u32 s34, 112\n"
      "s_cbranch_scc1 .LT%=\n"
      "v_mov_b32 v48, 0x22680\n"
      "ds_read_b32 v48, v48 \n"
      "s_waitcnt lgkmcnt(0)\n"
      "v_readfirstlane_b32 s12, v48\n"
      "s_mov_b32 s62, 0x05040100\n"
      "s_mov_b32 s63, 0x07060302\n"
      "s_lshl_b32 s42, s34, 3\n"
      "s_add_u32 s42, s42, 2048\n"
      "s_lshl_b32 s43, s48, 8\n"
      "s_and_b32 s50, s49, 1\n"
      "s_lshl_b32 s50, s50, 7\n"
      "s_add_u32 s43, s43, s50\n"
      "s_add_u32 s43, s43, 65536\n"
      "s_mov_b32 s20, s64\n"
      "s_and_b32 s21, s65, 0xffff\n"
      "s_mov_b32 s22, 0x400000\n"
      "s_mov_b32 s23, 0x20000\n"
      "v_lshrrev_b32 v39, 4, v43\n"
      "v_and_b32 v38, 15, v43\n"
      "v_lshlrev_b32 v38, 4, v38\n"
      "v_bfe_u32 v33, v43, 2, 2\n"
      "v_and_b32 v37, 1, v43\n"
      "v_lshl_add_u32 v33, v33, 1, v37\n"
      "v_lshlrev_b32 v33, 3, v33\n"
      "v_and_b32 v37, 2, v43\n"
      "v_lshl_add_u32 v37, v39, 2, v37\n"
      "s_lshl_b32 s50, s55, 9\n"
      "v_lshl_add_u32 v36, v37, 3, s50\n"
      "s_lshl_b32 s51, s55, 4\n"
      "v_lshrrev_b32 v40, 5, v43\n"
      "v_lshl_add_u32 v40, v40, 2, s51\n"
      "s_lshl_b32 s51, s55, 2\n"
      "s_add_u32 s51, s51, 140800\n"
      "v_mov_b32 v41, s51\n"
      "v_mov_b32 v42, 0x22600\n"
      "v_lshl_add_u32 v37, v43, 3, s50\n"
      "v_add_u32 v35, 0x22700, v37\n"
      "s_lshl_b32 s50, s55, 11\n"
      "s_add_u32 s50, s50, 141056\n"
      "v_lshl_add_u32 v34, v39, 9, v38\n"
      "v_add_u32 v34, s50, v34\n"
      "s_lshr_b32 s45, s35, 1\n"
      "s_lshl_b32 s45, s45, 16\n"
      "s_and_b32 s50, s35, 1\n"
      "s_lshl_b32 s50, s50, 11\n"
      "s_add_u32 s45, s45, s50\n"
      "s_add_u32 s45, s45, 524288\n"
      "s_mov_b32 s56, 0x3f80\n"
      "s_mov_b32 s57, 0x3f800000\n"
      "s_movk_i32 s61, 0x7ff\n"
      "s_mov_b32 s58, 131072\n"
      "s_mov_b32 s54, 0\n"
      "s_mov_b64 s[66:67], 0\n"
      "s_movk_i32 s70, 427\n"
      "s_waitcnt vmcnt(8)\n"
      "s_nop 0\n"
      "s_mov_b32 s60, 465920\n"
      "buffer_load_dwordx4 v[100:103], v32, s[24:27], s60 offen nt\n"
      "s_waitcnt vmcnt(8)\n"
      "s_nop 0\n"
      "s_mov_b32 s60, 473088\n"
      "buffer_load_dwordx4 v[104:107], v32, s[24:27], s60 offen nt\n"
      "v_perm_b32 v192, v68, v64, s62\n"
      "v_perm_b32 v196, v68, v64, s63\n"
      "v_perm_b32 v200, v69, v65, s62\n"
      "v_perm_b32 v204, v69, v65, s63\n"
      "v_perm_b32 v208, v70, v66, s62\n"
      "v_perm_b32 v212, v70, v66, s63\n"
      "v_perm_b32 v216, v71, v67, s62\n"
      "v_perm_b32 v220, v71, v67, s63\n"
      "s_waitcnt vmcnt(8)\n"
      "s_nop 0\n"
      "s_mov_b32 s60, 480256\n"
      "buffer_load_dwordx4 v[108:111], v32, s[24:27], s60 offen nt\n"
      "s_waitcnt vmcnt(8)\n"
      "s_nop 0\n"
      "s_mov_b32 s60, 487424\n"
      "buffer_load_dwordx4 v[112:115], v32, s[24:27], s60 offen nt\n"
      "v_perm_b32 v193, v76, v72, s62\n"
      "v_perm_b32 v197, v76, v72, s63\n"
      "v_perm_b32 v201, v77, v73, s62\n"
      "v_perm_b32 v205, v77, v73, s63\n"
      "v_perm_b32 v209, v78, v74, s62\n"
      "v_perm_b32 v213, v78, v74, s63\n"
      "v_perm_b32 v217, v79, v75, s62\n"
      "v_perm_b32 v221, v79, v75, s63\n"
      "s_waitcnt vmcnt(8)\n"
      "s_nop 0\n"
      "s_mov_b32 s60, 494592\n"
      "buffer_load_dwordx4 v[116:119], v32, s[24:27], s60 offen nt\n"
      "s_waitcnt vmcnt(8)\n"
      "s_nop 0\n"
      "s_mov_b32 s60, 501760\n"
      "buffer_load_dwordx4 v[120:123], v32, s[24:27], s60 offen nt\n"
      "v_perm_b32 v194, v84, v80, s62\n"
      "v_perm_b32 v198, v84, v80, s63\n"
      "v_perm_b32 v202, v85, v81, s62\n"
      "v_perm_b32 v206, v85, v81, s63\n"
      "v_perm_b32 v210, v86, v82, s62\n"
      "v_perm_b32 v214, v86, v82, s63\n"
      "v_perm_b32 v218, v87, v83, s62\n"
      "v_perm_b32 v222, v87, v83, s63\n"
      "s_waitcnt vmcnt(8)\n"
      "s_nop 0\n"
      "s_mov_b32 s60, 508928\n"
      "buffer_load_dwordx4 v[124:127], v32, s[24:27], s60 offen nt\n"
      "s_waitcnt vmcnt(8)\n"
      "s_nop 0\n"
      "s_mov_b32 s60, 917504\n"
      "buffer_load_dwordx4 v[128:131], v32, s[24:27], s60 offen nt\n"
      "v_perm_b32 v195, v92, v88, s62\n"
      "v_perm_b32 v199, v92, v88, s63\n"
      "v_perm_b32 v203, v93, v89, s62\n"
      "v_perm_b32 v207, v93, v89, s63\n"
      "v_perm_b32 v211, v94, v90, s62\n"
      "v_perm_b32 v215, v94, v90, s63\n"
      "v_perm_b32 v219, v95, v91, s62\n"
      "v_perm_b32 v223, v95, v91, s63\n"
      "s_waitcnt vmcnt(8)\n"
      "s_nop 0\n"
      "s_mov_b32 s60, 924672\n"
      "buffer_load_dwordx4 v[132:135], v32, s[24:27], s60 offen nt\n"
      "s_waitcnt vmcnt(8)\n"
      "s_nop 0\n"
      "s_mov_b32 s60, 931840\n"
      "buffer_load_dwordx4 v[136:139], v32, s[24:27], s60 offen nt\n"
      "v_perm_b32 v64, v100, v96, s62\n"
      "v_perm_b32 v68, v100, v96, s63\n"
      "v_perm_b32 v72, v101, v97, s62\n"
      "v_perm_b32 v76, v101, v97, s63\n"
      "v_perm_b32 v80, v102, v98, s62\n"
      "v_perm_b32 v84, v102, v98, s63\n"
      "v_perm_b32 v88, v103, v99, s62\n"
      "v_perm_b32 v92, v103, v99, s63\n"
      "s_waitcnt vmcnt(8)\n"
      "s_nop 0\n"
      "s_mov_b32 s60, 939008\n"
      "buffer_load_dwordx4 v[140:143], v32, s[24:27], s60 offen nt\n"
      "s_waitcnt vmcnt(8)\n"
      "s_nop 0\n"
      "s_mov_b32 s60, 946176\n"
      "buffer_load_dwordx4 v[144:147], v32, s[24:27], s60 offen nt\n"
      "v_perm_b32 v65, v108, v104, s62\n"
      "v_perm_b32 v69, v108, v104, s63\n"
      "v_perm_b32 v73, v109, v105, s62\n"
      "v_perm_b32 v77, v109, v105, s63\n"
      "v_perm_b32 v81, v110, v106, s62\n"
      "v_perm_b32 v85, v110, v106, s63\n"
      "v_perm_b32 v89, v111, v107, s62\n"
      "v_perm_b32 v93, v111, v107, s63\n"
      "s_waitcnt vmcnt(8)\n"
      "s_nop 0\n"
      "s_mov_b32 s60, 953344\n"
      "buffer_load_dwordx4 v[148:151], v32, s[24:27], s60 offen nt\n"
      "s_waitcnt vmcnt(8)\n"
      "s_nop 0\n"
      "s_mov_b32 s60, 960512\n"
      "buffer_load_dwordx4 v[152:155], v32, s[24:27], s60 offen nt\n"
      "v_perm_b32 v66, v116, v112, s62\n"
      "v_perm_b32 v70, v116, v112, s63\n"
      "v_perm_b32 v74, v117, v113, s62\n"
      "v_perm_b32 v78, v117, v113, s63\n"
      "v_perm_b32 v82, v118, v114, s62\n"
      "v_perm_b32 v86, v118, v114, s63\n"
      "v_perm_b32 v90, v119, v115, s62\n"
      "v_perm_b32 v94, v119, v115, s63\n"
      "s_waitcnt vmcnt(8)\n"
      "s_nop 0\n"
      "s_mov_b32 s60, 967680\n"
      "buffer_load_dwordx4 v[156:159], v32, s[24:27], s60 offen nt\n"
      "s_waitcnt vmcnt(8)\n"
      "s_nop 0\n"
      "s_mov_b32 s60, 1376256\n"
      "buffer_load_dwordx4 v[160:163], v32, s[24:27], s60 offen nt\n"
      "v_perm_b32 v67, v124, v120, s62\n"
      "v_perm_b32 v71, v124, v120, s63\n"
      "v_perm_b32 v75, v125, v121, s62\n"
      "v_perm_b32 v79, v125, v121, s63\n"
      "v_perm_b32 v83, v126, v122, s62\n"
      "v_perm_b32 v87, v126, v122, s63\n"
      "v_perm_b32 v91, v127, v123, s62\n"
      "v_perm_b32 v95, v127, v123, s63\n"
      "s_waitcnt vmcnt(8)\n"
      "s_nop 0\n"
      "s_mov_b32 s60, 1383424\n"
      "buffer_load_dwordx4 v[164:167], v32, s[24:27], s60 offen nt\n"
      "s_waitcnt vmcnt(8)\n"
      "s_nop 0\n"
      "s_mov_b32 s60, 1390592\n"
      "buffer_load_dwordx4 v[168:171], v32, s[24:27], s60 offen nt\n"
      "v_perm_b32 v96, v132, v128, s62\n"
      "v_perm_b32 v100, v132, v128, s63\n"
      "v_perm_b32 v104, v133, v129, s62\n"
      "v_perm_b32 v108, v133, v129, s63\n"
      "v_perm_b32 v112, v134, v130, s62\n"
      "v_perm_b32 v116, v134, v130, s63\n"
      "v_perm_b32 v120, v135, v131, s62\n"
      "v_perm_b32 v124, v135, v131, s63\n"
      "s_waitcnt vmcnt(8)\n"
      "s_nop 0\n"
      "s_mov_b32 s60, 1397760\n"
      "buffer_load_dwordx4 v[172:175], v32, s[24:27], s60 offen nt\n"
      "s_waitcnt vmcnt(8)\n"
      "s_nop 0\n"
      "s_mov_b32 s60, 1404928\n"
      "buffer_load_dwordx4 v[176:179], v32, s[24:27], s60 offen nt\n"
      "v_perm_b32 v97, v140, v136, s62\n"
      "v_perm_b32 v101, v140, v136, s63\n"
      "v_perm_b32 v105, v141, v137, s62\n"
      "v_perm_b32 v109, v141, v137, s63\n"
      "v_perm_b32 v113, v142, v138, s62\n"
      "v_perm_b32 v117, v142, v138, s63\n"
      "v_perm_b32 v121, v143, v139, s62\n"
      "v_perm_b32 v125, v143, v139, s63\n"
      "s_waitcnt vmcnt(8)\n"
      "s_nop 0\n"
      "s_mov_b32 s60, 1412096\n"
      "buffer_load_dwordx4 v[180:183], v32, s[24:27], s60 offen nt\n"
      "s_waitcnt vmcnt(8)\n"
      "s_nop 0\n"
      "s_mov_b32 s60, 1419264\n"
      "buffer_load_dwordx4 v[184:187], v32, s[24:27], s60 offen nt\n"
      "v_perm_b32 v98, v148, v144, s62\n"
      "v_perm_b32 v102, v148, v144, s63\n"
      "v_perm_b32 v106, v149, v145, s62\n"
      "v_perm_b32 v110, v149, v145, s63\n"
      "v_perm_b32 v114, v150, v146, s62\n"
      "v_perm_b32 v118, v150, v146, s63\n"
      "v_perm_b32 v122, v151, v147, s62\n"
      "v_perm_b32 v126, v151, v147, s63\n"
      "s_waitcnt vmcnt(8)\n"
      "s_nop 0\n"
      "s_mov_b32 s60, 1426432\n"
      "buffer_load_dwordx4 v[188:191], v32, s[24:27], s60 offen nt\n"
      "ds_write_b32 v41, v47 offset:80\n"
      "s_waitcnt vmcnt(8)\n"
      "s_nop 0\n"
      "v_perm_b32 v99, v156, v152, s62\n"
      "v_perm_b32 v103, v156, v152, s63\n"
      "v_perm_b32 v107, v157, v153, s62\n"
      "v_perm_b32 v111, v157, v153, s63\n"
      "v_perm_b32 v115, v158, v154, s62\n"
      "v_perm_b32 v119, v158, v154, s63\n"
      "v_perm_b32 v123, v159, v155, s62\n"
      "v_perm_b32 v127, v159, v155, s63\n"
      "s_waitcnt vmcnt(7)\n"
      "s_nop 0\n"
      "s_memrealtime s[68:69]\n"
      "s_waitcnt vmcnt(6)\n"
      "s_nop 0\n"
      "s_waitcnt lgkmcnt(0)\n"
      "s_sub_u32 s68, s68, s12\n"
      "s_cmp_lt_u32 s68, s70\n"
      "s_cbranch_scc1 .Lgn25%=\n"
      "buffer_load_dwordx2 v[8:9], v33, s[16:19], s43 offen sc1\n"
      "s_mov_b32 s70, -1\n"
      ".Lgn25%=:\n"
      "s_memrealtime s[68:69]\n"
      "v_perm_b32 v128, v164, v160, s62\n"
      "v_perm_b32 v132, v164, v160, s63\n"
      "v_perm_b32 v136, v165, v161, s62\n"
      "v_perm_b32 v140, v165, v161, s63\n"
      "v_perm_b32 v144, v166, v162, s62\n"
      "v_perm_b32 v148, v166, v162, s63\n"
      "v_perm_b32 v152, v167, v163, s62\n"
      "v_perm_b32 v156, v167, v163, s63\n"
      "s_waitcnt vmcnt(5)\n"
      "s_nop 0\n"
      "s_waitcnt lgkmcnt(0)\n"
      "s_sub_u32 s68, s68, s12\n"
      "s_cmp_lt_u32 s68, s70\n"
      "s_cbranch_scc1 .Lgn26%=\n"
      "buffer_load_dwordx2 v[8:9], v33, s[16:19], s43 offen sc1\n"
      "s_mov_b32 s70, -1\n"
      ".Lgn26%=:\n"
      "s_memrealtime s[68:69]\n"
      "s_waitcnt vmcnt(4)\n"
      "s_nop 0\n"
      "s_waitcnt lgkmcnt(0)\n"
      "s_sub_u32 s68, s68, s12\n"
      "s_cmp_lt_u32 s68, s70\n"
      "s_cbranch_scc1 .Lgn27%=\n"
      "buffer_load_dwordx2 v[8:9], v33, s[16:19], s43 offen sc1\n"
      "s_mov_b32 s70, -1\n"
      ".Lgn27%=:\n"
      "s_memrealtime s[68:69]\n"
      "v_perm_b32 v129, v172, v168, s62\n"
      "v_perm_b32 v133, v172, v168, s63\n"
      "v_perm_b32 v137, v173, v169, s62\n"
      "v_perm_b32 v141, v173, v169, s63\n"
      "v_perm_b32 v145, v174, v170, s62\n"
      "v_perm_b32 v149, v174, v170, s63\n"
      "v_perm_b32 v153, v175, v171, s62\n"
      "v_perm_b32 v157, v175, v171, s63\n"
      "s_waitcnt vmcnt(3)\n"
      "s_nop 0\n"
      "s_waitcnt lgkmcnt(0)\n"
      "s_sub_u32 s68, s68, s12\n"
      "s_cmp_lt_u32 s68, s70\n"
      "s_cbranch_scc1 .Lgn28%=\n"
      "buffer_load_dwordx2 v[8:9], v33, s[16:19], s43 offen sc1\n"
      "s_mov_b32 s70, -1\n"
      ".Lgn28%=:\n"
      "s_memrealtime s[68:69]\n"
      "s_waitcnt vmcnt(2)\n"
      "s_nop 0\n"
      "s_waitcnt lgkmcnt(0)\n"
      "s_sub_u32 s68, s68, s12\n"
      "s_cmp_lt_u32 s68, s70\n"
      "s_cbranch_scc1 .Lgn29%=\n"
      "buffer_load_dwordx2 v[8:9], v33, s[16:19], s43 offen sc1\n"
      "s_mov_b32 s70, -1\n"
      ".Lgn29%=:\n"
      "s_memrealtime s[68:69]\n"
      "v_perm_b32 v130, v180, v176, s62\n"
      "v_perm_b32 v134, v180, v176, s63\n"
      "v_perm_b32 v138, v181, v177, s62\n"
      "v_perm_b32 v142, v181, v177, s63\n"
      "v_perm_b32 v146, v182, v178, s62\n"
      "v_perm_b32 v150, v182, v178, s63\n"
      "v_perm_b32 v154, v183, v179, s62\n"
      "v_perm_b32 v158, v183, v179, s63\n"
      "s_waitcnt vmcnt(1)\n"
      "s_nop 0\n"
      "s_waitcnt lgkmcnt(0)\n"
      "s_sub_u32 s68, s68, s12\n"
      "s_cmp_lt_u32 s68, s70\n"
      "s_cbranch_scc1 .Lgn30%=\n"
      "buffer_load_dwordx2 v[8:9], v33, s[16:19], s43 offen sc1\n"
      "s_mov_b32 s70, -1\n"
      ".Lgn30%=:\n"
      "s_memrealtime s[68:69]\n"
      "s_waitcnt vmcnt(0)\n"
      "s_nop 0\n"
      "s_waitcnt lgkmcnt(0)\n"
      "s_sub_u32 s68, s68, s12\n"
      "s_cmp_lt_u32 s68, s70\n"
      "s_cbranch_scc1 .Lgn31%=\n"
      "buffer_load_dwordx2 v[8:9], v33, s[16:19], s43 offen sc1\n"
      "s_mov_b32 s70, -1\n"
      ".Lgn31%=:\n"
      "v_perm_b32 v131, v188, v184, s62\n"
      "v_perm_b32 v135, v188, v184, s63\n"
      "v_perm_b32 v139, v189, v185, s62\n"
      "v_perm_b32 v143, v189, v185, s63\n"
      "v_perm_b32 v147, v190, v186, s62\n"
      "v_perm_b32 v151, v190, v186, s63\n"
      "v_perm_b32 v155, v191, v187, s62\n"
      "v_perm_b32 v159, v191, v187, s63\n"
      "s_movk_i32 s59, 0x3fff\n"
      "s_cmp_eq_u32 s70, -1\n"
      "s_cbranch_scc1 .Lghc%=\n"
      ".Lgpc3_%=:\n"
      "s_memrealtime s[68:69]\n"
      "s_waitcnt lgkmcnt(0)\n"
      "s_sub_u32 s68, s68, s12\n"
      "s_cmp_ge_u32 s68, 435\n"
      "s_cbranch_scc1 .Lgpd3_%=\n"
      "s_sleep 1\n"
      "s_branch .Lgpc3_%=\n"
      ".Lgpd3_%=:\n"
      ".Lgh%=:\n"
      "buffer_load_dwordx2 v[8:9], v33, s[16:19], s43 offen sc1\n"
      ".Lghc%=:\n"
      "s_waitcnt vmcnt(0)\n"
      "v_cmp_ne_u32 vcc, s15, v9\n"
      "v_bitop3_b32 v10, v8, s61, v8 bitop3:0x0c\n"
      "s_sub_u32 s59, s59, 1\n"
      "s_cbranch_vccz .Lgho%=\n"
      "s_sleep 2\n"
      "s_cbranch_scc0 .Lgh%=\n"
      ".Lgho%=:\n"
      "v_lshrrev_b32 v45, 9, v10\n"
      "v_lshl_add_u32 v11, v10, 11, v36\n"
      "v_mul_u32_u24 v45, 0xf000, v45\n"
      "v_lshl_add_u32 v45, v10, 3, v45\n"
      "buffer_load_dwordx2 v[12:13], off, s[16:19], s43 offset:64 sc1\n"
      "buffer_load_dwordx2 v[62:63], v45, s[16:19], s58 offen sc1\n"
      "buffer_load_dwordx4 v[16:19], v11, s[20:23], 0 offen offset:0\n"
      "buffer_load_dwordx4 v[20:23], v11, s[20:23], 0 offen offset:128\n"
      "buffer_load_dwordx4 v[24:27], v11, s[20:23], 0 offen offset:256\n"
      "buffer_load_dwordx4 v[28:31], v11, s[20:23], 0 offen offset:384\n"
      "ds_write_b32 v41, v47 offset:64\n"
      ".Lghx%=:\n"
      "s_movk_i32 s59, 0x7fff\n"
      "s_waitcnt vmcnt(4)\n"
      "s_nop 0\n"
      ".Lgs%=:\n"
      "v_cmp_ne_u32 vcc, s15, v63\n"
      "v_lshlrev_b32 v10, 16, v62\n"
      "v_and_b32 v44, 0xffff0000, v62\n"
      "s_cbranch_vccnz .Lgss%=\n"
      ".Lgso%=:\n"
      "v_rcp_f32 v44, v44\n"
      "v_xor_b32 v10, 0x80000000, v10\n"
      "v_readfirstlane_b32 s68, v13\n"
      "v_readfirstlane_b32 s69, v12\n"
      "s_cmp_eq_u32 s68, s15\n"
      "s_cselect_b32 s12, s69, s12\n"
      "v_mov_b32 v12, s12\n"
      "ds_write_b32 v42, v12 offset:128\n"
      "s_waitcnt vmcnt(3)\n"
      "v_dot2_f32_bf16 v168, v16, s56, v10\n"
      "v_dot2_f32_bf16 v169, v16, s57, v10\n"
      "v_dot2_f32_bf16 v170, v17, s56, v10\n"
      "v_dot2_f32_bf16 v171, v17, s57, v10\n"
      "v_dot2_f32_bf16 v172, v18, s56, v10\n"
      "v_dot2_f32_bf16 v173, v18, s57, v10\n"
      "v_dot2_f32_bf16 v174, v19, s56, v10\n"
      "v_dot2_f32_bf16 v175, v19, s57, v10\n"
      "v_cvt_pk_bf16_f32 v16, v168, v169\n"
      "v_cvt_pk_bf16_f32 v17, v170, v171\n"
      "v_cvt_pk_bf16_f32 v18, v172, v173\n"
      "v_cvt_pk_bf16_f32 v19, v174, v175\n"
      "ds_bpermute_b32 v44, v40, v44\n"
      "s_waitcnt vmcnt(2)\n"
      "v_dot2_f32_bf16 v176, v20, s56, v10\n"
      "v_dot2_f32_bf16 v177, v20, s57, v10\n"
      "v_dot2_f32_bf16 v178, v21, s56, v10\n"
      "v_dot2_f32_bf16 v179, v21, s57, v10\n"
      "v_dot2_f32_bf16 v180, v22, s56, v10\n"
      "v_dot2_f32_bf16 v181, v22, s57, v10\n"
      "v_dot2_f32_bf16 v182, v23, s56, v10\n"
      "v_dot2_f32_bf16 v183, v23, s57, v10\n"
      "v_cvt_pk_bf16_f32 v20, v176, v177\n"
      "v_cvt_pk_bf16_f32 v21, v178, v179\n"
      "v_cvt_pk_bf16_f32 v22, v180, v181\n"
      "v_cvt_pk_bf16_f32 v23, v182, v183\n"
      "s_waitcnt vmcnt(1)\n"
      "v_dot2_f32_bf16 v168, v24, s56, v10\n"
      "v_dot2_f32_bf16 v169, v24, s57, v10\n"
      "v_dot2_f32_bf16 v170, v25, s56, v10\n"
      "v_dot2_f32_bf16 v171, v25, s57, v10\n"
      "v_dot2_f32_bf16 v172, v26, s56, v10\n"
      "v_dot2_f32_bf16 v173, v26, s57, v10\n"
      "v_dot2_f32_bf16 v174, v27, s56, v10\n"
      "v_dot2_f32_bf16 v175, v27, s57, v10\n"
      "v_cvt_pk_bf16_f32 v24, v168, v169\n"
      "v_cvt_pk_bf16_f32 v25, v170, v171\n"
      "v_cvt_pk_bf16_f32 v26, v172, v173\n"
      "v_cvt_pk_bf16_f32 v27, v174, v175\n"
      "s_waitcnt vmcnt(0)\n"
      "v_dot2_f32_bf16 v176, v28, s56, v10\n"
      "v_dot2_f32_bf16 v177, v28, s57, v10\n"
      "v_dot2_f32_bf16 v178, v29, s56, v10\n"
      "v_dot2_f32_bf16 v179, v29, s57, v10\n"
      "v_dot2_f32_bf16 v180, v30, s56, v10\n"
      "v_dot2_f32_bf16 v181, v30, s57, v10\n"
      "v_dot2_f32_bf16 v182, v31, s56, v10\n"
      "v_dot2_f32_bf16 v183, v31, s57, v10\n"
      "v_cvt_pk_bf16_f32 v28, v176, v177\n"
      "v_cvt_pk_bf16_f32 v29, v178, v179\n"
      "v_cvt_pk_bf16_f32 v30, v180, v181\n"
      "v_cvt_pk_bf16_f32 v31, v182, v183\n"
      "v_mfma_f32_16x16x32_bf16 v[224:227], v[16:19], v[192:195], 0\n"
      "v_mfma_f32_16x16x32_bf16 v[228:231], v[16:19], v[196:199], 0\n"
      "v_mfma_f32_16x16x32_bf16 v[232:235], v[16:19], v[200:203], 0\n"
      "v_mfma_f32_16x16x32_bf16 v[236:239], v[16:19], v[204:207], 0\n"
      "v_mfma_f32_16x16x32_bf16 v[240:243], v[16:19], v[208:211], 0\n"
      "v_mfma_f32_16x16x32_bf16 v[244:247], v[16:19], v[212:215], 0\n"
      "v_mfma_f32_16x16x32_bf16 v[248:251], v[16:19], v[216:219], 0\n"
      "v_mfma_f32_16x16x32_bf16 v[252:255], v[16:19], v[220:223], 0\n"
      "v_mfma_f32_16x16x32_bf16 v[224:227], v[20:23], v[64:67], v[224:227]\n"
      "v_mfma_f32_16x16x32_bf16 v[228:231], v[20:23], v[68:71], v[228:231]\n"
      "v_mfma_f32_16x16x32_bf16 v[232:235], v[20:23], v[72:75], v[232:235]\n"
      "v_mfma_f32_16x16x32_bf16 v[236:239], v[20:23], v[76:79], v[236:239]\n"
      "v_mfma_f32_16x16x32_bf16 v[240:243], v[20:23], v[80:83], v[240:243]\n"
      "v_mfma_f32_16x16x32_bf16 v[244:247], v[20:23], v[84:87], v[244:247]\n"
      "v_mfma_f32_16x16x32_bf16 v[248:251], v[20:23], v[88:91], v[248:251]\n"
      "v_mfma_f32_16x16x32_bf16 v[252:255], v[20:23], v[92:95], v[252:255]\n"
      "v_mfma_f32_16x16x32_bf16 v[224:227], v[24:27], v[96:99], v[224:227]\n"
      "v_mfma_f32_16x16x32_bf16 v[228:231], v[24:27], v[100:103], v[228:231]\n"
      "v_mfma_f32_16x16x32_bf16 v[232:235], v[24:27], v[104:107], v[232:235]\n"
      "v_mfma_f32_16x16x32_bf16 v[236:239], v[24:27], v[108:111], v[236:239]\n"
      "v_mfma_f32_16x16x32_bf16 v[240:243], v[24:27], v[112:115], v[240:243]\n"
      "v_mfma_f32_16x16x32_bf16 v[244:247], v[24:27], v[116:119], v[244:247]\n"
      "v_mfma_f32_16x16x32_bf16 v[248:251], v[24:27], v[120:123], v[248:251]\n"
      "v_mfma_f32_16x16x32_bf16 v[252:255], v[24:27], v[124:127], v[252:255]\n"
      "v_mfma_f32_16x16x32_bf16 v[224:227], v[28:31], v[128:131], v[224:227]\n"
      "v_mfma_f32_16x16x32_bf16 v[228:231], v[28:31], v[132:135], v[228:231]\n"
      "v_mfma_f32_16x16x32_bf16 v[232:235], v[28:31], v[136:139], v[232:235]\n"
      "v_mfma_f32_16x16x32_bf16 v[236:239], v[28:31], v[140:143], v[236:239]\n"
      "s_nop 4\n"
      "v_add_f32_dpp v160, v226, v224 row_ror:8 row_mask:0xf bank_mask:0x3\n"
      "v_add_f32_dpp v164, v227, v225 row_ror:8 row_mask:0xf bank_mask:0x3\n"
      "v_mfma_f32_16x16x32_bf16 v[240:243], v[28:31], v[144:147], v[240:243]\n"
      "v_add_f32_dpp v161, v230, v228 row_ror:8 row_mask:0xf bank_mask:0x3\n"
      "v_add_f32_dpp v165, v231, v229 row_ror:8 row_mask:0xf bank_mask:0x3\n"
      "v_mfma_f32_16x16x32_bf16 v[244:247], v[28:31], v[148:151], v[244:247]\n"
      "v_add_f32_dpp v162, v234, v232 row_ror:8 row_mask:0xf bank_mask:0x3\n"
      "v_add_f32_dpp v166, v235, v233 row_ror:8 row_mask:0xf bank_mask:0x3\n"
      "v_mfma_f32_16x16x32_bf16 v[248:251], v[28:31], v[152:155], v[248:251]\n"
      "v_add_f32_dpp v163, v238, v236 row_ror:8 row_mask:0xf bank_mask:0x3\n"
      "v_add_f32_dpp v167, v239, v237 row_ror:8 row_mask:0xf bank_mask:0x3\n"
      "v_mfma_f32_16x16x32_bf16 v[252:255], v[28:31], v[156:159], v[252:255]\n"
      "v_add_f32_dpp v160, v240, v242 row_ror:8 row_mask:0xf bank_mask:0xc\n"
      "v_add_f32_dpp v164, v241, v243 row_ror:8 row_mask:0xf bank_mask:0xc\n"
      "v_add_f32_dpp v161, v244, v246 row_ror:8 row_mask:0xf bank_mask:0xc\n"
      "v_add_f32_dpp v165, v245, v247 row_ror:8 row_mask:0xf bank_mask:0xc\n"
      "s_nop 0\n"
      "v_add_f32_dpp v162, v248, v250 row_ror:8 row_mask:0xf bank_mask:0xc\n"
      "v_add_f32_dpp v166, v249, v251 row_ror:8 row_mask:0xf bank_mask:0xc\n"
      "s_nop 0\n"
      "v_add_f32_dpp v163, v252, v254 row_ror:8 row_mask:0xf bank_mask:0xc\n"
      "v_add_f32_dpp v167, v253, v255 row_ror:8 row_mask:0xf bank_mask:0xc\n"
      "ds_write_b128 v34, v[160:163] \n"
      "ds_write_b128 v34, v[164:167] offset:256\n"
      "ds_write_b32 v41, v47 \n"
      "s_movk_i32 s59, 0x7fff\n"
      ".Lgw%=:\n"
      "ds_read_b128 v[72:75], v42 \n"
      "ds_read_b64 v[64:65], v35 offset:0\n"
      "ds_read_b64 v[66:67], v35 offset:2048\n"
      "ds_read_b64 v[68:69], v35 offset:4096\n"
      "ds_read_b64 v[70:71], v35 offset:6144\n"
      "s_sub_u32 s59, s59, 1\n"
      "s_cbranch_scc1 .Lgwo%=\n"
      "s_waitcnt lgkmcnt(4)\n"
      "v_xor_b32 v72, s15, v72\n"
      "v_bitop3_b32 v72, s15, v73, v72 bitop3:0xbe\n"
      "v_bitop3_b32 v72, s15, v74, v72 bitop3:0xbe\n"
      "v_bitop3_b32 v72, s15, v75, v72 bitop3:0xbe\n"
      "v_cmp_ne_u32 vcc, 0, v72\n"
      "s_waitcnt lgkmcnt(0)\n"
      "s_nop 0\n"
      "s_cbranch_vccnz .Lgw%=\n"
      ".Lgwo%=:\n"
      "s_waitcnt lgkmcnt(0)\n"
      "s_bitcmp1_b32 s49, 0\n"
      "v_pk_add_f32 v[64:65], v[64:65], v[66:67]\n"
      "v_pk_add_f32 v[68:69], v[68:69], v[70:71]\n"
      "v_pk_add_f32 v[64:65], v[64:65], v[68:69]\n"
      "v_mul_f32 v64, v64, v44\n"
      "v_mul_f32 v65, v65, v44\n"
      "v_cvt_pk_bf16_f32 v46, v64, v65\n"
      "s_cbranch_scc1 .Lgu%=\n"
      "v_lshlrev_b32 v64, 16, v46\n"
      "v_and_b32 v65, 0xffff0000, v46\n"
      "v_mul_f32 v66, 0xbfb8aa3b, v64\n"
      "v_mul_f32 v67, 0xbfb8aa3b, v65\n"
      "v_exp_f32 v66, v66\n"
      "v_exp_f32 v67, v67\n"
      "v_add_f32 v66, 1.0, v66\n"
      "v_add_f32 v67, 1.0, v67\n"
      "v_rcp_f32 v66, v66\n"
      "v_rcp_f32 v67, v67\n"
      "v_mul_f32 v64, v64, v66\n"
      "v_mul_f32 v65, v65, v67\n"
      "v_cvt_pk_bf16_f32 v46, v64, v65\n"
      ".Lgu%=:\n"
      "buffer_store_dwordx2 v[46:47], v37, s[16:19], s45 offen sc1\n"
      "s_cmp_lg_u32 s33, 4\n"
      "s_cbranch_scc1 .Lga%=\n"
      "v_mov_b32 v64, s15\n"
      "v_mov_b32 v65, s15\n"
      "buffer_store_dwordx2 v[64:65], off, s[16:19], s42 sc1\n"
      ".Lga%=:\n"
      "s_cmp_eq_u32 s33, 7\n"
      "s_mov_b32 s24, s36\n"
      "s_mov_b32 s25, s37\n"
      "s_mov_b32 s26, 0x400000\n"
      "s_cbranch_scc1 .LZ%=\n"
      "s_endpgm\n"
      ".Lgss%=:\n"
      "s_sleep 1\n"
      "s_sub_u32 s59, s59, 1\n"
      "s_cbranch_scc1 .Lgso%=\n"
      "buffer_load_dwordx2 v[62:63], v45, s[16:19], s58 offen sc1\n"
      "s_waitcnt vmcnt(0)\n"
      "s_branch .Lgs%=\n"
      ".LD2%=:\n"
      "s_setprio 0\n"
      "s_mul_i32 s35, s48, 14\n"
      "s_add_u32 s35, s35, s49\n"
      "s_lshr_b32 s52, s35, 2\n"
      "s_and_b32 s54, s35, 3\n"
      "v_lshlrev_b32 v42, 4, v49\n"
      "s_lshl_b32 s50, s52, 18\n"
      "s_lshl_b32 s51, s54, 9\n"
      "s_add_u32 s50, s50, s51\n"
      "s_lshl_b32 s51, s33, 7\n"
      "s_add_u32 s50, s50, s51\n"
      "v_lshl_add_u32 v32, v50, 14, v42\n"
      "v_add_u32 v32, s50, v32\n"
      "v_lshrrev_b32 v41, 1, v49\n"
      "v_and_b32 v43, 1, v41\n"
      "v_mul_u32_u24 v36, 0x120, v41\n"
      "v_lshlrev_b32 v43, 4, v43\n"
      "v_sub_u32 v36, v36, v43\n"
      "v_and_b32 v43, 1, v49\n"
      "v_lshl_add_u32 v36, v43, 7, v36\n"
      "v_lshrrev_b32 v41, 4, v48\n"
      "v_lshl_add_u32 v36, v41, 4, v36\n"
      "v_and_b32 v43, 8, v48\n"
      "v_add_u32 v36, v36, v43\n"
      "s_lshr_b32 s53, s33, 1\n"
      "s_mul_i32 s50, s53, 1152\n"
      "s_and_b32 s55, s33, 1\n"
      "s_lshl_b32 s51, s55, 6\n"
      "s_add_u32 s50, s50, s51\n"
      "s_add_u32 s50, s50, 149504\n"
      "v_add_u32 v36, s50, v36\n"
      "v_and_b32 v43, 1, v41\n"
      "v_mul_u32_u24 v37, 0x120, v41\n"
      "v_lshlrev_b32 v43, 4, v43\n"
      "v_sub_u32 v37, v37, v43\n"
      "v_and_b32 v43, 15, v48\n"
      "v_lshl_add_u32 v37, v43, 4, v37\n"
      "v_add_u32 v37, 0x24800, v37\n"
      "v_lshlrev_b32 v35, 4, v48\n"
      "s_lshl_b32 s46, s52, 17\n"
      "s_lshl_b32 s50, s53, 16\n"
      "s_add_u32 s46, s46, s50\n"
      "s_lshl_b32 s50, s55, 10\n"
      "s_add_u32 s46, s46, s50\n"
      "s_add_u32 s46, s46, 524288\n"
      "v_and_b32 v41, 3, v50\n"
      "v_lshrrev_b32 v43, 5, v48\n"
      "v_lshl_add_u32 v41, v41, 1, v43\n"
      "v_mul_u32_u24 v38, 0x1c00, v41\n"
      "v_add_u32 v38, v38, v42\n"
      "s_lshl_b32 s50, s54, 2\n"
      "s_add_u32 s50, s50, s33\n"
      "s_mul_i32 s47, s50, 57344\n"
      "s_lshl_b32 s50, s52, 8\n"
      "s_add_u32 s47, s47, s50\n"
      "s_add_u32 s47, s47, 4194304\n"
      "s_mov_b32 s62, 0x05040100\n"
      "s_mov_b32 s63, 0x07060302\n"
      "s_lshl_b32 s50, s33, 2\n"
      "s_add_u32 s50, s50, 140864\n"
      "v_mov_b32 v41, s50\n"
      "v_mov_b32 v42, 0x22640\n"
      "s_movk_i32 s59, 0x3fff\n"
      ".Ldf%=:\n"
      "s_sleep 1\n"
      "ds_read_b128 v[240:243], v42\n"
      "ds_read_b32 v43, v41 offset:16\n"
      "s_sub_u32 s59, s59, 1\n"
      "s_cbranch_scc1 .Ldfo%=\n"
      "s_waitcnt lgkmcnt(0)\n"
      "v_xor_b32 v240, s15, v240\n"
      "v_bitop3_b32 v240, s15, v241, v240 bitop3:0xbe\n"
      "v_bitop3_b32 v240, s15, v242, v240 bitop3:0xbe\n"
      "v_bitop3_b32 v240, s15, v243, v240 bitop3:0xbe\n"
      "v_bitop3_b32 v240, s15, v43, v240 bitop3:0xbe\n"
      "v_cmp_ne_u32 vcc, 0, v240\n"
      "s_nop 1\n"
      "s_cbranch_vccnz .Ldf%=\n"
      ".Ldfo%=:\n"
      "s_mov_b32 s60, 0\n"
      "buffer_load_dwordx4 v[128:131], v32, s[28:31], s60 offen nt\n"
      "s_mov_b32 s60, 2048\n"
      "buffer_load_dwordx4 v[132:135], v32, s[28:31], s60 offen nt\n"
      "s_mov_b32 s60, 4096\n"
      "buffer_load_dwordx4 v[136:139], v32, s[28:31], s60 offen nt\n"
      "s_mov_b32 s60, 6144\n"
      "buffer_load_dwordx4 v[140:143], v32, s[28:31], s60 offen nt\n"
      "s_mov_b32 s60, 8192\n"
      "buffer_load_dwordx4 v[144:147], v32, s[28:31], s60 offen nt\n"
      "s_mov_b32 s60, 10240\n"
      "buffer_load_dwordx4 v[148:151], v32, s[28:31], s60 offen nt\n"
      "s_mov_b32 s60, 12288\n"
      "buffer_load_dwordx4 v[152:155], v32, s[28:31], s60 offen nt\n"
      "s_mov_b32 s60, 14336\n"
      "buffer_load_dwordx4 v[156:159], v32, s[28:31], s60 offen nt\n"
      "s_waitcnt vmcnt(7)\n"
      "s_nop 0\n"
      "s_mov_b32 s60, 131072\n"
      "buffer_load_dwordx4 v[160:163], v32, s[28:31], s60 offen nt\n"
      "s_waitcnt vmcnt(7)\n"
      "s_nop 0\n"
      "s_mov_b32 s60, 133120\n"
      "buffer_load_dwordx4 v[164:167], v32, s[28:31], s60 offen nt\n"
      "v_perm_b32 v64, v132, v128, s62\n"
      "v_perm_b32 v68, v132, v128, s63\n"
      "v_perm_b32 v72, v133, v129, s62\n"
      "v_perm_b32 v76, v133, v129, s63\n"
      "v_perm_b32 v80, v134, v130, s62\n"
      "v_perm_b32 v84, v134, v130, s63\n"
      "v_perm_b32 v88, v135, v131, s62\n"
      "v_perm_b32 v92, v135, v131, s63\n"
      "s_waitcnt vmcnt(7)\n"
      "s_nop 0\n"
      "s_mov_b32 s60, 135168\n"
      "buffer_load_dwordx4 v[168:171], v32, s[28:31], s60 offen nt\n"
      "s_waitcnt vmcnt(7)\n"
      "s_nop 0\n"
      "s_mov_b32 s60, 137216\n"
      "buffer_load_dwordx4 v[172:175], v32, s[28:31], s60 offen nt\n"
      "v_perm_b32 v65, v140, v136, s62\n"
      "v_perm_b32 v69, v140, v136, s63\n"
      "v_perm_b32 v73, v141, v137, s62\n"
      "v_perm_b32 v77, v141, v137, s63\n"
      "v_perm_b32 v81, v142, v138, s62\n"
      "v_perm_b32 v85, v142, v138, s63\n"
      "v_perm_b32 v89, v143, v139, s62\n"
      "v_perm_b32 v93, v143, v139, s63\n"
      "s_waitcnt vmcnt(7)\n"
      "s_nop 0\n"
      "s_mov_b32 s60, 139264\n"
      "buffer_load_dwordx4 v[176:179], v32, s[28:31], s60 offen nt\n"
      "s_waitcnt vmcnt(7)\n"
      "s_nop 0\n"
      "s_mov_b32 s60, 141312\n"
      "buffer_load_dwordx4 v[180:183], v32, s[28:31], s60 offen nt\n"
      "v_perm_b32 v66, v148, v144, s62\n"
      "v_perm_b32 v70, v148, v144, s63\n"
      "v_perm_b32 v74, v149, v145, s62\n"
      "v_perm_b32 v78, v149, v145, s63\n"
      "v_perm_b32 v82, v150, v146, s62\n"
      "v_perm_b32 v86, v150, v146, s63\n"
      "v_perm_b32 v90, v151, v147, s62\n"
      "v_perm_b32 v94, v151, v147, s63\n"
      "s_waitcnt vmcnt(7)\n"
      "s_nop 0\n"
      "s_mov_b32 s60, 143360\n"
      "buffer_load_dwordx4 v[184:187], v32, s[28:31], s60 offen nt\n"
      "s_waitcnt vmcnt(7)\n"
      "s_nop 0\n"
      "s_mov_b32 s60, 145408\n"
      "buffer_load_dwordx4 v[188:191], v32, s[28:31], s60 offen nt\n"
      "v_perm_b32 v67, v156, v152, s62\n"
      "v_perm_b32 v71, v156, v152, s63\n"
      "v_perm_b32 v75, v157, v153, s62\n"
      "v_perm_b32 v79, v157, v153, s63\n"
      "v_perm_b32 v83, v158, v154, s62\n"
      "v_perm_b32 v87, v158, v154, s63\n"
      "v_perm_b32 v91, v159, v155, s62\n"
      "v_perm_b32 v95, v159, v155, s63\n"
      "s_waitcnt vmcnt(7)\n"
      "s_nop 0\n"
      "s_movk_i32 s59, 0x3fff\n"
      "v_add_u32 v41, 0xffffffc0, v41\n"
      ".Lde%=:\n"
      "ds_read_b32 v43, v41\n"
      "s_sub_u32 s59, s59, 1\n"
      "s_cbranch_scc1 .Ldeo%=\n"
      "s_waitcnt lgkmcnt(0)\n"
      "v_cmp_ne_u32 vcc, s15, v43\n"
      "s_nop 1\n"
      "s_cbranch_vccz .Ldeo%=\n"
      "s_sleep 2\n"
      "s_branch .Lde%=\n"
      ".Ldeo%=:\n"
      "s_cmp_lg_u32 s59, 0x3ffe\n"
      "s_cbranch_scc0 .Ldep%=\n"
      "s_sleep 6\n"
      ".Ldep%=:\n"
      "ds_read_b32 v43, v42 offset:64\n"
      "s_waitcnt lgkmcnt(0)\n"
      "v_readfirstlane_b32 s12, v43\n"
      "s_nop 0\n"
      ".Ldpc1_%=:\n"
      "s_memrealtime s[64:65]\n"
      "s_waitcnt lgkmcnt(0)\n"
      "s_sub_u32 s64, s64, s12\n"
      "s_cmp_ge_u32 s64, 640\n"
      "s_cbranch_scc1 .Ldpd1_%=\n"
      "s_sleep 1\n"
      "s_branch .Ldpc1_%=\n"
      ".Ldpd1_%=:\n"
      "buffer_load_dwordx4 v[224:227], v35, s[16:19], s46 offen sc1\n"
      "buffer_load_dwordx4 v[228:231], v35, s[16:19], s46 offen offset:2048 sc1\n"
      "s_waitcnt vmcnt(8)\n"
      "s_nop 0\n"
      "v_perm_b32 v96, v164, v160, s62\n"
      "v_perm_b32 v100, v164, v160, s63\n"
      "v_perm_b32 v104, v165, v161, s62\n"
      "v_perm_b32 v108, v165, v161, s63\n"
      "v_perm_b32 v112, v166, v162, s62\n"
      "v_perm_b32 v116, v166, v162, s63\n"
      "v_perm_b32 v120, v167, v163, s62\n"
      "v_perm_b32 v124, v167, v163, s63\n"
      "s_waitcnt vmcnt(7)\n"
      "s_nop 0\n"
      "s_waitcnt vmcnt(6)\n"
      "s_nop 0\n"
      "v_perm_b32 v97, v172, v168, s62\n"
      "v_perm_b32 v101, v172, v168, s63\n"
      "v_perm_b32 v105, v173, v169, s62\n"
      "v_perm_b32 v109, v173, v169, s63\n"
      "v_perm_b32 v113, v174, v170, s62\n"
      "v_perm_b32 v117, v174, v170, s63\n"
      "v_perm_b32 v121, v175, v171, s62\n"
      "v_perm_b32 v125, v175, v171, s63\n"
      "s_waitcnt vmcnt(5)\n"
      "s_nop 0\n"
      "s_waitcnt vmcnt(4)\n"
      "s_nop 0\n"
      "v_perm_b32 v98, v180, v176, s62\n"
      "v_perm_b32 v102, v180, v176, s63\n"
      "v_perm_b32 v106, v181, v177, s62\n"
      "v_perm_b32 v110, v181, v177, s63\n"
      "v_perm_b32 v114, v182, v178, s62\n"
      "v_perm_b32 v118, v182, v178, s63\n"
      "v_perm_b32 v122, v183, v179, s62\n"
      "v_perm_b32 v126, v183, v179, s63\n"
      "s_waitcnt vmcnt(3)\n"
      "s_nop 0\n"
      "s_waitcnt vmcnt(2)\n"
      "s_nop 0\n"
      "v_perm_b32 v99, v188, v184, s62\n"
      "v_perm_b32 v103, v188, v184, s63\n"
      "v_perm_b32 v107, v189, v185, s62\n"
      "v_perm_b32 v111, v189, v185, s63\n"
      "v_perm_b32 v115, v190, v186, s62\n"
      "v_perm_b32 v119, v190, v186, s63\n"
      "v_perm_b32 v123, v191, v187, s62\n"
      "v_perm_b32 v127, v191, v187, s63\n"
      "v_mov_b32 v137, s15\n"
      "v_mov_b32 v139, s15\n"
      "v_mov_b32 v141, s15\n"
      "v_mov_b32 v143, s15\n"
      "s_movk_i32 s59, 0x7fff\n"
      "s_waitcnt vmcnt(0)\n"
      "s_nop 0\n"
      ".Ldg%=:\n"
      "v_xor_b32 v232, s15, v225\n"
      "v_bitop3_b32 v232, s15, v227, v232 bitop3:0xbe\n"
      "v_bitop3_b32 v232, s15, v229, v232 bitop3:0xbe\n"
      "v_bitop3_b32 v232, s15, v231, v232 bitop3:0xbe\n"
      "v_cmp_ne_u32 vcc, 0, v232\n"
      "v_lshlrev_b32 v232, 16, v224\n"
      "v_and_b32 v233, 0xffff0000, v224\n"
      "v_lshlrev_b32 v234, 16, v226\n"
      "v_and_b32 v235, 0xffff0000, v226\n"
      "s_cbranch_vccnz .Ldgs%=\n"
      ".Ldgo%=:\n"
      "v_lshlrev_b32 v236, 16, v228\n"
      "v_and_b32 v237, 0xffff0000, v228\n"
      "v_lshlrev_b32 v238, 16, v230\n"
      "v_and_b32 v239, 0xffff0000, v230\n"
      "v_pk_mul_f32 v[232:233], v[232:233], v[236:237]\n"
      "v_pk_mul_f32 v[234:235], v[234:235], v[238:239]\n"
      "v_cvt_pk_bf16_f32 v232, v232, v233\n"
      "v_cvt_pk_bf16_f32 v233, v234, v235\n"
      "ds_write_b64 v36, v[232:233] \n"
      "ds_write_b32 v41, v137 offset:96\n"
      "ds_read_b128 v[240:243], v42 offset:32\n"
      "ds_read_b128 v[224:227], v37 \n"
      "ds_read_b128 v[228:231], v37 offset:1152\n"
      "s_movk_i32 s59, 0x7fff\n"
      ".Ldv%=:\n"
      "s_waitcnt lgkmcnt(2)\n"
      "v_xor_b32 v240, s15, v240\n"
      "v_bitop3_b32 v240, s15, v241, v240 bitop3:0xbe\n"
      "v_bitop3_b32 v240, s15, v242, v240 bitop3:0xbe\n"
      "v_bitop3_b32 v240, s15, v243, v240 bitop3:0xbe\n"
      "v_cmp_ne_u32 vcc, 0, v240\n"
      "s_nop 1\n"
      "s_cbranch_vccnz .Ldvs%=\n"
      ".Ldvo%=:\n"
      "s_waitcnt lgkmcnt(1)\n"
      "v_mfma_f32_16x16x32_bf16 v[192:195], v[64:67], v[224:227], 0\n"
      "s_waitcnt lgkmcnt(0)\n"
      "v_mfma_f32_16x16x32_bf16 v[192:195], v[96:99], v[228:231], v[192:195]\n"
      "v_mfma_f32_16x16x32_bf16 v[196:199], v[68:71], v[224:227], 0\n"
      "v_mfma_f32_16x16x32_bf16 v[196:199], v[100:103], v[228:231], v[196:199]\n"
      "v_mfma_f32_16x16x32_bf16 v[200:203], v[72:75], v[224:227], 0\n"
      "s_nop 4\n"
      "v_permlane32_swap_b32 v192, v193\n"
      "v_permlane32_swap_b32 v194, v195\n"
      "v_mfma_f32_16x16x32_bf16 v[200:203], v[104:107], v[228:231], v[200:203]\n"
      "v_mfma_f32_16x16x32_bf16 v[204:207], v[76:79], v[224:227], 0\n"
      "v_permlane32_swap_b32 v196, v197\n"
      "v_permlane32_swap_b32 v198, v199\n"
      "v_mfma_f32_16x16x32_bf16 v[204:207], v[108:111], v[228:231], v[204:207]\n"
      "v_add_f32_dpp v128, v193, v192 row_ror:8 row_mask:0xf bank_mask:0x3\n"
      "s_nop 1\n"
      "v_add_f32_dpp v128, v194, v195 row_ror:8 row_mask:0xf bank_mask:0xc\n"
      "v_mfma_f32_16x16x32_bf16 v[208:211], v[80:83], v[224:227], 0\n"
      "v_permlane32_swap_b32 v200, v201\n"
      "v_permlane32_swap_b32 v202, v203\n"
      "v_mfma_f32_16x16x32_bf16 v[208:211], v[112:115], v[228:231], v[208:211]\n"
      "v_add_f32_dpp v129, v197, v196 row_ror:8 row_mask:0xf bank_mask:0x3\n"
      "s_nop 1\n"
      "v_add_f32_dpp v129, v198, v199 row_ror:8 row_mask:0xf bank_mask:0xc\n"
      "v_mfma_f32_16x16x32_bf16 v[212:215], v[84:87], v[224:227], 0\n"
      "v_permlane32_swap_b32 v204, v205\n"
      "v_permlane32_swap_b32 v206, v207\n"
      "v_mfma_f32_16x16x32_bf16 v[212:215], v[116:119], v[228:231], v[212:215]\n"
      "v_add_f32_dpp v130, v201, v200 row_ror:8 row_mask:0xf bank_mask:0x3\n"
      "s_nop 1\n"
      "v_add_f32_dpp v130, v202, v203 row_ror:8 row_mask:0xf bank_mask:0xc\n"
      "v_mfma_f32_16x16x32_bf16 v[216:219], v[88:91], v[224:227], 0\n"
      "v_permlane32_swap_b32 v208, v209\n"
      "v_permlane32_swap_b32 v210, v211\n"
      "v_mfma_f32_16x16x32_bf16 v[216:219], v[120:123], v[228:231], v[216:219]\n"
      "v_add_f32_dpp v131, v205, v204 row_ror:8 row_mask:0xf bank_mask:0x3\n"
      "s_nop 1\n"
      "v_add_f32_dpp v131, v206, v207 row_ror:8 row_mask:0xf bank_mask:0xc\n"
      "v_mfma_f32_16x16x32_bf16 v[220:223], v[92:95], v[224:227], 0\n"
      "v_permlane32_swap_b32 v212, v213\n"
      "v_permlane32_swap_b32 v214, v215\n"
      "v_mfma_f32_16x16x32_bf16 v[220:223], v[124:127], v[228:231], v[220:223]\n"
      "v_add_f32_dpp v132, v209, v208 row_ror:8 row_mask:0xf bank_mask:0x3\n"
      "s_nop 1\n"
      "v_add_f32_dpp v132, v210, v211 row_ror:8 row_mask:0xf bank_mask:0xc\n"
      "v_cvt_pk_bf16_f32 v136, v128, v129\n"
      "v_cvt_pk_bf16_f32 v138, v130, v131\n"
      "buffer_store_dwordx4 v[136:139], v38, s[16:19], s47 offen offset:0 sc1\n"
      "v_permlane32_swap_b32 v216, v217\n"
      "v_permlane32_swap_b32 v218, v219\n"
      "v_add_f32_dpp v133, v213, v212 row_ror:8 row_mask:0xf bank_mask:0x3\n"
      "s_nop 1\n"
      "v_add_f32_dpp v133, v214, v215 row_ror:8 row_mask:0xf bank_mask:0xc\n"
      "v_permlane32_swap_b32 v220, v221\n"
      "v_permlane32_swap_b32 v222, v223\n"
      "v_add_f32_dpp v134, v217, v216 row_ror:8 row_mask:0xf bank_mask:0x3\n"
      "s_nop 1\n"
      "v_add_f32_dpp v134, v218, v219 row_ror:8 row_mask:0xf bank_mask:0xc\n"
      "v_add_f32_dpp v135, v221, v220 row_ror:8 row_mask:0xf bank_mask:0x3\n"
      "s_nop 1\n"
      "v_add_f32_dpp v135, v222, v223 row_ror:8 row_mask:0xf bank_mask:0xc\n"
      "v_cvt_pk_bf16_f32 v140, v132, v133\n"
      "v_cvt_pk_bf16_f32 v142, v134, v135\n"
      "buffer_store_dwordx4 v[140:143], v38, s[16:19], s47 offen offset:128 sc1\n"
      "s_endpgm\n"
      ".LZ%=:\n"
      "s_setprio 0\n"
      "v_and_b32 v48, 63, v0\n"
      "v_and_b32 v49, 7, v0\n"
      "v_lshlrev_b32 v108, 3, v49\n"
      "s_lshl4_add_u32 s35, s48, s49\n"
      "s_lshl_b32 s41, s35, 4\n"
      "s_and_b32 s43, s41, 0xffffff80\n"
      "s_add_u32 s43, s43, 67584\n"
      "buffer_load_dwordx2 v[104:105], v108, s[16:19], s43 offen sc1\n"
      "s_mul_i32 s44, s35, 7168\n"
      "s_add_u32 s44, s44, 4194304\n"
      "s_add_u32 s67, s44, 0x1000\n"
      "v_lshrrev_b32 v109, 5, v48\n"
      "v_and_b32 v110, 8, v48\n"
      "v_lshl_add_u32 v109, v109, 2, v110\n"
      "v_and_b32 v113, 16, v48\n"
      "s_movk_i32 s59, 0x7fff\n"
      "s_waitcnt vmcnt(0)\n"
      "s_movk_i32 s61, 0x7ff\n"
      ".Ldzh%=:\n"
      "v_cmp_ne_u32 vcc, s15, v105\n"
      "v_bitop3_b32 v110, v104, s61, v104 bitop3:0x0c\n"
      "v_not_b32 v112, v104\n"
      "s_cbranch_vccnz .Ldzs%=\n"
      ".Ldzo%=:\n"
      "v_lshl_add_u32 v110, v110, 11, v109\n"
      "v_lshlrev_b32 v108, 4, v48\n"
      "buffer_load_dword v111, v110, s[20:23], s41 offen\n"
      "v_lshl_or_b32 v113, v113, 26, v110\n"
      "v_ashrrev_i32 v112, 31, v112\n"
      "v_or_b32 v112, 0x80000000, v112\n"
      "v_xor_b32 v112, v112, v104\n"
      "v_and_b32 v112, 0xffff0000, v112\n"
      "v_mul_f32 v112, 0xbfb8aa3b, v112\n"
      "v_exp_f32 v112, v112\n"
      "s_nop 0\n"
      "v_add_f32 v112, 1.0, v112\n"
      "v_rcp_f32 v112, v112\n"
      "s_nop 0\n"
      "v_cvt_pk_bf16_f32 v112, v112, v112\n"
      "v_and_b32 v112, 0xffff0000, v112\n"
      ".Ldpc2_%=:\n"
      "s_memrealtime s[64:65]\n"
      "s_waitcnt lgkmcnt(0)\n"
      "s_sub_u32 s64, s64, s12\n"
      "s_cmp_ge_u32 s64, 815\n"
      "s_cbranch_scc1 .Ldpd2_%=\n"
      "s_sleep 1\n"
      "s_branch .Ldpc2_%=\n"
      ".Ldpd2_%=:\n"
      "buffer_load_dwordx4 v[64:67], v108, s[16:19], s44 offen offset:0 sc1\n"
      "buffer_load_dwordx4 v[68:71], v108, s[16:19], s44 offen offset:1024 sc1\n"
      "buffer_load_dwordx4 v[72:75], v108, s[16:19], s44 offen offset:2048 sc1\n"
      "buffer_load_dwordx4 v[76:79], v108, s[16:19], s44 offen offset:3072 sc1\n"
      "buffer_load_dwordx4 v[80:83], v108, s[16:19], s67 offen offset:0 sc1\n"
      "buffer_load_dwordx4 v[84:87], v108, s[16:19], s67 offen offset:1024 sc1\n"
      "buffer_load_dwordx4 v[88:91], v108, s[16:19], s67 offen offset:2048 sc1\n"
      "s_movk_i32 s59, 0x7fff\n"
      "s_mov_b32 s56, 0x3f80\n"
      "s_mov_b32 s57, 0x3f800000\n"
      "s_waitcnt vmcnt(7)\n"
      "v_lshlrev_b32 v106, 16, v111\n"
      "v_and_b32 v107, 0xffff0000, v111\n"
      ".Ldq%=:\n"
      "s_waitcnt vmcnt(6)\n"
      "v_xor_b32 v100, s15, v65\n"
      "v_xor_b32 v101, s15, v67\n"
      "v_lshlrev_b32 v96, 16, v64\n"
      "v_and_b32 v97, 0xffff0000, v64\n"
      "v_lshlrev_b32 v98, 16, v66\n"
      "v_and_b32 v99, 0xffff0000, v66\n"
      "s_waitcnt vmcnt(5)\n"
      "v_dot2_f32_bf16 v96, v68, s56, v96\n"
      "v_dot2_f32_bf16 v97, v68, s57, v97\n"
      "v_dot2_f32_bf16 v98, v70, s56, v98\n"
      "v_dot2_f32_bf16 v99, v70, s57, v99\n"
      "v_bitop3_b32 v100, s15, v69, v100 bitop3:0xbe\n"
      "v_bitop3_b32 v101, s15, v71, v101 bitop3:0xbe\n"
      "s_waitcnt vmcnt(4)\n"
      "v_dot2_f32_bf16 v96, v72, s56, v96\n"
      "v_dot2_f32_bf16 v97, v72, s57, v97\n"
      "v_dot2_f32_bf16 v98, v74, s56, v98\n"
      "v_dot2_f32_bf16 v99, v74, s57, v99\n"
      "v_bitop3_b32 v100, s15, v73, v100 bitop3:0xbe\n"
      "v_bitop3_b32 v101, s15, v75, v101 bitop3:0xbe\n"
      "s_waitcnt vmcnt(3)\n"
      "v_dot2_f32_bf16 v96, v76, s56, v96\n"
      "v_dot2_f32_bf16 v97, v76, s57, v97\n"
      "v_dot2_f32_bf16 v98, v78, s56, v98\n"
      "v_dot2_f32_bf16 v99, v78, s57, v99\n"
      "v_bitop3_b32 v100, s15, v77, v100 bitop3:0xbe\n"
      "v_bitop3_b32 v101, s15, v79, v101 bitop3:0xbe\n"
      "s_waitcnt vmcnt(2)\n"
      "v_dot2_f32_bf16 v96, v80, s56, v96\n"
      "v_dot2_f32_bf16 v97, v80, s57, v97\n"
      "v_dot2_f32_bf16 v98, v82, s56, v98\n"
      "v_dot2_f32_bf16 v99, v82, s57, v99\n"
      "v_bitop3_b32 v100, s15, v81, v100 bitop3:0xbe\n"
      "v_bitop3_b32 v101, s15, v83, v101 bitop3:0xbe\n"
      "s_waitcnt vmcnt(1)\n"
      "v_dot2_f32_bf16 v96, v84, s56, v96\n"
      "v_dot2_f32_bf16 v97, v84, s57, v97\n"
      "v_dot2_f32_bf16 v98, v86, s56, v98\n"
      "v_dot2_f32_bf16 v99, v86, s57, v99\n"
      "v_bitop3_b32 v100, s15, v85, v100 bitop3:0xbe\n"
      "v_bitop3_b32 v101, s15, v87, v101 bitop3:0xbe\n"
      "s_waitcnt vmcnt(0)\n"
      "v_dot2_f32_bf16 v96, v88, s56, v96\n"
      "v_dot2_f32_bf16 v97, v88, s57, v97\n"
      "v_dot2_f32_bf16 v98, v90, s56, v98\n"
      "v_dot2_f32_bf16 v99, v90, s57, v99\n"
      "v_bitop3_b32 v100, s15, v89, v100 bitop3:0xbe\n"
      "v_bitop3_b32 v101, s15, v91, v101 bitop3:0xbe\n"
      "v_or_b32 v100, v100, v101\n"
      "v_cmp_ne_u32 vcc, 0, v100\n"
      "v_permlane32_swap_b32 v96, v98\n"
      "v_permlane32_swap_b32 v97, v99\n"
      "s_cbranch_vccnz .Ldqs%=\n"
      ".Ldqo%=:\n"
      "v_add_f32 v96, v96, v98\n"
      "v_add_f32 v97, v97, v99\n"
      "v_mov_b32 v98, v96\n"
      "v_mov_b32 v99, v97\n"
      "s_nop 0\n"
      "v_permlane16_swap_b32 v96, v98\n"
      "v_permlane16_swap_b32 v97, v99\n"
      "v_add_f32 v96, v96, v98\n"
      "v_add_f32 v97, v97, v99\n"
      "v_cvt_pk_bf16_f32 v96, v96, v97\n"
      "v_and_b32 v97, 0xffff0000, v96\n"
      "v_lshlrev_b32 v96, 16, v96\n"
      "v_mul_f32 v97, v97, v112\n"
      "v_mul_f32 v96, v96, v112\n"
      "v_cvt_pk_bf16_f32 v96, v96, v97\n"
      "v_and_b32 v97, 0xffff0000, v96\n"
      "v_lshlrev_b32 v96, 16, v96\n"
      "v_add_f32 v97, v97, v107\n"
      "v_add_f32 v96, v96, v106\n"
      "v_cvt_pk_bf16_f32 v96, v96, v97\n"
      "buffer_atomic_pk_add_bf16 v96, v113, s[24:27], s41 offen\n"
      "s_endpgm\n"
      ".Ldgs%=:\n"
      "s_sleep 1\n"
      "s_sub_u32 s59, s59, 1\n"
      "s_cbranch_scc1 .Ldgo%=\n"
      "buffer_load_dwordx4 v[224:227], v35, s[16:19], s46 offen sc1\n"
      "buffer_load_dwordx4 v[228:231], v35, s[16:19], s46 offen offset:2048 sc1\n"
      "s_waitcnt vmcnt(0)\n"
      "s_branch .Ldg%=\n"
      ".Ldvs%=:\n"
      "s_sub_u32 s59, s59, 1\n"
      "s_cbranch_scc1 .Ldvo%=\n"
      "s_waitcnt lgkmcnt(0)\n"
      "ds_read_b128 v[240:243], v42 offset:32\n"
      "ds_read_b128 v[224:227], v37 \n"
      "ds_read_b128 v[228:231], v37 offset:1152\n"
      "s_waitcnt lgkmcnt(0)\n"
      "s_branch .Ldv%=\n"
      ".Ldzs%=:\n"
      "s_sleep 2\n"
      "s_sub_u32 s59, s59, 1\n"
      "s_cbranch_scc1 .Ldzo%=\n"
      "buffer_load_dwordx2 v[104:105], v108, s[16:19], s43 offen sc1\n"
      "s_waitcnt vmcnt(0)\n"
      "s_branch .Ldzh%=\n"
      ".Ldqs%=:\n"
      "s_sleep 1\n"
      "s_sub_u32 s59, s59, 1\n"
      "s_cbranch_scc1 .Ldqo%=\n"
      "v_cmp_ne_u32 vcc, s15, v65\n"
      "v_cmp_ne_u32 s[64:65], s15, v67\n"
      "s_nop 0\n"
      "s_or_b64 exec, vcc, s[64:65]\n"
      "s_cbranch_scc0 .Ldqk0%=\n"
      "buffer_load_dwordx4 v[64:67], v108, s[16:19], s44 offen offset:0 sc1\n"
      ".Ldqk0%=:\n"
      "s_mov_b64 exec, -1\n"
      "v_cmp_ne_u32 vcc, s15, v69\n"
      "v_cmp_ne_u32 s[64:65], s15, v71\n"
      "s_nop 0\n"
      "s_or_b64 exec, vcc, s[64:65]\n"
      "s_cbranch_scc0 .Ldqk1%=\n"
      "buffer_load_dwordx4 v[68:71], v108, s[16:19], s44 offen offset:1024 sc1\n"
      ".Ldqk1%=:\n"
      "s_mov_b64 exec, -1\n"
      "v_cmp_ne_u32 vcc, s15, v73\n"
      "v_cmp_ne_u32 s[64:65], s15, v75\n"
      "s_nop 0\n"
      "s_or_b64 exec, vcc, s[64:65]\n"
      "s_cbranch_scc0 .Ldqk2%=\n"
      "buffer_load_dwordx4 v[72:75], v108, s[16:19], s44 offen offset:2048 sc1\n"
      ".Ldqk2%=:\n"
      "s_mov_b64 exec, -1\n"
      "v_cmp_ne_u32 vcc, s15, v77\n"
      "v_cmp_ne_u32 s[64:65], s15, v79\n"
      "s_nop 0\n"
      "s_or_b64 exec, vcc, s[64:65]\n"
      "s_cbranch_scc0 .Ldqk3%=\n"
      "buffer_load_dwordx4 v[76:79], v108, s[16:19], s44 offen offset:3072 sc1\n"
      ".Ldqk3%=:\n"
      "s_mov_b64 exec, -1\n"
      "v_cmp_ne_u32 vcc, s15, v81\n"
      "v_cmp_ne_u32 s[64:65], s15, v83\n"
      "s_nop 0\n"
      "s_or_b64 exec, vcc, s[64:65]\n"
      "s_cbranch_scc0 .Ldqk4%=\n"
      "buffer_load_dwordx4 v[80:83], v108, s[16:19], s67 offen offset:0 sc1\n"
      ".Ldqk4%=:\n"
      "s_mov_b64 exec, -1\n"
      "v_cmp_ne_u32 vcc, s15, v85\n"
      "v_cmp_ne_u32 s[64:65], s15, v87\n"
      "s_nop 0\n"
      "s_or_b64 exec, vcc, s[64:65]\n"
      "s_cbranch_scc0 .Ldqk5%=\n"
      "buffer_load_dwordx4 v[84:87], v108, s[16:19], s67 offen offset:1024 sc1\n"
      ".Ldqk5%=:\n"
      "s_mov_b64 exec, -1\n"
      "v_cmp_ne_u32 vcc, s15, v89\n"
      "v_cmp_ne_u32 s[64:65], s15, v91\n"
      "s_nop 0\n"
      "s_or_b64 exec, vcc, s[64:65]\n"
      "s_cbranch_scc0 .Ldqk6%=\n"
      "buffer_load_dwordx4 v[88:91], v108, s[16:19], s67 offen offset:2048 sc1\n"
      ".Ldqk6%=:\n"
      "s_mov_b64 exec, -1\n"
      "s_waitcnt vmcnt(0)\n"
      "s_branch .Ldq%=\n"
      ".LT%=:\n"
      "v_mov_b32 v48, 0x22680\n"
      "ds_read_b32 v48, v48 \n"
      "s_waitcnt lgkmcnt(0)\n"
      "v_readfirstlane_b32 s12, v48\n"
      "v_mov_b32 v48, v43\n"
      "v_and_b32 v49, 7, v43\n"
      "v_lshrrev_b32 v50, 3, v43\n"
      "s_lshl_b32 s42, s34, 3\n"
      "s_add_u32 s42, s42, 2048\n"
      "s_lshl_b32 s43, s48, 8\n"
      "s_and_b32 s50, s49, 1\n"
      "s_lshl_b32 s50, s50, 7\n"
      "s_add_u32 s43, s43, s50\n"
      "s_add_u32 s43, s43, 65536\n"
      "s_mov_b32 s20, s64\n"
      "s_and_b32 s21, s65, 0xffff\n"
      "s_mov_b32 s22, 0x400000\n"
      "s_mov_b32 s23, 0x20000\n"
      "s_mov_b32 s28, s84\n"
      "s_and_b32 s29, s85, 0xffff\n"
      "s_mov_b32 s30, 0x700000\n"
      "s_mov_b32 s31, 0x20000\n"
      "s_sleep 0\n"
      ".LS%=:\n"
      "v_lshlrev_b32 v112, 4, v0\n"
      "v_lshlrev_b32 v113, 2, v0\n"
      "v_sub_u32 v113, 0xfff, v113\n"
      "v_lshlrev_b32 v115, 2, v48\n"
      "v_mov_b32 v116, 0\n"
      "ds_write_b32 v115, v116 offset:27136\n"
      "ds_write_b32 v116, v116 offset:26400\n"
      "v_add_u32 v115, 0x140, v115\n"
      "v_mov_b32 v114, v115\n"
      "s_lshl_b32 s50, s33, 2\n"
      "s_add_u32 s50, s50, 256\n"
      "v_writelane_b32 v114, s50, 63\n"
      "s_lshl_b32 s50, s33, 5\n"
      "v_lshl_add_u32 v117, v49, 2, s50\n"
      "v_lshlrev_b32 v118, 5, v50\n"
      "s_and_b32 s50, s49, 1\n"
      "s_lshl_b32 s50, s50, 10\n"
      "v_lshl_add_u32 v119, v50, 7, s50\n"
      "s_lshr_b32 s51, s33, 2\n"
      "s_mul_i32 s51, s51, 61440\n"
      "s_add_u32 s51, s51, 393216\n"
      "s_mov_b32 s52, 0x80008000\n"
      "s_mov_b32 s53, 0xffff0000\n"
      "s_movk_i32 s59, 0x7fff\n"
      ".Lt_pc4_%=:\n"
      "s_memrealtime s[64:65]\n"
      "s_waitcnt lgkmcnt(0)\n"
      "s_sub_u32 s64, s64, s12\n"
      "s_cmp_ge_u32 s64, 300\n"
      "s_cbranch_scc1 .Lt_pd4_%=\n"
      "s_sleep 1\n"
      "s_branch .Lt_pc4_%=\n"
      ".Lt_pd4_%=:\n"
      ".Lt_p%=:\n"
      "buffer_load_dwordx4 v[72:75], v112, s[16:19], s51 offen sc1\n"
      "s_waitcnt vmcnt(0)\n"
      "v_cmp_ne_u32 vcc, s15, v73\n"
      "v_cmp_ne_u32 s[54:55], s15, v75\n"
      "v_pk_ashrrev_i16 v81, 15, v72 op_sel_hi:[0,1]\n"
      "v_pk_ashrrev_i16 v82, 15, v74 op_sel_hi:[0,1]\n"
      "s_or_b64 vcc, vcc, s[54:55]\n"
      "s_cbranch_scc1 .Lt_ps%=\n"
      ".Lt_po%=:\n"
      "v_bitop3_b32 v81, v72, v81, s52 bitop3:0x1e\n"
      "v_bitop3_b32 v82, v74, v82, s52 bitop3:0x1e\n"
      "v_lshl_or_b32 v76, v81, 16, v113\n"
      "v_and_or_b32 v77, v81, s53, v113\n"
      "v_lshl_or_b32 v78, v82, 16, v113\n"
      "v_and_or_b32 v79, v82, s53, v113\n"
      "v_add_u32 v77, -1, v77\n"
      "v_add_u32 v78, -2, v78\n"
      "v_add_u32 v79, -3, v79\n"
      "v_max3_u32 v80, v76, v77, v78\n"
      "v_max_u32 v80, v80, v79\n"
      "v_mov_b32 v104, v80\n"
      "v_med3_u32 v105, v76, v77, v78\n"
      "v_max_u32_dpp v80, v80, v80 row_shr:1 row_mask:0xf bank_mask:0xf bound_ctrl:1\n"
      "v_max3_u32 v83, v76, v77, v78\n"
      "v_min3_u32 v106, v76, v77, v78\n"
      "v_max_u32_dpp v80, v80, v80 row_shr:2 row_mask:0xf bank_mask:0xf bound_ctrl:1\n"
      "v_med3_u32 v106, v105, v106, v79\n"
      "v_med3_u32 v105, v83, v105, v79\n"
      "v_max_u32_dpp v80, v80, v80 row_shr:4 row_mask:0xf bank_mask:0xf bound_ctrl:1\n"
      "v_mov_b32 v107, 0x100\n"
      "s_nop 0\n"
      "v_max_u32_dpp v80, v80, v80 row_shr:8 row_mask:0xf bank_mask:0xf bound_ctrl:1\n"
      "s_nop 1\n"
      "v_max_u32_dpp v80, v80, v80 row_bcast:15 row_mask:0xa bank_mask:0xf\n"
      "s_nop 1\n"
      "v_max_u32_dpp v80, v80, v80 row_bcast:31 row_mask:0xc bank_mask:0xf\n"
      "ds_write_b32 v114, v80 offset:26112\n"
      "s_waitcnt lgkmcnt(0)\n"
      "s_barrier\n"
      "ds_read_b128 v[92:95], v116 offset:26368\n"
      "ds_read_b128 v[96:99], v116 offset:26384\n"
      "s_waitcnt lgkmcnt(1)\n"
      "v_min3_u32 v81, v92, v93, v94\n"
      "s_waitcnt lgkmcnt(0)\n"
      "v_min3_u32 v82, v96, v97, v98\n"
      "v_min3_u32 v81, v81, v95, v99\n"
      "v_min_u32 v81, v81, v82\n"
      "v_cmp_ge_u32 s[62:63], v105, v81\n"
      "v_cmp_ge_u32 vcc, v106, v81\n"
      "v_cmp_ge_u32 s[60:61], v104, v81\n"
      "v_cndmask_b32 v82, 1, 2, s[62:63]\n"
      "v_cndmask_b32 v82, v82, v107, vcc\n"
      "s_mov_b64 exec, s[60:61]\n"
      "ds_add_rtn_u32 v83, v116, v82 offset:26400\n"
      "s_waitcnt lgkmcnt(0)\n"
      "v_min_u32 v83, 0x50, v83\n"
      "v_lshlrev_b32 v83, 2, v83\n"
      "ds_write_b32 v83, v104 offset:27136\n"
      "s_mov_b64 exec, s[62:63]\n"
      "ds_write_b32 v83, v105 offset:27140\n"
      "s_mov_b64 exec, -1\n"
      "s_waitcnt lgkmcnt(0)\n"
      "s_barrier\n"
      "s_memrealtime s[56:57]\n"
      "ds_read_b32 v81, v116 offset:26400\n"
      ".Lt_r%=:\n"
      "ds_read_b32 v100, v117 offset:27136\n"
      "ds_read_b128 v[92:95], v118 offset:27136\n"
      "ds_read_b128 v[96:99], v118 offset:27152\n"
      "v_mov_b32 v101, s15\n"
      "v_mov_b32 v89, 0\n"
      "s_waitcnt lgkmcnt(3)\n"
      "v_cmp_lt_u32 vcc, 64, v81\n"
      "s_movk_i32 s50, 0x7ff\n"
      "s_cbranch_vccnz .Lt_m%=\n"
      "s_waitcnt lgkmcnt(1)\n"
      "v_cmp_gt_u32 s[60:61], v92, v100\n"
      "v_cmp_gt_u32 s[62:63], v93, v100\n"
      "v_cmp_gt_u32 s[64:65], v94, v100\n"
      "v_cmp_gt_u32 s[66:67], v95, v100\n"
      "s_waitcnt lgkmcnt(0)\n"
      "v_cmp_gt_u32 s[68:69], v96, v100\n"
      "v_cmp_gt_u32 s[70:71], v97, v100\n"
      "v_cmp_gt_u32 s[72:73], v98, v100\n"
      "v_cmp_gt_u32 s[74:75], v99, v100\n"
      "v_bitop3_b32 v81, v100, s50, v100 bitop3:0x0c\n"
      "v_addc_co_u32 v89, vcc, 0, v89, s[60:61]\n"
      "v_addc_co_u32 v89, vcc, 0, v89, s[62:63]\n"
      "v_addc_co_u32 v89, vcc, 0, v89, s[64:65]\n"
      "v_addc_co_u32 v89, vcc, 0, v89, s[66:67]\n"
      "v_addc_co_u32 v89, vcc, 0, v89, s[68:69]\n"
      "v_addc_co_u32 v89, vcc, 0, v89, s[70:71]\n"
      "v_addc_co_u32 v89, vcc, 0, v89, s[72:73]\n"
      "v_addc_co_u32 v89, vcc, 0, v89, s[74:75]\n"
      "v_lshl_add_u32 v82, v81, 11, v119\n"
      "v_lshrrev_b32 v86, 9, v81\n"
      "v_mul_u32_u24 v86, 0xf000, v86\n"
      "v_lshl_add_u32 v81, v81, 3, v86\n"
      "v_add_u32_dpp v89, v89, v89 row_ror:8 row_mask:0xf bank_mask:0xf\n"
      "v_mov_b32 v83, v89\n"
      "s_nop 1\n"
      "v_permlane16_swap_b32 v89, v83\n"
      "v_add_u32 v89, v89, v83\n"
      "v_mov_b32 v83, v89\n"
      "s_nop 1\n"
      "v_permlane32_swap_b32 v89, v83\n"
      "v_add_u32 v89, v89, v83\n"
      "v_cmp_gt_u32 vcc, 8, v89\n"
      "v_lshlrev_b32 v89, 3, v89\n"
      "s_mov_b32 s51, 131072\n"
      "s_cbranch_vccz .Lt_n%=\n"
      "s_and_b64 exec, vcc, 0xff\n"
      "ds_write_b32 v89, v100 offset:26752\n"
      "buffer_load_dword v84, v81, s[16:19], s51 offen sc1\n"
      "s_mov_b64 exec, vcc\n"
      "buffer_load_dword v85, v82, s[20:23], 0 offen\n"
      "s_mov_b64 exec, -1\n"
      ".Lt_n%=:\n"
      "v_lshlrev_b32 v89, 3, v49\n"
      "s_waitcnt lgkmcnt(0)\n"
      "s_barrier\n"
      "s_sub_u32 s12, s56, 390\n"
      "s_movk_i32 s57, 64\n"
      "s_cmp_lg_u32 s33, 0\n"
      "s_cbranch_scc1 .Lt_a%=\n"
      "ds_read_b32 v100, v89 offset:26752\n"
      "v_writelane_b32 v89, s57, 8\n"
      "s_waitcnt lgkmcnt(0)\n"
      "v_writelane_b32 v100, s12, 8\n"
      "s_mov_b64 exec, 0x1ff\n"
      "buffer_store_dwordx2 v[100:101], v89, s[16:19], s43 offen sc1\n"
      "s_mov_b64 exec, -1\n"
      "s_nop 1\n"
      "v_mov_b32 v96, s15\n"
      "v_mov_b32 v97, s15\n"
      "buffer_store_dwordx2 v[96:97], off, s[16:19], s42 sc1\n"
      ".Lt_a%=:\n"
      "s_cmp_lg_u32 s33, 1\n"
      "s_cbranch_scc1 .Lt_c%=\n"
      "ds_read_b32 v100, v89 offset:26752\n"
      "v_lshlrev_b32 v81, 3, v50\n"
      "ds_read_b32 v81, v81 offset:26752\n"
      "v_mov_b32 v92, 0\n"
      "v_mov_b32 v93, 0\n"
      "v_mov_b32 v94, 0\n"
      "v_mov_b32 v95, 0\n"
      "s_and_b32 s51, s43, 0x7ff\n"
      "v_lshlrev_b32 v82, 4, v49\n"
      "s_waitcnt lgkmcnt(0)\n"
      "v_bitop3_b32 v81, v81, s50, v81 bitop3:0x0c\n"
      "v_lshl_add_u32 v81, v81, 11, v82\n"
      "buffer_store_dwordx4 v[92:95], v81, s[24:27], s51 offen sc1\n"
      "s_waitcnt vmcnt(0)\n"
      "s_mov_b64 exec, 0xff\n"
      "buffer_store_dwordx2 v[100:101], v89, s[16:19], s43 offen offset:2048 sc1\n"
      "s_mov_b64 exec, -1\n"
      "s_nop 1\n"
      ".Lt_c%=:\n"
      "s_cmp_lg_u32 s33, 3\n"
      "s_cbranch_scc1 .Lt_z%=\n"
      "s_sleep 55\n"
      "s_branch .LZ%=\n"
      ".Lt_z%=:\n"
      "s_cmp_lg_u32 s34, 127\n"
      "s_cbranch_scc1 .Lt_e%=\n"
      "s_cmp_lg_u32 s33, 0\n"
      "s_cbranch_scc1 .Lt_e%=\n"
      "s_movk_i32 s51, 2048\n"
      "s_movk_i32 s59, 0x7fff\n"
      ".Lt_k%=:\n"
      "s_sleep 16\n"
      "buffer_load_dwordx4 v[92:95], v112, s[16:19], s51 offen sc1\n"
      "buffer_load_dwordx4 v[96:99], v112, s[16:19], s51 offen offset:1024 sc1\n"
      "s_sub_u32 s59, s59, 1\n"
      "s_cbranch_scc1 .Lt_kb%=\n"
      "s_waitcnt vmcnt(0)\n"
      "v_xor_b32 v86, s15, v93\n"
      "v_bitop3_b32 v86, s15, v95, v86 bitop3:0xbe\n"
      "v_bitop3_b32 v86, s15, v97, v86 bitop3:0xbe\n"
      "v_bitop3_b32 v86, s15, v99, v86 bitop3:0xbe\n"
      "v_cmp_ne_u32 vcc, 0, v86\n"
      "s_nop 1\n"
      "s_cbranch_vccnz .Lt_k%=\n"
      ".Lt_kb%=:\n"
      "buffer_store_dword v101, off, s[16:19], 0 sc1\n"
      ".Lt_e%=:\n"
      "s_waitcnt vmcnt(0)\n"
      "s_endpgm\n"
      ".Lt_ps%=:\n"
      "s_sleep 1\n"
      "s_sub_u32 s59, s59, 1\n"
      "s_cbranch_scc0 .Lt_p%=\n"
      "s_branch .Lt_po%=\n"
      ".Lt_m%=:\n"
      "s_waitcnt lgkmcnt(0)\n"
      "s_barrier\n"
      "s_mov_b32 s59, 0\n"
      "v_mov_b32 v90, 0\n"
      ".Lt_ml%=:\n"
      "s_nop 1\n"
      "v_max3_u32 v80, v76, v77, v78\n"
      "v_max_u32 v80, v80, v79\n"
      "s_nop 1\n"
      "v_max_u32_dpp v80, v80, v80 row_shr:1 row_mask:0xf bank_mask:0xf bound_ctrl:1\n"
      "s_nop 1\n"
      "v_max_u32_dpp v80, v80, v80 row_shr:2 row_mask:0xf bank_mask:0xf bound_ctrl:1\n"
      "s_nop 1\n"
      "v_max_u32_dpp v80, v80, v80 row_shr:4 row_mask:0xf bank_mask:0xf bound_ctrl:1\n"
      "s_nop 1\n"
      "v_max_u32_dpp v80, v80, v80 row_shr:8 row_mask:0xf bank_mask:0xf bound_ctrl:1\n"
      "s_nop 1\n"
      "v_max_u32_dpp v80, v80, v80 row_bcast:15 row_mask:0xa bank_mask:0xf\n"
      "s_nop 1\n"
      "v_max_u32_dpp v80, v80, v80 row_bcast:31 row_mask:0xc bank_mask:0xf\n"
      "s_nop 0\n"
      "v_readlane_b32 s50, v80, 63\n"
      "s_mov_b32 m0, s59\n"
      "s_nop 0\n"
      "v_cmp_ne_u32 vcc, s50, v76\n"
      "s_nop 1\n"
      "v_cndmask_b32 v76, 0, v76, vcc\n"
      "v_cmp_ne_u32 vcc, s50, v77\n"
      "s_nop 1\n"
      "v_cndmask_b32 v77, 0, v77, vcc\n"
      "v_cmp_ne_u32 vcc, s50, v78\n"
      "s_nop 1\n"
      "v_cndmask_b32 v78, 0, v78, vcc\n"
      "v_cmp_ne_u32 vcc, s50, v79\n"
      "s_nop 1\n"
      "v_cndmask_b32 v79, 0, v79, vcc\n"
      "v_writelane_b32 v90, s50, m0\n"
      "s_add_u32 s59, s59, 1\n"
      "s_cmp_lt_u32 s59, 8\n"
      "s_cbranch_scc1 .Lt_ml%=\n"
      "s_mov_b64 exec, 0xff\n"
      "ds_write_b32 v117, v90 offset:27136\n"
      "s_mov_b64 exec, -1\n"
      "v_mov_b32 v81, 0\n"
      "s_waitcnt lgkmcnt(0)\n"
      "s_barrier\n"
      "s_branch .Lt_r%=\n"
      ".pushsection .rodata\n.p2align 7\n.Ltab%=:\n"
      ".long 0x0, 0x800023a, 0x6a006e0, 0x3a, 0x1100000, 0x65e022a, 0x5fe063e, 0x100878, 0x2200000, 0x6fe021a, 0x69e06de, 0x20003c, 0x3300000, 0x65c020a, 0x5fc063c, 0x30087a\n"
      ".long 0x4400110, 0x6fc01fa, 0x69c06dc, 0x440003e, 0x5500110, 0x65a01ea, 0x5fa063a, 0x450087c, 0x6600110, 0x6fa01da, 0x69a06da, 0x4600040, 0x7700110, 0x65801ca, 0x5f80638, 0x470087e\n"
      ".long 0x8800220, 0x6f801ba, 0x69806d8, 0x4a00580, 0x8800220, 0x65601aa, 0x5f60636, 0x4b00dbe, 0x8800220, 0x6f6019a, 0x69606d6, 0x4c00680, 0xbb00220, 0x654018a, 0x5f40634, 0x4d00ebe\n"
      ".long 0xcc00330, 0x6f4017a, 0x69406d4, 0x5000810, 0xcc00330, 0x652016a, 0x5f20632, 0x510104e, 0xcc00330, 0x6f2015a, 0x69206d2, 0x5200810, 0xff00330, 0x650014a, 0x5f00630, 0x530104e\n"
      ".long 0x11000440, 0x6f0013a, 0x69006d0, 0x5600810, 0x12100440, 0x64e012a, 0x5ee062e, 0x570104e, 0x13200440, 0x6ee011a, 0x68e06ce, 0x5800810, 0x14300440, 0x64c010a, 0x5ec062c, 0x590104e\n"
      ".long 0x15400550, 0x6ec00fa, 0x68c06cc, 0x5c00810, 0x16500550, 0x64a00ea, 0x5ea062a, 0x5d0104e, 0x17600550, 0x6ea00da, 0x68a06ca, 0x5e00810, 0x18700550, 0x64800ca, 0x5e80628, 0x5f0104e\n"
      ".long 0x9900660, 0x6e800ba, 0x68806c8, 0x6600810, 0x9900660, 0x64600aa, 0x5e60626, 0x670104e, 0x9900660, 0x6e6009a, 0x68606c6, 0x6800810, 0x9900660, 0x644008a, 0x5e40624, 0x690104e\n"
      ".long 0x9900770, 0x6e4007a, 0x68406c4, 0x7000810, 0x9900770, 0x642006a, 0x5e20622, 0x710104e, 0x9900770, 0x6e2005a, 0x68206c2, 0x7200810, 0x9900770, 0x640004a, 0x5e00620, 0x730104e\n"
      ".long 0x100010, 0x8000232, 0x47e05a0, 0x8400810, 0x1200010, 0x4fe0222, 0x53e04de, 0x850104e, 0x2300010, 0x5be0212, 0x47c059e, 0x8600810, 0x3400010, 0x4fc0202, 0x53c04dc, 0x870104e\n"
      ".long 0x4500120, 0x5bc01f2, 0x47a059c, 0xc700810, 0x5600120, 0x4fa01e2, 0x53a04da, 0xc80104e, 0x6700120, 0x5ba01d2, 0x478059a, 0xc900810, 0x7800120, 0x4f801c2, 0x53804d8, 0xca0104e\n"
      ".long 0x8900230, 0x5b801b2, 0x4760598, 0xcd00810, 0x8900230, 0x4f601a2, 0x53604d6, 0xce0104e, 0x8900230, 0x5b60192, 0x4740596, 0xcf00810, 0xbc00230, 0x4f40182, 0x53404d4, 0xd00104e\n"
      ".long 0xcd00340, 0x5b40172, 0x4720594, 0xd300810, 0xcd00340, 0x4f20162, 0x53204d2, 0xd40104e, 0xcd00340, 0x5b20152, 0x4700592, 0xd500810, 0x10000340, 0x4f00142, 0x53004d0, 0xd60104e\n"
      ".long 0x11100450, 0x5b00132, 0x46e0590, 0xd900810, 0x12200450, 0x4ee0122, 0x52e04ce, 0xda0104e, 0x13300450, 0x5ae0112, 0x46c058e, 0xdb00810, 0x14400450, 0x4ec0102, 0x52c04cc, 0xdc0104e\n"
      ".long 0x15500560, 0x5ac00f2, 0x46a058c, 0xdf00810, 0x16600560, 0x4ea00e2, 0x52a04ca, 0xe00104e, 0x17700560, 0x5aa00d2, 0x468058a, 0xe100810, 0x18800560, 0x4e800c2, 0x52804c8, 0xe20104e\n"
      ".long 0x9a00670, 0x5a800b2, 0x4660588, 0xe900810, 0x9a00670, 0x4e600a2, 0x52604c6, 0xea0104e, 0x9a00670, 0x5a60092, 0x4640586, 0xeb00810, 0x9a00670, 0x4e40082, 0x52404c4, 0xec0104e\n"
      ".long 0x9a00780, 0x5a40072, 0x4620584, 0xf300810, 0x9a00780, 0x4e20062, 0x52204c2, 0xf40104e, 0x9a00780, 0x5a20052, 0x4600582, 0xf500810, 0x9a00780, 0x4e00042, 0x52004c0, 0xf60104e\n"
      ".long 0xc9c085c, 0x0, 0x0, 0x880, 0x45c001c, 0x200010, 0x40008, 0x800990, 0xc980858, 0x400020, 0x80010, 0x8000aa0, 0x4580018, 0x600030, 0xc0018, 0x8800bb0\n"
      ".long 0xc940854, 0x800040, 0x102240, 0x10000880, 0x4540014, 0xa00050, 0x142248, 0x10800990, 0xc900850, 0xc00060, 0x182250, 0x18000aa0, 0x4500010, 0xe00070, 0x1c2258, 0x18800bb0\n"
      ".long 0xc8c084c, 0x1000080, 0xc204480, 0x20000880, 0x44c000c, 0x1200090, 0xc244488, 0x20800990, 0xc880848, 0x14000a0, 0xc284490, 0x28000aa0, 0x4480008, 0x16000b0, 0xc2c4498, 0x28800bb0\n"
      ".long 0xc840844, 0x18000c0, 0xc3066c0, 0x30000880, 0x4440004, 0x1a000d0, 0xc3466c8, 0x30800990, 0xc800840, 0x1c000e0, 0xc3866d0, 0x38000aa0, 0x4400000, 0x1e000f0, 0xc3c66d8, 0x38800bb0\n"
      ".long 0xca4086c, 0x100004, 0x20, 0x40000880, 0x464002c, 0x300014, 0x40028, 0x40800990, 0xca00868, 0x500024, 0x80030, 0x48000aa0, 0x4600028, 0x700034, 0xc0038, 0x48800bb0\n"
      ".long 0xc9c0864, 0x900044, 0x102260, 0x50000880, 0x45c0024, 0xb00054, 0x142268, 0x50800990, 0xc980860, 0xd00064, 0x182270, 0x58000aa0, 0x4580020, 0xf00074, 0x1c2278, 0x58800bb0\n"
      ".long 0xc94085c, 0x1100084, 0x82044a0, 0x60000880, 0x454001c, 0x1300094, 0x82444a8, 0x60800990, 0xc900858, 0x15000a4, 0x82844b0, 0x68000aa0, 0x4500018, 0x17000b4, 0x82c44b8, 0x68800bb0\n"
      ".long 0xc8c0854, 0x19000c4, 0x83066e0, 0x70000880, 0x44c0014, 0x1b000d4, 0x83466e8, 0x70800990, 0xc880850, 0x1d000e4, 0x83866f0, 0x78000aa0, 0x4480010, 0x1f000f4, 0x83c66f8, 0x78800bb0\n"
      ".long 0xcac087c, 0x200008, 0x800, 0x890, 0x46c003c, 0x400018, 0x40808, 0x8009a0, 0xca80878, 0x600028, 0x80810, 0x8000ab0, 0x4680038, 0x800038, 0xc0818, 0x8800bc0\n"
      ".long 0xca40874, 0xa00048, 0x102a40, 0x10000890, 0x4640034, 0xc00058, 0x142a48, 0x108009a0, 0xca00870, 0xe00068, 0x182a50, 0x18000ab0, 0x4600030, 0x1000078, 0x1c2a58, 0x18800bc0\n"
      ".long 0xc9c086c, 0x1200088, 0xc204c80, 0x20000890, 0x45c002c, 0x1400098, 0xc244c88, 0x208009a0, 0xc980868, 0x16000a8, 0xc284c90, 0x28000ab0, 0x4580028, 0x18000b8, 0xc2c4c98, 0x28800bc0\n"
      ".long 0xc940864, 0x1a000c8, 0xc306ec0, 0x30000890, 0x4540024, 0x1c000d8, 0xc346ec8, 0x308009a0, 0xc900860, 0x1e000e8, 0xc386ed0, 0x38000ab0, 0x4500020, 0x20000f8, 0xc3c6ed8, 0x38800bc0\n"
      ".long 0xcb4088c, 0x30000c, 0x820, 0x40000890, 0x474004c, 0x50001c, 0x40828, 0x408009a0, 0xcb00888, 0x70002c, 0x80830, 0x48000ab0, 0x4700048, 0x90003c, 0xc0838, 0x48800bc0\n"
      ".long 0xcac0884, 0xb0004c, 0x102a60, 0x50000890, 0x46c0044, 0xd0005c, 0x142a68, 0x508009a0, 0xca80880, 0xf0006c, 0x182a70, 0x58000ab0, 0x4680040, 0x110007c, 0x1c2a78, 0x58800bc0\n"
      ".long 0xca4087c, 0x130008c, 0x8204ca0, 0x60000890, 0x464003c, 0x150009c, 0x8244ca8, 0x608009a0, 0xca00878, 0x17000ac, 0x8284cb0, 0x68000ab0, 0x4600038, 0x19000bc, 0x82c4cb8, 0x68800bc0\n"
      ".long 0xc9c0874, 0x1b000cc, 0x8306ee0, 0x70000890, 0x45c0034, 0x1d000dc, 0x8346ee8, 0x708009a0, 0xc980870, 0x1f000ec, 0x8386ef0, 0x78000ab0, 0x4580030, 0x21000fc, 0x83c6ef8, 0x78800bc0\n"
      ".long 0x0, 0x0, 0x0, 0x0, 0x0, 0x0, 0x0, 0x0, 0x0, 0x0, 0x0, 0x0, 0x0, 0x0, 0x0, 0x0\n"
      ".long 0x0, 0x0, 0x0, 0x0, 0x0, 0x0, 0x0, 0x0, 0x0, 0x0, 0x0, 0x0, 0x0, 0x0, 0x0, 0x0\n"
      ".long 0x0, 0x0, 0x0, 0x0, 0x0, 0x0, 0x0, 0x0, 0x0, 0x0, 0x0, 0x0, 0x0, 0x0, 0x0, 0x0\n"
      ".long 0x0, 0x0, 0x0, 0x0, 0x0, 0x0, 0x0, 0x0, 0x0, 0x0, 0x0, 0x0, 0x0, 0x0, 0x0, 0x0\n"
      ".long 0x0, 0x0, 0xffffffff, 0x0, 0x0, 0x0, 0xffff0000, 0xffff, 0x0, 0x0, 0x0, 0xffffffff, 0x0, 0x0, 0x0, 0xffff0000\n"
      ".long 0x0, 0x0, 0x0, 0x0, 0x0, 0x0, 0x0, 0x0, 0x0, 0x0, 0x0, 0x0, 0x0, 0x0, 0x0, 0x0\n"
      ".long 0x0, 0x0, 0x0, 0x0, 0x0, 0x0, 0x0, 0x0, 0x0, 0x0, 0x0, 0x0, 0x0, 0x0, 0x0, 0x0\n"
      ".long 0x0, 0x0, 0x0, 0x0, 0x0, 0x0, 0x0, 0x0, 0x0, 0x0, 0x0, 0x0, 0x0, 0x0, 0x0, 0x0\n"
      ".long 0x0, 0x0, 0x0, 0x0, 0x0, 0x0, 0x0, 0x0, 0x0, 0x0, 0x0, 0x0, 0xffff, 0x0, 0x0, 0x0\n"
      ".long 0xffffffff, 0x0, 0x0, 0x0, 0xffff0000, 0xffff, 0x0, 0x0, 0x0, 0xffffffff, 0x0, 0x0, 0x0, 0xffff0000, 0xffff, 0x0\n"
      ".long 0x0, 0x0, 0xffffffff, 0x0, 0x0, 0x0, 0xffff0000, 0xffff, 0x0, 0x0, 0x0, 0xffffffff, 0x0, 0x0, 0x0, 0xffff0000\n"
      ".long 0x0, 0x0, 0x0, 0x0, 0x0, 0x0, 0x0, 0x0, 0x0, 0x0, 0x0, 0x0, 0x0, 0x0, 0x0, 0x0\n"
      ".long 0x0, 0x0, 0x0, 0x0, 0x0, 0x0, 0x0, 0x0, 0x0, 0x0, 0x0, 0x0, 0x0, 0x0, 0x0, 0x0\n"
      ".long 0x0, 0x0, 0x0, 0x0, 0x0, 0x0, 0x0, 0x0, 0x0, 0x0, 0x0, 0x0, 0x0, 0x0, 0x0, 0x0\n"
      ".long 0x0, 0x0, 0x0, 0x0, 0x0, 0x0, 0x0, 0x0, 0x0, 0x0, 0x0, 0x0, 0xffff, 0x0, 0x0, 0x0\n"
      ".long 0xffffffff, 0x0, 0x0, 0x0, 0xffff0000, 0xffff, 0x0, 0x0, 0x0, 0xffffffff, 0x0, 0x0, 0x0, 0xffff0000, 0xffff, 0x0\n"
      ".popsection\n"
      ".LEnd%=:\n"
      : "={s[80:95]}"(sts), "={s[96:99]}"(st2)
      : [ka] "s"(__builtin_amdgcn_kernarg_segment_ptr()), [bid] "s"(blockIdx.x), "{v0}"(threadIdx.x), "s"((u32)(uintptr_t)sm)
      : "v8", "v9", "v10", "v11", "v12", "v13", "v14", "v15", "v16", "v17", "v18", "v19", "v20", "v21", "v22", "v23", "v24", "v25", "v26", "v27",
        "v28", "v29", "v30", "v31", "v32", "v33", "v34", "v35", "v36", "v37", "v38", "v39", "v40", "v41", "v42", "v43", "v44", "v45", "v46", "v47",
        "v48", "v49", "v50", "v51", "v52", "v53", "v54", "v55", "v56", "v57", "v58", "v59", "v60", "v61", "v62", "v63", "v64", "v65", "v66", "v67",
        "v68", "v69", "v70", "v71", "v72", "v73", "v74", "v75", "v76", "v77", "v78", "v79", "v80", "v81", "v82", "v83", "v84", "v85", "v86", "v87",
        "v88", "v89", "v90", "v91", "v92", "v93", "v94", "v95", "v96", "v97", "v98", "v99", "v100", "v101", "v102", "v103", "v104", "v105", "v106", "v107",
        "v108", "v109", "v110", "v111", "v112", "v113", "v114", "v115", "v116", "v117", "v118", "v119", "v120", "v121", "v122", "v123", "v124", "v125", "v126", "v127",
        "v128", "v129", "v130", "v131", "v132", "v133", "v134", "v135", "v136", "v137", "v138", "v139", "v140", "v141", "v142", "v143", "v144", "v145", "v146", "v147",
        "v148", "v149", "v150", "v151", "v152", "v153", "v154", "v155", "v156", "v157", "v158", "v159", "v160", "v161", "v162", "v163", "v164", "v165", "v166", "v167",
        "v168", "v169", "v170", "v171", "v172", "v173", "v174", "v175", "v176", "v177", "v178", "v179", "v180", "v181", "v182", "v183", "v184", "v185", "v186", "v187",
        "v188", "v189", "v190", "v191", "v192", "v193", "v194", "v195", "v196", "v197", "v198", "v199", "v200", "v201", "v202", "v203", "v204", "v205", "v206", "v207",
        "v208", "v209", "v210", "v211", "v212", "v213", "v214", "v215", "v216", "v217", "v218", "v219", "v220", "v221", "v222", "v223", "v224", "v225", "v226", "v227",
        "v228", "v229", "v230", "v231", "v232", "v233", "v234", "v235", "v236", "v237", "v238", "v239", "v240", "v241", "v242", "v243", "v244", "v245", "v246", "v247",
        "v248", "v249", "v250", "v251", "v252", "v253", "v254", "v255", "s12", "s13", "s14", "s15", "s16", "s17", "s18", "s19", "s20", "s21", "s22", "s23",
        "s24", "s25", "s26", "s27", "s28", "s29", "s30", "s31", "s33", "s34", "s35", "s36", "s37", "s38", "s39", "s40", "s41", "s42", "s43", "s44",
        "s45", "s46", "s47", "s48", "s49", "s50", "s51", "s52", "s53", "s54", "s55", "s56", "s57", "s58", "s59", "s60", "s61", "s62", "s63", "s64",
        "s65", "s66", "s67", "s68", "s69", "s70", "s71", "s72", "s73", "s74", "s75", "s76", "s77", "s78", "s79",
        "vcc", "scc", "memory");
  const int b = blockIdx.x;
  GETST();
  STAMP(11);
  PEND();
}

extern "C" void kernel_launch(void* const* d_in, const int* in_sizes, int n_in, void* d_out, int out_size,
                              void* d_ws, size_t ws_size, hipStream_t stream) {
  const u16* const* in = (const u16* const*)d_in;
  fused<<<256, 512, 0, stream>>>(in[0], in[1], in[2], in[3], in[4], in[5], in[8], in[6], in[9], in[10], in[11],
                                 (u16*)d_out, (char*)d_ws);
}
```

```cpp
#include <hip/hip_runtime.h>
#include <stdint.h>

typedef unsigned short u16;
typedef unsigned int u32;
typedef unsigned long long u64;
typedef __attribute__((ext_vector_type(4))) u32 u32x4;
typedef __attribute__((ext_vector_type(16))) u32 u32x16;
#define AS3 __attribute__((address_space(3)))

#define STAMP(I)
#define PEND()
#define GETST()
constexpr int SPIN_MAX = 1 << 16;
constexpr int UNUSED = 0;

__global__ __launch_bounds__(512, 2) void fused(const u16* __restrict__ x, const u16* __restrict__ lar,
    const u16* __restrict__ aim, const u16* __restrict__ cre, const u16* __restrict__ cim,
    const u16* __restrict__ ldt, const u16* __restrict__ wr, const u16* __restrict__ dsk,
    const u16* __restrict__ wg, const u16* __restrict__ wu, const u16* __restrict__ wd,
    u16* __restrict__ out, char* ws) {
  __shared__ __attribute__((aligned(16))) char smem_[163840];
  AS3 char* sm = (AS3 char*)smem_;
  u64 ptm = 0, pc0 = 0, pt0 = 0, pte = 0, st_[16] = {};
  u32x16 sts;
  u32x4 st2;
  asm volatile(
      "s_memrealtime s[12:13]\n"
      "s_load_dwordx16 s[64:79], %[ka], 0x0\n"
      "s_load_dwordx8 s[80:87], %[ka], 0x40\n"
      "s_load_dwordx2 s[88:89], %[ka], 0x60\n"
      "v_readfirstlane_b32 s33, v0\n"
      "s_getpc_b64 s[44:45]\n"
      ".Lpc%=:\n"
      "s_mov_b32 s34, %[bid]\n"
      "v_and_b32 v43, 63, v0\n"
      "v_mov_b32 v15, 0\n"
      "s_lshr_b32 s33, s33, 6\n"
      "s_and_b32 s52, s34, -8\n"
      "s_add_u32 s52, s52, s33\n"
      "s_min_u32 s52, s52, (.LEnd%=-.Lpc%=)>>7\n"
      "s_lshl_b32 s52, s52, 7\n"
      "s_and_b32 s44, s44, 0xffffff80\n"
      "s_add_u32 s44, s44, s52\n"
      "s_addc_u32 s45, s45, 0\n"
      "global_load_dword v11, v15, s[44:45] \n"
      "s_and_b32 s48, s34, 7\n"
      "s_lshr_b32 s49, s34, 3\n"
      "s_and_b32 s55, s49, 15\n"
      "s_lshl4_add_u32 s35, s48, s55\n"
      "s_cmp_lt_u32 s34, 128\n"
      "s_cbranch_scc1 .LH%=\n"
      "s_lshl3_add_u32 s50, s35, s33\n"
      "s_lshr_b32 s51, s34, 5\n"
      "s_andn2_b32 s51, 4, s51\n"
      "s_add_u32 s50, s50, s51\n"
      "s_lshl_b32 s51, s50, 8\n"
      "s_lshl_b32 s50, s50, 1\n"
      "v_lshl_add_u32 v48, v43, 2, s51\n"
      "v_mov_b32 v49, s50\n"
      "s_waitcnt lgkmcnt(0)\n"
      "global_load_dword v16, v48, s[66:67] nt\n"
      "global_load_dword v17, v48, s[68:69] nt\n"
      "global_load_ushort v12, v49, s[74:75] \n"
      "global_load_dword v18, v48, s[70:71] nt\n"
      "global_load_dword v19, v48, s[72:73] nt\n"
      "global_load_ushort v13, v49, s[78:79] \n"
      "s_load_dword s15, s[88:89], 0x0\n"
      "s_setprio 3\n"
      "s_branch .LC%=\n"
      ".LH%=:\n"
      "s_cmp_ge_u32 s33, 4\n"
      "s_cbranch_scc1 .LG%=\n"
      ".LD%=:\n"
      "v_and_b32 v52, 48, v43\n"
      "v_and_b32 v53, 3, v43\n"
      "v_lshl_add_u32 v53, v53, 2, v52\n"
      "v_and_b32 v54, 12, v43\n"
      "v_lshlrev_b32 v53, 2, v53\n"
      "v_lshl_add_u32 v52, v54, 9, v53\n"
      "s_and_b32 s53, s34, 127\n"
      "s_and_b32 s52, s33, 3\n"
      "s_lshl2_add_u32 s14, s53, s52\n"
      "s_lshl_b32 s51, s14, 13\n"
      "v_lshl_add_u32 v53, v54, 6, v53\n"
      "s_waitcnt lgkmcnt(0)\n"
      "s_add_u32 s52, s64, s51\n"
      "s_addc_u32 s53, s65, 0\n"
      "global_load_dwordx4 v[224:227], v53, s[76:77] offset:0\n"
      "global_load_dwordx4 v[228:231], v53, s[76:77] offset:1024\n"
      "global_load_dwordx4 v[192:195], v52, s[52:53] offset:0\n"
      "global_load_dwordx4 v[196:199], v52, s[52:53] offset:256\n"
      "global_load_dwordx4 v[200:203], v52, s[52:53] offset:512\n"
      "global_load_dwordx4 v[204:207], v52, s[52:53] offset:768\n"
      "global_load_dwordx4 v[208:211], v52, s[52:53] offset:1024\n"
      "global_load_dwordx4 v[212:215], v52, s[52:53] offset:1280\n"
      "global_load_dwordx4 v[216:219], v52, s[52:53] offset:1536\n"
      "global_load_dwordx4 v[220:223], v52, s[52:53] offset:1792\n"
      "s_load_dword s15, s[88:89], 0x0\n"
      "s_setprio 3\n"
      "s_cmp_lg_u32 s33, 0\n"
      "s_cbranch_scc1 .Ltb%=\n"
      "v_mov_b32 v48, 0x22680\n"
      "v_mov_b32 v49, s12\n"
      "ds_write_b32 v48, v49 \n"
      "v_and_b32 v50, 31, v43\n"
      "v_lshlrev_b32 v50, 2, v50\n"
      "v_add_u32 v50, 0x22600, v50\n"
      "ds_write_b32 v50, v15 \n"
      "s_waitcnt lgkmcnt(0)\n"
      ".Ltb%=:\n"
      "s_barrier\n"
      "s_mov_b32 s16, s88\n"
      "s_and_b32 s17, s89, 0xffff\n"
      "s_mov_b32 s18, 0x6e0000\n"
      "s_mov_b32 s19, 0x20000\n"
      "s_mov_b32 s20, s64\n"
      "s_and_b32 s21, s65, 0xffff\n"
      "s_mov_b32 s22, 0x400000\n"
      "s_mov_b32 s23, 0x20000\n"
      "s_mov_b32 s28, s84\n"
      "s_and_b32 s29, s85, 0xffff\n"
      "s_mov_b32 s30, 0x700000\n"
      "s_mov_b32 s31, 0x20000\n"
      "s_mov_b32 s24, s86\n"
      "s_and_b32 s25, s87, 0xffff\n"
      "s_mov_b32 s26, 0x400000\n"
      "s_mov_b32 s27, 0x20000\n"
      "s_lshl_b32 s42, s34, 3\n"
      "s_add_u32 s42, s42, 2048\n"
      "s_lshl_b32 s43, s48, 8\n"
      "s_and_b32 s50, s49, 1\n"
      "s_lshl_b32 s50, s50, 7\n"
      "s_add_u32 s43, s43, s50\n"
      "s_add_u32 s43, s43, 65536\n"
      "v_mov_b32 v20, 0x3f803f80\n"
      "v_mov_b32 v21, 0x3f803f80\n"
      "v_mov_b32 v22, 0x3f803f80\n"
      "v_mov_b32 v23, 0x3f803f80\n"
      "v_mov_b32 v44, 0x7f000000\n"
      "v_mov_b32 v25, 0\n"
      "s_lshr_b32 s70, s14, 7\n"
      "s_mul_i32 s70, s70, 61440\n"
      "s_lshl_b32 s68, s14, 5\n"
      "s_add_u32 s68, s68, s70\n"
      "s_add_u32 s68, s68, 131072\n"
      "s_lshr_b32 s70, s14, 8\n"
      "s_mul_i32 s70, s70, 61440\n"
      "s_lshl4_add_u32 s69, s14, s70\n"
      "s_add_u32 s69, s69, 393216\n"
      "v_writelane_b32 v44, s68, 0\n"
      "s_add_u32 s68, s68, 8\n"
      "v_writelane_b32 v44, s68, 20\n"
      "s_add_u32 s68, s68, 8\n"
      "v_writelane_b32 v44, s68, 40\n"
      "s_add_u32 s68, s68, 8\n"
      "v_writelane_b32 v44, s68, 60\n"
      "s_add_u32 s68, s68, 8\n"
      "v_writelane_b32 v44, s69, 1\n"
      "s_add_u32 s69, s69, 8\n"
      "v_writelane_b32 v44, s69, 2\n"
      "s_waitcnt lgkmcnt(0)\n"
      "s_add_u32 s15, s15, 0x9e3779b1\n"
      "v_mov_b32 v47, s15\n"
      "s_waitcnt vmcnt(9)\n"
      "v_mfma_f32_16x16x32_bf16 v[16:19], v[20:23], v[224:227], 0\n"
      "s_waitcnt vmcnt(8)\n"
      "v_mfma_f32_16x16x32_bf16 v[16:19], v[20:23], v[228:231], v[16:19]\n"
      "s_waitcnt vmcnt(7)\n"
      "v_mfma_f32_16x16x32_bf16 v[232:235], v[192:195], v[224:227], 0\n"
      "v_mfma_f32_16x16x32_bf16 v[252:255], v[192:195], v[192:195], 0\n"
      "v_mfma_f32_16x16x32_bf16 v[248:251], v[192:195], v[20:23], 0\n"
      "s_waitcnt vmcnt(6)\n"
      "v_mfma_f32_16x16x32_bf16 v[236:239], v[196:199], v[224:227], 0\n"
      "v_mfma_f32_16x16x32_bf16 v[252:255], v[196:199], v[196:199], v[252:255]\n"
      "v_mfma_f32_16x16x32_bf16 v[248:251], v[196:199], v[20:23], v[248:251]\n"
      "s_waitcnt vmcnt(5)\n"
      "v_mfma_f32_16x16x32_bf16 v[240:243], v[200:203], v[224:227], 0\n"
      "v_mfma_f32_16x16x32_bf16 v[252:255], v[200:203], v[200:203], v[252:255]\n"
      "v_mfma_f32_16x16x32_bf16 v[248:251], v[200:203], v[20:23], v[248:251]\n"
      "s_waitcnt vmcnt(4)\n"
      "v_mfma_f32_16x16x32_bf16 v[244:247], v[204:207], v[224:227], 0\n"
      "v_mfma_f32_16x16x32_bf16 v[252:255], v[204:207], v[204:207], v[252:255]\n"
      "v_mfma_f32_16x16x32_bf16 v[248:251], v[204:207], v[20:23], v[248:251]\n"
      "s_waitcnt vmcnt(3)\n"
      "v_mfma_f32_16x16x32_bf16 v[232:235], v[208:211], v[228:231], v[232:235]\n"
      "v_mfma_f32_16x16x32_bf16 v[252:255], v[208:211], v[208:211], v[252:255]\n"
      "v_mfma_f32_16x16x32_bf16 v[248:251], v[208:211], v[20:23], v[248:251]\n"
      "v_add_f32_dpp v16, v16, v16 quad_perm:[1,0,3,2] row_mask:0xf bank_mask:0xf\n"
      "s_waitcnt vmcnt(2)\n"
      "v_mfma_f32_16x16x32_bf16 v[236:239], v[212:215], v[228:231], v[236:239]\n"
      "v_mfma_f32_16x16x32_bf16 v[252:255], v[212:215], v[212:215], v[252:255]\n"
      "v_mfma_f32_16x16x32_bf16 v[248:251], v[212:215], v[20:23], v[248:251]\n"
      "v_add_f32_dpp v16, v16, v16 quad_perm:[2,3,0,1] row_mask:0xf bank_mask:0xf\n"
      "v_add_f32_dpp v25, v232, v25 quad_perm:[0,0,0,0] row_mask:0xf bank_mask:0x1\n"
      "s_nop 1\n"
      "v_add_f32_dpp v25, v233, v25 quad_perm:[1,1,1,1] row_mask:0xf bank_mask:0x1\n"
      "s_nop 1\n"
      "v_add_f32_dpp v25, v234, v25 quad_perm:[2,2,2,2] row_mask:0xf bank_mask:0x1\n"
      "s_nop 1\n"
      "v_add_f32_dpp v25, v235, v25 quad_perm:[3,3,3,3] row_mask:0xf bank_mask:0x1\n"
      "s_waitcnt vmcnt(1)\n"
      "v_mfma_f32_16x16x32_bf16 v[240:243], v[216:219], v[228:231], v[240:243]\n"
      "v_mfma_f32_16x16x32_bf16 v[252:255], v[216:219], v[216:219], v[252:255]\n"
      "v_mfma_f32_16x16x32_bf16 v[248:251], v[216:219], v[20:23], v[248:251]\n"
      "v_add_f32_dpp v16, v16, v16 row_ror:4 row_mask:0xf bank_mask:0xf\n"
      "v_add_f32_dpp v25, v236, v25 quad_perm:[0,0,0,0] row_mask:0xf bank_mask:0x2\n"
      "s_nop 1\n"
      "v_add_f32_dpp v25, v237, v25 quad_perm:[1,1,1,1] row_mask:0xf bank_mask:0x2\n"
      "s_nop 1\n"
      "v_add_f32_dpp v25, v238, v25 quad_perm:[2,2,2,2] row_mask:0xf bank_mask:0x2\n"
      "s_nop 1\n"
      "v_add_f32_dpp v25, v239, v25 quad_perm:[3,3,3,3] row_mask:0xf bank_mask:0x2\n"
      "s_waitcnt vmcnt(0)\n"
      "v_mfma_f32_16x16x32_bf16 v[244:247], v[220:223], v[228:231], v[244:247]\n"
      "v_mfma_f32_16x16x32_bf16 v[252:255], v[220:223], v[220:223], v[252:255]\n"
      "v_mfma_f32_16x16x32_bf16 v[248:251], v[220:223], v[20:23], v[248:251]\n"
      "v_add_f32_dpp v16, v16, v16 row_ror:8 row_mask:0xf bank_mask:0xf\n"
      "v_add_f32_dpp v25, v240, v25 quad_perm:[0,0,0,0] row_mask:0xf bank_mask:0x4\n"
      "s_nop 1\n"
      "v_add_f32_dpp v25, v241, v25 quad_perm:[1,1,1,1] row_mask:0xf bank_mask:0x4\n"
      "s_nop 1\n"
      "v_add_f32_dpp v25, v242, v25 quad_perm:[2,2,2,2] row_mask:0xf bank_mask:0x4\n"
      "s_nop 1\n"
      "v_add_f32_dpp v25, v243, v25 quad_perm:[3,3,3,3] row_mask:0xf bank_mask:0x4\n"
      "s_nop 1\n"
      "v_add_f32_dpp v25, v244, v25 quad_perm:[0,0,0,0] row_mask:0xf bank_mask:0x8\n"
      "v_mov_b32_dpp v24, v252 quad_perm:[0,0,0,0] row_mask:0xf bank_mask:0xf\n"
      "v_mov_b32_dpp v27, v254 quad_perm:[2,2,2,2] row_mask:0xf bank_mask:0xf\n"
      "v_add_f32_dpp v25, v245, v25 quad_perm:[1,1,1,1] row_mask:0xf bank_mask:0x8\n"
      "v_add_f32 v248, v248, v249\n"
      "v_add_f32 v250, v250, v251\n"
      "v_add_f32_dpp v24, v253, v24 quad_perm:[1,1,1,1] row_mask:0xf bank_mask:0xf\n"
      "v_add_f32_dpp v27, v255, v27 quad_perm:[3,3,3,3] row_mask:0xf bank_mask:0xf\n"
      "v_add_f32_dpp v25, v246, v25 quad_perm:[2,2,2,2] row_mask:0xf bank_mask:0x8\n"
      "v_add_f32 v248, v248, v250\n"
      "v_add_f32 v24, v24, v27\n"
      "v_add_f32_dpp v25, v247, v25 quad_perm:[3,3,3,3] row_mask:0xf bank_mask:0x8\n"
      "v_mul_f32 v248, 0x3a800000, v248\n"
      "v_mul_f32 v24, 0x3a800000, v24\n"
      "v_add_f32_dpp v25, v25, v25 row_ror:4 row_mask:0xf bank_mask:0xf\n"
      "v_fma_f32 v24, -v248, v248, v24\n"
      "v_max_f32 v24, 0, v24\n"
      "v_add_f32_dpp v25, v25, v25 row_ror:8 row_mask:0xf bank_mask:0xf\n"
      "v_cvt_pk_bf16_f32 v248, v248, v24\n"
      "v_and_b32 v24, 0xffff0000, v248\n"
      "v_add_f32 v24, 0x3727c5ac, v24\n"
      "v_lshlrev_b32 v26, 16, v248\n"
      "v_cvt_pk_bf16_f32 v24, v24, v24\n"
      "v_fma_f32 v25, -v26, v16, v25\n"
      "v_and_b32 v24, 0xffff0000, v24\n"
      "v_sqrt_f32 v24, v24\n"
      "s_nop 0\n"
      "v_cvt_pk_bf16_f32 v46, v26, v24\n"
      "v_and_b32 v24, 0xffff0000, v46\n"
      "v_rcp_f32 v24, v24\n"
      "s_nop 0\n"
      "v_mul_f32 v25, v25, v24\n"
      "v_cvt_pk_bf16_f32 v25, v25, v25\n"
      "s_nop 0\n"
      "v_readlane_b32 s68, v25, 0\n"
      "v_readlane_b32 s69, v25, 20\n"
      "v_readlane_b32 s70, v25, 40\n"
      "v_readlane_b32 s71, v25, 60\n"
      "s_pack_ll_b32_b16 s68, s68, s69\n"
      "s_pack_ll_b32_b16 s70, s70, s71\n"
      "v_writelane_b32 v46, s68, 1\n"
      "v_writelane_b32 v46, s70, 2\n"
      "buffer_store_dwordx2 v[46:47], v44, s[16:19], 0 offen sc1\n"
      "v_mov_b32 v48, v43\n"
      "v_and_b32 v49, 7, v43\n"
      "v_lshrrev_b32 v50, 3, v43\n"
      "s_cmp_ge_u32 s34, 112\n"
      "s_cbranch_scc1 .LS%=\n"
      "s_branch .LD2%=\n"
      ".LC%=:\n"
      "s_mov_b32 s16, s88\n"
      "s_and_b32 s17, s89, 0xffff\n"
      "s_mov_b32 s18, 0x6e0000\n"
      "s_mov_b32 s19, 0x20000\n"
      "s_mov_b32 s20, s64\n"
      "s_and_b32 s21, s65, 0xffff\n"
      "s_mov_b32 s22, 0x400000\n"
      "s_mov_b32 s23, 0x20000\n"
      "s_mul_i32 s39, s33, 6528\n"
      "s_mul_i32 s40, s33, 8768\n"
      "s_add_u32 s52, s40, 52224\n"
      "s_add_u32 s53, s52, 4160\n"
      "s_lshl_b32 s42, s34, 3\n"
      "s_add_u32 s42, s42, 2048\n"
      "v_and_b32 v48, 31, v43\n"
      "v_lshrrev_b32 v49, 5, v43\n"
      "v_lshlrev_b32 v55, 4, v49\n"
      "v_lshrrev_b32 v52, 2, v48\n"
      "v_mul_u32_u24 v52, 0x110, v52\n"
      "v_add3_u32 v20, v52, v55, s52\n"
      "v_and_b32 v52, 3, v43\n"
      "v_mul_u32_u24 v52, 0x110, v52\n"
      "s_add_u32 s50, s52, 2176\n"
      "v_add3_u32 v178, v52, v55, s50\n"
      "v_lshl_add_u32 v52, v43, 2, s50\n"
      "v_mov_b32 v53, 0x3c00\n"
      "ds_write_b32 v52, v53 \n"
      "ds_write_b32 v52, v53 offset:3264\n"
      "v_and_b32 v52, 12, v43\n"
      "s_mov_b32 s50, 0x6600\n"
      "s_mov_b32 s51, 0x6600\n"
      "v_cndmask_b32 v52, v48, v52, s[50:51]\n"
      "s_mov_b32 s50, 0xff000000\n"
      "s_mov_b32 s51, 0xff000000\n"
      "v_cndmask_b32 v52, v52, 9, s[50:51]\n"
      "v_mul_u32_u24 v52, 0x110, v52\n"
      "v_add3_u32 v21, v52, v55, s39\n"
      "v_lshl_add_u32 v52, v43, 2, s39\n"
      "v_mov_b32 v53, 0x3c00\n"
      "ds_write_b32 v52, v53 \n"
      "s_mov_b32 s24, s86\n"
      "s_and_b32 s25, s87, 0xffff\n"
      "s_mov_b32 s26, 0x400000\n"
      "s_mov_b32 s27, 0x20000\n"
      "s_mul_i32 s54, s33, 4864\n"
      "s_add_u32 s54, s54, 122368\n"
      "s_lshl_b32 s41, s35, 4\n"
      "s_and_b32 s43, s41, 0xffffff80\n"
      "s_add_u32 s43, s43, 67584\n"
      "v_and_b32 v50, 15, v43\n"
      "v_lshrrev_b32 v51, 4, v43\n"
      "v_and_b32 v53, 1, v43\n"
      "s_and_b32 s58, s33, 3\n"
      "s_lshl4_add_u32 s50, s33, s55\n"
      "s_lshl_b32 s50, s50, 15\n"
      "s_and_b32 s51, s55, 8\n"
      "s_lshl_b32 s51, s51, 4\n"
      "s_sub_u32 s50, s50, s51\n"
      "v_bfe_u32 v52, v43, 1, 4\n"
      "v_lshl_add_u32 v52, v52, 4, v53\n"
      "v_lshlrev_b32 v52, 7, v52\n"
      "v_add_u32 v15, s50, v52\n"
      "s_bitcmp1_b32 s55, 0\n"
      "s_cselect_b32 s56, s82, s80\n"
      "s_cselect_b32 s57, s83, s81\n"
      "s_lshr_b32 s50, s56, 7\n"
      "s_sub_u32 s50, s48, s50\n"
      "s_add_u32 s50, s50, 3\n"
      "s_and_b32 s50, s50, 7\n"
      "s_min_u32 s50, s50, 6\n"
      "s_mul_i32 s51, s48, 7\n"
      "s_add_u32 s50, s50, s51\n"
      "s_lshl_b32 s50, s50, 7\n"
      "s_lshr_b32 s51, s55, 1\n"
      "s_mul_i32 s51, s51, 24\n"
      "s_lshl_b32 s58, s58, 8\n"
      "s_add_u32 s51, s51, s58\n"
      "s_add_u32 s51, s51, 64\n"
      "s_mul_i32 s51, s51, 7168\n"
      "s_add_u32 s50, s50, s51\n"
      "v_min_u32 v57, 23, v43\n"
      "v_mul_u32_u24 v57, 0x1c00, v57\n"
      "v_add_u32 v57, s50, v57\n"
      "v_lshl_add_u32 v52, v48, 1, v49\n"
      "v_lshlrev_b32 v54, 3, v52\n"
      "s_add_u32 s50, s53, 570\n"
      "v_sub_u32 v22, s50, v54\n"
      "s_lshl_b32 s50, s33, 17\n"
      "v_lshl_add_u32 v8, v52, 11, s50\n"
      "v_mov_b32 v52, 0x63e\n"
      "v_mov_b32 v54, 0x6de\n"
      "s_mov_b32 s50, 0x55555555\n"
      "s_mov_b32 s51, 0x55555555\n"
      "v_cndmask_b32 v24, v52, v54, s[50:51]\n"
      "v_mov_b32 v52, 0x4de\n"
      "v_mov_b32 v54, 0x59e\n"
      "v_cndmask_b32 v52, v52, v54, s[50:51]\n"
      "s_mov_b32 s58, 0\n"
      "s_mov_b32 s59, -1\n"
      "v_cndmask_b32 v24, v24, v52, s[58:59]\n"
      "v_add_u32 v52, -1, v48\n"
      "v_and_b32 v52, -2, v52\n"
      "v_sub_u32 v24, v24, v52\n"
      "v_add_u32 v24, s53, v24\n"
      "v_add_u32 v23, 32, v24\n"
      "s_add_u32 s60, s53, 2048\n"
      "v_writelane_b32 v23, s60, 0\n"
      "v_writelane_b32 v23, s60, 32\n"
      "v_mul_u32_u24 v52, 0xc1, v53\n"
      "v_sub_u32 v52, v52, v48\n"
      "s_add_u32 s60, s53, 1150\n"
      "v_add_u32 v52, s60, v52\n"
      "v_add_u32 v25, 0xffffffc0, v24\n"
      "v_cndmask_b32 v25, v25, v52, s[58:59]\n"
      "v_and_b32 v52, -2, v43\n"
      "v_add_u32 v52, 58, v52\n"
      "v_mov_b32 v54, 0x810\n"
      "s_mov_b32 s60, 0xfffff000\n"
      "s_mov_b32 s61, 0xffffffff\n"
      "v_cndmask_b32 v52, v52, v54, s[60:61]\n"
      "s_movk_i32 s60, 1408\n"
      "s_movk_i32 s61, 1664\n"
      "v_writelane_b32 v52, s60, 8\n"
      "v_writelane_b32 v52, s60, 9\n"
      "v_writelane_b32 v52, s61, 10\n"
      "v_writelane_b32 v52, s61, 11\n"
      "v_mul_u32_u24 v54, 0x83e, v53\n"
      "v_add3_u32 v26, v52, v54, s53\n"
      "v_lshlrev_b32 v52, 1, v50\n"
      "v_add_u32 v52, v52, v54\n"
      "s_add_u32 s60, s53, 2140\n"
      "v_sub_u32 v52, s60, v52\n"
      "v_lshl_add_u32 v29, v51, 4, v52\n"
      "v_lshl_add_u32 v30, v51, 3, v52\n"
      "v_add_u32 v30, 0x440, v30\n"
      "v_bfe_u32 v52, v43, 2, 3\n"
      "v_max_u32 v54, 5, v52\n"
      "v_mul_u32_u24 v52, 0x60, v52\n"
      "v_lshl_add_u32 v52, v54, 6, v52\n"
      "v_add_u32 v52, 0x2a0, v52\n"
      "s_mov_b32 s60, 0xf\n"
      "s_mov_b32 s61, 0xf\n"
      "v_cndmask_b32 v52, v52, v55, s[60:61]\n"
      "v_mul_u32_u24 v54, 0x830, v49\n"
      "v_add3_u32 v27, v52, v54, s53\n"
      "v_and_b32 v55, 3, v43\n"
      "v_lshl_add_u32 v27, v55, 4, v27\n"
      "s_add_u32 s60, s39, 3104\n"
      "s_add_u32 s61, s52, 2080\n"
      "v_mov_b32 v52, s39\n"
      "v_mov_b32 v54, s60\n"
      "s_mov_b32 s50, 0xff00ff00\n"
      "s_mov_b32 s51, 0xff00ff00\n"
      "v_cndmask_b32 v28, v52, v54, s[50:51]\n"
      "v_mov_b32 v52, s52\n"
      "v_mov_b32 v54, s61\n"
      "v_cndmask_b32 v52, v52, v54, s[50:51]\n"
      "s_mov_b32 s50, 0xffff0000\n"
      "s_mov_b32 s51, 0xffff0000\n"
      "v_cndmask_b32 v28, v28, v52, s[50:51]\n"
      "v_and_b32 v52, 7, v43\n"
      "v_lshl_add_u32 v28, v52, 2, v28\n"
      "v_lshlrev_b32 v9, 3, v52\n"
      "v_bfe_u32 v10, v43, 3, 2\n"
      "v_lshlrev_b32 v10, 2, v10\n"
      "v_lshl_add_u32 v58, v49, 8, v48\n"
      "s_lshl_b32 s50, s33, 5\n"
      "v_add_u32 v58, s50, v58\n"
      "s_mov_b32 s50, 122912\n"
      "v_lshl_add_u32 v32, v58, 2, s50\n"
      "v_lshlrev_b32 v41, 12, v58\n"
      "v_mul_u32_u24 v42, 0xf000, v49\n"
      "v_lshl_add_u32 v42, v58, 4, v42\n"
      "v_lshl_add_u32 v52, v50, 1, v51\n"
      "v_lshl_add_u32 v33, v52, 4, s54\n"
      "s_add_u32 s50, s54, 528\n"
      "v_lshl_add_u32 v34, v43, 4, s50\n"
      "v_lshl_add_u32 v35, v43, 3, s39\n"
      "v_lshl_add_u32 v36, v43, 3, s52\n"
      "s_add_u32 s50, s39, 32\n"
      "v_lshl_add_u32 v37, v43, 4, s50\n"
      "s_add_u32 s50, s52, 32\n"
      "v_lshl_add_u32 v38, v43, 4, s50\n"
      "v_lshl_add_u32 v52, v50, 2, v51\n"
      "v_lshl_add_u32 v39, v52, 2, s53\n"
      "v_bfe_u32 v52, v43, 2, 2\n"
      "v_mul_u32_u24 v52, 0x2240, v52\n"
      "v_lshl_add_u32 v52, v49, 11, v52\n"
      "v_lshl_add_u32 v52, v55, 3, v52\n"
      "v_and_b32 v54, 16, v43\n"
      "v_lshl_add_u32 v52, v54, 1, v52\n"
      "s_lshl_b32 s50, s33, 6\n"
      "s_add_u32 s50, s50, 56384\n"
      "v_add_u32 v40, s50, v52\n"
      "v_mov_b32 v54, 0\n"
      "v_mov_b32 v55, 0\n"
      "v_lshl_add_u32 v52, v43, 3, s54\n"
      "ds_write_b64 v52, v[54:55] offset:32\n"
      "s_mov_b32 s50, 161536\n"
      "v_lshl_add_u32 v52, v0, 2, s50\n"
      "ds_write_b32 v52, v54 \n"
      "v_mov_b32 v52, 0xffff\n"
      "v_mov_b32 v53, 0xffff0000\n"
      "s_mov_b32 s50, 0x0\n"
      "s_mov_b32 s51, 0x18000018\n"
      "v_cndmask_b32 v44, 0, v52, s[50:51]\n"
      "s_mov_b32 s60, 0x0\n"
      "s_mov_b32 s61, 0x30000030\n"
      "v_cndmask_b32 v54, 0, v53, s[60:61]\n"
      "v_or_b32 v44, v44, v54\n"
      "s_mov_b32 s50, 0x0\n"
      "s_mov_b32 s51, 0x60000060\n"
      "v_cndmask_b32 v45, 0, v52, s[50:51]\n"
      "s_mov_b32 s60, 0x0\n"
      "s_mov_b32 s61, 0xc00000c0\n"
      "v_cndmask_b32 v54, 0, v53, s[60:61]\n"
      "v_or_b32 v45, v45, v54\n"
      "s_mov_b32 s50, 0x10000\n"
      "s_mov_b32 s51, 0x80000180\n"
      "v_cndmask_b32 v46, 0, v52, s[50:51]\n"
      "s_mov_b32 s60, 0x30000\n"
      "s_mov_b32 s61, 0x300\n"
      "v_cndmask_b32 v54, 0, v53, s[60:61]\n"
      "v_or_b32 v46, v46, v54\n"
      "s_mov_b32 s50, 0x60000\n"
      "s_mov_b32 s51, 0x600\n"
      "v_cndmask_b32 v47, 0, v52, s[50:51]\n"
      "s_mov_b32 s60, 0xc0000\n"
      "s_mov_b32 s61, 0xc00\n"
      "v_cndmask_b32 v54, 0, v53, s[60:61]\n"
      "v_or_b32 v47, v47, v54\n"
      ".Lsc%=:\n"
      "s_waitcnt vmcnt(0)\n"
      "s_mov_b32 s50, 0x3fb8aa3b\n"
      "v_mov_b32 v14, v57\n"
      "v_lshlrev_b32 v48, 16, v12\n"
      "v_and_b32 v52, 0x7fff7fff, v18\n"
      "v_and_b32 v53, 0x7fff7fff, v19\n"
      "v_mul_f32 v49, s50, v48\n"
      "v_pk_max_u16 v52, v52, v53\n"
      "v_fma_f32 v50, v48, s50, -v49\n"
      "v_rndne_f32 v51, v49\n"
      "v_lshlrev_b32 v53, 16, v52\n"
      "v_fmamk_f32 v50, v48, 0x32a5705f, v50\n"
      "v_sub_f32 v49, v49, v51\n"
      "v_max_u32 v52, v52, v53\n"
      "v_add_f32 v49, v49, v50\n"
      "v_cvt_i32_f32 v51, v51\n"
      "v_max_u32_dpp v52, v52, v52 row_shr:1 row_mask:0xf bank_mask:0xf bound_ctrl:1\n"
      "v_exp_f32 v49, v49\n"
      "v_lshlrev_b32 v54, 16, v13\n"
      "v_max_u32_dpp v52, v52, v52 row_shr:2 row_mask:0xf bank_mask:0xf bound_ctrl:1\n"
      "v_ldexp_f32 v49, v49, v51\n"
      "v_add_f32 v54, 1.0, v54\n"
      "v_max_u32_dpp v52, v52, v52 row_shr:4 row_mask:0xf bank_mask:0xf bound_ctrl:1\n"
      "v_cvt_pk_bf16_f32 v49, v49, v49\n"
      "s_nop 0\n"
      "v_max_u32_dpp v52, v52, v52 row_shr:8 row_mask:0xf bank_mask:0xf bound_ctrl:1\n"
      "v_and_b32 v49, 0xffff0000, v49\n"
      "v_and_b32 v55, 0x7fffffff, v54\n"
      "v_max_u32_dpp v52, v52, v52 row_bcast:15 row_mask:0xa bank_mask:0xf\n"
      "v_mul_f32 v50, 0xbfb8aa3b, v49\n"
      "v_mul_f32 v51, 0xbe22f983, v49\n"
      "v_max_u32_dpp v52, v52, v52 row_bcast:31 row_mask:0xc bank_mask:0xf\n"
      "v_readfirstlane_b32 s36, v50\n"
      "v_readlane_b32 s50, v52, 63\n"
      "v_readfirstlane_b32 s37, v51\n"
      "v_mul_f32 v55, 0x3b800000, v55\n"
      "v_mul_f32 v52, s50, v49\n"
      "v_max_f32 v52, v52, v55\n"
      "v_frexp_exp_i32_f32 v52, v52\n"
      "s_nop 0\n"
      "v_readfirstlane_b32 s50, v52\n"
      "s_nop 0\n"
      "s_sub_i32 s50, 7, s50\n"
      "s_max_i32 s50, s50, -107\n"
      "s_min_i32 s50, s50, 93\n"
      "v_ldexp_f32 v49, v49, s50\n"
      "s_sub_i32 s51, s50, 1\n"
      "v_ldexp_f32 v54, v54, s51\n"
      "s_sub_i32 s14, 128, s50\n"
      "s_lshl_b32 s14, s14, 23\n"
      "v_readfirstlane_b32 s38, v49\n"
      "s_mov_b64 s[60:61], 1\n"
      "v_cndmask_b32 v31, 0, v54, s[60:61]\n"
      "s_lshl_b32 s50, s33, 2\n"
      "s_add_u32 s50, s50, 161280\n"
      "v_mov_b32 v50, s50\n"
      "v_mov_b32 v51, s14\n"
      "ds_write_b32 v50, v51 \n"
      "s_mov_b32 s60, s41\n"
      "s_add_u32 s61, s41, 0x200000\n"
      "s_mov_b32 s62, 131072\n"
      "s_mov_b32 s63, 262144\n"
      "s_mov_b32 m0, s40\n"
      "s_add_u32 s13, s40, 52224\n"
      "s_add_u32 s12, s39, 272\n"
      "v_lshlrev_b32 v72, 16, v16\n"
      "v_and_b32 v73, 0xffff0000, v16\n"
      "v_lshlrev_b32 v74, 16, v17\n"
      "v_and_b32 v75, 0xffff0000, v17\n"
      "s_mov_b32 s64, 0x3fb8aa3b\n"
      "s_mov_b32 s66, 0x43800000\n"
      "s_mov_b32 s68, 0x42000000\n"
      "s_mov_b32 s70, s37\n"
      "v_pk_mul_f32 v[72:73], s[64:65], v[72:73] op_sel_hi:[0,1]\n"
      "v_pk_mul_f32 v[74:75], s[70:71], v[74:75] op_sel_hi:[0,1]\n"
      "v_exp_f32 v72, v72\n"
      "v_exp_f32 v73, v73\n"
      "v_pk_mul_f32 v[78:79], s[66:67], v[74:75] op_sel_hi:[0,1]\n"
      "v_pk_mul_f32 v[80:81], s[68:69], v[74:75] op_sel_hi:[0,1]\n"
      "v_fract_f32 v76, v74\n"
      "v_fract_f32 v77, v75\n"
      "v_cvt_pk_bf16_f32 v72, v72, v73\n"
      "v_cos_f32 v90, v76\n"
      "v_cos_f32 v91, v77\n"
      "v_fract_f32 v78, v78\n"
      "v_fract_f32 v79, v79\n"
      "v_and_b32 v73, 0xffff0000, v72\n"
      "v_lshlrev_b32 v72, 16, v72\n"
      "v_sin_f32 v92, v76\n"
      "v_sin_f32 v93, v77\n"
      "v_pk_mul_f32 v[82:83], s[36:37], v[72:73] op_sel_hi:[0,1]\n"
      "v_fract_f32 v80, v80\n"
      "v_fract_f32 v81, v81\n"
      "v_exp_f32 v84, v82\n"
      "v_exp_f32 v85, v83\n"
      "v_pk_mul_f32 v[86:87], s[66:67], v[82:83] op_sel_hi:[0,1]\n"
      "v_cos_f32 v94, v78\n"
      "v_cos_f32 v95, v79\n"
      "v_pk_mul_f32 v[88:89], s[68:69], v[82:83] op_sel_hi:[0,1]\n"
      "v_sin_f32 v96, v78\n"
      "v_sin_f32 v97, v79\n"
      "v_pk_mul_f32 v[90:91], v[90:91], v[84:85]\n"
      "v_exp_f32 v86, v86\n"
      "v_exp_f32 v87, v87\n"
      "v_pk_mul_f32 v[92:93], v[92:93], v[84:85]\n"
      "s_cmp_ge_u32 s33, 4\n"
      "s_cbranch_scc1 .LcR%=\n"
      "buffer_load_dword v11, v15, s[20:23], s41 offen\n"
      "global_load_dword v11, v14, s[56:57]\n"
      ".LcR%=:\n"
      "v_pk_mul_f32 v[82:83], v[92:93], v[92:93]\n"
      "v_cos_f32 v202, v80\n"
      "v_pk_add_f32 v[84:85], v[90:91], v[90:91]\n"
      "v_cos_f32 v150, v81\n"
      "v_pk_fma_f32 v[98:99], v[90:91], v[90:91], v[82:83] neg_lo:[0,0,1] neg_hi:[0,0,1]\n"
      "v_pk_mul_f32 v[94:95], v[94:95], v[86:87]\n"
      "v_pk_mul_f32 v[100:101], v[84:85], v[92:93]\n"
      "v_pk_mul_f32 v[96:97], v[96:97], v[86:87]\n"
      "v_cvt_pk_f16_f32 v208, v90, v92\n"
      "v_sin_f32 v203, v80\n"
      "v_cvt_pk_f16_f32 v156, v91, v93\n"
      "v_sin_f32 v151, v81\n"
      "v_pk_mul_f32 v[82:83], v[100:101], v[100:101]\n"
      "v_exp_f32 v88, v88\n"
      "v_pk_add_f32 v[84:85], v[98:99], v[98:99]\n"
      "v_exp_f32 v89, v89\n"
      "v_pk_fma_f32 v[102:103], v[98:99], v[98:99], v[82:83] neg_lo:[0,0,1] neg_hi:[0,0,1]\n"
      "v_pk_mul_f32 v[114:115], v[96:97], v[96:97]\n"
      "v_pk_mul_f32 v[104:105], v[84:85], v[100:101]\n"
      "v_pk_add_f32 v[116:117], v[94:95], v[94:95]\n"
      "v_cvt_pk_f16_f32 v209, v98, v100\n"
      "v_pk_fma_f32 v[106:107], v[94:95], v[94:95], v[114:115] neg_lo:[0,0,1] neg_hi:[0,0,1]\n"
      "v_cvt_pk_f16_f32 v157, v99, v101\n"
      "v_pk_mul_f32 v[108:109], v[116:117], v[96:97]\n"
      "v_pk_mul_f32 v[82:83], v[104:105], v[104:105]\n"
      "v_cvt_pk_f16_f32 v212, v94, v96\n"
      "v_pk_add_f32 v[84:85], v[102:103], v[102:103]\n"
      "v_cvt_pk_f16_f32 v160, v95, v97\n"
      "v_pk_fma_f32 v[90:91], v[102:103], v[102:103], v[82:83] neg_lo:[0,0,1] neg_hi:[0,0,1]\n"
      "v_pk_mul_f32 v[202:203], v[202:203], v[88:89] op_sel:[0,0] op_sel_hi:[1,0]\n"
      "v_pk_mul_f32 v[92:93], v[84:85], v[104:105]\n"
      "v_pk_mul_f32 v[150:151], v[150:151], v[88:89] op_sel:[0,1] op_sel_hi:[1,1]\n"
      "v_cvt_pk_f16_f32 v210, v102, v104\n"
      "v_cvt_pk_f16_f32 v213, v106, v108\n"
      "v_cvt_pk_f16_f32 v158, v103, v105\n"
      "v_cvt_pk_f16_f32 v161, v107, v109\n"
      "v_pk_mul_f32 v[82:83], v[92:93], v[92:93]\n"
      "v_pk_mul_f32 v[114:115], v[108:109], v[108:109]\n"
      "v_pk_add_f32 v[84:85], v[90:91], v[90:91]\n"
      "v_pk_add_f32 v[116:117], v[106:107], v[106:107]\n"
      "v_pk_fma_f32 v[72:73], v[90:91], v[90:91], v[82:83] neg_lo:[0,0,1] neg_hi:[0,0,1]\n"
      "v_pk_fma_f32 v[94:95], v[106:107], v[106:107], v[114:115] neg_lo:[0,0,1] neg_hi:[0,0,1]\n"
      "v_pk_mul_f32 v[74:75], v[84:85], v[92:93]\n"
      "v_pk_mul_f32 v[96:97], v[116:117], v[108:109]\n"
      "v_cvt_pk_f16_f32 v205, v90, v92\n"
      "v_pk_mul_f16 v214, v212, v213 op_sel_hi:[0,1]\n"
      "v_cvt_pk_f16_f32 v153, v91, v93\n"
      "v_pk_mul_f16 v162, v160, v161 op_sel_hi:[0,1]\n"
      "v_pk_mul_f16 v211, v209, v210 op_sel_hi:[0,1]\n"
      "v_pk_fma_f16 v214, v212, v213, v214 op_sel:[1,1,0] op_sel_hi:[1,0,1] neg_lo:[0,1,0]\n"
      "v_pk_mul_f16 v159, v157, v158 op_sel_hi:[0,1]\n"
      "v_pk_fma_f16 v162, v160, v161, v162 op_sel:[1,1,0] op_sel_hi:[1,0,1] neg_lo:[0,1,0]\n"
      "v_pk_fma_f16 v211, v209, v210, v211 op_sel:[1,1,0] op_sel_hi:[1,0,1] neg_lo:[0,1,0]\n"
      "v_cvt_pk_f16_f32 v215, v94, v96\n"
      "v_pk_fma_f16 v159, v157, v158, v159 op_sel:[1,1,0] op_sel_hi:[1,0,1] neg_lo:[0,1,0]\n"
      "v_cvt_pk_f16_f32 v163, v95, v97\n"
      "v_cvt_pk_f16_f32 v206, v72, v74\n"
      "v_lshlrev_b32 v198, 16, v18\n"
      "v_cvt_pk_f16_f32 v154, v73, v75\n"
      "v_lshlrev_b32 v199, 16, v19\n"
      "v_pk_mul_f16 v207, v205, v206 op_sel_hi:[0,1]\n"
      "v_pk_mul_f32 v[198:199], s[38:39], v[198:199] op_sel_hi:[0,1]\n"
      "v_pk_mul_f16 v155, v153, v154 op_sel_hi:[0,1]\n"
      "v_and_b32 v146, 0xffff0000, v18\n"
      "v_pk_fma_f16 v207, v205, v206, v207 op_sel:[1,1,0] op_sel_hi:[1,0,1] neg_lo:[0,1,0]\n"
      "v_and_b32 v147, 0xffff0000, v19\n"
      "v_pk_fma_f16 v155, v153, v154, v155 op_sel:[1,1,0] op_sel_hi:[1,0,1] neg_lo:[0,1,0]\n"
      "v_pk_mul_f32 v[146:147], s[38:39], v[146:147] op_sel_hi:[0,1]\n"
      "s_waitcnt lgkmcnt(0)\n"
      "s_add_u32 s15, s15, 0x9e3779b1\n"
      "v_cvt_pk_f16_f32 v240, v198, v199\n"
      "v_cvt_pk_f16_f32 v241, v146, v147\n"
      "v_pk_mul_f32 v[200:201], v[198:199], v[202:203] op_sel_hi:[1,0]\n"
      "v_pk_mul_f32 v[148:149], v[146:147], v[150:151] op_sel_hi:[1,0]\n"
      "ds_write_addtid_b32 v240 offset:52224\n"
      "ds_write_addtid_b32 v241 offset:55488\n"
      "v_pk_fma_f32 v[200:201], v[198:199], v[202:203], v[200:201] op_sel:[1,1,0] op_sel_hi:[0,1,1] neg_hi:[0,1,0]\n"
      "v_pk_fma_f32 v[148:149], v[146:147], v[150:151], v[148:149] op_sel:[1,1,0] op_sel_hi:[0,1,1] neg_hi:[0,1,0]\n"
      "ds_write_addtid_b32 v205 offset:54672\n"
      "v_cvt_pk_f16_f32 v242, v200, v201\n"
      "v_cvt_pk_f16_f32 v243, v148, v149\n"
      "v_pk_mul_f32 v[198:199], v[200:201], v[202:203] op_sel_hi:[1,0]\n"
      "v_pk_mul_f32 v[146:147], v[148:149], v[150:151] op_sel_hi:[1,0]\n"
      "ds_write_addtid_b32 v242 offset:52496\n"
      "ds_write_addtid_b32 v243 offset:55760\n"
      "v_pk_fma_f32 v[198:199], v[200:201], v[202:203], v[198:199] op_sel:[1,1,0] op_sel_hi:[0,1,1] neg_hi:[0,1,0]\n"
      "v_pk_fma_f32 v[146:147], v[148:149], v[150:151], v[146:147] op_sel:[1,1,0] op_sel_hi:[0,1,1] neg_hi:[0,1,0]\n"
      "ds_write_addtid_b32 v153 offset:57936\n"
      "v_cvt_pk_f16_f32 v240, v198, v199\n"
      "v_cvt_pk_f16_f32 v241, v146, v147\n"
      "v_pk_mul_f32 v[200:201], v[198:199], v[202:203] op_sel_hi:[1,0]\n"
      "v_pk_mul_f32 v[148:149], v[146:147], v[150:151] op_sel_hi:[1,0]\n"
      "ds_write_addtid_b32 v240 offset:52768\n"
      "ds_write_addtid_b32 v241 offset:56032\n"
      "v_pk_fma_f32 v[200:201], v[198:199], v[202:203], v[200:201] op_sel:[1,1,0] op_sel_hi:[0,1,1] neg_hi:[0,1,0]\n"
      "v_pk_fma_f32 v[148:149], v[146:147], v[150:151], v[148:149] op_sel:[1,1,0] op_sel_hi:[0,1,1] neg_hi:[0,1,0]\n"
      "ds_write_addtid_b32 v206 offset:54944\n"
      "v_cvt_pk_f16_f32 v242, v200, v201\n"
      "v_cvt_pk_f16_f32 v243, v148, v149\n"
      "v_pk_mul_f32 v[198:199], v[200:201], v[202:203] op_sel_hi:[1,0]\n"
      "v_pk_mul_f32 v[146:147], v[148:149], v[150:151] op_sel_hi:[1,0]\n"
      "ds_write_addtid_b32 v242 offset:53040\n"
      "ds_write_addtid_b32 v243 offset:56304\n"
      "v_pk_fma_f32 v[198:199], v[200:201], v[202:203], v[198:199] op_sel:[1,1,0] op_sel_hi:[0,1,1] neg_hi:[0,1,0]\n"
      "v_pk_fma_f32 v[146:147], v[148:149], v[150:151], v[146:147] op_sel:[1,1,0] op_sel_hi:[0,1,1] neg_hi:[0,1,0]\n"
      "ds_write_addtid_b32 v154 offset:58208\n"
      "v_cvt_pk_f16_f32 v240, v198, v199\n"
      "v_cvt_pk_f16_f32 v241, v146, v147\n"
      "v_pk_mul_f32 v[200:201], v[198:199], v[202:203] op_sel_hi:[1,0]\n"
      "v_pk_mul_f32 v[148:149], v[146:147], v[150:151] op_sel_hi:[1,0]\n"
      "ds_write_addtid_b32 v240 offset:53312\n"
      "ds_write_addtid_b32 v241 offset:56576\n"
      "v_pk_fma_f32 v[200:201], v[198:199], v[202:203], v[200:201] op_sel:[1,1,0] op_sel_hi:[0,1,1] neg_hi:[0,1,0]\n"
      "v_pk_fma_f32 v[148:149], v[146:147], v[150:151], v[148:149] op_sel:[1,1,0] op_sel_hi:[0,1,1] neg_hi:[0,1,0]\n"
      "ds_write_addtid_b32 v207 offset:55216\n"
      "v_cvt_pk_f16_f32 v242, v200, v201\n"
      "v_cvt_pk_f16_f32 v243, v148, v149\n"
      "v_pk_mul_f32 v[198:199], v[200:201], v[202:203] op_sel_hi:[1,0]\n"
      "v_pk_mul_f32 v[146:147], v[148:149], v[150:151] op_sel_hi:[1,0]\n"
      "ds_write_addtid_b32 v242 offset:53584\n"
      "ds_write_addtid_b32 v243 offset:56848\n"
      "v_pk_fma_f32 v[198:199], v[200:201], v[202:203], v[198:199] op_sel:[1,1,0] op_sel_hi:[0,1,1] neg_hi:[0,1,0]\n"
      "v_pk_fma_f32 v[146:147], v[148:149], v[150:151], v[146:147] op_sel:[1,1,0] op_sel_hi:[0,1,1] neg_hi:[0,1,0]\n"
      "ds_write_addtid_b32 v155 offset:58480\n"
      "v_cvt_pk_f16_f32 v240, v198, v199\n"
      "v_cvt_pk_f16_f32 v241, v146, v147\n"
      "v_pk_mul_f32 v[200:201], v[198:199], v[202:203] op_sel_hi:[1,0]\n"
      "v_pk_mul_f32 v[148:149], v[146:147], v[150:151] op_sel_hi:[1,0]\n"
      "ds_write_addtid_b32 v240 offset:53856\n"
      "ds_write_addtid_b32 v241 offset:57120\n"
      "v_pk_fma_f32 v[200:201], v[198:199], v[202:203], v[200:201] op_sel:[1,1,0] op_sel_hi:[0,1,1] neg_hi:[0,1,0]\n"
      "v_pk_fma_f32 v[148:149], v[146:147], v[150:151], v[148:149] op_sel:[1,1,0] op_sel_hi:[0,1,1] neg_hi:[0,1,0]\n"
      "v_cvt_pk_f16_f32 v242, v200, v201\n"
      "v_cvt_pk_f16_f32 v243, v148, v149\n"
      "ds_write_addtid_b32 v242 offset:54128\n"
      "ds_write_addtid_b32 v243 offset:57392\n"
      "ds_read_b128 v[104:107], v20 offset:0\n"
      "ds_read_b128 v[108:111], v178 offset:0\n"
      "ds_read_b128 v[112:115], v20 offset:32\n"
      "ds_read_b128 v[116:119], v178 offset:32\n"
      "s_mov_b32 m0, s39\n"
      "s_setprio 2\n"
      "v_pk_mul_f16 v240, v209, v208 op_sel_hi:[0,1]\n"
      "v_pk_mul_f16 v241, v210, v208 op_sel_hi:[0,1]\n"
      "v_pk_mul_f16 v242, v211, v208 op_sel_hi:[0,1]\n"
      "ds_write_addtid_b32 v208 offset:272\n"
      "v_pk_fma_f16 v240, v209, v208, v240 op_sel:[1,1,0] op_sel_hi:[1,0,1] neg_lo:[0,1,0]\n"
      "ds_write_addtid_b32 v209 offset:544\n"
      "v_pk_fma_f16 v241, v210, v208, v241 op_sel:[1,1,0] op_sel_hi:[1,0,1] neg_lo:[0,1,0]\n"
      "ds_write_addtid_b32 v240 offset:816\n"
      "v_pk_fma_f16 v242, v211, v208, v242 op_sel:[1,1,0] op_sel_hi:[1,0,1] neg_lo:[0,1,0]\n"
      "ds_write_addtid_b32 v241 offset:1360\n"
      "ds_write_addtid_b32 v242 offset:1904\n"
      "v_pk_mul_f16 v243, v212, v209 op_sel_hi:[0,1]\n"
      "v_pk_mul_f16 v244, v212, v210 op_sel_hi:[0,1]\n"
      "v_pk_mul_f16 v245, v212, v211 op_sel_hi:[0,1]\n"
      "ds_write_addtid_b32 v210 offset:1088\n"
      "v_pk_fma_f16 v243, v212, v209, v243 op_sel:[1,1,0] op_sel_hi:[1,0,1] neg_lo:[0,1,0]\n"
      "ds_write_addtid_b32 v211 offset:1632\n"
      "v_pk_fma_f16 v244, v212, v210, v244 op_sel:[1,1,0] op_sel_hi:[1,0,1] neg_lo:[0,1,0]\n"
      "ds_write_addtid_b32 v243 offset:2992\n"
      "v_pk_fma_f16 v245, v212, v211, v245 op_sel:[1,1,0] op_sel_hi:[1,0,1] neg_lo:[0,1,0]\n"
      "ds_write_addtid_b32 v244 offset:3264\n"
      "ds_write_addtid_b32 v245 offset:4080\n"
      "v_pk_mul_f16 v246, v213, v210 op_sel_hi:[0,1]\n"
      "v_pk_mul_f16 v247, v214, v210 op_sel_hi:[0,1]\n"
      "v_pk_mul_f16 v248, v215, v215 op_sel_hi:[0,1]\n"
      "ds_write_addtid_b32 v212 offset:2176\n"
      "v_pk_fma_f16 v246, v213, v210, v246 op_sel:[1,1,0] op_sel_hi:[1,0,1] neg_lo:[0,1,0]\n"
      "ds_write_addtid_b32 v213 offset:5440\n"
      "v_pk_fma_f16 v247, v214, v210, v247 op_sel:[1,1,0] op_sel_hi:[1,0,1] neg_lo:[0,1,0]\n"
      "ds_write_addtid_b32 v246 offset:5984\n"
      "v_pk_fma_f16 v248, v215, v215, v248 op_sel:[1,1,0] op_sel_hi:[1,0,1] neg_lo:[0,1,0]\n"
      "ds_write_addtid_b32 v247 offset:6256\n"
      "ds_write_addtid_b32 v248 offset:2448\n"
      "v_pk_mul_f16 v249, v215, v212 op_sel_hi:[0,1]\n"
      "v_pk_mul_f16 v250, v215, v213 op_sel_hi:[0,1]\n"
      "v_pk_mul_f16 v251, v215, v214 op_sel_hi:[0,1]\n"
      "ds_write_addtid_b32 v214 offset:5712\n"
      "v_pk_fma_f16 v249, v215, v212, v249 op_sel:[1,1,0] op_sel_hi:[1,0,1] neg_lo:[0,1,0]\n"
      "ds_write_addtid_b32 v215 offset:4352\n"
      "v_pk_fma_f16 v250, v215, v213, v250 op_sel:[1,1,0] op_sel_hi:[1,0,1] neg_lo:[0,1,0]\n"
      "ds_write_addtid_b32 v249 offset:4624\n"
      "v_pk_fma_f16 v251, v215, v214, v251 op_sel:[1,1,0] op_sel_hi:[1,0,1] neg_lo:[0,1,0]\n"
      "ds_write_addtid_b32 v250 offset:4896\n"
      "ds_write_addtid_b32 v251 offset:5168\n"
      "ds_read_b128 v[72:75], v21 offset:0\n"
      "ds_read_b128 v[76:79], v21 offset:32\n"
      "ds_read_b128 v[80:83], v21 offset:64\n"
      "ds_read_b128 v[84:87], v21 offset:96\n"
      "ds_read_b128 v[88:91], v21 offset:128\n"
      "ds_read_b128 v[92:95], v21 offset:160\n"
      "ds_read_b128 v[96:99], v21 offset:192\n"
      "ds_read_b128 v[100:103], v21 offset:224\n"
      "ds_read_b128 v[120:123], v20 offset:64\n"
      "ds_read_b128 v[124:127], v178 offset:64\n"
      "s_waitcnt lgkmcnt(15)\n"
      "v_pk_mul_f16 v164, v104, v108 op_sel_hi:[0,1] neg_hi:[0,1]\n"
      "v_pk_mul_f16 v165, v105, v109 op_sel_hi:[0,1] neg_hi:[0,1]\n"
      "v_pk_mul_f16 v166, v106, v110 op_sel_hi:[0,1] neg_hi:[0,1]\n"
      "v_pk_mul_f16 v167, v107, v111 op_sel_hi:[0,1] neg_hi:[0,1]\n"
      "v_pk_fma_f16 v164, v104, v108, v164 op_sel:[1,1,0] op_sel_hi:[1,0,1]\n"
      "v_pk_fma_f16 v165, v105, v109, v165 op_sel:[1,1,0] op_sel_hi:[1,0,1]\n"
      "v_pk_fma_f16 v166, v106, v110, v166 op_sel:[1,1,0] op_sel_hi:[1,0,1]\n"
      "v_pk_fma_f16 v167, v107, v111, v167 op_sel:[1,1,0] op_sel_hi:[1,0,1]\n"
      "s_waitcnt lgkmcnt(9)\n"
      "s_nop 0\n"
      "v_mfma_f32_32x32x16_f16 v[180:195], v[72:75], v[164:167], 0\n"
      "v_pk_mul_f16 v240, v157, v156 op_sel_hi:[0,1]\n"
      "v_pk_mul_f16 v241, v158, v156 op_sel_hi:[0,1]\n"
      "v_pk_mul_f16 v242, v159, v156 op_sel_hi:[0,1]\n"
      "v_pk_mul_f16 v168, v112, v116 op_sel_hi:[0,1] neg_hi:[0,1]\n"
      "v_pk_mul_f16 v169, v113, v117 op_sel_hi:[0,1] neg_hi:[0,1]\n"
      "v_pk_mul_f16 v170, v114, v118 op_sel_hi:[0,1] neg_hi:[0,1]\n"
      "v_pk_mul_f16 v171, v115, v119 op_sel_hi:[0,1] neg_hi:[0,1]\n"
      "ds_write_addtid_b32 v156 offset:272\n"
      "v_pk_fma_f16 v240, v157, v156, v240 op_sel:[1,1,0] op_sel_hi:[1,0,1] neg_lo:[0,1,0]\n"
      "ds_write_addtid_b32 v157 offset:544\n"
      "v_pk_fma_f16 v168, v112, v116, v168 op_sel:[1,1,0] op_sel_hi:[1,0,1]\n"
      "v_pk_fma_f16 v169, v113, v117, v169 op_sel:[1,1,0] op_sel_hi:[1,0,1]\n"
      "v_pk_fma_f16 v170, v114, v118, v170 op_sel:[1,1,0] op_sel_hi:[1,0,1]\n"
      "v_pk_fma_f16 v171, v115, v119, v171 op_sel:[1,1,0] op_sel_hi:[1,0,1]\n"
      "ds_read_b128 v[104:107], v20 offset:96\n"
      "ds_read_b128 v[108:111], v178 offset:96\n"
      "s_waitcnt lgkmcnt(12)\n"
      "v_mfma_f32_32x32x16_f16 v[180:195], v[76:79], v[168:171], v[180:195]\n"
      "v_pk_fma_f16 v241, v158, v156, v241 op_sel:[1,1,0] op_sel_hi:[1,0,1] neg_lo:[0,1,0]\n"
      "ds_write_addtid_b32 v240 offset:816\n"
      "v_pk_fma_f16 v242, v159, v156, v242 op_sel:[1,1,0] op_sel_hi:[1,0,1] neg_lo:[0,1,0]\n"
      "s_waitcnt lgkmcnt(5)\n"
      "v_pk_mul_f16 v164, v120, v124 op_sel_hi:[0,1] neg_hi:[0,1]\n"
      "v_pk_mul_f16 v165, v121, v125 op_sel_hi:[0,1] neg_hi:[0,1]\n"
      "v_pk_mul_f16 v166, v122, v126 op_sel_hi:[0,1] neg_hi:[0,1]\n"
      "v_pk_mul_f16 v167, v123, v127 op_sel_hi:[0,1] neg_hi:[0,1]\n"
      "ds_write_addtid_b32 v241 offset:1360\n"
      "ds_write_addtid_b32 v242 offset:1904\n"
      "v_pk_mul_f16 v243, v160, v157 op_sel_hi:[0,1]\n"
      "v_pk_fma_f16 v164, v120, v124, v164 op_sel:[1,1,0] op_sel_hi:[1,0,1]\n"
      "v_pk_fma_f16 v165, v121, v125, v165 op_sel:[1,1,0] op_sel_hi:[1,0,1]\n"
      "v_pk_fma_f16 v166, v122, v126, v166 op_sel:[1,1,0] op_sel_hi:[1,0,1]\n"
      "v_pk_fma_f16 v167, v123, v127, v167 op_sel:[1,1,0] op_sel_hi:[1,0,1]\n"
      "ds_read_b128 v[112:115], v20 offset:128\n"
      "ds_read_b128 v[116:119], v178 offset:128\n"
      "v_mfma_f32_32x32x16_f16 v[180:195], v[80:83], v[164:167], v[180:195]\n"
      "v_pk_mul_f16 v244, v160, v158 op_sel_hi:[0,1]\n"
      "v_pk_mul_f16 v245, v160, v159 op_sel_hi:[0,1]\n"
      "ds_write_addtid_b32 v158 offset:1088\n"
      "s_waitcnt lgkmcnt(6)\n"
      "v_pk_mul_f16 v168, v104, v108 op_sel_hi:[0,1] neg_hi:[0,1]\n"
      "v_pk_mul_f16 v169, v105, v109 op_sel_hi:[0,1] neg_hi:[0,1]\n"
      "v_pk_mul_f16 v170, v106, v110 op_sel_hi:[0,1] neg_hi:[0,1]\n"
      "v_pk_mul_f16 v171, v107, v111 op_sel_hi:[0,1] neg_hi:[0,1]\n"
      "v_pk_fma_f16 v243, v160, v157, v243 op_sel:[1,1,0] op_sel_hi:[1,0,1] neg_lo:[0,1,0]\n"
      "ds_write_addtid_b32 v159 offset:1632\n"
      "v_pk_fma_f16 v244, v160, v158, v244 op_sel:[1,1,0] op_sel_hi:[1,0,1] neg_lo:[0,1,0]\n"
      "v_pk_fma_f16 v168, v104, v108, v168 op_sel:[1,1,0] op_sel_hi:[1,0,1]\n"
      "v_pk_fma_f16 v169, v105, v109, v169 op_sel:[1,1,0] op_sel_hi:[1,0,1]\n"
      "v_pk_fma_f16 v170, v106, v110, v170 op_sel:[1,1,0] op_sel_hi:[1,0,1]\n"
      "v_pk_fma_f16 v171, v107, v111, v171 op_sel:[1,1,0] op_sel_hi:[1,0,1]\n"
      "ds_read_b128 v[120:123], v20 offset:160\n"
      "ds_read_b128 v[124:127], v178 offset:160\n"
      "buffer_load_dwordx4 v[48:51], v41, s[20:23], s60 offen nt\n"
      "v_mfma_f32_32x32x16_f16 v[180:195], v[84:87], v[168:171], v[180:195]\n"
      "ds_write_addtid_b32 v243 offset:2992\n"
      "v_pk_fma_f16 v245, v160, v159, v245 op_sel:[1,1,0] op_sel_hi:[1,0,1] neg_lo:[0,1,0]\n"
      "ds_write_addtid_b32 v244 offset:3264\n"
      "s_waitcnt lgkmcnt(6)\n"
      "v_pk_mul_f16 v164, v112, v116 op_sel_hi:[0,1] neg_hi:[0,1]\n"
      "v_pk_mul_f16 v165, v113, v117 op_sel_hi:[0,1] neg_hi:[0,1]\n"
      "v_pk_mul_f16 v166, v114, v118 op_sel_hi:[0,1] neg_hi:[0,1]\n"
      "v_pk_mul_f16 v167, v115, v119 op_sel_hi:[0,1] neg_hi:[0,1]\n"
      "ds_write_addtid_b32 v245 offset:4080\n"
      "v_pk_mul_f16 v246, v161, v158 op_sel_hi:[0,1]\n"
      "v_pk_mul_f16 v247, v162, v158 op_sel_hi:[0,1]\n"
      "v_pk_fma_f16 v164, v112, v116, v164 op_sel:[1,1,0] op_sel_hi:[1,0,1]\n"
      "v_pk_fma_f16 v165, v113, v117, v165 op_sel:[1,1,0] op_sel_hi:[1,0,1]\n"
      "v_pk_fma_f16 v166, v114, v118, v166 op_sel:[1,1,0] op_sel_hi:[1,0,1]\n"
      "v_pk_fma_f16 v167, v115, v119, v167 op_sel:[1,1,0] op_sel_hi:[1,0,1]\n"
      "ds_read_b128 v[104:107], v20 offset:192\n"
      "ds_read_b128 v[108:111], v178 offset:192\n"
      "v_mfma_f32_32x32x16_f16 v[180:195], v[88:91], v[164:167], v[180:195]\n"
      "v_pk_mul_f16 v248, v163, v163 op_sel_hi:[0,1]\n"
      "ds_write_addtid_b32 v160 offset:2176\n"
      "v_pk_fma_f16 v246, v161, v158, v246 op_sel:[1,1,0] op_sel_hi:[1,0,1] neg_lo:[0,1,0]\n"
      "s_waitcnt lgkmcnt(6)\n"
      "v_pk_mul_f16 v168, v120, v124 op_sel_hi:[0,1] neg_hi:[0,1]\n"
      "v_pk_mul_f16 v169, v121, v125 op_sel_hi:[0,1] neg_hi:[0,1]\n"
      "v_pk_mul_f16 v170, v122, v126 op_sel_hi:[0,1] neg_hi:[0,1]\n"
      "v_pk_mul_f16 v171, v123, v127 op_sel_hi:[0,1] neg_hi:[0,1]\n"
      "ds_write_addtid_b32 v161 offset:5440\n"
      "v_pk_fma_f16 v247, v162, v158, v247 op_sel:[1,1,0] op_sel_hi:[1,0,1] neg_lo:[0,1,0]\n"
      "ds_write_addtid_b32 v246 offset:5984\n"
      "v_pk_fma_f16 v168, v120, v124, v168 op_sel:[1,1,0] op_sel_hi:[1,0,1]\n"
      "v_pk_fma_f16 v169, v121, v125, v169 op_sel:[1,1,0] op_sel_hi:[1,0,1]\n"
      "v_pk_fma_f16 v170, v122, v126, v170 op_sel:[1,1,0] op_sel_hi:[1,0,1]\n"
      "v_pk_fma_f16 v171, v123, v127, v171 op_sel:[1,1,0] op_sel_hi:[1,0,1]\n"
      "ds_read_b128 v[112:115], v20 offset:224\n"
      "ds_read_b128 v[116:119], v178 offset:224\n"
      "v_mfma_f32_32x32x16_f16 v[180:195], v[92:95], v[168:171], v[180:195]\n"
      "v_pk_fma_f16 v248, v163, v163, v248 op_sel:[1,1,0] op_sel_hi:[1,0,1] neg_lo:[0,1,0]\n"
      "ds_write_addtid_b32 v247 offset:6256\n"
      "ds_write_addtid_b32 v248 offset:2448\n"
      "s_waitcnt lgkmcnt(7)\n"
      "v_pk_mul_f16 v164, v104, v108 op_sel_hi:[0,1] neg_hi:[0,1]\n"
      "v_pk_mul_f16 v165, v105, v109 op_sel_hi:[0,1] neg_hi:[0,1]\n"
      "v_pk_mul_f16 v166, v106, v110 op_sel_hi:[0,1] neg_hi:[0,1]\n"
      "v_pk_mul_f16 v167, v107, v111 op_sel_hi:[0,1] neg_hi:[0,1]\n"
      "v_pk_mul_f16 v249, v163, v160 op_sel_hi:[0,1]\n"
      "v_pk_mul_f16 v250, v163, v161 op_sel_hi:[0,1]\n"
      "v_pk_mul_f16 v251, v163, v162 op_sel_hi:[0,1]\n"
      "v_pk_fma_f16 v164, v104, v108, v164 op_sel:[1,1,0] op_sel_hi:[1,0,1]\n"
      "v_pk_fma_f16 v165, v105, v109, v165 op_sel:[1,1,0] op_sel_hi:[1,0,1]\n"
      "v_pk_fma_f16 v166, v106, v110, v166 op_sel:[1,1,0] op_sel_hi:[1,0,1]\n"
      "v_pk_fma_f16 v167, v107, v111, v167 op_sel:[1,1,0] op_sel_hi:[1,0,1]\n"
      "ds_read_b128 v[120:123], v20 offset:3264\n"
      "ds_read_b128 v[124:127], v178 offset:3264\n"
      "v_mfma_f32_32x32x16_f16 v[180:195], v[96:99], v[164:167], v[180:195]\n"
      "ds_write_addtid_b32 v162 offset:5712\n"
      "v_pk_fma_f16 v249, v163, v160, v249 op_sel:[1,1,0] op_sel_hi:[1,0,1] neg_lo:[0,1,0]\n"
      "ds_write_addtid_b32 v163 offset:4352\n"
      "s_waitcnt lgkmcnt(6)\n"
      "v_pk_mul_f16 v168, v112, v116 op_sel_hi:[0,1] neg_hi:[0,1]\n"
      "v_pk_mul_f16 v169, v113, v117 op_sel_hi:[0,1] neg_hi:[0,1]\n"
      "v_pk_mul_f16 v170, v114, v118 op_sel_hi:[0,1] neg_hi:[0,1]\n"
      "v_pk_mul_f16 v171, v115, v119 op_sel_hi:[0,1] neg_hi:[0,1]\n"
      "v_pk_fma_f16 v250, v163, v161, v250 op_sel:[1,1,0] op_sel_hi:[1,0,1] neg_lo:[0,1,0]\n"
      "ds_write_addtid_b32 v249 offset:4624\n"
      "v_pk_fma_f16 v251, v163, v162, v251 op_sel:[1,1,0] op_sel_hi:[1,0,1] neg_lo:[0,1,0]\n"
      "v_pk_fma_f16 v168, v112, v116, v168 op_sel:[1,1,0] op_sel_hi:[1,0,1]\n"
      "v_pk_fma_f16 v169, v113, v117, v169 op_sel:[1,1,0] op_sel_hi:[1,0,1]\n"
      "v_pk_fma_f16 v170, v114, v118, v170 op_sel:[1,1,0] op_sel_hi:[1,0,1]\n"
      "v_pk_fma_f16 v171, v115, v119, v171 op_sel:[1,1,0] op_sel_hi:[1,0,1]\n"
      "ds_read_b128 v[104:107], v20 offset:3296\n"
      "ds_read_b128 v[108:111], v178 offset:3296\n"
      "v_mfma_f32_32x32x16_f16 v[180:195], v[100:103], v[168:171], v[180:195]\n"
      "ds_write_addtid_b32 v250 offset:4896\n"
      "ds_write_addtid_b32 v251 offset:5168\n"
      "s_waitcnt lgkmcnt(7)\n"
      "v_pk_mul_f16 v164, v120, v124 op_sel_hi:[0,1] neg_hi:[0,1]\n"
      "v_pk_mul_f16 v165, v121, v125 op_sel_hi:[0,1] neg_hi:[0,1]\n"
      "v_pk_mul_f16 v166, v122, v126 op_sel_hi:[0,1] neg_hi:[0,1]\n"
      "v_pk_mul_f16 v167, v123, v127 op_sel_hi:[0,1] neg_hi:[0,1]\n"
      "ds_read_b128 v[72:75], v21 offset:0\n"
      "ds_read_b128 v[76:79], v21 offset:32\n"
      "ds_read_b128 v[80:83], v21 offset:64\n"
      "ds_read_b128 v[84:87], v21 offset:96\n"
      "ds_read_b128 v[88:91], v21 offset:128\n"
      "ds_read_b128 v[92:95], v21 offset:160\n"
      "ds_read_b128 v[96:99], v21 offset:192\n"
      "ds_read_b128 v[100:103], v21 offset:224\n"
      "v_pk_fma_f16 v164, v120, v124, v164 op_sel:[1,1,0] op_sel_hi:[1,0,1]\n"
      "v_pk_fma_f16 v165, v121, v125, v165 op_sel:[1,1,0] op_sel_hi:[1,0,1]\n"
      "v_pk_fma_f16 v166, v122, v126, v166 op_sel:[1,1,0] op_sel_hi:[1,0,1]\n"
      "v_pk_fma_f16 v167, v123, v127, v167 op_sel:[1,1,0] op_sel_hi:[1,0,1]\n"
      "ds_read_b128 v[112:115], v20 offset:3328\n"
      "ds_read_b128 v[116:119], v178 offset:3328\n"
      "s_setprio 1\n"
      "s_waitcnt lgkmcnt(9)\n"
      "v_mfma_f32_32x32x16_f16 v[180:195], v[72:75], v[164:167], v[180:195]\n"
      "v_pk_mul_f16 v168, v104, v108 op_sel_hi:[0,1] neg_hi:[0,1]\n"
      "v_pk_mul_f16 v169, v105, v109 op_sel_hi:[0,1] neg_hi:[0,1]\n"
      "v_pk_mul_f16 v170, v106, v110 op_sel_hi:[0,1] neg_hi:[0,1]\n"
      "v_pk_mul_f16 v171, v107, v111 op_sel_hi:[0,1] neg_hi:[0,1]\n"
      "v_pk_fma_f16 v168, v104, v108, v168 op_sel:[1,1,0] op_sel_hi:[1,0,1]\n"
      "v_pk_fma_f16 v169, v105, v109, v169 op_sel:[1,1,0] op_sel_hi:[1,0,1]\n"
      "v_pk_fma_f16 v170, v106, v110, v170 op_sel:[1,1,0] op_sel_hi:[1,0,1]\n"
      "v_pk_fma_f16 v171, v107, v111, v171 op_sel:[1,1,0] op_sel_hi:[1,0,1]\n"
      "ds_read_b128 v[120:123], v20 offset:3360\n"
      "ds_read_b128 v[124:127], v178 offset:3360\n"
      "buffer_load_dwordx4 v[52:55], v41, s[20:23], s60 offen offset:2048 nt\n"
      "s_waitcnt lgkmcnt(10)\n"
      "v_mfma_f32_32x32x16_f16 v[180:195], v[76:79], v[168:171], v[180:195]\n"
      "s_waitcnt lgkmcnt(2)\n"
      "v_pk_mul_f16 v164, v112, v116 op_sel_hi:[0,1] neg_hi:[0,1]\n"
      "v_pk_mul_f16 v165, v113, v117 op_sel_hi:[0,1] neg_hi:[0,1]\n"
      "v_pk_mul_f16 v166, v114, v118 op_sel_hi:[0,1] neg_hi:[0,1]\n"
      "v_pk_mul_f16 v167, v115, v119 op_sel_hi:[0,1] neg_hi:[0,1]\n"
      "v_pk_fma_f16 v164, v112, v116, v164 op_sel:[1,1,0] op_sel_hi:[1,0,1]\n"
      "v_pk_fma_f16 v165, v113, v117, v165 op_sel:[1,1,0] op_sel_hi:[1,0,1]\n"
      "v_pk_fma_f16 v166, v114, v118, v166 op_sel:[1,1,0] op_sel_hi:[1,0,1]\n"
      "v_pk_fma_f16 v167, v115, v119, v167 op_sel:[1,1,0] op_sel_hi:[1,0,1]\n"
      "ds_read_b128 v[104:107], v20 offset:3392\n"
      "ds_read_b128 v[108:111], v178 offset:3392\n"
      "v_mfma_f32_32x32x16_f16 v[180:195], v[80:83], v[164:167], v[180:195]\n"
      "s_waitcnt lgkmcnt(2)\n"
      "v_pk_mul_f16 v168, v120, v124 op_sel_hi:[0,1] neg_hi:[0,1]\n"
      "v_pk_mul_f16 v169, v121, v125 op_sel_hi:[0,1] neg_hi:[0,1]\n"
      "v_pk_mul_f16 v170, v122, v126 op_sel_hi:[0,1] neg_hi:[0,1]\n"
      "v_pk_mul_f16 v171, v123, v127 op_sel_hi:[0,1] neg_hi:[0,1]\n"
      "v_pk_fma_f16 v168, v120, v124, v168 op_sel:[1,1,0] op_sel_hi:[1,0,1]\n"
      "v_pk_fma_f16 v169, v121, v125, v169 op_sel:[1,1,0] op_sel_hi:[1,0,1]\n"
      "v_pk_fma_f16 v170, v122, v126, v170 op_sel:[1,1,0] op_sel_hi:[1,0,1]\n"
      "v_pk_fma_f16 v171, v123, v127, v171 op_sel:[1,1,0] op_sel_hi:[1,0,1]\n"
      "ds_read_b128 v[112:115], v20 offset:3424\n"
      "ds_read_b128 v[116:119], v178 offset:3424\n"
      "v_mfma_f32_32x32x16_f16 v[180:195], v[84:87], v[168:171], v[180:195]\n"
      "s_waitcnt lgkmcnt(2)\n"
      "v_pk_mul_f16 v164, v104, v108 op_sel_hi:[0,1] neg_hi:[0,1]\n"
      "v_pk_mul_f16 v165, v105, v109 op_sel_hi:[0,1] neg_hi:[0,1]\n"
      "v_pk_mul_f16 v166, v106, v110 op_sel_hi:[0,1] neg_hi:[0,1]\n"
      "v_pk_mul_f16 v167, v107, v111 op_sel_hi:[0,1] neg_hi:[0,1]\n"
      "v_pk_fma_f16 v164, v104, v108, v164 op_sel:[1,1,0] op_sel_hi:[1,0,1]\n"
      "v_pk_fma_f16 v165, v105, v109, v165 op_sel:[1,1,0] op_sel_hi:[1,0,1]\n"
      "v_pk_fma_f16 v166, v106, v110, v166 op_sel:[1,1,0] op_sel_hi:[1,0,1]\n"
      "v_pk_fma_f16 v167, v107, v111, v167 op_sel:[1,1,0] op_sel_hi:[1,0,1]\n"
      "ds_read_b128 v[120:123], v20 offset:3456\n"
      "ds_read_b128 v[124:127], v178 offset:3456\n"
      "v_mfma_f32_32x32x16_f16 v[180:195], v[88:91], v[164:167], v[180:195]\n"
      "s_waitcnt lgkmcnt(2)\n"
      "v_pk_mul_f16 v168, v112, v116 op_sel_hi:[0,1] neg_hi:[0,1]\n"
      "v_pk_mul_f16 v169, v113, v117 op_sel_hi:[0,1] neg_hi:[0,1]\n"
      "v_pk_mul_f16 v170, v114, v118 op_sel_hi:[0,1] neg_hi:[0,1]\n"
      "v_pk_mul_f16 v171, v115, v119 op_sel_hi:[0,1] neg_hi:[0,1]\n"
      "v_pk_fma_f16 v168, v112, v116, v168 op_sel:[1,1,0] op_sel_hi:[1,0,1]\n"
      "v_pk_fma_f16 v169, v113, v117, v169 op_sel:[1,1,0] op_sel_hi:[1,0,1]\n"
      "v_pk_fma_f16 v170, v114, v118, v170 op_sel:[1,1,0] op_sel_hi:[1,0,1]\n"
      "v_pk_fma_f16 v171, v115, v119, v171 op_sel:[1,1,0] op_sel_hi:[1,0,1]\n"
      "ds_read_b128 v[104:107], v20 offset:3488\n"
      "ds_read_b128 v[108:111], v178 offset:3488\n"
      "v_mov_b32 v140, 0\n"
      "v_mov_b32 v141, 0\n"
      "v_mov_b32 v142, 0\n"
      "v_mov_b32 v143, 0\n"
      "ds_write_b128 v27, v[140:143] \n"
      "ds_write_b32 v28, v140 \n"
      "v_mfma_f32_32x32x16_f16 v[180:195], v[92:95], v[168:171], v[180:195]\n"
      "s_waitcnt lgkmcnt(2)\n"
      "v_pk_mul_f16 v164, v120, v124 op_sel_hi:[0,1] neg_hi:[0,1]\n"
      "v_pk_mul_f16 v165, v121, v125 op_sel_hi:[0,1] neg_hi:[0,1]\n"
      "v_pk_mul_f16 v166, v122, v126 op_sel_hi:[0,1] neg_hi:[0,1]\n"
      "v_pk_mul_f16 v167, v123, v127 op_sel_hi:[0,1] neg_hi:[0,1]\n"
      "v_pk_fma_f16 v164, v120, v124, v164 op_sel:[1,1,0] op_sel_hi:[1,0,1]\n"
      "v_pk_fma_f16 v165, v121, v125, v165 op_sel:[1,1,0] op_sel_hi:[1,0,1]\n"
      "v_pk_fma_f16 v166, v122, v126, v166 op_sel:[1,1,0] op_sel_hi:[1,0,1]\n"
      "v_pk_fma_f16 v167, v123, v127, v167 op_sel:[1,1,0] op_sel_hi:[1,0,1]\n"
      "s_nop 1\n"
      "v_mfma_f32_32x32x16_f16 v[180:195], v[96:99], v[164:167], v[180:195]\n"
      "s_waitcnt lgkmcnt(0)\n"
      "v_pk_mul_f16 v168, v104, v108 op_sel_hi:[0,1] neg_hi:[0,1]\n"
      "v_pk_mul_f16 v169, v105, v109 op_sel_hi:[0,1] neg_hi:[0,1]\n"
      "v_pk_mul_f16 v170, v106, v110 op_sel_hi:[0,1] neg_hi:[0,1]\n"
      "v_pk_mul_f16 v171, v107, v111 op_sel_hi:[0,1] neg_hi:[0,1]\n"
      "v_pk_fma_f16 v168, v104, v108, v168 op_sel:[1,1,0] op_sel_hi:[1,0,1]\n"
      "v_pk_fma_f16 v169, v105, v109, v169 op_sel:[1,1,0] op_sel_hi:[1,0,1]\n"
      "v_pk_fma_f16 v170, v106, v110, v170 op_sel:[1,1,0] op_sel_hi:[1,0,1]\n"
      "v_pk_fma_f16 v171, v107, v111, v171 op_sel:[1,1,0] op_sel_hi:[1,0,1]\n"
      "s_nop 1\n"
      "v_mfma_f32_32x32x16_f16 v[180:195], v[100:103], v[168:171], v[180:195]\n"
      "s_cmp_lt_u32 s33, 4\n"
      "s_cbranch_scc1 .Lct2%=\n"
      "buffer_load_dword v11, v15, s[20:23], s60 offen\n"
      ".Lct2%=:\n"
      "s_nop 10\n"
      "v_add_f32 v180, v180, v31\n"
      "buffer_load_dwordx4 v[64:67], v42, s[16:19], s62 offen sc1\n"
      "v_cvt_pk_f16_f32 v222, v187, v186\n"
      "v_cvt_pk_f16_f32 v223, v185, v184\n"
      "v_cvt_pk_f16_f32 v225, v186, v185\n"
      "v_cvt_pk_f16_f32 v226, v188, v189\n"
      "ds_write_b64 v22, v[222:223] offset:2110\n"
      "v_cvt_pk_f16_f32 v228, v192, v192\n"
      "ds_write_b16 v22, v222 \n"
      "v_cvt_pk_f16_f32 v220, v183, v182\n"
      "ds_write_b32 v22, v225 offset:2\n"
      "v_cvt_pk_f16_f32 v221, v181, v180\n"
      "ds_write_b16_d16_hi v22, v223 offset:6\n"
      "v_cvt_pk_f16_f32 v224, v182, v181\n"
      "v_readlane_b32 s64, v226, 0\n"
      "v_readlane_b32 s65, v226, 32\n"
      "v_readlane_b32 s66, v228, 0\n"
      "ds_write_b64 v22, v[220:221] offset:510\n"
      "v_cvt_pk_f16_f32 v227, v190, v191\n"
      "ds_write_b16 v22, v220 offset:2620\n"
      "v_mov_b32 v229, s65\n"
      "ds_write_b32 v22, v224 offset:2622\n"
      "v_writelane_b32 v229, s64, 8\n"
      "v_writelane_b32 v229, s64, 9\n"
      "ds_write_b16_d16_hi v22, v221 offset:2626\n"
      "v_writelane_b32 v229, s66, 10\n"
      "v_writelane_b32 v229, s66, 11\n"
      "s_pack_ll_b32_b16 s64, s64, s64\n"
      "ds_write_b16 v26, v229 \n"
      "v_add_u32 v230, 0x240, v29\n"
      "s_setprio 0\n"
      "v_and_b32 v44, s64, v44\n"
      "v_and_b32 v45, s64, v45\n"
      "v_and_b32 v46, s64, v46\n"
      "v_and_b32 v47, s64, v47\n"
      "ds_write_b16 v23, v226 \n"
      "ds_write_b16 v23, v226 offset:2110\n"
      "ds_write_b16_d16_hi v24, v226 \n"
      "ds_write_b16_d16_hi v24, v226 offset:2110\n"
      "ds_write_b16 v25, v227 offset:32\n"
      "ds_write_b16 v25, v227 offset:2142\n"
      "ds_write_b16_d16_hi v25, v227 \n"
      "ds_write_b16_d16_hi v25, v227 offset:2110\n"
      "ds_read2_b32 v[108:109], v230 offset0:0 offset1:1\n"
      "ds_read2_b32 v[110:111], v230 offset0:2 offset1:3\n"
      "ds_read2_b32 v[76:77], v29 offset0:16 offset1:17\n"
      "ds_read2_b32 v[78:79], v29 offset0:18 offset1:19\n"
      "ds_read2_b32 v[112:113], v230 offset0:16 offset1:17\n"
      "ds_read2_b32 v[114:115], v230 offset0:18 offset1:19\n"
      "ds_read2_b32 v[80:81], v29 offset0:32 offset1:33\n"
      "ds_read2_b32 v[82:83], v29 offset0:34 offset1:35\n"
      "ds_read2_b32 v[116:117], v230 offset0:32 offset1:33\n"
      "ds_read2_b32 v[118:119], v230 offset0:34 offset1:35\n"
      "ds_read2_b32 v[84:85], v29 offset0:48 offset1:49\n"
      "ds_read2_b32 v[86:87], v29 offset0:50 offset1:51\n"
      "s_movk_i32 s59, 0x7fff\n"
      "s_waitcnt vmcnt(0)\n"
      "s_nop 0\n"
      ".Lcsgk%=:\n"
      "v_cmp_ne_u32 s[64:65], s15, v65\n"
      "v_cmp_ne_u32 vcc, s15, v67\n"
      "s_nop 0\n"
      "s_or_b64 vcc, vcc, s[64:65]\n"
      "s_cbranch_scc1 .Lcsgsk%=\n"
      ".Lcsgok%=:\n"
      "v_and_b32 v196, 0xffff0000, v64\n"
      "v_lshlrev_b32 v198, 16, v64\n"
      "v_rcp_f32 v196, v196\n"
      "v_and_b32 v197, 0xffff0000, v66\n"
      "v_lshlrev_b32 v199, 16, v66\n"
      "v_rcp_f32 v197, v197\n"
      "v_mul_f32 v198, -v198, v196\n"
      "v_mul_f32 v199, -v199, v197\n"
      "ds_read2_b32 v[120:121], v230 offset0:48 offset1:49\n"
      "ds_read2_b32 v[122:123], v230 offset0:50 offset1:51\n"
      "v_lshlrev_b32 v204, 16, v48\n"
      "v_lshlrev_b32 v205, 16, v52\n"
      "v_and_b32 v206, 0xffff0000, v48\n"
      "v_and_b32 v207, 0xffff0000, v52\n"
      "v_pk_fma_f32 v[204:205], v[204:205], v[196:197], v[198:199]\n"
      "v_pk_fma_f32 v[206:207], v[206:207], v[196:197], v[198:199]\n"
      "v_cvt_pk_f16_f32 v204, v204, v205\n"
      "v_cvt_pk_f16_f32 v206, v206, v207\n"
      "ds_read2_b32 v[88:89], v29 offset0:64 offset1:65\n"
      "ds_read2_b32 v[90:91], v29 offset0:66 offset1:67\n"
      "ds_write2st64_b32 v32, v204, v206 offset0:0 offset1:19\n"
      "v_lshlrev_b32 v208, 16, v49\n"
      "v_lshlrev_b32 v209, 16, v53\n"
      "v_and_b32 v210, 0xffff0000, v49\n"
      "v_and_b32 v211, 0xffff0000, v53\n"
      "v_pk_fma_f32 v[208:209], v[208:209], v[196:197], v[198:199]\n"
      "v_pk_fma_f32 v[210:211], v[210:211], v[196:197], v[198:199]\n"
      "v_cvt_pk_f16_f32 v208, v208, v209\n"
      "ds_read2_b32 v[124:125], v230 offset0:64 offset1:65\n"
      "ds_read2_b32 v[126:127], v230 offset0:66 offset1:67\n"
      "v_cvt_pk_f16_f32 v210, v210, v211\n"
      "ds_write2st64_b32 v32, v208, v210 offset0:38 offset1:57\n"
      "v_lshlrev_b32 v204, 16, v50\n"
      "v_lshlrev_b32 v205, 16, v54\n"
      "v_and_b32 v206, 0xffff0000, v50\n"
      "v_and_b32 v207, 0xffff0000, v54\n"
      "v_pk_fma_f32 v[204:205], v[204:205], v[196:197], v[198:199]\n"
      "v_pk_fma_f32 v[206:207], v[206:207], v[196:197], v[198:199]\n"
      "ds_read2_b32 v[92:93], v29 offset0:80 offset1:81\n"
      "ds_read2_b32 v[94:95], v29 offset0:82 offset1:83\n"
      "v_cvt_pk_f16_f32 v204, v204, v205\n"
      "v_cvt_pk_f16_f32 v206, v206, v207\n"
      "ds_write2st64_b32 v32, v204, v206 offset0:76 offset1:95\n"
      "v_lshlrev_b32 v208, 16, v51\n"
      "v_lshlrev_b32 v209, 16, v55\n"
      "v_and_b32 v210, 0xffff0000, v51\n"
      "v_and_b32 v211, 0xffff0000, v55\n"
      "v_pk_fma_f32 v[208:209], v[208:209], v[196:197], v[198:199]\n"
      "ds_read2_b32 v[128:129], v230 offset0:80 offset1:81\n"
      "ds_read2_b32 v[130:131], v230 offset0:82 offset1:83\n"
      "v_pk_fma_f32 v[210:211], v[210:211], v[196:197], v[198:199]\n"
      "v_cvt_pk_f16_f32 v208, v208, v209\n"
      "v_cvt_pk_f16_f32 v210, v210, v211\n"
      "ds_write2st64_b32 v32, v208, v210 offset0:114 offset1:133\n"
      "s_mov_b32 s54, s41\n"
      "s_add_u32 s55, s41, 0x100000\n"
      "s_add_u32 s56, s41, 0x200000\n"
      "s_add_u32 s57, s41, 0x300000\n"
      "ds_read2_b32 v[96:97], v29 offset0:96 offset1:97\n"
      "ds_read2_b32 v[98:99], v29 offset0:98 offset1:99\n"
      "s_mov_b32 s58, 0x40000000\n"
      "v_lshrrev_b32 v14, 9, v8\n"
      "v_add_u32 v14, 0x27600, v14\n"
      "s_lshr_b32 s13, s33, 1\n"
      "ds_read2_b32 v[132:133], v230 offset0:96 offset1:97\n"
      "ds_read2_b32 v[134:135], v230 offset0:98 offset1:99\n"
      "ds_read2_b32 v[100:101], v29 offset0:112 offset1:113\n"
      "ds_read2_b32 v[102:103], v29 offset0:114 offset1:115\n"
      "ds_read2_b32 v[136:137], v230 offset0:112 offset1:113\n"
      "ds_read2_b32 v[138:139], v230 offset0:114 offset1:115\n"
      "ds_read2_b32 v[104:105], v29 offset0:128 offset1:129\n"
      "ds_read2_b32 v[106:107], v29 offset0:130 offset1:131\n"
      "ds_read2_b32 v[72:73], v29 offset0:0 offset1:1\n"
      "ds_read2_b32 v[74:75], v29 offset0:2 offset1:3\n"
      "s_waitcnt lgkmcnt(12)\n"
      ".Lccv%=:\n"
      "s_barrier\n"
      "s_setprio 3\n"
      "ds_read_b128 v[216:219], v33 offset:64\n"
      "ds_read_b128 v[220:223], v33 offset:128\n"
      "ds_read_b128 v[224:227], v33 offset:192\n"
      "ds_read_b128 v[212:215], v33 offset:256\n"
      "v_permlane32_swap_b32 v48, v52\n"
      "v_permlane32_swap_b32 v49, v53\n"
      "v_permlane32_swap_b32 v50, v54\n"
      "v_permlane32_swap_b32 v51, v55\n"
      "s_cmp_lg_u32 s33, 0\n"
      "s_cbranch_scc1 .Lca%=\n"
      "v_mov_b32 v16, s15\n"
      "v_mov_b32 v17, s15\n"
      "buffer_store_dwordx2 v[16:17], off, s[16:19], s42 sc1\n"
      ".Lca%=:\n"
      "s_waitcnt lgkmcnt(2)\n"
      "v_mfma_f32_16x16x32_f16 v[180:183], v[108:111], v[216:219], 0\n"
      "v_lshlrev_b32 v29, 2, v43\n"
      "v_add_u32 v29, 0x27600, v29\n"
      "ds_read_b32 v255, v29 \n"
      "ds_read_b128 v[236:239], v34 offset:0\n"
      "ds_read_b128 v[240:243], v34 offset:16\n"
      "v_mfma_f32_16x16x32_f16 v[184:187], v[76:79], v[216:219], 0\n"
      "ds_read2_b32 v[140:141], v30 offset0:16 offset1:17\n"
      "ds_read_b128 v[216:219], v33 offset:320\n"
      "v_mfma_f32_16x16x32_f16 v[180:183], v[112:115], v[220:223], v[180:183]\n"
      "v_mfma_f32_16x16x32_f16 v[184:187], v[80:83], v[220:223], v[184:187]\n"
      "ds_read2_b32 v[142:143], v30 offset0:64 offset1:65\n"
      "ds_read_b128 v[220:223], v33 offset:384\n"
      "s_waitcnt lgkmcnt(7)\n"
      "v_mfma_f32_16x16x32_f16 v[180:183], v[116:119], v[224:227], v[180:183]\n"
      "s_waitcnt lgkmcnt(5)\n"
      "v_alignbit_b32 v245, v238, v237, 16\n"
      "v_alignbit_b32 v246, v239, v238, 16\n"
      "v_mfma_f32_16x16x32_f16 v[184:187], v[84:87], v[224:227], v[184:187]\n"
      "ds_read2_b32 v[144:145], v30 offset0:8 offset1:9\n"
      "ds_read_b128 v[224:227], v33 offset:448\n"
      "s_waitcnt lgkmcnt(4)\n"
      "v_alignbit_b32 v247, v240, v239, 16\n"
      "v_alignbit_b32 v248, v241, v240, 16\n"
      "v_mfma_f32_16x16x32_f16 v[180:183], v[120:123], v[212:215], v[180:183]\n"
      "v_alignbit_b32 v249, v242, v241, 16\n"
      "v_pk_add_f16 v16, v245, v246\n"
      "v_mfma_f32_16x16x32_f16 v[184:187], v[88:91], v[212:215], v[184:187]\n"
      "ds_read2_b32 v[146:147], v30 offset0:56 offset1:57\n"
      "ds_read_b128 v[212:215], v33 offset:512\n"
      "v_pk_add_f16 v17, v246, v247\n"
      "v_pk_add_f16 v18, v247, v248\n"
      "v_mfma_f32_16x16x32_f16 v[180:183], v[124:127], v[216:219], v[180:183]\n"
      "v_pk_add_f16 v19, v248, v249\n"
      "v_pk_add_f16 v20, v16, v238\n"
      "v_mfma_f32_16x16x32_f16 v[184:187], v[92:95], v[216:219], v[184:187]\n"
      "ds_read2_b32 v[148:149], v30 offset0:0 offset1:1\n"
      "v_pk_add_f16 v21, v17, v239\n"
      "v_pk_add_f16 v22, v18, v240\n"
      "s_waitcnt lgkmcnt(3)\n"
      "v_mfma_f32_16x16x32_f16 v[180:183], v[128:131], v[220:223], v[180:183]\n"
      "v_pk_add_f16 v23, v19, v241\n"
      "ds_write_b128 v37, v[20:23] offset:0\n"
      "v_pk_add_f16 v16, v237, v239\n"
      "v_mfma_f32_16x16x32_f16 v[184:187], v[96:99], v[220:223], v[184:187]\n"
      "ds_read2_b32 v[150:151], v30 offset0:48 offset1:49\n"
      "ds_read_b128 v[216:219], v33 offset:576\n"
      "v_pk_add_f16 v17, v238, v240\n"
      "v_pk_add_f16 v18, v239, v241\n"
      "v_mfma_f32_16x16x32_f16 v[180:183], v[132:135], v[224:227], v[180:183]\n"
      "v_pk_add_f16 v19, v240, v242\n"
      "v_pk_add_f16 v20, v20, v16\n"
      "v_mfma_f32_16x16x32_f16 v[184:187], v[100:103], v[224:227], v[184:187]\n"
      "ds_read2_b32 v[152:153], v30 offset0:40 offset1:41\n"
      "ds_read_b128 v[220:223], v33 offset:640\n"
      "v_pk_add_f16 v21, v21, v17\n"
      "v_pk_add_f16 v22, v22, v18\n"
      "s_waitcnt lgkmcnt(4)\n"
      "v_mfma_f32_16x16x32_f16 v[180:183], v[136:139], v[212:215], v[180:183]\n"
      "v_pk_add_f16 v23, v23, v19\n"
      "ds_write_b128 v37, v[20:23] offset:3104\n"
      "v_alignbit_b32 v244, v237, v236, 16\n"
      "v_mfma_f32_16x16x32_f16 v[184:187], v[104:107], v[212:215], v[184:187]\n"
      "ds_read2_b32 v[154:155], v30 offset0:88 offset1:89\n"
      "ds_read_b128 v[224:227], v33 offset:704\n"
      "v_alignbit_b32 v250, v243, v242, 16\n"
      "v_pk_add_f16 v16, v244, v247\n"
      "v_mfma_f32_16x16x32_f16 v[188:191], v[72:75], v[212:215], 0\n"
      "v_pk_add_f16 v17, v245, v248\n"
      "v_pk_add_f16 v18, v246, v249\n"
      "v_mfma_f32_16x16x32_f16 v[196:199], v[44:47], v[212:215], 0\n"
      "ds_read2_b32 v[156:157], v30 offset0:32 offset1:33\n"
      "ds_read_b128 v[212:215], v33 offset:768\n"
      "v_pk_add_f16 v19, v247, v250\n"
      "v_pk_add_f16 v20, v20, v16\n"
      "s_waitcnt lgkmcnt(4)\n"
      "v_mfma_f32_16x16x32_f16 v[184:187], v[108:111], v[216:219], v[184:187]\n"
      "v_cvt_pk_bf16_f32 v24, v180, v182\n"
      "v_cvt_pk_bf16_f32 v25, v181, v183\n"
      "ds_write2st64_b32 v39, v24, v25 offset0:0 offset1:8\n"
      "v_mfma_f32_16x16x32_f16 v[188:191], v[76:79], v[216:219], v[188:191]\n"
      "ds_read2_b32 v[158:159], v30 offset0:80 offset1:81\n"
      "ds_read_b128 v[216:219], v33 offset:832\n"
      "v_pk_add_f16 v21, v21, v17\n"
      "v_pk_add_f16 v22, v22, v18\n"
      "v_mfma_f32_16x16x32_f16 v[184:187], v[112:115], v[220:223], v[184:187]\n"
      "buffer_load_dwordx4 v[56:59], v41, s[20:23], s61 offen nt\n"
      "buffer_load_dwordx4 v[68:71], v42, s[16:19], s63 offen sc1\n"
      "v_pk_add_f16 v23, v23, v19\n"
      "ds_write_b128 v38, v[20:23] offset:0\n"
      "v_pk_add_f16 v16, v236, v240\n"
      "v_mfma_f32_16x16x32_f16 v[188:191], v[80:83], v[220:223], v[188:191]\n"
      "ds_read2_b32 v[160:161], v30 offset0:24 offset1:25\n"
      "ds_read_b128 v[220:223], v33 offset:896\n"
      "v_pk_add_f16 v17, v237, v241\n"
      "v_pk_add_f16 v18, v238, v242\n"
      "s_waitcnt lgkmcnt(3)\n"
      "v_mfma_f32_16x16x32_f16 v[184:187], v[116:119], v[224:227], v[184:187]\n"
      "v_pk_add_f16 v19, v239, v243\n"
      "v_pk_add_f16 v20, v20, v16\n"
      "s_setprio 2\n"
      "v_mfma_f32_16x16x32_f16 v[188:191], v[84:87], v[224:227], v[188:191]\n"
      "ds_read2_b32 v[162:163], v30 offset0:72 offset1:73\n"
      "ds_read_b128 v[224:227], v33 offset:960\n"
      "v_pk_add_f16 v21, v21, v17\n"
      "v_pk_add_f16 v22, v22, v18\n"
      "ds_read_b128 v[236:239], v34 offset:1024\n"
      "ds_read_b128 v[240:243], v34 offset:1040\n"
      "v_mfma_f32_16x16x32_f16 v[184:187], v[120:123], v[212:215], v[184:187]\n"
      "v_pk_add_f16 v23, v23, v19\n"
      "ds_write_b128 v38, v[20:23] offset:2080\n"
      "v_mfma_f32_16x16x32_f16 v[188:191], v[88:91], v[212:215], v[188:191]\n"
      "ds_read2_b32 v[164:165], v30 offset0:128 offset1:129\n"
      "ds_read_b128 v[212:215], v33 offset:1024\n"
      "v_mfma_f32_16x16x32_f16 v[184:187], v[124:127], v[216:219], v[184:187]\n"
      "v_mfma_f32_16x16x32_f16 v[188:191], v[92:95], v[216:219], v[188:191]\n"
      "s_waitcnt lgkmcnt(4)\n"
      "v_alignbit_b32 v245, v238, v237, 16\n"
      "v_alignbit_b32 v246, v239, v238, 16\n"
      "v_mfma_f32_16x16x32_f16 v[184:187], v[128:131], v[220:223], v[184:187]\n"
      "s_waitcnt lgkmcnt(3)\n"
      "v_alignbit_b32 v247, v240, v239, 16\n"
      "v_alignbit_b32 v248, v241, v240, 16\n"
      "v_mfma_f32_16x16x32_f16 v[188:191], v[96:99], v[220:223], v[188:191]\n"
      "ds_read2_b32 v[166:167], v30 offset0:168 offset1:169\n"
      "ds_read_b64_tr_b16 v[228:229], v35 offset:32\n"
      "ds_read_b64_tr_b16 v[230:231], v35 offset:16\n"
      "v_alignbit_b32 v249, v242, v241, 16\n"
      "v_pk_add_f16 v16, v245, v246\n"
      "v_mfma_f32_16x16x32_f16 v[184:187], v[132:135], v[224:227], v[184:187]\n"
      "v_pk_add_f16 v17, v246, v247\n"
      "v_pk_add_f16 v18, v247, v248\n"
      "v_mfma_f32_16x16x32_f16 v[188:191], v[100:103], v[224:227], v[188:191]\n"
      "v_pk_add_f16 v19, v248, v249\n"
      "v_pk_add_f16 v20, v16, v238\n"
      "s_waitcnt lgkmcnt(3)\n"
      "v_mfma_f32_16x16x32_f16 v[184:187], v[136:139], v[212:215], v[184:187]\n"
      "ds_read_b128 v[216:219], v33 offset:1088\n"
      "buffer_load_dwordx4 v[60:63], v41, s[20:23], s61 offen offset:2048 nt\n"
      "v_pk_add_f16 v21, v17, v239\n"
      "v_pk_add_f16 v22, v18, v240\n"
      "v_mfma_f32_16x16x32_f16 v[188:191], v[104:107], v[212:215], v[188:191]\n"
      "ds_read2_b32 v[168:169], v30 offset0:120 offset1:121\n"
      "v_pk_add_f16 v23, v19, v241\n"
      "ds_write_b128 v37, v[20:23] offset:1024\n"
      "v_pk_add_f16 v16, v237, v239\n"
      "v_mfma_f32_16x16x32_f16 v[192:195], v[72:75], v[212:215], 0\n"
      "ds_read_b128 v[220:223], v33 offset:1152\n"
      "v_pk_add_f16 v17, v238, v240\n"
      "v_pk_add_f16 v18, v239, v241\n"
      "v_mfma_f32_16x16x32_f16 v[200:203], v[44:47], v[212:215], 0\n"
      "v_pk_add_f16 v19, v240, v242\n"
      "v_pk_add_f16 v20, v20, v16\n"
      "s_waitcnt lgkmcnt(3)\n"
      "v_mfma_f32_16x16x32_f16 v[188:191], v[228:231], v[140:143], v[188:191]\n"
      "ds_read_b128 v[224:227], v33 offset:1216\n"
      "v_cvt_pk_bf16_f32 v24, v184, v186\n"
      "v_cvt_pk_bf16_f32 v25, v185, v187\n"
      "ds_write2st64_b32 v39, v24, v25 offset0:1 offset1:9\n"
      "v_mfma_f32_16x16x32_f16 v[192:195], v[228:231], v[144:147], v[192:195]\n"
      "ds_read2_b32 v[170:171], v30 offset0:160 offset1:161\n"
      "v_pk_add_f16 v21, v21, v17\n"
      "v_pk_add_f16 v22, v22, v18\n"
      "v_mfma_f32_16x16x32_f16 v[196:199], v[228:231], v[148:151], v[196:199]\n"
      "ds_read_b64_tr_b16 v[232:233], v35 offset:3128\n"
      "ds_read_b64_tr_b16 v[234:235], v35 offset:3112\n"
      "v_pk_add_f16 v23, v23, v19\n"
      "ds_write_b128 v37, v[20:23] offset:4128\n"
      "v_alignbit_b32 v244, v237, v236, 16\n"
      "v_mfma_f32_16x16x32_f16 v[188:191], v[108:111], v[216:219], v[188:191]\n"
      "v_alignbit_b32 v250, v243, v242, 16\n"
      "v_pk_add_f16 v16, v244, v247\n"
      "v_mfma_f32_16x16x32_f16 v[192:195], v[76:79], v[216:219], v[192:195]\n"
      "v_pk_add_f16 v17, v245, v248\n"
      "v_pk_add_f16 v18, v246, v249\n"
      "s_waitcnt lgkmcnt(3)\n"
      "v_mfma_f32_16x16x32_f16 v[188:191], v[112:115], v[220:223], v[188:191]\n"
      "ds_read2_b32 v[172:173], v30 offset0:112 offset1:113\n"
      "ds_read_b128 v[212:215], v33 offset:1280\n"
      "v_pk_add_f16 v19, v247, v250\n"
      "v_pk_add_f16 v20, v20, v16\n"
      "v_mfma_f32_16x16x32_f16 v[192:195], v[80:83], v[220:223], v[192:195]\n"
      "v_pk_add_f16 v21, v21, v17\n"
      "v_pk_add_f16 v22, v22, v18\n"
      "v_mfma_f32_16x16x32_f16 v[188:191], v[116:119], v[224:227], v[188:191]\n"
      "ds_read_b128 v[216:219], v33 offset:1344\n"
      "v_pk_add_f16 v23, v23, v19\n"
      "ds_write_b128 v38, v[20:23] offset:1024\n"
      "v_pk_add_f16 v16, v236, v240\n"
      "v_mfma_f32_16x16x32_f16 v[192:195], v[84:87], v[224:227], v[192:195]\n"
      "v_pk_add_f16 v17, v237, v241\n"
      "v_pk_add_f16 v18, v238, v242\n"
      "s_setprio 1\n"
      "s_waitcnt lgkmcnt(2)\n"
      "v_mfma_f32_16x16x32_f16 v[188:191], v[232:235], v[152:155], v[188:191]\n"
      "ds_read2_b32 v[174:175], v30 offset0:152 offset1:153\n"
      "ds_read_b128 v[220:223], v33 offset:1408\n"
      "v_pk_add_f16 v19, v239, v243\n"
      "v_pk_add_f16 v20, v20, v16\n"
      "v_mfma_f32_16x16x32_f16 v[192:195], v[232:235], v[156:159], v[192:195]\n"
      "v_pk_add_f16 v21, v21, v17\n"
      "v_pk_add_f16 v22, v22, v18\n"
      "v_mfma_f32_16x16x32_f16 v[196:199], v[232:235], v[160:163], v[196:199]\n"
      "ds_read_b128 v[224:227], v33 offset:1472\n"
      "v_pk_add_f16 v23, v23, v19\n"
      "ds_write_b128 v38, v[20:23] offset:3104\n"
      "v_mfma_f32_16x16x32_f16 v[188:191], v[120:123], v[212:215], v[188:191]\n"
      "v_readlane_b32 s72, v255, 0\n"
      "v_mfma_f32_16x16x32_f16 v[192:195], v[88:91], v[212:215], v[192:195]\n"
      "ds_read2_b32 v[176:177], v30 offset0:104 offset1:105\n"
      "ds_read_b128 v[212:215], v33 offset:1536\n"
      "v_readlane_b32 s73, v255, 1\n"
      "s_waitcnt lgkmcnt(4)\n"
      "v_mfma_f32_16x16x32_f16 v[188:191], v[124:127], v[216:219], v[188:191]\n"
      "v_readlane_b32 s74, v255, 2\n"
      "v_mfma_f32_16x16x32_f16 v[192:195], v[92:95], v[216:219], v[192:195]\n"
      "v_readlane_b32 s75, v255, 3\n"
      "v_mfma_f32_16x16x32_f16 v[188:191], v[128:131], v[220:223], v[188:191]\n"
      "v_readlane_b32 s76, v255, 4\n"
      "v_mfma_f32_16x16x32_f16 v[192:195], v[96:99], v[220:223], v[192:195]\n"
      "ds_read2_b32 v[178:179], v30 offset0:144 offset1:145\n"
      "ds_read_b64_tr_b16 v[228:229], v35 offset:544\n"
      "ds_read_b64_tr_b16 v[230:231], v35 offset:528\n"
      "v_readlane_b32 s77, v255, 5\n"
      "s_waitcnt lgkmcnt(3)\n"
      "v_mfma_f32_16x16x32_f16 v[188:191], v[132:135], v[224:227], v[188:191]\n"
      "v_readlane_b32 s78, v255, 6\n"
      "v_mfma_f32_16x16x32_f16 v[192:195], v[100:103], v[224:227], v[192:195]\n"
      "v_readlane_b32 s79, v255, 7\n"
      "v_mfma_f32_16x16x32_f16 v[188:191], v[136:139], v[212:215], v[188:191]\n"
      "ds_read_b128 v[216:219], v33 offset:1600\n"
      "s_movk_i32 s59, 0x7fff\n"
      "s_waitcnt vmcnt(1)\n"
      "s_nop 0\n"
      ".Lcsgh%=:\n"
      "v_cmp_ne_u32 s[64:65], s15, v69\n"
      "v_cmp_ne_u32 vcc, s15, v71\n"
      "s_nop 0\n"
      "s_or_b64 vcc, vcc, s[64:65]\n"
      "s_cbranch_scc1 .Lcsgsh%=\n"
      ".Lcsgoh%=:\n"
      "v_mov_b32 v42, v8\n"
      "v_mfma_f32_16x16x32_f16 v[192:195], v[104:107], v[212:215], v[192:195]\n"
      "v_and_b32 v26, 0xffff0000, v68\n"
      "v_lshlrev_b32 v28, 16, v68\n"
      "v_mfma_f32_16x16x32_f16 v[196:199], v[72:75], v[212:215], v[196:199]\n"
      "ds_read_b128 v[220:223], v33 offset:1664\n"
      "v_rcp_f32 v26, v26\n"
      "v_and_b32 v27, 0xffff0000, v70\n"
      "v_mfma_f32_16x16x32_f16 v[204:207], v[44:47], v[212:215], 0\n"
      "v_lshlrev_b32 v29, 16, v70\n"
      "v_rcp_f32 v27, v27\n"
      "s_waitcnt lgkmcnt(1)\n"
      "v_mfma_f32_16x16x32_f16 v[192:195], v[228:231], v[140:143], v[192:195]\n"
      "ds_read_b128 v[224:227], v33 offset:1728\n"
      "v_cvt_pk_bf16_f32 v24, v188, v190\n"
      "v_cvt_pk_bf16_f32 v25, v189, v191\n"
      "ds_write2st64_b32 v39, v24, v25 offset0:2 offset1:10\n"
      "v_mfma_f32_16x16x32_f16 v[196:199], v[228:231], v[144:147], v[196:199]\n"
      "v_mul_f32 v28, -v28, v26\n"
      "v_mul_f32 v29, -v29, v27\n"
      "v_mfma_f32_16x16x32_f16 v[200:203], v[228:231], v[148:151], v[200:203]\n"
      "ds_read_b64_tr_b16 v[232:233], v35 offset:3640\n"
      "ds_read_b64_tr_b16 v[234:235], v35 offset:3624\n"
      "v_lshlrev_b32 v180, 16, v56\n"
      "s_waitcnt vmcnt(0)\n"
      "v_lshlrev_b32 v181, 16, v60\n"
      "v_mfma_f32_16x16x32_f16 v[192:195], v[108:111], v[216:219], v[192:195]\n"
      "v_and_b32 v182, 0xffff0000, v56\n"
      "v_and_b32 v183, 0xffff0000, v60\n"
      "v_mfma_f32_16x16x32_f16 v[196:199], v[76:79], v[216:219], v[196:199]\n"
      "v_fma_f32 v180, v180, v26, v28\n"
      "v_fma_f32 v181, v181, v27, v29\n"
      "s_setprio 0\n"
      "s_waitcnt lgkmcnt(2)\n"
      "v_mfma_f32_16x16x32_f16 v[192:195], v[112:115], v[220:223], v[192:195]\n"
      "ds_read_b128 v[212:215], v33 offset:1792\n"
      "v_fma_f32 v182, v182, v26, v28\n"
      "v_fma_f32 v183, v183, v27, v29\n"
      "v_mfma_f32_16x16x32_f16 v[196:199], v[80:83], v[220:223], v[196:199]\n"
      "v_cvt_pk_f16_f32 v180, v180, v181\n"
      "v_cvt_pk_f16_f32 v182, v182, v183\n"
      "ds_write2st64_b32 v32, v180, v182 offset0:8 offset1:27\n"
      "v_mfma_f32_16x16x32_f16 v[192:195], v[116:119], v[224:227], v[192:195]\n"
      "ds_read_b128 v[216:219], v33 offset:1856\n"
      "v_lshlrev_b32 v184, 16, v57\n"
      "v_lshlrev_b32 v185, 16, v61\n"
      "v_mfma_f32_16x16x32_f16 v[196:199], v[84:87], v[224:227], v[196:199]\n"
      "v_and_b32 v186, 0xffff0000, v57\n"
      "v_and_b32 v187, 0xffff0000, v61\n"
      "s_waitcnt lgkmcnt(2)\n"
      "v_mfma_f32_16x16x32_f16 v[192:195], v[232:235], v[152:155], v[192:195]\n"
      "ds_read_b128 v[220:223], v33 offset:1920\n"
      "v_fma_f32 v184, v184, v26, v28\n"
      "v_fma_f32 v185, v185, v27, v29\n"
      "v_mfma_f32_16x16x32_f16 v[196:199], v[232:235], v[156:159], v[196:199]\n"
      "v_fma_f32 v186, v186, v26, v28\n"
      "v_fma_f32 v187, v187, v27, v29\n"
      "s_cmp_lg_u32 s33, 0\n"
      "s_cbranch_scc1 .Lcg%=\n"
      "buffer_load_dwordx2 v[252:253], v9, s[16:19], s43 offen sc1\n"
      ".Lcg%=:\n"
      "v_mfma_f32_16x16x32_f16 v[200:203], v[232:235], v[160:163], v[200:203]\n"
      "ds_read_b128 v[224:227], v33 offset:1984\n"
      "v_cvt_pk_f16_f32 v184, v184, v185\n"
      "v_cvt_pk_f16_f32 v186, v186, v187\n"
      "ds_write2st64_b32 v32, v184, v186 offset0:46 offset1:65\n"
      "v_mfma_f32_16x16x32_f16 v[192:195], v[120:123], v[212:215], v[192:195]\n"
      "v_lshlrev_b32 v180, 16, v58\n"
      "v_lshlrev_b32 v181, 16, v62\n"
      "v_mfma_f32_16x16x32_f16 v[196:199], v[88:91], v[212:215], v[196:199]\n"
      "ds_read_b128 v[212:215], v33 offset:2048\n"
      "v_and_b32 v182, 0xffff0000, v58\n"
      "v_and_b32 v183, 0xffff0000, v62\n"
      "s_waitcnt lgkmcnt(3)\n"
      "v_mfma_f32_16x16x32_f16 v[192:195], v[124:127], v[216:219], v[192:195]\n"
      "v_fma_f32 v180, v180, v26, v28\n"
      "v_fma_f32 v181, v181, v27, v29\n"
      "v_mfma_f32_16x16x32_f16 v[196:199], v[92:95], v[216:219], v[196:199]\n"
      "v_fma_f32 v182, v182, v26, v28\n"
      "v_fma_f32 v183, v183, v27, v29\n"
      "v_mfma_f32_16x16x32_f16 v[192:195], v[128:131], v[220:223], v[192:195]\n"
      "v_cvt_pk_f16_f32 v180, v180, v181\n"
      "v_cvt_pk_f16_f32 v182, v182, v183\n"
      "ds_write2st64_b32 v32, v180, v182 offset0:84 offset1:103\n"
      "v_mfma_f32_16x16x32_f16 v[196:199], v[96:99], v[220:223], v[196:199]\n"
      "ds_read_b64_tr_b16 v[228:229], v36 offset:24\n"
      "ds_read_b64_tr_b16 v[230:231], v36 offset:2088\n"
      "v_lshlrev_b32 v184, 16, v59\n"
      "v_lshlrev_b32 v185, 16, v63\n"
      "s_waitcnt lgkmcnt(3)\n"
      "v_mfma_f32_16x16x32_f16 v[192:195], v[132:135], v[224:227], v[192:195]\n"
      "v_and_b32 v186, 0xffff0000, v59\n"
      "v_and_b32 v187, 0xffff0000, v63\n"
      "v_mfma_f32_16x16x32_f16 v[196:199], v[100:103], v[224:227], v[196:199]\n"
      "v_fma_f32 v184, v184, v26, v28\n"
      "v_fma_f32 v185, v185, v27, v29\n"
      "v_mfma_f32_16x16x32_f16 v[192:195], v[136:139], v[212:215], v[192:195]\n"
      "v_fma_f32 v186, v186, v26, v28\n"
      "v_fma_f32 v187, v187, v27, v29\n"
      "v_mfma_f32_16x16x32_f16 v[196:199], v[104:107], v[212:215], v[196:199]\n"
      "ds_read_b64_tr_b16 v[232:233], v36 offset:536\n"
      "ds_read_b64_tr_b16 v[234:235], v36 offset:2600\n"
      "v_cvt_pk_f16_f32 v184, v184, v185\n"
      "v_cvt_pk_f16_f32 v186, v186, v187\n"
      "ds_write2st64_b32 v32, v184, v186 offset0:122 offset1:141\n"
      "v_mfma_f32_16x16x32_f16 v[200:203], v[72:75], v[212:215], v[200:203]\n"
      "v_permlane32_swap_b32 v56, v60\n"
      "v_permlane32_swap_b32 v57, v61\n"
      "v_mfma_f32_16x16x32_f16 v[208:211], v[44:47], v[212:215], 0\n"
      "v_permlane32_swap_b32 v58, v62\n"
      "v_permlane32_swap_b32 v59, v63\n"
      "s_waitcnt lgkmcnt(3)\n"
      "v_mfma_f32_16x16x32_f16 v[196:199], v[228:231], v[164:167], v[196:199]\n"
      "v_cvt_pk_bf16_f32 v24, v192, v194\n"
      "v_cvt_pk_bf16_f32 v25, v193, v195\n"
      "ds_write2st64_b32 v39, v24, v25 offset0:3 offset1:11\n"
      "v_mfma_f32_16x16x32_f16 v[200:203], v[228:231], v[168:171], v[200:203]\n"
      "v_mfma_f32_16x16x32_f16 v[204:207], v[228:231], v[172:175], v[204:207]\n"
      "v_mfma_f32_16x16x32_f16 v[208:211], v[228:231], v[176:179], v[208:211]\n"
      "ds_read_b64_tr_b16 v[228:229], v35 offset:1056\n"
      "ds_read_b64_tr_b16 v[230:231], v35 offset:1040\n"
      "s_cmp_eq_u32 s33, 0\n"
      "s_cbranch_scc1 .Lch%=\n"
      ".Lcho%=:\n"
      "s_waitcnt lgkmcnt(2)\n"
      "s_barrier\n"
      "s_setprio 3\n"
      "v_mfma_f32_16x16x32_f16 v[200:203], v[232:235], v[164:167], v[200:203]\n"
      "ds_read_b64_tr_b16 v[64:65], v40 offset:0\n"
      "ds_read_b64_tr_b16 v[66:67], v40 offset:35072\n"
      "v_lshlrev_b32 v68, 16, v48\n"
      "v_and_b32 v69, 0xffff0000, v48\n"
      "v_mfma_f32_16x16x32_f16 v[204:207], v[232:235], v[168:171], v[204:207]\n"
      "v_lshlrev_b32 v70, 16, v49\n"
      "v_and_b32 v71, 0xffff0000, v49\n"
      "ds_read_b128 v[236:239], v34 offset:2048\n"
      "ds_read_b128 v[240:243], v34 offset:2064\n"
      "ds_read_b32 v12, v14 offset:256\n"
      "v_mfma_f32_16x16x32_f16 v[208:211], v[232:235], v[172:175], v[208:211]\n"
      "ds_read_b128 v[216:219], v33 offset:2112\n"
      "ds_read_b64_tr_b16 v[232:233], v35 offset:4152\n"
      "ds_read_b64_tr_b16 v[234:235], v35 offset:4136\n"
      "v_lshlrev_b32 v26, 16, v50\n"
      "v_and_b32 v27, 0xffff0000, v50\n"
      "s_waitcnt lgkmcnt(6)\n"
      "v_mfma_f32_16x16x32_f16 v[196:199], v[228:231], v[140:143], v[196:199]\n"
      "v_lshlrev_b32 v28, 16, v51\n"
      "v_and_b32 v29, 0xffff0000, v51\n"
      "v_mfma_f32_16x16x32_f16 v[200:203], v[228:231], v[144:147], v[200:203]\n"
      "ds_read_b128 v[220:223], v33 offset:2176\n"
      "v_lshlrev_b32 v48, 16, v64\n"
      "v_and_b32 v64, 0xffff0000, v64\n"
      "v_mfma_f32_16x16x32_f16 v[204:207], v[228:231], v[148:151], v[204:207]\n"
      "v_fmac_f32 v68, s72, v48\n"
      "v_fmac_f32 v69, s73, v64\n"
      "s_waitcnt lgkmcnt(1)\n"
      "v_mfma_f32_16x16x32_f16 v[196:199], v[232:235], v[152:155], v[196:199]\n"
      "ds_read_b128 v[224:227], v33 offset:2240\n"
      "v_lshlrev_b32 v49, 16, v65\n"
      "v_and_b32 v65, 0xffff0000, v65\n"
      "v_mfma_f32_16x16x32_f16 v[200:203], v[232:235], v[156:159], v[200:203]\n"
      "v_fmac_f32 v70, s74, v49\n"
      "v_fmac_f32 v71, s75, v65\n"
      "v_mfma_f32_16x16x32_f16 v[204:207], v[232:235], v[160:163], v[204:207]\n"
      "ds_read_b128 v[212:215], v33 offset:2304\n"
      "v_lshlrev_b32 v50, 16, v66\n"
      "v_and_b32 v66, 0xffff0000, v66\n"
      "v_mfma_f32_16x16x32_f16 v[196:199], v[108:111], v[216:219], v[196:199]\n"
      "v_fmac_f32 v26, s76, v50\n"
      "v_fmac_f32 v27, s77, v66\n"
      "v_mfma_f32_16x16x32_f16 v[200:203], v[76:79], v[216:219], v[200:203]\n"
      "ds_read_b64_tr_b16 v[228:229], v35 offset:1568\n"
      "ds_read_b64_tr_b16 v[230:231], v35 offset:1552\n"
      "v_lshlrev_b32 v51, 16, v67\n"
      "v_and_b32 v67, 0xffff0000, v67\n"
      "s_waitcnt lgkmcnt(3)\n"
      "v_mfma_f32_16x16x32_f16 v[196:199], v[112:115], v[220:223], v[196:199]\n"
      "v_fmac_f32 v28, s78, v51\n"
      "v_fmac_f32 v29, s79, v67\n"
      "v_mfma_f32_16x16x32_f16 v[200:203], v[80:83], v[220:223], v[200:203]\n"
      "v_cvt_pk_bf16_f32 v48, v68, v69\n"
      "v_cvt_pk_bf16_f32 v49, v70, v71\n"
      "v_mfma_f32_16x16x32_f16 v[196:199], v[116:119], v[224:227], v[196:199]\n"
      "ds_read_b128 v[216:219], v33 offset:2368\n"
      "v_cvt_pk_bf16_f32 v50, v26, v27\n"
      "v_cvt_pk_bf16_f32 v51, v28, v29\n"
      "s_setprio 2\n"
      "v_mfma_f32_16x16x32_f16 v[200:203], v[84:87], v[224:227], v[200:203]\n"
      "v_lshlrev_b32 v13, 24, v12\n"
      "v_and_or_b32 v13, v13, s58, v42\n"
      "s_waitcnt lgkmcnt(1)\n"
      "v_mfma_f32_16x16x32_f16 v[196:199], v[120:123], v[212:215], v[196:199]\n"
      "ds_read_b128 v[220:223], v33 offset:2432\n"
      "s_cmp_lg_u32 s13, 0\n"
      "s_cbranch_scc1 .Lcs00%=\n"
      "buffer_store_dwordx4 v[48:51], v13, s[24:27], s54 offen\n"
      ".Lcs00%=:\n"
      "v_alignbit_b32 v245, v238, v237, 16\n"
      "v_alignbit_b32 v246, v239, v238, 16\n"
      "v_mfma_f32_16x16x32_f16 v[200:203], v[88:91], v[212:215], v[200:203]\n"
      "v_alignbit_b32 v247, v240, v239, 16\n"
      "v_alignbit_b32 v248, v241, v240, 16\n"
      "v_mfma_f32_16x16x32_f16 v[200:203], v[228:231], v[140:143], v[200:203]\n"
      "ds_read_b128 v[224:227], v33 offset:2496\n"
      "s_cmp_lg_u32 s13, 1\n"
      "s_cbranch_scc1 .Lcs01%=\n"
      "buffer_store_dwordx4 v[48:51], v13, s[24:27], s54 offen\n"
      ".Lcs01%=:\n"
      "v_alignbit_b32 v249, v242, v241, 16\n"
      "v_pk_add_f16 v16, v245, v246\n"
      "v_mfma_f32_16x16x32_f16 v[204:207], v[228:231], v[144:147], v[204:207]\n"
      "v_pk_add_f16 v17, v246, v247\n"
      "v_pk_add_f16 v18, v247, v248\n"
      "v_mfma_f32_16x16x32_f16 v[208:211], v[228:231], v[148:151], v[208:211]\n"
      "ds_read_b128 v[212:215], v33 offset:2560\n"
      "s_cmp_lg_u32 s13, 2\n"
      "s_cbranch_scc1 .Lcs02%=\n"
      "buffer_store_dwordx4 v[48:51], v13, s[24:27], s54 offen\n"
      ".Lcs02%=:\n"
      "v_pk_add_f16 v19, v248, v249\n"
      "v_pk_add_f16 v20, v16, v238\n"
      "s_waitcnt lgkmcnt(1)\n"
      "v_mfma_f32_16x16x32_f16 v[196:199], v[124:127], v[216:219], v[196:199]\n"
      "v_pk_add_f16 v21, v17, v239\n"
      "v_pk_add_f16 v22, v18, v240\n"
      "v_mfma_f32_16x16x32_f16 v[200:203], v[92:95], v[216:219], v[200:203]\n"
      "s_cmp_lg_u32 s13, 3\n"
      "s_cbranch_scc1 .Lcs03%=\n"
      "buffer_store_dwordx4 v[48:51], v13, s[24:27], s54 offen\n"
      ".Lcs03%=:\n"
      "v_pk_add_f16 v23, v19, v241\n"
      "ds_write_b128 v37, v[20:23] offset:2048\n"
      "v_pk_add_f16 v16, v237, v239\n"
      "v_mfma_f32_16x16x32_f16 v[196:199], v[128:131], v[220:223], v[196:199]\n"
      "ds_read_b64_tr_b16 v[232:233], v35 offset:4664\n"
      "ds_read_b64_tr_b16 v[234:235], v35 offset:4648\n"
      "ds_read_b64_tr_b16 v[64:65], v40 offset:512\n"
      "ds_read_b64_tr_b16 v[66:67], v40 offset:35584\n"
      "v_lshlrev_b32 v68, 16, v52\n"
      "v_and_b32 v69, 0xffff0000, v52\n"
      "v_mfma_f32_16x16x32_f16 v[200:203], v[96:99], v[220:223], v[200:203]\n"
      "v_lshlrev_b32 v70, 16, v53\n"
      "v_and_b32 v71, 0xffff0000, v53\n"
      "v_mfma_f32_16x16x32_f16 v[196:199], v[132:135], v[224:227], v[196:199]\n"
      "v_lshlrev_b32 v26, 16, v54\n"
      "v_and_b32 v27, 0xffff0000, v54\n"
      "v_mfma_f32_16x16x32_f16 v[200:203], v[100:103], v[224:227], v[200:203]\n"
      "ds_read_b128 v[216:219], v33 offset:2624\n"
      "v_lshlrev_b32 v28, 16, v55\n"
      "v_and_b32 v29, 0xffff0000, v55\n"
      "s_waitcnt lgkmcnt(1)\n"
      "v_mfma_f32_16x16x32_f16 v[196:199], v[136:139], v[212:215], v[196:199]\n"
      "v_lshlrev_b32 v52, 16, v64\n"
      "v_and_b32 v64, 0xffff0000, v64\n"
      "v_mfma_f32_16x16x32_f16 v[200:203], v[104:107], v[212:215], v[200:203]\n"
      "ds_read_b128 v[220:223], v33 offset:2688\n"
      "v_fmac_f32 v68, s72, v52\n"
      "v_fmac_f32 v69, s73, v64\n"
      "s_setprio 1\n"
      "v_mfma_f32_16x16x32_f16 v[204:207], v[72:75], v[212:215], v[204:207]\n"
      "v_lshlrev_b32 v53, 16, v65\n"
      "v_and_b32 v65, 0xffff0000, v65\n"
      "v_mfma_f32_16x16x32_f16 v[200:203], v[232:235], v[152:155], v[200:203]\n"
      "ds_read_b128 v[224:227], v33 offset:2752\n"
      "v_fmac_f32 v70, s74, v53\n"
      "v_fmac_f32 v71, s75, v65\n"
      "v_mfma_f32_16x16x32_f16 v[204:207], v[232:235], v[156:159], v[204:207]\n"
      "v_cvt_pk_bf16_f32 v24, v196, v198\n"
      "v_cvt_pk_bf16_f32 v25, v197, v199\n"
      "ds_write2st64_b32 v39, v24, v25 offset0:4 offset1:12\n"
      "v_mfma_f32_16x16x32_f16 v[208:211], v[232:235], v[160:163], v[208:211]\n"
      "ds_read_b128 v[212:215], v33 offset:2816\n"
      "v_lshlrev_b32 v54, 16, v66\n"
      "v_and_b32 v66, 0xffff0000, v66\n"
      "s_waitcnt lgkmcnt(3)\n"
      "v_mfma_f32_16x16x32_f16 v[200:203], v[108:111], v[216:219], v[200:203]\n"
      "v_fmac_f32 v26, s76, v54\n"
      "v_fmac_f32 v27, s77, v66\n"
      "v_mfma_f32_16x16x32_f16 v[204:207], v[76:79], v[216:219], v[204:207]\n"
      "ds_read_b128 v[216:219], v33 offset:2880\n"
      "v_lshlrev_b32 v55, 16, v67\n"
      "v_and_b32 v67, 0xffff0000, v67\n"
      "v_mfma_f32_16x16x32_f16 v[200:203], v[112:115], v[220:223], v[200:203]\n"
      "v_fmac_f32 v28, s78, v55\n"
      "v_fmac_f32 v29, s79, v67\n"
      "v_mfma_f32_16x16x32_f16 v[204:207], v[80:83], v[220:223], v[204:207]\n"
      "ds_read_b128 v[220:223], v33 offset:2944\n"
      "v_cvt_pk_bf16_f32 v52, v68, v69\n"
      "v_cvt_pk_bf16_f32 v53, v70, v71\n"
      "s_waitcnt lgkmcnt(2)\n"
      "v_mfma_f32_16x16x32_f16 v[200:203], v[116:119], v[224:227], v[200:203]\n"
      "v_cvt_pk_bf16_f32 v54, v26, v27\n"
      "v_cvt_pk_bf16_f32 v55, v28, v29\n"
      "v_mfma_f32_16x16x32_f16 v[204:207], v[84:87], v[224:227], v[204:207]\n"
      "ds_read_b128 v[224:227], v33 offset:3008\n"
      "v_lshlrev_b32 v13, 16, v12\n"
      "v_and_or_b32 v13, v13, s58, v42\n"
      "v_mfma_f32_16x16x32_f16 v[200:203], v[120:123], v[212:215], v[200:203]\n"
      "s_cmp_lg_u32 s13, 0\n"
      "s_cbranch_scc1 .Lcs10%=\n"
      "buffer_store_dwordx4 v[52:55], v13, s[24:27], s55 offen\n"
      ".Lcs10%=:\n"
      "v_pk_add_f16 v17, v238, v240\n"
      "v_pk_add_f16 v18, v239, v241\n"
      "s_setprio 0\n"
      "v_mfma_f32_16x16x32_f16 v[204:207], v[88:91], v[212:215], v[204:207]\n"
      "ds_read_b128 v[212:215], v33 offset:3072\n"
      "v_pk_add_f16 v19, v240, v242\n"
      "v_pk_add_f16 v20, v20, v16\n"
      "s_waitcnt lgkmcnt(1)\n"
      "v_mfma_f32_16x16x32_f16 v[200:203], v[124:127], v[216:219], v[200:203]\n"
      "s_cmp_lg_u32 s13, 1\n"
      "s_cbranch_scc1 .Lcs11%=\n"
      "buffer_store_dwordx4 v[52:55], v13, s[24:27], s55 offen\n"
      ".Lcs11%=:\n"
      "v_pk_add_f16 v21, v21, v17\n"
      "v_pk_add_f16 v22, v22, v18\n"
      "v_mfma_f32_16x16x32_f16 v[204:207], v[92:95], v[216:219], v[204:207]\n"
      "v_pk_add_f16 v23, v23, v19\n"
      "ds_write_b128 v37, v[20:23] offset:5152\n"
      "v_mfma_f32_16x16x32_f16 v[200:203], v[128:131], v[220:223], v[200:203]\n"
      "ds_read_b64_tr_b16 v[228:229], v36 offset:1048\n"
      "ds_read_b64_tr_b16 v[230:231], v36 offset:3112\n"
      "s_cmp_lg_u32 s13, 2\n"
      "s_cbranch_scc1 .Lcs12%=\n"
      "buffer_store_dwordx4 v[52:55], v13, s[24:27], s55 offen\n"
      ".Lcs12%=:\n"
      "v_mfma_f32_16x16x32_f16 v[204:207], v[96:99], v[220:223], v[204:207]\n"
      "v_mfma_f32_16x16x32_f16 v[200:203], v[132:135], v[224:227], v[200:203]\n"
      "ds_read_b128 v[216:219], v33 offset:3136\n"
      "s_cmp_lg_u32 s13, 3\n"
      "s_cbranch_scc1 .Lcs13%=\n"
      "buffer_store_dwordx4 v[52:55], v13, s[24:27], s55 offen\n"
      ".Lcs13%=:\n"
      "v_mfma_f32_16x16x32_f16 v[204:207], v[100:103], v[224:227], v[204:207]\n"
      "s_waitcnt lgkmcnt(1)\n"
      "v_mfma_f32_16x16x32_f16 v[200:203], v[136:139], v[212:215], v[200:203]\n"
      "ds_read_b128 v[220:223], v33 offset:3200\n"
      "v_mfma_f32_16x16x32_f16 v[204:207], v[104:107], v[212:215], v[204:207]\n"
      "v_mfma_f32_16x16x32_f16 v[208:211], v[72:75], v[212:215], v[208:211]\n"
      "ds_read_b128 v[224:227], v33 offset:3264\n"
      "v_mfma_f32_16x16x32_f16 v[204:207], v[228:231], v[164:167], v[204:207]\n"
      "v_mfma_f32_16x16x32_f16 v[208:211], v[228:231], v[168:171], v[208:211]\n"
      "ds_read_b64_tr_b16 v[232:233], v36 offset:1560\n"
      "ds_read_b64_tr_b16 v[234:235], v36 offset:3624\n"
      "v_cvt_pk_bf16_f32 v24, v200, v202\n"
      "v_cvt_pk_bf16_f32 v25, v201, v203\n"
      "ds_write2st64_b32 v39, v24, v25 offset0:5 offset1:13\n"
      "s_waitcnt lgkmcnt(5)\n"
      "v_mfma_f32_16x16x32_f16 v[204:207], v[108:111], v[216:219], v[204:207]\n"
      "ds_read_b128 v[212:215], v33 offset:3328\n"
      "v_mfma_f32_16x16x32_f16 v[208:211], v[76:79], v[216:219], v[208:211]\n"
      "s_waitcnt lgkmcnt(5)\n"
      "v_mfma_f32_16x16x32_f16 v[204:207], v[112:115], v[220:223], v[204:207]\n"
      "ds_read_b128 v[216:219], v33 offset:3392\n"
      "s_waitcnt lgkmcnt(2)\n"
      "s_barrier\n"
      "s_setprio 3\n"
      "v_mfma_f32_16x16x32_f16 v[208:211], v[80:83], v[220:223], v[208:211]\n"
      "ds_read_b64_tr_b16 v[64:65], v40 offset:1024\n"
      "ds_read_b64_tr_b16 v[66:67], v40 offset:36096\n"
      "v_lshlrev_b32 v68, 16, v56\n"
      "v_and_b32 v69, 0xffff0000, v56\n"
      "v_mfma_f32_16x16x32_f16 v[204:207], v[116:119], v[224:227], v[204:207]\n"
      "ds_read_b128 v[220:223], v33 offset:3456\n"
      "v_lshlrev_b32 v70, 16, v57\n"
      "v_and_b32 v71, 0xffff0000, v57\n"
      "v_mfma_f32_16x16x32_f16 v[208:211], v[84:87], v[224:227], v[208:211]\n"
      "v_lshlrev_b32 v26, 16, v58\n"
      "v_and_b32 v27, 0xffff0000, v58\n"
      "v_mfma_f32_16x16x32_f16 v[208:211], v[232:235], v[164:167], v[208:211]\n"
      "ds_read_b64_tr_b16 v[228:229], v35 offset:2080\n"
      "ds_read_b64_tr_b16 v[230:231], v35 offset:2064\n"
      "v_lshlrev_b32 v28, 16, v59\n"
      "v_and_b32 v29, 0xffff0000, v59\n"
      "s_waitcnt lgkmcnt(3)\n"
      "v_mfma_f32_16x16x32_f16 v[204:207], v[120:123], v[212:215], v[204:207]\n"
      "v_lshlrev_b32 v56, 16, v64\n"
      "v_and_b32 v64, 0xffff0000, v64\n"
      "v_mfma_f32_16x16x32_f16 v[208:211], v[88:91], v[212:215], v[208:211]\n"
      "ds_read_b128 v[224:227], v33 offset:3520\n"
      "v_fmac_f32 v68, s72, v56\n"
      "v_fmac_f32 v69, s73, v64\n"
      "v_mfma_f32_16x16x32_f16 v[204:207], v[124:127], v[216:219], v[204:207]\n"
      "v_lshlrev_b32 v57, 16, v65\n"
      "v_and_b32 v65, 0xffff0000, v65\n"
      "v_mfma_f32_16x16x32_f16 v[208:211], v[92:95], v[216:219], v[208:211]\n"
      "ds_read_b128 v[212:215], v33 offset:3584\n"
      "v_fmac_f32 v70, s74, v57\n"
      "v_fmac_f32 v71, s75, v65\n"
      "s_setprio 2\n"
      "s_waitcnt lgkmcnt(2)\n"
      "v_mfma_f32_16x16x32_f16 v[204:207], v[128:131], v[220:223], v[204:207]\n"
      "v_lshlrev_b32 v58, 16, v66\n"
      "v_and_b32 v66, 0xffff0000, v66\n"
      "v_mfma_f32_16x16x32_f16 v[208:211], v[96:99], v[220:223], v[208:211]\n"
      "ds_read_b128 v[216:219], v33 offset:3648\n"
      "v_fmac_f32 v26, s76, v58\n"
      "v_fmac_f32 v27, s77, v66\n"
      "v_mfma_f32_16x16x32_f16 v[204:207], v[228:231], v[140:143], v[204:207]\n"
      "ds_read_b64_tr_b16 v[232:233], v35 offset:5176\n"
      "ds_read_b64_tr_b16 v[234:235], v35 offset:5160\n"
      "v_lshlrev_b32 v59, 16, v67\n"
      "v_and_b32 v67, 0xffff0000, v67\n"
      "v_mfma_f32_16x16x32_f16 v[208:211], v[228:231], v[144:147], v[208:211]\n"
      "v_fmac_f32 v28, s78, v59\n"
      "v_fmac_f32 v29, s79, v67\n"
      "s_waitcnt lgkmcnt(3)\n"
      "v_mfma_f32_16x16x32_f16 v[204:207], v[132:135], v[224:227], v[204:207]\n"
      "ds_read_b128 v[220:223], v33 offset:3712\n"
      "v_cvt_pk_bf16_f32 v56, v68, v69\n"
      "v_cvt_pk_bf16_f32 v57, v70, v71\n"
      "v_mfma_f32_16x16x32_f16 v[208:211], v[100:103], v[224:227], v[208:211]\n"
      "ds_read_b128 v[224:227], v33 offset:3776\n"
      "v_cvt_pk_bf16_f32 v58, v26, v27\n"
      "v_cvt_pk_bf16_f32 v59, v28, v29\n"
      "v_mfma_f32_16x16x32_f16 v[204:207], v[136:139], v[212:215], v[204:207]\n"
      "v_lshlrev_b32 v13, 8, v12\n"
      "v_and_or_b32 v13, v13, s58, v42\n"
      "s_setprio 1\n"
      "v_mfma_f32_16x16x32_f16 v[208:211], v[104:107], v[212:215], v[208:211]\n"
      "ds_read_b64_tr_b16 v[228:229], v35 offset:2592\n"
      "ds_read_b64_tr_b16 v[230:231], v35 offset:2576\n"
      "ds_read_b128 v[212:215], v33 offset:3840\n"
      "s_cmp_lg_u32 s13, 0\n"
      "s_cbranch_scc1 .Lcs20%=\n"
      "buffer_store_dwordx4 v[56:59], v13, s[24:27], s56 offen\n"
      ".Lcs20%=:\n"
      "v_lshlrev_b32 v68, 16, v60\n"
      "v_and_b32 v69, 0xffff0000, v60\n"
      "s_waitcnt lgkmcnt(3)\n"
      "v_mfma_f32_16x16x32_f16 v[208:211], v[108:111], v[216:219], v[208:211]\n"
      "ds_read_b128 v[216:219], v33 offset:3904\n"
      "v_lshlrev_b32 v70, 16, v61\n"
      "v_and_b32 v71, 0xffff0000, v61\n"
      "v_mfma_f32_16x16x32_f16 v[204:207], v[232:235], v[152:155], v[204:207]\n"
      "s_cmp_lg_u32 s13, 1\n"
      "s_cbranch_scc1 .Lcs21%=\n"
      "buffer_store_dwordx4 v[56:59], v13, s[24:27], s56 offen\n"
      ".Lcs21%=:\n"
      "v_lshlrev_b32 v26, 16, v62\n"
      "v_and_b32 v27, 0xffff0000, v62\n"
      "v_mfma_f32_16x16x32_f16 v[208:211], v[232:235], v[156:159], v[208:211]\n"
      "ds_read_b64_tr_b16 v[232:233], v35 offset:5688\n"
      "ds_read_b64_tr_b16 v[234:235], v35 offset:5672\n"
      "v_lshlrev_b32 v28, 16, v63\n"
      "v_and_b32 v29, 0xffff0000, v63\n"
      "v_mfma_f32_16x16x32_f16 v[208:211], v[112:115], v[220:223], v[208:211]\n"
      "ds_read_b128 v[220:223], v33 offset:3968\n"
      "s_cmp_lg_u32 s13, 2\n"
      "s_cbranch_scc1 .Lcs22%=\n"
      "buffer_store_dwordx4 v[56:59], v13, s[24:27], s56 offen\n"
      ".Lcs22%=:\n"
      "s_setprio 0\n"
      "v_mfma_f32_16x16x32_f16 v[208:211], v[116:119], v[224:227], v[208:211]\n"
      "ds_read_b128 v[224:227], v33 offset:4032\n"
      "s_waitcnt lgkmcnt(3)\n"
      "v_mfma_f32_16x16x32_f16 v[208:211], v[120:123], v[212:215], v[208:211]\n"
      "ds_read_b128 v[212:215], v33 offset:4096\n"
      "v_cvt_pk_bf16_f32 v24, v204, v206\n"
      "v_cvt_pk_bf16_f32 v25, v205, v207\n"
      "ds_write2st64_b32 v39, v24, v25 offset0:6 offset1:14\n"
      "s_cmp_lg_u32 s13, 3\n"
      "s_cbranch_scc1 .Lcs23%=\n"
      "buffer_store_dwordx4 v[56:59], v13, s[24:27], s56 offen\n"
      ".Lcs23%=:\n"
      "v_mfma_f32_16x16x32_f16 v[208:211], v[228:231], v[140:143], v[208:211]\n"
      "v_mfma_f32_16x16x32_f16 v[208:211], v[124:127], v[216:219], v[208:211]\n"
      "s_waitcnt lgkmcnt(2)\n"
      "v_mfma_f32_16x16x32_f16 v[208:211], v[128:131], v[220:223], v[208:211]\n"
      "v_mfma_f32_16x16x32_f16 v[208:211], v[232:235], v[152:155], v[208:211]\n"
      "v_mfma_f32_16x16x32_f16 v[208:211], v[132:135], v[224:227], v[208:211]\n"
      "s_waitcnt lgkmcnt(1)\n"
      "v_mfma_f32_16x16x32_f16 v[208:211], v[136:139], v[212:215], v[208:211]\n"
      "s_nop 7\n"
      "v_cvt_pk_bf16_f32 v24, v208, v210\n"
      "v_cvt_pk_bf16_f32 v25, v209, v211\n"
      "ds_write2st64_b32 v39, v24, v25 offset0:7 offset1:15\n"
      "s_waitcnt lgkmcnt(0)\n"
      "s_barrier\n"
      "ds_read_b64_tr_b16 v[64:65], v40 offset:1536\n"
      "ds_read_b64_tr_b16 v[66:67], v40 offset:36608\n"
      "s_cmp_lg_u32 s33, 0\n"
      "s_cbranch_scc1 .Lcb%=\n"
      "ds_read_u16_d16_hi v252, v8 offset:56384\n"
      "ds_read_u16_d16_hi v253, v8 offset:65152\n"
      "s_waitcnt lgkmcnt(0)\n"
      "v_pk_mul_f32 v[252:253], v[252:253], v[254:255]\n"
      "v_cvt_pk_bf16_f32 v252, v252, v253\n"
      "buffer_atomic_pk_add_bf16 v252, v251, s[24:27], s41 offen\n"
      ".Lcb%=:\n"
      "s_waitcnt lgkmcnt(1)\n"
      "v_lshlrev_b32 v60, 16, v64\n"
      "v_and_b32 v64, 0xffff0000, v64\n"
      "v_fmac_f32 v68, s72, v60\n"
      "v_fmac_f32 v69, s73, v64\n"
      "v_lshlrev_b32 v61, 16, v65\n"
      "v_and_b32 v65, 0xffff0000, v65\n"
      "v_fmac_f32 v70, s74, v61\n"
      "v_fmac_f32 v71, s75, v65\n"
      "s_waitcnt lgkmcnt(0)\n"
      "v_lshlrev_b32 v62, 16, v66\n"
      "v_and_b32 v66, 0xffff0000, v66\n"
      "v_fmac_f32 v26, s76, v62\n"
      "v_fmac_f32 v27, s77, v66\n"
      "v_lshlrev_b32 v63, 16, v67\n"
      "v_and_b32 v67, 0xffff0000, v67\n"
      "v_fmac_f32 v28, s78, v63\n"
      "v_fmac_f32 v29, s79, v67\n"
      "v_cvt_pk_bf16_f32 v60, v68, v69\n"
      "v_cvt_pk_bf16_f32 v61, v70, v71\n"
      "v_cvt_pk_bf16_f32 v62, v26, v27\n"
      "v_cvt_pk_bf16_f32 v63, v28, v29\n"
      "v_and_or_b32 v13, v12, s58, v42\n"
      "buffer_store_dwordx4 v[60:63], v13, s[24:27], s57 offen\n"
      ".LEx%=:\ns_endpgm\n"
      ".Lcsgsk%=:\n"
      "s_sleep 4\n"
      "s_sub_u32 s59, s59, 1\n"
      "s_cbranch_scc1 .Lcsgok%=\n"
      "buffer_load_dwordx4 v[64:67], v42, s[16:19], s62 offen sc1\n"
      "s_waitcnt vmcnt(0)\n"
      "s_branch .Lcsgk%=\n"
      ".Lcsgsh%=:\n"
      "s_sleep 4\n"
      "s_sub_u32 s59, s59, 1\n"
      "s_cbranch_scc1 .Lcsgoh%=\n"
      "buffer_load_dwordx4 v[68:71], v42, s[16:19], s63 offen sc1\n"
      "s_waitcnt vmcnt(0)\n"
      "s_branch .Lcsgh%=\n"
      ".Lch%=:\n"
      "s_movk_i32 s59, 0x7fff\n"
      ".Lchl%=:\n"
      "s_waitcnt vmcnt(0)\n"
      "v_cmp_ne_u32 vcc, s15, v253\n"
      "v_not_b32 v255, v252\n"
      "s_sub_u32 s59, s59, 1\n"
      "s_cbranch_scc1 .Lchk%=\n"
      "s_cbranch_vccz .Lchk%=\n"
      "s_sleep 1\n"
      "buffer_load_dwordx2 v[252:253], v9, s[16:19], s43 offen sc1\n"
      "s_branch .Lchl%=\n"
      ".Lchk%=:\n"
      "v_and_b32 v31, 0x1ff, v255\n"
      "v_bfe_u32 v8, v255, 9, 2\n"
      "v_lshl_add_u32 v31, v31, 2, v8\n"
      "v_mov_b32 v8, 64\n"
      "v_add_u32 v31, 0x27700, v31\n"
      "ds_write_b8 v31, v8 \n"
      "v_and_b32 v31, 1, v255\n"
      "v_and_b32 v8, 0x7fe, v255\n"
      "v_lshl_add_u32 v8, v31, 11, v8\n"
      "v_mul_u32_u24 v31, 0x1120, v10\n"
      "v_add_u32 v8, v8, v31\n"
      "v_lshlrev_b32 v31, 1, v10\n"
      "v_add_u32 v31, 0x27600, v31\n"
      "v_and_b32 v255, 0x7ff, v255\n"
      "v_lshl_add_u32 v251, v255, 11, v10\n"
      "v_and_b32 v252, 32, v43\n"
      "v_lshl_or_b32 v251, v252, 25, v251\n"
      "ds_read_b64 v[254:255], v31 \n"
      "v_mov_b32 v252, 0\n"
      "v_mov_b32 v253, 0\n"
      "s_waitcnt lgkmcnt(0)\n"
      "s_branch .Lcho%=\n"
      ".LG%=:\n"
      "s_setprio 3\n"
      "s_mul_i32 s35, s48, 14\n"
      "s_add_u32 s35, s35, s49\n"
      "s_and_b32 s55, s33, 3\n"
      "s_mul_i32 s50, s55, 1835008\n"
      "s_lshr_b32 s51, s35, 1\n"
      "s_lshl_b32 s51, s51, 7\n"
      "s_add_u32 s50, s50, s51\n"
      "v_lshrrev_b32 v37, 3, v43\n"
      "v_and_b32 v38, 7, v43\n"
      "v_mul_u32_u24 v37, 0xe000, v37\n"
      "v_lshlrev_b32 v38, 4, v38\n"
      "v_add3_u32 v32, v37, v38, s50\n"
      "s_cmp_lt_u32 s34, 112\n"
      "s_cselect_b32 s26, 0x700000, 0\n"
      "s_mov_b32 s27, 0x20000\n"
      "s_bitcmp1_b32 s49, 0\n"
      "s_waitcnt lgkmcnt(0)\n"
      "s_load_dword s15, s[88:89], 0x0\n"
      "s_cselect_b32 s24, s82, s80\n"
      "s_cselect_b32 s25, s83, s81\n"
      "s_and_b32 s25, s25, 0xffff\n"
      "s_mov_b32 s16, s88\n"
      "s_and_b32 s17, s89, 0xffff\n"
      "s_mov_b32 s18, 0x6e0000\n"
      "s_mov_b32 s19, 0x20000\n"
      "s_mov_b32 s36, s86\n"
      "s_and_b32 s37, s87, 0xffff\n"
      "s_barrier\n"
      "s_sleep 20\n"
      "s_mov_b32 s60, 0\n"
      "buffer_load_dwordx4 v[64:67], v32, s[24:27], s60 offen nt\n"
      "s_sleep 2\n"
      "s_mov_b32 s60, 7168\n"
      "buffer_load_dwordx4 v[68:71], v32, s[24:27], s60 offen nt\n"
      "s_sleep 2\n"
      "s_mov_b32 s60, 14336\n"
      "buffer_load_dwordx4 v[72:75], v32, s[24:27], s60 offen nt\n"
      "s_sleep 2\n"
      "s_mov_b32 s60, 21504\n"
      "buffer_load_dwordx4 v[76:79], v32, s[24:27], s60 offen nt\n"
      "s_sleep 2\n"
      "s_mov_b32 s60, 28672\n"
      "buffer_load_dwordx4 v[80:83], v32, s[24:27], s60 offen nt\n"
      "s_sleep 2\n"
      "s_mov_b32 s60, 35840\n"
      "buffer_load_dwordx4 v[84:87], v32, s[24:27], s60 offen nt\n"
      "s_sleep 2\n"
      "s_mov_b32 s60, 43008\n"
      "buffer_load_dwordx4 v[88:91], v32, s[24:27], s60 offen nt\n"
      "s_sleep 2\n"
      "s_mov_b32 s60, 50176\n"
      "buffer_load_dwordx4 v[92:95], v32, s[24:27], s60 offen nt\n"
      "s_sleep 2\n"
      "s_mov_b32 s60, 458752\n"
      "buffer_load_dwordx4 v[96:99], v32, s[24:27], s60 offen nt\n"
      "s_sleep 2\n"
      "s_waitcnt lgkmcnt(0)\n"
      "s_add_u32 s15, s15, 0x9e3779b1\n"
      "v_mov_b32 v47, s15\n"
      "s_cmp_ge_u32 s34, 112\n"
      "s_cbranch_scc1 .LT%=\n"
      "v_mov_b32 v48, 0x22680\n"
      "ds_read_b32 v48, v48 \n"
      "s_waitcnt lgkmcnt(0)\n"
      "v_readfirstlane_b32 s12, v48\n"
      "s_mov_b32 s62, 0x05040100\n"
      "s_mov_b32 s63, 0x07060302\n"
      "s_lshl_b32 s42, s34, 3\n"
      "s_add_u32 s42, s42, 2048\n"
      "s_lshl_b32 s43, s48, 8\n"
      "s_and_b32 s50, s49, 1\n"
      "s_lshl_b32 s50, s50, 7\n"
      "s_add_u32 s43, s43, s50\n"
      "s_add_u32 s43, s43, 65536\n"
      "s_mov_b32 s20, s64\n"
      "s_and_b32 s21, s65, 0xffff\n"
      "s_mov_b32 s22, 0x400000\n"
      "s_mov_b32 s23, 0x20000\n"
      "v_lshrrev_b32 v39, 4, v43\n"
      "v_and_b32 v38, 15, v43\n"
      "v_lshlrev_b32 v38, 4, v38\n"
      "v_bfe_u32 v33, v43, 2, 2\n"
      "v_and_b32 v37, 1, v43\n"
      "v_lshl_add_u32 v33, v33, 1, v37\n"
      "v_lshlrev_b32 v33, 3, v33\n"
      "v_and_b32 v37, 2, v43\n"
      "v_lshl_add_u32 v37, v39, 2, v37\n"
      "s_lshl_b32 s50, s55, 9\n"
      "v_lshl_add_u32 v36, v37, 3, s50\n"
      "s_lshl_b32 s51, s55, 4\n"
      "v_lshrrev_b32 v40, 5, v43\n"
      "v_lshl_add_u32 v40, v40, 2, s51\n"
      "s_lshl_b32 s51, s55, 2\n"
      "s_add_u32 s51, s51, 140800\n"
      "v_mov_b32 v41, s51\n"
      "v_mov_b32 v42, 0x22600\n"
      "v_lshl_add_u32 v37, v43, 3, s50\n"
      "v_add_u32 v35, 0x22700, v37\n"
      "s_lshl_b32 s50, s55, 11\n"
      "s_add_u32 s50, s50, 141056\n"
      "v_lshl_add_u32 v34, v39, 9, v38\n"
      "v_add_u32 v34, s50, v34\n"
      "s_lshr_b32 s45, s35, 1\n"
      "s_lshl_b32 s45, s45, 16\n"
      "s_and_b32 s50, s35, 1\n"
      "s_lshl_b32 s50, s50, 11\n"
      "s_add_u32 s45, s45, s50\n"
      "s_add_u32 s45, s45, 524288\n"
      "s_mov_b32 s56, 0x3f80\n"
      "s_mov_b32 s57, 0x3f800000\n"
      "s_movk_i32 s61, 0x7ff\n"
      "s_mov_b32 s58, 131072\n"
      "s_mov_b32 s54, 0\n"
      "s_mov_b64 s[66:67], 0\n"
      "s_movk_i32 s70, 427\n"
      "s_waitcnt vmcnt(8)\n"
      "s_nop 0\n"
      "s_mov_b32 s60, 465920\n"
      "buffer_load_dwordx4 v[100:103], v32, s[24:27], s60 offen nt\n"
      "s_waitcnt vmcnt(8)\n"
      "s_nop 0\n"
      "s_mov_b32 s60, 473088\n"
      "buffer_load_dwordx4 v[104:107], v32, s[24:27], s60 offen nt\n"
      "v_perm_b32 v192, v68, v64, s62\n"
      "v_perm_b32 v196, v68, v64, s63\n"
      "v_perm_b32 v200, v69, v65, s62\n"
      "v_perm_b32 v204, v69, v65, s63\n"
      "v_perm_b32 v208, v70, v66, s62\n"
      "v_perm_b32 v212, v70, v66, s63\n"
      "v_perm_b32 v216, v71, v67, s62\n"
      "v_perm_b32 v220, v71, v67, s63\n"
      "s_waitcnt vmcnt(8)\n"
      "s_nop 0\n"
      "s_mov_b32 s60, 480256\n"
      "buffer_load_dwordx4 v[108:111], v32, s[24:27], s60 offen nt\n"
      "s_waitcnt vmcnt(8)\n"
      "s_nop 0\n"
      "s_mov_b32 s60, 487424\n"
      "buffer_load_dwordx4 v[112:115], v32, s[24:27], s60 offen nt\n"
      "v_perm_b32 v193, v76, v72, s62\n"
      "v_perm_b32 v197, v76, v72, s63\n"
      "v_perm_b32 v201, v77, v73, s62\n"
      "v_perm_b32 v205, v77, v73, s63\n"
      "v_perm_b32 v209, v78, v74, s62\n"
      "v_perm_b32 v213, v78, v74, s63\n"
      "v_perm_b32 v217, v79, v75, s62\n"
      "v_perm_b32 v221, v79, v75, s63\n"
      "s_waitcnt vmcnt(8)\n"
      "s_nop 0\n"
      "s_mov_b32 s60, 494592\n"
      "buffer_load_dwordx4 v[116:119], v32, s[24:27], s60 offen nt\n"
      "s_waitcnt vmcnt(8)\n"
      "s_nop 0\n"
      "s_mov_b32 s60, 501760\n"
      "buffer_load_dwordx4 v[120:123], v32, s[24:27], s60 offen nt\n"
      "v_perm_b32 v194, v84, v80, s62\n"
      "v_perm_b32 v198, v84, v80, s63\n"
      "v_perm_b32 v202, v85, v81, s62\n"
      "v_perm_b32 v206, v85, v81, s63\n"
      "v_perm_b32 v210, v86, v82, s62\n"
      "v_perm_b32 v214, v86, v82, s63\n"
      "v_perm_b32 v218, v87, v83, s62\n"
      "v_perm_b32 v222, v87, v83, s63\n"
      "s_waitcnt vmcnt(8)\n"
      "s_nop 0\n"
      "s_mov_b32 s60, 508928\n"
      "buffer_load_dwordx4 v[124:127], v32, s[24:27], s60 offen nt\n"
      "s_waitcnt vmcnt(8)\n"
      "s_nop 0\n"
      "s_mov_b32 s60, 917504\n"
      "buffer_load_dwordx4 v[128:131], v32, s[24:27], s60 offen nt\n"
      "v_perm_b32 v195, v92, v88, s62\n"
      "v_perm_b32 v199, v92, v88, s63\n"
      "v_perm_b32 v203, v93, v89, s62\n"
      "v_perm_b32 v207, v93, v89, s63\n"
      "v_perm_b32 v211, v94, v90, s62\n"
      "v_perm_b32 v215, v94, v90, s63\n"
      "v_perm_b32 v219, v95, v91, s62\n"
      "v_perm_b32 v223, v95, v91, s63\n"
      "s_waitcnt vmcnt(8)\n"
      "s_nop 0\n"
      "s_mov_b32 s60, 924672\n"
      "buffer_load_dwordx4 v[132:135], v32, s[24:27], s60 offen nt\n"
      "s_waitcnt vmcnt(8)\n"
      "s_nop 0\n"
      "s_mov_b32 s60, 931840\n"
      "buffer_load_dwordx4 v[136:139], v32, s[24:27], s60 offen nt\n"
      "v_perm_b32 v64, v100, v96, s62\n"
      "v_perm_b32 v68, v100, v96, s63\n"
      "v_perm_b32 v72, v101, v97, s62\n"
      "v_perm_b32 v76, v101, v97, s63\n"
      "v_perm_b32 v80, v102, v98, s62\n"
      "v_perm_b32 v84, v102, v98, s63\n"
      "v_perm_b32 v88, v103, v99, s62\n"
      "v_perm_b32 v92, v103, v99, s63\n"
      "s_waitcnt vmcnt(8)\n"
      "s_nop 0\n"
      "s_mov_b32 s60, 939008\n"
      "buffer_load_dwordx4 v[140:143], v32, s[24:27], s60 offen nt\n"
      "s_waitcnt vmcnt(8)\n"
      "s_nop 0\n"
      "s_mov_b32 s60, 946176\n"
      "buffer_load_dwordx4 v[144:147], v32, s[24:27], s60 offen nt\n"
      "v_perm_b32 v65, v108, v104, s62\n"
      "v_perm_b32 v69, v108, v104, s63\n"
      "v_perm_b32 v73, v109, v105, s62\n"
      "v_perm_b32 v77, v109, v105, s63\n"
      "v_perm_b32 v81, v110, v106, s62\n"
      "v_perm_b32 v85, v110, v106, s63\n"
      "v_perm_b32 v89, v111, v107, s62\n"
      "v_perm_b32 v93, v111, v107, s63\n"
      "s_waitcnt vmcnt(8)\n"
      "s_nop 0\n"
      "s_mov_b32 s60, 953344\n"
      "buffer_load_dwordx4 v[148:151], v32, s[24:27], s60 offen nt\n"
      "s_waitcnt vmcnt(8)\n"
      "s_nop 0\n"
      "s_mov_b32 s60, 960512\n"
      "buffer_load_dwordx4 v[152:155], v32, s[24:27], s60 offen nt\n"
      "v_perm_b32 v66, v116, v112, s62\n"
      "v_perm_b32 v70, v116, v112, s63\n"
      "v_perm_b32 v74, v117, v113, s62\n"
      "v_perm_b32 v78, v117, v113, s63\n"
      "v_perm_b32 v82, v118, v114, s62\n"
      "v_perm_b32 v86, v118, v114, s63\n"
      "v_perm_b32 v90, v119, v115, s62\n"
      "v_perm_b32 v94, v119, v115, s63\n"
      "s_waitcnt vmcnt(8)\n"
      "s_nop 0\n"
      "s_mov_b32 s60, 967680\n"
      "buffer_load_dwordx4 v[156:159], v32, s[24:27], s60 offen nt\n"
      "s_waitcnt vmcnt(8)\n"
      "s_nop 0\n"
      "s_mov_b32 s60, 1376256\n"
      "buffer_load_dwordx4 v[160:163], v32, s[24:27], s60 offen nt\n"
      "v_perm_b32 v67, v124, v120, s62\n"
      "v_perm_b32 v71, v124, v120, s63\n"
      "v_perm_b32 v75, v125, v121, s62\n"
      "v_perm_b32 v79, v125, v121, s63\n"
      "v_perm_b32 v83, v126, v122, s62\n"
      "v_perm_b32 v87, v126, v122, s63\n"
      "v_perm_b32 v91, v127, v123, s62\n"
      "v_perm_b32 v95, v127, v123, s63\n"
      "s_waitcnt vmcnt(8)\n"
      "s_nop 0\n"
      "s_mov_b32 s60, 1383424\n"
      "buffer_load_dwordx4 v[164:167], v32, s[24:27], s60 offen nt\n"
      "s_waitcnt vmcnt(8)\n"
      "s_nop 0\n"
      "s_mov_b32 s60, 1390592\n"
      "buffer_load_dwordx4 v[168:171], v32, s[24:27], s60 offen nt\n"
      "v_perm_b32 v96, v132, v128, s62\n"
      "v_perm_b32 v100, v132, v128, s63\n"
      "v_perm_b32 v104, v133, v129, s62\n"
      "v_perm_b32 v108, v133, v129, s63\n"
      "v_perm_b32 v112, v134, v130, s62\n"
      "v_perm_b32 v116, v134, v130, s63\n"
      "v_perm_b32 v120, v135, v131, s62\n"
      "v_perm_b32 v124, v135, v131, s63\n"
      "s_waitcnt vmcnt(8)\n"
      "s_nop 0\n"
      "s_mov_b32 s60, 1397760\n"
      "buffer_load_dwordx4 v[172:175], v32, s[24:27], s60 offen nt\n"
      "s_waitcnt vmcnt(8)\n"
      "s_nop 0\n"
      "s_mov_b32 s60, 1404928\n"
      "buffer_load_dwordx4 v[176:179], v32, s[24:27], s60 offen nt\n"
      "v_perm_b32 v97, v140, v136, s62\n"
      "v_perm_b32 v101, v140, v136, s63\n"
      "v_perm_b32 v105, v141, v137, s62\n"
      "v_perm_b32 v109, v141, v137, s63\n"
      "v_perm_b32 v113, v142, v138, s62\n"
      "v_perm_b32 v117, v142, v138, s63\n"
      "v_perm_b32 v121, v143, v139, s62\n"
      "v_perm_b32 v125, v143, v139, s63\n"
      "s_waitcnt vmcnt(8)\n"
      "s_nop 0\n"
      "s_mov_b32 s60, 1412096\n"
      "buffer_load_dwordx4 v[180:183], v32, s[24:27], s60 offen nt\n"
      "s_waitcnt vmcnt(8)\n"
      "s_nop 0\n"
      "s_mov_b32 s60, 1419264\n"
      "buffer_load_dwordx4 v[184:187], v32, s[24:27], s60 offen nt\n"
      "v_perm_b32 v98, v148, v144, s62\n"
      "v_perm_b32 v102, v148, v144, s63\n"
      "v_perm_b32 v106, v149, v145, s62\n"
      "v_perm_b32 v110, v149, v145, s63\n"
      "v_perm_b32 v114, v150, v146, s62\n"
      "v_perm_b32 v118, v150, v146, s63\n"
      "v_perm_b32 v122, v151, v147, s62\n"
      "v_perm_b32 v126, v151, v147, s63\n"
      "s_waitcnt vmcnt(8)\n"
      "s_nop 0\n"
      "s_mov_b32 s60, 1426432\n"
      "buffer_load_dwordx4 v[188:191], v32, s[24:27], s60 offen nt\n"
      "ds_write_b32 v41, v47 offset:80\n"
      "s_waitcnt vmcnt(8)\n"
      "s_nop 0\n"
      "v_perm_b32 v99, v156, v152, s62\n"
      "v_perm_b32 v103, v156, v152, s63\n"
      "v_perm_b32 v107, v157, v153, s62\n"
      "v_perm_b32 v111, v157, v153, s63\n"
      "v_perm_b32 v115, v158, v154, s62\n"
      "v_perm_b32 v119, v158, v154, s63\n"
      "v_perm_b32 v123, v159, v155, s62\n"
      "v_perm_b32 v127, v159, v155, s63\n"
      "s_waitcnt vmcnt(7)\n"
      "s_nop 0\n"
      "s_memrealtime s[68:69]\n"
      "s_waitcnt vmcnt(6)\n"
      "s_nop 0\n"
      "s_waitcnt lgkmcnt(0)\n"
      "s_sub_u32 s68, s68, s12\n"
      "s_cmp_lt_u32 s68, s70\n"
      "s_cbranch_scc1 .Lgn25%=\n"
      "buffer_load_dwordx2 v[8:9], v33, s[16:19], s43 offen sc1\n"
      "s_mov_b32 s70, -1\n"
      ".Lgn25%=:\n"
      "s_memrealtime s[68:69]\n"
      "v_perm_b32 v128, v164, v160, s62\n"
      "v_perm_b32 v132, v164, v160, s63\n"
      "v_perm_b32 v136, v165, v161, s62\n"
      "v_perm_b32 v140, v165, v161, s63\n"
      "v_perm_b32 v144, v166, v162, s62\n"
      "v_perm_b32 v148, v166, v162, s63\n"
      "v_perm_b32 v152, v167, v163, s62\n"
      "v_perm_b32 v156, v167, v163, s63\n"
      "s_waitcnt vmcnt(5)\n"
      "s_nop 0\n"
      "s_waitcnt lgkmcnt(0)\n"
      "s_sub_u32 s68, s68, s12\n"
      "s_cmp_lt_u32 s68, s70\n"
      "s_cbranch_scc1 .Lgn26%=\n"
      "buffer_load_dwordx2 v[8:9], v33, s[16:19], s43 offen sc1\n"
      "s_mov_b32 s70, -1\n"
      ".Lgn26%=:\n"
      "s_memrealtime s[68:69]\n"
      "s_waitcnt vmcnt(4)\n"
      "s_nop 0\n"
      "s_waitcnt lgkmcnt(0)\n"
      "s_sub_u32 s68, s68, s12\n"
      "s_cmp_lt_u32 s68, s70\n"
      "s_cbranch_scc1 .Lgn27%=\n"
      "buffer_load_dwordx2 v[8:9], v33, s[16:19], s43 offen sc1\n"
      "s_mov_b32 s70, -1\n"
      ".Lgn27%=:\n"
      "s_memrealtime s[68:69]\n"
      "v_perm_b32 v129, v172, v168, s62\n"
      "v_perm_b32 v133, v172, v168, s63\n"
      "v_perm_b32 v137, v173, v169, s62\n"
      "v_perm_b32 v141, v173, v169, s63\n"
      "v_perm_b32 v145, v174, v170, s62\n"
      "v_perm_b32 v149, v174, v170, s63\n"
      "v_perm_b32 v153, v175, v171, s62\n"
      "v_perm_b32 v157, v175, v171, s63\n"
      "s_waitcnt vmcnt(3)\n"
      "s_nop 0\n"
      "s_waitcnt lgkmcnt(0)\n"
      "s_sub_u32 s68, s68, s12\n"
      "s_cmp_lt_u32 s68, s70\n"
      "s_cbranch_scc1 .Lgn28%=\n"
      "buffer_load_dwordx2 v[8:9], v33, s[16:19], s43 offen sc1\n"
      "s_mov_b32 s70, -1\n"
      ".Lgn28%=:\n"
      "s_memrealtime s[68:69]\n"
      "s_waitcnt vmcnt(2)\n"
      "s_nop 0\n"
      "s_waitcnt lgkmcnt(0)\n"
      "s_sub_u32 s68, s68, s12\n"
      "s_cmp_lt_u32 s68, s70\n"
      "s_cbranch_scc1 .Lgn29%=\n"
      "buffer_load_dwordx2 v[8:9], v33, s[16:19], s43 offen sc1\n"
      "s_mov_b32 s70, -1\n"
      ".Lgn29%=:\n"
      "s_memrealtime s[68:69]\n"
      "v_perm_b32 v130, v180, v176, s62\n"
      "v_perm_b32 v134, v180, v176, s63\n"
      "v_perm_b32 v138, v181, v177, s62\n"
      "v_perm_b32 v142, v181, v177, s63\n"
      "v_perm_b32 v146, v182, v178, s62\n"
      "v_perm_b32 v150, v182, v178, s63\n"
      "v_perm_b32 v154, v183, v179, s62\n"
      "v_perm_b32 v158, v183, v179, s63\n"
      "s_waitcnt vmcnt(1)\n"
      "s_nop 0\n"
      "s_waitcnt lgkmcnt(0)\n"
      "s_sub_u32 s68, s68, s12\n"
      "s_cmp_lt_u32 s68, s70\n"
      "s_cbranch_scc1 .Lgn30%=\n"
      "buffer_load_dwordx2 v[8:9], v33, s[16:19], s43 offen sc1\n"
      "s_mov_b32 s70, -1\n"
      ".Lgn30%=:\n"
      "s_memrealtime s[68:69]\n"
      "s_waitcnt vmcnt(0)\n"
      "s_nop 0\n"
      "s_waitcnt lgkmcnt(0)\n"
      "s_sub_u32 s68, s68, s12\n"
      "s_cmp_lt_u32 s68, s70\n"
      "s_cbranch_scc1 .Lgn31%=\n"
      "buffer_load_dwordx2 v[8:9], v33, s[16:19], s43 offen sc1\n"
      "s_mov_b32 s70, -1\n"
      ".Lgn31%=:\n"
      "v_perm_b32 v131, v188, v184, s62\n"
      "v_perm_b32 v135, v188, v184, s63\n"
      "v_perm_b32 v139, v189, v185, s62\n"
      "v_perm_b32 v143, v189, v185, s63\n"
      "v_perm_b32 v147, v190, v186, s62\n"
      "v_perm_b32 v151, v190, v186, s63\n"
      "v_perm_b32 v155, v191, v187, s62\n"
      "v_perm_b32 v159, v191, v187, s63\n"
      "s_movk_i32 s59, 0x3fff\n"
      "s_cmp_eq_u32 s70, -1\n"
      "s_cbranch_scc1 .Lghc%=\n"
      ".Lgpc3_%=:\n"
      "s_memrealtime s[68:69]\n"
      "s_waitcnt lgkmcnt(0)\n"
      "s_sub_u32 s68, s68, s12\n"
      "s_cmp_ge_u32 s68, 435\n"
      "s_cbranch_scc1 .Lgpd3_%=\n"
      "s_sleep 1\n"
      "s_branch .Lgpc3_%=\n"
      ".Lgpd3_%=:\n"
      ".Lgh%=:\n"
      "buffer_load_dwordx2 v[8:9], v33, s[16:19], s43 offen sc1\n"
      ".Lghc%=:\n"
      "s_waitcnt vmcnt(0)\n"
      "v_cmp_ne_u32 vcc, s15, v9\n"
      "v_bitop3_b32 v10, v8, s61, v8 bitop3:0x0c\n"
      "s_sub_u32 s59, s59, 1\n"
      "s_cbranch_vccz .Lgho%=\n"
      "s_sleep 2\n"
      "s_cbranch_scc0 .Lgh%=\n"
      ".Lgho%=:\n"
      "v_lshrrev_b32 v45, 9, v10\n"
      "v_lshl_add_u32 v11, v10, 11, v36\n"
      "v_mul_u32_u24 v45, 0xf000, v45\n"
      "v_lshl_add_u32 v45, v10, 3, v45\n"
      "buffer_load_dwordx2 v[12:13], off, s[16:19], s43 offset:64 sc1\n"
      "buffer_load_dwordx2 v[62:63], v45, s[16:19], s58 offen sc1\n"
      "buffer_load_dwordx4 v[16:19], v11, s[20:23], 0 offen offset:0\n"
      "buffer_load_dwordx4 v[20:23], v11, s[20:23], 0 offen offset:128\n"
      "buffer_load_dwordx4 v[24:27], v11, s[20:23], 0 offen offset:256\n"
      "buffer_load_dwordx4 v[28:31], v11, s[20:23], 0 offen offset:384\n"
      "ds_write_b32 v41, v47 offset:64\n"
      ".Lghx%=:\n"
      "s_movk_i32 s59, 0x7fff\n"
      "s_waitcnt vmcnt(4)\n"
      "s_nop 0\n"
      ".Lgs%=:\n"
      "v_cmp_ne_u32 vcc, s15, v63\n"
      "v_lshlrev_b32 v10, 16, v62\n"
      "v_and_b32 v44, 0xffff0000, v62\n"
      "s_cbranch_vccnz .Lgss%=\n"
      ".Lgso%=:\n"
      "v_rcp_f32 v44, v44\n"
      "v_xor_b32 v10, 0x80000000, v10\n"
      "v_readfirstlane_b32 s68, v13\n"
      "v_readfirstlane_b32 s69, v12\n"
      "s_cmp_eq_u32 s68, s15\n"
      "s_cselect_b32 s12, s69, s12\n"
      "v_mov_b32 v12, s12\n"
      "ds_write_b32 v42, v12 offset:128\n"
      "s_waitcnt vmcnt(3)\n"
      "v_dot2_f32_bf16 v168, v16, s56, v10\n"
      "v_dot2_f32_bf16 v169, v16, s57, v10\n"
      "v_dot2_f32_bf16 v170, v17, s56, v10\n"
      "v_dot2_f32_bf16 v171, v17, s57, v10\n"
      "v_dot2_f32_bf16 v172, v18, s56, v10\n"
      "v_dot2_f32_bf16 v173, v18, s57, v10\n"
      "v_dot2_f32_bf16 v174, v19, s56, v10\n"
      "v_dot2_f32_bf16 v175, v19, s57, v10\n"
      "v_cvt_pk_bf16_f32 v16, v168, v169\n"
      "v_cvt_pk_bf16_f32 v17, v170, v171\n"
      "v_cvt_pk_bf16_f32 v18, v172, v173\n"
      "v_cvt_pk_bf16_f32 v19, v174, v175\n"
      "ds_bpermute_b32 v44, v40, v44\n"
      "s_waitcnt vmcnt(2)\n"
      "v_dot2_f32_bf16 v176, v20, s56, v10\n"
      "v_dot2_f32_bf16 v177, v20, s57, v10\n"
      "v_dot2_f32_bf16 v178, v21, s56, v10\n"
      "v_dot2_f32_bf16 v179, v21, s57, v10\n"
      "v_dot2_f32_bf16 v180, v22, s56, v10\n"
      "v_dot2_f32_bf16 v181, v22, s57, v10\n"
      "v_dot2_f32_bf16 v182, v23, s56, v10\n"
      "v_dot2_f32_bf16 v183, v23, s57, v10\n"
      "v_cvt_pk_bf16_f32 v20, v176, v177\n"
      "v_cvt_pk_bf16_f32 v21, v178, v179\n"
      "v_cvt_pk_bf16_f32 v22, v180, v181\n"
      "v_cvt_pk_bf16_f32 v23, v182, v183\n"
      "s_waitcnt vmcnt(1)\n"
      "v_dot2_f32_bf16 v168, v24, s56, v10\n"
      "v_dot2_f32_bf16 v169, v24, s57, v10\n"
      "v_dot2_f32_bf16 v170, v25, s56, v10\n"
      "v_dot2_f32_bf16 v171, v25, s57, v10\n"
      "v_dot2_f32_bf16 v172, v26, s56, v10\n"
      "v_dot2_f32_bf16 v173, v26, s57, v10\n"
      "v_dot2_f32_bf16 v174, v27, s56, v10\n"
      "v_dot2_f32_bf16 v175, v27, s57, v10\n"
      "v_cvt_pk_bf16_f32 v24, v168, v169\n"
      "v_cvt_pk_bf16_f32 v25, v170, v171\n"
      "v_cvt_pk_bf16_f32 v26, v172, v173\n"
      "v_cvt_pk_bf16_f32 v27, v174, v175\n"
      "s_waitcnt vmcnt(0)\n"
      "v_dot2_f32_bf16 v176, v28, s56, v10\n"
      "v_dot2_f32_bf16 v177, v28, s57, v10\n"
      "v_dot2_f32_bf16 v178, v29, s56, v10\n"
      "v_dot2_f32_bf16 v179, v29, s57, v10\n"
      "v_dot2_f32_bf16 v180, v30, s56, v10\n"
      "v_dot2_f32_bf16 v181, v30, s57, v10\n"
      "v_dot2_f32_bf16 v182, v31, s56, v10\n"
      "v_dot2_f32_bf16 v183, v31, s57, v10\n"
      "v_cvt_pk_bf16_f32 v28, v176, v177\n"
      "v_cvt_pk_bf16_f32 v29, v178, v179\n"
      "v_cvt_pk_bf16_f32 v30, v180, v181\n"
      "v_cvt_pk_bf16_f32 v31, v182, v183\n"
      "v_mfma_f32_16x16x32_bf16 v[224:227], v[16:19], v[192:195], 0\n"
      "v_mfma_f32_16x16x32_bf16 v[228:231], v[16:19], v[196:199], 0\n"
      "v_mfma_f32_16x16x32_bf16 v[232:235], v[16:19], v[200:203], 0\n"
      "v_mfma_f32_16x16x32_bf16 v[236:239], v[16:19], v[204:207], 0\n"
      "v_mfma_f32_16x16x32_bf16 v[240:243], v[16:19], v[208:211], 0\n"
      "v_mfma_f32_16x16x32_bf16 v[244:247], v[16:19], v[212:215], 0\n"
      "v_mfma_f32_16x16x32_bf16 v[248:251], v[16:19], v[216:219], 0\n"
      "v_mfma_f32_16x16x32_bf16 v[252:255], v[16:19], v[220:223], 0\n"
      "v_mfma_f32_16x16x32_bf16 v[224:227], v[20:23], v[64:67], v[224:227]\n"
      "v_mfma_f32_16x16x32_bf16 v[228:231], v[20:23], v[68:71], v[228:231]\n"
      "v_mfma_f32_16x16x32_bf16 v[232:235], v[20:23], v[72:75], v[232:235]\n"
      "v_mfma_f32_16x16x32_bf16 v[236:239], v[20:23], v[76:79], v[236:239]\n"
      "v_mfma_f32_16x16x32_bf16 v[240:243], v[20:23], v[80:83], v[240:243]\n"
      "v_mfma_f32_16x16x32_bf16 v[244:247], v[20:23], v[84:87], v[244:247]\n"
      "v_mfma_f32_16x16x32_bf16 v[248:251], v[20:23], v[88:91], v[248:251]\n"
      "v_mfma_f32_16x16x32_bf16 v[252:255], v[20:23], v[92:95], v[252:255]\n"
      "v_mfma_f32_16x16x32_bf16 v[224:227], v[24:27], v[96:99], v[224:227]\n"
      "v_mfma_f32_16x16x32_bf16 v[228:231], v[24:27], v[100:103], v[228:231]\n"
      "v_mfma_f32_16x16x32_bf16 v[232:235], v[24:27], v[104:107], v[232:235]\n"
      "v_mfma_f32_16x16x32_bf16 v[236:239], v[24:27], v[108:111], v[236:239]\n"
      "v_mfma_f32_16x16x32_bf16 v[240:243], v[24:27], v[112:115], v[240:243]\n"
      "v_mfma_f32_16x16x32_bf16 v[244:247], v[24:27], v[116:119], v[244:247]\n"
      "v_mfma_f32_16x16x32_bf16 v[248:251], v[24:27], v[120:123], v[248:251]\n"
      "v_mfma_f32_16x16x32_bf16 v[252:255], v[24:27], v[124:127], v[252:255]\n"
      "v_mfma_f32_16x16x32_bf16 v[224:227], v[28:31], v[128:131], v[224:227]\n"
      "v_mfma_f32_16x16x32_bf16 v[228:231], v[28:31], v[132:135], v[228:231]\n"
      "v_mfma_f32_16x16x32_bf16 v[232:235], v[28:31], v[136:139], v[232:235]\n"
      "v_mfma_f32_16x16x32_bf16 v[236:239], v[28:31], v[140:143], v[236:239]\n"
      "s_nop 4\n"
      "v_add_f32_dpp v160, v226, v224 row_ror:8 row_mask:0xf bank_mask:0x3\n"
      "v_add_f32_dpp v164, v227, v225 row_ror:8 row_mask:0xf bank_mask:0x3\n"
      "v_mfma_f32_16x16x32_bf16 v[240:243], v[28:31], v[144:147], v[240:243]\n"
      "v_add_f32_dpp v161, v230, v228 row_ror:8 row_mask:0xf bank_mask:0x3\n"
      "v_add_f32_dpp v165, v231, v229 row_ror:8 row_mask:0xf bank_mask:0x3\n"
      "v_mfma_f32_16x16x32_bf16 v[244:247], v[28:31], v[148:151], v[244:247]\n"
      "v_add_f32_dpp v162, v234, v232 row_ror:8 row_mask:0xf bank_mask:0x3\n"
      "v_add_f32_dpp v166, v235, v233 row_ror:8 row_mask:0xf bank_mask:0x3\n"
      "v_mfma_f32_16x16x32_bf16 v[248:251], v[28:31], v[152:155], v[248:251]\n"
      "v_add_f32_dpp v163, v238, v236 row_ror:8 row_mask:0xf bank_mask:0x3\n"
      "v_add_f32_dpp v167, v239, v237 row_ror:8 row_mask:0xf bank_mask:0x3\n"
      "v_mfma_f32_16x16x32_bf16 v[252:255], v[28:31], v[156:159], v[252:255]\n"
      "v_add_f32_dpp v160, v240, v242 row_ror:8 row_mask:0xf bank_mask:0xc\n"
      "v_add_f32_dpp v164, v241, v243 row_ror:8 row_mask:0xf bank_mask:0xc\n"
      "v_add_f32_dpp v161, v244, v246 row_ror:8 row_mask:0xf bank_mask:0xc\n"
      "v_add_f32_dpp v165, v245, v247 row_ror:8 row_mask:0xf bank_mask:0xc\n"
      "s_nop 0\n"
      "v_add_f32_dpp v162, v248, v250 row_ror:8 row_mask:0xf bank_mask:0xc\n"
      "v_add_f32_dpp v166, v249, v251 row_ror:8 row_mask:0xf bank_mask:0xc\n"
      "s_nop 0\n"
      "v_add_f32_dpp v163, v252, v254 row_ror:8 row_mask:0xf bank_mask:0xc\n"
      "v_add_f32_dpp v167, v253, v255 row_ror:8 row_mask:0xf bank_mask:0xc\n"
      "ds_write_b128 v34, v[160:163] \n"
      "ds_write_b128 v34, v[164:167] offset:256\n"
      "ds_write_b32 v41, v47 \n"
      "s_movk_i32 s59, 0x7fff\n"
      ".Lgw%=:\n"
      "ds_read_b128 v[72:75], v42 \n"
      "ds_read_b64 v[64:65], v35 offset:0\n"
      "ds_read_b64 v[66:67], v35 offset:2048\n"
      "ds_read_b64 v[68:69], v35 offset:4096\n"
      "ds_read_b64 v[70:71], v35 offset:6144\n"
      "s_sub_u32 s59, s59, 1\n"
      "s_cbranch_scc1 .Lgwo%=\n"
      "s_waitcnt lgkmcnt(4)\n"
      "v_xor_b32 v72, s15, v72\n"
      "v_bitop3_b32 v72, s15, v73, v72 bitop3:0xbe\n"
      "v_bitop3_b32 v72, s15, v74, v72 bitop3:0xbe\n"
      "v_bitop3_b32 v72, s15, v75, v72 bitop3:0xbe\n"
      "v_cmp_ne_u32 vcc, 0, v72\n"
      "s_waitcnt lgkmcnt(0)\n"
      "s_nop 0\n"
      "s_cbranch_vccnz .Lgw%=\n"
      ".Lgwo%=:\n"
      "s_waitcnt lgkmcnt(0)\n"
      "s_bitcmp1_b32 s49, 0\n"
      "v_pk_add_f32 v[64:65], v[64:65], v[66:67]\n"
      "v_pk_add_f32 v[68:69], v[68:69], v[70:71]\n"
      "v_pk_add_f32 v[64:65], v[64:65], v[68:69]\n"
      "v_mul_f32 v64, v64, v44\n"
      "v_mul_f32 v65, v65, v44\n"
      "v_cvt_pk_bf16_f32 v46, v64, v65\n"
      "s_cbranch_scc1 .Lgu%=\n"
      "v_lshlrev_b32 v64, 16, v46\n"
      "v_and_b32 v65, 0xffff0000, v46\n"
      "v_mul_f32 v66, 0xbfb8aa3b, v64\n"
      "v_mul_f32 v67, 0xbfb8aa3b, v65\n"
      "v_exp_f32 v66, v66\n"
      "v_exp_f32 v67, v67\n"
      "v_add_f32 v66, 1.0, v66\n"
      "v_add_f32 v67, 1.0, v67\n"
      "v_rcp_f32 v66, v66\n"
      "v_rcp_f32 v67, v67\n"
      "v_mul_f32 v64, v64, v66\n"
      "v_mul_f32 v65, v65, v67\n"
      "v_cvt_pk_bf16_f32 v46, v64, v65\n"
      ".Lgu%=:\n"
      "buffer_store_dwordx2 v[46:47], v37, s[16:19], s45 offen sc1\n"
      "s_cmp_lg_u32 s33, 4\n"
      "s_cbranch_scc1 .Lga%=\n"
      "v_mov_b32 v64, s15\n"
      "v_mov_b32 v65, s15\n"
      "buffer_store_dwordx2 v[64:65], off, s[16:19], s42 sc1\n"
      ".Lga%=:\n"
      "s_cmp_eq_u32 s33, 7\n"
      "s_mov_b32 s24, s36\n"
      "s_mov_b32 s25, s37\n"
      "s_mov_b32 s26, 0x400000\n"
      "s_cbranch_scc1 .LZ%=\n"
      "s_endpgm\n"
      ".Lgss%=:\n"
      "s_sleep 1\n"
      "s_sub_u32 s59, s59, 1\n"
      "s_cbranch_scc1 .Lgso%=\n"
      "buffer_load_dwordx2 v[62:63], v45, s[16:19], s58 offen sc1\n"
      "s_waitcnt vmcnt(0)\n"
      "s_branch .Lgs%=\n"
      ".LD2%=:\n"
      "s_setprio 0\n"
      "s_mul_i32 s35, s48, 14\n"
      "s_add_u32 s35, s35, s49\n"
      "s_lshr_b32 s52, s35, 2\n"
      "s_and_b32 s54, s35, 3\n"
      "v_lshlrev_b32 v42, 4, v49\n"
      "s_lshl_b32 s50, s52, 18\n"
      "s_lshl_b32 s51, s54, 9\n"
      "s_add_u32 s50, s50, s51\n"
      "s_lshl_b32 s51, s33, 7\n"
      "s_add_u32 s50, s50, s51\n"
      "v_lshl_add_u32 v32, v50, 14, v42\n"
      "v_add_u32 v32, s50, v32\n"
      "v_lshrrev_b32 v41, 1, v49\n"
      "v_and_b32 v43, 1, v41\n"
      "v_mul_u32_u24 v36, 0x120, v41\n"
      "v_lshlrev_b32 v43, 4, v43\n"
      "v_sub_u32 v36, v36, v43\n"
      "v_and_b32 v43, 1, v49\n"
      "v_lshl_add_u32 v36, v43, 7, v36\n"
      "v_lshrrev_b32 v41, 4, v48\n"
      "v_lshl_add_u32 v36, v41, 4, v36\n"
      "v_and_b32 v43, 8, v48\n"
      "v_add_u32 v36, v36, v43\n"
      "s_lshr_b32 s53, s33, 1\n"
      "s_mul_i32 s50, s53, 1152\n"
      "s_and_b32 s55, s33, 1\n"
      "s_lshl_b32 s51, s55, 6\n"
      "s_add_u32 s50, s50, s51\n"
      "s_add_u32 s50, s50, 149504\n"
      "v_add_u32 v36, s50, v36\n"
      "v_and_b32 v43, 1, v41\n"
      "v_mul_u32_u24 v37, 0x120, v41\n"
      "v_lshlrev_b32 v43, 4, v43\n"
      "v_sub_u32 v37, v37, v43\n"
      "v_and_b32 v43, 15, v48\n"
      "v_lshl_add_u32 v37, v43, 4, v37\n"
      "v_add_u32 v37, 0x24800, v37\n"
      "v_lshlrev_b32 v35, 4, v48\n"
      "s_lshl_b32 s46, s52, 17\n"
      "s_lshl_b32 s50, s53, 16\n"
      "s_add_u32 s46, s46, s50\n"
      "s_lshl_b32 s50, s55, 10\n"
      "s_add_u32 s46, s46, s50\n"
      "s_add_u32 s46, s46, 524288\n"
      "v_and_b32 v41, 3, v50\n"
      "v_lshrrev_b32 v43, 5, v48\n"
      "v_lshl_add_u32 v41, v41, 1, v43\n"
      "v_mul_u32_u24 v38, 0x1c00, v41\n"
      "v_add_u32 v38, v38, v42\n"
      "s_lshl_b32 s50, s54, 2\n"
      "s_add_u32 s50, s50, s33\n"
      "s_mul_i32 s47, s50, 57344\n"
      "s_lshl_b32 s50, s52, 8\n"
      "s_add_u32 s47, s47, s50\n"
      "s_add_u32 s47, s47, 4194304\n"
      "s_mov_b32 s62, 0x05040100\n"
      "s_mov_b32 s63, 0x07060302\n"
      "s_lshl_b32 s50, s33, 2\n"
      "s_add_u32 s50, s50, 140864\n"
      "v_mov_b32 v41, s50\n"
      "v_mov_b32 v42, 0x22640\n"
      "s_movk_i32 s59, 0x3fff\n"
      ".Ldf%=:\n"
      "s_sleep 1\n"
      "ds_read_b128 v[240:243], v42\n"
      "ds_read_b32 v43, v41 offset:16\n"
      "s_sub_u32 s59, s59, 1\n"
      "s_cbranch_scc1 .Ldfo%=\n"
      "s_waitcnt lgkmcnt(0)\n"
      "v_xor_b32 v240, s15, v240\n"
      "v_bitop3_b32 v240, s15, v241, v240 bitop3:0xbe\n"
      "v_bitop3_b32 v240, s15, v242, v240 bitop3:0xbe\n"
      "v_bitop3_b32 v240, s15, v243, v240 bitop3:0xbe\n"
      "v_bitop3_b32 v240, s15, v43, v240 bitop3:0xbe\n"
      "v_cmp_ne_u32 vcc, 0, v240\n"
      "s_nop 1\n"
      "s_cbranch_vccnz .Ldf%=\n"
      ".Ldfo%=:\n"
      "s_mov_b32 s60, 0\n"
      "buffer_load_dwordx4 v[128:131], v32, s[28:31], s60 offen nt\n"
      "s_mov_b32 s60, 2048\n"
      "buffer_load_dwordx4 v[132:135], v32, s[28:31], s60 offen nt\n"
      "s_mov_b32 s60, 4096\n"
      "buffer_load_dwordx4 v[136:139], v32, s[28:31], s60 offen nt\n"
      "s_mov_b32 s60, 6144\n"
      "buffer_load_dwordx4 v[140:143], v32, s[28:31], s60 offen nt\n"
      "s_mov_b32 s60, 8192\n"
      "buffer_load_dwordx4 v[144:147], v32, s[28:31], s60 offen nt\n"
      "s_mov_b32 s60, 10240\n"
      "buffer_load_dwordx4 v[148:151], v32, s[28:31], s60 offen nt\n"
      "s_mov_b32 s60, 12288\n"
      "buffer_load_dwordx4 v[152:155], v32, s[28:31], s60 offen nt\n"
      "s_mov_b32 s60, 14336\n"
      "buffer_load_dwordx4 v[156:159], v32, s[28:31], s60 offen nt\n"
      "s_waitcnt vmcnt(7)\n"
      "s_nop 0\n"
      "s_mov_b32 s60, 131072\n"
      "buffer_load_dwordx4 v[160:163], v32, s[28:31], s60 offen nt\n"
      "s_waitcnt vmcnt(7)\n"
      "s_nop 0\n"
      "s_mov_b32 s60, 133120\n"
      "buffer_load_dwordx4 v[164:167], v32, s[28:31], s60 offen nt\n"
      "v_perm_b32 v64, v132, v128, s62\n"
      "v_perm_b32 v68, v132, v128, s63\n"
      "v_perm_b32 v72, v133, v129, s62\n"
      "v_perm_b32 v76, v133, v129, s63\n"
      "v_perm_b32 v80, v134, v130, s62\n"
      "v_perm_b32 v84, v134, v130, s63\n"
      "v_perm_b32 v88, v135, v131, s62\n"
      "v_perm_b32 v92, v135, v131, s63\n"
      "s_waitcnt vmcnt(7)\n"
      "s_nop 0\n"
      "s_mov_b32 s60, 135168\n"
      "buffer_load_dwordx4 v[168:171], v32, s[28:31], s60 offen nt\n"
      "s_waitcnt vmcnt(7)\n"
      "s_nop 0\n"
      "s_mov_b32 s60, 137216\n"
      "buffer_load_dwordx4 v[172:175], v32, s[28:31], s60 offen nt\n"
      "v_perm_b32 v65, v140, v136, s62\n"
      "v_perm_b32 v69, v140, v136, s63\n"
      "v_perm_b32 v73, v141, v137, s62\n"
      "v_perm_b32 v77, v141, v137, s63\n"
      "v_perm_b32 v81, v142, v138, s62\n"
      "v_perm_b32 v85, v142, v138, s63\n"
      "v_perm_b32 v89, v143, v139, s62\n"
      "v_perm_b32 v93, v143, v139, s63\n"
      "s_waitcnt vmcnt(7)\n"
      "s_nop 0\n"
      "s_mov_b32 s60, 139264\n"
      "buffer_load_dwordx4 v[176:179], v32, s[28:31], s60 offen nt\n"
      "s_waitcnt vmcnt(7)\n"
      "s_nop 0\n"
      "s_mov_b32 s60, 141312\n"
      "buffer_load_dwordx4 v[180:183], v32, s[28:31], s60 offen nt\n"
      "v_perm_b32 v66, v148, v144, s62\n"
      "v_perm_b32 v70, v148, v144, s63\n"
      "v_perm_b32 v74, v149, v145, s62\n"
      "v_perm_b32 v78, v149, v145, s63\n"
      "v_perm_b32 v82, v150, v146, s62\n"
      "v_perm_b32 v86, v150, v146, s63\n"
      "v_perm_b32 v90, v151, v147, s62\n"
      "v_perm_b32 v94, v151, v147, s63\n"
      "s_waitcnt vmcnt(7)\n"
      "s_nop 0\n"
      "s_mov_b32 s60, 143360\n"
      "buffer_load_dwordx4 v[184:187], v32, s[28:31], s60 offen nt\n"
      "s_waitcnt vmcnt(7)\n"
      "s_nop 0\n"
      "s_mov_b32 s60, 145408\n"
      "buffer_load_dwordx4 v[188:191], v32, s[28:31], s60 offen nt\n"
      "v_perm_b32 v67, v156, v152, s62\n"
      "v_perm_b32 v71, v156, v152, s63\n"
      "v_perm_b32 v75, v157, v153, s62\n"
      "v_perm_b32 v79, v157, v153, s63\n"
      "v_perm_b32 v83, v158, v154, s62\n"
      "v_perm_b32 v87, v158, v154, s63\n"
      "v_perm_b32 v91, v159, v155, s62\n"
      "v_perm_b32 v95, v159, v155, s63\n"
      "s_waitcnt vmcnt(7)\n"
      "s_nop 0\n"
      "s_movk_i32 s59, 0x3fff\n"
      "v_add_u32 v41, 0xffffffc0, v41\n"
      ".Lde%=:\n"
      "ds_read_b32 v43, v41\n"
      "s_sub_u32 s59, s59, 1\n"
      "s_cbranch_scc1 .Ldeo%=\n"
      "s_waitcnt lgkmcnt(0)\n"
      "v_cmp_ne_u32 vcc, s15, v43\n"
      "s_nop 1\n"
      "s_cbranch_vccz .Ldeo%=\n"
      "s_sleep 2\n"
      "s_branch .Lde%=\n"
      ".Ldeo%=:\n"
      "s_cmp_lg_u32 s59, 0x3ffe\n"
      "s_cbranch_scc0 .Ldep%=\n"
      "s_sleep 6\n"
      ".Ldep%=:\n"
      "ds_read_b32 v43, v42 offset:64\n"
      "s_waitcnt lgkmcnt(0)\n"
      "v_readfirstlane_b32 s12, v43\n"
      "s_nop 0\n"
      ".Ldpc1_%=:\n"
      "s_memrealtime s[64:65]\n"
      "s_waitcnt lgkmcnt(0)\n"
      "s_sub_u32 s64, s64, s12\n"
      "s_cmp_ge_u32 s64, 640\n"
      "s_cbranch_scc1 .Ldpd1_%=\n"
      "s_sleep 1\n"
      "s_branch .Ldpc1_%=\n"
      ".Ldpd1_%=:\n"
      "buffer_load_dwordx4 v[224:227], v35, s[16:19], s46 offen sc1\n"
      "buffer_load_dwordx4 v[228:231], v35, s[16:19], s46 offen offset:2048 sc1\n"
      "s_waitcnt vmcnt(8)\n"
      "s_nop 0\n"
      "v_perm_b32 v96, v164, v160, s62\n"
      "v_perm_b32 v100, v164, v160, s63\n"
      "v_perm_b32 v104, v165, v161, s62\n"
      "v_perm_b32 v108, v165, v161, s63\n"
      "v_perm_b32 v112, v166, v162, s62\n"
      "v_perm_b32 v116, v166, v162, s63\n"
      "v_perm_b32 v120, v167, v163, s62\n"
      "v_perm_b32 v124, v167, v163, s63\n"
      "s_waitcnt vmcnt(7)\n"
      "s_nop 0\n"
      "s_waitcnt vmcnt(6)\n"
      "s_nop 0\n"
      "v_perm_b32 v97, v172, v168, s62\n"
      "v_perm_b32 v101, v172, v168, s63\n"
      "v_perm_b32 v105, v173, v169, s62\n"
      "v_perm_b32 v109, v173, v169, s63\n"
      "v_perm_b32 v113, v174, v170, s62\n"
      "v_perm_b32 v117, v174, v170, s63\n"
      "v_perm_b32 v121, v175, v171, s62\n"
      "v_perm_b32 v125, v175, v171, s63\n"
      "s_waitcnt vmcnt(5)\n"
      "s_nop 0\n"
      "s_waitcnt vmcnt(4)\n"
      "s_nop 0\n"
      "v_perm_b32 v98, v180, v176, s62\n"
      "v_perm_b32 v102, v180, v176, s63\n"
      "v_perm_b32 v106, v181, v177, s62\n"
      "v_perm_b32 v110, v181, v177, s63\n"
      "v_perm_b32 v114, v182, v178, s62\n"
      "v_perm_b32 v118, v182, v178, s63\n"
      "v_perm_b32 v122, v183, v179, s62\n"
      "v_perm_b32 v126, v183, v179, s63\n"
      "s_waitcnt vmcnt(3)\n"
      "s_nop 0\n"
      "s_waitcnt vmcnt(2)\n"
      "s_nop 0\n"
      "v_perm_b32 v99, v188, v184, s62\n"
      "v_perm_b32 v103, v188, v184, s63\n"
      "v_perm_b32 v107, v189, v185, s62\n"
      "v_perm_b32 v111, v189, v185, s63\n"
      "v_perm_b32 v115, v190, v186, s62\n"
      "v_perm_b32 v119, v190, v186, s63\n"
      "v_perm_b32 v123, v191, v187, s62\n"
      "v_perm_b32 v127, v191, v187, s63\n"
      "v_mov_b32 v137, s15\n"
      "v_mov_b32 v139, s15\n"
      "v_mov_b32 v141, s15\n"
      "v_mov_b32 v143, s15\n"
      "s_movk_i32 s59, 0x7fff\n"
      "s_waitcnt vmcnt(0)\n"
      "s_nop 0\n"
      ".Ldg%=:\n"
      "v_xor_b32 v232, s15, v225\n"
      "v_bitop3_b32 v232, s15, v227, v232 bitop3:0xbe\n"
      "v_bitop3_b32 v232, s15, v229, v232 bitop3:0xbe\n"
      "v_bitop3_b32 v232, s15, v231, v232 bitop3:0xbe\n"
      "v_cmp_ne_u32 vcc, 0, v232\n"
      "v_lshlrev_b32 v232, 16, v224\n"
      "v_and_b32 v233, 0xffff0000, v224\n"
      "v_lshlrev_b32 v234, 16, v226\n"
      "v_and_b32 v235, 0xffff0000, v226\n"
      "s_cbranch_vccnz .Ldgs%=\n"
      ".Ldgo%=:\n"
      "v_lshlrev_b32 v236, 16, v228\n"
      "v_and_b32 v237, 0xffff0000, v228\n"
      "v_lshlrev_b32 v238, 16, v230\n"
      "v_and_b32 v239, 0xffff0000, v230\n"
      "v_pk_mul_f32 v[232:233], v[232:233], v[236:237]\n"
      "v_pk_mul_f32 v[234:235], v[234:235], v[238:239]\n"
      "v_cvt_pk_bf16_f32 v232, v232, v233\n"
      "v_cvt_pk_bf16_f32 v233, v234, v235\n"
      "ds_write_b64 v36, v[232:233] \n"
      "ds_write_b32 v41, v137 offset:96\n"
      "ds_read_b128 v[240:243], v42 offset:32\n"
      "ds_read_b128 v[224:227], v37 \n"
      "ds_read_b128 v[228:231], v37 offset:1152\n"
      "s_movk_i32 s59, 0x7fff\n"
      ".Ldv%=:\n"
      "s_waitcnt lgkmcnt(2)\n"
      "v_xor_b32 v240, s15, v240\n"
      "v_bitop3_b32 v240, s15, v241, v240 bitop3:0xbe\n"
      "v_bitop3_b32 v240, s15, v242, v240 bitop3:0xbe\n"
      "v_bitop3_b32 v240, s15, v243, v240 bitop3:0xbe\n"
      "v_cmp_ne_u32 vcc, 0, v240\n"
      "s_nop 1\n"
      "s_cbranch_vccnz .Ldvs%=\n"
      ".Ldvo%=:\n"
      "s_waitcnt lgkmcnt(1)\n"
      "v_mfma_f32_16x16x32_bf16 v[192:195], v[64:67], v[224:227], 0\n"
      "s_waitcnt lgkmcnt(0)\n"
      "v_mfma_f32_16x16x32_bf16 v[192:195], v[96:99], v[228:231], v[192:195]\n"
      "v_mfma_f32_16x16x32_bf16 v[196:199], v[68:71], v[224:227], 0\n"
      "v_mfma_f32_16x16x32_bf16 v[196:199], v[100:103], v[228:231], v[196:199]\n"
      "v_mfma_f32_16x16x32_bf16 v[200:203], v[72:75], v[224:227], 0\n"
      "s_nop 4\n"
      "v_permlane32_swap_b32 v192, v193\n"
      "v_permlane32_swap_b32 v194, v195\n"
      "v_mfma_f32_16x16x32_bf16 v[200:203], v[104:107], v[228:231], v[200:203]\n"
      "v_mfma_f32_16x16x32_bf16 v[204:207], v[76:79], v[224:227], 0\n"
      "v_permlane32_swap_b32 v196, v197\n"
      "v_permlane32_swap_b32 v198, v199\n"
      "v_mfma_f32_16x16x32_bf16 v[204:207], v[108:111], v[228:231], v[204:207]\n"
      "v_add_f32_dpp v128, v193, v192 row_ror:8 row_mask:0xf bank_mask:0x3\n"
      "s_nop 1\n"
      "v_add_f32_dpp v128, v194, v195 row_ror:8 row_mask:0xf bank_mask:0xc\n"
      "v_mfma_f32_16x16x32_bf16 v[208:211], v[80:83], v[224:227], 0\n"
      "v_permlane32_swap_b32 v200, v201\n"
      "v_permlane32_swap_b32 v202, v203\n"
      "v_mfma_f32_16x16x32_bf16 v[208:211], v[112:115], v[228:231], v[208:211]\n"
      "v_add_f32_dpp v129, v197, v196 row_ror:8 row_mask:0xf bank_mask:0x3\n"
      "s_nop 1\n"
      "v_add_f32_dpp v129, v198, v199 row_ror:8 row_mask:0xf bank_mask:0xc\n"
      "v_mfma_f32_16x16x32_bf16 v[212:215], v[84:87], v[224:227], 0\n"
      "v_permlane32_swap_b32 v204, v205\n"
      "v_permlane32_swap_b32 v206, v207\n"
      "v_mfma_f32_16x16x32_bf16 v[212:215], v[116:119], v[228:231], v[212:215]\n"
      "v_add_f32_dpp v130, v201, v200 row_ror:8 row_mask:0xf bank_mask:0x3\n"
      "s_nop 1\n"
      "v_add_f32_dpp v130, v202, v203 row_ror:8 row_mask:0xf bank_mask:0xc\n"
      "v_mfma_f32_16x16x32_bf16 v[216:219], v[88:91], v[224:227], 0\n"
      "v_permlane32_swap_b32 v208, v209\n"
      "v_permlane32_swap_b32 v210, v211\n"
      "v_mfma_f32_16x16x32_bf16 v[216:219], v[120:123], v[228:231], v[216:219]\n"
      "v_add_f32_dpp v131, v205, v204 row_ror:8 row_mask:0xf bank_mask:0x3\n"
      "s_nop 1\n"
      "v_add_f32_dpp v131, v206, v207 row_ror:8 row_mask:0xf bank_mask:0xc\n"
      "v_mfma_f32_16x16x32_bf16 v[220:223], v[92:95], v[224:227], 0\n"
      "v_permlane32_swap_b32 v212, v213\n"
      "v_permlane32_swap_b32 v214, v215\n"
      "v_mfma_f32_16x16x32_bf16 v[220:223], v[124:127], v[228:231], v[220:223]\n"
      "v_add_f32_dpp v132, v209, v208 row_ror:8 row_mask:0xf bank_mask:0x3\n"
      "s_nop 1\n"
      "v_add_f32_dpp v132, v210, v211 row_ror:8 row_mask:0xf bank_mask:0xc\n"
      "v_cvt_pk_bf16_f32 v136, v128, v129\n"
      "v_cvt_pk_bf16_f32 v138, v130, v131\n"
      "buffer_store_dwordx4 v[136:139], v38, s[16:19], s47 offen offset:0 sc1\n"
      "v_permlane32_swap_b32 v216, v217\n"
      "v_permlane32_swap_b32 v218, v219\n"
      "v_add_f32_dpp v133, v213, v212 row_ror:8 row_mask:0xf bank_mask:0x3\n"
      "s_nop 1\n"
      "v_add_f32_dpp v133, v214, v215 row_ror:8 row_mask:0xf bank_mask:0xc\n"
      "v_permlane32_swap_b32 v220, v221\n"
      "v_permlane32_swap_b32 v222, v223\n"
      "v_add_f32_dpp v134, v217, v216 row_ror:8 row_mask:0xf bank_mask:0x3\n"
      "s_nop 1\n"
      "v_add_f32_dpp v134, v218, v219 row_ror:8 row_mask:0xf bank_mask:0xc\n"
      "v_add_f32_dpp v135, v221, v220 row_ror:8 row_mask:0xf bank_mask:0x3\n"
      "s_nop 1\n"
      "v_add_f32_dpp v135, v222, v223 row_ror:8 row_mask:0xf bank_mask:0xc\n"
      "v_cvt_pk_bf16_f32 v140, v132, v133\n"
      "v_cvt_pk_bf16_f32 v142, v134, v135\n"
      "buffer_store_dwordx4 v[140:143], v38, s[16:19], s47 offen offset:128 sc1\n"
      "s_endpgm\n"
      ".LZ%=:\n"
      "s_setprio 0\n"
      "v_and_b32 v48, 63, v0\n"
      "v_and_b32 v49, 7, v0\n"
      "v_lshlrev_b32 v108, 3, v49\n"
      "s_lshl4_add_u32 s35, s48, s49\n"
      "s_lshl_b32 s41, s35, 4\n"
      "s_and_b32 s43, s41, 0xffffff80\n"
      "s_add_u32 s43, s43, 67584\n"
      "buffer_load_dwordx2 v[104:105], v108, s[16:19], s43 offen sc1\n"
      "s_mul_i32 s44, s35, 7168\n"
      "s_add_u32 s44, s44, 4194304\n"
      "s_add_u32 s67, s44, 0x1000\n"
      "v_lshrrev_b32 v109, 5, v48\n"
      "v_and_b32 v110, 8, v48\n"
      "v_lshl_add_u32 v109, v109, 2, v110\n"
      "v_and_b32 v113, 16, v48\n"
      "s_movk_i32 s59, 0x7fff\n"
      "s_waitcnt vmcnt(0)\n"
      "s_movk_i32 s61, 0x7ff\n"
      ".Ldzh%=:\n"
      "v_cmp_ne_u32 vcc, s15, v105\n"
      "v_bitop3_b32 v110, v104, s61, v104 bitop3:0x0c\n"
      "v_not_b32 v112, v104\n"
      "s_cbranch_vccnz .Ldzs%=\n"
      ".Ldzo%=:\n"
      "v_lshl_add_u32 v110, v110, 11, v109\n"
      "v_lshlrev_b32 v108, 4, v48\n"
      "buffer_load_dword v111, v110, s[20:23], s41 offen\n"
      "v_lshl_or_b32 v113, v113, 26, v110\n"
      "v_ashrrev_i32 v112, 31, v112\n"
      "v_or_b32 v112, 0x80000000, v112\n"
      "v_xor_b32 v112, v112, v104\n"
      "v_and_b32 v112, 0xffff0000, v112\n"
      "v_mul_f32 v112, 0xbfb8aa3b, v112\n"
      "v_exp_f32 v112, v112\n"
      "s_nop 0\n"
      "v_add_f32 v112, 1.0, v112\n"
      "v_rcp_f32 v112, v112\n"
      "s_nop 0\n"
      "v_cvt_pk_bf16_f32 v112, v112, v112\n"
      "v_and_b32 v112, 0xffff0000, v112\n"
      ".Ldpc2_%=:\n"
      "s_memrealtime s[64:65]\n"
      "s_waitcnt lgkmcnt(0)\n"
      "s_sub_u32 s64, s64, s12\n"
      "s_cmp_ge_u32 s64, 815\n"
      "s_cbranch_scc1 .Ldpd2_%=\n"
      "s_sleep 1\n"
      "s_branch .Ldpc2_%=\n"
      ".Ldpd2_%=:\n"
      "buffer_load_dwordx4 v[64:67], v108, s[16:19], s44 offen offset:0 sc1\n"
      "buffer_load_dwordx4 v[68:71], v108, s[16:19], s44 offen offset:1024 sc1\n"
      "buffer_load_dwordx4 v[72:75], v108, s[16:19], s44 offen offset:2048 sc1\n"
      "buffer_load_dwordx4 v[76:79], v108, s[16:19], s44 offen offset:3072 sc1\n"
      "buffer_load_dwordx4 v[80:83], v108, s[16:19], s67 offen offset:0 sc1\n"
      "buffer_load_dwordx4 v[84:87], v108, s[16:19], s67 offen offset:1024 sc1\n"
      "buffer_load_dwordx4 v[88:91], v108, s[16:19], s67 offen offset:2048 sc1\n"
      "s_movk_i32 s59, 0x7fff\n"
      "s_mov_b32 s56, 0x3f80\n"
      "s_mov_b32 s57, 0x3f800000\n"
      "s_waitcnt vmcnt(7)\n"
      "v_lshlrev_b32 v106, 16, v111\n"
      "v_and_b32 v107, 0xffff0000, v111\n"
      ".Ldq%=:\n"
      "s_waitcnt vmcnt(6)\n"
      "v_xor_b32 v100, s15, v65\n"
      "v_xor_b32 v101, s15, v67\n"
      "v_lshlrev_b32 v96, 16, v64\n"
      "v_and_b32 v97, 0xffff0000, v64\n"
      "v_lshlrev_b32 v98, 16, v66\n"
      "v_and_b32 v99, 0xffff0000, v66\n"
      "s_waitcnt vmcnt(5)\n"
      "v_dot2_f32_bf16 v96, v68, s56, v96\n"
      "v_dot2_f32_bf16 v97, v68, s57, v97\n"
      "v_dot2_f32_bf16 v98, v70, s56, v98\n"
      "v_dot2_f32_bf16 v99, v70, s57, v99\n"
      "v_bitop3_b32 v100, s15, v69, v100 bitop3:0xbe\n"
      "v_bitop3_b32 v101, s15, v71, v101 bitop3:0xbe\n"
      "s_waitcnt vmcnt(4)\n"
      "v_dot2_f32_bf16 v96, v72, s56, v96\n"
      "v_dot2_f32_bf16 v97, v72, s57, v97\n"
      "v_dot2_f32_bf16 v98, v74, s56, v98\n"
      "v_dot2_f32_bf16 v99, v74, s57, v99\n"
      "v_bitop3_b32 v100, s15, v73, v100 bitop3:0xbe\n"
      "v_bitop3_b32 v101, s15, v75, v101 bitop3:0xbe\n"
      "s_waitcnt vmcnt(3)\n"
      "v_dot2_f32_bf16 v96, v76, s56, v96\n"
      "v_dot2_f32_bf16 v97, v76, s57, v97\n"
      "v_dot2_f32_bf16 v98, v78, s56, v98\n"
      "v_dot2_f32_bf16 v99, v78, s57, v99\n"
      "v_bitop3_b32 v100, s15, v77, v100 bitop3:0xbe\n"
      "v_bitop3_b32 v101, s15, v79, v101 bitop3:0xbe\n"
      "s_waitcnt vmcnt(2)\n"
      "v_dot2_f32_bf16 v96, v80, s56, v96\n"
      "v_dot2_f32_bf16 v97, v80, s57, v97\n"
      "v_dot2_f32_bf16 v98, v82, s56, v98\n"
      "v_dot2_f32_bf16 v99, v82, s57, v99\n"
      "v_bitop3_b32 v100, s15, v81, v100 bitop3:0xbe\n"
      "v_bitop3_b32 v101, s15, v83, v101 bitop3:0xbe\n"
      "s_waitcnt vmcnt(1)\n"
      "v_dot2_f32_bf16 v96, v84, s56, v96\n"
      "v_dot2_f32_bf16 v97, v84, s57, v97\n"
      "v_dot2_f32_bf16 v98, v86, s56, v98\n"
      "v_dot2_f32_bf16 v99, v86, s57, v99\n"
      "v_bitop3_b32 v100, s15, v85, v100 bitop3:0xbe\n"
      "v_bitop3_b32 v101, s15, v87, v101 bitop3:0xbe\n"
      "s_waitcnt vmcnt(0)\n"
      "v_dot2_f32_bf16 v96, v88, s56, v96\n"
      "v_dot2_f32_bf16 v97, v88, s57, v97\n"
      "v_dot2_f32_bf16 v98, v90, s56, v98\n"
      "v_dot2_f32_bf16 v99, v90, s57, v99\n"
      "v_bitop3_b32 v100, s15, v89, v100 bitop3:0xbe\n"
      "v_bitop3_b32 v101, s15, v91, v101 bitop3:0xbe\n"
      "v_or_b32 v100, v100, v101\n"
      "v_cmp_ne_u32 vcc, 0, v100\n"
      "v_permlane32_swap_b32 v96, v98\n"
      "v_permlane32_swap_b32 v97, v99\n"
      "s_cbranch_vccnz .Ldqs%=\n"
      ".Ldqo%=:\n"
      "v_add_f32 v96, v96, v98\n"
      "v_add_f32 v97, v97, v99\n"
      "v_mov_b32 v98, v96\n"
      "v_mov_b32 v99, v97\n"
      "s_nop 0\n"
      "v_permlane16_swap_b32 v96, v98\n"
      "v_permlane16_swap_b32 v97, v99\n"
      "v_add_f32 v96, v96, v98\n"
      "v_add_f32 v97, v97, v99\n"
      "v_cvt_pk_bf16_f32 v96, v96, v97\n"
      "v_and_b32 v97, 0xffff0000, v96\n"
      "v_lshlrev_b32 v96, 16, v96\n"
      "v_mul_f32 v97, v97, v112\n"
      "v_mul_f32 v96, v96, v112\n"
      "v_cvt_pk_bf16_f32 v96, v96, v97\n"
      "v_and_b32 v97, 0xffff0000, v96\n"
      "v_lshlrev_b32 v96, 16, v96\n"
      "v_add_f32 v97, v97, v107\n"
      "v_add_f32 v96, v96, v106\n"
      "v_cvt_pk_bf16_f32 v96, v96, v97\n"
      "buffer_atomic_pk_add_bf16 v96, v113, s[24:27], s41 offen\n"
      "s_endpgm\n"
      ".Ldgs%=:\n"
      "s_sleep 1\n"
      "s_sub_u32 s59, s59, 1\n"
      "s_cbranch_scc1 .Ldgo%=\n"
      "buffer_load_dwordx4 v[224:227], v35, s[16:19], s46 offen sc1\n"
      "buffer_load_dwordx4 v[228:231], v35, s[16:19], s46 offen offset:2048 sc1\n"
      "s_waitcnt vmcnt(0)\n"
      "s_branch .Ldg%=\n"
      ".Ldvs%=:\n"
      "s_sub_u32 s59, s59, 1\n"
      "s_cbranch_scc1 .Ldvo%=\n"
      "s_waitcnt lgkmcnt(0)\n"
      "ds_read_b128 v[240:243], v42 offset:32\n"
      "ds_read_b128 v[224:227], v37 \n"
      "ds_read_b128 v[228:231], v37 offset:1152\n"
      "s_waitcnt lgkmcnt(0)\n"
      "s_branch .Ldv%=\n"
      ".Ldzs%=:\n"
      "s_sleep 2\n"
      "s_sub_u32 s59, s59, 1\n"
      "s_cbranch_scc1 .Ldzo%=\n"
      "buffer_load_dwordx2 v[104:105], v108, s[16:19], s43 offen sc1\n"
      "s_waitcnt vmcnt(0)\n"
      "s_branch .Ldzh%=\n"
      ".Ldqs%=:\n"
      "s_sleep 1\n"
      "s_sub_u32 s59, s59, 1\n"
      "s_cbranch_scc1 .Ldqo%=\n"
      "v_cmp_ne_u32 vcc, s15, v65\n"
      "v_cmp_ne_u32 s[64:65], s15, v67\n"
      "s_nop 0\n"
      "s_or_b64 exec, vcc, s[64:65]\n"
      "s_cbranch_scc0 .Ldqk0%=\n"
      "buffer_load_dwordx4 v[64:67], v108, s[16:19], s44 offen offset:0 sc1\n"
      ".Ldqk0%=:\n"
      "s_mov_b64 exec, -1\n"
      "v_cmp_ne_u32 vcc, s15, v69\n"
      "v_cmp_ne_u32 s[64:65], s15, v71\n"
      "s_nop 0\n"
      "s_or_b64 exec, vcc, s[64:65]\n"
      "s_cbranch_scc0 .Ldqk1%=\n"
      "buffer_load_dwordx4 v[68:71], v108, s[16:19], s44 offen offset:1024 sc1\n"
      ".Ldqk1%=:\n"
      "s_mov_b64 exec, -1\n"
      "v_cmp_ne_u32 vcc, s15, v73\n"
      "v_cmp_ne_u32 s[64:65], s15, v75\n"
      "s_nop 0\n"
      "s_or_b64 exec, vcc, s[64:65]\n"
      "s_cbranch_scc0 .Ldqk2%=\n"
      "buffer_load_dwordx4 v[72:75], v108, s[16:19], s44 offen offset:2048 sc1\n"
      ".Ldqk2%=:\n"
      "s_mov_b64 exec, -1\n"
      "v_cmp_ne_u32 vcc, s15, v77\n"
      "v_cmp_ne_u32 s[64:65], s15, v79\n"
      "s_nop 0\n"
      "s_or_b64 exec, vcc, s[64:65]\n"
      "s_cbranch_scc0 .Ldqk3%=\n"
      "buffer_load_dwordx4 v[76:79], v108, s[16:19], s44 offen offset:3072 sc1\n"
      ".Ldqk3%=:\n"
      "s_mov_b64 exec, -1\n"
      "v_cmp_ne_u32 vcc, s15, v81\n"
      "v_cmp_ne_u32 s[64:65], s15, v83\n"
      "s_nop 0\n"
      "s_or_b64 exec, vcc, s[64:65]\n"
      "s_cbranch_scc0 .Ldqk4%=\n"
      "buffer_load_dwordx4 v[80:83], v108, s[16:19], s67 offen offset:0 sc1\n"
      ".Ldqk4%=:\n"
      "s_mov_b64 exec, -1\n"
      "v_cmp_ne_u32 vcc, s15, v85\n"
      "v_cmp_ne_u32 s[64:65], s15, v87\n"
      "s_nop 0\n"
      "s_or_b64 exec, vcc, s[64:65]\n"
      "s_cbranch_scc0 .Ldqk5%=\n"
      "buffer_load_dwordx4 v[84:87], v108, s[16:19], s67 offen offset:1024 sc1\n"
      ".Ldqk5%=:\n"
      "s_mov_b64 exec, -1\n"
      "v_cmp_ne_u32 vcc, s15, v89\n"
      "v_cmp_ne_u32 s[64:65], s15, v91\n"
      "s_nop 0\n"
      "s_or_b64 exec, vcc, s[64:65]\n"
      "s_cbranch_scc0 .Ldqk6%=\n"
      "buffer_load_dwordx4 v[88:91], v108, s[16:19], s67 offen offset:2048 sc1\n"
      ".Ldqk6%=:\n"
      "s_mov_b64 exec, -1\n"
      "s_waitcnt vmcnt(0)\n"
      "s_branch .Ldq%=\n"
      ".LT%=:\n"
      "v_mov_b32 v48, 0x22680\n"
      "ds_read_b32 v48, v48 \n"
      "s_waitcnt lgkmcnt(0)\n"
      "v_readfirstlane_b32 s12, v48\n"
      "v_mov_b32 v48, v43\n"
      "v_and_b32 v49, 7, v43\n"
      "v_lshrrev_b32 v50, 3, v43\n"
      "s_lshl_b32 s42, s34, 3\n"
      "s_add_u32 s42, s42, 2048\n"
      "s_lshl_b32 s43, s48, 8\n"
      "s_and_b32 s50, s49, 1\n"
      "s_lshl_b32 s50, s50, 7\n"
      "s_add_u32 s43, s43, s50\n"
      "s_add_u32 s43, s43, 65536\n"
      "s_mov_b32 s20, s64\n"
      "s_and_b32 s21, s65, 0xffff\n"
      "s_mov_b32 s22, 0x400000\n"
      "s_mov_b32 s23, 0x20000\n"
      "s_mov_b32 s28, s84\n"
      "s_and_b32 s29, s85, 0xffff\n"
      "s_mov_b32 s30, 0x700000\n"
      "s_mov_b32 s31, 0x20000\n"
      "s_sleep 0\n"
      ".LS%=:\n"
      "v_lshlrev_b32 v112, 4, v0\n"
      "v_lshlrev_b32 v113, 2, v0\n"
      "v_sub_u32 v113, 0xfff, v113\n"
      "v_lshlrev_b32 v115, 2, v48\n"
      "v_mov_b32 v116, 0\n"
      "ds_write_b32 v115, v116 offset:27136\n"
      "ds_write_b32 v116, v116 offset:26400\n"
      "v_add_u32 v115, 0x140, v115\n"
      "v_mov_b32 v114, v115\n"
      "s_lshl_b32 s50, s33, 2\n"
      "s_add_u32 s50, s50, 256\n"
      "v_writelane_b32 v114, s50, 63\n"
      "s_lshl_b32 s50, s33, 5\n"
      "v_lshl_add_u32 v117, v49, 2, s50\n"
      "v_lshlrev_b32 v118, 5, v50\n"
      "s_and_b32 s50, s49, 1\n"
      "s_lshl_b32 s50, s50, 10\n"
      "v_lshl_add_u32 v119, v50, 7, s50\n"
      "s_lshr_b32 s51, s33, 2\n"
      "s_mul_i32 s51, s51, 61440\n"
      "s_add_u32 s51, s51, 393216\n"
      "s_mov_b32 s52, 0x80008000\n"
      "s_mov_b32 s53, 0xffff0000\n"
      "s_movk_i32 s59, 0x7fff\n"
      ".Lt_pc4_%=:\n"
      "s_memrealtime s[64:65]\n"
      "s_waitcnt lgkmcnt(0)\n"
      "s_sub_u32 s64, s64, s12\n"
      "s_cmp_ge_u32 s64, 300\n"
      "s_cbranch_scc1 .Lt_pd4_%=\n"
      "s_sleep 1\n"
      "s_branch .Lt_pc4_%=\n"
      ".Lt_pd4_%=:\n"
      ".Lt_p%=:\n"
      "buffer_load_dwordx4 v[72:75], v112, s[16:19], s51 offen sc1\n"
      "s_waitcnt vmcnt(0)\n"
      "v_cmp_ne_u32 vcc, s15, v73\n"
      "v_cmp_ne_u32 s[54:55], s15, v75\n"
      "v_pk_ashrrev_i16 v81, 15, v72 op_sel_hi:[0,1]\n"
      "v_pk_ashrrev_i16 v82, 15, v74 op_sel_hi:[0,1]\n"
      "s_or_b64 vcc, vcc, s[54:55]\n"
      "s_cbranch_scc1 .Lt_ps%=\n"
      ".Lt_po%=:\n"
      "v_bitop3_b32 v81, v72, v81, s52 bitop3:0x1e\n"
      "v_bitop3_b32 v82, v74, v82, s52 bitop3:0x1e\n"
      "v_lshl_or_b32 v76, v81, 16, v113\n"
      "v_and_or_b32 v77, v81, s53, v113\n"
      "v_lshl_or_b32 v78, v82, 16, v113\n"
      "v_and_or_b32 v79, v82, s53, v113\n"
      "v_add_u32 v77, -1, v77\n"
      "v_add_u32 v78, -2, v78\n"
      "v_add_u32 v79, -3, v79\n"
      "v_max3_u32 v80, v76, v77, v78\n"
      "v_max_u32 v80, v80, v79\n"
      "v_mov_b32 v104, v80\n"
      "v_med3_u32 v105, v76, v77, v78\n"
      "v_max_u32_dpp v80, v80, v80 row_shr:1 row_mask:0xf bank_mask:0xf bound_ctrl:1\n"
      "v_max3_u32 v83, v76, v77, v78\n"
      "v_min3_u32 v106, v76, v77, v78\n"
      "v_max_u32_dpp v80, v80, v80 row_shr:2 row_mask:0xf bank_mask:0xf bound_ctrl:1\n"
      "v_med3_u32 v106, v105, v106, v79\n"
      "v_med3_u32 v105, v83, v105, v79\n"
      "v_max_u32_dpp v80, v80, v80 row_shr:4 row_mask:0xf bank_mask:0xf bound_ctrl:1\n"
      "v_mov_b32 v107, 0x100\n"
      "s_nop 0\n"
      "v_max_u32_dpp v80, v80, v80 row_shr:8 row_mask:0xf bank_mask:0xf bound_ctrl:1\n"
      "s_nop 1\n"
      "v_max_u32_dpp v80, v80, v80 row_bcast:15 row_mask:0xa bank_mask:0xf\n"
      "s_nop 1\n"
      "v_max_u32_dpp v80, v80, v80 row_bcast:31 row_mask:0xc bank_mask:0xf\n"
      "ds_write_b32 v114, v80 offset:26112\n"
      "s_waitcnt lgkmcnt(0)\n"
      "s_barrier\n"
      "ds_read_b128 v[92:95], v116 offset:26368\n"
      "ds_read_b128 v[96:99], v116 offset:26384\n"
      "s_waitcnt lgkmcnt(1)\n"
      "v_min3_u32 v81, v92, v93, v94\n"
      "s_waitcnt lgkmcnt(0)\n"
      "v_min3_u32 v82, v96, v97, v98\n"
      "v_min3_u32 v81, v81, v95, v99\n"
      "v_min_u32 v81, v81, v82\n"
      "v_cmp_ge_u32 s[62:63], v105, v81\n"
      "v_cmp_ge_u32 vcc, v106, v81\n"
      "v_cmp_ge_u32 s[60:61], v104, v81\n"
      "v_cndmask_b32 v82, 1, 2, s[62:63]\n"
      "v_cndmask_b32 v82, v82, v107, vcc\n"
      "s_mov_b64 exec, s[60:61]\n"
      "ds_add_rtn_u32 v83, v116, v82 offset:26400\n"
      "s_waitcnt lgkmcnt(0)\n"
      "v_min_u32 v83, 0x50, v83\n"
      "v_lshlrev_b32 v83, 2, v83\n"
      "ds_write_b32 v83, v104 offset:27136\n"
      "s_mov_b64 exec, s[62:63]\n"
      "ds_write_b32 v83, v105 offset:27140\n"
      "s_mov_b64 exec, -1\n"
      "s_waitcnt lgkmcnt(0)\n"
      "s_barrier\n"
      "s_memrealtime s[56:57]\n"
      "ds_read_b32 v81, v116 offset:26400\n"
      ".Lt_r%=:\n"
      "ds_read_b32 v100, v117 offset:27136\n"
      "ds_read_b128 v[92:95], v118 offset:27136\n"
      "ds_read_b128 v[96:99], v118 offset:27152\n"
      "v_mov_b32 v101, s15\n"
      "v_mov_b32 v89, 0\n"
      "s_waitcnt lgkmcnt(3)\n"
      "v_cmp_lt_u32 vcc, 64, v81\n"
      "s_movk_i32 s50, 0x7ff\n"
      "s_cbranch_vccnz .Lt_m%=\n"
      "s_waitcnt lgkmcnt(1)\n"
      "v_cmp_gt_u32 s[60:61], v92, v100\n"
      "v_cmp_gt_u32 s[62:63], v93, v100\n"
      "v_cmp_gt_u32 s[64:65], v94, v100\n"
      "v_cmp_gt_u32 s[66:67], v95, v100\n"
      "s_waitcnt lgkmcnt(0)\n"
      "v_cmp_gt_u32 s[68:69], v96, v100\n"
      "v_cmp_gt_u32 s[70:71], v97, v100\n"
      "v_cmp_gt_u32 s[72:73], v98, v100\n"
      "v_cmp_gt_u32 s[74:75], v99, v100\n"
      "v_bitop3_b32 v81, v100, s50, v100 bitop3:0x0c\n"
      "v_addc_co_u32 v89, vcc, 0, v89, s[60:61]\n"
      "v_addc_co_u32 v89, vcc, 0, v89, s[62:63]\n"
      "v_addc_co_u32 v89, vcc, 0, v89, s[64:65]\n"
      "v_addc_co_u32 v89, vcc, 0, v89, s[66:67]\n"
      "v_addc_co_u32 v89, vcc, 0, v89, s[68:69]\n"
      "v_addc_co_u32 v89, vcc, 0, v89, s[70:71]\n"
      "v_addc_co_u32 v89, vcc, 0, v89, s[72:73]\n"
      "v_addc_co_u32 v89, vcc, 0, v89, s[74:75]\n"
      "v_lshl_add_u32 v82, v81, 11, v119\n"
      "v_lshrrev_b32 v86, 9, v81\n"
      "v_mul_u32_u24 v86, 0xf000, v86\n"
      "v_lshl_add_u32 v81, v81, 3, v86\n"
      "v_add_u32_dpp v89, v89, v89 row_ror:8 row_mask:0xf bank_mask:0xf\n"
      "v_mov_b32 v83, v89\n"
      "s_nop 1\n"
      "v_permlane16_swap_b32 v89, v83\n"
      "v_add_u32 v89, v89, v83\n"
      "v_mov_b32 v83, v89\n"
      "s_nop 1\n"
      "v_permlane32_swap_b32 v89, v83\n"
      "v_add_u32 v89, v89, v83\n"
      "v_cmp_gt_u32 vcc, 8, v89\n"
      "v_lshlrev_b32 v89, 3, v89\n"
      "s_mov_b32 s51, 131072\n"
      "s_cbranch_vccz .Lt_n%=\n"
      "s_and_b64 exec, vcc, 0xff\n"
      "ds_write_b32 v89, v100 offset:26752\n"
      "buffer_load_dword v84, v81, s[16:19], s51 offen sc1\n"
      "s_mov_b64 exec, vcc\n"
      "buffer_load_dword v85, v82, s[20:23], 0 offen\n"
      "s_mov_b64 exec, -1\n"
      ".Lt_n%=:\n"
      "v_lshlrev_b32 v89, 3, v49\n"
      "s_waitcnt lgkmcnt(0)\n"
      "s_barrier\n"
      "s_sub_u32 s12, s56, 390\n"
      "s_movk_i32 s57, 64\n"
      "s_cmp_lg_u32 s33, 0\n"
      "s_cbranch_scc1 .Lt_a%=\n"
      "ds_read_b32 v100, v89 offset:26752\n"
      "v_writelane_b32 v89, s57, 8\n"
      "s_waitcnt lgkmcnt(0)\n"
      "v_writelane_b32 v100, s12, 8\n"
      "s_mov_b64 exec, 0x1ff\n"
      "buffer_store_dwordx2 v[100:101], v89, s[16:19], s43 offen sc1\n"
      "s_mov_b64 exec, -1\n"
      "s_nop 1\n"
      "v_mov_b32 v96, s15\n"
      "v_mov_b32 v97, s15\n"
      "buffer_store_dwordx2 v[96:97], off, s[16:19], s42 sc1\n"
      ".Lt_a%=:\n"
      "s_cmp_lg_u32 s33, 1\n"
      "s_cbranch_scc1 .Lt_c%=\n"
      "ds_read_b32 v100, v89 offset:26752\n"
      "v_lshlrev_b32 v81, 3, v50\n"
      "ds_read_b32 v81, v81 offset:26752\n"
      "v_mov_b32 v92, 0\n"
      "v_mov_b32 v93, 0\n"
      "v_mov_b32 v94, 0\n"
      "v_mov_b32 v95, 0\n"
      "s_and_b32 s51, s43, 0x7ff\n"
      "v_lshlrev_b32 v82, 4, v49\n"
      "s_waitcnt lgkmcnt(0)\n"
      "v_bitop3_b32 v81, v81, s50, v81 bitop3:0x0c\n"
      "v_lshl_add_u32 v81, v81, 11, v82\n"
      "buffer_store_dwordx4 v[92:95], v81, s[24:27], s51 offen sc1\n"
      "s_waitcnt vmcnt(0)\n"
      "s_mov_b64 exec, 0xff\n"
      "buffer_store_dwordx2 v[100:101], v89, s[16:19], s43 offen offset:2048 sc1\n"
      "s_mov_b64 exec, -1\n"
      "s_nop 1\n"
      ".Lt_c%=:\n"
      "s_cmp_lg_u32 s33, 3\n"
      "s_cbranch_scc1 .Lt_z%=\n"
      "s_sleep 55\n"
      "s_branch .LZ%=\n"
      ".Lt_z%=:\n"
      "s_cmp_lg_u32 s34, 127\n"
      "s_cbranch_scc1 .Lt_e%=\n"
      "s_cmp_lg_u32 s33, 0\n"
      "s_cbranch_scc1 .Lt_e%=\n"
      "s_movk_i32 s51, 2048\n"
      "s_movk_i32 s59, 0x7fff\n"
      ".Lt_k%=:\n"
      "s_sleep 16\n"
      "buffer_load_dwordx4 v[92:95], v112, s[16:19], s51 offen sc1\n"
      "buffer_load_dwordx4 v[96:99], v112, s[16:19], s51 offen offset:1024 sc1\n"
      "s_sub_u32 s59, s59, 1\n"
      "s_cbranch_scc1 .Lt_kb%=\n"
      "s_waitcnt vmcnt(0)\n"
      "v_xor_b32 v86, s15, v93\n"
      "v_bitop3_b32 v86, s15, v95, v86 bitop3:0xbe\n"
      "v_bitop3_b32 v86, s15, v97, v86 bitop3:0xbe\n"
      "v_bitop3_b32 v86, s15, v99, v86 bitop3:0xbe\n"
      "v_cmp_ne_u32 vcc, 0, v86\n"
      "s_nop 1\n"
      "s_cbranch_vccnz .Lt_k%=\n"
      ".Lt_kb%=:\n"
      "buffer_store_dword v101, off, s[16:19], 0 sc1\n"
      ".Lt_e%=:\n"
      "s_waitcnt vmcnt(0)\n"
      "s_endpgm\n"
      ".Lt_ps%=:\n"
      "s_sleep 1\n"
      "s_sub_u32 s59, s59, 1\n"
      "s_cbranch_scc0 .Lt_p%=\n"
      "s_branch .Lt_po%=\n"
      ".Lt_m%=:\n"
      "s_waitcnt lgkmcnt(0)\n"
      "s_barrier\n"
      "s_mov_b32 s59, 0\n"
      "v_mov_b32 v90, 0\n"
      ".Lt_ml%=:\n"
      "s_nop 1\n"
      "v_max3_u32 v80, v76, v77, v78\n"
      "v_max_u32 v80, v80, v79\n"
      "s_nop 1\n"
      "v_max_u32_dpp v80, v80, v80 row_shr:1 row_mask:0xf bank_mask:0xf bound_ctrl:1\n"
      "s_nop 1\n"
      "v_max_u32_dpp v80, v80, v80 row_shr:2 row_mask:0xf bank_mask:0xf bound_ctrl:1\n"
      "s_nop 1\n"
      "v_max_u32_dpp v80, v80, v80 row_shr:4 row_mask:0xf bank_mask:0xf bound_ctrl:1\n"
      "s_nop 1\n"
      "v_max_u32_dpp v80, v80, v80 row_shr:8 row_mask:0xf bank_mask:0xf bound_ctrl:1\n"
      "s_nop 1\n"
      "v_max_u32_dpp v80, v80, v80 row_bcast:15 row_mask:0xa bank_mask:0xf\n"
      "s_nop 1\n"
      "v_max_u32_dpp v80, v80, v80 row_bcast:31 row_mask:0xc bank_mask:0xf\n"
      "s_nop 0\n"
      "v_readlane_b32 s50, v80, 63\n"
      "s_mov_b32 m0, s59\n"
      "s_nop 0\n"
      "v_cmp_ne_u32 vcc, s50, v76\n"
      "s_nop 1\n"
      "v_cndmask_b32 v76, 0, v76, vcc\n"
      "v_cmp_ne_u32 vcc, s50, v77\n"
      "s_nop 1\n"
      "v_cndmask_b32 v77, 0, v77, vcc\n"
      "v_cmp_ne_u32 vcc, s50, v78\n"
      "s_nop 1\n"
      "v_cndmask_b32 v78, 0, v78, vcc\n"
      "v_cmp_ne_u32 vcc, s50, v79\n"
      "s_nop 1\n"
      "v_cndmask_b32 v79, 0, v79, vcc\n"
      "v_writelane_b32 v90, s50, m0\n"
      "s_add_u32 s59, s59, 1\n"
      "s_cmp_lt_u32 s59, 8\n"
      "s_cbranch_scc1 .Lt_ml%=\n"
      "s_mov_b64 exec, 0xff\n"
      "ds_write_b32 v117, v90 offset:27136\n"
      "s_mov_b64 exec, -1\n"
      "v_mov_b32 v81, 0\n"
      "s_waitcnt lgkmcnt(0)\n"
      "s_barrier\n"
      "s_branch .Lt_r%=\n"
      ".LEnd%=:\n"
      : "={s[80:95]}"(sts), "={s[96:99]}"(st2)
      : [ka] "s"(__builtin_amdgcn_kernarg_segment_ptr()), [bid] "s"(blockIdx.x), "{v0}"(threadIdx.x), "s"((u32)(uintptr_t)sm)
      : "v8", "v9", "v10", "v11", "v12", "v13", "v14", "v15", "v16", "v17", "v18", "v19", "v20", "v21", "v22", "v23", "v24", "v25", "v26", "v27",
        "v28", "v29", "v30", "v31", "v32", "v33", "v34", "v35", "v36", "v37", "v38", "v39", "v40", "v41", "v42", "v43", "v44", "v45", "v46", "v47",
        "v48", "v49", "v50", "v51", "v52", "v53", "v54", "v55", "v56", "v57", "v58", "v59", "v60", "v61", "v62", "v63", "v64", "v65", "v66", "v67",
        "v68", "v69", "v70", "v71", "v72", "v73", "v74", "v75", "v76", "v77", "v78", "v79", "v80", "v81", "v82", "v83", "v84", "v85", "v86", "v87",
        "v88", "v89", "v90", "v91", "v92", "v93", "v94", "v95", "v96", "v97", "v98", "v99", "v100", "v101", "v102", "v103", "v104", "v105", "v106", "v107",
        "v108", "v109", "v110", "v111", "v112", "v113", "v114", "v115", "v116", "v117", "v118", "v119", "v120", "v121", "v122", "v123", "v124", "v125", "v126", "v127",
        "v128", "v129", "v130", "v131", "v132", "v133", "v134", "v135", "v136", "v137", "v138", "v139", "v140", "v141", "v142", "v143", "v144", "v145", "v146", "v147",
        "v148", "v149", "v150", "v151", "v152", "v153", "v154", "v155", "v156", "v157", "v158", "v159", "v160", "v161", "v162", "v163", "v164", "v165", "v166", "v167",
        "v168", "v169", "v170", "v171", "v172", "v173", "v174", "v175", "v176", "v177", "v178", "v179", "v180", "v181", "v182", "v183", "v184", "v185", "v186", "v187",
        "v188", "v189", "v190", "v191", "v192", "v193", "v194", "v195", "v196", "v197", "v198", "v199", "v200", "v201", "v202", "v203", "v204", "v205", "v206", "v207",
        "v208", "v209", "v210", "v211", "v212", "v213", "v214", "v215", "v216", "v217", "v218", "v219", "v220", "v221", "v222", "v223", "v224", "v225", "v226", "v227",
        "v228", "v229", "v230", "v231", "v232", "v233", "v234", "v235", "v236", "v237", "v238", "v239", "v240", "v241", "v242", "v243", "v244", "v245", "v246", "v247",
        "v248", "v249", "v250", "v251", "v252", "v253", "v254", "v255", "s12", "s13", "s14", "s15", "s16", "s17", "s18", "s19", "s20", "s21", "s22", "s23",
        "s24", "s25", "s26", "s27", "s28", "s29", "s30", "s31", "s33", "s34", "s35", "s36", "s37", "s38", "s39", "s40", "s41", "s42", "s43", "s44",
        "s45", "s46", "s47", "s48", "s49", "s50", "s51", "s52", "s53", "s54", "s55", "s56", "s57", "s58", "s59", "s60", "s61", "s62", "s63", "s64",
        "s65", "s66", "s67", "s68", "s69", "s70", "s71", "s72", "s73", "s74", "s75", "s76", "s77", "s78", "s79",
        "vcc", "scc", "memory");
  const int b = blockIdx.x;
  GETST();
  STAMP(11);
  PEND();
}

extern "C" void kernel_launch(void* const* d_in, const int* in_sizes, int n_in, void* d_out, int out_size,
                              void* d_ws, size_t ws_size, hipStream_t stream) {
  const u16* const* in = (const u16* const*)d_in;
  fused<<<256, 512, 0, stream>>>(in[0], in[1], in[2], in[3], in[4], in[5], in[8], in[6], in[9], in[10], in[11],
                                 (u16*)d_out, (char*)d_ws);
}
```

```cpp
#include <hip/hip_runtime.h>
#include <stdint.h>

typedef unsigned short u16;
typedef unsigned int u32;
typedef unsigned long long u64;
typedef __attribute__((ext_vector_type(4))) u32 u32x4;
typedef __attribute__((ext_vector_type(16))) u32 u32x16;
#define AS3 __attribute__((address_space(3)))

#define STAMP(I)
#define PEND()
#define GETST()
constexpr int SPIN_MAX = 1 << 16;
constexpr int UNUSED = 0;

__global__ __launch_bounds__(512, 2) void fused(const u16* __restrict__ x, const u16* __restrict__ lar,
    const u16* __restrict__ aim, const u16* __restrict__ cre, const u16* __restrict__ cim,
    const u16* __restrict__ ldt, const u16* __restrict__ wr, const u16* __restrict__ dsk,
    const u16* __restrict__ wg, const u16* __restrict__ wu, const u16* __restrict__ wd,
    u16* __restrict__ out, char* ws) {
  __shared__ __attribute__((aligned(16))) char smem_[163840];
  AS3 char* sm = (AS3 char*)smem_;
  u64 ptm = 0, pc0 = 0, pt0 = 0, pte = 0, st_[16] = {};
  u32x16 sts;
  u32x4 st2;
  asm volatile(
      "s_memrealtime s[12:13]\n"
      "s_load_dwordx16 s[64:79], %[ka], 0x0\n"
      "s_load_dwordx8 s[80:87], %[ka], 0x40\n"
      "s_load_dwordx2 s[88:89], %[ka], 0x60\n"
      "v_readfirstlane_b32 s33, v0\n"
      "s_getpc_b64 s[44:45]\n"
      ".Lpc%=:\n"
      "s_mov_b32 s34, %[bid]\n"
      "v_and_b32 v43, 63, v0\n"
      "v_mov_b32 v15, 0\n"
      "s_lshr_b32 s33, s33, 6\n"
      "s_and_b32 s52, s34, -8\n"
      "s_add_u32 s52, s52, s33\n"
      "s_getpc_b64 s[46:47]\n"
      "s_add_u32 s46, s46, .Ltab%=@rel32@lo+4\n"
      "s_addc_u32 s47, s47, .Ltab%=@rel32@hi+12\n"
      "s_sub_u32 s53, s52, 232\n"
      "s_cselect_b64 s[44:45], s[44:45], s[46:47]\n"
      "s_cselect_b32 s52, s52, s53\n"
      "s_min_u32 s52, s52, (.LEnd%=-.Lpc%=)>>7\n"
      "s_lshl_b32 s52, s52, 7\n"
      "s_and_b32 s44, s44, 0xffffff80\n"
      "s_add_u32 s44, s44, s52\n"
      "s_addc_u32 s45, s45, 0\n"
      "global_load_dword v11, v15, s[44:45] \n"
      "s_and_b32 s48, s34, 7\n"
      "s_lshr_b32 s49, s34, 3\n"
      "s_and_b32 s55, s49, 15\n"
      "s_lshl4_add_u32 s35, s48, s55\n"
      "s_cmp_lt_u32 s34, 128\n"
      "s_cbranch_scc1 .LH%=\n"
      "s_lshl3_add_u32 s50, s35, s33\n"
      "s_lshl_b32 s51, s50, 8\n"
      "s_lshl_b32 s50, s50, 1\n"
      "v_lshl_add_u32 v48, v43, 2, s51\n"
      "v_mov_b32 v49, s50\n"
      "v_lshlrev_b32 v59, 4, v43\n"
      "s_waitcnt lgkmcnt(0)\n"
      "global_load_dword v16, v48, s[66:67] nt\n"
      "global_load_dword v17, v48, s[68:69] nt\n"
      "global_load_ushort v12, v49, s[74:75] \n"
      "global_load_dword v18, v48, s[70:71] nt\n"
      "global_load_dword v19, v48, s[72:73] nt\n"
      "global_load_ushort v13, v49, s[78:79] \n"
      "global_load_dwordx4 v[64:67], v59, s[46:47] \n"
      "global_load_dwordx4 v[68:71], v59, s[46:47] offset:1024\n"
      "global_load_dwordx4 v[44:47], v59, s[46:47] offset:2048\n"
      "s_load_dword s15, s[88:89], 0x0\n"
      "s_setprio 3\n"
      "s_branch .LC%=\n"
      ".LH%=:\n"
      "s_cmp_ge_u32 s33, 4\n"
      "s_cbranch_scc1 .LG%=\n"
      ".LD%=:\n"
      "v_and_b32 v52, 48, v43\n"
      "v_and_b32 v53, 3, v43\n"
      "v_lshl_add_u32 v53, v53, 2, v52\n"
      "v_and_b32 v54, 12, v43\n"
      "v_lshlrev_b32 v53, 2, v53\n"
      "v_lshl_add_u32 v52, v54, 9, v53\n"
      "s_and_b32 s53, s34, 127\n"
      "s_and_b32 s52, s33, 3\n"
      "s_lshl2_add_u32 s14, s53, s52\n"
      "s_lshl_b32 s51, s14, 13\n"
      "v_lshl_add_u32 v53, v54, 6, v53\n"
      "s_waitcnt lgkmcnt(0)\n"
      "s_add_u32 s52, s64, s51\n"
      "s_addc_u32 s53, s65, 0\n"
      "global_load_dwordx4 v[224:227], v53, s[76:77] offset:0\n"
      "global_load_dwordx4 v[228:231], v53, s[76:77] offset:1024\n"
      "global_load_dwordx4 v[192:195], v52, s[52:53] offset:0\n"
      "global_load_dwordx4 v[196:199], v52, s[52:53] offset:256\n"
      "global_load_dwordx4 v[200:203], v52, s[52:53] offset:512\n"
      "global_load_dwordx4 v[204:207], v52, s[52:53] offset:768\n"
      "global_load_dwordx4 v[208:211], v52, s[52:53] offset:1024\n"
      "global_load_dwordx4 v[212:215], v52, s[52:53] offset:1280\n"
      "global_load_dwordx4 v[216:219], v52, s[52:53] offset:1536\n"
      "global_load_dwordx4 v[220:223], v52, s[52:53] offset:1792\n"
      "s_load_dword s15, s[88:89], 0x0\n"
      "s_setprio 3\n"
      "s_cmp_lg_u32 s33, 0\n"
      "s_cbranch_scc1 .Ltb%=\n"
      "v_mov_b32 v48, 0x22680\n"
      "v_mov_b32 v49, s12\n"
      "ds_write_b32 v48, v49 \n"
      "v_and_b32 v50, 31, v43\n"
      "v_lshlrev_b32 v50, 2, v50\n"
      "v_add_u32 v50, 0x22600, v50\n"
      "ds_write_b32 v50, v15 \n"
      "s_waitcnt lgkmcnt(0)\n"
      ".Ltb%=:\n"
      "s_barrier\n"
      "s_mov_b32 s16, s88\n"
      "s_and_b32 s17, s89, 0xffff\n"
      "s_mov_b32 s18, 0x6e0000\n"
      "s_mov_b32 s19, 0x20000\n"
      "s_mov_b32 s20, s64\n"
      "s_and_b32 s21, s65, 0xffff\n"
      "s_mov_b32 s22, 0x400000\n"
      "s_mov_b32 s23, 0x20000\n"
      "s_mov_b32 s28, s84\n"
      "s_and_b32 s29, s85, 0xffff\n"
      "s_mov_b32 s30, 0x700000\n"
      "s_mov_b32 s31, 0x20000\n"
      "s_mov_b32 s24, s86\n"
      "s_and_b32 s25, s87, 0xffff\n"
      "s_mov_b32 s26, 0x400000\n"
      "s_mov_b32 s27, 0x20000\n"
      "s_lshl_b32 s42, s34, 3\n"
      "s_add_u32 s42, s42, 2048\n"
      "s_lshl_b32 s43, s48, 8\n"
      "s_and_b32 s50, s49, 1\n"
      "s_lshl_b32 s50, s50, 7\n"
      "s_add_u32 s43, s43, s50\n"
      "s_add_u32 s43, s43, 65536\n"
      "v_mov_b32 v20, 0x3f803f80\n"
      "v_mov_b32 v21, 0x3f803f80\n"
      "v_mov_b32 v22, 0x3f803f80\n"
      "v_mov_b32 v23, 0x3f803f80\n"
      "v_mov_b32 v44, 0x7f000000\n"
      "v_mov_b32 v25, 0\n"
      "s_lshr_b32 s70, s14, 7\n"
      "s_mul_i32 s70, s70, 61440\n"
      "s_lshl_b32 s68, s14, 5\n"
      "s_add_u32 s68, s68, s70\n"
      "s_add_u32 s68, s68, 131072\n"
      "s_lshr_b32 s70, s14, 8\n"
      "s_mul_i32 s70, s70, 61440\n"
      "s_lshl4_add_u32 s69, s14, s70\n"
      "s_add_u32 s69, s69, 393216\n"
      "v_writelane_b32 v44, s68, 0\n"
      "s_add_u32 s68, s68, 8\n"
      "v_writelane_b32 v44, s68, 20\n"
      "s_add_u32 s68, s68, 8\n"
      "v_writelane_b32 v44, s68, 40\n"
      "s_add_u32 s68, s68, 8\n"
      "v_writelane_b32 v44, s68, 60\n"
      "s_add_u32 s68, s68, 8\n"
      "v_writelane_b32 v44, s69, 1\n"
      "s_add_u32 s69, s69, 8\n"
      "v_writelane_b32 v44, s69, 2\n"
      "s_waitcnt lgkmcnt(0)\n"
      "s_add_u32 s15, s15, 0x9e3779b1\n"
      "v_mov_b32 v47, s15\n"
      "s_waitcnt vmcnt(9)\n"
      "v_mfma_f32_16x16x32_bf16 v[16:19], v[20:23], v[224:227], 0\n"
      "s_waitcnt vmcnt(8)\n"
      "v_mfma_f32_16x16x32_bf16 v[16:19], v[20:23], v[228:231], v[16:19]\n"
      "s_waitcnt vmcnt(7)\n"
      "v_mfma_f32_16x16x32_bf16 v[232:235], v[192:195], v[224:227], 0\n"
      "v_mfma_f32_16x16x32_bf16 v[252:255], v[192:195], v[192:195], 0\n"
      "v_mfma_f32_16x16x32_bf16 v[248:251], v[192:195], v[20:23], 0\n"
      "s_waitcnt vmcnt(6)\n"
      "v_mfma_f32_16x16x32_bf16 v[236:239], v[196:199], v[224:227], 0\n"
      "v_mfma_f32_16x16x32_bf16 v[252:255], v[196:199], v[196:199], v[252:255]\n"
      "v_mfma_f32_16x16x32_bf16 v[248:251], v[196:199], v[20:23], v[248:251]\n"
      "s_waitcnt vmcnt(5)\n"
      "v_mfma_f32_16x16x32_bf16 v[240:243], v[200:203], v[224:227], 0\n"
      "v_mfma_f32_16x16x32_bf16 v[252:255], v[200:203], v[200:203], v[252:255]\n"
      "v_mfma_f32_16x16x32_bf16 v[248:251], v[200:203], v[20:23], v[248:251]\n"
      "s_waitcnt vmcnt(4)\n"
      "v_mfma_f32_16x16x32_bf16 v[244:247], v[204:207], v[224:227], 0\n"
      "v_mfma_f32_16x16x32_bf16 v[252:255], v[204:207], v[204:207], v[252:255]\n"
      "v_mfma_f32_16x16x32_bf16 v[248:251], v[204:207], v[20:23], v[248:251]\n"
      "s_waitcnt vmcnt(3)\n"
      "v_mfma_f32_16x16x32_bf16 v[232:235], v[208:211], v[228:231], v[232:235]\n"
      "v_mfma_f32_16x16x32_bf16 v[252:255], v[208:211], v[208:211], v[252:255]\n"
      "v_mfma_f32_16x16x32_bf16 v[248:251], v[208:211], v[20:23], v[248:251]\n"
      "v_add_f32_dpp v16, v16, v16 quad_perm:[1,0,3,2] row_mask:0xf bank_mask:0xf\n"
      "s_waitcnt vmcnt(2)\n"
      "v_mfma_f32_16x16x32_bf16 v[236:239], v[212:215], v[228:231], v[236:239]\n"
      "v_mfma_f32_16x16x32_bf16 v[252:255], v[212:215], v[212:215], v[252:255]\n"
      "v_mfma_f32_16x16x32_bf16 v[248:251], v[212:215], v[20:23], v[248:251]\n"
      "v_add_f32_dpp v16, v16, v16 quad_perm:[2,3,0,1] row_mask:0xf bank_mask:0xf\n"
      "v_add_f32_dpp v25, v232, v25 quad_perm:[0,0,0,0] row_mask:0xf bank_mask:0x1\n"
      "s_nop 1\n"
      "v_add_f32_dpp v25, v233, v25 quad_perm:[1,1,1,1] row_mask:0xf bank_mask:0x1\n"
      "s_nop 1\n"
      "v_add_f32_dpp v25, v234, v25 quad_perm:[2,2,2,2] row_mask:0xf bank_mask:0x1\n"
      "s_nop 1\n"
      "v_add_f32_dpp v25, v235, v25 quad_perm:[3,3,3,3] row_mask:0xf bank_mask:0x1\n"
      "s_waitcnt vmcnt(1)\n"
      "v_mfma_f32_16x16x32_bf16 v[240:243], v[216:219], v[228:231], v[240:243]\n"
      "v_mfma_f32_16x16x32_bf16 v[252:255], v[216:219], v[216:219], v[252:255]\n"
      "v_mfma_f32_16x16x32_bf16 v[248:251], v[216:219], v[20:23], v[248:251]\n"
      "v_add_f32_dpp v16, v16, v16 row_ror:4 row_mask:0xf bank_mask:0xf\n"
      "v_add_f32_dpp v25, v236, v25 quad_perm:[0,0,0,0] row_mask:0xf bank_mask:0x2\n"
      "s_nop 1\n"
      "v_add_f32_dpp v25, v237, v25 quad_perm:[1,1,1,1] row_mask:0xf bank_mask:0x2\n"
      "s_nop 1\n"
      "v_add_f32_dpp v25, v238, v25 quad_perm:[2,2,2,2] row_mask:0xf bank_mask:0x2\n"
      "s_nop 1\n"
      "v_add_f32_dpp v25, v239, v25 quad_perm:[3,3,3,3] row_mask:0xf bank_mask:0x2\n"
      "s_waitcnt vmcnt(0)\n"
      "v_mfma_f32_16x16x32_bf16 v[244:247], v[220:223], v[228:231], v[244:247]\n"
      "v_mfma_f32_16x16x32_bf16 v[252:255], v[220:223], v[220:223], v[252:255]\n"
      "v_mfma_f32_16x16x32_bf16 v[248:251], v[220:223], v[20:23], v[248:251]\n"
      "v_add_f32_dpp v16, v16, v16 row_ror:8 row_mask:0xf bank_mask:0xf\n"
      "v_add_f32_dpp v25, v240, v25 quad_perm:[0,0,0,0] row_mask:0xf bank_mask:0x4\n"
      "s_nop 1\n"
      "v_add_f32_dpp v25, v241, v25 quad_perm:[1,1,1,1] row_mask:0xf bank_mask:0x4\n"
      "s_nop 1\n"
      "v_add_f32_dpp v25, v242, v25 quad_perm:[2,2,2,2] row_mask:0xf bank_mask:0x4\n"
      "s_nop 1\n"
      "v_add_f32_dpp v25, v243, v25 quad_perm:[3,3,3,3] row_mask:0xf bank_mask:0x4\n"
      "s_nop 1\n"
      "v_add_f32_dpp v25, v244, v25 quad_perm:[0,0,0,0] row_mask:0xf bank_mask:0x8\n"
      "v_mov_b32_dpp v24, v252 quad_perm:[0,0,0,0] row_mask:0xf bank_mask:0xf\n"
      "v_mov_b32_dpp v27, v254 quad_perm:[2,2,2,2] row_mask:0xf bank_mask:0xf\n"
      "v_add_f32_dpp v25, v245, v25 quad_perm:[1,1,1,1] row_mask:0xf bank_mask:0x8\n"
      "v_add_f32 v248, v248, v249\n"
      "v_add_f32 v250, v250, v251\n"
      "v_add_f32_dpp v24, v253, v24 quad_perm:[1,1,1,1] row_mask:0xf bank_mask:0xf\n"
      "v_add_f32_dpp v27, v255, v27 quad_perm:[3,3,3,3] row_mask:0xf bank_mask:0xf\n"
      "v_add_f32_dpp v25, v246, v25 quad_perm:[2,2,2,2] row_mask:0xf bank_mask:0x8\n"
      "v_add_f32 v248, v248, v250\n"
      "v_add_f32 v24, v24, v27\n"
      "v_add_f32_dpp v25, v247, v25 quad_perm:[3,3,3,3] row_mask:0xf bank_mask:0x8\n"
      "v_mul_f32 v248, 0x3a800000, v248\n"
      "v_mul_f32 v24, 0x3a800000, v24\n"
      "v_add_f32_dpp v25, v25, v25 row_ror:4 row_mask:0xf bank_mask:0xf\n"
      "v_fma_f32 v24, -v248, v248, v24\n"
      "v_max_f32 v24, 0, v24\n"
      "v_add_f32_dpp v25, v25, v25 row_ror:8 row_mask:0xf bank_mask:0xf\n"
      "v_cvt_pk_bf16_f32 v248, v248, v24\n"
      "v_and_b32 v24, 0xffff0000, v248\n"
      "v_add_f32 v24, 0x3727c5ac, v24\n"
      "v_lshlrev_b32 v26, 16, v248\n"
      "v_cvt_pk_bf16_f32 v24, v24, v24\n"
      "v_fma_f32 v25, -v26, v16, v25\n"
      "v_and_b32 v24, 0xffff0000, v24\n"
      "v_sqrt_f32 v24, v24\n"
      "s_nop 0\n"
      "v_cvt_pk_bf16_f32 v46, v26, v24\n"
      "v_and_b32 v24, 0xffff0000, v46\n"
      "v_rcp_f32 v24, v24\n"
      "s_nop 0\n"
      "v_mul_f32 v25, v25, v24\n"
      "v_cvt_pk_bf16_f32 v25, v25, v25\n"
      "s_nop 0\n"
      "v_readlane_b32 s68, v25, 0\n"
      "v_readlane_b32 s69, v25, 20\n"
      "v_readlane_b32 s70, v25, 40\n"
      "v_readlane_b32 s71, v25, 60\n"
      "s_pack_ll_b32_b16 s68, s68, s69\n"
      "s_pack_ll_b32_b16 s70, s70, s71\n"
      "v_writelane_b32 v46, s68, 1\n"
      "v_writelane_b32 v46, s70, 2\n"
      "buffer_store_dwordx2 v[46:47], v44, s[16:19], 0 offen sc1\n"
      "v_mov_b32 v48, v43\n"
      "v_and_b32 v49, 7, v43\n"
      "v_lshrrev_b32 v50, 3, v43\n"
      "s_cmp_ge_u32 s34, 112\n"
      "s_cbranch_scc1 .LS%=\n"
      "s_branch .LD2%=\n"
      ".LC%=:\n"
      "s_mov_b32 s16, s88\n"
      "s_and_b32 s17, s89, 0xffff\n"
      "s_mov_b32 s18, 0x6e0000\n"
      "s_mov_b32 s19, 0x20000\n"
      "s_mov_b32 s20, s64\n"
      "s_and_b32 s21, s65, 0xffff\n"
      "s_mov_b32 s22, 0x400000\n"
      "s_mov_b32 s23, 0x20000\n"
      "s_mov_b32 s24, s86\n"
      "s_and_b32 s25, s87, 0xffff\n"
      "s_mov_b32 s26, 0x400000\n"
      "s_mov_b32 s27, 0x20000\n"
      "s_mul_i32 s39, s33, 6528\n"
      "s_mul_i32 s40, s33, 8768\n"
      "s_add_u32 s52, s40, 52224\n"
      "s_add_u32 s53, s52, 4160\n"
      "s_mul_i32 s54, s33, 4864\n"
      "s_add_u32 s54, s54, 122368\n"
      "s_lshl_b32 s41, s35, 4\n"
      "s_and_b32 s43, s41, 0xffffff80\n"
      "s_add_u32 s43, s43, 67584\n"
      "s_lshl_b32 s42, s34, 3\n"
      "s_add_u32 s42, s42, 2048\n"
      "v_and_b32 v48, 31, v43\n"
      "v_lshrrev_b32 v49, 5, v43\n"
      "v_lshl_add_u32 v52, v43, 2, s52\n"
      "v_mov_b32 v53, 0x3c00\n"
      "ds_write_b32 v52, v53 offset:2176\n"
      "ds_write_b32 v52, v53 offset:5440\n"
      "v_lshl_add_u32 v52, v43, 2, s39\n"
      "ds_write_b32 v52, v53 \n"
      "v_lshl_add_u32 v52, v48, 1, v49\n"
      "s_lshl_b32 s50, s33, 17\n"
      "v_lshl_add_u32 v8, v52, 11, s50\n"
      "v_lshl_add_u32 v58, v49, 8, v48\n"
      "s_lshl_b32 s50, s33, 5\n"
      "v_add_u32 v58, s50, v58\n"
      "s_mov_b32 s50, 122912\n"
      "v_lshl_add_u32 v32, v58, 2, s50\n"
      "v_lshlrev_b32 v41, 12, v58\n"
      "v_mul_u32_u24 v42, 0xf000, v49\n"
      "v_lshl_add_u32 v42, v58, 4, v42\n"
      "s_add_u32 s50, s54, 528\n"
      "v_add_u32 v34, s50, v59\n"
      "v_lshl_add_u32 v35, v43, 3, s39\n"
      "v_lshl_add_u32 v36, v43, 3, s52\n"
      "s_add_u32 s50, s39, 32\n"
      "v_add_u32 v37, s50, v59\n"
      "s_add_u32 s50, s52, 32\n"
      "v_add_u32 v38, s50, v59\n"
      "v_mov_b32 v54, 0\n"
      "v_mov_b32 v55, 0\n"
      "v_lshl_add_u32 v52, v43, 3, s54\n"
      "ds_write_b64 v52, v[54:55] offset:32\n"
      "s_mov_b32 s50, 161536\n"
      "v_lshl_add_u32 v52, v0, 2, s50\n"
      "ds_write_b32 v52, v54 \n"
      "s_sub_u32 s50, s52, s39\n"
      "v_bfe_u32 v52, v43, 4, 1\n"
      "v_mul_u32_u24 v28, s50, v52\n"
      "v_add_u32 v28, s39, v28\n"
      "s_lshl_b32 s44, s33, 6\n"
      "s_add_u32 s44, s44, 56384\n"
      "s_lshl4_add_u32 s58, s33, s55\n"
      "s_lshl_b32 s58, s58, 15\n"
      "s_and_b32 s51, s41, 0x80\n"
      "s_sub_u32 s58, s58, s51\n"
      "s_cmp_ge_u32 s33, 4\n"
      "s_cbranch_scc1 .Lyo%=\n"
      "s_bitcmp1_b32 s55, 0\n"
      "s_cselect_b64 s[56:57], s[82:83], s[80:81]\n"
      "s_lshr_b32 s50, s56, 7\n"
      "s_sub_u32 s50, s48, s50\n"
      "s_add_u32 s50, s50, 3\n"
      "s_and_b32 s50, s50, 7\n"
      "s_min_u32 s50, s50, 6\n"
      "s_mul_i32 s51, s48, 7\n"
      "s_add_u32 s50, s50, s51\n"
      "s_lshl_b32 s50, s50, 7\n"
      "s_lshr_b32 s51, s55, 1\n"
      "s_mul_i32 s51, s51, 172032\n"
      "s_add_u32 s50, s50, s51\n"
      "s_mul_i32 s51, s33, 1835008\n"
      "s_add_u32 s50, s50, s51\n"
      "s_add_u32 s50, s50, 458752\n"
      "v_min_u32 v14, 23, v43\n"
      "v_mul_u32_u24 v14, 0x1c00, v14\n"
      "v_add_u32 v14, s50, v14\n"
      "v_and_b32 v9, 7, v43\n"
      "v_lshlrev_b32 v9, 3, v9\n"
      "v_bfe_u32 v10, v43, 3, 2\n"
      "v_lshlrev_b32 v10, 2, v10\n"
      ".Lyo%=:\n"
      "v_bfe_u32 v52, v43, 1, 4\n"
      "v_and_b32 v53, 1, v43\n"
      "v_lshl_add_u32 v52, v52, 4, v53\n"
      "v_lshl_add_u32 v15, v52, 7, s58\n"
      ".Lsc%=:\n"
      "s_mov_b32 s60, s41\n"
      "s_add_u32 s61, s41, 0x200000\n"
      "s_mov_b32 s62, 131072\n"
      "s_mov_b32 s63, 262144\n"
      "s_mov_b32 m0, s40\n"
      "s_mov_b32 s64, 0x3fb8aa3b\n"
      "s_mov_b32 s66, 0x43800000\n"
      "s_mov_b32 s68, 0x42000000\n"
      "s_mov_b64 s[58:59], 1\n"
      "s_lshl_b32 s50, s33, 2\n"
      "s_add_u32 s50, s50, 161280\n"
      "v_mov_b32 v56, s50\n"
      "s_waitcnt vmcnt(3)\n"
      "s_mov_b32 s50, 0x3fb8aa3b\n"
      "v_lshlrev_b32 v48, 16, v12\n"
      "v_and_b32 v52, 0x7fff7fff, v18\n"
      "v_and_b32 v53, 0x7fff7fff, v19\n"
      "v_mul_f32 v49, s50, v48\n"
      "v_pk_max_u16 v52, v52, v53\n"
      "v_fma_f32 v50, v48, s50, -v49\n"
      "v_rndne_f32 v51, v49\n"
      "v_lshlrev_b32 v53, 16, v52\n"
      "v_fmamk_f32 v50, v48, 0x32a5705f, v50\n"
      "v_sub_f32 v49, v49, v51\n"
      "v_max_u32 v52, v52, v53\n"
      "v_add_f32 v49, v49, v50\n"
      "v_cvt_i32_f32 v51, v51\n"
      "v_max_u32_dpp v52, v52, v52 row_shr:1 row_mask:0xf bank_mask:0xf bound_ctrl:1\n"
      "v_exp_f32 v49, v49\n"
      "v_lshlrev_b32 v54, 16, v13\n"
      "v_max_u32_dpp v52, v52, v52 row_shr:2 row_mask:0xf bank_mask:0xf bound_ctrl:1\n"
      "v_ldexp_f32 v49, v49, v51\n"
      "v_add_f32 v54, 1.0, v54\n"
      "v_max_u32_dpp v52, v52, v52 row_shr:4 row_mask:0xf bank_mask:0xf bound_ctrl:1\n"
      "v_cvt_pk_bf16_f32 v49, v49, v49\n"
      "s_nop 0\n"
      "v_max_u32_dpp v52, v52, v52 row_shr:8 row_mask:0xf bank_mask:0xf bound_ctrl:1\n"
      "v_and_b32 v49, 0xffff0000, v49\n"
      "v_and_b32 v55, 0x7fffffff, v54\n"
      "v_max_u32_dpp v52, v52, v52 row_bcast:15 row_mask:0xa bank_mask:0xf\n"
      "v_mul_f32 v50, 0xbfb8aa3b, v49\n"
      "v_mul_f32 v51, 0xbe22f983, v49\n"
      "v_max_u32_dpp v52, v52, v52 row_bcast:31 row_mask:0xc bank_mask:0xf\n"
      "v_readfirstlane_b32 s36, v50\n"
      "v_readlane_b32 s50, v52, 63\n"
      "v_readfirstlane_b32 s37, v51\n"
      "v_mul_f32 v55, 0x3b800000, v55\n"
      "v_mul_f32 v52, s50, v49\n"
      "v_max_f32 v52, v52, v55\n"
      "v_frexp_exp_i32_f32 v52, v52\n"
      "s_nop 0\n"
      "v_readfirstlane_b32 s50, v52\n"
      "s_nop 0\n"
      "s_sub_i32 s50, 7, s50\n"
      "s_max_i32 s50, s50, -107\n"
      "s_min_i32 s50, s50, 93\n"
      "v_ldexp_f32 v49, v49, s50\n"
      "s_sub_i32 s51, s50, 1\n"
      "v_ldexp_f32 v54, v54, s51\n"
      "s_sub_i32 s14, 128, s50\n"
      "s_lshl_b32 s14, s14, 23\n"
      "v_readfirstlane_b32 s38, v49\n"
      "v_cndmask_b32 v31, 0, v54, s[58:59]\n"
      "v_mov_b32 v51, s14\n"
      "ds_write_b32 v56, v51 \n"
      "v_lshlrev_b32 v72, 16, v16\n"
      "v_and_b32 v73, 0xffff0000, v16\n"
      "v_lshlrev_b32 v74, 16, v17\n"
      "v_and_b32 v75, 0xffff0000, v17\n"
      "s_mov_b32 s70, s37\n"
      "v_pk_mul_f32 v[72:73], s[64:65], v[72:73] op_sel_hi:[0,1]\n"
      "v_pk_mul_f32 v[74:75], s[70:71], v[74:75] op_sel_hi:[0,1]\n"
      "v_exp_f32 v72, v72\n"
      "v_exp_f32 v73, v73\n"
      "v_pk_mul_f32 v[78:79], s[66:67], v[74:75] op_sel_hi:[0,1]\n"
      "v_pk_mul_f32 v[80:81], s[68:69], v[74:75] op_sel_hi:[0,1]\n"
      "v_fract_f32 v76, v74\n"
      "v_fract_f32 v77, v75\n"
      "v_cvt_pk_bf16_f32 v72, v72, v73\n"
      "v_cos_f32 v90, v76\n"
      "v_cos_f32 v91, v77\n"
      "v_fract_f32 v78, v78\n"
      "v_fract_f32 v79, v79\n"
      "v_and_b32 v73, 0xffff0000, v72\n"
      "v_lshlrev_b32 v72, 16, v72\n"
      "v_sin_f32 v92, v76\n"
      "v_sin_f32 v93, v77\n"
      "v_pk_mul_f32 v[82:83], s[36:37], v[72:73] op_sel_hi:[0,1]\n"
      "v_fract_f32 v80, v80\n"
      "v_fract_f32 v81, v81\n"
      "v_exp_f32 v84, v82\n"
      "v_exp_f32 v85, v83\n"
      "v_pk_mul_f32 v[86:87], s[66:67], v[82:83] op_sel_hi:[0,1]\n"
      "v_cos_f32 v94, v78\n"
      "v_cos_f32 v95, v79\n"
      "v_pk_mul_f32 v[88:89], s[68:69], v[82:83] op_sel_hi:[0,1]\n"
      "v_sin_f32 v96, v78\n"
      "v_sin_f32 v97, v79\n"
      "v_pk_mul_f32 v[90:91], v[90:91], v[84:85]\n"
      "v_exp_f32 v86, v86\n"
      "v_exp_f32 v87, v87\n"
      "v_pk_mul_f32 v[92:93], v[92:93], v[84:85]\n"
      "v_pk_mul_f32 v[82:83], v[92:93], v[92:93]\n"
      "v_cos_f32 v202, v80\n"
      "v_pk_add_f32 v[84:85], v[90:91], v[90:91]\n"
      "v_cos_f32 v150, v81\n"
      "v_pk_fma_f32 v[98:99], v[90:91], v[90:91], v[82:83] neg_lo:[0,0,1] neg_hi:[0,0,1]\n"
      "v_pk_mul_f32 v[94:95], v[94:95], v[86:87]\n"
      "v_pk_mul_f32 v[100:101], v[84:85], v[92:93]\n"
      "v_pk_mul_f32 v[96:97], v[96:97], v[86:87]\n"
      "v_cvt_pk_f16_f32 v208, v90, v92\n"
      "v_sin_f32 v203, v80\n"
      "v_cvt_pk_f16_f32 v156, v91, v93\n"
      "v_sin_f32 v151, v81\n"
      "v_pk_mul_f32 v[82:83], v[100:101], v[100:101]\n"
      "v_exp_f32 v88, v88\n"
      "v_pk_add_f32 v[84:85], v[98:99], v[98:99]\n"
      "v_exp_f32 v89, v89\n"
      "v_pk_fma_f32 v[102:103], v[98:99], v[98:99], v[82:83] neg_lo:[0,0,1] neg_hi:[0,0,1]\n"
      "v_pk_mul_f32 v[114:115], v[96:97], v[96:97]\n"
      "v_pk_mul_f32 v[104:105], v[84:85], v[100:101]\n"
      "v_pk_add_f32 v[116:117], v[94:95], v[94:95]\n"
      "v_cvt_pk_f16_f32 v209, v98, v100\n"
      "v_pk_fma_f32 v[106:107], v[94:95], v[94:95], v[114:115] neg_lo:[0,0,1] neg_hi:[0,0,1]\n"
      "v_cvt_pk_f16_f32 v157, v99, v101\n"
      "v_pk_mul_f32 v[108:109], v[116:117], v[96:97]\n"
      "v_pk_mul_f32 v[82:83], v[104:105], v[104:105]\n"
      "v_cvt_pk_f16_f32 v212, v94, v96\n"
      "v_pk_add_f32 v[84:85], v[102:103], v[102:103]\n"
      "v_cvt_pk_f16_f32 v160, v95, v97\n"
      "v_pk_fma_f32 v[90:91], v[102:103], v[102:103], v[82:83] neg_lo:[0,0,1] neg_hi:[0,0,1]\n"
      "v_pk_mul_f32 v[202:203], v[202:203], v[88:89] op_sel:[0,0] op_sel_hi:[1,0]\n"
      "v_pk_mul_f32 v[92:93], v[84:85], v[104:105]\n"
      "v_pk_mul_f32 v[150:151], v[150:151], v[88:89] op_sel:[0,1] op_sel_hi:[1,1]\n"
      "v_cvt_pk_f16_f32 v210, v102, v104\n"
      "s_waitcnt vmcnt(0)\n"
      "s_nop 0\n"
      "s_cmp_ge_u32 s33, 4\n"
      "s_cbranch_scc1 .LcR%=\n"
      "buffer_load_dword v11, v15, s[20:23], s41 offen\n"
      "global_load_dword v11, v14, s[56:57]\n"
      ".LcR%=:\n"
      "v_cvt_pk_f16_f32 v213, v106, v108\n"
      "v_cvt_pk_f16_f32 v158, v103, v105\n"
      "v_cvt_pk_f16_f32 v161, v107, v109\n"
      "v_pk_mul_f32 v[82:83], v[92:93], v[92:93]\n"
      "v_pk_mul_f32 v[114:115], v[108:109], v[108:109]\n"
      "v_pk_add_f32 v[84:85], v[90:91], v[90:91]\n"
      "v_pk_add_f32 v[116:117], v[106:107], v[106:107]\n"
      "v_pk_fma_f32 v[72:73], v[90:91], v[90:91], v[82:83] neg_lo:[0,0,1] neg_hi:[0,0,1]\n"
      "v_pk_fma_f32 v[94:95], v[106:107], v[106:107], v[114:115] neg_lo:[0,0,1] neg_hi:[0,0,1]\n"
      "v_pk_mul_f32 v[74:75], v[84:85], v[92:93]\n"
      "v_pk_mul_f32 v[96:97], v[116:117], v[108:109]\n"
      "v_cvt_pk_f16_f32 v205, v90, v92\n"
      "v_pk_mul_f16 v214, v212, v213 op_sel_hi:[0,1]\n"
      "v_cvt_pk_f16_f32 v153, v91, v93\n"
      "v_pk_mul_f16 v162, v160, v161 op_sel_hi:[0,1]\n"
      "v_pk_mul_f16 v211, v209, v210 op_sel_hi:[0,1]\n"
      "v_pk_fma_f16 v214, v212, v213, v214 op_sel:[1,1,0] op_sel_hi:[1,0,1] neg_lo:[0,1,0]\n"
      "v_pk_mul_f16 v159, v157, v158 op_sel_hi:[0,1]\n"
      "v_pk_fma_f16 v162, v160, v161, v162 op_sel:[1,1,0] op_sel_hi:[1,0,1] neg_lo:[0,1,0]\n"
      "v_pk_fma_f16 v211, v209, v210, v211 op_sel:[1,1,0] op_sel_hi:[1,0,1] neg_lo:[0,1,0]\n"
      "v_cvt_pk_f16_f32 v215, v94, v96\n"
      "v_pk_fma_f16 v159, v157, v158, v159 op_sel:[1,1,0] op_sel_hi:[1,0,1] neg_lo:[0,1,0]\n"
      "v_cvt_pk_f16_f32 v163, v95, v97\n"
      "v_cvt_pk_f16_f32 v206, v72, v74\n"
      "v_lshlrev_b32 v198, 16, v18\n"
      "v_cvt_pk_f16_f32 v154, v73, v75\n"
      "v_lshlrev_b32 v199, 16, v19\n"
      "v_pk_mul_f16 v207, v205, v206 op_sel_hi:[0,1]\n"
      "v_pk_mul_f32 v[198:199], s[38:39], v[198:199] op_sel_hi:[0,1]\n"
      "v_pk_mul_f16 v155, v153, v154 op_sel_hi:[0,1]\n"
      "v_and_b32 v146, 0xffff0000, v18\n"
      "v_pk_fma_f16 v207, v205, v206, v207 op_sel:[1,1,0] op_sel_hi:[1,0,1] neg_lo:[0,1,0]\n"
      "v_and_b32 v147, 0xffff0000, v19\n"
      "v_pk_fma_f16 v155, v153, v154, v155 op_sel:[1,1,0] op_sel_hi:[1,0,1] neg_lo:[0,1,0]\n"
      "v_pk_mul_f32 v[146:147], s[38:39], v[146:147] op_sel_hi:[0,1]\n"
      "s_waitcnt lgkmcnt(0)\n"
      "s_add_u32 s15, s15, 0x9e3779b1\n"
      "v_cvt_pk_f16_f32 v240, v198, v199\n"
      "v_cvt_pk_f16_f32 v241, v146, v147\n"
      "v_pk_mul_f32 v[200:201], v[198:199], v[202:203] op_sel_hi:[1,0]\n"
      "v_pk_mul_f32 v[148:149], v[146:147], v[150:151] op_sel_hi:[1,0]\n"
      "ds_write_addtid_b32 v240 offset:52224\n"
      "ds_write_addtid_b32 v241 offset:55488\n"
      "v_pk_fma_f32 v[200:201], v[198:199], v[202:203], v[200:201] op_sel:[1,1,0] op_sel_hi:[0,1,1] neg_hi:[0,1,0]\n"
      "v_pk_fma_f32 v[148:149], v[146:147], v[150:151], v[148:149] op_sel:[1,1,0] op_sel_hi:[0,1,1] neg_hi:[0,1,0]\n"
      "ds_write_addtid_b32 v205 offset:54672\n"
      "v_cvt_pk_f16_f32 v242, v200, v201\n"
      "v_cvt_pk_f16_f32 v243, v148, v149\n"
      "v_pk_mul_f32 v[198:199], v[200:201], v[202:203] op_sel_hi:[1,0]\n"
      "v_pk_mul_f32 v[146:147], v[148:149], v[150:151] op_sel_hi:[1,0]\n"
      "ds_write_addtid_b32 v242 offset:52496\n"
      "ds_write_addtid_b32 v243 offset:55760\n"
      "v_pk_fma_f32 v[198:199], v[200:201], v[202:203], v[198:199] op_sel:[1,1,0] op_sel_hi:[0,1,1] neg_hi:[0,1,0]\n"
      "v_pk_fma_f32 v[146:147], v[148:149], v[150:151], v[146:147] op_sel:[1,1,0] op_sel_hi:[0,1,1] neg_hi:[0,1,0]\n"
      "ds_write_addtid_b32 v153 offset:57936\n"
      "v_cvt_pk_f16_f32 v240, v198, v199\n"
      "v_cvt_pk_f16_f32 v241, v146, v147\n"
      "v_pk_mul_f32 v[200:201], v[198:199], v[202:203] op_sel_hi:[1,0]\n"
      "v_pk_mul_f32 v[148:149], v[146:147], v[150:151] op_sel_hi:[1,0]\n"
      "ds_write_addtid_b32 v240 offset:52768\n"
      "ds_write_addtid_b32 v241 offset:56032\n"
      "v_pk_fma_f32 v[200:201], v[198:199], v[202:203], v[200:201] op_sel:[1,1,0] op_sel_hi:[0,1,1] neg_hi:[0,1,0]\n"
      "v_pk_fma_f32 v[148:149], v[146:147], v[150:151], v[148:149] op_sel:[1,1,0] op_sel_hi:[0,1,1] neg_hi:[0,1,0]\n"
      "ds_write_addtid_b32 v206 offset:54944\n"
      "v_cvt_pk_f16_f32 v242, v200, v201\n"
      "v_cvt_pk_f16_f32 v243, v148, v149\n"
      "v_pk_mul_f32 v[198:199], v[200:201], v[202:203] op_sel_hi:[1,0]\n"
      "v_pk_mul_f32 v[146:147], v[148:149], v[150:151] op_sel_hi:[1,0]\n"
      "ds_write_addtid_b32 v242 offset:53040\n"
      "ds_write_addtid_b32 v243 offset:56304\n"
      "v_pk_fma_f32 v[198:199], v[200:201], v[202:203], v[198:199] op_sel:[1,1,0] op_sel_hi:[0,1,1] neg_hi:[0,1,0]\n"
      "v_pk_fma_f32 v[146:147], v[148:149], v[150:151], v[146:147] op_sel:[1,1,0] op_sel_hi:[0,1,1] neg_hi:[0,1,0]\n"
      "ds_write_addtid_b32 v154 offset:58208\n"
      "v_cvt_pk_f16_f32 v240, v198, v199\n"
      "v_cvt_pk_f16_f32 v241, v146, v147\n"
      "v_pk_mul_f32 v[200:201], v[198:199], v[202:203] op_sel_hi:[1,0]\n"
      "v_pk_mul_f32 v[148:149], v[146:147], v[150:151] op_sel_hi:[1,0]\n"
      "ds_write_addtid_b32 v240 offset:53312\n"
      "ds_write_addtid_b32 v241 offset:56576\n"
      "v_pk_fma_f32 v[200:201], v[198:199], v[202:203], v[200:201] op_sel:[1,1,0] op_sel_hi:[0,1,1] neg_hi:[0,1,0]\n"
      "v_pk_fma_f32 v[148:149], v[146:147], v[150:151], v[148:149] op_sel:[1,1,0] op_sel_hi:[0,1,1] neg_hi:[0,1,0]\n"
      "ds_write_addtid_b32 v207 offset:55216\n"
      "v_add_u32_sdwa v20, s52, v64 dst_sel:DWORD dst_unused:UNUSED_PAD src0_sel:DWORD src1_sel:WORD_0\n"
      "v_add_u32_sdwa v21, s39, v64 dst_sel:DWORD dst_unused:UNUSED_PAD src0_sel:DWORD src1_sel:WORD_1\n"
      "v_add_u32_sdwa v178, s52, v71 dst_sel:DWORD dst_unused:UNUSED_PAD src0_sel:DWORD src1_sel:WORD_0\n"
      "v_add_u32_sdwa v22, s53, v65 dst_sel:DWORD dst_unused:UNUSED_PAD src0_sel:DWORD src1_sel:WORD_0\n"
      "v_cvt_pk_f16_f32 v242, v200, v201\n"
      "v_cvt_pk_f16_f32 v243, v148, v149\n"
      "v_pk_mul_f32 v[198:199], v[200:201], v[202:203] op_sel_hi:[1,0]\n"
      "v_pk_mul_f32 v[146:147], v[148:149], v[150:151] op_sel_hi:[1,0]\n"
      "ds_write_addtid_b32 v242 offset:53584\n"
      "ds_write_addtid_b32 v243 offset:56848\n"
      "v_pk_fma_f32 v[198:199], v[200:201], v[202:203], v[198:199] op_sel:[1,1,0] op_sel_hi:[0,1,1] neg_hi:[0,1,0]\n"
      "v_pk_fma_f32 v[146:147], v[148:149], v[150:151], v[146:147] op_sel:[1,1,0] op_sel_hi:[0,1,1] neg_hi:[0,1,0]\n"
      "ds_write_addtid_b32 v155 offset:58480\n"
      "v_add_u32_sdwa v23, s53, v65 dst_sel:DWORD dst_unused:UNUSED_PAD src0_sel:DWORD src1_sel:WORD_1\n"
      "v_add_u32_sdwa v24, s53, v66 dst_sel:DWORD dst_unused:UNUSED_PAD src0_sel:DWORD src1_sel:WORD_0\n"
      "v_add_u32_sdwa v25, s53, v66 dst_sel:DWORD dst_unused:UNUSED_PAD src0_sel:DWORD src1_sel:WORD_1\n"
      "v_add_u32_sdwa v26, s53, v67 dst_sel:DWORD dst_unused:UNUSED_PAD src0_sel:DWORD src1_sel:WORD_0\n"
      "v_cvt_pk_f16_f32 v240, v198, v199\n"
      "v_cvt_pk_f16_f32 v241, v146, v147\n"
      "v_pk_mul_f32 v[200:201], v[198:199], v[202:203] op_sel_hi:[1,0]\n"
      "v_pk_mul_f32 v[148:149], v[146:147], v[150:151] op_sel_hi:[1,0]\n"
      "ds_write_addtid_b32 v240 offset:53856\n"
      "ds_write_addtid_b32 v241 offset:57120\n"
      "v_pk_fma_f32 v[200:201], v[198:199], v[202:203], v[200:201] op_sel:[1,1,0] op_sel_hi:[0,1,1] neg_hi:[0,1,0]\n"
      "v_pk_fma_f32 v[148:149], v[146:147], v[150:151], v[148:149] op_sel:[1,1,0] op_sel_hi:[0,1,1] neg_hi:[0,1,0]\n"
      "v_add_u32_sdwa v27, s53, v67 dst_sel:DWORD dst_unused:UNUSED_PAD src0_sel:DWORD src1_sel:WORD_1\n"
      "v_add_u32_sdwa v29, s53, v68 dst_sel:DWORD dst_unused:UNUSED_PAD src0_sel:DWORD src1_sel:WORD_0\n"
      "v_add_u32_sdwa v30, s53, v68 dst_sel:DWORD dst_unused:UNUSED_PAD src0_sel:DWORD src1_sel:WORD_1\n"
      "v_add_u32_sdwa v39, s53, v69 dst_sel:DWORD dst_unused:UNUSED_PAD src0_sel:DWORD src1_sel:WORD_0\n"
      "v_cvt_pk_f16_f32 v242, v200, v201\n"
      "v_cvt_pk_f16_f32 v243, v148, v149\n"
      "ds_write_addtid_b32 v242 offset:54128\n"
      "ds_write_addtid_b32 v243 offset:57392\n"
      "v_add_u32_sdwa v33, s54, v69 dst_sel:DWORD dst_unused:UNUSED_PAD src0_sel:DWORD src1_sel:WORD_1\n"
      "v_add_u32_sdwa v40, s44, v70 dst_sel:DWORD dst_unused:UNUSED_PAD src0_sel:DWORD src1_sel:WORD_0\n"
      "v_add_u32_sdwa v28, v28, v70 dst_sel:DWORD dst_unused:UNUSED_PAD src0_sel:DWORD src1_sel:WORD_1\n"
      "ds_read_b128 v[104:107], v20 offset:0\n"
      "ds_read_b128 v[108:111], v178 offset:0\n"
      "ds_read_b128 v[112:115], v20 offset:32\n"
      "ds_read_b128 v[116:119], v178 offset:32\n"
      "s_mov_b32 m0, s39\n"
      "s_setprio 2\n"
      "v_pk_mul_f16 v240, v209, v208 op_sel_hi:[0,1]\n"
      "v_pk_mul_f16 v241, v210, v208 op_sel_hi:[0,1]\n"
      "v_pk_mul_f16 v242, v211, v208 op_sel_hi:[0,1]\n"
      "ds_write_addtid_b32 v208 offset:272\n"
      "v_pk_fma_f16 v240, v209, v208, v240 op_sel:[1,1,0] op_sel_hi:[1,0,1] neg_lo:[0,1,0]\n"
      "ds_write_addtid_b32 v209 offset:544\n"
      "v_pk_fma_f16 v241, v210, v208, v241 op_sel:[1,1,0] op_sel_hi:[1,0,1] neg_lo:[0,1,0]\n"
      "ds_write_addtid_b32 v240 offset:816\n"
      "v_pk_fma_f16 v242, v211, v208, v242 op_sel:[1,1,0] op_sel_hi:[1,0,1] neg_lo:[0,1,0]\n"
      "ds_write_addtid_b32 v241 offset:1360\n"
      "ds_write_addtid_b32 v242 offset:1904\n"
      "v_pk_mul_f16 v243, v212, v209 op_sel_hi:[0,1]\n"
      "v_pk_mul_f16 v244, v212, v210 op_sel_hi:[0,1]\n"
      "v_pk_mul_f16 v245, v212, v211 op_sel_hi:[0,1]\n"
      "ds_write_addtid_b32 v210 offset:1088\n"
      "v_pk_fma_f16 v243, v212, v209, v243 op_sel:[1,1,0] op_sel_hi:[1,0,1] neg_lo:[0,1,0]\n"
      "ds_write_addtid_b32 v211 offset:1632\n"
      "v_pk_fma_f16 v244, v212, v210, v244 op_sel:[1,1,0] op_sel_hi:[1,0,1] neg_lo:[0,1,0]\n"
      "ds_write_addtid_b32 v243 offset:2992\n"
      "v_pk_fma_f16 v245, v212, v211, v245 op_sel:[1,1,0] op_sel_hi:[1,0,1] neg_lo:[0,1,0]\n"
      "ds_write_addtid_b32 v244 offset:3264\n"
      "ds_write_addtid_b32 v245 offset:4080\n"
      "v_pk_mul_f16 v246, v213, v210 op_sel_hi:[0,1]\n"
      "v_pk_mul_f16 v247, v214, v210 op_sel_hi:[0,1]\n"
      "v_pk_mul_f16 v248, v215, v215 op_sel_hi:[0,1]\n"
      "ds_write_addtid_b32 v212 offset:2176\n"
      "v_pk_fma_f16 v246, v213, v210, v246 op_sel:[1,1,0] op_sel_hi:[1,0,1] neg_lo:[0,1,0]\n"
      "ds_write_addtid_b32 v213 offset:5440\n"
      "v_pk_fma_f16 v247, v214, v210, v247 op_sel:[1,1,0] op_sel_hi:[1,0,1] neg_lo:[0,1,0]\n"
      "ds_write_addtid_b32 v246 offset:5984\n"
      "v_pk_fma_f16 v248, v215, v215, v248 op_sel:[1,1,0] op_sel_hi:[1,0,1] neg_lo:[0,1,0]\n"
      "ds_write_addtid_b32 v247 offset:6256\n"
      "ds_write_addtid_b32 v248 offset:2448\n"
      "v_pk_mul_f16 v249, v215, v212 op_sel_hi:[0,1]\n"
      "v_pk_mul_f16 v250, v215, v213 op_sel_hi:[0,1]\n"
      "v_pk_mul_f16 v251, v215, v214 op_sel_hi:[0,1]\n"
      "ds_write_addtid_b32 v214 offset:5712\n"
      "v_pk_fma_f16 v249, v215, v212, v249 op_sel:[1,1,0] op_sel_hi:[1,0,1] neg_lo:[0,1,0]\n"
      "ds_write_addtid_b32 v215 offset:4352\n"
      "v_pk_fma_f16 v250, v215, v213, v250 op_sel:[1,1,0] op_sel_hi:[1,0,1] neg_lo:[0,1,0]\n"
      "ds_write_addtid_b32 v249 offset:4624\n"
      "v_pk_fma_f16 v251, v215, v214, v251 op_sel:[1,1,0] op_sel_hi:[1,0,1] neg_lo:[0,1,0]\n"
      "ds_write_addtid_b32 v250 offset:4896\n"
      "ds_write_addtid_b32 v251 offset:5168\n"
      "ds_read_b128 v[72:75], v21 offset:0\n"
      "ds_read_b128 v[76:79], v21 offset:32\n"
      "ds_read_b128 v[80:83], v21 offset:64\n"
      "ds_read_b128 v[84:87], v21 offset:96\n"
      "ds_read_b128 v[88:91], v21 offset:128\n"
      "ds_read_b128 v[92:95], v21 offset:160\n"
      "ds_read_b128 v[96:99], v21 offset:192\n"
      "ds_read_b128 v[100:103], v21 offset:224\n"
      "ds_read_b128 v[120:123], v20 offset:64\n"
      "ds_read_b128 v[124:127], v178 offset:64\n"
      "s_waitcnt lgkmcnt(15)\n"
      "v_pk_mul_f16 v164, v104, v108 op_sel_hi:[0,1] neg_hi:[0,1]\n"
      "v_pk_mul_f16 v165, v105, v109 op_sel_hi:[0,1] neg_hi:[0,1]\n"
      "v_pk_mul_f16 v166, v106, v110 op_sel_hi:[0,1] neg_hi:[0,1]\n"
      "v_pk_mul_f16 v167, v107, v111 op_sel_hi:[0,1] neg_hi:[0,1]\n"
      "v_pk_fma_f16 v164, v104, v108, v164 op_sel:[1,1,0] op_sel_hi:[1,0,1]\n"
      "v_pk_fma_f16 v165, v105, v109, v165 op_sel:[1,1,0] op_sel_hi:[1,0,1]\n"
      "v_pk_fma_f16 v166, v106, v110, v166 op_sel:[1,1,0] op_sel_hi:[1,0,1]\n"
      "v_pk_fma_f16 v167, v107, v111, v167 op_sel:[1,1,0] op_sel_hi:[1,0,1]\n"
      "s_waitcnt lgkmcnt(9)\n"
      "s_nop 0\n"
      "v_mfma_f32_32x32x16_f16 v[180:195], v[72:75], v[164:167], 0\n"
      "v_pk_mul_f16 v240, v157, v156 op_sel_hi:[0,1]\n"
      "v_pk_mul_f16 v241, v158, v156 op_sel_hi:[0,1]\n"
      "v_pk_mul_f16 v242, v159, v156 op_sel_hi:[0,1]\n"
      "v_pk_mul_f16 v168, v112, v116 op_sel_hi:[0,1] neg_hi:[0,1]\n"
      "v_pk_mul_f16 v169, v113, v117 op_sel_hi:[0,1] neg_hi:[0,1]\n"
      "v_pk_mul_f16 v170, v114, v118 op_sel_hi:[0,1] neg_hi:[0,1]\n"
      "v_pk_mul_f16 v171, v115, v119 op_sel_hi:[0,1] neg_hi:[0,1]\n"
      "ds_write_addtid_b32 v156 offset:272\n"
      "v_pk_fma_f16 v240, v157, v156, v240 op_sel:[1,1,0] op_sel_hi:[1,0,1] neg_lo:[0,1,0]\n"
      "ds_write_addtid_b32 v157 offset:544\n"
      "v_pk_fma_f16 v168, v112, v116, v168 op_sel:[1,1,0] op_sel_hi:[1,0,1]\n"
      "v_pk_fma_f16 v169, v113, v117, v169 op_sel:[1,1,0] op_sel_hi:[1,0,1]\n"
      "v_pk_fma_f16 v170, v114, v118, v170 op_sel:[1,1,0] op_sel_hi:[1,0,1]\n"
      "v_pk_fma_f16 v171, v115, v119, v171 op_sel:[1,1,0] op_sel_hi:[1,0,1]\n"
      "ds_read_b128 v[104:107], v20 offset:96\n"
      "ds_read_b128 v[108:111], v178 offset:96\n"
      "s_waitcnt lgkmcnt(12)\n"
      "v_mfma_f32_32x32x16_f16 v[180:195], v[76:79], v[168:171], v[180:195]\n"
      "v_pk_fma_f16 v241, v158, v156, v241 op_sel:[1,1,0] op_sel_hi:[1,0,1] neg_lo:[0,1,0]\n"
      "ds_write_addtid_b32 v240 offset:816\n"
      "v_pk_fma_f16 v242, v159, v156, v242 op_sel:[1,1,0] op_sel_hi:[1,0,1] neg_lo:[0,1,0]\n"
      "s_waitcnt lgkmcnt(5)\n"
      "v_pk_mul_f16 v164, v120, v124 op_sel_hi:[0,1] neg_hi:[0,1]\n"
      "v_pk_mul_f16 v165, v121, v125 op_sel_hi:[0,1] neg_hi:[0,1]\n"
      "v_pk_mul_f16 v166, v122, v126 op_sel_hi:[0,1] neg_hi:[0,1]\n"
      "v_pk_mul_f16 v167, v123, v127 op_sel_hi:[0,1] neg_hi:[0,1]\n"
      "ds_write_addtid_b32 v241 offset:1360\n"
      "ds_write_addtid_b32 v242 offset:1904\n"
      "v_pk_mul_f16 v243, v160, v157 op_sel_hi:[0,1]\n"
      "v_pk_fma_f16 v164, v120, v124, v164 op_sel:[1,1,0] op_sel_hi:[1,0,1]\n"
      "v_pk_fma_f16 v165, v121, v125, v165 op_sel:[1,1,0] op_sel_hi:[1,0,1]\n"
      "v_pk_fma_f16 v166, v122, v126, v166 op_sel:[1,1,0] op_sel_hi:[1,0,1]\n"
      "v_pk_fma_f16 v167, v123, v127, v167 op_sel:[1,1,0] op_sel_hi:[1,0,1]\n"
      "ds_read_b128 v[112:115], v20 offset:128\n"
      "ds_read_b128 v[116:119], v178 offset:128\n"
      "v_mfma_f32_32x32x16_f16 v[180:195], v[80:83], v[164:167], v[180:195]\n"
      "v_pk_mul_f16 v244, v160, v158 op_sel_hi:[0,1]\n"
      "v_pk_mul_f16 v245, v160, v159 op_sel_hi:[0,1]\n"
      "ds_write_addtid_b32 v158 offset:1088\n"
      "s_waitcnt lgkmcnt(6)\n"
      "v_pk_mul_f16 v168, v104, v108 op_sel_hi:[0,1] neg_hi:[0,1]\n"
      "v_pk_mul_f16 v169, v105, v109 op_sel_hi:[0,1] neg_hi:[0,1]\n"
      "v_pk_mul_f16 v170, v106, v110 op_sel_hi:[0,1] neg_hi:[0,1]\n"
      "v_pk_mul_f16 v171, v107, v111 op_sel_hi:[0,1] neg_hi:[0,1]\n"
      "v_pk_fma_f16 v243, v160, v157, v243 op_sel:[1,1,0] op_sel_hi:[1,0,1] neg_lo:[0,1,0]\n"
      "ds_write_addtid_b32 v159 offset:1632\n"
      "v_pk_fma_f16 v244, v160, v158, v244 op_sel:[1,1,0] op_sel_hi:[1,0,1] neg_lo:[0,1,0]\n"
      "v_pk_fma_f16 v168, v104, v108, v168 op_sel:[1,1,0] op_sel_hi:[1,0,1]\n"
      "v_pk_fma_f16 v169, v105, v109, v169 op_sel:[1,1,0] op_sel_hi:[1,0,1]\n"
      "v_pk_fma_f16 v170, v106, v110, v170 op_sel:[1,1,0] op_sel_hi:[1,0,1]\n"
      "v_pk_fma_f16 v171, v107, v111, v171 op_sel:[1,1,0] op_sel_hi:[1,0,1]\n"
      "ds_read_b128 v[120:123], v20 offset:160\n"
      "ds_read_b128 v[124:127], v178 offset:160\n"
      "buffer_load_dwordx4 v[48:51], v41, s[20:23], s60 offen nt\n"
      "v_mfma_f32_32x32x16_f16 v[180:195], v[84:87], v[168:171], v[180:195]\n"
      "ds_write_addtid_b32 v243 offset:2992\n"
      "v_pk_fma_f16 v245, v160, v159, v245 op_sel:[1,1,0] op_sel_hi:[1,0,1] neg_lo:[0,1,0]\n"
      "ds_write_addtid_b32 v244 offset:3264\n"
      "s_waitcnt lgkmcnt(6)\n"
      "v_pk_mul_f16 v164, v112, v116 op_sel_hi:[0,1] neg_hi:[0,1]\n"
      "v_pk_mul_f16 v165, v113, v117 op_sel_hi:[0,1] neg_hi:[0,1]\n"
      "v_pk_mul_f16 v166, v114, v118 op_sel_hi:[0,1] neg_hi:[0,1]\n"
      "v_pk_mul_f16 v167, v115, v119 op_sel_hi:[0,1] neg_hi:[0,1]\n"
      "ds_write_addtid_b32 v245 offset:4080\n"
      "v_pk_mul_f16 v246, v161, v158 op_sel_hi:[0,1]\n"
      "v_pk_mul_f16 v247, v162, v158 op_sel_hi:[0,1]\n"
      "v_pk_fma_f16 v164, v112, v116, v164 op_sel:[1,1,0] op_sel_hi:[1,0,1]\n"
      "v_pk_fma_f16 v165, v113, v117, v165 op_sel:[1,1,0] op_sel_hi:[1,0,1]\n"
      "v_pk_fma_f16 v166, v114, v118, v166 op_sel:[1,1,0] op_sel_hi:[1,0,1]\n"
      "v_pk_fma_f16 v167, v115, v119, v167 op_sel:[1,1,0] op_sel_hi:[1,0,1]\n"
      "ds_read_b128 v[104:107], v20 offset:192\n"
      "ds_read_b128 v[108:111], v178 offset:192\n"
      "v_mfma_f32_32x32x16_f16 v[180:195], v[88:91], v[164:167], v[180:195]\n"
      "v_pk_mul_f16 v248, v163, v163 op_sel_hi:[0,1]\n"
      "ds_write_addtid_b32 v160 offset:2176\n"
      "v_pk_fma_f16 v246, v161, v158, v246 op_sel:[1,1,0] op_sel_hi:[1,0,1] neg_lo:[0,1,0]\n"
      "s_waitcnt lgkmcnt(6)\n"
      "v_pk_mul_f16 v168, v120, v124 op_sel_hi:[0,1] neg_hi:[0,1]\n"
      "v_pk_mul_f16 v169, v121, v125 op_sel_hi:[0,1] neg_hi:[0,1]\n"
      "v_pk_mul_f16 v170, v122, v126 op_sel_hi:[0,1] neg_hi:[0,1]\n"
      "v_pk_mul_f16 v171, v123, v127 op_sel_hi:[0,1] neg_hi:[0,1]\n"
      "ds_write_addtid_b32 v161 offset:5440\n"
      "v_pk_fma_f16 v247, v162, v158, v247 op_sel:[1,1,0] op_sel_hi:[1,0,1] neg_lo:[0,1,0]\n"
      "ds_write_addtid_b32 v246 offset:5984\n"
      "v_pk_fma_f16 v168, v120, v124, v168 op_sel:[1,1,0] op_sel_hi:[1,0,1]\n"
      "v_pk_fma_f16 v169, v121, v125, v169 op_sel:[1,1,0] op_sel_hi:[1,0,1]\n"
      "v_pk_fma_f16 v170, v122, v126, v170 op_sel:[1,1,0] op_sel_hi:[1,0,1]\n"
      "v_pk_fma_f16 v171, v123, v127, v171 op_sel:[1,1,0] op_sel_hi:[1,0,1]\n"
      "ds_read_b128 v[112:115], v20 offset:224\n"
      "ds_read_b128 v[116:119], v178 offset:224\n"
      "v_mfma_f32_32x32x16_f16 v[180:195], v[92:95], v[168:171], v[180:195]\n"
      "v_pk_fma_f16 v248, v163, v163, v248 op_sel:[1,1,0] op_sel_hi:[1,0,1] neg_lo:[0,1,0]\n"
      "ds_write_addtid_b32 v247 offset:6256\n"
      "ds_write_addtid_b32 v248 offset:2448\n"
      "s_waitcnt lgkmcnt(7)\n"
      "v_pk_mul_f16 v164, v104, v108 op_sel_hi:[0,1] neg_hi:[0,1]\n"
      "v_pk_mul_f16 v165, v105, v109 op_sel_hi:[0,1] neg_hi:[0,1]\n"
      "v_pk_mul_f16 v166, v106, v110 op_sel_hi:[0,1] neg_hi:[0,1]\n"
      "v_pk_mul_f16 v167, v107, v111 op_sel_hi:[0,1] neg_hi:[0,1]\n"
      "v_pk_mul_f16 v249, v163, v160 op_sel_hi:[0,1]\n"
      "v_pk_mul_f16 v250, v163, v161 op_sel_hi:[0,1]\n"
      "v_pk_mul_f16 v251, v163, v162 op_sel_hi:[0,1]\n"
      "v_pk_fma_f16 v164, v104, v108, v164 op_sel:[1,1,0] op_sel_hi:[1,0,1]\n"
      "v_pk_fma_f16 v165, v105, v109, v165 op_sel:[1,1,0] op_sel_hi:[1,0,1]\n"
      "v_pk_fma_f16 v166, v106, v110, v166 op_sel:[1,1,0] op_sel_hi:[1,0,1]\n"
      "v_pk_fma_f16 v167, v107, v111, v167 op_sel:[1,1,0] op_sel_hi:[1,0,1]\n"
      "ds_read_b128 v[120:123], v20 offset:3264\n"
      "ds_read_b128 v[124:127], v178 offset:3264\n"
      "v_mfma_f32_32x32x16_f16 v[180:195], v[96:99], v[164:167], v[180:195]\n"
      "ds_write_addtid_b32 v162 offset:5712\n"
      "v_pk_fma_f16 v249, v163, v160, v249 op_sel:[1,1,0] op_sel_hi:[1,0,1] neg_lo:[0,1,0]\n"
      "ds_write_addtid_b32 v163 offset:4352\n"
      "s_waitcnt lgkmcnt(6)\n"
      "v_pk_mul_f16 v168, v112, v116 op_sel_hi:[0,1] neg_hi:[0,1]\n"
      "v_pk_mul_f16 v169, v113, v117 op_sel_hi:[0,1] neg_hi:[0,1]\n"
      "v_pk_mul_f16 v170, v114, v118 op_sel_hi:[0,1] neg_hi:[0,1]\n"
      "v_pk_mul_f16 v171, v115, v119 op_sel_hi:[0,1] neg_hi:[0,1]\n"
      "v_pk_fma_f16 v250, v163, v161, v250 op_sel:[1,1,0] op_sel_hi:[1,0,1] neg_lo:[0,1,0]\n"
      "ds_write_addtid_b32 v249 offset:4624\n"
      "v_pk_fma_f16 v251, v163, v162, v251 op_sel:[1,1,0] op_sel_hi:[1,0,1] neg_lo:[0,1,0]\n"
      "v_pk_fma_f16 v168, v112, v116, v168 op_sel:[1,1,0] op_sel_hi:[1,0,1]\n"
      "v_pk_fma_f16 v169, v113, v117, v169 op_sel:[1,1,0] op_sel_hi:[1,0,1]\n"
      "v_pk_fma_f16 v170, v114, v118, v170 op_sel:[1,1,0] op_sel_hi:[1,0,1]\n"
      "v_pk_fma_f16 v171, v115, v119, v171 op_sel:[1,1,0] op_sel_hi:[1,0,1]\n"
      "ds_read_b128 v[104:107], v20 offset:3296\n"
      "ds_read_b128 v[108:111], v178 offset:3296\n"
      "v_mfma_f32_32x32x16_f16 v[180:195], v[100:103], v[168:171], v[180:195]\n"
      "ds_write_addtid_b32 v250 offset:4896\n"
      "ds_write_addtid_b32 v251 offset:5168\n"
      "s_waitcnt lgkmcnt(7)\n"
      "v_pk_mul_f16 v164, v120, v124 op_sel_hi:[0,1] neg_hi:[0,1]\n"
      "v_pk_mul_f16 v165, v121, v125 op_sel_hi:[0,1] neg_hi:[0,1]\n"
      "v_pk_mul_f16 v166, v122, v126 op_sel_hi:[0,1] neg_hi:[0,1]\n"
      "v_pk_mul_f16 v167, v123, v127 op_sel_hi:[0,1] neg_hi:[0,1]\n"
      "ds_read_b128 v[72:75], v21 offset:0\n"
      "ds_read_b128 v[76:79], v21 offset:32\n"
      "ds_read_b128 v[80:83], v21 offset:64\n"
      "ds_read_b128 v[84:87], v21 offset:96\n"
      "ds_read_b128 v[88:91], v21 offset:128\n"
      "ds_read_b128 v[92:95], v21 offset:160\n"
      "ds_read_b128 v[96:99], v21 offset:192\n"
      "ds_read_b128 v[100:103], v21 offset:224\n"
      "v_pk_fma_f16 v164, v120, v124, v164 op_sel:[1,1,0] op_sel_hi:[1,0,1]\n"
      "v_pk_fma_f16 v165, v121, v125, v165 op_sel:[1,1,0] op_sel_hi:[1,0,1]\n"
      "v_pk_fma_f16 v166, v122, v126, v166 op_sel:[1,1,0] op_sel_hi:[1,0,1]\n"
      "v_pk_fma_f16 v167, v123, v127, v167 op_sel:[1,1,0] op_sel_hi:[1,0,1]\n"
      "ds_read_b128 v[112:115], v20 offset:3328\n"
      "ds_read_b128 v[116:119], v178 offset:3328\n"
      "s_setprio 1\n"
      "s_waitcnt lgkmcnt(9)\n"
      "v_mfma_f32_32x32x16_f16 v[180:195], v[72:75], v[164:167], v[180:195]\n"
      "v_pk_mul_f16 v168, v104, v108 op_sel_hi:[0,1] neg_hi:[0,1]\n"
      "v_pk_mul_f16 v169, v105, v109 op_sel_hi:[0,1] neg_hi:[0,1]\n"
      "v_pk_mul_f16 v170, v106, v110 op_sel_hi:[0,1] neg_hi:[0,1]\n"
      "v_pk_mul_f16 v171, v107, v111 op_sel_hi:[0,1] neg_hi:[0,1]\n"
      "v_pk_fma_f16 v168, v104, v108, v168 op_sel:[1,1,0] op_sel_hi:[1,0,1]\n"
      "v_pk_fma_f16 v169, v105, v109, v169 op_sel:[1,1,0] op_sel_hi:[1,0,1]\n"
      "v_pk_fma_f16 v170, v106, v110, v170 op_sel:[1,1,0] op_sel_hi:[1,0,1]\n"
      "v_pk_fma_f16 v171, v107, v111, v171 op_sel:[1,1,0] op_sel_hi:[1,0,1]\n"
      "ds_read_b128 v[120:123], v20 offset:3360\n"
      "ds_read_b128 v[124:127], v178 offset:3360\n"
      "buffer_load_dwordx4 v[52:55], v41, s[20:23], s60 offen offset:2048 nt\n"
      "s_waitcnt lgkmcnt(10)\n"
      "v_mfma_f32_32x32x16_f16 v[180:195], v[76:79], v[168:171], v[180:195]\n"
      "s_waitcnt lgkmcnt(2)\n"
      "v_pk_mul_f16 v164, v112, v116 op_sel_hi:[0,1] neg_hi:[0,1]\n"
      "v_pk_mul_f16 v165, v113, v117 op_sel_hi:[0,1] neg_hi:[0,1]\n"
      "v_pk_mul_f16 v166, v114, v118 op_sel_hi:[0,1] neg_hi:[0,1]\n"
      "v_pk_mul_f16 v167, v115, v119 op_sel_hi:[0,1] neg_hi:[0,1]\n"
      "v_pk_fma_f16 v164, v112, v116, v164 op_sel:[1,1,0] op_sel_hi:[1,0,1]\n"
      "v_pk_fma_f16 v165, v113, v117, v165 op_sel:[1,1,0] op_sel_hi:[1,0,1]\n"
      "v_pk_fma_f16 v166, v114, v118, v166 op_sel:[1,1,0] op_sel_hi:[1,0,1]\n"
      "v_pk_fma_f16 v167, v115, v119, v167 op_sel:[1,1,0] op_sel_hi:[1,0,1]\n"
      "ds_read_b128 v[104:107], v20 offset:3392\n"
      "ds_read_b128 v[108:111], v178 offset:3392\n"
      "v_mfma_f32_32x32x16_f16 v[180:195], v[80:83], v[164:167], v[180:195]\n"
      "s_waitcnt lgkmcnt(2)\n"
      "v_pk_mul_f16 v168, v120, v124 op_sel_hi:[0,1] neg_hi:[0,1]\n"
      "v_pk_mul_f16 v169, v121, v125 op_sel_hi:[0,1] neg_hi:[0,1]\n"
      "v_pk_mul_f16 v170, v122, v126 op_sel_hi:[0,1] neg_hi:[0,1]\n"
      "v_pk_mul_f16 v171, v123, v127 op_sel_hi:[0,1] neg_hi:[0,1]\n"
      "v_pk_fma_f16 v168, v120, v124, v168 op_sel:[1,1,0] op_sel_hi:[1,0,1]\n"
      "v_pk_fma_f16 v169, v121, v125, v169 op_sel:[1,1,0] op_sel_hi:[1,0,1]\n"
      "v_pk_fma_f16 v170, v122, v126, v170 op_sel:[1,1,0] op_sel_hi:[1,0,1]\n"
      "v_pk_fma_f16 v171, v123, v127, v171 op_sel:[1,1,0] op_sel_hi:[1,0,1]\n"
      "ds_read_b128 v[112:115], v20 offset:3424\n"
      "ds_read_b128 v[116:119], v178 offset:3424\n"
      "v_mfma_f32_32x32x16_f16 v[180:195], v[84:87], v[168:171], v[180:195]\n"
      "s_waitcnt lgkmcnt(2)\n"
      "v_pk_mul_f16 v164, v104, v108 op_sel_hi:[0,1] neg_hi:[0,1]\n"
      "v_pk_mul_f16 v165, v105, v109 op_sel_hi:[0,1] neg_hi:[0,1]\n"
      "v_pk_mul_f16 v166, v106, v110 op_sel_hi:[0,1] neg_hi:[0,1]\n"
      "v_pk_mul_f16 v167, v107, v111 op_sel_hi:[0,1] neg_hi:[0,1]\n"
      "v_pk_fma_f16 v164, v104, v108, v164 op_sel:[1,1,0] op_sel_hi:[1,0,1]\n"
      "v_pk_fma_f16 v165, v105, v109, v165 op_sel:[1,1,0] op_sel_hi:[1,0,1]\n"
      "v_pk_fma_f16 v166, v106, v110, v166 op_sel:[1,1,0] op_sel_hi:[1,0,1]\n"
      "v_pk_fma_f16 v167, v107, v111, v167 op_sel:[1,1,0] op_sel_hi:[1,0,1]\n"
      "ds_read_b128 v[120:123], v20 offset:3456\n"
      "ds_read_b128 v[124:127], v178 offset:3456\n"
      "v_mfma_f32_32x32x16_f16 v[180:195], v[88:91], v[164:167], v[180:195]\n"
      "s_waitcnt lgkmcnt(2)\n"
      "v_pk_mul_f16 v168, v112, v116 op_sel_hi:[0,1] neg_hi:[0,1]\n"
      "v_pk_mul_f16 v169, v113, v117 op_sel_hi:[0,1] neg_hi:[0,1]\n"
      "v_pk_mul_f16 v170, v114, v118 op_sel_hi:[0,1] neg_hi:[0,1]\n"
      "v_pk_mul_f16 v171, v115, v119 op_sel_hi:[0,1] neg_hi:[0,1]\n"
      "v_pk_fma_f16 v168, v112, v116, v168 op_sel:[1,1,0] op_sel_hi:[1,0,1]\n"
      "v_pk_fma_f16 v169, v113, v117, v169 op_sel:[1,1,0] op_sel_hi:[1,0,1]\n"
      "v_pk_fma_f16 v170, v114, v118, v170 op_sel:[1,1,0] op_sel_hi:[1,0,1]\n"
      "v_pk_fma_f16 v171, v115, v119, v171 op_sel:[1,1,0] op_sel_hi:[1,0,1]\n"
      "ds_read_b128 v[104:107], v20 offset:3488\n"
      "ds_read_b128 v[108:111], v178 offset:3488\n"
      "v_mov_b32 v140, 0\n"
      "v_mov_b32 v141, 0\n"
      "v_mov_b32 v142, 0\n"
      "v_mov_b32 v143, 0\n"
      "ds_write_b128 v27, v[140:143] \n"
      "ds_write_b32 v28, v140 \n"
      "v_mfma_f32_32x32x16_f16 v[180:195], v[92:95], v[168:171], v[180:195]\n"
      "s_waitcnt lgkmcnt(2)\n"
      "v_pk_mul_f16 v164, v120, v124 op_sel_hi:[0,1] neg_hi:[0,1]\n"
      "v_pk_mul_f16 v165, v121, v125 op_sel_hi:[0,1] neg_hi:[0,1]\n"
      "v_pk_mul_f16 v166, v122, v126 op_sel_hi:[0,1] neg_hi:[0,1]\n"
      "v_pk_mul_f16 v167, v123, v127 op_sel_hi:[0,1] neg_hi:[0,1]\n"
      "v_pk_fma_f16 v164, v120, v124, v164 op_sel:[1,1,0] op_sel_hi:[1,0,1]\n"
      "v_pk_fma_f16 v165, v121, v125, v165 op_sel:[1,1,0] op_sel_hi:[1,0,1]\n"
      "v_pk_fma_f16 v166, v122, v126, v166 op_sel:[1,1,0] op_sel_hi:[1,0,1]\n"
      "v_pk_fma_f16 v167, v123, v127, v167 op_sel:[1,1,0] op_sel_hi:[1,0,1]\n"
      "s_nop 1\n"
      "v_mfma_f32_32x32x16_f16 v[180:195], v[96:99], v[164:167], v[180:195]\n"
      "s_waitcnt lgkmcnt(0)\n"
      "v_pk_mul_f16 v168, v104, v108 op_sel_hi:[0,1] neg_hi:[0,1]\n"
      "v_pk_mul_f16 v169, v105, v109 op_sel_hi:[0,1] neg_hi:[0,1]\n"
      "v_pk_mul_f16 v170, v106, v110 op_sel_hi:[0,1] neg_hi:[0,1]\n"
      "v_pk_mul_f16 v171, v107, v111 op_sel_hi:[0,1] neg_hi:[0,1]\n"
      "v_pk_fma_f16 v168, v104, v108, v168 op_sel:[1,1,0] op_sel_hi:[1,0,1]\n"
      "v_pk_fma_f16 v169, v105, v109, v169 op_sel:[1,1,0] op_sel_hi:[1,0,1]\n"
      "v_pk_fma_f16 v170, v106, v110, v170 op_sel:[1,1,0] op_sel_hi:[1,0,1]\n"
      "v_pk_fma_f16 v171, v107, v111, v171 op_sel:[1,1,0] op_sel_hi:[1,0,1]\n"
      "s_nop 1\n"
      "v_mfma_f32_32x32x16_f16 v[180:195], v[100:103], v[168:171], v[180:195]\n"
      "s_cmp_lt_u32 s33, 4\n"
      "s_cbranch_scc1 .Lct2%=\n"
      "buffer_load_dword v11, v15, s[20:23], s60 offen\n"
      ".Lct2%=:\n"
      "s_nop 10\n"
      "v_add_f32 v180, v180, v31\n"
      "buffer_load_dwordx4 v[64:67], v42, s[16:19], s62 offen sc1\n"
      "v_cvt_pk_f16_f32 v222, v187, v186\n"
      "v_cvt_pk_f16_f32 v223, v185, v184\n"
      "v_cvt_pk_f16_f32 v225, v186, v185\n"
      "v_cvt_pk_f16_f32 v226, v188, v189\n"
      "ds_write_b64 v22, v[222:223] offset:2110\n"
      "v_cvt_pk_f16_f32 v228, v192, v192\n"
      "ds_write_b16 v22, v222 \n"
      "v_cvt_pk_f16_f32 v220, v183, v182\n"
      "ds_write_b32 v22, v225 offset:2\n"
      "v_cvt_pk_f16_f32 v221, v181, v180\n"
      "ds_write_b16_d16_hi v22, v223 offset:6\n"
      "v_cvt_pk_f16_f32 v224, v182, v181\n"
      "v_readlane_b32 s64, v226, 0\n"
      "v_readlane_b32 s65, v226, 32\n"
      "v_readlane_b32 s66, v228, 0\n"
      "ds_write_b64 v22, v[220:221] offset:510\n"
      "v_cvt_pk_f16_f32 v227, v190, v191\n"
      "ds_write_b16 v22, v220 offset:2620\n"
      "v_mov_b32 v229, s65\n"
      "ds_write_b32 v22, v224 offset:2622\n"
      "v_writelane_b32 v229, s64, 8\n"
      "v_writelane_b32 v229, s64, 9\n"
      "ds_write_b16_d16_hi v22, v221 offset:2626\n"
      "v_writelane_b32 v229, s66, 10\n"
      "v_writelane_b32 v229, s66, 11\n"
      "s_pack_ll_b32_b16 s64, s64, s64\n"
      "ds_write_b16 v26, v229 \n"
      "v_add_u32 v230, 0x240, v29\n"
      "s_setprio 0\n"
      "v_and_b32 v44, s64, v44\n"
      "v_and_b32 v45, s64, v45\n"
      "v_and_b32 v46, s64, v46\n"
      "v_and_b32 v47, s64, v47\n"
      "ds_write_b16 v23, v226 \n"
      "ds_write_b16 v23, v226 offset:2110\n"
      "ds_write_b16_d16_hi v24, v226 \n"
      "ds_write_b16_d16_hi v24, v226 offset:2110\n"
      "ds_write_b16 v25, v227 offset:32\n"
      "ds_write_b16 v25, v227 offset:2142\n"
      "ds_write_b16_d16_hi v25, v227 \n"
      "ds_write_b16_d16_hi v25, v227 offset:2110\n"
      "ds_read2_b32 v[108:109], v230 offset0:0 offset1:1\n"
      "ds_read2_b32 v[110:111], v230 offset0:2 offset1:3\n"
      "ds_read2_b32 v[76:77], v29 offset0:16 offset1:17\n"
      "ds_read2_b32 v[78:79], v29 offset0:18 offset1:19\n"
      "s_movk_i32 s59, 0x7fff\n"
      "s_waitcnt vmcnt(0)\n"
      "s_nop 0\n"
      ".Lcsgk%=:\n"
      "v_cmp_ne_u32 s[64:65], s15, v65\n"
      "v_cmp_ne_u32 vcc, s15, v67\n"
      "s_nop 0\n"
      "s_or_b64 vcc, vcc, s[64:65]\n"
      "s_cbranch_scc1 .Lcsgsk%=\n"
      ".Lcsgok%=:\n"
      "v_and_b32 v196, 0xffff0000, v64\n"
      "v_lshlrev_b32 v198, 16, v64\n"
      "v_rcp_f32 v196, v196\n"
      "v_and_b32 v197, 0xffff0000, v66\n"
      "v_lshlrev_b32 v199, 16, v66\n"
      "v_rcp_f32 v197, v197\n"
      "v_mul_f32 v198, -v198, v196\n"
      "v_mul_f32 v199, -v199, v197\n"
      "ds_read2_b32 v[112:113], v230 offset0:16 offset1:17\n"
      "ds_read2_b32 v[114:115], v230 offset0:18 offset1:19\n"
      "v_lshlrev_b32 v204, 16, v48\n"
      "v_lshlrev_b32 v205, 16, v52\n"
      "v_and_b32 v206, 0xffff0000, v48\n"
      "v_and_b32 v207, 0xffff0000, v52\n"
      "v_pk_fma_f32 v[204:205], v[204:205], v[196:197], v[198:199]\n"
      "v_pk_fma_f32 v[206:207], v[206:207], v[196:197], v[198:199]\n"
      "v_cvt_pk_f16_f32 v204, v204, v205\n"
      "v_cvt_pk_f16_f32 v206, v206, v207\n"
      "ds_read2_b32 v[80:81], v29 offset0:32 offset1:33\n"
      "ds_read2_b32 v[82:83], v29 offset0:34 offset1:35\n"
      "ds_write2st64_b32 v32, v204, v206 offset0:0 offset1:19\n"
      "v_lshlrev_b32 v208, 16, v49\n"
      "v_lshlrev_b32 v209, 16, v53\n"
      "v_and_b32 v210, 0xffff0000, v49\n"
      "v_and_b32 v211, 0xffff0000, v53\n"
      "v_pk_fma_f32 v[208:209], v[208:209], v[196:197], v[198:199]\n"
      "v_pk_fma_f32 v[210:211], v[210:211], v[196:197], v[198:199]\n"
      "v_cvt_pk_f16_f32 v208, v208, v209\n"
      "ds_read2_b32 v[116:117], v230 offset0:32 offset1:33\n"
      "ds_read2_b32 v[118:119], v230 offset0:34 offset1:35\n"
      "v_cvt_pk_f16_f32 v210, v210, v211\n"
      "ds_write2st64_b32 v32, v208, v210 offset0:38 offset1:57\n"
      "v_lshlrev_b32 v204, 16, v50\n"
      "v_lshlrev_b32 v205, 16, v54\n"
      "v_and_b32 v206, 0xffff0000, v50\n"
      "v_and_b32 v207, 0xffff0000, v54\n"
      "v_pk_fma_f32 v[204:205], v[204:205], v[196:197], v[198:199]\n"
      "v_pk_fma_f32 v[206:207], v[206:207], v[196:197], v[198:199]\n"
      "ds_read2_b32 v[84:85], v29 offset0:48 offset1:49\n"
      "ds_read2_b32 v[86:87], v29 offset0:50 offset1:51\n"
      "v_cvt_pk_f16_f32 v204, v204, v205\n"
      "v_cvt_pk_f16_f32 v206, v206, v207\n"
      "ds_write2st64_b32 v32, v204, v206 offset0:76 offset1:95\n"
      "v_lshlrev_b32 v208, 16, v51\n"
      "v_lshlrev_b32 v209, 16, v55\n"
      "v_and_b32 v210, 0xffff0000, v51\n"
      "v_and_b32 v211, 0xffff0000, v55\n"
      "v_pk_fma_f32 v[208:209], v[208:209], v[196:197], v[198:199]\n"
      "ds_read2_b32 v[120:121], v230 offset0:48 offset1:49\n"
      "ds_read2_b32 v[122:123], v230 offset0:50 offset1:51\n"
      "v_pk_fma_f32 v[210:211], v[210:211], v[196:197], v[198:199]\n"
      "v_cvt_pk_f16_f32 v208, v208, v209\n"
      "v_cvt_pk_f16_f32 v210, v210, v211\n"
      "ds_write2st64_b32 v32, v208, v210 offset0:114 offset1:133\n"
      "s_mov_b32 s54, s41\n"
      "s_add_u32 s55, s41, 0x100000\n"
      "s_add_u32 s56, s41, 0x200000\n"
      "s_add_u32 s57, s41, 0x300000\n"
      "ds_read2_b32 v[88:89], v29 offset0:64 offset1:65\n"
      "ds_read2_b32 v[90:91], v29 offset0:66 offset1:67\n"
      "s_mov_b32 s58, 0x40000000\n"
      "v_lshrrev_b32 v14, 9, v8\n"
      "v_add_u32 v14, 0x27600, v14\n"
      "s_lshr_b32 s13, s33, 1\n"
      "ds_read2_b32 v[124:125], v230 offset0:64 offset1:65\n"
      "ds_read2_b32 v[126:127], v230 offset0:66 offset1:67\n"
      "ds_read2_b32 v[92:93], v29 offset0:80 offset1:81\n"
      "ds_read2_b32 v[94:95], v29 offset0:82 offset1:83\n"
      "ds_read2_b32 v[128:129], v230 offset0:80 offset1:81\n"
      "ds_read2_b32 v[130:131], v230 offset0:82 offset1:83\n"
      "ds_read2_b32 v[96:97], v29 offset0:96 offset1:97\n"
      "ds_read2_b32 v[98:99], v29 offset0:98 offset1:99\n"
      "ds_read2_b32 v[132:133], v230 offset0:96 offset1:97\n"
      "ds_read2_b32 v[134:135], v230 offset0:98 offset1:99\n"
      "s_waitcnt lgkmcnt(12)\n"
      ".Lccv%=:\n"
      "s_barrier\n"
      "s_setprio 3\n"
      "ds_read_b128 v[236:239], v34 offset:0\n"
      "ds_read_b128 v[240:243], v34 offset:16\n"
      "ds_read_b128 v[216:219], v33 offset:64\n"
      "ds_read_b128 v[220:223], v33 offset:128\n"
      "ds_read_b128 v[224:227], v33 offset:192\n"
      "ds_read_b128 v[212:215], v33 offset:256\n"
      "v_permlane32_swap_b32 v48, v52\n"
      "v_permlane32_swap_b32 v49, v53\n"
      "v_permlane32_swap_b32 v50, v54\n"
      "v_permlane32_swap_b32 v51, v55\n"
      "s_cmp_lg_u32 s33, 0\n"
      "s_cbranch_scc1 .Lca%=\n"
      "v_mov_b32 v16, s15\n"
      "v_mov_b32 v17, s15\n"
      "buffer_store_dwordx2 v[16:17], off, s[16:19], s42 sc1\n"
      ".Lca%=:\n"
      "s_waitcnt lgkmcnt(2)\n"
      "v_mfma_f32_16x16x32_f16 v[180:183], v[108:111], v[216:219], 0\n"
      "ds_read2_b32 v[100:101], v29 offset0:112 offset1:113\n"
      "v_mfma_f32_16x16x32_f16 v[184:187], v[76:79], v[216:219], 0\n"
      "ds_read2_b32 v[102:103], v29 offset0:114 offset1:115\n"
      "ds_read_b128 v[216:219], v33 offset:320\n"
      "v_mfma_f32_16x16x32_f16 v[180:183], v[112:115], v[220:223], v[180:183]\n"
      "ds_read2_b32 v[136:137], v230 offset0:112 offset1:113\n"
      "v_alignbit_b32 v245, v238, v237, 16\n"
      "v_alignbit_b32 v246, v239, v238, 16\n"
      "v_mfma_f32_16x16x32_f16 v[184:187], v[80:83], v[220:223], v[184:187]\n"
      "ds_read2_b32 v[138:139], v230 offset0:114 offset1:115\n"
      "ds_read_b128 v[220:223], v33 offset:384\n"
      "v_alignbit_b32 v247, v240, v239, 16\n"
      "v_alignbit_b32 v248, v241, v240, 16\n"
      "s_waitcnt lgkmcnt(5)\n"
      "v_mfma_f32_16x16x32_f16 v[180:183], v[116:119], v[224:227], v[180:183]\n"
      "ds_read2_b32 v[104:105], v29 offset0:128 offset1:129\n"
      "v_alignbit_b32 v249, v242, v241, 16\n"
      "v_pk_add_f16 v16, v245, v246\n"
      "v_mfma_f32_16x16x32_f16 v[184:187], v[84:87], v[224:227], v[184:187]\n"
      "ds_read2_b32 v[106:107], v29 offset0:130 offset1:131\n"
      "ds_read_b128 v[224:227], v33 offset:448\n"
      "v_pk_add_f16 v17, v246, v247\n"
      "v_pk_add_f16 v18, v247, v248\n"
      "v_mfma_f32_16x16x32_f16 v[180:183], v[120:123], v[212:215], v[180:183]\n"
      "ds_read2_b32 v[72:73], v29 offset0:0 offset1:1\n"
      "v_pk_add_f16 v19, v248, v249\n"
      "v_pk_add_f16 v20, v16, v238\n"
      "v_mfma_f32_16x16x32_f16 v[184:187], v[88:91], v[212:215], v[184:187]\n"
      "ds_read2_b32 v[74:75], v29 offset0:2 offset1:3\n"
      "ds_read_b128 v[212:215], v33 offset:512\n"
      "v_pk_add_f16 v21, v17, v239\n"
      "v_pk_add_f16 v22, v18, v240\n"
      "s_waitcnt lgkmcnt(4)\n"
      "v_mfma_f32_16x16x32_f16 v[180:183], v[124:127], v[216:219], v[180:183]\n"
      "v_lshlrev_b32 v29, 2, v43\n"
      "v_add_u32 v29, 0x27600, v29\n"
      "ds_read_b32 v255, v29 \n"
      "v_mfma_f32_16x16x32_f16 v[184:187], v[92:95], v[216:219], v[184:187]\n"
      "v_pk_add_f16 v23, v19, v241\n"
      "ds_write_b128 v37, v[20:23] offset:0\n"
      "v_pk_add_f16 v16, v237, v239\n"
      "v_mfma_f32_16x16x32_f16 v[180:183], v[128:131], v[220:223], v[180:183]\n"
      "v_pk_add_f16 v17, v238, v240\n"
      "v_pk_add_f16 v18, v239, v241\n"
      "v_mfma_f32_16x16x32_f16 v[184:187], v[96:99], v[220:223], v[184:187]\n"
      "ds_read_b128 v[216:219], v33 offset:576\n"
      "v_pk_add_f16 v19, v240, v242\n"
      "v_pk_add_f16 v20, v20, v16\n"
      "s_waitcnt lgkmcnt(3)\n"
      "v_mfma_f32_16x16x32_f16 v[180:183], v[132:135], v[224:227], v[180:183]\n"
      "v_pk_add_f16 v21, v21, v17\n"
      "v_pk_add_f16 v22, v22, v18\n"
      "v_mfma_f32_16x16x32_f16 v[184:187], v[100:103], v[224:227], v[184:187]\n"
      "ds_read_b128 v[220:223], v33 offset:640\n"
      "v_pk_add_f16 v23, v23, v19\n"
      "ds_write_b128 v37, v[20:23] offset:3104\n"
      "v_alignbit_b32 v244, v237, v236, 16\n"
      "v_mfma_f32_16x16x32_f16 v[180:183], v[136:139], v[212:215], v[180:183]\n"
      "ds_read2_b32 v[140:141], v30 offset0:16 offset1:17\n"
      "v_alignbit_b32 v250, v243, v242, 16\n"
      "v_pk_add_f16 v16, v244, v247\n"
      "v_mfma_f32_16x16x32_f16 v[184:187], v[104:107], v[212:215], v[184:187]\n"
      "ds_read_b128 v[224:227], v33 offset:704\n"
      "v_pk_add_f16 v17, v245, v248\n"
      "v_pk_add_f16 v18, v246, v249\n"
      "v_mfma_f32_16x16x32_f16 v[188:191], v[72:75], v[212:215], 0\n"
      "ds_read2_b32 v[142:143], v30 offset0:64 offset1:65\n"
      "v_pk_add_f16 v19, v247, v250\n"
      "v_pk_add_f16 v20, v20, v16\n"
      "v_mfma_f32_16x16x32_f16 v[196:199], v[44:47], v[212:215], 0\n"
      "ds_read_b128 v[212:215], v33 offset:768\n"
      "v_pk_add_f16 v21, v21, v17\n"
      "v_pk_add_f16 v22, v22, v18\n"
      "s_waitcnt lgkmcnt(3)\n"
      "v_mfma_f32_16x16x32_f16 v[184:187], v[108:111], v[216:219], v[184:187]\n"
      "ds_read2_b32 v[144:145], v30 offset0:8 offset1:9\n"
      "v_cvt_pk_bf16_f32 v24, v180, v182\n"
      "v_cvt_pk_bf16_f32 v25, v181, v183\n"
      "ds_write2st64_b32 v39, v24, v25 offset0:0 offset1:8\n"
      "v_mfma_f32_16x16x32_f16 v[188:191], v[76:79], v[216:219], v[188:191]\n"
      "ds_read_b128 v[216:219], v33 offset:832\n"
      "v_pk_add_f16 v23, v23, v19\n"
      "ds_write_b128 v38, v[20:23] offset:0\n"
      "v_pk_add_f16 v16, v236, v240\n"
      "v_mfma_f32_16x16x32_f16 v[184:187], v[112:115], v[220:223], v[184:187]\n"
      "ds_read2_b32 v[146:147], v30 offset0:56 offset1:57\n"
      "buffer_load_dwordx4 v[56:59], v41, s[20:23], s61 offen nt\n"
      "buffer_load_dwordx4 v[68:71], v42, s[16:19], s63 offen sc1\n"
      "v_pk_add_f16 v17, v237, v241\n"
      "v_pk_add_f16 v18, v238, v242\n"
      "v_mfma_f32_16x16x32_f16 v[188:191], v[80:83], v[220:223], v[188:191]\n"
      "ds_read_b128 v[220:223], v33 offset:896\n"
      "v_pk_add_f16 v19, v239, v243\n"
      "v_pk_add_f16 v20, v20, v16\n"
      "s_waitcnt lgkmcnt(3)\n"
      "v_mfma_f32_16x16x32_f16 v[184:187], v[116:119], v[224:227], v[184:187]\n"
      "ds_read2_b32 v[148:149], v30 offset0:0 offset1:1\n"
      "v_pk_add_f16 v21, v21, v17\n"
      "v_pk_add_f16 v22, v22, v18\n"
      "ds_read_b128 v[236:239], v34 offset:1024\n"
      "ds_read_b128 v[240:243], v34 offset:1040\n"
      "s_setprio 2\n"
      "v_mfma_f32_16x16x32_f16 v[188:191], v[84:87], v[224:227], v[188:191]\n"
      "ds_read_b128 v[224:227], v33 offset:960\n"
      "v_pk_add_f16 v23, v23, v19\n"
      "ds_write_b128 v38, v[20:23] offset:2080\n"
      "v_mfma_f32_16x16x32_f16 v[184:187], v[120:123], v[212:215], v[184:187]\n"
      "ds_read2_b32 v[150:151], v30 offset0:48 offset1:49\n"
      "v_mfma_f32_16x16x32_f16 v[188:191], v[88:91], v[212:215], v[188:191]\n"
      "ds_read_b128 v[212:215], v33 offset:1024\n"
      "v_mfma_f32_16x16x32_f16 v[184:187], v[124:127], v[216:219], v[184:187]\n"
      "ds_read2_b32 v[152:153], v30 offset0:40 offset1:41\n"
      "s_waitcnt lgkmcnt(6)\n"
      "v_alignbit_b32 v245, v238, v237, 16\n"
      "v_alignbit_b32 v246, v239, v238, 16\n"
      "v_mfma_f32_16x16x32_f16 v[188:191], v[92:95], v[216:219], v[188:191]\n"
      "s_waitcnt lgkmcnt(4)\n"
      "v_alignbit_b32 v247, v240, v239, 16\n"
      "v_alignbit_b32 v248, v241, v240, 16\n"
      "v_mfma_f32_16x16x32_f16 v[184:187], v[128:131], v[220:223], v[184:187]\n"
      "ds_read2_b32 v[154:155], v30 offset0:88 offset1:89\n"
      "v_alignbit_b32 v249, v242, v241, 16\n"
      "v_pk_add_f16 v16, v245, v246\n"
      "v_mfma_f32_16x16x32_f16 v[188:191], v[96:99], v[220:223], v[188:191]\n"
      "ds_read_b64_tr_b16 v[228:229], v35 offset:32\n"
      "ds_read_b64_tr_b16 v[230:231], v35 offset:16\n"
      "v_pk_add_f16 v17, v246, v247\n"
      "v_pk_add_f16 v18, v247, v248\n"
      "v_mfma_f32_16x16x32_f16 v[184:187], v[132:135], v[224:227], v[184:187]\n"
      "ds_read2_b32 v[156:157], v30 offset0:32 offset1:33\n"
      "v_pk_add_f16 v19, v248, v249\n"
      "v_pk_add_f16 v20, v16, v238\n"
      "v_mfma_f32_16x16x32_f16 v[188:191], v[100:103], v[224:227], v[188:191]\n"
      "v_pk_add_f16 v21, v17, v239\n"
      "v_pk_add_f16 v22, v18, v240\n"
      "s_waitcnt lgkmcnt(3)\n"
      "v_mfma_f32_16x16x32_f16 v[184:187], v[136:139], v[212:215], v[184:187]\n"
      "ds_read2_b32 v[158:159], v30 offset0:80 offset1:81\n"
      "ds_read_b128 v[216:219], v33 offset:1088\n"
      "buffer_load_dwordx4 v[60:63], v41, s[20:23], s61 offen offset:2048 nt\n"
      "v_pk_add_f16 v23, v19, v241\n"
      "ds_write_b128 v37, v[20:23] offset:1024\n"
      "v_pk_add_f16 v16, v237, v239\n"
      "v_mfma_f32_16x16x32_f16 v[188:191], v[104:107], v[212:215], v[188:191]\n"
      "v_pk_add_f16 v17, v238, v240\n"
      "v_pk_add_f16 v18, v239, v241\n"
      "v_mfma_f32_16x16x32_f16 v[192:195], v[72:75], v[212:215], 0\n"
      "ds_read2_b32 v[160:161], v30 offset0:24 offset1:25\n"
      "ds_read_b128 v[220:223], v33 offset:1152\n"
      "v_pk_add_f16 v19, v240, v242\n"
      "v_pk_add_f16 v20, v20, v16\n"
      "v_mfma_f32_16x16x32_f16 v[200:203], v[44:47], v[212:215], 0\n"
      "v_pk_add_f16 v21, v21, v17\n"
      "v_pk_add_f16 v22, v22, v18\n"
      "s_waitcnt lgkmcnt(2)\n"
      "v_mfma_f32_16x16x32_f16 v[188:191], v[228:231], v[140:143], v[188:191]\n"
      "ds_read2_b32 v[162:163], v30 offset0:72 offset1:73\n"
      "ds_read_b128 v[224:227], v33 offset:1216\n"
      "v_cvt_pk_bf16_f32 v24, v184, v186\n"
      "v_cvt_pk_bf16_f32 v25, v185, v187\n"
      "ds_write2st64_b32 v39, v24, v25 offset0:1 offset1:9\n"
      "v_mfma_f32_16x16x32_f16 v[192:195], v[228:231], v[144:147], v[192:195]\n"
      "v_pk_add_f16 v23, v23, v19\n"
      "ds_write_b128 v37, v[20:23] offset:4128\n"
      "v_alignbit_b32 v244, v237, v236, 16\n"
      "v_mfma_f32_16x16x32_f16 v[196:199], v[228:231], v[148:151], v[196:199]\n"
      "ds_read2_b32 v[164:165], v30 offset0:128 offset1:129\n"
      "ds_read_b64_tr_b16 v[232:233], v35 offset:3128\n"
      "ds_read_b64_tr_b16 v[234:235], v35 offset:3112\n"
      "v_alignbit_b32 v250, v243, v242, 16\n"
      "v_pk_add_f16 v16, v244, v247\n"
      "v_mfma_f32_16x16x32_f16 v[188:191], v[108:111], v[216:219], v[188:191]\n"
      "v_pk_add_f16 v17, v245, v248\n"
      "v_pk_add_f16 v18, v246, v249\n"
      "v_mfma_f32_16x16x32_f16 v[192:195], v[76:79], v[216:219], v[192:195]\n"
      "v_pk_add_f16 v19, v247, v250\n"
      "v_pk_add_f16 v20, v20, v16\n"
      "s_waitcnt lgkmcnt(3)\n"
      "v_mfma_f32_16x16x32_f16 v[188:191], v[112:115], v[220:223], v[188:191]\n"
      "ds_read_b128 v[212:215], v33 offset:1280\n"
      "v_pk_add_f16 v21, v21, v17\n"
      "v_pk_add_f16 v22, v22, v18\n"
      "v_mfma_f32_16x16x32_f16 v[192:195], v[80:83], v[220:223], v[192:195]\n"
      "ds_read2_b32 v[166:167], v30 offset0:168 offset1:169\n"
      "v_pk_add_f16 v23, v23, v19\n"
      "ds_write_b128 v38, v[20:23] offset:1024\n"
      "v_pk_add_f16 v16, v236, v240\n"
      "v_mfma_f32_16x16x32_f16 v[188:191], v[116:119], v[224:227], v[188:191]\n"
      "ds_read_b128 v[216:219], v33 offset:1344\n"
      "v_pk_add_f16 v17, v237, v241\n"
      "v_pk_add_f16 v18, v238, v242\n"
      "v_mfma_f32_16x16x32_f16 v[192:195], v[84:87], v[224:227], v[192:195]\n"
      "v_pk_add_f16 v19, v239, v243\n"
      "v_pk_add_f16 v20, v20, v16\n"
      "s_setprio 1\n"
      "s_waitcnt lgkmcnt(3)\n"
      "v_mfma_f32_16x16x32_f16 v[188:191], v[232:235], v[152:155], v[188:191]\n"
      "ds_read_b128 v[220:223], v33 offset:1408\n"
      "v_pk_add_f16 v21, v21, v17\n"
      "v_pk_add_f16 v22, v22, v18\n"
      "v_mfma_f32_16x16x32_f16 v[192:195], v[232:235], v[156:159], v[192:195]\n"
      "ds_read2_b32 v[168:169], v30 offset0:120 offset1:121\n"
      "v_pk_add_f16 v23, v23, v19\n"
      "ds_write_b128 v38, v[20:23] offset:3104\n"
      "v_mfma_f32_16x16x32_f16 v[196:199], v[232:235], v[160:163], v[196:199]\n"
      "ds_read_b128 v[224:227], v33 offset:1472\n"
      "v_mfma_f32_16x16x32_f16 v[188:191], v[120:123], v[212:215], v[188:191]\n"
      "v_mfma_f32_16x16x32_f16 v[192:195], v[88:91], v[212:215], v[192:195]\n"
      "ds_read_b128 v[212:215], v33 offset:1536\n"
      "s_waitcnt lgkmcnt(5)\n"
      "v_mfma_f32_16x16x32_f16 v[188:191], v[124:127], v[216:219], v[188:191]\n"
      "ds_read2_b32 v[170:171], v30 offset0:160 offset1:161\n"
      "v_mfma_f32_16x16x32_f16 v[192:195], v[92:95], v[216:219], v[192:195]\n"
      "s_waitcnt lgkmcnt(5)\n"
      "v_mfma_f32_16x16x32_f16 v[188:191], v[128:131], v[220:223], v[188:191]\n"
      "v_mfma_f32_16x16x32_f16 v[192:195], v[96:99], v[220:223], v[192:195]\n"
      "ds_read_b64_tr_b16 v[228:229], v35 offset:544\n"
      "ds_read_b64_tr_b16 v[230:231], v35 offset:528\n"
      "s_waitcnt lgkmcnt(4)\n"
      "v_mfma_f32_16x16x32_f16 v[188:191], v[132:135], v[224:227], v[188:191]\n"
      "ds_read2_b32 v[172:173], v30 offset0:112 offset1:113\n"
      "v_mfma_f32_16x16x32_f16 v[192:195], v[100:103], v[224:227], v[192:195]\n"
      "s_waitcnt lgkmcnt(4)\n"
      "v_mfma_f32_16x16x32_f16 v[188:191], v[136:139], v[212:215], v[188:191]\n"
      "ds_read_b128 v[216:219], v33 offset:1600\n"
      "v_readlane_b32 s72, v255, 0\n"
      "s_movk_i32 s59, 0x7fff\n"
      "s_waitcnt vmcnt(1)\n"
      "s_nop 0\n"
      ".Lcsgh%=:\n"
      "v_cmp_ne_u32 s[64:65], s15, v69\n"
      "v_cmp_ne_u32 vcc, s15, v71\n"
      "s_nop 0\n"
      "s_or_b64 vcc, vcc, s[64:65]\n"
      "s_cbranch_scc1 .Lcsgsh%=\n"
      ".Lcsgoh%=:\n"
      "v_mov_b32 v42, v8\n"
      "v_mfma_f32_16x16x32_f16 v[192:195], v[104:107], v[212:215], v[192:195]\n"
      "v_readlane_b32 s73, v255, 1\n"
      "v_and_b32 v26, 0xffff0000, v68\n"
      "v_mfma_f32_16x16x32_f16 v[196:199], v[72:75], v[212:215], v[196:199]\n"
      "ds_read2_b32 v[174:175], v30 offset0:152 offset1:153\n"
      "ds_read_b128 v[220:223], v33 offset:1664\n"
      "v_readlane_b32 s74, v255, 2\n"
      "v_lshlrev_b32 v28, 16, v68\n"
      "v_mfma_f32_16x16x32_f16 v[204:207], v[44:47], v[212:215], 0\n"
      "v_readlane_b32 s75, v255, 3\n"
      "v_rcp_f32 v26, v26\n"
      "s_waitcnt lgkmcnt(2)\n"
      "v_mfma_f32_16x16x32_f16 v[192:195], v[228:231], v[140:143], v[192:195]\n"
      "ds_read_b128 v[224:227], v33 offset:1728\n"
      "v_cvt_pk_bf16_f32 v24, v188, v190\n"
      "v_cvt_pk_bf16_f32 v25, v189, v191\n"
      "ds_write2st64_b32 v39, v24, v25 offset0:2 offset1:10\n"
      "v_readlane_b32 s76, v255, 4\n"
      "v_mfma_f32_16x16x32_f16 v[196:199], v[228:231], v[144:147], v[196:199]\n"
      "v_readlane_b32 s77, v255, 5\n"
      "v_and_b32 v27, 0xffff0000, v70\n"
      "v_mfma_f32_16x16x32_f16 v[200:203], v[228:231], v[148:151], v[200:203]\n"
      "ds_read2_b32 v[176:177], v30 offset0:104 offset1:105\n"
      "ds_read_b64_tr_b16 v[232:233], v35 offset:3640\n"
      "ds_read_b64_tr_b16 v[234:235], v35 offset:3624\n"
      "v_readlane_b32 s78, v255, 6\n"
      "v_lshlrev_b32 v29, 16, v70\n"
      "v_mfma_f32_16x16x32_f16 v[192:195], v[108:111], v[216:219], v[192:195]\n"
      "v_readlane_b32 s79, v255, 7\n"
      "v_rcp_f32 v27, v27\n"
      "v_mfma_f32_16x16x32_f16 v[196:199], v[76:79], v[216:219], v[196:199]\n"
      "v_mul_f32 v28, -v28, v26\n"
      "v_mul_f32 v29, -v29, v27\n"
      "s_setprio 0\n"
      "s_waitcnt lgkmcnt(3)\n"
      "v_mfma_f32_16x16x32_f16 v[192:195], v[112:115], v[220:223], v[192:195]\n"
      "ds_read_b128 v[212:215], v33 offset:1792\n"
      "v_lshlrev_b32 v180, 16, v56\n"
      "s_waitcnt vmcnt(0)\n"
      "v_lshlrev_b32 v181, 16, v60\n"
      "v_mfma_f32_16x16x32_f16 v[196:199], v[80:83], v[220:223], v[196:199]\n"
      "ds_read2_b32 v[178:179], v30 offset0:144 offset1:145\n"
      "v_and_b32 v182, 0xffff0000, v56\n"
      "v_and_b32 v183, 0xffff0000, v60\n"
      "v_mfma_f32_16x16x32_f16 v[192:195], v[116:119], v[224:227], v[192:195]\n"
      "ds_read_b128 v[216:219], v33 offset:1856\n"
      "v_fma_f32 v180, v180, v26, v28\n"
      "v_fma_f32 v181, v181, v27, v29\n"
      "v_mfma_f32_16x16x32_f16 v[196:199], v[84:87], v[224:227], v[196:199]\n"
      "v_fma_f32 v182, v182, v26, v28\n"
      "v_fma_f32 v183, v183, v27, v29\n"
      "s_waitcnt lgkmcnt(2)\n"
      "v_mfma_f32_16x16x32_f16 v[192:195], v[232:235], v[152:155], v[192:195]\n"
      "ds_read_b128 v[220:223], v33 offset:1920\n"
      "v_cvt_pk_f16_f32 v180, v180, v181\n"
      "v_cvt_pk_f16_f32 v182, v182, v183\n"
      "ds_write2st64_b32 v32, v180, v182 offset0:8 offset1:27\n"
      "v_mfma_f32_16x16x32_f16 v[196:199], v[232:235], v[156:159], v[196:199]\n"
      "v_lshlrev_b32 v184, 16, v57\n"
      "v_lshlrev_b32 v185, 16, v61\n"
      "s_cmp_lg_u32 s33, 0\n"
      "s_cbranch_scc1 .Lcg%=\n"
      "buffer_load_dwordx2 v[252:253], v9, s[16:19], s43 offen sc1\n"
      ".Lcg%=:\n"
      "v_mfma_f32_16x16x32_f16 v[200:203], v[232:235], v[160:163], v[200:203]\n"
      "ds_read_b128 v[224:227], v33 offset:1984\n"
      "v_and_b32 v186, 0xffff0000, v57\n"
      "v_and_b32 v187, 0xffff0000, v61\n"
      "v_mfma_f32_16x16x32_f16 v[192:195], v[120:123], v[212:215], v[192:195]\n"
      "v_fma_f32 v184, v184, v26, v28\n"
      "v_fma_f32 v185, v185, v27, v29\n"
      "v_mfma_f32_16x16x32_f16 v[196:199], v[88:91], v[212:215], v[196:199]\n"
      "ds_read_b128 v[212:215], v33 offset:2048\n"
      "v_fma_f32 v186, v186, v26, v28\n"
      "v_fma_f32 v187, v187, v27, v29\n"
      "s_waitcnt lgkmcnt(2)\n"
      "v_mfma_f32_16x16x32_f16 v[192:195], v[124:127], v[216:219], v[192:195]\n"
      "v_cvt_pk_f16_f32 v184, v184, v185\n"
      "v_cvt_pk_f16_f32 v186, v186, v187\n"
      "ds_write2st64_b32 v32, v184, v186 offset0:46 offset1:65\n"
      "v_mfma_f32_16x16x32_f16 v[196:199], v[92:95], v[216:219], v[196:199]\n"
      "v_lshlrev_b32 v180, 16, v58\n"
      "v_lshlrev_b32 v181, 16, v62\n"
      "v_mfma_f32_16x16x32_f16 v[192:195], v[128:131], v[220:223], v[192:195]\n"
      "v_and_b32 v182, 0xffff0000, v58\n"
      "v_and_b32 v183, 0xffff0000, v62\n"
      "v_mfma_f32_16x16x32_f16 v[196:199], v[96:99], v[220:223], v[196:199]\n"
      "ds_read_b64_tr_b16 v[228:229], v36 offset:24\n"
      "ds_read_b64_tr_b16 v[230:231], v36 offset:2088\n"
      "v_fma_f32 v180, v180, v26, v28\n"
      "v_fma_f32 v181, v181, v27, v29\n"
      "s_waitcnt lgkmcnt(3)\n"
      "v_mfma_f32_16x16x32_f16 v[192:195], v[132:135], v[224:227], v[192:195]\n"
      "v_fma_f32 v182, v182, v26, v28\n"
      "v_fma_f32 v183, v183, v27, v29\n"
      "v_mfma_f32_16x16x32_f16 v[196:199], v[100:103], v[224:227], v[196:199]\n"
      "v_cvt_pk_f16_f32 v180, v180, v181\n"
      "v_cvt_pk_f16_f32 v182, v182, v183\n"
      "ds_write2st64_b32 v32, v180, v182 offset0:84 offset1:103\n"
      "v_mfma_f32_16x16x32_f16 v[192:195], v[136:139], v[212:215], v[192:195]\n"
      "v_lshlrev_b32 v184, 16, v59\n"
      "v_lshlrev_b32 v185, 16, v63\n"
      "v_mfma_f32_16x16x32_f16 v[196:199], v[104:107], v[212:215], v[196:199]\n"
      "ds_read_b64_tr_b16 v[232:233], v36 offset:536\n"
      "ds_read_b64_tr_b16 v[234:235], v36 offset:2600\n"
      "v_and_b32 v186, 0xffff0000, v59\n"
      "v_and_b32 v187, 0xffff0000, v63\n"
      "v_mfma_f32_16x16x32_f16 v[200:203], v[72:75], v[212:215], v[200:203]\n"
      "v_fma_f32 v184, v184, v26, v28\n"
      "v_fma_f32 v185, v185, v27, v29\n"
      "v_mfma_f32_16x16x32_f16 v[208:211], v[44:47], v[212:215], 0\n"
      "v_fma_f32 v186, v186, v26, v28\n"
      "v_fma_f32 v187, v187, v27, v29\n"
      "s_waitcnt lgkmcnt(2)\n"
      "v_mfma_f32_16x16x32_f16 v[196:199], v[228:231], v[164:167], v[196:199]\n"
      "v_cvt_pk_bf16_f32 v24, v192, v194\n"
      "v_cvt_pk_bf16_f32 v25, v193, v195\n"
      "ds_write2st64_b32 v39, v24, v25 offset0:3 offset1:11\n"
      "v_mfma_f32_16x16x32_f16 v[200:203], v[228:231], v[168:171], v[200:203]\n"
      "v_cvt_pk_f16_f32 v184, v184, v185\n"
      "v_cvt_pk_f16_f32 v186, v186, v187\n"
      "ds_write2st64_b32 v32, v184, v186 offset0:122 offset1:141\n"
      "v_mfma_f32_16x16x32_f16 v[204:207], v[228:231], v[172:175], v[204:207]\n"
      "v_permlane32_swap_b32 v56, v60\n"
      "v_permlane32_swap_b32 v57, v61\n"
      "v_mfma_f32_16x16x32_f16 v[208:211], v[228:231], v[176:179], v[208:211]\n"
      "ds_read_b64_tr_b16 v[228:229], v35 offset:1056\n"
      "ds_read_b64_tr_b16 v[230:231], v35 offset:1040\n"
      "v_permlane32_swap_b32 v58, v62\n"
      "v_permlane32_swap_b32 v59, v63\n"
      "s_cmp_eq_u32 s33, 0\n"
      "s_cbranch_scc1 .Lch%=\n"
      ".Lcho%=:\n"
      "s_waitcnt lgkmcnt(2)\n"
      "s_barrier\n"
      "s_setprio 3\n"
      "v_mfma_f32_16x16x32_f16 v[200:203], v[232:235], v[164:167], v[200:203]\n"
      "ds_read_b64_tr_b16 v[64:65], v40 offset:0\n"
      "ds_read_b64_tr_b16 v[66:67], v40 offset:35072\n"
      "v_lshlrev_b32 v68, 16, v48\n"
      "v_and_b32 v69, 0xffff0000, v48\n"
      "v_mfma_f32_16x16x32_f16 v[204:207], v[232:235], v[168:171], v[204:207]\n"
      "v_lshlrev_b32 v70, 16, v49\n"
      "v_and_b32 v71, 0xffff0000, v49\n"
      "ds_read_b128 v[236:239], v34 offset:2048\n"
      "ds_read_b128 v[240:243], v34 offset:2064\n"
      "ds_read_b32 v12, v14 offset:256\n"
      "v_mfma_f32_16x16x32_f16 v[208:211], v[232:235], v[172:175], v[208:211]\n"
      "ds_read_b128 v[216:219], v33 offset:2112\n"
      "ds_read_b64_tr_b16 v[232:233], v35 offset:4152\n"
      "ds_read_b64_tr_b16 v[234:235], v35 offset:4136\n"
      "v_lshlrev_b32 v26, 16, v50\n"
      "v_and_b32 v27, 0xffff0000, v50\n"
      "s_waitcnt lgkmcnt(6)\n"
      "v_mfma_f32_16x16x32_f16 v[196:199], v[228:231], v[140:143], v[196:199]\n"
      "v_lshlrev_b32 v28, 16, v51\n"
      "v_and_b32 v29, 0xffff0000, v51\n"
      "v_mfma_f32_16x16x32_f16 v[200:203], v[228:231], v[144:147], v[200:203]\n"
      "ds_read_b128 v[220:223], v33 offset:2176\n"
      "v_lshlrev_b32 v48, 16, v64\n"
      "v_and_b32 v64, 0xffff0000, v64\n"
      "v_mfma_f32_16x16x32_f16 v[204:207], v[228:231], v[148:151], v[204:207]\n"
      "v_fmac_f32 v68, s72, v48\n"
      "v_fmac_f32 v69, s73, v64\n"
      "s_waitcnt lgkmcnt(1)\n"
      "v_mfma_f32_16x16x32_f16 v[196:199], v[232:235], v[152:155], v[196:199]\n"
      "ds_read_b128 v[224:227], v33 offset:2240\n"
      "v_lshlrev_b32 v49, 16, v65\n"
      "v_and_b32 v65, 0xffff0000, v65\n"
      "v_mfma_f32_16x16x32_f16 v[200:203], v[232:235], v[156:159], v[200:203]\n"
      "v_fmac_f32 v70, s74, v49\n"
      "v_fmac_f32 v71, s75, v65\n"
      "v_mfma_f32_16x16x32_f16 v[204:207], v[232:235], v[160:163], v[204:207]\n"
      "ds_read_b128 v[212:215], v33 offset:2304\n"
      "v_lshlrev_b32 v50, 16, v66\n"
      "v_and_b32 v66, 0xffff0000, v66\n"
      "v_mfma_f32_16x16x32_f16 v[196:199], v[108:111], v[216:219], v[196:199]\n"
      "v_fmac_f32 v26, s76, v50\n"
      "v_fmac_f32 v27, s77, v66\n"
      "v_mfma_f32_16x16x32_f16 v[200:203], v[76:79], v[216:219], v[200:203]\n"
      "ds_read_b64_tr_b16 v[228:229], v35 offset:1568\n"
      "ds_read_b64_tr_b16 v[230:231], v35 offset:1552\n"
      "v_lshlrev_b32 v51, 16, v67\n"
      "v_and_b32 v67, 0xffff0000, v67\n"
      "s_waitcnt lgkmcnt(3)\n"
      "v_mfma_f32_16x16x32_f16 v[196:199], v[112:115], v[220:223], v[196:199]\n"
      "v_fmac_f32 v28, s78, v51\n"
      "v_fmac_f32 v29, s79, v67\n"
      "v_mfma_f32_16x16x32_f16 v[200:203], v[80:83], v[220:223], v[200:203]\n"
      "v_cvt_pk_bf16_f32 v48, v68, v69\n"
      "v_cvt_pk_bf16_f32 v49, v70, v71\n"
      "v_mfma_f32_16x16x32_f16 v[196:199], v[116:119], v[224:227], v[196:199]\n"
      "ds_read_b128 v[216:219], v33 offset:2368\n"
      "v_cvt_pk_bf16_f32 v50, v26, v27\n"
      "v_cvt_pk_bf16_f32 v51, v28, v29\n"
      "s_setprio 2\n"
      "v_mfma_f32_16x16x32_f16 v[200:203], v[84:87], v[224:227], v[200:203]\n"
      "v_lshlrev_b32 v13, 24, v12\n"
      "v_and_or_b32 v13, v13, s58, v42\n"
      "s_waitcnt lgkmcnt(1)\n"
      "v_mfma_f32_16x16x32_f16 v[196:199], v[120:123], v[212:215], v[196:199]\n"
      "ds_read_b128 v[220:223], v33 offset:2432\n"
      "s_cmp_lg_u32 s13, 0\n"
      "s_cbranch_scc1 .Lcs00%=\n"
      "buffer_store_dwordx4 v[48:51], v13, s[24:27], s54 offen\n"
      ".Lcs00%=:\n"
      "v_alignbit_b32 v245, v238, v237, 16\n"
      "v_alignbit_b32 v246, v239, v238, 16\n"
      "v_mfma_f32_16x16x32_f16 v[200:203], v[88:91], v[212:215], v[200:203]\n"
      "v_alignbit_b32 v247, v240, v239, 16\n"
      "v_alignbit_b32 v248, v241, v240, 16\n"
      "v_mfma_f32_16x16x32_f16 v[200:203], v[228:231], v[140:143], v[200:203]\n"
      "ds_read_b128 v[224:227], v33 offset:2496\n"
      "s_cmp_lg_u32 s13, 1\n"
      "s_cbranch_scc1 .Lcs01%=\n"
      "buffer_store_dwordx4 v[48:51], v13, s[24:27], s54 offen\n"
      ".Lcs01%=:\n"
      "v_alignbit_b32 v249, v242, v241, 16\n"
      "v_pk_add_f16 v16, v245, v246\n"
      "v_mfma_f32_16x16x32_f16 v[204:207], v[228:231], v[144:147], v[204:207]\n"
      "v_pk_add_f16 v17, v246, v247\n"
      "v_pk_add_f16 v18, v247, v248\n"
      "v_mfma_f32_16x16x32_f16 v[208:211], v[228:231], v[148:151], v[208:211]\n"
      "ds_read_b128 v[212:215], v33 offset:2560\n"
      "s_cmp_lg_u32 s13, 2\n"
      "s_cbranch_scc1 .Lcs02%=\n"
      "buffer_store_dwordx4 v[48:51], v13, s[24:27], s54 offen\n"
      ".Lcs02%=:\n"
      "v_pk_add_f16 v19, v248, v249\n"
      "v_pk_add_f16 v20, v16, v238\n"
      "s_waitcnt lgkmcnt(1)\n"
      "v_mfma_f32_16x16x32_f16 v[196:199], v[124:127], v[216:219], v[196:199]\n"
      "v_pk_add_f16 v21, v17, v239\n"
      "v_pk_add_f16 v22, v18, v240\n"
      "v_mfma_f32_16x16x32_f16 v[200:203], v[92:95], v[216:219], v[200:203]\n"
      "s_cmp_lg_u32 s13, 3\n"
      "s_cbranch_scc1 .Lcs03%=\n"
      "buffer_store_dwordx4 v[48:51], v13, s[24:27], s54 offen\n"
      ".Lcs03%=:\n"
      "v_pk_add_f16 v23, v19, v241\n"
      "ds_write_b128 v37, v[20:23] offset:2048\n"
      "v_pk_add_f16 v16, v237, v239\n"
      "v_mfma_f32_16x16x32_f16 v[196:199], v[128:131], v[220:223], v[196:199]\n"
      "ds_read_b64_tr_b16 v[232:233], v35 offset:4664\n"
      "ds_read_b64_tr_b16 v[234:235], v35 offset:4648\n"
      "ds_read_b64_tr_b16 v[64:65], v40 offset:512\n"
      "ds_read_b64_tr_b16 v[66:67], v40 offset:35584\n"
      "v_lshlrev_b32 v68, 16, v52\n"
      "v_and_b32 v69, 0xffff0000, v52\n"
      "v_mfma_f32_16x16x32_f16 v[200:203], v[96:99], v[220:223], v[200:203]\n"
      "v_lshlrev_b32 v70, 16, v53\n"
      "v_and_b32 v71, 0xffff0000, v53\n"
      "v_mfma_f32_16x16x32_f16 v[196:199], v[132:135], v[224:227], v[196:199]\n"
      "v_lshlrev_b32 v26, 16, v54\n"
      "v_and_b32 v27, 0xffff0000, v54\n"
      "v_mfma_f32_16x16x32_f16 v[200:203], v[100:103], v[224:227], v[200:203]\n"
      "ds_read_b128 v[216:219], v33 offset:2624\n"
      "v_lshlrev_b32 v28, 16, v55\n"
      "v_and_b32 v29, 0xffff0000, v55\n"
      "s_waitcnt lgkmcnt(1)\n"
      "v_mfma_f32_16x16x32_f16 v[196:199], v[136:139], v[212:215], v[196:199]\n"
      "v_lshlrev_b32 v52, 16, v64\n"
      "v_and_b32 v64, 0xffff0000, v64\n"
      "v_mfma_f32_16x16x32_f16 v[200:203], v[104:107], v[212:215], v[200:203]\n"
      "ds_read_b128 v[220:223], v33 offset:2688\n"
      "v_fmac_f32 v68, s72, v52\n"
      "v_fmac_f32 v69, s73, v64\n"
      "s_setprio 1\n"
      "v_mfma_f32_16x16x32_f16 v[204:207], v[72:75], v[212:215], v[204:207]\n"
      "v_lshlrev_b32 v53, 16, v65\n"
      "v_and_b32 v65, 0xffff0000, v65\n"
      "v_mfma_f32_16x16x32_f16 v[200:203], v[232:235], v[152:155], v[200:203]\n"
      "ds_read_b128 v[224:227], v33 offset:2752\n"
      "v_fmac_f32 v70, s74, v53\n"
      "v_fmac_f32 v71, s75, v65\n"
      "v_mfma_f32_16x16x32_f16 v[204:207], v[232:235], v[156:159], v[204:207]\n"
      "v_cvt_pk_bf16_f32 v24, v196, v198\n"
      "v_cvt_pk_bf16_f32 v25, v197, v199\n"
      "ds_write2st64_b32 v39, v24, v25 offset0:4 offset1:12\n"
      "v_mfma_f32_16x16x32_f16 v[208:211], v[232:235], v[160:163], v[208:211]\n"
      "ds_read_b128 v[212:215], v33 offset:2816\n"
      "v_lshlrev_b32 v54, 16, v66\n"
      "v_and_b32 v66, 0xffff0000, v66\n"
      "s_waitcnt lgkmcnt(3)\n"
      "v_mfma_f32_16x16x32_f16 v[200:203], v[108:111], v[216:219], v[200:203]\n"
      "v_fmac_f32 v26, s76, v54\n"
      "v_fmac_f32 v27, s77, v66\n"
      "v_mfma_f32_16x16x32_f16 v[204:207], v[76:79], v[216:219], v[204:207]\n"
      "ds_read_b128 v[216:219], v33 offset:2880\n"
      "v_lshlrev_b32 v55, 16, v67\n"
      "v_and_b32 v67, 0xffff0000, v67\n"
      "v_mfma_f32_16x16x32_f16 v[200:203], v[112:115], v[220:223], v[200:203]\n"
      "v_fmac_f32 v28, s78, v55\n"
      "v_fmac_f32 v29, s79, v67\n"
      "v_mfma_f32_16x16x32_f16 v[204:207], v[80:83], v[220:223], v[204:207]\n"
      "ds_read_b128 v[220:223], v33 offset:2944\n"
      "v_cvt_pk_bf16_f32 v52, v68, v69\n"
      "v_cvt_pk_bf16_f32 v53, v70, v71\n"
      "s_waitcnt lgkmcnt(2)\n"
      "v_mfma_f32_16x16x32_f16 v[200:203], v[116:119], v[224:227], v[200:203]\n"
      "v_cvt_pk_bf16_f32 v54, v26, v27\n"
      "v_cvt_pk_bf16_f32 v55, v28, v29\n"
      "v_mfma_f32_16x16x32_f16 v[204:207], v[84:87], v[224:227], v[204:207]\n"
      "ds_read_b128 v[224:227], v33 offset:3008\n"
      "v_lshlrev_b32 v13, 16, v12\n"
      "v_and_or_b32 v13, v13, s58, v42\n"
      "v_mfma_f32_16x16x32_f16 v[200:203], v[120:123], v[212:215], v[200:203]\n"
      "s_cmp_lg_u32 s13, 0\n"
      "s_cbranch_scc1 .Lcs10%=\n"
      "buffer_store_dwordx4 v[52:55], v13, s[24:27], s55 offen\n"
      ".Lcs10%=:\n"
      "v_pk_add_f16 v17, v238, v240\n"
      "v_pk_add_f16 v18, v239, v241\n"
      "s_setprio 0\n"
      "v_mfma_f32_16x16x32_f16 v[204:207], v[88:91], v[212:215], v[204:207]\n"
      "ds_read_b128 v[212:215], v33 offset:3072\n"
      "v_pk_add_f16 v19, v240, v242\n"
      "v_pk_add_f16 v20, v20, v16\n"
      "s_waitcnt lgkmcnt(1)\n"
      "v_mfma_f32_16x16x32_f16 v[200:203], v[124:127], v[216:219], v[200:203]\n"
      "s_cmp_lg_u32 s13, 1\n"
      "s_cbranch_scc1 .Lcs11%=\n"
      "buffer_store_dwordx4 v[52:55], v13, s[24:27], s55 offen\n"
      ".Lcs11%=:\n"
      "v_pk_add_f16 v21, v21, v17\n"
      "v_pk_add_f16 v22, v22, v18\n"
      "v_mfma_f32_16x16x32_f16 v[204:207], v[92:95], v[216:219], v[204:207]\n"
      "v_pk_add_f16 v23, v23, v19\n"
      "ds_write_b128 v37, v[20:23] offset:5152\n"
      "v_mfma_f32_16x16x32_f16 v[200:203], v[128:131], v[220:223], v[200:203]\n"
      "ds_read_b64_tr_b16 v[228:229], v36 offset:1048\n"
      "ds_read_b64_tr_b16 v[230:231], v36 offset:3112\n"
      "s_cmp_lg_u32 s13, 2\n"
      "s_cbranch_scc1 .Lcs12%=\n"
      "buffer_store_dwordx4 v[52:55], v13, s[24:27], s55 offen\n"
      ".Lcs12%=:\n"
      "v_mfma_f32_16x16x32_f16 v[204:207], v[96:99], v[220:223], v[204:207]\n"
      "v_mfma_f32_16x16x32_f16 v[200:203], v[132:135], v[224:227], v[200:203]\n"
      "ds_read_b128 v[216:219], v33 offset:3136\n"
      "s_cmp_lg_u32 s13, 3\n"
      "s_cbranch_scc1 .Lcs13%=\n"
      "buffer_store_dwordx4 v[52:55], v13, s[24:27], s55 offen\n"
      ".Lcs13%=:\n"
      "v_mfma_f32_16x16x32_f16 v[204:207], v[100:103], v[224:227], v[204:207]\n"
      "s_waitcnt lgkmcnt(1)\n"
      "v_mfma_f32_16x16x32_f16 v[200:203], v[136:139], v[212:215], v[200:203]\n"
      "ds_read_b128 v[220:223], v33 offset:3200\n"
      "v_mfma_f32_16x16x32_f16 v[204:207], v[104:107], v[212:215], v[204:207]\n"
      "v_mfma_f32_16x16x32_f16 v[208:211], v[72:75], v[212:215], v[208:211]\n"
      "ds_read_b128 v[224:227], v33 offset:3264\n"
      "v_mfma_f32_16x16x32_f16 v[204:207], v[228:231], v[164:167], v[204:207]\n"
      "v_mfma_f32_16x16x32_f16 v[208:211], v[228:231], v[168:171], v[208:211]\n"
      "ds_read_b64_tr_b16 v[232:233], v36 offset:1560\n"
      "ds_read_b64_tr_b16 v[234:235], v36 offset:3624\n"
      "v_cvt_pk_bf16_f32 v24, v200, v202\n"
      "v_cvt_pk_bf16_f32 v25, v201, v203\n"
      "ds_write2st64_b32 v39, v24, v25 offset0:5 offset1:13\n"
      "s_waitcnt lgkmcnt(5)\n"
      "v_mfma_f32_16x16x32_f16 v[204:207], v[108:111], v[216:219], v[204:207]\n"
      "ds_read_b128 v[212:215], v33 offset:3328\n"
      "v_mfma_f32_16x16x32_f16 v[208:211], v[76:79], v[216:219], v[208:211]\n"
      "s_waitcnt lgkmcnt(5)\n"
      "v_mfma_f32_16x16x32_f16 v[204:207], v[112:115], v[220:223], v[204:207]\n"
      "ds_read_b128 v[216:219], v33 offset:3392\n"
      "s_waitcnt lgkmcnt(2)\n"
      "s_barrier\n"
      "s_setprio 3\n"
      "v_mfma_f32_16x16x32_f16 v[208:211], v[80:83], v[220:223], v[208:211]\n"
      "ds_read_b64_tr_b16 v[64:65], v40 offset:1024\n"
      "ds_read_b64_tr_b16 v[66:67], v40 offset:36096\n"
      "v_lshlrev_b32 v68, 16, v56\n"
      "v_and_b32 v69, 0xffff0000, v56\n"
      "v_mfma_f32_16x16x32_f16 v[204:207], v[116:119], v[224:227], v[204:207]\n"
      "ds_read_b128 v[220:223], v33 offset:3456\n"
      "v_lshlrev_b32 v70, 16, v57\n"
      "v_and_b32 v71, 0xffff0000, v57\n"
      "v_mfma_f32_16x16x32_f16 v[208:211], v[84:87], v[224:227], v[208:211]\n"
      "v_lshlrev_b32 v26, 16, v58\n"
      "v_and_b32 v27, 0xffff0000, v58\n"
      "v_mfma_f32_16x16x32_f16 v[208:211], v[232:235], v[164:167], v[208:211]\n"
      "ds_read_b64_tr_b16 v[228:229], v35 offset:2080\n"
      "ds_read_b64_tr_b16 v[230:231], v35 offset:2064\n"
      "v_lshlrev_b32 v28, 16, v59\n"
      "v_and_b32 v29, 0xffff0000, v59\n"
      "s_waitcnt lgkmcnt(3)\n"
      "v_mfma_f32_16x16x32_f16 v[204:207], v[120:123], v[212:215], v[204:207]\n"
      "v_lshlrev_b32 v56, 16, v64\n"
      "v_and_b32 v64, 0xffff0000, v64\n"
      "v_mfma_f32_16x16x32_f16 v[208:211], v[88:91], v[212:215], v[208:211]\n"
      "ds_read_b128 v[224:227], v33 offset:3520\n"
      "v_fmac_f32 v68, s72, v56\n"
      "v_fmac_f32 v69, s73, v64\n"
      "v_mfma_f32_16x16x32_f16 v[204:207], v[124:127], v[216:219], v[204:207]\n"
      "v_lshlrev_b32 v57, 16, v65\n"
      "v_and_b32 v65, 0xffff0000, v65\n"
      "v_mfma_f32_16x16x32_f16 v[208:211], v[92:95], v[216:219], v[208:211]\n"
      "ds_read_b128 v[212:215], v33 offset:3584\n"
      "v_fmac_f32 v70, s74, v57\n"
      "v_fmac_f32 v71, s75, v65\n"
      "s_setprio 2\n"
      "s_waitcnt lgkmcnt(2)\n"
      "v_mfma_f32_16x16x32_f16 v[204:207], v[128:131], v[220:223], v[204:207]\n"
      "v_lshlrev_b32 v58, 16, v66\n"
      "v_and_b32 v66, 0xffff0000, v66\n"
      "v_mfma_f32_16x16x32_f16 v[208:211], v[96:99], v[220:223], v[208:211]\n"
      "ds_read_b128 v[216:219], v33 offset:3648\n"
      "v_fmac_f32 v26, s76, v58\n"
      "v_fmac_f32 v27, s77, v66\n"
      "v_mfma_f32_16x16x32_f16 v[204:207], v[228:231], v[140:143], v[204:207]\n"
      "ds_read_b64_tr_b16 v[232:233], v35 offset:5176\n"
      "ds_read_b64_tr_b16 v[234:235], v35 offset:5160\n"
      "v_lshlrev_b32 v59, 16, v67\n"
      "v_and_b32 v67, 0xffff0000, v67\n"
      "v_mfma_f32_16x16x32_f16 v[208:211], v[228:231], v[144:147], v[208:211]\n"
      "v_fmac_f32 v28, s78, v59\n"
      "v_fmac_f32 v29, s79, v67\n"
      "s_waitcnt lgkmcnt(3)\n"
      "v_mfma_f32_16x16x32_f16 v[204:207], v[132:135], v[224:227], v[204:207]\n"
      "ds_read_b128 v[220:223], v33 offset:3712\n"
      "v_cvt_pk_bf16_f32 v56, v68, v69\n"
      "v_cvt_pk_bf16_f32 v57, v70, v71\n"
      "v_mfma_f32_16x16x32_f16 v[208:211], v[100:103], v[224:227], v[208:211]\n"
      "ds_read_b128 v[224:227], v33 offset:3776\n"
      "v_cvt_pk_bf16_f32 v58, v26, v27\n"
      "v_cvt_pk_bf16_f32 v59, v28, v29\n"
      "v_mfma_f32_16x16x32_f16 v[204:207], v[136:139], v[212:215], v[204:207]\n"
      "v_lshlrev_b32 v13, 8, v12\n"
      "v_and_or_b32 v13, v13, s58, v42\n"
      "s_setprio 1\n"
      "v_mfma_f32_16x16x32_f16 v[208:211], v[104:107], v[212:215], v[208:211]\n"
      "ds_read_b64_tr_b16 v[228:229], v35 offset:2592\n"
      "ds_read_b64_tr_b16 v[230:231], v35 offset:2576\n"
      "ds_read_b128 v[212:215], v33 offset:3840\n"
      "s_cmp_lg_u32 s13, 0\n"
      "s_cbranch_scc1 .Lcs20%=\n"
      "buffer_store_dwordx4 v[56:59], v13, s[24:27], s56 offen\n"
      ".Lcs20%=:\n"
      "v_lshlrev_b32 v68, 16, v60\n"
      "v_and_b32 v69, 0xffff0000, v60\n"
      "s_waitcnt lgkmcnt(3)\n"
      "v_mfma_f32_16x16x32_f16 v[208:211], v[108:111], v[216:219], v[208:211]\n"
      "ds_read_b128 v[216:219], v33 offset:3904\n"
      "v_lshlrev_b32 v70, 16, v61\n"
      "v_and_b32 v71, 0xffff0000, v61\n"
      "v_mfma_f32_16x16x32_f16 v[204:207], v[232:235], v[152:155], v[204:207]\n"
      "s_cmp_lg_u32 s13, 1\n"
      "s_cbranch_scc1 .Lcs21%=\n"
      "buffer_store_dwordx4 v[56:59], v13, s[24:27], s56 offen\n"
      ".Lcs21%=:\n"
      "v_lshlrev_b32 v26, 16, v62\n"
      "v_and_b32 v27, 0xffff0000, v62\n"
      "v_mfma_f32_16x16x32_f16 v[208:211], v[232:235], v[156:159], v[208:211]\n"
      "ds_read_b64_tr_b16 v[232:233], v35 offset:5688\n"
      "ds_read_b64_tr_b16 v[234:235], v35 offset:5672\n"
      "v_lshlrev_b32 v28, 16, v63\n"
      "v_and_b32 v29, 0xffff0000, v63\n"
      "v_mfma_f32_16x16x32_f16 v[208:211], v[112:115], v[220:223], v[208:211]\n"
      "ds_read_b128 v[220:223], v33 offset:3968\n"
      "s_cmp_lg_u32 s13, 2\n"
      "s_cbranch_scc1 .Lcs22%=\n"
      "buffer_store_dwordx4 v[56:59], v13, s[24:27], s56 offen\n"
      ".Lcs22%=:\n"
      "s_setprio 0\n"
      "v_mfma_f32_16x16x32_f16 v[208:211], v[116:119], v[224:227], v[208:211]\n"
      "ds_read_b128 v[224:227], v33 offset:4032\n"
      "s_waitcnt lgkmcnt(3)\n"
      "v_mfma_f32_16x16x32_f16 v[208:211], v[120:123], v[212:215], v[208:211]\n"
      "ds_read_b128 v[212:215], v33 offset:4096\n"
      "v_cvt_pk_bf16_f32 v24, v204, v206\n"
      "v_cvt_pk_bf16_f32 v25, v205, v207\n"
      "ds_write2st64_b32 v39, v24, v25 offset0:6 offset1:14\n"
      "s_cmp_lg_u32 s13, 3\n"
      "s_cbranch_scc1 .Lcs23%=\n"
      "buffer_store_dwordx4 v[56:59], v13, s[24:27], s56 offen\n"
      ".Lcs23%=:\n"
      "v_mfma_f32_16x16x32_f16 v[208:211], v[228:231], v[140:143], v[208:211]\n"
      "v_mfma_f32_16x16x32_f16 v[208:211], v[124:127], v[216:219], v[208:211]\n"
      "s_waitcnt lgkmcnt(2)\n"
      "v_mfma_f32_16x16x32_f16 v[208:211], v[128:131], v[220:223], v[208:211]\n"
      "v_mfma_f32_16x16x32_f16 v[208:211], v[232:235], v[152:155], v[208:211]\n"
      "v_mfma_f32_16x16x32_f16 v[208:211], v[132:135], v[224:227], v[208:211]\n"
      "s_waitcnt lgkmcnt(1)\n"
      "v_mfma_f32_16x16x32_f16 v[208:211], v[136:139], v[212:215], v[208:211]\n"
      "s_nop 7\n"
      "v_cvt_pk_bf16_f32 v24, v208, v210\n"
      "v_cvt_pk_bf16_f32 v25, v209, v211\n"
      "ds_write2st64_b32 v39, v24, v25 offset0:7 offset1:15\n"
      "s_waitcnt lgkmcnt(0)\n"
      "s_barrier\n"
      "ds_read_b64_tr_b16 v[64:65], v40 offset:1536\n"
      "ds_read_b64_tr_b16 v[66:67], v40 offset:36608\n"
      "s_cmp_lg_u32 s33, 0\n"
      "s_cbranch_scc1 .Lcb%=\n"
      "ds_read_u16_d16_hi v252, v8 offset:56384\n"
      "ds_read_u16_d16_hi v253, v8 offset:65152\n"
      "s_waitcnt lgkmcnt(0)\n"
      "v_pk_mul_f32 v[252:253], v[252:253], v[254:255]\n"
      "v_cvt_pk_bf16_f32 v252, v252, v253\n"
      "buffer_atomic_pk_add_bf16 v252, v251, s[24:27], s41 offen\n"
      ".Lcb%=:\n"
      "s_waitcnt lgkmcnt(1)\n"
      "v_lshlrev_b32 v60, 16, v64\n"
      "v_and_b32 v64, 0xffff0000, v64\n"
      "v_fmac_f32 v68, s72, v60\n"
      "v_fmac_f32 v69, s73, v64\n"
      "v_lshlrev_b32 v61, 16, v65\n"
      "v_and_b32 v65, 0xffff0000, v65\n"
      "v_fmac_f32 v70, s74, v61\n"
      "v_fmac_f32 v71, s75, v65\n"
      "s_waitcnt lgkmcnt(0)\n"
      "v_lshlrev_b32 v62, 16, v66\n"
      "v_and_b32 v66, 0xffff0000, v66\n"
      "v_fmac_f32 v26, s76, v62\n"
      "v_fmac_f32 v27, s77, v66\n"
      "v_lshlrev_b32 v63, 16, v67\n"
      "v_and_b32 v67, 0xffff0000, v67\n"
      "v_fmac_f32 v28, s78, v63\n"
      "v_fmac_f32 v29, s79, v67\n"
      "v_cvt_pk_bf16_f32 v60, v68, v69\n"
      "v_cvt_pk_bf16_f32 v61, v70, v71\n"
      "v_cvt_pk_bf16_f32 v62, v26, v27\n"
      "v_cvt_pk_bf16_f32 v63, v28, v29\n"
      "v_and_or_b32 v13, v12, s58, v42\n"
      "buffer_store_dwordx4 v[60:63], v13, s[24:27], s57 offen\n"
      ".LEx%=:\ns_endpgm\n"
      ".Lcsgsk%=:\n"
      "s_sleep 4\n"
      "s_sub_u32 s59, s59, 1\n"
      "s_cbranch_scc1 .Lcsgok%=\n"
      "buffer_load_dwordx4 v[64:67], v42, s[16:19], s62 offen sc1\n"
      "s_waitcnt vmcnt(0)\n"
      "s_branch .Lcsgk%=\n"
      ".Lcsgsh%=:\n"
      "s_sleep 4\n"
      "s_sub_u32 s59, s59, 1\n"
      "s_cbranch_scc1 .Lcsgoh%=\n"
      "buffer_load_dwordx4 v[68:71], v42, s[16:19], s63 offen sc1\n"
      "s_waitcnt vmcnt(0)\n"
      "s_branch .Lcsgh%=\n"
      ".Lch%=:\n"
      "s_movk_i32 s59, 0x7fff\n"
      ".Lchl%=:\n"
      "s_waitcnt vmcnt(0)\n"
      "v_cmp_ne_u32 vcc, s15, v253\n"
      "v_not_b32 v255, v252\n"
      "s_sub_u32 s59, s59, 1\n"
      "s_cbranch_scc1 .Lchk%=\n"
      "s_cbranch_vccz .Lchk%=\n"
      "s_sleep 1\n"
      "buffer_load_dwordx2 v[252:253], v9, s[16:19], s43 offen sc1\n"
      "s_branch .Lchl%=\n"
      ".Lchk%=:\n"
      "v_and_b32 v31, 0x1ff, v255\n"
      "v_bfe_u32 v8, v255, 9, 2\n"
      "v_lshl_add_u32 v31, v31, 2, v8\n"
      "v_mov_b32 v8, 64\n"
      "v_add_u32 v31, 0x27700, v31\n"
      "ds_write_b8 v31, v8 \n"
      "v_and_b32 v31, 1, v255\n"
      "v_and_b32 v8, 0x7fe, v255\n"
      "v_lshl_add_u32 v8, v31, 11, v8\n"
      "v_mul_u32_u24 v31, 0x1120, v10\n"
      "v_add_u32 v8, v8, v31\n"
      "v_lshlrev_b32 v31, 1, v10\n"
      "v_add_u32 v31, 0x27600, v31\n"
      "v_and_b32 v255, 0x7ff, v255\n"
      "v_lshl_add_u32 v251, v255, 11, v10\n"
      "v_and_b32 v252, 32, v43\n"
      "v_lshl_or_b32 v251, v252, 25, v251\n"
      "ds_read_b64 v[254:255], v31 \n"
      "v_mov_b32 v252, 0\n"
      "v_mov_b32 v253, 0\n"
      "s_waitcnt lgkmcnt(0)\n"
      "s_branch .Lcho%=\n"
      ".LG%=:\n"
      "s_setprio 3\n"
      "s_mul_i32 s35, s48, 14\n"
      "s_add_u32 s35, s35, s49\n"
      "s_and_b32 s55, s33, 3\n"
      "s_mul_i32 s50, s55, 1835008\n"
      "s_lshr_b32 s51, s35, 1\n"
      "s_lshl_b32 s51, s51, 7\n"
      "s_add_u32 s50, s50, s51\n"
      "v_lshrrev_b32 v37, 3, v43\n"
      "v_and_b32 v38, 7, v43\n"
      "v_mul_u32_u24 v37, 0xe000, v37\n"
      "v_lshlrev_b32 v38, 4, v38\n"
      "v_add3_u32 v32, v37, v38, s50\n"
      "s_cmp_lt_u32 s34, 112\n"
      "s_cselect_b32 s26, 0x700000, 0\n"
      "s_mov_b32 s27, 0x20000\n"
      "s_bitcmp1_b32 s49, 0\n"
      "s_waitcnt lgkmcnt(0)\n"
      "s_load_dword s15, s[88:89], 0x0\n"
      "s_cselect_b32 s24, s82, s80\n"
      "s_cselect_b32 s25, s83, s81\n"
      "s_and_b32 s25, s25, 0xffff\n"
      "s_mov_b32 s16, s88\n"
      "s_and_b32 s17, s89, 0xffff\n"
      "s_mov_b32 s18, 0x6e0000\n"
      "s_mov_b32 s19, 0x20000\n"
      "s_mov_b32 s36, s86\n"
      "s_and_b32 s37, s87, 0xffff\n"
      "s_barrier\n"
      "s_sleep 20\n"
      "s_mov_b32 s60, 0\n"
      "buffer_load_dwordx4 v[64:67], v32, s[24:27], s60 offen nt\n"
      "s_sleep 2\n"
      "s_mov_b32 s60, 7168\n"
      "buffer_load_dwordx4 v[68:71], v32, s[24:27], s60 offen nt\n"
      "s_sleep 2\n"
      "s_mov_b32 s60, 14336\n"
      "buffer_load_dwordx4 v[72:75], v32, s[24:27], s60 offen nt\n"
      "s_sleep 2\n"
      "s_mov_b32 s60, 21504\n"
      "buffer_load_dwordx4 v[76:79], v32, s[24:27], s60 offen nt\n"
      "s_sleep 2\n"
      "s_mov_b32 s60, 28672\n"
      "buffer_load_dwordx4 v[80:83], v32, s[24:27], s60 offen nt\n"
      "s_sleep 2\n"
      "s_mov_b32 s60, 35840\n"
      "buffer_load_dwordx4 v[84:87], v32, s[24:27], s60 offen nt\n"
      "s_sleep 2\n"
      "s_mov_b32 s60, 43008\n"
      "buffer_load_dwordx4 v[88:91], v32, s[24:27], s60 offen nt\n"
      "s_sleep 2\n"
      "s_mov_b32 s60, 50176\n"
      "buffer_load_dwordx4 v[92:95], v32, s[24:27], s60 offen nt\n"
      "s_sleep 2\n"
      "s_mov_b32 s60, 458752\n"
      "buffer_load_dwordx4 v[96:99], v32, s[24:27], s60 offen nt\n"
      "s_sleep 2\n"
      "s_waitcnt lgkmcnt(0)\n"
      "s_add_u32 s15, s15, 0x9e3779b1\n"
      "v_mov_b32 v47, s15\n"
      "s_cmp_ge_u32 s34, 112\n"
      "s_cbranch_scc1 .LT%=\n"
      "v_mov_b32 v48, 0x22680\n"
      "ds_read_b32 v48, v48 \n"
      "s_waitcnt lgkmcnt(0)\n"
      "v_readfirstlane_b32 s12, v48\n"
      "s_mov_b32 s62, 0x05040100\n"
      "s_mov_b32 s63, 0x07060302\n"
      "s_lshl_b32 s42, s34, 3\n"
      "s_add_u32 s42, s42, 2048\n"
      "s_lshl_b32 s43, s48, 8\n"
      "s_and_b32 s50, s49, 1\n"
      "s_lshl_b32 s50, s50, 7\n"
      "s_add_u32 s43, s43, s50\n"
      "s_add_u32 s43, s43, 65536\n"
      "s_mov_b32 s20, s64\n"
      "s_and_b32 s21, s65, 0xffff\n"
      "s_mov_b32 s22, 0x400000\n"
      "s_mov_b32 s23, 0x20000\n"
      "v_lshrrev_b32 v39, 4, v43\n"
      "v_and_b32 v38, 15, v43\n"
      "v_lshlrev_b32 v38, 4, v38\n"
      "v_bfe_u32 v33, v43, 2, 2\n"
      "v_and_b32 v37, 1, v43\n"
      "v_lshl_add_u32 v33, v33, 1, v37\n"
      "v_lshlrev_b32 v33, 3, v33\n"
      "v_and_b32 v37, 2, v43\n"
      "v_lshl_add_u32 v37, v39, 2, v37\n"
      "s_lshl_b32 s50, s55, 9\n"
      "v_lshl_add_u32 v36, v37, 3, s50\n"
      "s_lshl_b32 s51, s55, 4\n"
      "v_lshrrev_b32 v40, 5, v43\n"
      "v_lshl_add_u32 v40, v40, 2, s51\n"
      "s_lshl_b32 s51, s55, 2\n"
      "s_add_u32 s51, s51, 140800\n"
      "v_mov_b32 v41, s51\n"
      "v_mov_b32 v42, 0x22600\n"
      "v_lshl_add_u32 v37, v43, 3, s50\n"
      "v_add_u32 v35, 0x22700, v37\n"
      "s_lshl_b32 s50, s55, 11\n"
      "s_add_u32 s50, s50, 141056\n"
      "v_lshl_add_u32 v34, v39, 9, v38\n"
      "v_add_u32 v34, s50, v34\n"
      "s_lshr_b32 s45, s35, 1\n"
      "s_lshl_b32 s45, s45, 16\n"
      "s_and_b32 s50, s35, 1\n"
      "s_lshl_b32 s50, s50, 11\n"
      "s_add_u32 s45, s45, s50\n"
      "s_add_u32 s45, s45, 524288\n"
      "s_mov_b32 s56, 0x3f80\n"
      "s_mov_b32 s57, 0x3f800000\n"
      "s_movk_i32 s61, 0x7ff\n"
      "s_mov_b32 s58, 131072\n"
      "s_mov_b32 s54, 0\n"
      "s_mov_b64 s[66:67], 0\n"
      "s_movk_i32 s70, 427\n"
      "s_waitcnt vmcnt(8)\n"
      "s_nop 0\n"
      "s_mov_b32 s60, 465920\n"
      "buffer_load_dwordx4 v[100:103], v32, s[24:27], s60 offen nt\n"
      "s_waitcnt vmcnt(8)\n"
      "s_nop 0\n"
      "s_mov_b32 s60, 473088\n"
      "buffer_load_dwordx4 v[104:107], v32, s[24:27], s60 offen nt\n"
      "v_perm_b32 v192, v68, v64, s62\n"
      "v_perm_b32 v196, v68, v64, s63\n"
      "v_perm_b32 v200, v69, v65, s62\n"
      "v_perm_b32 v204, v69, v65, s63\n"
      "v_perm_b32 v208, v70, v66, s62\n"
      "v_perm_b32 v212, v70, v66, s63\n"
      "v_perm_b32 v216, v71, v67, s62\n"
      "v_perm_b32 v220, v71, v67, s63\n"
      "s_waitcnt vmcnt(8)\n"
      "s_nop 0\n"
      "s_mov_b32 s60, 480256\n"
      "buffer_load_dwordx4 v[108:111], v32, s[24:27], s60 offen nt\n"
      "s_waitcnt vmcnt(8)\n"
      "s_nop 0\n"
      "s_mov_b32 s60, 487424\n"
      "buffer_load_dwordx4 v[112:115], v32, s[24:27], s60 offen nt\n"
      "v_perm_b32 v193, v76, v72, s62\n"
      "v_perm_b32 v197, v76, v72, s63\n"
      "v_perm_b32 v201, v77, v73, s62\n"
      "v_perm_b32 v205, v77, v73, s63\n"
      "v_perm_b32 v209, v78, v74, s62\n"
      "v_perm_b32 v213, v78, v74, s63\n"
      "v_perm_b32 v217, v79, v75, s62\n"
      "v_perm_b32 v221, v79, v75, s63\n"
      "s_waitcnt vmcnt(8)\n"
      "s_nop 0\n"
      "s_mov_b32 s60, 494592\n"
      "buffer_load_dwordx4 v[116:119], v32, s[24:27], s60 offen nt\n"
      "s_waitcnt vmcnt(8)\n"
      "s_nop 0\n"
      "s_mov_b32 s60, 501760\n"
      "buffer_load_dwordx4 v[120:123], v32, s[24:27], s60 offen nt\n"
      "v_perm_b32 v194, v84, v80, s62\n"
      "v_perm_b32 v198, v84, v80, s63\n"
      "v_perm_b32 v202, v85, v81, s62\n"
      "v_perm_b32 v206, v85, v81, s63\n"
      "v_perm_b32 v210, v86, v82, s62\n"
      "v_perm_b32 v214, v86, v82, s63\n"
      "v_perm_b32 v218, v87, v83, s62\n"
      "v_perm_b32 v222, v87, v83, s63\n"
      "s_waitcnt vmcnt(8)\n"
      "s_nop 0\n"
      "s_mov_b32 s60, 508928\n"
      "buffer_load_dwordx4 v[124:127], v32, s[24:27], s60 offen nt\n"
      "s_waitcnt vmcnt(8)\n"
      "s_nop 0\n"
      "s_mov_b32 s60, 917504\n"
      "buffer_load_dwordx4 v[128:131], v32, s[24:27], s60 offen nt\n"
      "v_perm_b32 v195, v92, v88, s62\n"
      "v_perm_b32 v199, v92, v88, s63\n"
      "v_perm_b32 v203, v93, v89, s62\n"
      "v_perm_b32 v207, v93, v89, s63\n"
      "v_perm_b32 v211, v94, v90, s62\n"
      "v_perm_b32 v215, v94, v90, s63\n"
      "v_perm_b32 v219, v95, v91, s62\n"
      "v_perm_b32 v223, v95, v91, s63\n"
      "s_waitcnt vmcnt(8)\n"
      "s_nop 0\n"
      "s_mov_b32 s60, 924672\n"
      "buffer_load_dwordx4 v[132:135], v32, s[24:27], s60 offen nt\n"
      "s_waitcnt vmcnt(8)\n"
      "s_nop 0\n"
      "s_mov_b32 s60, 931840\n"
      "buffer_load_dwordx4 v[136:139], v32, s[24:27], s60 offen nt\n"
      "v_perm_b32 v64, v100, v96, s62\n"
      "v_perm_b32 v68, v100, v96, s63\n"
      "v_perm_b32 v72, v101, v97, s62\n"
      "v_perm_b32 v76, v101, v97, s63\n"
      "v_perm_b32 v80, v102, v98, s62\n"
      "v_perm_b32 v84, v102, v98, s63\n"
      "v_perm_b32 v88, v103, v99, s62\n"
      "v_perm_b32 v92, v103, v99, s63\n"
      "s_waitcnt vmcnt(8)\n"
      "s_nop 0\n"
      "s_mov_b32 s60, 939008\n"
      "buffer_load_dwordx4 v[140:143], v32, s[24:27], s60 offen nt\n"
      "s_waitcnt vmcnt(8)\n"
      "s_nop 0\n"
      "s_mov_b32 s60, 946176\n"
      "buffer_load_dwordx4 v[144:147], v32, s[24:27], s60 offen nt\n"
      "v_perm_b32 v65, v108, v104, s62\n"
      "v_perm_b32 v69, v108, v104, s63\n"
      "v_perm_b32 v73, v109, v105, s62\n"
      "v_perm_b32 v77, v109, v105, s63\n"
      "v_perm_b32 v81, v110, v106, s62\n"
      "v_perm_b32 v85, v110, v106, s63\n"
      "v_perm_b32 v89, v111, v107, s62\n"
      "v_perm_b32 v93, v111, v107, s63\n"
      "s_waitcnt vmcnt(8)\n"
      "s_nop 0\n"
      "s_mov_b32 s60, 953344\n"
      "buffer_load_dwordx4 v[148:151], v32, s[24:27], s60 offen nt\n"
      "s_waitcnt vmcnt(8)\n"
      "s_nop 0\n"
      "s_mov_b32 s60, 960512\n"
      "buffer_load_dwordx4 v[152:155], v32, s[24:27], s60 offen nt\n"
      "v_perm_b32 v66, v116, v112, s62\n"
      "v_perm_b32 v70, v116, v112, s63\n"
      "v_perm_b32 v74, v117, v113, s62\n"
      "v_perm_b32 v78, v117, v113, s63\n"
      "v_perm_b32 v82, v118, v114, s62\n"
      "v_perm_b32 v86, v118, v114, s63\n"
      "v_perm_b32 v90, v119, v115, s62\n"
      "v_perm_b32 v94, v119, v115, s63\n"
      "s_waitcnt vmcnt(8)\n"
      "s_nop 0\n"
      "s_mov_b32 s60, 967680\n"
      "buffer_load_dwordx4 v[156:159], v32, s[24:27], s60 offen nt\n"
      "s_waitcnt vmcnt(8)\n"
      "s_nop 0\n"
      "s_mov_b32 s60, 1376256\n"
      "buffer_load_dwordx4 v[160:163], v32, s[24:27], s60 offen nt\n"
      "v_perm_b32 v67, v124, v120, s62\n"
      "v_perm_b32 v71, v124, v120, s63\n"
      "v_perm_b32 v75, v125, v121, s62\n"
      "v_perm_b32 v79, v125, v121, s63\n"
      "v_perm_b32 v83, v126, v122, s62\n"
      "v_perm_b32 v87, v126, v122, s63\n"
      "v_perm_b32 v91, v127, v123, s62\n"
      "v_perm_b32 v95, v127, v123, s63\n"
      "s_waitcnt vmcnt(8)\n"
      "s_nop 0\n"
      "s_mov_b32 s60, 1383424\n"
      "buffer_load_dwordx4 v[164:167], v32, s[24:27], s60 offen nt\n"
      "s_waitcnt vmcnt(8)\n"
      "s_nop 0\n"
      "s_mov_b32 s60, 1390592\n"
      "buffer_load_dwordx4 v[168:171], v32, s[24:27], s60 offen nt\n"
      "v_perm_b32 v96, v132, v128, s62\n"
      "v_perm_b32 v100, v132, v128, s63\n"
      "v_perm_b32 v104, v133, v129, s62\n"
      "v_perm_b32 v108, v133, v129, s63\n"
      "v_perm_b32 v112, v134, v130, s62\n"
      "v_perm_b32 v116, v134, v130, s63\n"
      "v_perm_b32 v120, v135, v131, s62\n"
      "v_perm_b32 v124, v135, v131, s63\n"
      "s_waitcnt vmcnt(8)\n"
      "s_nop 0\n"
      "s_mov_b32 s60, 1397760\n"
      "buffer_load_dwordx4 v[172:175], v32, s[24:27], s60 offen nt\n"
      "s_waitcnt vmcnt(8)\n"
      "s_nop 0\n"
      "s_mov_b32 s60, 1404928\n"
      "buffer_load_dwordx4 v[176:179], v32, s[24:27], s60 offen nt\n"
      "v_perm_b32 v97, v140, v136, s62\n"
      "v_perm_b32 v101, v140, v136, s63\n"
      "v_perm_b32 v105, v141, v137, s62\n"
      "v_perm_b32 v109, v141, v137, s63\n"
      "v_perm_b32 v113, v142, v138, s62\n"
      "v_perm_b32 v117, v142, v138, s63\n"
      "v_perm_b32 v121, v143, v139, s62\n"
      "v_perm_b32 v125, v143, v139, s63\n"
      "s_waitcnt vmcnt(8)\n"
      "s_nop 0\n"
      "s_mov_b32 s60, 1412096\n"
      "buffer_load_dwordx4 v[180:183], v32, s[24:27], s60 offen nt\n"
      "s_waitcnt vmcnt(8)\n"
      "s_nop 0\n"
      "s_mov_b32 s60, 1419264\n"
      "buffer_load_dwordx4 v[184:187], v32, s[24:27], s60 offen nt\n"
      "v_perm_b32 v98, v148, v144, s62\n"
      "v_perm_b32 v102, v148, v144, s63\n"
      "v_perm_b32 v106, v149, v145, s62\n"
      "v_perm_b32 v110, v149, v145, s63\n"
      "v_perm_b32 v114, v150, v146, s62\n"
      "v_perm_b32 v118, v150, v146, s63\n"
      "v_perm_b32 v122, v151, v147, s62\n"
      "v_perm_b32 v126, v151, v147, s63\n"
      "s_waitcnt vmcnt(8)\n"
      "s_nop 0\n"
      "s_mov_b32 s60, 1426432\n"
      "buffer_load_dwordx4 v[188:191], v32, s[24:27], s60 offen nt\n"
      "ds_write_b32 v41, v47 offset:80\n"
      "s_waitcnt vmcnt(8)\n"
      "s_nop 0\n"
      "v_perm_b32 v99, v156, v152, s62\n"
      "v_perm_b32 v103, v156, v152, s63\n"
      "v_perm_b32 v107, v157, v153, s62\n"
      "v_perm_b32 v111, v157, v153, s63\n"
      "v_perm_b32 v115, v158, v154, s62\n"
      "v_perm_b32 v119, v158, v154, s63\n"
      "v_perm_b32 v123, v159, v155, s62\n"
      "v_perm_b32 v127, v159, v155, s63\n"
      "s_waitcnt vmcnt(7)\n"
      "s_nop 0\n"
      "s_memrealtime s[68:69]\n"
      "s_waitcnt vmcnt(6)\n"
      "s_nop 0\n"
      "s_waitcnt lgkmcnt(0)\n"
      "s_sub_u32 s68, s68, s12\n"
      "s_cmp_lt_u32 s68, s70\n"
      "s_cbranch_scc1 .Lgn25%=\n"
      "buffer_load_dwordx2 v[8:9], v33, s[16:19], s43 offen sc1\n"
      "s_mov_b32 s70, -1\n"
      ".Lgn25%=:\n"
      "s_memrealtime s[68:69]\n"
      "v_perm_b32 v128, v164, v160, s62\n"
      "v_perm_b32 v132, v164, v160, s63\n"
      "v_perm_b32 v136, v165, v161, s62\n"
      "v_perm_b32 v140, v165, v161, s63\n"
      "v_perm_b32 v144, v166, v162, s62\n"
      "v_perm_b32 v148, v166, v162, s63\n"
      "v_perm_b32 v152, v167, v163, s62\n"
      "v_perm_b32 v156, v167, v163, s63\n"
      "s_waitcnt vmcnt(5)\n"
      "s_nop 0\n"
      "s_waitcnt lgkmcnt(0)\n"
      "s_sub_u32 s68, s68, s12\n"
      "s_cmp_lt_u32 s68, s70\n"
      "s_cbranch_scc1 .Lgn26%=\n"
      "buffer_load_dwordx2 v[8:9], v33, s[16:19], s43 offen sc1\n"
      "s_mov_b32 s70, -1\n"
      ".Lgn26%=:\n"
      "s_memrealtime s[68:69]\n"
      "s_waitcnt vmcnt(4)\n"
      "s_nop 0\n"
      "s_waitcnt lgkmcnt(0)\n"
      "s_sub_u32 s68, s68, s12\n"
      "s_cmp_lt_u32 s68, s70\n"
      "s_cbranch_scc1 .Lgn27%=\n"
      "buffer_load_dwordx2 v[8:9], v33, s[16:19], s43 offen sc1\n"
      "s_mov_b32 s70, -1\n"
      ".Lgn27%=:\n"
      "s_memrealtime s[68:69]\n"
      "v_perm_b32 v129, v172, v168, s62\n"
      "v_perm_b32 v133, v172, v168, s63\n"
      "v_perm_b32 v137, v173, v169, s62\n"
      "v_perm_b32 v141, v173, v169, s63\n"
      "v_perm_b32 v145, v174, v170, s62\n"
      "v_perm_b32 v149, v174, v170, s63\n"
      "v_perm_b32 v153, v175, v171, s62\n"
      "v_perm_b32 v157, v175, v171, s63\n"
      "s_waitcnt vmcnt(3)\n"
      "s_nop 0\n"
      "s_waitcnt lgkmcnt(0)\n"
      "s_sub_u32 s68, s68, s12\n"
      "s_cmp_lt_u32 s68, s70\n"
      "s_cbranch_scc1 .Lgn28%=\n"
      "buffer_load_dwordx2 v[8:9], v33, s[16:19], s43 offen sc1\n"
      "s_mov_b32 s70, -1\n"
      ".Lgn28%=:\n"
      "s_memrealtime s[68:69]\n"
      "s_waitcnt vmcnt(2)\n"
      "s_nop 0\n"
      "s_waitcnt lgkmcnt(0)\n"
      "s_sub_u32 s68, s68, s12\n"
      "s_cmp_lt_u32 s68, s70\n"
      "s_cbranch_scc1 .Lgn29%=\n"
      "buffer_load_dwordx2 v[8:9], v33, s[16:19], s43 offen sc1\n"
      "s_mov_b32 s70, -1\n"
      ".Lgn29%=:\n"
      "s_memrealtime s[68:69]\n"
      "v_perm_b32 v130, v180, v176, s62\n"
      "v_perm_b32 v134, v180, v176, s63\n"
      "v_perm_b32 v138, v181, v177, s62\n"
      "v_perm_b32 v142, v181, v177, s63\n"
      "v_perm_b32 v146, v182, v178, s62\n"
      "v_perm_b32 v150, v182, v178, s63\n"
      "v_perm_b32 v154, v183, v179, s62\n"
      "v_perm_b32 v158, v183, v179, s63\n"
      "s_waitcnt vmcnt(1)\n"
      "s_nop 0\n"
      "s_waitcnt lgkmcnt(0)\n"
      "s_sub_u32 s68, s68, s12\n"
      "s_cmp_lt_u32 s68, s70\n"
      "s_cbranch_scc1 .Lgn30%=\n"
      "buffer_load_dwordx2 v[8:9], v33, s[16:19], s43 offen sc1\n"
      "s_mov_b32 s70, -1\n"
      ".Lgn30%=:\n"
      "s_memrealtime s[68:69]\n"
      "s_waitcnt vmcnt(0)\n"
      "s_nop 0\n"
      "s_waitcnt lgkmcnt(0)\n"
      "s_sub_u32 s68, s68, s12\n"
      "s_cmp_lt_u32 s68, s70\n"
      "s_cbranch_scc1 .Lgn31%=\n"
      "buffer_load_dwordx2 v[8:9], v33, s[16:19], s43 offen sc1\n"
      "s_mov_b32 s70, -1\n"
      ".Lgn31%=:\n"
      "v_perm_b32 v131, v188, v184, s62\n"
      "v_perm_b32 v135, v188, v184, s63\n"
      "v_perm_b32 v139, v189, v185, s62\n"
      "v_perm_b32 v143, v189, v185, s63\n"
      "v_perm_b32 v147, v190, v186, s62\n"
      "v_perm_b32 v151, v190, v186, s63\n"
      "v_perm_b32 v155, v191, v187, s62\n"
      "v_perm_b32 v159, v191, v187, s63\n"
      "s_movk_i32 s59, 0x3fff\n"
      "s_cmp_eq_u32 s70, -1\n"
      "s_cbranch_scc1 .Lghc%=\n"
      ".Lgpc3_%=:\n"
      "s_memrealtime s[68:69]\n"
      "s_waitcnt lgkmcnt(0)\n"
      "s_sub_u32 s68, s68, s12\n"
      "s_cmp_ge_u32 s68, 435\n"
      "s_cbranch_scc1 .Lgpd3_%=\n"
      "s_sleep 1\n"
      "s_branch .Lgpc3_%=\n"
      ".Lgpd3_%=:\n"
      ".Lgh%=:\n"
      "buffer_load_dwordx2 v[8:9], v33, s[16:19], s43 offen sc1\n"
      ".Lghc%=:\n"
      "s_waitcnt vmcnt(0)\n"
      "v_cmp_ne_u32 vcc, s15, v9\n"
      "v_bitop3_b32 v10, v8, s61, v8 bitop3:0x0c\n"
      "s_sub_u32 s59, s59, 1\n"
      "s_cbranch_vccz .Lgho%=\n"
      "s_sleep 2\n"
      "s_cbranch_scc0 .Lgh%=\n"
      ".Lgho%=:\n"
      "v_lshrrev_b32 v45, 9, v10\n"
      "v_lshl_add_u32 v11, v10, 11, v36\n"
      "v_mul_u32_u24 v45, 0xf000, v45\n"
      "v_lshl_add_u32 v45, v10, 3, v45\n"
      "buffer_load_dwordx2 v[12:13], off, s[16:19], s43 offset:64 sc1\n"
      "buffer_load_dwordx2 v[62:63], v45, s[16:19], s58 offen sc1\n"
      "buffer_load_dwordx4 v[16:19], v11, s[20:23], 0 offen offset:0\n"
      "buffer_load_dwordx4 v[20:23], v11, s[20:23], 0 offen offset:128\n"
      "buffer_load_dwordx4 v[24:27], v11, s[20:23], 0 offen offset:256\n"
      "buffer_load_dwordx4 v[28:31], v11, s[20:23], 0 offen offset:384\n"
      "ds_write_b32 v41, v47 offset:64\n"
      ".Lghx%=:\n"
      "s_movk_i32 s59, 0x7fff\n"
      "s_waitcnt vmcnt(4)\n"
      "s_nop 0\n"
      ".Lgs%=:\n"
      "v_cmp_ne_u32 vcc, s15, v63\n"
      "v_lshlrev_b32 v10, 16, v62\n"
      "v_and_b32 v44, 0xffff0000, v62\n"
      "s_cbranch_vccnz .Lgss%=\n"
      ".Lgso%=:\n"
      "v_rcp_f32 v44, v44\n"
      "v_xor_b32 v10, 0x80000000, v10\n"
      "v_readfirstlane_b32 s68, v13\n"
      "v_readfirstlane_b32 s69, v12\n"
      "s_cmp_eq_u32 s68, s15\n"
      "s_cselect_b32 s12, s69, s12\n"
      "v_mov_b32 v12, s12\n"
      "ds_write_b32 v42, v12 offset:128\n"
      "s_waitcnt vmcnt(3)\n"
      "v_dot2_f32_bf16 v168, v16, s56, v10\n"
      "v_dot2_f32_bf16 v169, v16, s57, v10\n"
      "v_dot2_f32_bf16 v170, v17, s56, v10\n"
      "v_dot2_f32_bf16 v171, v17, s57, v10\n"
      "v_dot2_f32_bf16 v172, v18, s56, v10\n"
      "v_dot2_f32_bf16 v173, v18, s57, v10\n"
      "v_dot2_f32_bf16 v174, v19, s56, v10\n"
      "v_dot2_f32_bf16 v175, v19, s57, v10\n"
      "v_cvt_pk_bf16_f32 v16, v168, v169\n"
      "v_cvt_pk_bf16_f32 v17, v170, v171\n"
      "v_cvt_pk_bf16_f32 v18, v172, v173\n"
      "v_cvt_pk_bf16_f32 v19, v174, v175\n"
      "ds_bpermute_b32 v44, v40, v44\n"
      "s_waitcnt vmcnt(2)\n"
      "v_dot2_f32_bf16 v176, v20, s56, v10\n"
      "v_dot2_f32_bf16 v177, v20, s57, v10\n"
      "v_dot2_f32_bf16 v178, v21, s56, v10\n"
      "v_dot2_f32_bf16 v179, v21, s57, v10\n"
      "v_dot2_f32_bf16 v180, v22, s56, v10\n"
      "v_dot2_f32_bf16 v181, v22, s57, v10\n"
      "v_dot2_f32_bf16 v182, v23, s56, v10\n"
      "v_dot2_f32_bf16 v183, v23, s57, v10\n"
      "v_cvt_pk_bf16_f32 v20, v176, v177\n"
      "v_cvt_pk_bf16_f32 v21, v178, v179\n"
      "v_cvt_pk_bf16_f32 v22, v180, v181\n"
      "v_cvt_pk_bf16_f32 v23, v182, v183\n"
      "s_waitcnt vmcnt(1)\n"
      "v_dot2_f32_bf16 v168, v24, s56, v10\n"
      "v_dot2_f32_bf16 v169, v24, s57, v10\n"
      "v_dot2_f32_bf16 v170, v25, s56, v10\n"
      "v_dot2_f32_bf16 v171, v25, s57, v10\n"
      "v_dot2_f32_bf16 v172, v26, s56, v10\n"
      "v_dot2_f32_bf16 v173, v26, s57, v10\n"
      "v_dot2_f32_bf16 v174, v27, s56, v10\n"
      "v_dot2_f32_bf16 v175, v27, s57, v10\n"
      "v_cvt_pk_bf16_f32 v24, v168, v169\n"
      "v_cvt_pk_bf16_f32 v25, v170, v171\n"
      "v_cvt_pk_bf16_f32 v26, v172, v173\n"
      "v_cvt_pk_bf16_f32 v27, v174, v175\n"
      "s_waitcnt vmcnt(0)\n"
      "v_dot2_f32_bf16 v176, v28, s56, v10\n"
      "v_dot2_f32_bf16 v177, v28, s57, v10\n"
      "v_dot2_f32_bf16 v178, v29, s56, v10\n"
      "v_dot2_f32_bf16 v179, v29, s57, v10\n"
      "v_dot2_f32_bf16 v180, v30, s56, v10\n"
      "v_dot2_f32_bf16 v181, v30, s57, v10\n"
      "v_dot2_f32_bf16 v182, v31, s56, v10\n"
      "v_dot2_f32_bf16 v183, v31, s57, v10\n"
      "v_cvt_pk_bf16_f32 v28, v176, v177\n"
      "v_cvt_pk_bf16_f32 v29, v178, v179\n"
      "v_cvt_pk_bf16_f32 v30, v180, v181\n"
      "v_cvt_pk_bf16_f32 v31, v182, v183\n"
      "v_mfma_f32_16x16x32_bf16 v[224:227], v[16:19], v[192:195], 0\n"
      "v_mfma_f32_16x16x32_bf16 v[228:231], v[16:19], v[196:199], 0\n"
      "v_mfma_f32_16x16x32_bf16 v[232:235], v[16:19], v[200:203], 0\n"
      "v_mfma_f32_16x16x32_bf16 v[236:239], v[16:19], v[204:207], 0\n"
      "v_mfma_f32_16x16x32_bf16 v[240:243], v[16:19], v[208:211], 0\n"
      "v_mfma_f32_16x16x32_bf16 v[244:247], v[16:19], v[212:215], 0\n"
      "v_mfma_f32_16x16x32_bf16 v[248:251], v[16:19], v[216:219], 0\n"
      "v_mfma_f32_16x16x32_bf16 v[252:255], v[16:19], v[220:223], 0\n"
      "v_mfma_f32_16x16x32_bf16 v[224:227], v[20:23], v[64:67], v[224:227]\n"
      "v_mfma_f32_16x16x32_bf16 v[228:231], v[20:23], v[68:71], v[228:231]\n"
      "v_mfma_f32_16x16x32_bf16 v[232:235], v[20:23], v[72:75], v[232:235]\n"
      "v_mfma_f32_16x16x32_bf16 v[236:239], v[20:23], v[76:79], v[236:239]\n"
      "v_mfma_f32_16x16x32_bf16 v[240:243], v[20:23], v[80:83], v[240:243]\n"
      "v_mfma_f32_16x16x32_bf16 v[244:247], v[20:23], v[84:87], v[244:247]\n"
      "v_mfma_f32_16x16x32_bf16 v[248:251], v[20:23], v[88:91], v[248:251]\n"
      "v_mfma_f32_16x16x32_bf16 v[252:255], v[20:23], v[92:95], v[252:255]\n"
      "v_mfma_f32_16x16x32_bf16 v[224:227], v[24:27], v[96:99], v[224:227]\n"
      "v_mfma_f32_16x16x32_bf16 v[228:231], v[24:27], v[100:103], v[228:231]\n"
      "v_mfma_f32_16x16x32_bf16 v[232:235], v[24:27], v[104:107], v[232:235]\n"
      "v_mfma_f32_16x16x32_bf16 v[236:239], v[24:27], v[108:111], v[236:239]\n"
      "v_mfma_f32_16x16x32_bf16 v[240:243], v[24:27], v[112:115], v[240:243]\n"
      "v_mfma_f32_16x16x32_bf16 v[244:247], v[24:27], v[116:119], v[244:247]\n"
      "v_mfma_f32_16x16x32_bf16 v[248:251], v[24:27], v[120:123], v[248:251]\n"
      "v_mfma_f32_16x16x32_bf16 v[252:255], v[24:27], v[124:127], v[252:255]\n"
      "v_mfma_f32_16x16x32_bf16 v[224:227], v[28:31], v[128:131], v[224:227]\n"
      "v_mfma_f32_16x16x32_bf16 v[228:231], v[28:31], v[132:135], v[228:231]\n"
      "v_mfma_f32_16x16x32_bf16 v[232:235], v[28:31], v[136:139], v[232:235]\n"
      "v_mfma_f32_16x16x32_bf16 v[236:239], v[28:31], v[140:143], v[236:239]\n"
      "s_nop 4\n"
      "v_add_f32_dpp v160, v226, v224 row_ror:8 row_mask:0xf bank_mask:0x3\n"
      "v_add_f32_dpp v164, v227, v225 row_ror:8 row_mask:0xf bank_mask:0x3\n"
      "v_mfma_f32_16x16x32_bf16 v[240:243], v[28:31], v[144:147], v[240:243]\n"
      "v_add_f32_dpp v161, v230, v228 row_ror:8 row_mask:0xf bank_mask:0x3\n"
      "v_add_f32_dpp v165, v231, v229 row_ror:8 row_mask:0xf bank_mask:0x3\n"
      "v_mfma_f32_16x16x32_bf16 v[244:247], v[28:31], v[148:151], v[244:247]\n"
      "v_add_f32_dpp v162, v234, v232 row_ror:8 row_mask:0xf bank_mask:0x3\n"
      "v_add_f32_dpp v166, v235, v233 row_ror:8 row_mask:0xf bank_mask:0x3\n"
      "v_mfma_f32_16x16x32_bf16 v[248:251], v[28:31], v[152:155], v[248:251]\n"
      "v_add_f32_dpp v163, v238, v236 row_ror:8 row_mask:0xf bank_mask:0x3\n"
      "v_add_f32_dpp v167, v239, v237 row_ror:8 row_mask:0xf bank_mask:0x3\n"
      "v_mfma_f32_16x16x32_bf16 v[252:255], v[28:31], v[156:159], v[252:255]\n"
      "v_add_f32_dpp v160, v240, v242 row_ror:8 row_mask:0xf bank_mask:0xc\n"
      "v_add_f32_dpp v164, v241, v243 row_ror:8 row_mask:0xf bank_mask:0xc\n"
      "v_add_f32_dpp v161, v244, v246 row_ror:8 row_mask:0xf bank_mask:0xc\n"
      "v_add_f32_dpp v165, v245, v247 row_ror:8 row_mask:0xf bank_mask:0xc\n"
      "s_nop 0\n"
      "v_add_f32_dpp v162, v248, v250 row_ror:8 row_mask:0xf bank_mask:0xc\n"
      "v_add_f32_dpp v166, v249, v251 row_ror:8 row_mask:0xf bank_mask:0xc\n"
      "s_nop 0\n"
      "v_add_f32_dpp v163, v252, v254 row_ror:8 row_mask:0xf bank_mask:0xc\n"
      "v_add_f32_dpp v167, v253, v255 row_ror:8 row_mask:0xf bank_mask:0xc\n"
      "ds_write_b128 v34, v[160:163] \n"
      "ds_write_b128 v34, v[164:167] offset:256\n"
      "ds_write_b32 v41, v47 \n"
      "s_movk_i32 s59, 0x7fff\n"
      ".Lgw%=:\n"
      "ds_read_b128 v[72:75], v42 \n"
      "ds_read_b64 v[64:65], v35 offset:0\n"
      "ds_read_b64 v[66:67], v35 offset:2048\n"
      "ds_read_b64 v[68:69], v35 offset:4096\n"
      "ds_read_b64 v[70:71], v35 offset:6144\n"
      "s_sub_u32 s59, s59, 1\n"
      "s_cbranch_scc1 .Lgwo%=\n"
      "s_waitcnt lgkmcnt(4)\n"
      "v_xor_b32 v72, s15, v72\n"
      "v_bitop3_b32 v72, s15, v73, v72 bitop3:0xbe\n"
      "v_bitop3_b32 v72, s15, v74, v72 bitop3:0xbe\n"
      "v_bitop3_b32 v72, s15, v75, v72 bitop3:0xbe\n"
      "v_cmp_ne_u32 vcc, 0, v72\n"
      "s_waitcnt lgkmcnt(0)\n"
      "s_nop 0\n"
      "s_cbranch_vccnz .Lgw%=\n"
      ".Lgwo%=:\n"
      "s_waitcnt lgkmcnt(0)\n"
      "s_bitcmp1_b32 s49, 0\n"
      "v_pk_add_f32 v[64:65], v[64:65], v[66:67]\n"
      "v_pk_add_f32 v[68:69], v[68:69], v[70:71]\n"
      "v_pk_add_f32 v[64:65], v[64:65], v[68:69]\n"
      "v_mul_f32 v64, v64, v44\n"
      "v_mul_f32 v65, v65, v44\n"
      "v_cvt_pk_bf16_f32 v46, v64, v65\n"
      "s_cbranch_scc1 .Lgu%=\n"
      "v_lshlrev_b32 v64, 16, v46\n"
      "v_and_b32 v65, 0xffff0000, v46\n"
      "v_mul_f32 v66, 0xbfb8aa3b, v64\n"
      "v_mul_f32 v67, 0xbfb8aa3b, v65\n"
      "v_exp_f32 v66, v66\n"
      "v_exp_f32 v67, v67\n"
      "v_add_f32 v66, 1.0, v66\n"
      "v_add_f32 v67, 1.0, v67\n"
      "v_rcp_f32 v66, v66\n"
      "v_rcp_f32 v67, v67\n"
      "v_mul_f32 v64, v64, v66\n"
      "v_mul_f32 v65, v65, v67\n"
      "v_cvt_pk_bf16_f32 v46, v64, v65\n"
      ".Lgu%=:\n"
      "buffer_store_dwordx2 v[46:47], v37, s[16:19], s45 offen sc1\n"
      "s_cmp_lg_u32 s33, 4\n"
      "s_cbranch_scc1 .Lga%=\n"
      "v_mov_b32 v64, s15\n"
      "v_mov_b32 v65, s15\n"
      "buffer_store_dwordx2 v[64:65], off, s[16:19], s42 sc1\n"
      ".Lga%=:\n"
      "s_cmp_eq_u32 s33, 7\n"
      "s_mov_b32 s24, s36\n"
      "s_mov_b32 s25, s37\n"
      "s_mov_b32 s26, 0x400000\n"
      "s_cbranch_scc1 .LZ%=\n"
      "s_endpgm\n"
      ".Lgss%=:\n"
      "s_sleep 1\n"
      "s_sub_u32 s59, s59, 1\n"
      "s_cbranch_scc1 .Lgso%=\n"
      "buffer_load_dwordx2 v[62:63], v45, s[16:19], s58 offen sc1\n"
      "s_waitcnt vmcnt(0)\n"
      "s_branch .Lgs%=\n"
      ".LD2%=:\n"
      "s_setprio 0\n"
      "s_mul_i32 s35, s48, 14\n"
      "s_add_u32 s35, s35, s49\n"
      "s_lshr_b32 s52, s35, 2\n"
      "s_and_b32 s54, s35, 3\n"
      "v_lshlrev_b32 v42, 4, v49\n"
      "s_lshl_b32 s50, s52, 18\n"
      "s_lshl_b32 s51, s54, 9\n"
      "s_add_u32 s50, s50, s51\n"
      "s_lshl_b32 s51, s33, 7\n"
      "s_add_u32 s50, s50, s51\n"
      "v_lshl_add_u32 v32, v50, 14, v42\n"
      "v_add_u32 v32, s50, v32\n"
      "v_lshrrev_b32 v41, 1, v49\n"
      "v_and_b32 v43, 1, v41\n"
      "v_mul_u32_u24 v36, 0x120, v41\n"
      "v_lshlrev_b32 v43, 4, v43\n"
      "v_sub_u32 v36, v36, v43\n"
      "v_and_b32 v43, 1, v49\n"
      "v_lshl_add_u32 v36, v43, 7, v36\n"
      "v_lshrrev_b32 v41, 4, v48\n"
      "v_lshl_add_u32 v36, v41, 4, v36\n"
      "v_and_b32 v43, 8, v48\n"
      "v_add_u32 v36, v36, v43\n"
      "s_lshr_b32 s53, s33, 1\n"
      "s_mul_i32 s50, s53, 1152\n"
      "s_and_b32 s55, s33, 1\n"
      "s_lshl_b32 s51, s55, 6\n"
      "s_add_u32 s50, s50, s51\n"
      "s_add_u32 s50, s50, 149504\n"
      "v_add_u32 v36, s50, v36\n"
      "v_and_b32 v43, 1, v41\n"
      "v_mul_u32_u24 v37, 0x120, v41\n"
      "v_lshlrev_b32 v43, 4, v43\n"
      "v_sub_u32 v37, v37, v43\n"
      "v_and_b32 v43, 15, v48\n"
      "v_lshl_add_u32 v37, v43, 4, v37\n"
      "v_add_u32 v37, 0x24800, v37\n"
      "v_lshlrev_b32 v35, 4, v48\n"
      "s_lshl_b32 s46, s52, 17\n"
      "s_lshl_b32 s50, s53, 16\n"
      "s_add_u32 s46, s46, s50\n"
      "s_lshl_b32 s50, s55, 10\n"
      "s_add_u32 s46, s46, s50\n"
      "s_add_u32 s46, s46, 524288\n"
      "v_and_b32 v41, 3, v50\n"
      "v_lshrrev_b32 v43, 5, v48\n"
      "v_lshl_add_u32 v41, v41, 1, v43\n"
      "v_mul_u32_u24 v38, 0x1c00, v41\n"
      "v_add_u32 v38, v38, v42\n"
      "s_lshl_b32 s50, s54, 2\n"
      "s_add_u32 s50, s50, s33\n"
      "s_mul_i32 s47, s50, 57344\n"
      "s_lshl_b32 s50, s52, 8\n"
      "s_add_u32 s47, s47, s50\n"
      "s_add_u32 s47, s47, 4194304\n"
      "s_mov_b32 s62, 0x05040100\n"
      "s_mov_b32 s63, 0x07060302\n"
      "s_lshl_b32 s50, s33, 2\n"
      "s_add_u32 s50, s50, 140864\n"
      "v_mov_b32 v41, s50\n"
      "v_mov_b32 v42, 0x22640\n"
      "s_movk_i32 s59, 0x3fff\n"
      ".Ldf%=:\n"
      "s_sleep 1\n"
      "ds_read_b128 v[240:243], v42\n"
      "ds_read_b32 v43, v41 offset:16\n"
      "s_sub_u32 s59, s59, 1\n"
      "s_cbranch_scc1 .Ldfo%=\n"
      "s_waitcnt lgkmcnt(0)\n"
      "v_xor_b32 v240, s15, v240\n"
      "v_bitop3_b32 v240, s15, v241, v240 bitop3:0xbe\n"
      "v_bitop3_b32 v240, s15, v242, v240 bitop3:0xbe\n"
      "v_bitop3_b32 v240, s15, v243, v240 bitop3:0xbe\n"
      "v_bitop3_b32 v240, s15, v43, v240 bitop3:0xbe\n"
      "v_cmp_ne_u32 vcc, 0, v240\n"
      "s_nop 1\n"
      "s_cbranch_vccnz .Ldf%=\n"
      ".Ldfo%=:\n"
      "s_mov_b32 s60, 0\n"
      "buffer_load_dwordx4 v[128:131], v32, s[28:31], s60 offen nt\n"
      "s_mov_b32 s60, 2048\n"
      "buffer_load_dwordx4 v[132:135], v32, s[28:31], s60 offen nt\n"
      "s_mov_b32 s60, 4096\n"
      "buffer_load_dwordx4 v[136:139], v32, s[28:31], s60 offen nt\n"
      "s_mov_b32 s60, 6144\n"
      "buffer_load_dwordx4 v[140:143], v32, s[28:31], s60 offen nt\n"
      "s_mov_b32 s60, 8192\n"
      "buffer_load_dwordx4 v[144:147], v32, s[28:31], s60 offen nt\n"
      "s_mov_b32 s60, 10240\n"
      "buffer_load_dwordx4 v[148:151], v32, s[28:31], s60 offen nt\n"
      "s_mov_b32 s60, 12288\n"
      "buffer_load_dwordx4 v[152:155], v32, s[28:31], s60 offen nt\n"
      "s_mov_b32 s60, 14336\n"
      "buffer_load_dwordx4 v[156:159], v32, s[28:31], s60 offen nt\n"
      "s_waitcnt vmcnt(7)\n"
      "s_nop 0\n"
      "s_mov_b32 s60, 131072\n"
      "buffer_load_dwordx4 v[160:163], v32, s[28:31], s60 offen nt\n"
      "s_waitcnt vmcnt(7)\n"
      "s_nop 0\n"
      "s_mov_b32 s60, 133120\n"
      "buffer_load_dwordx4 v[164:167], v32, s[28:31], s60 offen nt\n"
      "v_perm_b32 v64, v132, v128, s62\n"
      "v_perm_b32 v68, v132, v128, s63\n"
      "v_perm_b32 v72, v133, v129, s62\n"
      "v_perm_b32 v76, v133, v129, s63\n"
      "v_perm_b32 v80, v134, v130, s62\n"
      "v_perm_b32 v84, v134, v130, s63\n"
      "v_perm_b32 v88, v135, v131, s62\n"
      "v_perm_b32 v92, v135, v131, s63\n"
      "s_waitcnt vmcnt(7)\n"
      "s_nop 0\n"
      "s_mov_b32 s60, 135168\n"
      "buffer_load_dwordx4 v[168:171], v32, s[28:31], s60 offen nt\n"
      "s_waitcnt vmcnt(7)\n"
      "s_nop 0\n"
      "s_mov_b32 s60, 137216\n"
      "buffer_load_dwordx4 v[172:175], v32, s[28:31], s60 offen nt\n"
      "v_perm_b32 v65, v140, v136, s62\n"
      "v_perm_b32 v69, v140, v136, s63\n"
      "v_perm_b32 v73, v141, v137, s62\n"
      "v_perm_b32 v77, v141, v137, s63\n"
      "v_perm_b32 v81, v142, v138, s62\n"
      "v_perm_b32 v85, v142, v138, s63\n"
      "v_perm_b32 v89, v143, v139, s62\n"
      "v_perm_b32 v93, v143, v139, s63\n"
      "s_waitcnt vmcnt(7)\n"
      "s_nop 0\n"
      "s_mov_b32 s60, 139264\n"
      "buffer_load_dwordx4 v[176:179], v32, s[28:31], s60 offen nt\n"
      "s_waitcnt vmcnt(7)\n"
      "s_nop 0\n"
      "s_mov_b32 s60, 141312\n"
      "buffer_load_dwordx4 v[180:183], v32, s[28:31], s60 offen nt\n"
      "v_perm_b32 v66, v148, v144, s62\n"
      "v_perm_b32 v70, v148, v144, s63\n"
      "v_perm_b32 v74, v149, v145, s62\n"
      "v_perm_b32 v78, v149, v145, s63\n"
      "v_perm_b32 v82, v150, v146, s62\n"
      "v_perm_b32 v86, v150, v146, s63\n"
      "v_perm_b32 v90, v151, v147, s62\n"
      "v_perm_b32 v94, v151, v147, s63\n"
      "s_waitcnt vmcnt(7)\n"
      "s_nop 0\n"
      "s_mov_b32 s60, 143360\n"
      "buffer_load_dwordx4 v[184:187], v32, s[28:31], s60 offen nt\n"
      "s_waitcnt vmcnt(7)\n"
      "s_nop 0\n"
      "s_mov_b32 s60, 145408\n"
      "buffer_load_dwordx4 v[188:191], v32, s[28:31], s60 offen nt\n"
      "v_perm_b32 v67, v156, v152, s62\n"
      "v_perm_b32 v71, v156, v152, s63\n"
      "v_perm_b32 v75, v157, v153, s62\n"
      "v_perm_b32 v79, v157, v153, s63\n"
      "v_perm_b32 v83, v158, v154, s62\n"
      "v_perm_b32 v87, v158, v154, s63\n"
      "v_perm_b32 v91, v159, v155, s62\n"
      "v_perm_b32 v95, v159, v155, s63\n"
      "s_waitcnt vmcnt(7)\n"
      "s_nop 0\n"
      "s_movk_i32 s59, 0x3fff\n"
      "v_add_u32 v41, 0xffffffc0, v41\n"
      ".Lde%=:\n"
      "ds_read_b32 v43, v41\n"
      "s_sub_u32 s59, s59, 1\n"
      "s_cbranch_scc1 .Ldeo%=\n"
      "s_waitcnt lgkmcnt(0)\n"
      "v_cmp_ne_u32 vcc, s15, v43\n"
      "s_nop 1\n"
      "s_cbranch_vccz .Ldeo%=\n"
      "s_sleep 2\n"
      "s_branch .Lde%=\n"
      ".Ldeo%=:\n"
      "s_cmp_lg_u32 s59, 0x3ffe\n"
      "s_cbranch_scc0 .Ldep%=\n"
      "s_sleep 6\n"
      ".Ldep%=:\n"
      "ds_read_b32 v43, v42 offset:64\n"
      "s_waitcnt lgkmcnt(0)\n"
      "v_readfirstlane_b32 s12, v43\n"
      "s_nop 0\n"
      ".Ldpc1_%=:\n"
      "s_memrealtime s[64:65]\n"
      "s_waitcnt lgkmcnt(0)\n"
      "s_sub_u32 s64, s64, s12\n"
      "s_cmp_ge_u32 s64, 640\n"
      "s_cbranch_scc1 .Ldpd1_%=\n"
      "s_sleep 1\n"
      "s_branch .Ldpc1_%=\n"
      ".Ldpd1_%=:\n"
      "buffer_load_dwordx4 v[224:227], v35, s[16:19], s46 offen sc1\n"
      "buffer_load_dwordx4 v[228:231], v35, s[16:19], s46 offen offset:2048 sc1\n"
      "s_waitcnt vmcnt(8)\n"
      "s_nop 0\n"
      "v_perm_b32 v96, v164, v160, s62\n"
      "v_perm_b32 v100, v164, v160, s63\n"
      "v_perm_b32 v104, v165, v161, s62\n"
      "v_perm_b32 v108, v165, v161, s63\n"
      "v_perm_b32 v112, v166, v162, s62\n"
      "v_perm_b32 v116, v166, v162, s63\n"
      "v_perm_b32 v120, v167, v163, s62\n"
      "v_perm_b32 v124, v167, v163, s63\n"
      "s_waitcnt vmcnt(7)\n"
      "s_nop 0\n"
      "s_waitcnt vmcnt(6)\n"
      "s_nop 0\n"
      "v_perm_b32 v97, v172, v168, s62\n"
      "v_perm_b32 v101, v172, v168, s63\n"
      "v_perm_b32 v105, v173, v169, s62\n"
      "v_perm_b32 v109, v173, v169, s63\n"
      "v_perm_b32 v113, v174, v170, s62\n"
      "v_perm_b32 v117, v174, v170, s63\n"
      "v_perm_b32 v121, v175, v171, s62\n"
      "v_perm_b32 v125, v175, v171, s63\n"
      "s_waitcnt vmcnt(5)\n"
      "s_nop 0\n"
      "s_waitcnt vmcnt(4)\n"
      "s_nop 0\n"
      "v_perm_b32 v98, v180, v176, s62\n"
      "v_perm_b32 v102, v180, v176, s63\n"
      "v_perm_b32 v106, v181, v177, s62\n"
      "v_perm_b32 v110, v181, v177, s63\n"
      "v_perm_b32 v114, v182, v178, s62\n"
      "v_perm_b32 v118, v182, v178, s63\n"
      "v_perm_b32 v122, v183, v179, s62\n"
      "v_perm_b32 v126, v183, v179, s63\n"
      "s_waitcnt vmcnt(3)\n"
      "s_nop 0\n"
      "s_waitcnt vmcnt(2)\n"
      "s_nop 0\n"
      "v_perm_b32 v99, v188, v184, s62\n"
      "v_perm_b32 v103, v188, v184, s63\n"
      "v_perm_b32 v107, v189, v185, s62\n"
      "v_perm_b32 v111, v189, v185, s63\n"
      "v_perm_b32 v115, v190, v186, s62\n"
      "v_perm_b32 v119, v190, v186, s63\n"
      "v_perm_b32 v123, v191, v187, s62\n"
      "v_perm_b32 v127, v191, v187, s63\n"
      "v_mov_b32 v137, s15\n"
      "v_mov_b32 v139, s15\n"
      "v_mov_b32 v141, s15\n"
      "v_mov_b32 v143, s15\n"
      "s_movk_i32 s59, 0x7fff\n"
      "s_waitcnt vmcnt(0)\n"
      "s_nop 0\n"
      ".Ldg%=:\n"
      "v_xor_b32 v232, s15, v225\n"
      "v_bitop3_b32 v232, s15, v227, v232 bitop3:0xbe\n"
      "v_bitop3_b32 v232, s15, v229, v232 bitop3:0xbe\n"
      "v_bitop3_b32 v232, s15, v231, v232 bitop3:0xbe\n"
      "v_cmp_ne_u32 vcc, 0, v232\n"
      "v_lshlrev_b32 v232, 16, v224\n"
      "v_and_b32 v233, 0xffff0000, v224\n"
      "v_lshlrev_b32 v234, 16, v226\n"
      "v_and_b32 v235, 0xffff0000, v226\n"
      "s_cbranch_vccnz .Ldgs%=\n"
      ".Ldgo%=:\n"
      "v_lshlrev_b32 v236, 16, v228\n"
      "v_and_b32 v237, 0xffff0000, v228\n"
      "v_lshlrev_b32 v238, 16, v230\n"
      "v_and_b32 v239, 0xffff0000, v230\n"
      "v_pk_mul_f32 v[232:233], v[232:233], v[236:237]\n"
      "v_pk_mul_f32 v[234:235], v[234:235], v[238:239]\n"
      "v_cvt_pk_bf16_f32 v232, v232, v233\n"
      "v_cvt_pk_bf16_f32 v233, v234, v235\n"
      "ds_write_b64 v36, v[232:233] \n"
      "ds_write_b32 v41, v137 offset:96\n"
      "ds_read_b128 v[240:243], v42 offset:32\n"
      "ds_read_b128 v[224:227], v37 \n"
      "ds_read_b128 v[228:231], v37 offset:1152\n"
      "s_movk_i32 s59, 0x7fff\n"
      ".Ldv%=:\n"
      "s_waitcnt lgkmcnt(2)\n"
      "v_xor_b32 v240, s15, v240\n"
      "v_bitop3_b32 v240, s15, v241, v240 bitop3:0xbe\n"
      "v_bitop3_b32 v240, s15, v242, v240 bitop3:0xbe\n"
      "v_bitop3_b32 v240, s15, v243, v240 bitop3:0xbe\n"
      "v_cmp_ne_u32 vcc, 0, v240\n"
      "s_nop 1\n"
      "s_cbranch_vccnz .Ldvs%=\n"
      ".Ldvo%=:\n"
      "s_waitcnt lgkmcnt(1)\n"
      "v_mfma_f32_16x16x32_bf16 v[192:195], v[64:67], v[224:227], 0\n"
      "s_waitcnt lgkmcnt(0)\n"
      "v_mfma_f32_16x16x32_bf16 v[192:195], v[96:99], v[228:231], v[192:195]\n"
      "v_mfma_f32_16x16x32_bf16 v[196:199], v[68:71], v[224:227], 0\n"
      "v_mfma_f32_16x16x32_bf16 v[196:199], v[100:103], v[228:231], v[196:199]\n"
      "v_mfma_f32_16x16x32_bf16 v[200:203], v[72:75], v[224:227], 0\n"
      "s_nop 4\n"
      "v_permlane32_swap_b32 v192, v193\n"
      "v_permlane32_swap_b32 v194, v195\n"
      "v_mfma_f32_16x16x32_bf16 v[200:203], v[104:107], v[228:231], v[200:203]\n"
      "v_mfma_f32_16x16x32_bf16 v[204:207], v[76:79], v[224:227], 0\n"
      "v_permlane32_swap_b32 v196, v197\n"
      "v_permlane32_swap_b32 v198, v199\n"
      "v_mfma_f32_16x16x32_bf16 v[204:207], v[108:111], v[228:231], v[204:207]\n"
      "v_add_f32_dpp v128, v193, v192 row_ror:8 row_mask:0xf bank_mask:0x3\n"
      "s_nop 1\n"
      "v_add_f32_dpp v128, v194, v195 row_ror:8 row_mask:0xf bank_mask:0xc\n"
      "v_mfma_f32_16x16x32_bf16 v[208:211], v[80:83], v[224:227], 0\n"
      "v_permlane32_swap_b32 v200, v201\n"
      "v_permlane32_swap_b32 v202, v203\n"
      "v_mfma_f32_16x16x32_bf16 v[208:211], v[112:115], v[228:231], v[208:211]\n"
      "v_add_f32_dpp v129, v197, v196 row_ror:8 row_mask:0xf bank_mask:0x3\n"
      "s_nop 1\n"
      "v_add_f32_dpp v129, v198, v199 row_ror:8 row_mask:0xf bank_mask:0xc\n"
      "v_mfma_f32_16x16x32_bf16 v[212:215], v[84:87], v[224:227], 0\n"
      "v_permlane32_swap_b32 v204, v205\n"
      "v_permlane32_swap_b32 v206, v207\n"
      "v_mfma_f32_16x16x32_bf16 v[212:215], v[116:119], v[228:231], v[212:215]\n"
      "v_add_f32_dpp v130, v201, v200 row_ror:8 row_mask:0xf bank_mask:0x3\n"
      "s_nop 1\n"
      "v_add_f32_dpp v130, v202, v203 row_ror:8 row_mask:0xf bank_mask:0xc\n"
      "v_mfma_f32_16x16x32_bf16 v[216:219], v[88:91], v[224:227], 0\n"
      "v_permlane32_swap_b32 v208, v209\n"
      "v_permlane32_swap_b32 v210, v211\n"
      "v_mfma_f32_16x16x32_bf16 v[216:219], v[120:123], v[228:231], v[216:219]\n"
      "v_add_f32_dpp v131, v205, v204 row_ror:8 row_mask:0xf bank_mask:0x3\n"
      "s_nop 1\n"
      "v_add_f32_dpp v131, v206, v207 row_ror:8 row_mask:0xf bank_mask:0xc\n"
      "v_mfma_f32_16x16x32_bf16 v[220:223], v[92:95], v[224:227], 0\n"
      "v_permlane32_swap_b32 v212, v213\n"
      "v_permlane32_swap_b32 v214, v215\n"
      "v_mfma_f32_16x16x32_bf16 v[220:223], v[124:127], v[228:231], v[220:223]\n"
      "v_add_f32_dpp v132, v209, v208 row_ror:8 row_mask:0xf bank_mask:0x3\n"
      "s_nop 1\n"
      "v_add_f32_dpp v132, v210, v211 row_ror:8 row_mask:0xf bank_mask:0xc\n"
      "v_cvt_pk_bf16_f32 v136, v128, v129\n"
      "v_cvt_pk_bf16_f32 v138, v130, v131\n"
      "buffer_store_dwordx4 v[136:139], v38, s[16:19], s47 offen offset:0 sc1\n"
      "v_permlane32_swap_b32 v216, v217\n"
      "v_permlane32_swap_b32 v218, v219\n"
      "v_add_f32_dpp v133, v213, v212 row_ror:8 row_mask:0xf bank_mask:0x3\n"
      "s_nop 1\n"
      "v_add_f32_dpp v133, v214, v215 row_ror:8 row_mask:0xf bank_mask:0xc\n"
      "v_permlane32_swap_b32 v220, v221\n"
      "v_permlane32_swap_b32 v222, v223\n"
      "v_add_f32_dpp v134, v217, v216 row_ror:8 row_mask:0xf bank_mask:0x3\n"
      "s_nop 1\n"
      "v_add_f32_dpp v134, v218, v219 row_ror:8 row_mask:0xf bank_mask:0xc\n"
      "v_add_f32_dpp v135, v221, v220 row_ror:8 row_mask:0xf bank_mask:0x3\n"
      "s_nop 1\n"
      "v_add_f32_dpp v135, v222, v223 row_ror:8 row_mask:0xf bank_mask:0xc\n"
      "v_cvt_pk_bf16_f32 v140, v132, v133\n"
      "v_cvt_pk_bf16_f32 v142, v134, v135\n"
      "buffer_store_dwordx4 v[140:143], v38, s[16:19], s47 offen offset:128 sc1\n"
      "s_endpgm\n"
      ".LZ%=:\n"
      "s_setprio 0\n"
      "v_and_b32 v48, 63, v0\n"
      "v_and_b32 v49, 7, v0\n"
      "v_lshlrev_b32 v108, 3, v49\n"
      "s_lshl4_add_u32 s35, s48, s49\n"
      "s_lshl_b32 s41, s35, 4\n"
      "s_and_b32 s43, s41, 0xffffff80\n"
      "s_add_u32 s43, s43, 67584\n"
      "buffer_load_dwordx2 v[104:105], v108, s[16:19], s43 offen sc1\n"
      "s_mul_i32 s44, s35, 7168\n"
      "s_add_u32 s44, s44, 4194304\n"
      "s_add_u32 s67, s44, 0x1000\n"
      "v_lshrrev_b32 v109, 5, v48\n"
      "v_and_b32 v110, 8, v48\n"
      "v_lshl_add_u32 v109, v109, 2, v110\n"
      "v_and_b32 v113, 16, v48\n"
      "s_movk_i32 s59, 0x7fff\n"
      "s_waitcnt vmcnt(0)\n"
      "s_movk_i32 s61, 0x7ff\n"
      ".Ldzh%=:\n"
      "v_cmp_ne_u32 vcc, s15, v105\n"
      "v_bitop3_b32 v110, v104, s61, v104 bitop3:0x0c\n"
      "v_not_b32 v112, v104\n"
      "s_cbranch_vccnz .Ldzs%=\n"
      ".Ldzo%=:\n"
      "v_lshl_add_u32 v110, v110, 11, v109\n"
      "v_lshlrev_b32 v108, 4, v48\n"
      "buffer_load_dword v111, v110, s[20:23], s41 offen\n"
      "v_lshl_or_b32 v113, v113, 26, v110\n"
      "v_ashrrev_i32 v112, 31, v112\n"
      "v_or_b32 v112, 0x80000000, v112\n"
      "v_xor_b32 v112, v112, v104\n"
      "v_and_b32 v112, 0xffff0000, v112\n"
      "v_mul_f32 v112, 0xbfb8aa3b, v112\n"
      "v_exp_f32 v112, v112\n"
      "s_nop 0\n"
      "v_add_f32 v112, 1.0, v112\n"
      "v_rcp_f32 v112, v112\n"
      "s_nop 0\n"
      "v_cvt_pk_bf16_f32 v112, v112, v112\n"
      "v_and_b32 v112, 0xffff0000, v112\n"
      ".Ldpc2_%=:\n"
      "s_memrealtime s[64:65]\n"
      "s_waitcnt lgkmcnt(0)\n"
      "s_sub_u32 s64, s64, s12\n"
      "s_cmp_ge_u32 s64, 815\n"
      "s_cbranch_scc1 .Ldpd2_%=\n"
      "s_sleep 1\n"
      "s_branch .Ldpc2_%=\n"
      ".Ldpd2_%=:\n"
      "buffer_load_dwordx4 v[64:67], v108, s[16:19], s44 offen offset:0 sc1\n"
      "buffer_load_dwordx4 v[68:71], v108, s[16:19], s44 offen offset:1024 sc1\n"
      "buffer_load_dwordx4 v[72:75], v108, s[16:19], s44 offen offset:2048 sc1\n"
      "buffer_load_dwordx4 v[76:79], v108, s[16:19], s44 offen offset:3072 sc1\n"
      "buffer_load_dwordx4 v[80:83], v108, s[16:19], s67 offen offset:0 sc1\n"
      "buffer_load_dwordx4 v[84:87], v108, s[16:19], s67 offen offset:1024 sc1\n"
      "buffer_load_dwordx4 v[88:91], v108, s[16:19], s67 offen offset:2048 sc1\n"
      "s_movk_i32 s59, 0x7fff\n"
      "s_mov_b32 s56, 0x3f80\n"
      "s_mov_b32 s57, 0x3f800000\n"
      "s_waitcnt vmcnt(7)\n"
      "v_lshlrev_b32 v106, 16, v111\n"
      "v_and_b32 v107, 0xffff0000, v111\n"
      ".Ldq%=:\n"
      "s_waitcnt vmcnt(6)\n"
      "v_xor_b32 v100, s15, v65\n"
      "v_xor_b32 v101, s15, v67\n"
      "v_lshlrev_b32 v96, 16, v64\n"
      "v_and_b32 v97, 0xffff0000, v64\n"
      "v_lshlrev_b32 v98, 16, v66\n"
      "v_and_b32 v99, 0xffff0000, v66\n"
      "s_waitcnt vmcnt(5)\n"
      "v_dot2_f32_bf16 v96, v68, s56, v96\n"
      "v_dot2_f32_bf16 v97, v68, s57, v97\n"
      "v_dot2_f32_bf16 v98, v70, s56, v98\n"
      "v_dot2_f32_bf16 v99, v70, s57, v99\n"
      "v_bitop3_b32 v100, s15, v69, v100 bitop3:0xbe\n"
      "v_bitop3_b32 v101, s15, v71, v101 bitop3:0xbe\n"
      "s_waitcnt vmcnt(4)\n"
      "v_dot2_f32_bf16 v96, v72, s56, v96\n"
      "v_dot2_f32_bf16 v97, v72, s57, v97\n"
      "v_dot2_f32_bf16 v98, v74, s56, v98\n"
      "v_dot2_f32_bf16 v99, v74, s57, v99\n"
      "v_bitop3_b32 v100, s15, v73, v100 bitop3:0xbe\n"
      "v_bitop3_b32 v101, s15, v75, v101 bitop3:0xbe\n"
      "s_waitcnt vmcnt(3)\n"
      "v_dot2_f32_bf16 v96, v76, s56, v96\n"
      "v_dot2_f32_bf16 v97, v76, s57, v97\n"
      "v_dot2_f32_bf16 v98, v78, s56, v98\n"
      "v_dot2_f32_bf16 v99, v78, s57, v99\n"
      "v_bitop3_b32 v100, s15, v77, v100 bitop3:0xbe\n"
      "v_bitop3_b32 v101, s15, v79, v101 bitop3:0xbe\n"
      "s_waitcnt vmcnt(2)\n"
      "v_dot2_f32_bf16 v96, v80, s56, v96\n"
      "v_dot2_f32_bf16 v97, v80, s57, v97\n"
      "v_dot2_f32_bf16 v98, v82, s56, v98\n"
      "v_dot2_f32_bf16 v99, v82, s57, v99\n"
      "v_bitop3_b32 v100, s15, v81, v100 bitop3:0xbe\n"
      "v_bitop3_b32 v101, s15, v83, v101 bitop3:0xbe\n"
      "s_waitcnt vmcnt(1)\n"
      "v_dot2_f32_bf16 v96, v84, s56, v96\n"
      "v_dot2_f32_bf16 v97, v84, s57, v97\n"
      "v_dot2_f32_bf16 v98, v86, s56, v98\n"
      "v_dot2_f32_bf16 v99, v86, s57, v99\n"
      "v_bitop3_b32 v100, s15, v85, v100 bitop3:0xbe\n"
      "v_bitop3_b32 v101, s15, v87, v101 bitop3:0xbe\n"
      "s_waitcnt vmcnt(0)\n"
      "v_dot2_f32_bf16 v96, v88, s56, v96\n"
      "v_dot2_f32_bf16 v97, v88, s57, v97\n"
      "v_dot2_f32_bf16 v98, v90, s56, v98\n"
      "v_dot2_f32_bf16 v99, v90, s57, v99\n"
      "v_bitop3_b32 v100, s15, v89, v100 bitop3:0xbe\n"
      "v_bitop3_b32 v101, s15, v91, v101 bitop3:0xbe\n"
      "v_or_b32 v100, v100, v101\n"
      "v_cmp_ne_u32 vcc, 0, v100\n"
      "v_permlane32_swap_b32 v96, v98\n"
      "v_permlane32_swap_b32 v97, v99\n"
      "s_cbranch_vccnz .Ldqs%=\n"
      ".Ldqo%=:\n"
      "v_add_f32 v96, v96, v98\n"
      "v_add_f32 v97, v97, v99\n"
      "v_mov_b32 v98, v96\n"
      "v_mov_b32 v99, v97\n"
      "s_nop 0\n"
      "v_permlane16_swap_b32 v96, v98\n"
      "v_permlane16_swap_b32 v97, v99\n"
      "v_add_f32 v96, v96, v98\n"
      "v_add_f32 v97, v97, v99\n"
      "v_cvt_pk_bf16_f32 v96, v96, v97\n"
      "v_and_b32 v97, 0xffff0000, v96\n"
      "v_lshlrev_b32 v96, 16, v96\n"
      "v_mul_f32 v97, v97, v112\n"
      "v_mul_f32 v96, v96, v112\n"
      "v_cvt_pk_bf16_f32 v96, v96, v97\n"
      "v_and_b32 v97, 0xffff0000, v96\n"
      "v_lshlrev_b32 v96, 16, v96\n"
      "v_add_f32 v97, v97, v107\n"
      "v_add_f32 v96, v96, v106\n"
      "v_cvt_pk_bf16_f32 v96, v96, v97\n"
      "buffer_atomic_pk_add_bf16 v96, v113, s[24:27], s41 offen\n"
      "s_endpgm\n"
      ".Ldgs%=:\n"
      "s_sleep 1\n"
      "s_sub_u32 s59, s59, 1\n"
      "s_cbranch_scc1 .Ldgo%=\n"
      "buffer_load_dwordx4 v[224:227], v35, s[16:19], s46 offen sc1\n"
      "buffer_load_dwordx4 v[228:231], v35, s[16:19], s46 offen offset:2048 sc1\n"
      "s_waitcnt vmcnt(0)\n"
      "s_branch .Ldg%=\n"
      ".Ldvs%=:\n"
      "s_sub_u32 s59, s59, 1\n"
      "s_cbranch_scc1 .Ldvo%=\n"
      "s_waitcnt lgkmcnt(0)\n"
      "ds_read_b128 v[240:243], v42 offset:32\n"
      "ds_read_b128 v[224:227], v37 \n"
      "ds_read_b128 v[228:231], v37 offset:1152\n"
      "s_waitcnt lgkmcnt(0)\n"
      "s_branch .Ldv%=\n"
      ".Ldzs%=:\n"
      "s_sleep 2\n"
      "s_sub_u32 s59, s59, 1\n"
      "s_cbranch_scc1 .Ldzo%=\n"
      "buffer_load_dwordx2 v[104:105], v108, s[16:19], s43 offen sc1\n"
      "s_waitcnt vmcnt(0)\n"
      "s_branch .Ldzh%=\n"
      ".Ldqs%=:\n"
      "s_sleep 1\n"
      "s_sub_u32 s59, s59, 1\n"
      "s_cbranch_scc1 .Ldqo%=\n"
      "v_cmp_ne_u32 vcc, s15, v65\n"
      "v_cmp_ne_u32 s[64:65], s15, v67\n"
      "s_nop 0\n"
      "s_or_b64 exec, vcc, s[64:65]\n"
      "s_cbranch_scc0 .Ldqk0%=\n"
      "buffer_load_dwordx4 v[64:67], v108, s[16:19], s44 offen offset:0 sc1\n"
      ".Ldqk0%=:\n"
      "s_mov_b64 exec, -1\n"
      "v_cmp_ne_u32 vcc, s15, v69\n"
      "v_cmp_ne_u32 s[64:65], s15, v71\n"
      "s_nop 0\n"
      "s_or_b64 exec, vcc, s[64:65]\n"
      "s_cbranch_scc0 .Ldqk1%=\n"
      "buffer_load_dwordx4 v[68:71], v108, s[16:19], s44 offen offset:1024 sc1\n"
      ".Ldqk1%=:\n"
      "s_mov_b64 exec, -1\n"
      "v_cmp_ne_u32 vcc, s15, v73\n"
      "v_cmp_ne_u32 s[64:65], s15, v75\n"
      "s_nop 0\n"
      "s_or_b64 exec, vcc, s[64:65]\n"
      "s_cbranch_scc0 .Ldqk2%=\n"
      "buffer_load_dwordx4 v[72:75], v108, s[16:19], s44 offen offset:2048 sc1\n"
      ".Ldqk2%=:\n"
      "s_mov_b64 exec, -1\n"
      "v_cmp_ne_u32 vcc, s15, v77\n"
      "v_cmp_ne_u32 s[64:65], s15, v79\n"
      "s_nop 0\n"
      "s_or_b64 exec, vcc, s[64:65]\n"
      "s_cbranch_scc0 .Ldqk3%=\n"
      "buffer_load_dwordx4 v[76:79], v108, s[16:19], s44 offen offset:3072 sc1\n"
      ".Ldqk3%=:\n"
      "s_mov_b64 exec, -1\n"
      "v_cmp_ne_u32 vcc, s15, v81\n"
      "v_cmp_ne_u32 s[64:65], s15, v83\n"
      "s_nop 0\n"
      "s_or_b64 exec, vcc, s[64:65]\n"
      "s_cbranch_scc0 .Ldqk4%=\n"
      "buffer_load_dwordx4 v[80:83], v108, s[16:19], s67 offen offset:0 sc1\n"
      ".Ldqk4%=:\n"
      "s_mov_b64 exec, -1\n"
      "v_cmp_ne_u32 vcc, s15, v85\n"
      "v_cmp_ne_u32 s[64:65], s15, v87\n"
      "s_nop 0\n"
      "s_or_b64 exec, vcc, s[64:65]\n"
      "s_cbranch_scc0 .Ldqk5%=\n"
      "buffer_load_dwordx4 v[84:87], v108, s[16:19], s67 offen offset:1024 sc1\n"
      ".Ldqk5%=:\n"
      "s_mov_b64 exec, -1\n"
      "v_cmp_ne_u32 vcc, s15, v89\n"
      "v_cmp_ne_u32 s[64:65], s15, v91\n"
      "s_nop 0\n"
      "s_or_b64 exec, vcc, s[64:65]\n"
      "s_cbranch_scc0 .Ldqk6%=\n"
      "buffer_load_dwordx4 v[88:91], v108, s[16:19], s67 offen offset:2048 sc1\n"
      ".Ldqk6%=:\n"
      "s_mov_b64 exec, -1\n"
      "s_waitcnt vmcnt(0)\n"
      "s_branch .Ldq%=\n"
      ".LT%=:\n"
      "v_mov_b32 v48, 0x22680\n"
      "ds_read_b32 v48, v48 \n"
      "s_waitcnt lgkmcnt(0)\n"
      "v_readfirstlane_b32 s12, v48\n"
      "v_mov_b32 v48, v43\n"
      "v_and_b32 v49, 7, v43\n"
      "v_lshrrev_b32 v50, 3, v43\n"
      "s_lshl_b32 s42, s34, 3\n"
      "s_add_u32 s42, s42, 2048\n"
      "s_lshl_b32 s43, s48, 8\n"
      "s_and_b32 s50, s49, 1\n"
      "s_lshl_b32 s50, s50, 7\n"
      "s_add_u32 s43, s43, s50\n"
      "s_add_u32 s43, s43, 65536\n"
      "s_mov_b32 s20, s64\n"
      "s_and_b32 s21, s65, 0xffff\n"
      "s_mov_b32 s22, 0x400000\n"
      "s_mov_b32 s23, 0x20000\n"
      "s_mov_b32 s28, s84\n"
      "s_and_b32 s29, s85, 0xffff\n"
      "s_mov_b32 s30, 0x700000\n"
      "s_mov_b32 s31, 0x20000\n"
      "s_sleep 0\n"
      ".LS%=:\n"
      "v_lshlrev_b32 v112, 4, v0\n"
      "v_lshlrev_b32 v113, 2, v0\n"
      "v_sub_u32 v113, 0xfff, v113\n"
      "v_lshlrev_b32 v115, 2, v48\n"
      "v_mov_b32 v116, 0\n"
      "ds_write_b32 v115, v116 offset:27136\n"
      "ds_write_b32 v116, v116 offset:26400\n"
      "v_add_u32 v115, 0x140, v115\n"
      "v_mov_b32 v114, v115\n"
      "s_lshl_b32 s50, s33, 2\n"
      "s_add_u32 s50, s50, 256\n"
      "v_writelane_b32 v114, s50, 63\n"
      "s_lshl_b32 s50, s33, 5\n"
      "v_lshl_add_u32 v117, v49, 2, s50\n"
      "v_lshlrev_b32 v118, 5, v50\n"
      "s_and_b32 s50, s49, 1\n"
      "s_lshl_b32 s50, s50, 10\n"
      "v_lshl_add_u32 v119, v50, 7, s50\n"
      "s_lshr_b32 s51, s33, 2\n"
      "s_mul_i32 s51, s51, 61440\n"
      "s_add_u32 s51, s51, 393216\n"
      "s_mov_b32 s52, 0x80008000\n"
      "s_mov_b32 s53, 0xffff0000\n"
      "s_movk_i32 s59, 0x7fff\n"
      ".Lt_pc4_%=:\n"
      "s_memrealtime s[64:65]\n"
      "s_waitcnt lgkmcnt(0)\n"
      "s_sub_u32 s64, s64, s12\n"
      "s_cmp_ge_u32 s64, 300\n"
      "s_cbranch_scc1 .Lt_pd4_%=\n"
      "s_sleep 1\n"
      "s_branch .Lt_pc4_%=\n"
      ".Lt_pd4_%=:\n"
      ".Lt_p%=:\n"
      "buffer_load_dwordx4 v[72:75], v112, s[16:19], s51 offen sc1\n"
      "s_waitcnt vmcnt(0)\n"
      "v_cmp_ne_u32 vcc, s15, v73\n"
      "v_cmp_ne_u32 s[54:55], s15, v75\n"
      "v_pk_ashrrev_i16 v81, 15, v72 op_sel_hi:[0,1]\n"
      "v_pk_ashrrev_i16 v82, 15, v74 op_sel_hi:[0,1]\n"
      "s_or_b64 vcc, vcc, s[54:55]\n"
      "s_cbranch_scc1 .Lt_ps%=\n"
      ".Lt_po%=:\n"
      "v_bitop3_b32 v81, v72, v81, s52 bitop3:0x1e\n"
      "v_bitop3_b32 v82, v74, v82, s52 bitop3:0x1e\n"
      "v_lshl_or_b32 v76, v81, 16, v113\n"
      "v_and_or_b32 v77, v81, s53, v113\n"
      "v_lshl_or_b32 v78, v82, 16, v113\n"
      "v_and_or_b32 v79, v82, s53, v113\n"
      "v_add_u32 v77, -1, v77\n"
      "v_add_u32 v78, -2, v78\n"
      "v_add_u32 v79, -3, v79\n"
      "v_max3_u32 v80, v76, v77, v78\n"
      "v_max_u32 v80, v80, v79\n"
      "v_mov_b32 v104, v80\n"
      "v_med3_u32 v105, v76, v77, v78\n"
      "v_max_u32_dpp v80, v80, v80 row_shr:1 row_mask:0xf bank_mask:0xf bound_ctrl:1\n"
      "v_max3_u32 v83, v76, v77, v78\n"
      "v_min3_u32 v106, v76, v77, v78\n"
      "v_max_u32_dpp v80, v80, v80 row_shr:2 row_mask:0xf bank_mask:0xf bound_ctrl:1\n"
      "v_med3_u32 v106, v105, v106, v79\n"
      "v_med3_u32 v105, v83, v105, v79\n"
      "v_max_u32_dpp v80, v80, v80 row_shr:4 row_mask:0xf bank_mask:0xf bound_ctrl:1\n"
      "v_mov_b32 v107, 0x100\n"
      "s_nop 0\n"
      "v_max_u32_dpp v80, v80, v80 row_shr:8 row_mask:0xf bank_mask:0xf bound_ctrl:1\n"
      "s_nop 1\n"
      "v_max_u32_dpp v80, v80, v80 row_bcast:15 row_mask:0xa bank_mask:0xf\n"
      "s_nop 1\n"
      "v_max_u32_dpp v80, v80, v80 row_bcast:31 row_mask:0xc bank_mask:0xf\n"
      "ds_write_b32 v114, v80 offset:26112\n"
      "s_waitcnt lgkmcnt(0)\n"
      "s_barrier\n"
      "ds_read_b128 v[92:95], v116 offset:26368\n"
      "ds_read_b128 v[96:99], v116 offset:26384\n"
      "s_waitcnt lgkmcnt(1)\n"
      "v_min3_u32 v81, v92, v93, v94\n"
      "s_waitcnt lgkmcnt(0)\n"
      "v_min3_u32 v82, v96, v97, v98\n"
      "v_min3_u32 v81, v81, v95, v99\n"
      "v_min_u32 v81, v81, v82\n"
      "v_cmp_ge_u32 s[62:63], v105, v81\n"
      "v_cmp_ge_u32 vcc, v106, v81\n"
      "v_cmp_ge_u32 s[60:61], v104, v81\n"
      "v_cndmask_b32 v82, 1, 2, s[62:63]\n"
      "v_cndmask_b32 v82, v82, v107, vcc\n"
      "s_mov_b64 exec, s[60:61]\n"
      "ds_add_rtn_u32 v83, v116, v82 offset:26400\n"
      "s_waitcnt lgkmcnt(0)\n"
      "v_min_u32 v83, 0x50, v83\n"
      "v_lshlrev_b32 v83, 2, v83\n"
      "ds_write_b32 v83, v104 offset:27136\n"
      "s_mov_b64 exec, s[62:63]\n"
      "ds_write_b32 v83, v105 offset:27140\n"
      "s_mov_b64 exec, -1\n"
      "s_waitcnt lgkmcnt(0)\n"
      "s_barrier\n"
      "s_memrealtime s[56:57]\n"
      "ds_read_b32 v81, v116 offset:26400\n"
      ".Lt_r%=:\n"
      "ds_read_b32 v100, v117 offset:27136\n"
      "ds_read_b128 v[92:95], v118 offset:27136\n"
      "ds_read_b128 v[96:99], v118 offset:27152\n"
      "v_mov_b32 v101, s15\n"
      "v_mov_b32 v89, 0\n"
      "s_waitcnt lgkmcnt(3)\n"
      "v_cmp_lt_u32 vcc, 64, v81\n"
      "s_movk_i32 s50, 0x7ff\n"
      "s_cbranch_vccnz .Lt_m%=\n"
      "s_waitcnt lgkmcnt(1)\n"
      "v_cmp_gt_u32 s[60:61], v92, v100\n"
      "v_cmp_gt_u32 s[62:63], v93, v100\n"
      "v_cmp_gt_u32 s[64:65], v94, v100\n"
      "v_cmp_gt_u32 s[66:67], v95, v100\n"
      "s_waitcnt lgkmcnt(0)\n"
      "v_cmp_gt_u32 s[68:69], v96, v100\n"
      "v_cmp_gt_u32 s[70:71], v97, v100\n"
      "v_cmp_gt_u32 s[72:73], v98, v100\n"
      "v_cmp_gt_u32 s[74:75], v99, v100\n"
      "v_bitop3_b32 v81, v100, s50, v100 bitop3:0x0c\n"
      "v_addc_co_u32 v89, vcc, 0, v89, s[60:61]\n"
      "v_addc_co_u32 v89, vcc, 0, v89, s[62:63]\n"
      "v_addc_co_u32 v89, vcc, 0, v89, s[64:65]\n"
      "v_addc_co_u32 v89, vcc, 0, v89, s[66:67]\n"
      "v_addc_co_u32 v89, vcc, 0, v89, s[68:69]\n"
      "v_addc_co_u32 v89, vcc, 0, v89, s[70:71]\n"
      "v_addc_co_u32 v89, vcc, 0, v89, s[72:73]\n"
      "v_addc_co_u32 v89, vcc, 0, v89, s[74:75]\n"
      "v_lshl_add_u32 v82, v81, 11, v119\n"
      "v_lshrrev_b32 v86, 9, v81\n"
      "v_mul_u32_u24 v86, 0xf000, v86\n"
      "v_lshl_add_u32 v81, v81, 3, v86\n"
      "v_add_u32_dpp v89, v89, v89 row_ror:8 row_mask:0xf bank_mask:0xf\n"
      "v_mov_b32 v83, v89\n"
      "s_nop 1\n"
      "v_permlane16_swap_b32 v89, v83\n"
      "v_add_u32 v89, v89, v83\n"
      "v_mov_b32 v83, v89\n"
      "s_nop 1\n"
      "v_permlane32_swap_b32 v89, v83\n"
      "v_add_u32 v89, v89, v83\n"
      "v_cmp_gt_u32 vcc, 8, v89\n"
      "v_lshlrev_b32 v89, 3, v89\n"
      "s_mov_b32 s51, 131072\n"
      "s_cbranch_vccz .Lt_n%=\n"
      "s_and_b64 exec, vcc, 0xff\n"
      "ds_write_b32 v89, v100 offset:26752\n"
      "buffer_load_dword v84, v81, s[16:19], s51 offen sc1\n"
      "s_mov_b64 exec, vcc\n"
      "buffer_load_dword v85, v82, s[20:23], 0 offen\n"
      "s_mov_b64 exec, -1\n"
      ".Lt_n%=:\n"
      "v_lshlrev_b32 v89, 3, v49\n"
      "s_waitcnt lgkmcnt(0)\n"
      "s_barrier\n"
      "s_sub_u32 s12, s56, 390\n"
      "s_movk_i32 s57, 64\n"
      "s_cmp_lg_u32 s33, 0\n"
      "s_cbranch_scc1 .Lt_a%=\n"
      "ds_read_b32 v100, v89 offset:26752\n"
      "v_writelane_b32 v89, s57, 8\n"
      "s_waitcnt lgkmcnt(0)\n"
      "v_writelane_b32 v100, s12, 8\n"
      "s_mov_b64 exec, 0x1ff\n"
      "buffer_store_dwordx2 v[100:101], v89, s[16:19], s43 offen sc1\n"
      "s_mov_b64 exec, -1\n"
      "s_nop 1\n"
      "v_mov_b32 v96, s15\n"
      "v_mov_b32 v97, s15\n"
      "buffer_store_dwordx2 v[96:97], off, s[16:19], s42 sc1\n"
      ".Lt_a%=:\n"
      "s_cmp_lg_u32 s33, 1\n"
      "s_cbranch_scc1 .Lt_c%=\n"
      "ds_read_b32 v100, v89 offset:26752\n"
      "v_lshlrev_b32 v81, 3, v50\n"
      "ds_read_b32 v81, v81 offset:26752\n"
      "v_mov_b32 v92, 0\n"
      "v_mov_b32 v93, 0\n"
      "v_mov_b32 v94, 0\n"
      "v_mov_b32 v95, 0\n"
      "s_and_b32 s51, s43, 0x7ff\n"
      "v_lshlrev_b32 v82, 4, v49\n"
      "s_waitcnt lgkmcnt(0)\n"
      "v_bitop3_b32 v81, v81, s50, v81 bitop3:0x0c\n"
      "v_lshl_add_u32 v81, v81, 11, v82\n"
      "buffer_store_dwordx4 v[92:95], v81, s[24:27], s51 offen sc1\n"
      "s_waitcnt vmcnt(0)\n"
      "s_mov_b64 exec, 0xff\n"
      "buffer_store_dwordx2 v[100:101], v89, s[16:19], s43 offen offset:2048 sc1\n"
      "s_mov_b64 exec, -1\n"
      "s_nop 1\n"
      ".Lt_c%=:\n"
      "s_cmp_lg_u32 s33, 3\n"
      "s_cbranch_scc1 .Lt_z%=\n"
      "s_sleep 55\n"
      "s_branch .LZ%=\n"
      ".Lt_z%=:\n"
      "s_cmp_lg_u32 s34, 127\n"
      "s_cbranch_scc1 .Lt_e%=\n"
      "s_cmp_lg_u32 s33, 0\n"
      "s_cbranch_scc1 .Lt_e%=\n"
      "s_movk_i32 s51, 2048\n"
      "s_movk_i32 s59, 0x7fff\n"
      ".Lt_k%=:\n"
      "s_sleep 16\n"
      "buffer_load_dwordx4 v[92:95], v112, s[16:19], s51 offen sc1\n"
      "buffer_load_dwordx4 v[96:99], v112, s[16:19], s51 offen offset:1024 sc1\n"
      "s_sub_u32 s59, s59, 1\n"
      "s_cbranch_scc1 .Lt_kb%=\n"
      "s_waitcnt vmcnt(0)\n"
      "v_xor_b32 v86, s15, v93\n"
      "v_bitop3_b32 v86, s15, v95, v86 bitop3:0xbe\n"
      "v_bitop3_b32 v86, s15, v97, v86 bitop3:0xbe\n"
      "v_bitop3_b32 v86, s15, v99, v86 bitop3:0xbe\n"
      "v_cmp_ne_u32 vcc, 0, v86\n"
      "s_nop 1\n"
      "s_cbranch_vccnz .Lt_k%=\n"
      ".Lt_kb%=:\n"
      "buffer_store_dword v101, off, s[16:19], 0 sc1\n"
      ".Lt_e%=:\n"
      "s_waitcnt vmcnt(0)\n"
      "s_endpgm\n"
      ".Lt_ps%=:\n"
      "s_sleep 1\n"
      "s_sub_u32 s59, s59, 1\n"
      "s_cbranch_scc0 .Lt_p%=\n"
      "s_branch .Lt_po%=\n"
      ".Lt_m%=:\n"
      "s_waitcnt lgkmcnt(0)\n"
      "s_barrier\n"
      "s_mov_b32 s59, 0\n"
      "v_mov_b32 v90, 0\n"
      ".Lt_ml%=:\n"
      "s_nop 1\n"
      "v_max3_u32 v80, v76, v77, v78\n"
      "v_max_u32 v80, v80, v79\n"
      "s_nop 1\n"
      "v_max_u32_dpp v80, v80, v80 row_shr:1 row_mask:0xf bank_mask:0xf bound_ctrl:1\n"
      "s_nop 1\n"
      "v_max_u32_dpp v80, v80, v80 row_shr:2 row_mask:0xf bank_mask:0xf bound_ctrl:1\n"
      "s_nop 1\n"
      "v_max_u32_dpp v80, v80, v80 row_shr:4 row_mask:0xf bank_mask:0xf bound_ctrl:1\n"
      "s_nop 1\n"
      "v_max_u32_dpp v80, v80, v80 row_shr:8 row_mask:0xf bank_mask:0xf bound_ctrl:1\n"
      "s_nop 1\n"
      "v_max_u32_dpp v80, v80, v80 row_bcast:15 row_mask:0xa bank_mask:0xf\n"
      "s_nop 1\n"
      "v_max_u32_dpp v80, v80, v80 row_bcast:31 row_mask:0xc bank_mask:0xf\n"
      "s_nop 0\n"
      "v_readlane_b32 s50, v80, 63\n"
      "s_mov_b32 m0, s59\n"
      "s_nop 0\n"
      "v_cmp_ne_u32 vcc, s50, v76\n"
      "s_nop 1\n"
      "v_cndmask_b32 v76, 0, v76, vcc\n"
      "v_cmp_ne_u32 vcc, s50, v77\n"
      "s_nop 1\n"
      "v_cndmask_b32 v77, 0, v77, vcc\n"
      "v_cmp_ne_u32 vcc, s50, v78\n"
      "s_nop 1\n"
      "v_cndmask_b32 v78, 0, v78, vcc\n"
      "v_cmp_ne_u32 vcc, s50, v79\n"
      "s_nop 1\n"
      "v_cndmask_b32 v79, 0, v79, vcc\n"
      "v_writelane_b32 v90, s50, m0\n"
      "s_add_u32 s59, s59, 1\n"
      "s_cmp_lt_u32 s59, 8\n"
      "s_cbranch_scc1 .Lt_ml%=\n"
      "s_mov_b64 exec, 0xff\n"
      "ds_write_b32 v117, v90 offset:27136\n"
      "s_mov_b64 exec, -1\n"
      "v_mov_b32 v81, 0\n"
      "s_waitcnt lgkmcnt(0)\n"
      "s_barrier\n"
      "s_branch .Lt_r%=\n"
      ".pushsection .rodata\n.p2align 7\n.Ltab%=:\n"
      ".long 0x0, 0x800023a, 0x6a006e0, 0x3a, 0x1100000, 0x65e022a, 0x5fe063e, 0x100878, 0x2200000, 0x6fe021a, 0x69e06de, 0x20003c, 0x3300000, 0x65c020a, 0x5fc063c, 0x30087a\n"
      ".long 0x4400110, 0x6fc01fa, 0x69c06dc, 0x440003e, 0x5500110, 0x65a01ea, 0x5fa063a, 0x450087c, 0x6600110, 0x6fa01da, 0x69a06da, 0x4600040, 0x7700110, 0x65801ca, 0x5f80638, 0x470087e\n"
      ".long 0x8800220, 0x6f801ba, 0x69806d8, 0x4a00580, 0x8800220, 0x65601aa, 0x5f60636, 0x4b00dbe, 0x8800220, 0x6f6019a, 0x69606d6, 0x4c00680, 0xbb00220, 0x654018a, 0x5f40634, 0x4d00ebe\n"
      ".long 0xcc00330, 0x6f4017a, 0x69406d4, 0x5000810, 0xcc00330, 0x652016a, 0x5f20632, 0x510104e, 0xcc00330, 0x6f2015a, 0x69206d2, 0x5200810, 0xff00330, 0x650014a, 0x5f00630, 0x530104e\n"
      ".long 0x11000440, 0x6f0013a, 0x69006d0, 0x5600810, 0x12100440, 0x64e012a, 0x5ee062e, 0x570104e, 0x13200440, 0x6ee011a, 0x68e06ce, 0x5800810, 0x14300440, 0x64c010a, 0x5ec062c, 0x590104e\n"
      ".long 0x15400550, 0x6ec00fa, 0x68c06cc, 0x5c00810, 0x16500550, 0x64a00ea, 0x5ea062a, 0x5d0104e, 0x17600550, 0x6ea00da, 0x68a06ca, 0x5e00810, 0x18700550, 0x64800ca, 0x5e80628, 0x5f0104e\n"
      ".long 0x9900660, 0x6e800ba, 0x68806c8, 0x6600810, 0x9900660, 0x64600aa, 0x5e60626, 0x670104e, 0x9900660, 0x6e6009a, 0x68606c6, 0x6800810, 0x9900660, 0x644008a, 0x5e40624, 0x690104e\n"
      ".long 0x9900770, 0x6e4007a, 0x68406c4, 0x7000810, 0x9900770, 0x642006a, 0x5e20622, 0x710104e, 0x9900770, 0x6e2005a, 0x68206c2, 0x7200810, 0x9900770, 0x640004a, 0x5e00620, 0x730104e\n"
      ".long 0x100010, 0x8000232, 0x47e05a0, 0x8400810, 0x1200010, 0x4fe0222, 0x53e04de, 0x850104e, 0x2300010, 0x5be0212, 0x47c059e, 0x8600810, 0x3400010, 0x4fc0202, 0x53c04dc, 0x870104e\n"
      ".long 0x4500120, 0x5bc01f2, 0x47a059c, 0xc700810, 0x5600120, 0x4fa01e2, 0x53a04da, 0xc80104e, 0x6700120, 0x5ba01d2, 0x478059a, 0xc900810, 0x7800120, 0x4f801c2, 0x53804d8, 0xca0104e\n"
      ".long 0x8900230, 0x5b801b2, 0x4760598, 0xcd00810, 0x8900230, 0x4f601a2, 0x53604d6, 0xce0104e, 0x8900230, 0x5b60192, 0x4740596, 0xcf00810, 0xbc00230, 0x4f40182, 0x53404d4, 0xd00104e\n"
      ".long 0xcd00340, 0x5b40172, 0x4720594, 0xd300810, 0xcd00340, 0x4f20162, 0x53204d2, 0xd40104e, 0xcd00340, 0x5b20152, 0x4700592, 0xd500810, 0x10000340, 0x4f00142, 0x53004d0, 0xd60104e\n"
      ".long 0x11100450, 0x5b00132, 0x46e0590, 0xd900810, 0x12200450, 0x4ee0122, 0x52e04ce, 0xda0104e, 0x13300450, 0x5ae0112, 0x46c058e, 0xdb00810, 0x14400450, 0x4ec0102, 0x52c04cc, 0xdc0104e\n"
      ".long 0x15500560, 0x5ac00f2, 0x46a058c, 0xdf00810, 0x16600560, 0x4ea00e2, 0x52a04ca, 0xe00104e, 0x17700560, 0x5aa00d2, 0x468058a, 0xe100810, 0x18800560, 0x4e800c2, 0x52804c8, 0xe20104e\n"
      ".long 0x9a00670, 0x5a800b2, 0x4660588, 0xe900810, 0x9a00670, 0x4e600a2, 0x52604c6, 0xea0104e, 0x9a00670, 0x5a60092, 0x4640586, 0xeb00810, 0x9a00670, 0x4e40082, 0x52404c4, 0xec0104e\n"
      ".long 0x9a00780, 0x5a40072, 0x4620584, 0xf300810, 0x9a00780, 0x4e20062, 0x52204c2, 0xf40104e, 0x9a00780, 0x5a20052, 0x4600582, 0xf500810, 0x9a00780, 0x4e00042, 0x52004c0, 0xf60104e\n"
      ".long 0xc9c085c, 0x0, 0x0, 0x880, 0x45c001c, 0x200010, 0x40008, 0x800990, 0xc980858, 0x400020, 0x80010, 0x8000aa0, 0x4580018, 0x600030, 0xc0018, 0x8800bb0\n"
      ".long 0xc940854, 0x800040, 0x102240, 0x10000880, 0x4540014, 0xa00050, 0x142248, 0x10800990, 0xc900850, 0xc00060, 0x182250, 0x18000aa0, 0x4500010, 0xe00070, 0x1c2258, 0x18800bb0\n"
      ".long 0xc8c084c, 0x1000080, 0xc204480, 0x20000880, 0x44c000c, 0x1200090, 0xc244488, 0x20800990, 0xc880848, 0x14000a0, 0xc284490, 0x28000aa0, 0x4480008, 0x16000b0, 0xc2c4498, 0x28800bb0\n"
      ".long 0xc840844, 0x18000c0, 0xc3066c0, 0x30000880, 0x4440004, 0x1a000d0, 0xc3466c8, 0x30800990, 0xc800840, 0x1c000e0, 0xc3866d0, 0x38000aa0, 0x4400000, 0x1e000f0, 0xc3c66d8, 0x38800bb0\n"
      ".long 0xca4086c, 0x100004, 0x20, 0x40000880, 0x464002c, 0x300014, 0x40028, 0x40800990, 0xca00868, 0x500024, 0x80030, 0x48000aa0, 0x4600028, 0x700034, 0xc0038, 0x48800bb0\n"
      ".long 0xc9c0864, 0x900044, 0x102260, 0x50000880, 0x45c0024, 0xb00054, 0x142268, 0x50800990, 0xc980860, 0xd00064, 0x182270, 0x58000aa0, 0x4580020, 0xf00074, 0x1c2278, 0x58800bb0\n"
      ".long 0xc94085c, 0x1100084, 0x82044a0, 0x60000880, 0x454001c, 0x1300094, 0x82444a8, 0x60800990, 0xc900858, 0x15000a4, 0x82844b0, 0x68000aa0, 0x4500018, 0x17000b4, 0x82c44b8, 0x68800bb0\n"
      ".long 0xc8c0854, 0x19000c4, 0x83066e0, 0x70000880, 0x44c0014, 0x1b000d4, 0x83466e8, 0x70800990, 0xc880850, 0x1d000e4, 0x83866f0, 0x78000aa0, 0x4480010, 0x1f000f4, 0x83c66f8, 0x78800bb0\n"
      ".long 0xcac087c, 0x200008, 0x800, 0x890, 0x46c003c, 0x400018, 0x40808, 0x8009a0, 0xca80878, 0x600028, 0x80810, 0x8000ab0, 0x4680038, 0x800038, 0xc0818, 0x8800bc0\n"
      ".long 0xca40874, 0xa00048, 0x102a40, 0x10000890, 0x4640034, 0xc00058, 0x142a48, 0x108009a0, 0xca00870, 0xe00068, 0x182a50, 0x18000ab0, 0x4600030, 0x1000078, 0x1c2a58, 0x18800bc0\n"
      ".long 0xc9c086c, 0x1200088, 0xc204c80, 0x20000890, 0x45c002c, 0x1400098, 0xc244c88, 0x208009a0, 0xc980868, 0x16000a8, 0xc284c90, 0x28000ab0, 0x4580028, 0x18000b8, 0xc2c4c98, 0x28800bc0\n"
      ".long 0xc940864, 0x1a000c8, 0xc306ec0, 0x30000890, 0x4540024, 0x1c000d8, 0xc346ec8, 0x308009a0, 0xc900860, 0x1e000e8, 0xc386ed0, 0x38000ab0, 0x4500020, 0x20000f8, 0xc3c6ed8, 0x38800bc0\n"
      ".long 0xcb4088c, 0x30000c, 0x820, 0x40000890, 0x474004c, 0x50001c, 0x40828, 0x408009a0, 0xcb00888, 0x70002c, 0x80830, 0x48000ab0, 0x4700048, 0x90003c, 0xc0838, 0x48800bc0\n"
      ".long 0xcac0884, 0xb0004c, 0x102a60, 0x50000890, 0x46c0044, 0xd0005c, 0x142a68, 0x508009a0, 0xca80880, 0xf0006c, 0x182a70, 0x58000ab0, 0x4680040, 0x110007c, 0x1c2a78, 0x58800bc0\n"
      ".long 0xca4087c, 0x130008c, 0x8204ca0, 0x60000890, 0x464003c, 0x150009c, 0x8244ca8, 0x608009a0, 0xca00878, 0x17000ac, 0x8284cb0, 0x68000ab0, 0x4600038, 0x19000bc, 0x82c4cb8, 0x68800bc0\n"
      ".long 0xc9c0874, 0x1b000cc, 0x8306ee0, 0x70000890, 0x45c0034, 0x1d000dc, 0x8346ee8, 0x708009a0, 0xc980870, 0x1f000ec, 0x8386ef0, 0x78000ab0, 0x4580030, 0x21000fc, 0x83c6ef8, 0x78800bc0\n"
      ".long 0x0, 0x0, 0x0, 0x0, 0x0, 0x0, 0x0, 0x0, 0x0, 0x0, 0x0, 0x0, 0x0, 0x0, 0x0, 0x0\n"
      ".long 0x0, 0x0, 0x0, 0x0, 0x0, 0x0, 0x0, 0x0, 0x0, 0x0, 0x0, 0x0, 0x0, 0x0, 0x0, 0x0\n"
      ".long 0x0, 0x0, 0x0, 0x0, 0x0, 0x0, 0x0, 0x0, 0x0, 0x0, 0x0, 0x0, 0x0, 0x0, 0x0, 0x0\n"
      ".long 0x0, 0x0, 0x0, 0x0, 0x0, 0x0, 0x0, 0x0, 0x0, 0x0, 0x0, 0x0, 0x0, 0x0, 0x0, 0x0\n"
      ".long 0x0, 0x0, 0xffffffff, 0x0, 0x0, 0x0, 0xffff0000, 0xffff, 0x0, 0x0, 0x0, 0xffffffff, 0x0, 0x0, 0x0, 0xffff0000\n"
      ".long 0x0, 0x0, 0x0, 0x0, 0x0, 0x0, 0x0, 0x0, 0x0, 0x0, 0x0, 0x0, 0x0, 0x0, 0x0, 0x0\n"
      ".long 0x0, 0x0, 0x0, 0x0, 0x0, 0x0, 0x0, 0x0, 0x0, 0x0, 0x0, 0x0, 0x0, 0x0, 0x0, 0x0\n"
      ".long 0x0, 0x0, 0x0, 0x0, 0x0, 0x0, 0x0, 0x0, 0x0, 0x0, 0x0, 0x0, 0x0, 0x0, 0x0, 0x0\n"
      ".long 0x0, 0x0, 0x0, 0x0, 0x0, 0x0, 0x0, 0x0, 0x0, 0x0, 0x0, 0x0, 0xffff, 0x0, 0x0, 0x0\n"
      ".long 0xffffffff, 0x0, 0x0, 0x0, 0xffff0000, 0xffff, 0x0, 0x0, 0x0, 0xffffffff, 0x0, 0x0, 0x0, 0xffff0000, 0xffff, 0x0\n"
      ".long 0x0, 0x0, 0xffffffff, 0x0, 0x0, 0x0, 0xffff0000, 0xffff, 0x0, 0x0, 0x0, 0xffffffff, 0x0, 0x0, 0x0, 0xffff0000\n"
      ".long 0x0, 0x0, 0x0, 0x0, 0x0, 0x0, 0x0, 0x0, 0x0, 0x0, 0x0, 0x0, 0x0, 0x0, 0x0, 0x0\n"
      ".long 0x0, 0x0, 0x0, 0x0, 0x0, 0x0, 0x0, 0x0, 0x0, 0x0, 0x0, 0x0, 0x0, 0x0, 0x0, 0x0\n"
      ".long 0x0, 0x0, 0x0, 0x0, 0x0, 0x0, 0x0, 0x0, 0x0, 0x0, 0x0, 0x0, 0x0, 0x0, 0x0, 0x0\n"
      ".long 0x0, 0x0, 0x0, 0x0, 0x0, 0x0, 0x0, 0x0, 0x0, 0x0, 0x0, 0x0, 0xffff, 0x0, 0x0, 0x0\n"
      ".long 0xffffffff, 0x0, 0x0, 0x0, 0xffff0000, 0xffff, 0x0, 0x0, 0x0, 0xffffffff, 0x0, 0x0, 0x0, 0xffff0000, 0xffff, 0x0\n"
      ".popsection\n"
      ".LEnd%=:\n"
      : "={s[80:95]}"(sts), "={s[96:99]}"(st2)
      : [ka] "s"(__builtin_amdgcn_kernarg_segment_ptr()), [bid] "s"(blockIdx.x), "{v0}"(threadIdx.x), "s"((u32)(uintptr_t)sm)
      : "v8", "v9", "v10", "v11", "v12", "v13", "v14", "v15", "v16", "v17", "v18", "v19", "v20", "v21", "v22", "v23", "v24", "v25", "v26", "v27",
        "v28", "v29", "v30", "v31", "v32", "v33", "v34", "v35", "v36", "v37", "v38", "v39", "v40", "v41", "v42", "v43", "v44", "v45", "v46", "v47",
        "v48", "v49", "v50", "v51", "v52", "v53", "v54", "v55", "v56", "v57", "v58", "v59", "v60", "v61", "v62", "v63", "v64", "v65", "v66", "v67",
        "v68", "v69", "v70", "v71", "v72", "v73", "v74", "v75", "v76", "v77", "v78", "v79", "v80", "v81", "v82", "v83", "v84", "v85", "v86", "v87",
        "v88", "v89", "v90", "v91", "v92", "v93", "v94", "v95", "v96", "v97", "v98", "v99", "v100", "v101", "v102", "v103", "v104", "v105", "v106", "v107",
        "v108", "v109", "v110", "v111", "v112", "v113", "v114", "v115", "v116", "v117", "v118", "v119", "v120", "v121", "v122", "v123", "v124", "v125", "v126", "v127",
        "v128", "v129", "v130", "v131", "v132", "v133", "v134", "v135", "v136", "v137", "v138", "v139", "v140", "v141", "v142", "v143", "v144", "v145", "v146", "v147",
        "v148", "v149", "v150", "v151", "v152", "v153", "v154", "v155", "v156", "v157", "v158", "v159", "v160", "v161", "v162", "v163", "v164", "v165", "v166", "v167",
        "v168", "v169", "v170", "v171", "v172", "v173", "v174", "v175", "v176", "v177", "v178", "v179", "v180", "v181", "v182", "v183", "v184", "v185", "v186", "v187",
        "v188", "v189", "v190", "v191", "v192", "v193", "v194", "v195", "v196", "v197", "v198", "v199", "v200", "v201", "v202", "v203", "v204", "v205", "v206", "v207",
        "v208", "v209", "v210", "v211", "v212", "v213", "v214", "v215", "v216", "v217", "v218", "v219", "v220", "v221", "v222", "v223", "v224", "v225", "v226", "v227",
        "v228", "v229", "v230", "v231", "v232", "v233", "v234", "v235", "v236", "v237", "v238", "v239", "v240", "v241", "v242", "v243", "v244", "v245", "v246", "v247",
        "v248", "v249", "v250", "v251", "v252", "v253", "v254", "v255", "s12", "s13", "s14", "s15", "s16", "s17", "s18", "s19", "s20", "s21", "s22", "s23",
        "s24", "s25", "s26", "s27", "s28", "s29", "s30", "s31", "s33", "s34", "s35", "s36", "s37", "s38", "s39", "s40", "s41", "s42", "s43", "s44",
        "s45", "s46", "s47", "s48", "s49", "s50", "s51", "s52", "s53", "s54", "s55", "s56", "s57", "s58", "s59", "s60", "s61", "s62", "s63", "s64",
        "s65", "s66", "s67", "s68", "s69", "s70", "s71", "s72", "s73", "s74", "s75", "s76", "s77", "s78", "s79",
        "vcc", "scc", "memory");
  const int b = blockIdx.x;
  GETST();
  STAMP(11);
  PEND();
}

extern "C" void kernel_launch(void* const* d_in, const int* in_sizes, int n_in, void* d_out, int out_size,
                              void* d_ws, size_t ws_size, hipStream_t stream) {
  const u16* const* in = (const u16* const*)d_in;
  fused<<<256, 512, 0, stream>>>(in[0], in[1], in[2], in[3], in[4], in[5], in[8], in[6], in[9], in[10], in[11],
                                 (u16*)d_out, (char*)d_ws);
}
```
